```python
import torch

def _softmax(x, axis=-1):
    return torch.softmax(x, dim=axis)
def _gelu(x):
    return torch.nn.functional.gelu(x, approximate="tanh")
def _silu(x):
    return torch.nn.functional.silu(x)
def _sigmoid(x):
    return torch.sigmoid(x)
def _layernorm(x, axis=-1, eps=1e-5):
    m = x.mean(dim=axis, keepdim=True)
    v = x.var(dim=axis, keepdim=True, unbiased=False)
    return (x - m) / torch.sqrt(v + eps)
def _scatter_add(tgt, idx, src, axis=0):
    return tgt.clone().index_add_(axis, idx, src)
def _segment_sum(src, seg, n):
    out = torch.zeros((n,) + src.shape[1:], dtype=src.dtype)
    return out.index_add_(0, seg, src)
def _pad(x, pw):
    flat = tuple(v for pair in reversed(pw) for v in pair)
    return torch.nn.functional.pad(x, flat)

kernel_name = 'parallel_sum__linear_attn_block__moe_arch_mod__prompt'

def setup_inputs():

    _w = (lambda a: (lambda t: t.to(torch.bfloat16) if t.is_floating_point() else t)(torch.as_tensor(a)))
    def _A_setup():
        B, S, D = 1, 2048, 1024
        return {
            "x": _w((lambda *s: torch.randn(*s, dtype=torch.bfloat16) * 0.05)(B, S, D)),
            "wqkv": _w((lambda *s: torch.randn(*s, dtype=torch.bfloat16) * 0.4)(D, 3 * D)),
            "wo": _w((lambda *s: torch.randn(*s, dtype=torch.bfloat16) * 0.2)(D, D)),
        }
    _A_in = _A_setup()

    def _B_setup():
        B, S, D, F, C = 1, 2048, 1024, 3584, 8
        return {
            "x": _w((lambda *s: torch.randn(*s, dtype=torch.bfloat16) * 0.05)(B, S, D)),
            "w_r": _w((lambda *s: torch.randn(*s, dtype=torch.bfloat16) * 0.05)(D)),
            "w_gate": _w((lambda *s: torch.randn(*s, dtype=torch.bfloat16) * 0.05)(D, F)),
            "w_up": _w((lambda *s: torch.randn(*s, dtype=torch.bfloat16) * 0.05)(D, F)),
            "w_down": _w((lambda *s: torch.randn(*s, dtype=torch.bfloat16) * 0.05)(F, D)),
        }
    _B_in = _B_setup()

    return {
        "A_x": _A_in["x"],
        "A_wqkv": _A_in["wqkv"],
        "A_wo": _A_in["wo"],
        "B_x": _B_in["x"],
        "B_w_r": _B_in["w_r"],
        "B_w_gate": _B_in["w_gate"],
        "B_w_up": _B_in["w_up"],
        "B_w_down": _B_in["w_down"],
    }

def reference(A_x, A_wqkv, A_wo, B_x, B_w_r, B_w_gate, B_w_up, B_w_down):
    def _A_ref(x, wqkv, wo):
        B, S, D = x.shape
        H = 32; Dh = D // H
        qkv = (lambda x, *d: x.permute(*d))((x @ wqkv).reshape(B, S, 3, H, Dh), 2, 0, 3, 1, 4)
        q, k, v = qkv[0], qkv[1], qkv[2]
        phi = lambda t: torch.where(t > 0, t + 1, torch.exp(t)) + 1e-6
        pq, pk = phi(q), phi(k)
        kv = torch.einsum("bhsd,bhse->bhde", pk, v)
        z = torch.einsum("bhsd->bhd", pk)
        num = torch.einsum("bhqd,bhde->bhqe", pq, kv)
        den = torch.einsum("bhqd,bhd->bhq", pq, z)[..., None] + 1e-6
        o = num / den
        return (lambda x, *d: x.permute(*d))(o, 0, 2, 1, 3).reshape(B, S, D) @ wo

    def _B_ref(x, w_r, w_gate, w_up, w_down):
        B, S, D = x.shape
        C = 8
        r = x @ w_r
        idx = torch.argsort(-r, axis=-1)[:, :C]
        idx_e = (lambda x, n, axis: torch.repeat_interleave(x, n, dim=axis))(idx.reshape(B, C, 1), D, axis=2)
        x_sel = (lambda x, i, axis: torch.take_along_dim(x, i, dim=axis))(x, idx_e, axis=1)
        g = _silu(x_sel @ w_gate)
        u = x_sel @ w_up
        y = (g * u) @ w_down
        r_sel = _sigmoid((lambda x, i, axis: torch.take_along_dim(x, i, dim=axis))(r, idx, axis=1))
        y = y * r_sel.reshape(B, C, 1)
        b_off = (torch.arange(B) * S).reshape(B, 1)
        flat_idx = (idx + b_off).reshape(B * C)
        zero = (x * 0.0).reshape(B * S, D)
        upd = _scatter_add(zero, flat_idx, y.reshape(B * C, D), axis=0)
        return x + upd.reshape(B, S, D)

    x = A_x
    xn = _layernorm(x, axis=-1)
    return x + _A_ref(xn, A_wqkv, A_wo) + _B_ref(xn, B_w_r, B_w_gate, B_w_up, B_w_down)
```

```cpp
#include <hip/hip_runtime.h>
#include <hip/hip_bf16.h>
#include <stdint.h>

typedef unsigned short u16;
typedef short s4 __attribute__((ext_vector_type(4)));
typedef short s8 __attribute__((ext_vector_type(8)));
typedef float f16v __attribute__((ext_vector_type(16)));
typedef float f4 __attribute__((ext_vector_type(4)));
typedef float f2 __attribute__((ext_vector_type(2)));
typedef unsigned u4 __attribute__((ext_vector_type(4)));
typedef unsigned u2 __attribute__((ext_vector_type(2)));
typedef __bf16 bf2 __attribute__((ext_vector_type(2)));
#define LAS __attribute__((address_space(3)))
#define GAS __attribute__((address_space(1)))
#define DEV __device__ __forceinline__

constexpr int S = 2048, D = 1024, F = 3584;
template <int N> struct ic { static constexpr int value = N; };

constexpr size_t WS_XN = 0;
constexpr size_t WS_PQF = 4u << 20;
constexpr size_t WS_KVP = 8u << 20;
constexpr size_t WS_GP = (10u << 20) + (256u << 10);
constexpr size_t WS_YP = WS_GP + (896u << 10);
constexpr size_t WS_H = WS_YP + (448u << 10);
constexpr size_t WS_R = WS_H + (64u << 10);
constexpr size_t WS_SEL = WS_R + (8u << 10);
constexpr size_t WS_CNT = WS_SEL + 4096;

DEV float bf2f(u16 v) { return __uint_as_float(((unsigned)v) << 16); }
DEV unsigned pk2(float a, float b) { bf2 v; v[0] = (__bf16)a; v[1] = (__bf16)b; return __builtin_bit_cast(unsigned, v); }
DEV float lo(unsigned u) { return __uint_as_float(u << 16); }
DEV float hi(unsigned u) { return __uint_as_float(u & 0xFFFF0000u); }
DEV float rbf(float a) { return lo(pk2(a, a)); }
DEV float phif(float t) {
  float e = __builtin_amdgcn_fmed3f(__builtin_amdgcn_exp2f(t * 1.44269504f), 0.f, 1.f);
  return (e + 1e-6f) + fmaxf(t, 0.f);
}
#define RD128(dst, addr, OFF) asm volatile("ds_read_b128 %0, %1 offset:%2" : "=v"(dst) : "v"(addr), "i"(OFF) : "memory")
#define RDTR(dst, addr, OFF) asm volatile("ds_read_b64_tr_b16 %0, %1 offset:%2" : "=v"(dst) : "v"(addr), "i"(OFF) : "memory")
#define SBAR() __builtin_amdgcn_sched_barrier(0)
#define CAT(x, y) __builtin_shufflevector(x, y, 0, 1, 2, 3, 4, 5, 6, 7)
#define WAITV(n) asm volatile("s_waitcnt vmcnt(%0)" ::"i"(n) : "memory")
#define WAITL(n) asm volatile("s_waitcnt lgkmcnt(%0)" ::"i"(n) : "memory")
DEV const char* uptr(const char* p) {
  unsigned long long v = (unsigned long long)p;
  unsigned lo_ = __builtin_amdgcn_readfirstlane((unsigned)v), hi_ = __builtin_amdgcn_readfirstlane((unsigned)(v >> 32));
  return (const char*)(((unsigned long long)hi_ << 32) | lo_);
}
#define GLDS(voff, sbase, m0v) asm volatile("s_mov_b32 m0, %2\n s_nop 0\n global_load_lds_dwordx4 %0, %1" ::"v"(voff), "s"(uptr(sbase)), "s"(__builtin_amdgcn_readfirstlane(m0v)) : "memory")
#define MFMA32(a, b, c) __builtin_amdgcn_mfma_f32_32x32x16_bf16(a, b, c, 0, 0, 0)
#define MFMA16(a, b, c) __builtin_amdgcn_mfma_f32_16x16x32_bf16(a, b, c, 0, 0, 0)

DEV void lds_arrive(LAS unsigned* c, int l) {
  if (l == 0) __hip_atomic_fetch_add(c, 1u, __ATOMIC_RELAXED, __HIP_MEMORY_SCOPE_WORKGROUP);
}
DEV void lds_wait(LAS unsigned* c, unsigned target) {
  while (__builtin_amdgcn_readfirstlane(__hip_atomic_load(c, __ATOMIC_RELAXED, __HIP_MEMORY_SCOPE_WORKGROUP)) < target) {}
  asm volatile("" ::: "memory");
}
DEV void lds_wait_s(LAS unsigned* c, unsigned target) {
  while (__builtin_amdgcn_readfirstlane(__hip_atomic_load(c, __ATOMIC_RELAXED, __HIP_MEMORY_SCOPE_WORKGROUP)) < target) __builtin_amdgcn_s_sleep(4);
  asm volatile("" ::: "memory");
}
DEV void bwait(bool leader, GAS unsigned* c, unsigned target, LAS unsigned* f, int l) {
  if (leader) {
    for (unsigned it_ = 0; it_ < (1u << 22) && (unsigned)__builtin_amdgcn_readfirstlane(__hip_atomic_load(c, __ATOMIC_RELAXED, __HIP_MEMORY_SCOPE_AGENT)) < target; ++it_) __builtin_amdgcn_s_sleep(8);
    asm volatile("" ::: "memory");
    lds_arrive(f, l);
  } else lds_wait_s(f, 1);
}
DEV void gwait(GAS unsigned* c, unsigned target) {
  for (unsigned it_ = 0; it_ < (1u << 22) && (unsigned)__builtin_amdgcn_readfirstlane(__hip_atomic_load(c, __ATOMIC_RELAXED, __HIP_MEMORY_SCOPE_AGENT)) < target; ++it_) __builtin_amdgcn_s_sleep(2);
  asm volatile("" ::: "memory");
}

#define STMP(kid, idx)
#define STMC(kid, idx)
#define WSS ws
DEV float wave_sum(float v) {
  v += __int_as_float(__builtin_amdgcn_update_dpp(0, __float_as_int(v), 0x111, 0xf, 0xf, false));
  v += __int_as_float(__builtin_amdgcn_update_dpp(0, __float_as_int(v), 0x112, 0xf, 0xf, false));
  v += __int_as_float(__builtin_amdgcn_update_dpp(0, __float_as_int(v), 0x114, 0xf, 0xf, false));
  v += __int_as_float(__builtin_amdgcn_update_dpp(0, __float_as_int(v), 0x118, 0xf, 0xf, false));
  v += __int_as_float(__builtin_amdgcn_update_dpp(0, __float_as_int(v), 0x142, 0xa, 0xf, false));
  v += __int_as_float(__builtin_amdgcn_update_dpp(0, __float_as_int(v), 0x143, 0xc, 0xf, false));
  return __int_as_float(__builtin_amdgcn_readlane(__float_as_int(v), 63));
}
__global__ __launch_bounds__(256) void k1_ln(const u16* __restrict__ x, const u16* __restrict__ wr, char* __restrict__ ws, const u16* __restrict__ wqkv, const u16* __restrict__ wo) {
  u16* xn = (u16*)(ws + WS_XN);
  float* r = (float*)(ws + WS_R);
  int w = threadIdx.x >> 6, l = threadIdx.x & 63;
  if (blockIdx.x == 0 && threadIdx.x == 0) *(unsigned long long*)(ws + (33u<<20)) = __builtin_amdgcn_s_memrealtime();
  if (blockIdx.x == 0 && threadIdx.x < 128) ((unsigned*)(ws + WS_CNT))[threadIdx.x * 32] = 0u;
  u4 a[2], b[2];
#pragma unroll
  for (int i = 0; i < 2; ++i) {
    int row = blockIdx.x * 8 + w * 2 + i;
    a[i] = *(const u4*)(x + row * D + l * 8); b[i] = *(const u4*)(x + row * D + 512 + l * 8);
  }
  u4 wv0 = *(const u4*)(wr + l * 8), wv1 = *(const u4*)(wr + 512 + l * 8);
#pragma unroll
  for (int i = 0; i < 2; ++i) {
    int row = blockIdx.x * 8 + w * 2 + i;
    float v[16];
#pragma unroll
    for (int j = 0; j < 4; ++j) { v[2*j] = lo(a[i][j]); v[2*j+1] = hi(a[i][j]); v[8+2*j] = lo(b[i][j]); v[9+2*j] = hi(b[i][j]); }
    float s = 0;
#pragma unroll
    for (int j = 0; j < 16; ++j) s += v[j];
    s = wave_sum(s);
    float m = s * (1.f / D), q = 0;
#pragma unroll
    for (int j = 0; j < 16; ++j) { v[j] -= m; q += v[j] * v[j]; }
    q = wave_sum(q);
    float rs = 1.f / sqrtf(q * (1.f / D) + 1e-5f);
    u4 oa, ob; float d = 0;
#pragma unroll
    for (int j = 0; j < 4; ++j) {
      oa[j] = pk2(v[2*j] * rs, v[2*j+1] * rs); ob[j] = pk2(v[8+2*j] * rs, v[9+2*j] * rs);
      d += lo(oa[j]) * lo(wv0[j]) + hi(oa[j]) * hi(wv0[j]) + lo(ob[j]) * lo(wv1[j]) + hi(ob[j]) * hi(wv1[j]);
    }
    d = wave_sum(d);
    *(u4*)(xn + row * D + l * 8) = oa; *(u4*)(xn + row * D + 512 + l * 8) = ob;
    if (l == 0) r[row] = rbf(d);
  }
  if (w == 0) STMP(0, 1);
}

constexpr int K2_STG = 40960;
constexpr int L_KVX = 3 * K2_STG;
constexpr int L_RED1 = L_KVX + 17408;
constexpr int L_TOP = L_RED1 + 16384;
constexpr int L_BAR = L_TOP + 256;
constexpr int K2_LDS = L_BAR + 64;

DEV unsigned wave_max(unsigned v) {
  v = max(v, (unsigned)__builtin_amdgcn_update_dpp(0, (int)v, 0x111, 0xf, 0xf, false));
  v = max(v, (unsigned)__builtin_amdgcn_update_dpp(0, (int)v, 0x112, 0xf, 0xf, false));
  v = max(v, (unsigned)__builtin_amdgcn_update_dpp(0, (int)v, 0x114, 0xf, 0xf, false));
  v = max(v, (unsigned)__builtin_amdgcn_update_dpp(0, (int)v, 0x118, 0xf, 0xf, false));
  v = max(v, (unsigned)__builtin_amdgcn_update_dpp(0, (int)v, 0x142, 0xa, 0xf, false));
  v = max(v, (unsigned)__builtin_amdgcn_update_dpp(0, (int)v, 0x143, 0xc, 0xf, false));
  return (unsigned)__builtin_amdgcn_readlane((int)v, 63);
}

struct FR { s8 a0, a1; s4 b[6]; };
#define LWM lds_wait
#define NTL(p) __builtin_nontemporal_load(p)

DEV void k2_moe(char* lds, char* ws, const u16* __restrict__ wg, const u16* __restrict__ wu, const u16* __restrict__ wd, int mw, int l, const u16* __restrict__ wq_) {
  if (blockIdx.x >= 224) return;
  const int u = blockIdx.x;
  const u16* xn = (const u16*)(ws + WS_XN);
  const float* r = (const float*)(ws + WS_R);
  LAS unsigned* barm = (LAS unsigned*)(lds + L_BAR + 4);
  const int np = l & 15, kq = l >> 4;
  const int chunk = u >> 2, ksp = u & 3;
  float rv[8];
#pragma unroll
  for (int i = 0; i < 8; ++i) rv[i] = r[mw * 512 + i * 64 + l];
  u4 gw[2][8];
  {
    const u16* wp = ((np >> 3) ? wu : wg) + (size_t)(256 * ksp + 64 * mw + 8 * kq) * F + 64 * chunk + 8 * (np & 7);
#pragma unroll
    for (int it = 0; it < 2; ++it)
#pragma unroll
      for (int rr = 0; rr < 8; ++rr) gw[it][rr] = NTL((const u4*)(wp + (size_t)(32 * it + rr) * F));
  }
  const int ks = u >> 4, cc = u & 15, kset = np >> 3;
  u4 dw[8];
  {
    const u16* dp = wd + (size_t)(256 * ks + 64 * mw + 32 * kset + 8 * kq) * D + 64 * cc + 8 * (np & 7);
#pragma unroll
    for (int rr = 0; rr < 8; ++rr) dw[rr] = NTL((const u4*)(dp + (size_t)rr * D));
  }
  if (mw == 0) STMP(1, 8);
  unsigned key[8];
#pragma unroll
  for (int i = 0; i < 8; ++i) {
    unsigned b = __float_as_uint(rv[i]) >> 16;
    unsigned mono = (b & 0x8000u) ? (~b & 0xFFFFu) : (b | 0x8000u);
    key[i] = (mono << 16) | (unsigned)(0xFFFF - (mw * 512 + i * 64 + l));
  }
  unsigned* cand = (unsigned*)(lds + L_TOP);
  int* sel = (int*)(lds + L_TOP + 128) + mw * 8;
#pragma unroll 1
  for (int it = 0; it < 8; ++it) {
    unsigned m = max(max(max(key[0], key[1]), max(key[2], key[3])), max(max(key[4], key[5]), max(key[6], key[7])));
    m = wave_max(m);
#pragma unroll
    for (int i = 0; i < 8; ++i) key[i] = key[i] == m ? 0u : key[i];
    if (l == 0) cand[mw * 8 + it] = m;
  }
  asm volatile("s_waitcnt lgkmcnt(0)" ::: "memory");
  lds_arrive(barm, l); lds_wait_s(barm, 4);
  {
    unsigned mine = cand[l & 31];
    int rank = 0;
#pragma unroll
    for (int j = 0; j < 32; ++j) rank += ((unsigned)__builtin_amdgcn_readlane((int)mine, j) > mine) ? 1 : 0;
    if (l < 32 && rank < 8) sel[rank] = 0xFFFF - (int)(mine & 0xFFFF);
  }
  int idx = sel[l & 7];
  if (mw == 0) STMP(1, 9);
  if (u == 0 && mw == 0) {
    if (l < 8) {
      float rr = r[idx];
      float sg = rbf(1.f / (1.f + __expf(-rr)));
      __hip_atomic_store((GAS unsigned long long*)(ws + WS_SEL) + l, ((unsigned long long)__float_as_uint(sg) << 32) | (unsigned)idx, __ATOMIC_RELAXED, __HIP_MEMORY_SCOPE_AGENT);
    }
    asm volatile("s_waitcnt vmcnt(0)" ::: "memory");
    if (l == 0) __hip_atomic_fetch_add((GAS unsigned*)(ws + WS_CNT) + 120 * 32, 1u, __ATOMIC_RELAXED, __HIP_MEMORY_SCOPE_AGENT);
  }
  f4 acc[8];
#pragma unroll
  for (int c = 0; c < 8; ++c) acc[c] = (f4)(0.f);
  {
    const u16* xp = xn + (size_t)idx * D + 256 * ksp + 64 * mw + 8 * kq;
    u4 xa[2];
#pragma unroll
    for (int it = 0; it < 2; ++it) xa[it] = *(const u4*)(xp + 32 * it);
#pragma unroll
    for (int it = 0; it < 2; ++it) {
#pragma unroll
      for (int c = 0; c < 8; ++c) {
        u4 f;
#pragma unroll
        for (int q = 0; q < 4; ++q) {
          unsigned e = gw[it][2 * q][c >> 1], o = gw[it][2 * q + 1][c >> 1];
          f[q] = (c & 1) ? __builtin_amdgcn_perm(o, e, 0x07060302u) : __builtin_amdgcn_perm(o, e, 0x05040100u);
        }
        acc[c] = MFMA16(__builtin_bit_cast(s8, xa[it]), __builtin_bit_cast(s8, f), acc[c]);
      }
    }
  }
  if (mw == 0) STMP(1, 10);
  float* red1 = (float*)(lds + L_RED1);
  if (kq < 2) {
#pragma unroll
    for (int e = 0; e < 4; ++e) {
      float* p = red1 + ((mw * 8 + 4 * kq + e) * 2 + (np >> 3)) * 64 + 8 * (np & 7);
      *(f4*)p = (f4){acc[0][e], acc[1][e], acc[2][e], acc[3][e]};
      *(f4*)(p + 4) = (f4){acc[4][e], acc[5][e], acc[6][e], acc[7][e]};
    }
  }
  asm volatile("s_waitcnt lgkmcnt(0)" ::: "memory");
  lds_arrive(barm, l); lds_wait_s(barm, 8);
  GAS unsigned* gcnt = (GAS unsigned*)(ws + WS_CNT) + (32 + chunk) * 32;
  GAS unsigned* hcnt = (GAS unsigned*)(ws + WS_CNT) + (16 + (chunk >> 2)) * 32;
  float* gpart = (float*)(ws + WS_GP) + (size_t)chunk * 4096;
  __amdgpu_buffer_rsrc_t grs = __builtin_amdgcn_make_buffer_rsrc(gpart, 0, 16384, 0x00020000);
  unsigned* tick = (unsigned*)(lds + L_TOP + 192);
  {
    int tm = mw * 64 + l;
    f4 sacc = *(const f4*)(red1 + tm * 4) + *(const f4*)(red1 + 1024 + tm * 4) + *(const f4*)(red1 + 2048 + tm * 4) + *(const f4*)(red1 + 3072 + tm * 4);
    __builtin_amdgcn_raw_buffer_store_b128(__builtin_bit_cast(u4, sacc), grs, (ksp * 1024 + tm * 4) * 4, 0, 16);
    asm volatile("s_waitcnt vmcnt(0)" ::: "memory");
  }
  lds_arrive(barm, l);
  if (mw == 0) {
    lds_wait_s(barm, 12);
    unsigned tk = 0;
    if (l == 0) tk = __hip_atomic_fetch_add(gcnt, 1u, __ATOMIC_RELAXED, __HIP_MEMORY_SCOPE_AGENT);
    tk = __builtin_amdgcn_readfirstlane(tk);
    if (tk == 3) {
      int tok = l >> 3, c8 = (l & 7) * 8;
      u4 pv[4][4];
#pragma unroll
      for (int p = 0; p < 4; ++p)
#pragma unroll
        for (int q = 0; q < 4; ++q)
          pv[p][q] = __builtin_amdgcn_raw_buffer_load_b128(grs, (p * 1024 + (tok * 2 + (q >> 1)) * 64 + c8 + (q & 1) * 4) * 4, 0, 16);
      f4 gq[4];
#pragma unroll
      for (int q = 0; q < 4; ++q) gq[q] = (__builtin_bit_cast(f4, pv[0][q]) + __builtin_bit_cast(f4, pv[1][q])) + (__builtin_bit_cast(f4, pv[2][q]) + __builtin_bit_cast(f4, pv[3][q]));
      float hv[8];
#pragma unroll
      for (int e = 0; e < 8; ++e) {
        float gg = rbf(gq[e >> 2][e & 3]), u1 = rbf(gq[2 + (e >> 2)][e & 3]);
        float sg = rbf(gg / (1.f + __expf(-gg)));
        hv[e] = sg * u1;
      }
      u4 ho; ho[0] = pk2(hv[0], hv[1]); ho[1] = pk2(hv[2], hv[3]); ho[2] = pk2(hv[4], hv[5]); ho[3] = pk2(hv[6], hv[7]);
      __amdgpu_buffer_rsrc_t hrs = __builtin_amdgcn_make_buffer_rsrc(ws + WS_H, 0, 8 * F * 2, 0x00020000);
      __builtin_amdgcn_raw_buffer_store_b128(ho, hrs, (tok * F + 64 * chunk + c8) * 2, 0, 16);
      asm volatile("s_waitcnt vmcnt(0)" ::: "memory");
      if (l == 0) __hip_atomic_fetch_add(hcnt, 1u, __ATOMIC_RELAXED, __HIP_MEMORY_SCOPE_AGENT);
    }
  }
  if (mw == 0) STMP(1, 11);
  GAS unsigned* hcnt2 = (GAS unsigned*)(ws + WS_CNT) + (16 + ks) * 32;
  bwait(mw == 0, hcnt2, 4, (LAS unsigned*)(lds + L_BAR + 24), l);
  if (mw == 0) STMP(1, 12);
  f4 acd[8];
  {
    __amdgpu_buffer_rsrc_t hrs = __builtin_amdgcn_make_buffer_rsrc(ws + WS_H, 0, 8 * F * 2, 0x00020000);
    u4 ha = __builtin_amdgcn_raw_buffer_load_b128(hrs, ((l & 7) * F + 256 * ks + 64 * mw + 32 * ((l >> 3) & 1) + 8 * kq) * 2, 0, 16);
#pragma unroll
    for (int c = 0; c < 8; ++c) {
      u4 f;
#pragma unroll
      for (int q = 0; q < 4; ++q) {
        unsigned e = dw[2 * q][c >> 1], o = dw[2 * q + 1][c >> 1];
        f[q] = (c & 1) ? __builtin_amdgcn_perm(o, e, 0x07060302u) : __builtin_amdgcn_perm(o, e, 0x05040100u);
      }
      acd[c] = MFMA16(__builtin_bit_cast(s8, ha), __builtin_bit_cast(s8, f), (f4)(0.f));
    }
  }
  float* red2 = (float*)(lds + L_RED1);
  lds_wait_s(barm, 12);
  if ((kq >> 1) == kset) {
#pragma unroll
    for (int e = 0; e < 4; ++e) {
      float* p = red2 + ((mw * 2 + kset) * 8 + 4 * (kq & 1) + e) * 64 + 8 * (np & 7);
      *(f4*)p = (f4){acd[0][e], acd[1][e], acd[2][e], acd[3][e]};
      *(f4*)(p + 4) = (f4){acd[4][e], acd[5][e], acd[6][e], acd[7][e]};
    }
  }
  asm volatile("s_waitcnt lgkmcnt(0)" ::: "memory");
  lds_arrive(barm, l); lds_wait_s(barm, 16);
  {
    int tm = mw * 64 + l, tok = tm >> 5, c2 = (tm & 31) * 2;
    f2 sacc = (f2)(0.f);
#pragma unroll
    for (int p = 0; p < 8; ++p) sacc += *(const f2*)(red2 + (p * 8 + tok) * 64 + c2);
    __hip_atomic_store((GAS unsigned long long*)((float*)(ws + WS_YP) + (size_t)(ks * 8 + tok) * D + 64 * cc + c2), __builtin_bit_cast(unsigned long long, sacc), __ATOMIC_RELAXED, __HIP_MEMORY_SCOPE_AGENT);
    asm volatile("s_waitcnt vmcnt(0)" ::: "memory");
    if (l == 0) __hip_atomic_fetch_add((GAS unsigned*)(ws + WS_CNT) + (104 + cc) * 32, 1u, __ATOMIC_RELAXED, __HIP_MEMORY_SCOPE_AGENT);
  }
  if (mw == 0) STMP(1, 13);
  asm volatile(".Lk2_moe_end:");
}

__global__ __launch_bounds__(512) void k2(const u16* __restrict__ wqkv, const u16* __restrict__ wg, const u16* __restrict__ wu,
                                          const u16* __restrict__ wd, char* __restrict__ ws, const u16* __restrict__ x,
                                          const u16* __restrict__ wo, u16* __restrict__ out) {
  extern __shared__ __attribute__((aligned(16))) char lds[];
  const int t0 = threadIdx.x, w = __builtin_amdgcn_readfirstlane(t0 >> 6), l = t0 & 63;
  if (t0 < 16) ((unsigned*)(lds + L_BAR))[t0] = 0u;
  __syncthreads();
  if (w == 0) STMP(1, 0);
  asm volatile(".Lk2_code_beg:");
  if (w == 7) {
    unsigned long long pc; unsigned len, off;
    asm volatile("s_getpc_b64 %0\n.Lk2_here:" : "=s"(pc));
    asm volatile("s_mov_b32 %0, (.Lk2_here - .Lk2_code_beg)" : "=s"(off));
    asm volatile("s_mov_b32 %0, (.Lk2_code_end - .Lk2_code_beg)" : "=s"(len));
    int len2;
    asm volatile("s_mov_b32 %0, (.Lk2_moe_end - .Lk2_code_beg)" : "=s"(len2));
    if (len2 > (int)len) len = (unsigned)len2;
    pc -= off;
    unsigned acc_ = 0;
    for (unsigned o = l * 64; o < len; o += 4096) acc_ ^= *(const unsigned*)((const char*)pc + o);
    asm volatile("" ::"v"(acc_));
  }
  if (w >= 4) {
    k2_moe(lds, ws, wg, wu, wd, w - 4, l, wqkv);
    return;
  }
  const int t = t0, wr = w >> 1, wc = w & 1, r31 = l & 31, h = l >> 5;
  const int g = blockIdx.x, xcd = g & 7, j = g >> 3;
  const int mb = (xcd & 1) * 8 + (j >> 2), hp = (xcd >> 1) * 4 + (j & 3);
  LAS unsigned* barc = (LAS unsigned*)(lds + L_BAR);
  unsigned aoffv, boffv;
  { int row = t >> 3, slot = t & 7; aoffv = row * 2048 + ((slot ^ ((row >> 1) & 7)) * 16); boffv = row * 6144 + ((slot ^ (((row >> 1) & 1) << 2)) * 16); }
  const char* Ab = ws + WS_XN + (size_t)mb * 128 * 2048;
  const char* Bb = (const char*)wqkv + hp * 128;
  const unsigned ldsb = (unsigned)(size_t)(LAS char*)lds;
  const unsigned m0base = ldsb + w * 1024;
  const int rot = 0;
  auto gl = [&](int kt_, int buf, int i) {
    const int kt = (kt_ + rot) & 15;
    unsigned m0v = m0base + buf * K2_STG;
    if (i < 4) { const char* sb = Ab + kt * 128 + i * 65536; GLDS(aoffv, sb, m0v + i * 4096); }
    else { int ii = i - 4; const char* sb = Bb + (size_t)(kt * 64 + (ii & 1) * 32) * 6144 + (ii >> 1) * 2048; GLDS(boffv, sb, m0v + 16384 + ii * 4096); }
  };
  unsigned aad[4];
  { int aoff = (wr * 64 + r31) * 128, asw = (r31 >> 1) & 7;
#pragma unroll
    for (int s = 0; s < 4; ++s) aad[s] = ldsb + aoff + (((2 * s + h) ^ asw) * 16); }
  const int q = (l & 15) >> 2, p = l & 3;
  const unsigned bad = ldsb + 16384 + (8 * h + q) * 128 + ((wc ^ (q >> 1)) * 64) + ((l >> 4) & 1) * 32 + p * 8;
  f16v acc[2][3];
#pragma unroll
  for (int m = 0; m < 2; ++m)
#pragma unroll
    for (int n = 0; n < 3; ++n) acc[m][n] = (f16v)(0.f);
#define MF(m, n, Fq) do { if (n == 0) acc[m][0] = MFMA32(CAT(Fq.b[0], Fq.b[1]), (m ? Fq.a1 : Fq.a0), acc[m][0]); \
     else acc[m][n] = MFMA32((m ? Fq.a1 : Fq.a0), CAT(Fq.b[2 * n], Fq.b[2 * n + 1]), acc[m][n]); } while (0)
#define GL(i) do { if (dost_) gl(kt + 2, sbuf, (i)); } while (0)
#define ARRIVE(addr) asm volatile("s_mov_b64 exec, 1\n ds_add_u32 %0, %1\n s_mov_b64 exec, -1" ::"v"(addr), "v"(onev) : "memory")
#define POLL(dst, addr) asm volatile("ds_read_b32 %0, %1" : "=v"(dst) : "v"(addr) : "memory")
#define KEEP(v) asm volatile("" ::"v"(v))
#define CHECK(v, c, target) do { if ((unsigned)__builtin_amdgcn_readfirstlane(v) < (unsigned)(target)) lds_wait(c, target); } while (0)
#define SUB(Fq, G, va, vb, SS, G0, CHK) do { \
    WAITL(5); SBAR(); \
    MF(0, 0, Fq); SBAR(); RD128(G.a0, va, 0); RDTR(G.b[0], vb, SS * 2048); RDTR(G.b[1], vb, SS * 2048 + 512); RDTR(G.b[2], vb, 8192 + SS * 2048); RDTR(G.b[3], vb, 8192 + SS * 2048 + 512); SBAR(); \
    WAITL(8); SBAR(); \
    MF(0, 1, Fq); SBAR(); RD128(G.a1, va, 4096); RDTR(G.b[4], vb, 16384 + SS * 2048); RDTR(G.b[5], vb, 16384 + SS * 2048 + 512); SBAR(); \
    WAITL(8); SBAR(); \
    MF(0, 2, Fq); SBAR(); if (CHK) { KEEP(vB); if (dost_) CHECK(vB, barB, 4 * kt); } GL(G0); SBAR(); \
    MF(1, 0, Fq); SBAR(); GL(G0 + 1); SBAR(); MF(1, 1, Fq); SBAR(); GL(G0 + 2); SBAR(); MF(1, 2, Fq); SBAR(); } while (0)
#define KSTEP(DOST, LAST) do { \
    constexpr bool dost_ = DOST; \
    int nbuf = buf == 2 ? 0 : buf + 1, sbuf = buf == 0 ? 2 : buf - 1; \
    unsigned bo = buf * K2_STG, nbo = nbuf * K2_STG; \
    unsigned vb = bad + bo; \
    { unsigned va = aad[1] + bo; SUB(f0, f1, va, vb, 1, 0, true); } \
    { unsigned va = aad[2] + bo; SUB(f1, f0, va, vb, 2, 3, false); } \
    if (!LAST) { if (dost_) WAITV(6); else WAITV(0); ARRIVE(adA); SBAR(); } \
    { unsigned va = aad[3] + bo; SUB(f0, f1, va, vb, 3, 6, false); } \
    GL(9); \
    if (!LAST) { \
      POLL(vA, adA); \
      WAITL(0); SBAR(); \
      ARRIVE(adB); \
      MF(0, 0, f1); SBAR(); \
      CHECK(vA, barc, 4 * (kt + 2)); \
      SBAR(); \
      unsigned va = aad[0] + nbo, vn = bad + nbo; \
      RD128(f0.a0, va, 0); RDTR(f0.b[0], vn, 0); RDTR(f0.b[1], vn, 512); RDTR(f0.b[2], vn, 8192); RDTR(f0.b[3], vn, 8192 + 512); SBAR(); \
      MF(0, 1, f1); SBAR(); \
      RD128(f0.a1, va, 4096); RDTR(f0.b[4], vn, 16384); RDTR(f0.b[5], vn, 16384 + 512); SBAR(); \
      MF(0, 2, f1); MF(1, 0, f1); MF(1, 1, f1); SBAR(); \
      POLL(vB, adB); SBAR(); \
      MF(1, 2, f1); SBAR(); \
    } else { WAITL(0); SBAR(); MF(0, 0, f1); MF(0, 1, f1); MF(0, 2, f1); MF(1, 0, f1); MF(1, 1, f1); MF(1, 2, f1); } \
    buf = nbuf; } while (0)
  const unsigned adA = ldsb + L_BAR, adB = ldsb + L_BAR + 8, onev = 1u;
  LAS unsigned* barB = (LAS unsigned*)(lds + L_BAR + 8);
  unsigned vA, vB = 0;
#pragma unroll
  for (int s = 0; s < 2; ++s)
#pragma unroll
    for (int i = 0; i < 10; ++i) gl(s, s, i);
  WAITV(10);
  lds_arrive(barc, l); lds_wait(barc, 4);
  if (w == 0) { STMP(1, 1); STMC(1, 40); }
  FR f0, f1;
  { unsigned va = aad[0], vb = bad;
    RD128(f0.a0, va, 0); RDTR(f0.b[0], vb, 0); RDTR(f0.b[1], vb, 512); RDTR(f0.b[2], vb, 8192); RDTR(f0.b[3], vb, 8192 + 512); RD128(f0.a1, va, 4096); RDTR(f0.b[4], vb, 16384); RDTR(f0.b[5], vb, 16384 + 512);
    SBAR(); }
  int buf = 0;
  int kt = 0;
#pragma unroll 1
  for (; kt < 14; ++kt) KSTEP(true, false);
  KSTEP(false, false);
  ++kt;
  KSTEP(false, true);
  if (w == 0) { STMP(1, 2); STMC(1, 41); }
  const int head = hp * 2 + wc;
  f16v kv = (f16v)(0.f), zz16 = (f16v)(0.f);
  const u4 ones = (u4){0x3F803F80u, 0x3F803F80u, 0x3F803F80u, 0x3F803F80u};
#pragma unroll
  for (int m = 0; m < 2; ++m) {
#pragma unroll
    for (int s2 = 0; s2 < 2; ++s2) {
      u4 kf, vf;
#pragma unroll
      for (int e = 0; e < 4; ++e) {
        kf[e] = pk2(phif(acc[m][1][8 * s2 + 2 * e]), phif(acc[m][1][8 * s2 + 2 * e + 1]));
        vf[e] = pk2(acc[m][2][8 * s2 + 2 * e], acc[m][2][8 * s2 + 2 * e + 1]);
      }
      kv = MFMA32(__builtin_bit_cast(s8, kf), __builtin_bit_cast(s8, vf), kv);
      zz16 = MFMA32(__builtin_bit_cast(s8, kf), __builtin_bit_cast(s8, ones), zz16);
    }
  }
  float z;
  { float t0_ = zz16[0];
#pragma unroll
    for (int i = 1; i < 16; ++i) t0_ = (r31 == i) ? zz16[i] : t0_;
    z = t0_; }
  float* cb = (float*)(lds + L_KVX) + wc * 1088;
  if (wr) {
#pragma unroll
    for (int i4 = 0; i4 < 4; ++i4) *(f4*)(cb + i4 * 256 + l * 4) = (f4){kv[4 * i4], kv[4 * i4 + 1], kv[4 * i4 + 2], kv[4 * i4 + 3]};
    cb[1024 + l] = z;
  }
  asm volatile("s_waitcnt lgkmcnt(0)" ::: "memory");
  lds_arrive(barc, l);
  GAS unsigned* kcnt = (GAS unsigned*)(ws + WS_CNT) + hp * 32;
  if (wr == 0) {
    lds_wait(barc, 4 * 17);
    float* part = (float*)(ws + WS_KVP) + (size_t)(mb * 16 + hp) * 2176;
    __amdgpu_buffer_rsrc_t rs = __builtin_amdgcn_make_buffer_rsrc(part, 0, 2176 * 4, 0x00020000);
#pragma unroll
    for (int i4 = 0; i4 < 4; ++i4) {
      f4 o = *(const f4*)(cb + i4 * 256 + l * 4);
      f4 v = (f4){kv[4 * i4] + o[0], kv[4 * i4 + 1] + o[1], kv[4 * i4 + 2] + o[2], kv[4 * i4 + 3] + o[3]};
      __builtin_amdgcn_raw_buffer_store_b128(__builtin_bit_cast(u4, v), rs, (wc * 1088 + i4 * 256 + l * 4) * 4, 0, 16);
    }
    float zz = z + cb[1024 + l];
    __builtin_amdgcn_raw_buffer_store_b32(__float_as_uint(zz), rs, (wc * 1088 + 1024 + l) * 4, 0, 16);
    asm volatile("s_waitcnt vmcnt(0)" ::: "memory");
    if (l == 0) __hip_atomic_fetch_add(kcnt, 1u, __ATOMIC_RELAXED, __HIP_MEMORY_SCOPE_AGENT);
  }
  unsigned wovoff; { int row = l >> 3, slot = l & 7; wovoff = row * 2048 + ((slot ^ (((row >> 1) & 1) << 2)) * 16); }
  const char* wob = (const char*)wo + hp * 128;
  if (wr == 1) {
    lds_wait(barc, 4 * 17);
#pragma unroll
    for (int c = 0; c < 7; ++c)
#pragma unroll
      for (int pc = 0; pc < 8; ++pc)
        GLDS(wovoff, wob + (size_t)(wc * 512 + c * 64 + pc * 8) * 2048, ldsb + c * 16384 + wc * 8192 + pc * 1024);
  }
  if (w == 0) STMP(1, 3);
  u4 pf[2][2];
#pragma unroll
  for (int m = 0; m < 2; ++m)
#pragma unroll
    for (int s2 = 0; s2 < 2; ++s2)
#pragma unroll
      for (int e = 0; e < 4; ++e) pf[m][s2][e] = pk2(phif(acc[m][0][8 * s2 + 2 * e]), phif(acc[m][0][8 * s2 + 2 * e + 1]));
  bwait(w == 0, kcnt, 32, (LAS unsigned*)(lds + L_BAR + 28), l);
  if (w == 0) STMP(1, 4);
  f4 ks4[4]; float zs;
  {
    const float* src = (const float*)(ws + WS_KVP) + (size_t)hp * 2176 + wc * 1088;
    __amdgpu_buffer_rsrc_t rr = __builtin_amdgcn_make_buffer_rsrc((void*)src, 0, (15u * 16 * 2176 + 1088) * 4, 0x00020000);
    u4 v[8][4]; unsigned zv[8];
#pragma unroll
    for (int pp = 0; pp < 8; ++pp) {
#pragma unroll
      for (int i4 = 0; i4 < 4; ++i4) v[pp][i4] = __builtin_amdgcn_raw_buffer_load_b128(rr, ((wr * 8 + pp) * 16 * 2176 + i4 * 256 + l * 4) * 4, 0, 16);
      zv[pp] = __builtin_amdgcn_raw_buffer_load_b32(rr, ((wr * 8 + pp) * 16 * 2176 + 1024 + l) * 4, 0, 16);
    }
#pragma unroll
    for (int i4 = 0; i4 < 4; ++i4) {
      ks4[i4] = __builtin_bit_cast(f4, v[0][i4]);
#pragma unroll
      for (int pp = 1; pp < 8; ++pp) ks4[i4] += __builtin_bit_cast(f4, v[pp][i4]);
    }
    zs = __uint_as_float(zv[0]);
#pragma unroll
    for (int pp = 1; pp < 8; ++pp) zs += __uint_as_float(zv[pp]);
  }
  float* xb = (float*)(lds + L_KVX) + (wr * 2 + wc) * 1088;
#pragma unroll
  for (int i4 = 0; i4 < 4; ++i4) *(f4*)(xb + i4 * 256 + l * 4) = ks4[i4];
  xb[1024 + l] = zs;
  asm volatile("s_waitcnt lgkmcnt(0)" ::: "memory");
  lds_arrive(barc, l); lds_wait(barc, 4 * 18);
  u4 kA[2], zf[2];
  {
    const float* ob = (const float*)(lds + L_KVX) + ((wr ^ 1) * 2 + wc) * 1088;
#pragma unroll
    for (int i4 = 0; i4 < 4; ++i4) {
      f4 o = *(const f4*)(ob + i4 * 256 + l * 4);
      f4 a = wr ? o + ks4[i4] : ks4[i4] + o;
      kA[i4 >> 1][(i4 & 1) * 2] = pk2(a[0], a[1]); kA[i4 >> 1][(i4 & 1) * 2 + 1] = pk2(a[2], a[3]);
    }
    const float* z0p = (const float*)(lds + L_KVX) + (0 * 2 + wc) * 1088 + 1024 + h * 32;
    const float* z1p = (const float*)(lds + L_KVX) + (1 * 2 + wc) * 1088 + 1024 + h * 32;
#pragma unroll
    for (int q4 = 0; q4 < 4; ++q4) {
      f4 a = *(const f4*)(z0p + q4 * 4) + *(const f4*)(z1p + q4 * 4);
      zf[q4 >> 1][(q4 & 1) * 2] = pk2(a[0], a[1]); zf[q4 >> 1][(q4 & 1) * 2 + 1] = pk2(a[2], a[3]);
    }
  }
  asm volatile("s_waitcnt lgkmcnt(0)" ::: "memory");
  lds_arrive(barc, l);
  __amdgpu_buffer_rsrc_t ors = __builtin_amdgcn_make_buffer_rsrc(ws + WS_PQF, 0, 4u << 20, 0x00020000);
#pragma unroll
  for (int m = 0; m < 2; ++m) {
    f16v X = MFMA32(__builtin_bit_cast(s8, kA[0]), __builtin_bit_cast(s8, pf[m][0]), (f16v)(0.f));
    X = MFMA32(__builtin_bit_cast(s8, kA[1]), __builtin_bit_cast(s8, pf[m][1]), X);
    float den = 0;
#pragma unroll
    for (int e = 0; e < 4; ++e) {
      unsigned a0 = pf[m][0][e], a1 = pf[m][1][e], c0 = zf[0][e], c1 = zf[1][e];
      den = __builtin_amdgcn_fdot2_f32_bf16(__builtin_bit_cast(bf2, a0), __builtin_bit_cast(bf2, c0), den, false);
      den = __builtin_amdgcn_fdot2_f32_bf16(__builtin_bit_cast(bf2, a1), __builtin_bit_cast(bf2, c1), den, false);
    }
    auto rr = __builtin_amdgcn_permlane32_swap(__float_as_uint(den), __float_as_uint(den), false, false);
    den = __uint_as_float(rr[0]) + __uint_as_float(rr[1]);
    den = rbf(rbf(den) + 1e-6f);
    float inv = __builtin_amdgcn_rcpf(den);
    u4 o0, o1;
#pragma unroll
    for (int e = 0; e < 4; ++e) { o0[e] = pk2(X[2 * e] * inv, X[2 * e + 1] * inv); o1[e] = pk2(X[8 + 2 * e] * inv, X[9 + 2 * e] * inv); }
    unsigned dofs = (((mb * 4 + wr * 2 + m) * 32 + head) * 2) * 1024 + l * 16;
    __builtin_amdgcn_raw_buffer_store_b128(o0, ors, dofs, 0, 16);
    __builtin_amdgcn_raw_buffer_store_b128(o1, ors, dofs + 1024, 0, 16);
  }
  asm volatile("s_waitcnt vmcnt(0)" ::: "memory");
  GAS unsigned* ocnt = (GAS unsigned*)(ws + WS_CNT) + (88 + mb) * 32;
  if (l == 0) __hip_atomic_fetch_add(ocnt, 1u, __ATOMIC_RELAXED, __HIP_MEMORY_SCOPE_AGENT);
  if (w == 0) STMP(1, 5);
  lds_wait(barc, 4 * 19);
  {
#pragma unroll
    for (int pc = 0; pc < 4; ++pc)
      GLDS(wovoff, wob + (size_t)((w >> 1) * 512 + 7 * 64 + ((w & 1) * 4 + pc) * 8) * 2048, ldsb + 7 * 16384 + (w >> 1) * 8192 + ((w & 1) * 4 + pc) * 1024);
  }
  const int nb = hp, rt = w;
  const int tok = mb * 128 + rt * 32 + r31;
  const u16* xn = (const u16*)(ws + WS_XN);
  u2 xv[8], nv[8];
#pragma unroll
  for (int i8 = 0; i8 < 8; ++i8) {
    int col = nb * 64 + (i8 >> 2) * 32 + 8 * (i8 & 3) + 4 * h;
    xv[i8] = *(const u2*)(x + (size_t)tok * D + col); nv[i8] = *(const u2*)(xn + (size_t)tok * D + col);
  }
  WAITV(16);
  lds_arrive(barc, l);
  bwait(w == 0, (GAS unsigned*)(ws + WS_CNT) + 120 * 32, 1, (LAS unsigned*)(lds + L_BAR + 36), l);
  int sel = -1; float sg = 0.f;
#pragma unroll
  for (int i = 0; i < 8; ++i) {
    unsigned long long e = __hip_atomic_load((GAS unsigned long long*)(ws + WS_SEL) + i, __ATOMIC_RELAXED, __HIP_MEMORY_SCOPE_AGENT);
    if ((int)(unsigned)e == tok) { sel = i; sg = __uint_as_float((unsigned)(e >> 32)); }
  }
  bwait(w == 0, ocnt, 64, (LAS unsigned*)(lds + L_BAR + 32), l);
  if (w == 0) STMP(1, 6);
  const unsigned abase = ((mb * 4 + rt) * 32) * 2048 + l * 16;
  u4 a[4][8];
#define K3LDA(c) do { _Pragma("unroll") for (int f = 0; f < 8; ++f) \
    a[(c) & 3][f] = __builtin_amdgcn_raw_buffer_load_b128(ors, abase + ((f >> 2) * 16 + 2 * (c) + ((f >> 1) & 1)) * 2048 + (f & 1) * 1024, 0, 16); } while (0)
  K3LDA(0); K3LDA(1); K3LDA(2); K3LDA(3);
  lds_wait(barc, 4 * 20);
  const unsigned woad0 = ldsb + (4 * h + q) * 128 + (((q >> 1) & 1) * 64) + ((l >> 4) & 1) * 32 + p * 8;
  const unsigned woad1 = woad0 ^ 64;
  f16v oc[2];
  oc[0] = (f16v)(0.f); oc[1] = (f16v)(0.f);
#define K3HALF(c, kh_) do { \
    s4 b[16]; \
    const unsigned w0_ = woad0 + ((c) >> 2) * 65536, w1_ = woad1 + ((c) >> 2) * 65536; \
    _Pragma("unroll") for (int hh = 0; hh < 2; ++hh) { \
      RDTR(b[hh * 8 + 0], w0_, ((c) & 3) * 16384 + (kh_) * 8192 + hh * 4096); RDTR(b[hh * 8 + 1], w0_, ((c) & 3) * 16384 + (kh_) * 8192 + hh * 4096 + 1024); \
      RDTR(b[hh * 8 + 2], w1_, ((c) & 3) * 16384 + (kh_) * 8192 + hh * 4096); RDTR(b[hh * 8 + 3], w1_, ((c) & 3) * 16384 + (kh_) * 8192 + hh * 4096 + 1024); \
      RDTR(b[hh * 8 + 4], w0_, ((c) & 3) * 16384 + (kh_) * 8192 + hh * 4096 + 2048); RDTR(b[hh * 8 + 5], w0_, ((c) & 3) * 16384 + (kh_) * 8192 + hh * 4096 + 3072); \
      RDTR(b[hh * 8 + 6], w1_, ((c) & 3) * 16384 + (kh_) * 8192 + hh * 4096 + 2048); RDTR(b[hh * 8 + 7], w1_, ((c) & 3) * 16384 + (kh_) * 8192 + hh * 4096 + 3072); } \
    WAITL(12); SBAR(); \
    oc[0] = MFMA32(CAT(b[0], b[1]), __builtin_bit_cast(s8, a[(c) & 3][(kh_) * 4 + 0]), oc[0]); \
    oc[1] = MFMA32(CAT(b[2], b[3]), __builtin_bit_cast(s8, a[(c) & 3][(kh_) * 4 + 0]), oc[1]); SBAR(); \
    WAITL(8); SBAR(); \
    oc[0] = MFMA32(CAT(b[4], b[5]), __builtin_bit_cast(s8, a[(c) & 3][(kh_) * 4 + 1]), oc[0]); \
    oc[1] = MFMA32(CAT(b[6], b[7]), __builtin_bit_cast(s8, a[(c) & 3][(kh_) * 4 + 1]), oc[1]); SBAR(); \
    WAITL(4); SBAR(); \
    oc[0] = MFMA32(CAT(b[8], b[9]), __builtin_bit_cast(s8, a[(c) & 3][(kh_) * 4 + 2]), oc[0]); \
    oc[1] = MFMA32(CAT(b[10], b[11]), __builtin_bit_cast(s8, a[(c) & 3][(kh_) * 4 + 2]), oc[1]); SBAR(); \
    WAITL(0); SBAR(); \
    oc[0] = MFMA32(CAT(b[12], b[13]), __builtin_bit_cast(s8, a[(c) & 3][(kh_) * 4 + 3]), oc[0]); \
    oc[1] = MFMA32(CAT(b[14], b[15]), __builtin_bit_cast(s8, a[(c) & 3][(kh_) * 4 + 3]), oc[1]); SBAR(); } while (0)
#define K3C(c) do { K3HALF(c, 0); K3HALF(c, 1); if ((c) < 4) { K3LDA((c) + 4); } SBAR(); } while (0)
  K3C(0); K3C(1); K3C(2); K3C(3); K3C(4); K3C(5); K3C(6); K3C(7);
  if (w == 0) STMP(1, 7);
  const bool anysel = __ballot(sel >= 0) != 0ull;
  if (anysel) gwait((GAS unsigned*)(ws + WS_CNT) + (104 + nb) * 32, 56);
  __amdgpu_buffer_rsrc_t yrs = __builtin_amdgcn_make_buffer_rsrc(ws + WS_YP, 0, 14 * 8 * D * 4, 0x00020000);
  u2 ovs[8];
#pragma unroll
  for (int i8 = 0; i8 < 8; ++i8) {
    const f16v& oo = oc[i8 >> 2];
    const int i4 = i8 & 3;
    int col = nb * 64 + (i8 >> 2) * 32 + 8 * i4 + 4 * h;
    float av[4] = {oo[4 * i4], oo[4 * i4 + 1], oo[4 * i4 + 2], oo[4 * i4 + 3]};
    float b[4] = {lo(nv[i8][0]), hi(nv[i8][0]), lo(nv[i8][1]), hi(nv[i8][1])};
    float xx[4] = {lo(xv[i8][0]), hi(xv[i8][0]), lo(xv[i8][1]), hi(xv[i8][1])};
    if (sel >= 0) {
      f4 y = (f4)(0.f);
#pragma unroll
      for (int ks = 0; ks < 14; ++ks) y += __builtin_bit_cast(f4, __builtin_amdgcn_raw_buffer_load_b128(yrs, ((ks * 8 + sel) * D + col) * 4, 0, 16));
#pragma unroll
      for (int e = 0; e < 4; ++e) b[e] = rbf(b[e] + rbf(rbf(y[e]) * sg));
    }
    u2 ov;
#pragma unroll
    for (int e = 0; e < 4; ++e) av[e] = rbf(xx[e] + rbf(av[e])) + b[e];
    ov[0] = pk2(av[0], av[1]); ov[1] = pk2(av[2], av[3]);
    ovs[i8] = ov;
  }
#pragma unroll
  for (int k2 = 0; k2 < 4; ++k2) {
    auto r0 = __builtin_amdgcn_permlane32_swap(ovs[2 * k2][0], ovs[2 * k2 + 1][0], false, false);
    auto r1 = __builtin_amdgcn_permlane32_swap(ovs[2 * k2][1], ovs[2 * k2 + 1][1], false, false);
    u4 o4; o4[0] = r0[0]; o4[1] = r1[0]; o4[2] = r0[1]; o4[3] = r1[1];
    __builtin_amdgcn_raw_buffer_store_b128(o4, __builtin_amdgcn_make_buffer_rsrc(out, 0, 4u << 20, 0x00020000), (tok * D + nb * 64 + 16 * k2 + 8 * h) * 2, 0, 16);
  }
  { unsigned long long t0_ = *(const unsigned long long*)(ws + (33u<<20)); while (__builtin_amdgcn_s_memrealtime() - t0_ < 6000ull) __builtin_amdgcn_s_sleep(1); }
  asm volatile(".Lk2_code_end:");
}

extern "C" void kernel_launch(void* const* d_in, const int* in_sizes, int n_in, void* d_out, int out_size,
                              void* d_ws, size_t ws_size, hipStream_t stream) {
  const u16* x = (const u16*)d_in[0];
  const u16* wqkv = (const u16*)d_in[1];
  const u16* wo = (const u16*)d_in[2];
  const u16* wr = (const u16*)d_in[4];
  const u16* wg = (const u16*)d_in[5];
  const u16* wu = (const u16*)d_in[6];
  const u16* wd = (const u16*)d_in[7];
  char* ws = (char*)d_ws;
  (void)hipFuncSetAttribute((const void*)k2, hipFuncAttributeMaxDynamicSharedMemorySize, K2_LDS);
  k1_ln<<<256, 256, 0, stream>>>(x, wr, ws, wqkv, wo);
  k2<<<256, 512, K2_LDS, stream>>>(wqkv, wg, wu, wd, ws, x, wo, (u16*)d_out);
}
```

```cpp
#include <hip/hip_runtime.h>
#include <hip/hip_bf16.h>
#include <stdint.h>

typedef unsigned short u16;
typedef short s4 __attribute__((ext_vector_type(4)));
typedef short s8 __attribute__((ext_vector_type(8)));
typedef float f16v __attribute__((ext_vector_type(16)));
typedef float f4 __attribute__((ext_vector_type(4)));
typedef float f2 __attribute__((ext_vector_type(2)));
typedef unsigned u4 __attribute__((ext_vector_type(4)));
typedef unsigned u2 __attribute__((ext_vector_type(2)));
typedef __bf16 bf2 __attribute__((ext_vector_type(2)));
#define LAS __attribute__((address_space(3)))
#define GAS __attribute__((address_space(1)))
#define DEV __device__ __forceinline__

constexpr int S = 2048, D = 1024, F = 3584;
template <int N> struct ic { static constexpr int value = N; };

constexpr size_t WS_XN = 0;
constexpr size_t WS_PQF = 4u << 20;
constexpr size_t WS_KVP = 8u << 20;
constexpr size_t WS_GP = (10u << 20) + (256u << 10);
constexpr size_t WS_YP = WS_GP + (896u << 10);
constexpr size_t WS_H = WS_YP + (448u << 10);
constexpr size_t WS_R = WS_H + (64u << 10);
constexpr size_t WS_SEL = WS_R + (8u << 10);
constexpr size_t WS_CNT = WS_SEL + 4096;

DEV float bf2f(u16 v) { return __uint_as_float(((unsigned)v) << 16); }
DEV unsigned pk2(float a, float b) { bf2 v; v[0] = (__bf16)a; v[1] = (__bf16)b; return __builtin_bit_cast(unsigned, v); }
DEV float lo(unsigned u) { return __uint_as_float(u << 16); }
DEV float hi(unsigned u) { return __uint_as_float(u & 0xFFFF0000u); }
DEV float rbf(float a) { return lo(pk2(a, a)); }
DEV float phif(float t) {
  float e = __builtin_amdgcn_fmed3f(__builtin_amdgcn_exp2f(t * 1.44269504f), 0.f, 1.f);
  return (e + 1e-6f) + fmaxf(t, 0.f);
}
#define RD128(dst, addr, OFF) asm volatile("ds_read_b128 %0, %1 offset:%2" : "=v"(dst) : "v"(addr), "i"(OFF) : "memory")
#define RDTR(dst, addr, OFF) asm volatile("ds_read_b64_tr_b16 %0, %1 offset:%2" : "=v"(dst) : "v"(addr), "i"(OFF) : "memory")
#define SBAR() __builtin_amdgcn_sched_barrier(0)
#define CAT(x, y) __builtin_shufflevector(x, y, 0, 1, 2, 3, 4, 5, 6, 7)
#define WAITV(n) asm volatile("s_waitcnt vmcnt(%0)" ::"i"(n) : "memory")
#define WAITL(n) asm volatile("s_waitcnt lgkmcnt(%0)" ::"i"(n) : "memory")
DEV const char* uptr(const char* p) {
  unsigned long long v = (unsigned long long)p;
  unsigned lo_ = __builtin_amdgcn_readfirstlane((unsigned)v), hi_ = __builtin_amdgcn_readfirstlane((unsigned)(v >> 32));
  return (const char*)(((unsigned long long)hi_ << 32) | lo_);
}
#define GLDS(voff, sbase, m0v) asm volatile("s_mov_b32 m0, %2\n s_nop 0\n global_load_lds_dwordx4 %0, %1" ::"v"(voff), "s"(uptr(sbase)), "s"(__builtin_amdgcn_readfirstlane(m0v)) : "memory")
#define MFMA32(a, b, c) __builtin_amdgcn_mfma_f32_32x32x16_bf16(a, b, c, 0, 0, 0)
#define MFMA16(a, b, c) __builtin_amdgcn_mfma_f32_16x16x32_bf16(a, b, c, 0, 0, 0)

DEV void lds_arrive(LAS unsigned* c, int l) {
  if (l == 0) __hip_atomic_fetch_add(c, 1u, __ATOMIC_RELAXED, __HIP_MEMORY_SCOPE_WORKGROUP);
}
DEV void lds_wait(LAS unsigned* c, unsigned target) {
  while (__builtin_amdgcn_readfirstlane(__hip_atomic_load(c, __ATOMIC_RELAXED, __HIP_MEMORY_SCOPE_WORKGROUP)) < target) {}
  asm volatile("" ::: "memory");
}
DEV void lds_wait_s(LAS unsigned* c, unsigned target) {
  while (__builtin_amdgcn_readfirstlane(__hip_atomic_load(c, __ATOMIC_RELAXED, __HIP_MEMORY_SCOPE_WORKGROUP)) < target) __builtin_amdgcn_s_sleep(4);
  asm volatile("" ::: "memory");
}
DEV void bwait(bool leader, GAS unsigned* c, unsigned target, LAS unsigned* f, int l) {
  if (leader) {
    for (unsigned it_ = 0; it_ < (1u << 22) && (unsigned)__builtin_amdgcn_readfirstlane(__hip_atomic_load(c, __ATOMIC_RELAXED, __HIP_MEMORY_SCOPE_AGENT)) < target; ++it_) __builtin_amdgcn_s_sleep(8);
    asm volatile("" ::: "memory");
    lds_arrive(f, l);
  } else lds_wait_s(f, 1);
}
DEV void gwait(GAS unsigned* c, unsigned target) {
  for (unsigned it_ = 0; it_ < (1u << 22) && (unsigned)__builtin_amdgcn_readfirstlane(__hip_atomic_load(c, __ATOMIC_RELAXED, __HIP_MEMORY_SCOPE_AGENT)) < target; ++it_) __builtin_amdgcn_s_sleep(2);
  asm volatile("" ::: "memory");
}

#define STMP(kid, idx)
#define STMC(kid, idx)
#define WSS ws
DEV float wave_sum(float v) {
  v += __int_as_float(__builtin_amdgcn_update_dpp(0, __float_as_int(v), 0x111, 0xf, 0xf, false));
  v += __int_as_float(__builtin_amdgcn_update_dpp(0, __float_as_int(v), 0x112, 0xf, 0xf, false));
  v += __int_as_float(__builtin_amdgcn_update_dpp(0, __float_as_int(v), 0x114, 0xf, 0xf, false));
  v += __int_as_float(__builtin_amdgcn_update_dpp(0, __float_as_int(v), 0x118, 0xf, 0xf, false));
  v += __int_as_float(__builtin_amdgcn_update_dpp(0, __float_as_int(v), 0x142, 0xa, 0xf, false));
  v += __int_as_float(__builtin_amdgcn_update_dpp(0, __float_as_int(v), 0x143, 0xc, 0xf, false));
  return __int_as_float(__builtin_amdgcn_readlane(__float_as_int(v), 63));
}
__global__ __launch_bounds__(256) void k1_ln(const u16* __restrict__ x, const u16* __restrict__ wr, char* __restrict__ ws, const u16* __restrict__ wqkv, const u16* __restrict__ wo) {
  u16* xn = (u16*)(ws + WS_XN);
  float* r = (float*)(ws + WS_R);
  int w = threadIdx.x >> 6, l = threadIdx.x & 63;
  if (blockIdx.x == 0 && threadIdx.x == 0) *(unsigned long long*)(ws + (33u<<20)) = __builtin_amdgcn_s_memrealtime();
  if (blockIdx.x == 0 && threadIdx.x < 128) ((unsigned*)(ws + WS_CNT))[threadIdx.x * 32] = 0u;
  u4 a[2], b[2];
#pragma unroll
  for (int i = 0; i < 2; ++i) {
    int row = blockIdx.x * 8 + w * 2 + i;
    a[i] = *(const u4*)(x + row * D + l * 8); b[i] = *(const u4*)(x + row * D + 512 + l * 8);
  }
  u4 wv0 = *(const u4*)(wr + l * 8), wv1 = *(const u4*)(wr + 512 + l * 8);
#pragma unroll
  for (int i = 0; i < 2; ++i) {
    int row = blockIdx.x * 8 + w * 2 + i;
    float v[16];
#pragma unroll
    for (int j = 0; j < 4; ++j) { v[2*j] = lo(a[i][j]); v[2*j+1] = hi(a[i][j]); v[8+2*j] = lo(b[i][j]); v[9+2*j] = hi(b[i][j]); }
    float s = 0;
#pragma unroll
    for (int j = 0; j < 16; ++j) s += v[j];
    s = wave_sum(s);
    float m = s * (1.f / D), q = 0;
#pragma unroll
    for (int j = 0; j < 16; ++j) { v[j] -= m; q += v[j] * v[j]; }
    q = wave_sum(q);
    float rs = 1.f / sqrtf(q * (1.f / D) + 1e-5f);
    u4 oa, ob; float d = 0;
#pragma unroll
    for (int j = 0; j < 4; ++j) {
      oa[j] = pk2(v[2*j] * rs, v[2*j+1] * rs); ob[j] = pk2(v[8+2*j] * rs, v[9+2*j] * rs);
      d += lo(oa[j]) * lo(wv0[j]) + hi(oa[j]) * hi(wv0[j]) + lo(ob[j]) * lo(wv1[j]) + hi(ob[j]) * hi(wv1[j]);
    }
    d = wave_sum(d);
    *(u4*)(xn + row * D + l * 8) = oa; *(u4*)(xn + row * D + 512 + l * 8) = ob;
    if (l == 0) r[row] = rbf(d);
  }
  if (w == 0) STMP(0, 1);
}

constexpr int K2_STG = 40960;
constexpr int L_KVX = 3 * K2_STG;
constexpr int L_RED1 = L_KVX + 17408;
constexpr int L_TOP = L_RED1 + 16384;
constexpr int L_BAR = L_TOP + 256;
constexpr int K2_LDS = L_BAR + 64;

DEV unsigned wave_max(unsigned v) {
  v = max(v, (unsigned)__builtin_amdgcn_update_dpp(0, (int)v, 0x111, 0xf, 0xf, false));
  v = max(v, (unsigned)__builtin_amdgcn_update_dpp(0, (int)v, 0x112, 0xf, 0xf, false));
  v = max(v, (unsigned)__builtin_amdgcn_update_dpp(0, (int)v, 0x114, 0xf, 0xf, false));
  v = max(v, (unsigned)__builtin_amdgcn_update_dpp(0, (int)v, 0x118, 0xf, 0xf, false));
  v = max(v, (unsigned)__builtin_amdgcn_update_dpp(0, (int)v, 0x142, 0xa, 0xf, false));
  v = max(v, (unsigned)__builtin_amdgcn_update_dpp(0, (int)v, 0x143, 0xc, 0xf, false));
  return (unsigned)__builtin_amdgcn_readlane((int)v, 63);
}

struct FR { s8 a0, a1; s4 b[6]; };
#define LWM lds_wait
#define NTL(p) __builtin_nontemporal_load(p)

DEV void k2_moe(char* lds, char* ws, const u16* __restrict__ wg, const u16* __restrict__ wu, const u16* __restrict__ wd, int mw, int l, const u16* __restrict__ wq_) {
  if (blockIdx.x >= 224) return;
  const int u = blockIdx.x;
  const u16* xn = (const u16*)(ws + WS_XN);
  const float* r = (const float*)(ws + WS_R);
  LAS unsigned* barm = (LAS unsigned*)(lds + L_BAR + 4);
  const int np = l & 15, kq = l >> 4;
  const int chunk = u >> 2, ksp = u & 3;
  float rv[8];
#pragma unroll
  for (int i = 0; i < 8; ++i) rv[i] = r[mw * 512 + i * 64 + l];
  u4 gw[2][8];
  {
    const u16* wp = ((np >> 3) ? wu : wg) + (size_t)(256 * ksp + 64 * mw + 8 * kq) * F + 64 * chunk + 8 * (np & 7);
#pragma unroll
    for (int it = 0; it < 2; ++it)
#pragma unroll
      for (int rr = 0; rr < 8; ++rr) gw[it][rr] = NTL((const u4*)(wp + (size_t)(32 * it + rr) * F));
  }
  const int ks = u >> 4, cc = u & 15, kset = np >> 3;
  u4 dw[8];
  {
    const u16* dp = wd + (size_t)(256 * ks + 64 * mw + 32 * kset + 8 * kq) * D + 64 * cc + 8 * (np & 7);
#pragma unroll
    for (int rr = 0; rr < 8; ++rr) dw[rr] = NTL((const u4*)(dp + (size_t)rr * D));
  }
  if (mw == 0) STMP(1, 8);
  unsigned key[8];
#pragma unroll
  for (int i = 0; i < 8; ++i) {
    unsigned b = __float_as_uint(rv[i]) >> 16;
    unsigned mono = (b & 0x8000u) ? (~b & 0xFFFFu) : (b | 0x8000u);
    key[i] = (mono << 16) | (unsigned)(0xFFFF - (mw * 512 + i * 64 + l));
  }
  unsigned* cand = (unsigned*)(lds + L_TOP);
  int* sel = (int*)(lds + L_TOP + 128) + mw * 8;
#pragma unroll 1
  for (int it = 0; it < 8; ++it) {
    unsigned m = max(max(max(key[0], key[1]), max(key[2], key[3])), max(max(key[4], key[5]), max(key[6], key[7])));
    m = wave_max(m);
#pragma unroll
    for (int i = 0; i < 8; ++i) key[i] = key[i] == m ? 0u : key[i];
    if (l == 0) cand[mw * 8 + it] = m;
  }
  asm volatile("s_waitcnt lgkmcnt(0)" ::: "memory");
  lds_arrive(barm, l); lds_wait_s(barm, 4);
  {
    unsigned mine = cand[l & 31];
    int rank = 0;
#pragma unroll
    for (int j = 0; j < 32; ++j) rank += ((unsigned)__builtin_amdgcn_readlane((int)mine, j) > mine) ? 1 : 0;
    if (l < 32 && rank < 8) sel[rank] = 0xFFFF - (int)(mine & 0xFFFF);
  }
  int idx = sel[l & 7];
  if (mw == 0) STMP(1, 9);
  if (u == 0 && mw == 0) {
    if (l < 8) {
      float rr = r[idx];
      float sg = rbf(1.f / (1.f + __expf(-rr)));
      __hip_atomic_store((GAS unsigned long long*)(ws + WS_SEL) + l, ((unsigned long long)__float_as_uint(sg) << 32) | (unsigned)idx, __ATOMIC_RELAXED, __HIP_MEMORY_SCOPE_AGENT);
    }
    asm volatile("s_waitcnt vmcnt(0)" ::: "memory");
    if (l == 0) __hip_atomic_fetch_add((GAS unsigned*)(ws + WS_CNT) + 120 * 32, 1u, __ATOMIC_RELAXED, __HIP_MEMORY_SCOPE_AGENT);
  }
  f4 acc[8];
#pragma unroll
  for (int c = 0; c < 8; ++c) acc[c] = (f4)(0.f);
  {
    const u16* xp = xn + (size_t)idx * D + 256 * ksp + 64 * mw + 8 * kq;
    u4 xa[2];
#pragma unroll
    for (int it = 0; it < 2; ++it) xa[it] = *(const u4*)(xp + 32 * it);
#pragma unroll
    for (int it = 0; it < 2; ++it) {
#pragma unroll
      for (int c = 0; c < 8; ++c) {
        u4 f;
#pragma unroll
        for (int q = 0; q < 4; ++q) {
          unsigned e = gw[it][2 * q][c >> 1], o = gw[it][2 * q + 1][c >> 1];
          f[q] = (c & 1) ? __builtin_amdgcn_perm(o, e, 0x07060302u) : __builtin_amdgcn_perm(o, e, 0x05040100u);
        }
        acc[c] = MFMA16(__builtin_bit_cast(s8, xa[it]), __builtin_bit_cast(s8, f), acc[c]);
      }
    }
  }
  if (mw == 0) STMP(1, 10);
  float* red1 = (float*)(lds + L_RED1);
  if (kq < 2) {
#pragma unroll
    for (int e = 0; e < 4; ++e) {
      float* p = red1 + ((mw * 8 + 4 * kq + e) * 2 + (np >> 3)) * 64 + 8 * (np & 7);
      *(f4*)p = (f4){acc[0][e], acc[1][e], acc[2][e], acc[3][e]};
      *(f4*)(p + 4) = (f4){acc[4][e], acc[5][e], acc[6][e], acc[7][e]};
    }
  }
  asm volatile("s_waitcnt lgkmcnt(0)" ::: "memory");
  lds_arrive(barm, l); lds_wait_s(barm, 8);
  GAS unsigned* gcnt = (GAS unsigned*)(ws + WS_CNT) + (32 + chunk) * 32;
  GAS unsigned* hcnt = (GAS unsigned*)(ws + WS_CNT) + (16 + (chunk >> 2)) * 32;
  float* gpart = (float*)(ws + WS_GP) + (size_t)chunk * 4096;
  __amdgpu_buffer_rsrc_t grs = __builtin_amdgcn_make_buffer_rsrc(gpart, 0, 16384, 0x00020000);
  unsigned* tick = (unsigned*)(lds + L_TOP + 192);
  {
    int tm = mw * 64 + l;
    f4 sacc = *(const f4*)(red1 + tm * 4) + *(const f4*)(red1 + 1024 + tm * 4) + *(const f4*)(red1 + 2048 + tm * 4) + *(const f4*)(red1 + 3072 + tm * 4);
    __builtin_amdgcn_raw_buffer_store_b128(__builtin_bit_cast(u4, sacc), grs, (ksp * 1024 + tm * 4) * 4, 0, 16);
    asm volatile("s_waitcnt vmcnt(0)" ::: "memory");
  }
  lds_arrive(barm, l);
  if (mw == 0) {
    lds_wait_s(barm, 12);
    unsigned tk = 0;
    if (l == 0) tk = __hip_atomic_fetch_add(gcnt, 1u, __ATOMIC_RELAXED, __HIP_MEMORY_SCOPE_AGENT);
    tk = __builtin_amdgcn_readfirstlane(tk);
    if (tk == 3) {
      int tok = l >> 3, c8 = (l & 7) * 8;
      u4 pv[4][4];
#pragma unroll
      for (int p = 0; p < 4; ++p)
#pragma unroll
        for (int q = 0; q < 4; ++q)
          pv[p][q] = __builtin_amdgcn_raw_buffer_load_b128(grs, (p * 1024 + (tok * 2 + (q >> 1)) * 64 + c8 + (q & 1) * 4) * 4, 0, 16);
      f4 gq[4];
#pragma unroll
      for (int q = 0; q < 4; ++q) gq[q] = (__builtin_bit_cast(f4, pv[0][q]) + __builtin_bit_cast(f4, pv[1][q])) + (__builtin_bit_cast(f4, pv[2][q]) + __builtin_bit_cast(f4, pv[3][q]));
      float hv[8];
#pragma unroll
      for (int e = 0; e < 8; ++e) {
        float gg = rbf(gq[e >> 2][e & 3]), u1 = rbf(gq[2 + (e >> 2)][e & 3]);
        float sg = rbf(gg / (1.f + __expf(-gg)));
        hv[e] = sg * u1;
      }
      u4 ho; ho[0] = pk2(hv[0], hv[1]); ho[1] = pk2(hv[2], hv[3]); ho[2] = pk2(hv[4], hv[5]); ho[3] = pk2(hv[6], hv[7]);
      __amdgpu_buffer_rsrc_t hrs = __builtin_amdgcn_make_buffer_rsrc(ws + WS_H, 0, 8 * F * 2, 0x00020000);
      __builtin_amdgcn_raw_buffer_store_b128(ho, hrs, (tok * F + 64 * chunk + c8) * 2, 0, 16);
      asm volatile("s_waitcnt vmcnt(0)" ::: "memory");
      if (l == 0) __hip_atomic_fetch_add(hcnt, 1u, __ATOMIC_RELAXED, __HIP_MEMORY_SCOPE_AGENT);
    }
  }
  if (mw == 0) STMP(1, 11);
  GAS unsigned* hcnt2 = (GAS unsigned*)(ws + WS_CNT) + (16 + ks) * 32;
  bwait(mw == 0, hcnt2, 4, (LAS unsigned*)(lds + L_BAR + 24), l);
  if (mw == 0) STMP(1, 12);
  f4 acd[8];
  {
    __amdgpu_buffer_rsrc_t hrs = __builtin_amdgcn_make_buffer_rsrc(ws + WS_H, 0, 8 * F * 2, 0x00020000);
    u4 ha = __builtin_amdgcn_raw_buffer_load_b128(hrs, ((l & 7) * F + 256 * ks + 64 * mw + 32 * ((l >> 3) & 1) + 8 * kq) * 2, 0, 16);
#pragma unroll
    for (int c = 0; c < 8; ++c) {
      u4 f;
#pragma unroll
      for (int q = 0; q < 4; ++q) {
        unsigned e = dw[2 * q][c >> 1], o = dw[2 * q + 1][c >> 1];
        f[q] = (c & 1) ? __builtin_amdgcn_perm(o, e, 0x07060302u) : __builtin_amdgcn_perm(o, e, 0x05040100u);
      }
      acd[c] = MFMA16(__builtin_bit_cast(s8, ha), __builtin_bit_cast(s8, f), (f4)(0.f));
    }
  }
  float* red2 = (float*)(lds + L_RED1);
  lds_wait_s(barm, 12);
  if ((kq >> 1) == kset) {
#pragma unroll
    for (int e = 0; e < 4; ++e) {
      float* p = red2 + ((mw * 2 + kset) * 8 + 4 * (kq & 1) + e) * 64 + 8 * (np & 7);
      *(f4*)p = (f4){acd[0][e], acd[1][e], acd[2][e], acd[3][e]};
      *(f4*)(p + 4) = (f4){acd[4][e], acd[5][e], acd[6][e], acd[7][e]};
    }
  }
  asm volatile("s_waitcnt lgkmcnt(0)" ::: "memory");
  lds_arrive(barm, l); lds_wait_s(barm, 16);
  {
    int tm = mw * 64 + l, tok = tm >> 5, c2 = (tm & 31) * 2;
    f2 sacc = (f2)(0.f);
#pragma unroll
    for (int p = 0; p < 8; ++p) sacc += *(const f2*)(red2 + (p * 8 + tok) * 64 + c2);
    __hip_atomic_store((GAS unsigned long long*)((float*)(ws + WS_YP) + (size_t)(ks * 8 + tok) * D + 64 * cc + c2), __builtin_bit_cast(unsigned long long, sacc), __ATOMIC_RELAXED, __HIP_MEMORY_SCOPE_AGENT);
    asm volatile("s_waitcnt vmcnt(0)" ::: "memory");
    if (l == 0) __hip_atomic_fetch_add((GAS unsigned*)(ws + WS_CNT) + (104 + cc) * 32, 1u, __ATOMIC_RELAXED, __HIP_MEMORY_SCOPE_AGENT);
  }
  if (mw == 0) STMP(1, 13);
  asm volatile(".Lk2_moe_end:");
}

__global__ __launch_bounds__(512) void k2(const u16* __restrict__ wqkv, const u16* __restrict__ wg, const u16* __restrict__ wu,
                                          const u16* __restrict__ wd, char* __restrict__ ws, const u16* __restrict__ x,
                                          const u16* __restrict__ wo, u16* __restrict__ out) {
  extern __shared__ __attribute__((aligned(16))) char lds[];
  const int t0 = threadIdx.x, w = __builtin_amdgcn_readfirstlane(t0 >> 6), l = t0 & 63;
  if (t0 < 16) ((unsigned*)(lds + L_BAR))[t0] = 0u;
  __syncthreads();
  const unsigned long long tb0_ = __builtin_amdgcn_s_memrealtime();
  asm volatile(".Lk2_code_beg:");
  if (w == 7) {
    unsigned long long pc; unsigned len, off;
    asm volatile("s_getpc_b64 %0\n.Lk2_here:" : "=s"(pc));
    asm volatile("s_mov_b32 %0, (.Lk2_here - .Lk2_code_beg)" : "=s"(off));
    asm volatile("s_mov_b32 %0, (.Lk2_code_end - .Lk2_code_beg)" : "=s"(len));
    int len2;
    asm volatile("s_mov_b32 %0, (.Lk2_moe_end - .Lk2_code_beg)" : "=s"(len2));
    if (len2 > (int)len) len = (unsigned)len2;
    pc -= off;
    unsigned acc_ = 0;
    for (unsigned o = l * 64; o < len; o += 4096) acc_ ^= *(const unsigned*)((const char*)pc + o);
    asm volatile("" ::"v"(acc_));
  }
  if (w >= 4) {
    k2_moe(lds, ws, wg, wu, wd, w - 4, l, wqkv);
    return;
  }
  const int t = t0, wr = w >> 1, wc = w & 1, r31 = l & 31, h = l >> 5;
  const int g = blockIdx.x, xcd = g & 7, j = g >> 3;
  const int mb = (xcd & 1) * 8 + (j >> 2), hp = (xcd >> 1) * 4 + (j & 3);
  LAS unsigned* barc = (LAS unsigned*)(lds + L_BAR);
  unsigned aoffv, boffv;
  { int row = t >> 3, slot = t & 7; aoffv = row * 2048 + ((slot ^ ((row >> 1) & 7)) * 16); boffv = row * 6144 + ((slot ^ (((row >> 1) & 1) << 2)) * 16); }
  const char* Ab = ws + WS_XN + (size_t)mb * 128 * 2048;
  const char* Bb = (const char*)wqkv + hp * 128;
  const unsigned ldsb = (unsigned)(size_t)(LAS char*)lds;
  const unsigned m0base = ldsb + w * 1024;
  const int rot = 0;
  auto gl = [&](int kt_, int buf, int i) {
    const int kt = (kt_ + rot) & 15;
    unsigned m0v = m0base + buf * K2_STG;
    if (i < 4) { const char* sb = Ab + kt * 128 + i * 65536; GLDS(aoffv, sb, m0v + i * 4096); }
    else { int ii = i - 4; const char* sb = Bb + (size_t)(kt * 64 + (ii & 1) * 32) * 6144 + (ii >> 1) * 2048; GLDS(boffv, sb, m0v + 16384 + ii * 4096); }
  };
  unsigned aad[4];
  { int aoff = (wr * 64 + r31) * 128, asw = (r31 >> 1) & 7;
#pragma unroll
    for (int s = 0; s < 4; ++s) aad[s] = ldsb + aoff + (((2 * s + h) ^ asw) * 16); }
  const int q = (l & 15) >> 2, p = l & 3;
  const unsigned bad = ldsb + 16384 + (8 * h + q) * 128 + ((wc ^ (q >> 1)) * 64) + ((l >> 4) & 1) * 32 + p * 8;
  f16v acc[2][3];
#pragma unroll
  for (int m = 0; m < 2; ++m)
#pragma unroll
    for (int n = 0; n < 3; ++n) acc[m][n] = (f16v)(0.f);
#define MF(m, n, Fq) do { if (n == 0) acc[m][0] = MFMA32(CAT(Fq.b[0], Fq.b[1]), (m ? Fq.a1 : Fq.a0), acc[m][0]); \
     else acc[m][n] = MFMA32((m ? Fq.a1 : Fq.a0), CAT(Fq.b[2 * n], Fq.b[2 * n + 1]), acc[m][n]); } while (0)
#define GL(i) do { if (dost_) gl(kt + 2, sbuf, (i)); } while (0)
#define ARRIVE(addr) asm volatile("s_mov_b64 exec, 1\n ds_add_u32 %0, %1\n s_mov_b64 exec, -1" ::"v"(addr), "v"(onev) : "memory")
#define POLL(dst, addr) asm volatile("ds_read_b32 %0, %1" : "=v"(dst) : "v"(addr) : "memory")
#define KEEP(v) asm volatile("" ::"v"(v))
#define CHECK(v, c, target) do { if ((unsigned)__builtin_amdgcn_readfirstlane(v) < (unsigned)(target)) lds_wait(c, target); } while (0)
#define SUB(Fq, G, va, vb, SS, G0, CHK) do { \
    WAITL(5); SBAR(); \
    MF(0, 0, Fq); SBAR(); RD128(G.a0, va, 0); RDTR(G.b[0], vb, SS * 2048); RDTR(G.b[1], vb, SS * 2048 + 512); RDTR(G.b[2], vb, 8192 + SS * 2048); RDTR(G.b[3], vb, 8192 + SS * 2048 + 512); SBAR(); \
    WAITL(8); SBAR(); \
    MF(0, 1, Fq); SBAR(); RD128(G.a1, va, 4096); RDTR(G.b[4], vb, 16384 + SS * 2048); RDTR(G.b[5], vb, 16384 + SS * 2048 + 512); SBAR(); \
    WAITL(8); SBAR(); \
    MF(0, 2, Fq); SBAR(); if (CHK) { KEEP(vB); if (dost_) CHECK(vB, barB, 4 * kt); } GL(G0); SBAR(); \
    MF(1, 0, Fq); SBAR(); GL(G0 + 1); SBAR(); MF(1, 1, Fq); SBAR(); GL(G0 + 2); SBAR(); MF(1, 2, Fq); SBAR(); } while (0)
#define KSTEP(DOST, LAST) do { \
    constexpr bool dost_ = DOST; \
    int nbuf = buf == 2 ? 0 : buf + 1, sbuf = buf == 0 ? 2 : buf - 1; \
    unsigned bo = buf * K2_STG, nbo = nbuf * K2_STG; \
    unsigned vb = bad + bo; \
    { unsigned va = aad[1] + bo; SUB(f0, f1, va, vb, 1, 0, true); } \
    { unsigned va = aad[2] + bo; SUB(f1, f0, va, vb, 2, 3, false); } \
    if (!LAST) { if (dost_) WAITV(6); else WAITV(0); ARRIVE(adA); SBAR(); } \
    { unsigned va = aad[3] + bo; SUB(f0, f1, va, vb, 3, 6, false); } \
    GL(9); \
    if (!LAST) { \
      POLL(vA, adA); \
      WAITL(0); SBAR(); \
      ARRIVE(adB); \
      MF(0, 0, f1); SBAR(); \
      CHECK(vA, barc, 4 * (kt + 2)); \
      SBAR(); \
      unsigned va = aad[0] + nbo, vn = bad + nbo; \
      RD128(f0.a0, va, 0); RDTR(f0.b[0], vn, 0); RDTR(f0.b[1], vn, 512); RDTR(f0.b[2], vn, 8192); RDTR(f0.b[3], vn, 8192 + 512); SBAR(); \
      MF(0, 1, f1); SBAR(); \
      RD128(f0.a1, va, 4096); RDTR(f0.b[4], vn, 16384); RDTR(f0.b[5], vn, 16384 + 512); SBAR(); \
      MF(0, 2, f1); MF(1, 0, f1); MF(1, 1, f1); SBAR(); \
      POLL(vB, adB); SBAR(); \
      MF(1, 2, f1); SBAR(); \
    } else { WAITL(0); SBAR(); MF(0, 0, f1); MF(0, 1, f1); MF(0, 2, f1); MF(1, 0, f1); MF(1, 1, f1); MF(1, 2, f1); } \
    buf = nbuf; } while (0)
  const unsigned adA = ldsb + L_BAR, adB = ldsb + L_BAR + 8, onev = 1u;
  LAS unsigned* barB = (LAS unsigned*)(lds + L_BAR + 8);
  unsigned vA, vB = 0;
#pragma unroll
  for (int s = 0; s < 2; ++s)
#pragma unroll
    for (int i = 0; i < 10; ++i) gl(s, s, i);
  WAITV(10);
  lds_arrive(barc, l); lds_wait(barc, 4);
  const unsigned long long tb1_ = __builtin_amdgcn_s_memrealtime();
  FR f0, f1;
  { unsigned va = aad[0], vb = bad;
    RD128(f0.a0, va, 0); RDTR(f0.b[0], vb, 0); RDTR(f0.b[1], vb, 512); RDTR(f0.b[2], vb, 8192); RDTR(f0.b[3], vb, 8192 + 512); RD128(f0.a1, va, 4096); RDTR(f0.b[4], vb, 16384); RDTR(f0.b[5], vb, 16384 + 512);
    SBAR(); }
  int buf = 0;
  int kt = 0;
#pragma unroll 1
  for (; kt < 14; ++kt) KSTEP(true, false);
  KSTEP(false, false);
  ++kt;
  KSTEP(false, true);
  if (w == 0) { STMP(1, 2); STMC(1, 41); }
  const int head = hp * 2 + wc;
  f16v kv = (f16v)(0.f), zz16 = (f16v)(0.f);
  const u4 ones = (u4){0x3F803F80u, 0x3F803F80u, 0x3F803F80u, 0x3F803F80u};
#pragma unroll
  for (int m = 0; m < 2; ++m) {
#pragma unroll
    for (int s2 = 0; s2 < 2; ++s2) {
      u4 kf, vf;
#pragma unroll
      for (int e = 0; e < 4; ++e) {
        kf[e] = pk2(phif(acc[m][1][8 * s2 + 2 * e]), phif(acc[m][1][8 * s2 + 2 * e + 1]));
        vf[e] = pk2(acc[m][2][8 * s2 + 2 * e], acc[m][2][8 * s2 + 2 * e + 1]);
      }
      kv = MFMA32(__builtin_bit_cast(s8, kf), __builtin_bit_cast(s8, vf), kv);
      zz16 = MFMA32(__builtin_bit_cast(s8, kf), __builtin_bit_cast(s8, ones), zz16);
    }
  }
  float z;
  { float t0_ = zz16[0];
#pragma unroll
    for (int i = 1; i < 16; ++i) t0_ = (r31 == i) ? zz16[i] : t0_;
    z = t0_; }
  float* cb = (float*)(lds + L_KVX) + wc * 1088;
  if (wr) {
#pragma unroll
    for (int i4 = 0; i4 < 4; ++i4) *(f4*)(cb + i4 * 256 + l * 4) = (f4){kv[4 * i4], kv[4 * i4 + 1], kv[4 * i4 + 2], kv[4 * i4 + 3]};
    cb[1024 + l] = z;
  }
  asm volatile("s_waitcnt lgkmcnt(0)" ::: "memory");
  lds_arrive(barc, l);
  GAS unsigned* kcnt = (GAS unsigned*)(ws + WS_CNT) + hp * 32;
  if (wr == 0) {
    lds_wait(barc, 4 * 17);
    float* part = (float*)(ws + WS_KVP) + (size_t)(mb * 16 + hp) * 2176;
    __amdgpu_buffer_rsrc_t rs = __builtin_amdgcn_make_buffer_rsrc(part, 0, 2176 * 4, 0x00020000);
#pragma unroll
    for (int i4 = 0; i4 < 4; ++i4) {
      f4 o = *(const f4*)(cb + i4 * 256 + l * 4);
      f4 v = (f4){kv[4 * i4] + o[0], kv[4 * i4 + 1] + o[1], kv[4 * i4 + 2] + o[2], kv[4 * i4 + 3] + o[3]};
      __builtin_amdgcn_raw_buffer_store_b128(__builtin_bit_cast(u4, v), rs, (wc * 1088 + i4 * 256 + l * 4) * 4, 0, 16);
    }
    float zz = z + cb[1024 + l];
    __builtin_amdgcn_raw_buffer_store_b32(__float_as_uint(zz), rs, (wc * 1088 + 1024 + l) * 4, 0, 16);
    asm volatile("s_waitcnt vmcnt(0)" ::: "memory");
    if (l == 0) __hip_atomic_fetch_add(kcnt, 1u, __ATOMIC_RELAXED, __HIP_MEMORY_SCOPE_AGENT);
  }
  unsigned wovoff; { int row = l >> 3, slot = l & 7; wovoff = row * 2048 + ((slot ^ (((row >> 1) & 1) << 2)) * 16); }
  const char* wob = (const char*)wo + hp * 128;
  if (wr == 1) {
    lds_wait(barc, 4 * 17);
#pragma unroll
    for (int c = 0; c < 7; ++c)
#pragma unroll
      for (int pc = 0; pc < 8; ++pc)
        GLDS(wovoff, wob + (size_t)(wc * 512 + c * 64 + pc * 8) * 2048, ldsb + c * 16384 + wc * 8192 + pc * 1024);
  }
  if (w == 0) STMP(1, 3);
  u4 pf[2][2];
#pragma unroll
  for (int m = 0; m < 2; ++m)
#pragma unroll
    for (int s2 = 0; s2 < 2; ++s2)
#pragma unroll
      for (int e = 0; e < 4; ++e) pf[m][s2][e] = pk2(phif(acc[m][0][8 * s2 + 2 * e]), phif(acc[m][0][8 * s2 + 2 * e + 1]));
  bwait(w == 0, kcnt, 32, (LAS unsigned*)(lds + L_BAR + 28), l);
  if (w == 0) STMP(1, 4);
  f4 ks4[4]; float zs;
  {
    const float* src = (const float*)(ws + WS_KVP) + (size_t)hp * 2176 + wc * 1088;
    __amdgpu_buffer_rsrc_t rr = __builtin_amdgcn_make_buffer_rsrc((void*)src, 0, (15u * 16 * 2176 + 1088) * 4, 0x00020000);
    u4 v[8][4]; unsigned zv[8];
#pragma unroll
    for (int pp = 0; pp < 8; ++pp) {
#pragma unroll
      for (int i4 = 0; i4 < 4; ++i4) v[pp][i4] = __builtin_amdgcn_raw_buffer_load_b128(rr, ((wr * 8 + pp) * 16 * 2176 + i4 * 256 + l * 4) * 4, 0, 16);
      zv[pp] = __builtin_amdgcn_raw_buffer_load_b32(rr, ((wr * 8 + pp) * 16 * 2176 + 1024 + l) * 4, 0, 16);
    }
#pragma unroll
    for (int i4 = 0; i4 < 4; ++i4) {
      ks4[i4] = __builtin_bit_cast(f4, v[0][i4]);
#pragma unroll
      for (int pp = 1; pp < 8; ++pp) ks4[i4] += __builtin_bit_cast(f4, v[pp][i4]);
    }
    zs = __uint_as_float(zv[0]);
#pragma unroll
    for (int pp = 1; pp < 8; ++pp) zs += __uint_as_float(zv[pp]);
  }
  float* xb = (float*)(lds + L_KVX) + (wr * 2 + wc) * 1088;
#pragma unroll
  for (int i4 = 0; i4 < 4; ++i4) *(f4*)(xb + i4 * 256 + l * 4) = ks4[i4];
  xb[1024 + l] = zs;
  asm volatile("s_waitcnt lgkmcnt(0)" ::: "memory");
  lds_arrive(barc, l); lds_wait(barc, 4 * 18);
  u4 kA[2], zf[2];
  {
    const float* ob = (const float*)(lds + L_KVX) + ((wr ^ 1) * 2 + wc) * 1088;
#pragma unroll
    for (int i4 = 0; i4 < 4; ++i4) {
      f4 o = *(const f4*)(ob + i4 * 256 + l * 4);
      f4 a = wr ? o + ks4[i4] : ks4[i4] + o;
      kA[i4 >> 1][(i4 & 1) * 2] = pk2(a[0], a[1]); kA[i4 >> 1][(i4 & 1) * 2 + 1] = pk2(a[2], a[3]);
    }
    const float* z0p = (const float*)(lds + L_KVX) + (0 * 2 + wc) * 1088 + 1024 + h * 32;
    const float* z1p = (const float*)(lds + L_KVX) + (1 * 2 + wc) * 1088 + 1024 + h * 32;
#pragma unroll
    for (int q4 = 0; q4 < 4; ++q4) {
      f4 a = *(const f4*)(z0p + q4 * 4) + *(const f4*)(z1p + q4 * 4);
      zf[q4 >> 1][(q4 & 1) * 2] = pk2(a[0], a[1]); zf[q4 >> 1][(q4 & 1) * 2 + 1] = pk2(a[2], a[3]);
    }
  }
  asm volatile("s_waitcnt lgkmcnt(0)" ::: "memory");
  lds_arrive(barc, l);
  __amdgpu_buffer_rsrc_t ors = __builtin_amdgcn_make_buffer_rsrc(ws + WS_PQF, 0, 4u << 20, 0x00020000);
#pragma unroll
  for (int m = 0; m < 2; ++m) {
    f16v X = MFMA32(__builtin_bit_cast(s8, kA[0]), __builtin_bit_cast(s8, pf[m][0]), (f16v)(0.f));
    X = MFMA32(__builtin_bit_cast(s8, kA[1]), __builtin_bit_cast(s8, pf[m][1]), X);
    float den = 0;
#pragma unroll
    for (int e = 0; e < 4; ++e) {
      unsigned a0 = pf[m][0][e], a1 = pf[m][1][e], c0 = zf[0][e], c1 = zf[1][e];
      den = __builtin_amdgcn_fdot2_f32_bf16(__builtin_bit_cast(bf2, a0), __builtin_bit_cast(bf2, c0), den, false);
      den = __builtin_amdgcn_fdot2_f32_bf16(__builtin_bit_cast(bf2, a1), __builtin_bit_cast(bf2, c1), den, false);
    }
    auto rr = __builtin_amdgcn_permlane32_swap(__float_as_uint(den), __float_as_uint(den), false, false);
    den = __uint_as_float(rr[0]) + __uint_as_float(rr[1]);
    den = rbf(rbf(den) + 1e-6f);
    float inv = __builtin_amdgcn_rcpf(den);
    u4 o0, o1;
#pragma unroll
    for (int e = 0; e < 4; ++e) { o0[e] = pk2(X[2 * e] * inv, X[2 * e + 1] * inv); o1[e] = pk2(X[8 + 2 * e] * inv, X[9 + 2 * e] * inv); }
    unsigned dofs = (((mb * 4 + wr * 2 + m) * 32 + head) * 2) * 1024 + l * 16;
    __builtin_amdgcn_raw_buffer_store_b128(o0, ors, dofs, 0, 16);
    __builtin_amdgcn_raw_buffer_store_b128(o1, ors, dofs + 1024, 0, 16);
  }
  asm volatile("s_waitcnt vmcnt(0)" ::: "memory");
  GAS unsigned* ocnt = (GAS unsigned*)(ws + WS_CNT) + (88 + mb) * 32;
  if (l == 0) __hip_atomic_fetch_add(ocnt, 1u, __ATOMIC_RELAXED, __HIP_MEMORY_SCOPE_AGENT);
  if (w == 0) STMP(1, 5);
  lds_wait(barc, 4 * 19);
  {
#pragma unroll
    for (int pc = 0; pc < 4; ++pc)
      GLDS(wovoff, wob + (size_t)((w >> 1) * 512 + 7 * 64 + ((w & 1) * 4 + pc) * 8) * 2048, ldsb + 7 * 16384 + (w >> 1) * 8192 + ((w & 1) * 4 + pc) * 1024);
  }
  const int nb = hp, rt = w;
  const int tok = mb * 128 + rt * 32 + r31;
  const u16* xn = (const u16*)(ws + WS_XN);
  u2 xv[8], nv[8];
#pragma unroll
  for (int i8 = 0; i8 < 8; ++i8) {
    int col = nb * 64 + (i8 >> 2) * 32 + 8 * (i8 & 3) + 4 * h;
    xv[i8] = *(const u2*)(x + (size_t)tok * D + col); nv[i8] = *(const u2*)(xn + (size_t)tok * D + col);
  }
  WAITV(16);
  lds_arrive(barc, l);
  bwait(w == 0, (GAS unsigned*)(ws + WS_CNT) + 120 * 32, 1, (LAS unsigned*)(lds + L_BAR + 36), l);
  int sel = -1; float sg = 0.f;
#pragma unroll
  for (int i = 0; i < 8; ++i) {
    unsigned long long e = __hip_atomic_load((GAS unsigned long long*)(ws + WS_SEL) + i, __ATOMIC_RELAXED, __HIP_MEMORY_SCOPE_AGENT);
    if ((int)(unsigned)e == tok) { sel = i; sg = __uint_as_float((unsigned)(e >> 32)); }
  }
  bwait(w == 0, ocnt, 64, (LAS unsigned*)(lds + L_BAR + 32), l);
  if (w == 0) STMP(1, 6);
  const unsigned abase = ((mb * 4 + rt) * 32) * 2048 + l * 16;
  u4 a[4][8];
#define K3LDA(c) do { _Pragma("unroll") for (int f = 0; f < 8; ++f) \
    a[(c) & 3][f] = __builtin_amdgcn_raw_buffer_load_b128(ors, abase + ((f >> 2) * 16 + 2 * (c) + ((f >> 1) & 1)) * 2048 + (f & 1) * 1024, 0, 16); } while (0)
  K3LDA(0); K3LDA(1); K3LDA(2); K3LDA(3);
  lds_wait(barc, 4 * 20);
  const unsigned woad0 = ldsb + (4 * h + q) * 128 + (((q >> 1) & 1) * 64) + ((l >> 4) & 1) * 32 + p * 8;
  const unsigned woad1 = woad0 ^ 64;
  f16v oc[2];
  oc[0] = (f16v)(0.f); oc[1] = (f16v)(0.f);
#define K3HALF(c, kh_) do { \
    s4 b[16]; \
    const unsigned w0_ = woad0 + ((c) >> 2) * 65536, w1_ = woad1 + ((c) >> 2) * 65536; \
    _Pragma("unroll") for (int hh = 0; hh < 2; ++hh) { \
      RDTR(b[hh * 8 + 0], w0_, ((c) & 3) * 16384 + (kh_) * 8192 + hh * 4096); RDTR(b[hh * 8 + 1], w0_, ((c) & 3) * 16384 + (kh_) * 8192 + hh * 4096 + 1024); \
      RDTR(b[hh * 8 + 2], w1_, ((c) & 3) * 16384 + (kh_) * 8192 + hh * 4096); RDTR(b[hh * 8 + 3], w1_, ((c) & 3) * 16384 + (kh_) * 8192 + hh * 4096 + 1024); \
      RDTR(b[hh * 8 + 4], w0_, ((c) & 3) * 16384 + (kh_) * 8192 + hh * 4096 + 2048); RDTR(b[hh * 8 + 5], w0_, ((c) & 3) * 16384 + (kh_) * 8192 + hh * 4096 + 3072); \
      RDTR(b[hh * 8 + 6], w1_, ((c) & 3) * 16384 + (kh_) * 8192 + hh * 4096 + 2048); RDTR(b[hh * 8 + 7], w1_, ((c) & 3) * 16384 + (kh_) * 8192 + hh * 4096 + 3072); } \
    WAITL(12); SBAR(); \
    oc[0] = MFMA32(CAT(b[0], b[1]), __builtin_bit_cast(s8, a[(c) & 3][(kh_) * 4 + 0]), oc[0]); \
    oc[1] = MFMA32(CAT(b[2], b[3]), __builtin_bit_cast(s8, a[(c) & 3][(kh_) * 4 + 0]), oc[1]); SBAR(); \
    WAITL(8); SBAR(); \
    oc[0] = MFMA32(CAT(b[4], b[5]), __builtin_bit_cast(s8, a[(c) & 3][(kh_) * 4 + 1]), oc[0]); \
    oc[1] = MFMA32(CAT(b[6], b[7]), __builtin_bit_cast(s8, a[(c) & 3][(kh_) * 4 + 1]), oc[1]); SBAR(); \
    WAITL(4); SBAR(); \
    oc[0] = MFMA32(CAT(b[8], b[9]), __builtin_bit_cast(s8, a[(c) & 3][(kh_) * 4 + 2]), oc[0]); \
    oc[1] = MFMA32(CAT(b[10], b[11]), __builtin_bit_cast(s8, a[(c) & 3][(kh_) * 4 + 2]), oc[1]); SBAR(); \
    WAITL(0); SBAR(); \
    oc[0] = MFMA32(CAT(b[12], b[13]), __builtin_bit_cast(s8, a[(c) & 3][(kh_) * 4 + 3]), oc[0]); \
    oc[1] = MFMA32(CAT(b[14], b[15]), __builtin_bit_cast(s8, a[(c) & 3][(kh_) * 4 + 3]), oc[1]); SBAR(); } while (0)
#define K3C(c) do { K3HALF(c, 0); K3HALF(c, 1); if ((c) < 4) { K3LDA((c) + 4); } SBAR(); } while (0)
  K3C(0); K3C(1); K3C(2); K3C(3); K3C(4); K3C(5); K3C(6); K3C(7);
  if (w == 0) STMP(1, 7);
  const bool anysel = __ballot(sel >= 0) != 0ull;
  if (anysel) gwait((GAS unsigned*)(ws + WS_CNT) + (104 + nb) * 32, 56);
  __amdgpu_buffer_rsrc_t yrs = __builtin_amdgcn_make_buffer_rsrc(ws + WS_YP, 0, 14 * 8 * D * 4, 0x00020000);
  u2 ovs[8];
#pragma unroll
  for (int i8 = 0; i8 < 8; ++i8) {
    const f16v& oo = oc[i8 >> 2];
    const int i4 = i8 & 3;
    int col = nb * 64 + (i8 >> 2) * 32 + 8 * i4 + 4 * h;
    float av[4] = {oo[4 * i4], oo[4 * i4 + 1], oo[4 * i4 + 2], oo[4 * i4 + 3]};
    float b[4] = {lo(nv[i8][0]), hi(nv[i8][0]), lo(nv[i8][1]), hi(nv[i8][1])};
    float xx[4] = {lo(xv[i8][0]), hi(xv[i8][0]), lo(xv[i8][1]), hi(xv[i8][1])};
    if (sel >= 0) {
      f4 y = (f4)(0.f);
#pragma unroll
      for (int ks = 0; ks < 14; ++ks) y += __builtin_bit_cast(f4, __builtin_amdgcn_raw_buffer_load_b128(yrs, ((ks * 8 + sel) * D + col) * 4, 0, 16));
#pragma unroll
      for (int e = 0; e < 4; ++e) b[e] = rbf(b[e] + rbf(rbf(y[e]) * sg));
    }
    u2 ov;
#pragma unroll
    for (int e = 0; e < 4; ++e) av[e] = rbf(xx[e] + rbf(av[e])) + b[e];
    ov[0] = pk2(av[0], av[1]); ov[1] = pk2(av[2], av[3]);
    ovs[i8] = ov;
  }
#pragma unroll
  for (int k2 = 0; k2 < 4; ++k2) {
    auto r0 = __builtin_amdgcn_permlane32_swap(ovs[2 * k2][0], ovs[2 * k2 + 1][0], false, false);
    auto r1 = __builtin_amdgcn_permlane32_swap(ovs[2 * k2][1], ovs[2 * k2 + 1][1], false, false);
    u4 o4; o4[0] = r0[0]; o4[1] = r1[0]; o4[2] = r0[1]; o4[3] = r1[1];
    *(u4*)(out + (size_t)tok * D + nb * 64 + 16 * k2 + 8 * h) = o4;
  }
  { unsigned long long t0_ = *(const unsigned long long*)(ws + (33u<<20)); unsigned long long d_ = tb1_ - tb0_; while (__builtin_amdgcn_s_memrealtime() - t0_ < 6000ull + 4ull * d_) __builtin_amdgcn_s_sleep(1); }
  asm volatile(".Lk2_code_end:");
}

extern "C" void kernel_launch(void* const* d_in, const int* in_sizes, int n_in, void* d_out, int out_size,
                              void* d_ws, size_t ws_size, hipStream_t stream) {
  const u16* x = (const u16*)d_in[0];
  const u16* wqkv = (const u16*)d_in[1];
  const u16* wo = (const u16*)d_in[2];
  const u16* wr = (const u16*)d_in[4];
  const u16* wg = (const u16*)d_in[5];
  const u16* wu = (const u16*)d_in[6];
  const u16* wd = (const u16*)d_in[7];
  char* ws = (char*)d_ws;
  (void)hipFuncSetAttribute((const void*)k2, hipFuncAttributeMaxDynamicSharedMemorySize, K2_LDS);
  k1_ln<<<256, 256, 0, stream>>>(x, wr, ws, wqkv, wo);
  k2<<<256, 512, K2_LDS, stream>>>(wqkv, wg, wu, wd, ws, x, wo, (u16*)d_out);
}
```

```cpp
#include <hip/hip_runtime.h>
#include <hip/hip_bf16.h>
#include <stdint.h>

typedef unsigned short u16;
typedef short s4 __attribute__((ext_vector_type(4)));
typedef short s8 __attribute__((ext_vector_type(8)));
typedef float f16v __attribute__((ext_vector_type(16)));
typedef float f4 __attribute__((ext_vector_type(4)));
typedef float f2 __attribute__((ext_vector_type(2)));
typedef unsigned u4 __attribute__((ext_vector_type(4)));
typedef unsigned u2 __attribute__((ext_vector_type(2)));
typedef __bf16 bf2 __attribute__((ext_vector_type(2)));
#define LAS __attribute__((address_space(3)))
#define GAS __attribute__((address_space(1)))
#define DEV __device__ __forceinline__

constexpr int S = 2048, D = 1024, F = 3584;
template <int N> struct ic { static constexpr int value = N; };

constexpr size_t WS_XN = 0;
constexpr size_t WS_PQF = 4u << 20;
constexpr size_t WS_KVP = 8u << 20;
constexpr size_t WS_GP = (10u << 20) + (256u << 10);
constexpr size_t WS_YP = WS_GP + (896u << 10);
constexpr size_t WS_H = WS_YP + (448u << 10);
constexpr size_t WS_R = WS_H + (64u << 10);
constexpr size_t WS_SEL = WS_R + (8u << 10);
constexpr size_t WS_CNT = WS_SEL + 4096;

DEV float bf2f(u16 v) { return __uint_as_float(((unsigned)v) << 16); }
DEV unsigned pk2(float a, float b) { bf2 v; v[0] = (__bf16)a; v[1] = (__bf16)b; return __builtin_bit_cast(unsigned, v); }
DEV float lo(unsigned u) { return __uint_as_float(u << 16); }
DEV float hi(unsigned u) { return __uint_as_float(u & 0xFFFF0000u); }
DEV float rbf(float a) { return lo(pk2(a, a)); }
DEV float phif(float t) {
  float e = __builtin_amdgcn_fmed3f(__builtin_amdgcn_exp2f(t * 1.44269504f), 0.f, 1.f);
  return (e + 1e-6f) + fmaxf(t, 0.f);
}
#define RD128(dst, addr, OFF) asm volatile("ds_read_b128 %0, %1 offset:%2" : "=v"(dst) : "v"(addr), "i"(OFF) : "memory")
#define RDTR(dst, addr, OFF) asm volatile("ds_read_b64_tr_b16 %0, %1 offset:%2" : "=v"(dst) : "v"(addr), "i"(OFF) : "memory")
#define SBAR() __builtin_amdgcn_sched_barrier(0)
#define CAT(x, y) __builtin_shufflevector(x, y, 0, 1, 2, 3, 4, 5, 6, 7)
#define WAITV(n) asm volatile("s_waitcnt vmcnt(%0)" ::"i"(n) : "memory")
#define WAITL(n) asm volatile("s_waitcnt lgkmcnt(%0)" ::"i"(n) : "memory")
DEV const char* uptr(const char* p) {
  unsigned long long v = (unsigned long long)p;
  unsigned lo_ = __builtin_amdgcn_readfirstlane((unsigned)v), hi_ = __builtin_amdgcn_readfirstlane((unsigned)(v >> 32));
  return (const char*)(((unsigned long long)hi_ << 32) | lo_);
}
#define GLDS(voff, sbase, m0v) asm volatile("s_mov_b32 m0, %2\n s_nop 0\n global_load_lds_dwordx4 %0, %1" ::"v"(voff), "s"(uptr(sbase)), "s"(__builtin_amdgcn_readfirstlane(m0v)) : "memory")
#define MFMA32(a, b, c) __builtin_amdgcn_mfma_f32_32x32x16_bf16(a, b, c, 0, 0, 0)
#define MFMA16(a, b, c) __builtin_amdgcn_mfma_f32_16x16x32_bf16(a, b, c, 0, 0, 0)

DEV void lds_arrive(LAS unsigned* c, int l) {
  if (l == 0) __hip_atomic_fetch_add(c, 1u, __ATOMIC_RELAXED, __HIP_MEMORY_SCOPE_WORKGROUP);
}
DEV void lds_wait(LAS unsigned* c, unsigned target) {
  while (__builtin_amdgcn_readfirstlane(__hip_atomic_load(c, __ATOMIC_RELAXED, __HIP_MEMORY_SCOPE_WORKGROUP)) < target) {}
  asm volatile("" ::: "memory");
}
DEV void lds_wait_s(LAS unsigned* c, unsigned target) {
  while (__builtin_amdgcn_readfirstlane(__hip_atomic_load(c, __ATOMIC_RELAXED, __HIP_MEMORY_SCOPE_WORKGROUP)) < target) __builtin_amdgcn_s_sleep(4);
  asm volatile("" ::: "memory");
}
DEV void bwait(bool leader, GAS unsigned* c, unsigned target, LAS unsigned* f, int l) {
  if (leader) {
    for (unsigned it_ = 0; it_ < (1u << 22) && (unsigned)__builtin_amdgcn_readfirstlane(__hip_atomic_load(c, __ATOMIC_RELAXED, __HIP_MEMORY_SCOPE_AGENT)) < target; ++it_) __builtin_amdgcn_s_sleep(8);
    asm volatile("" ::: "memory");
    lds_arrive(f, l);
  } else lds_wait_s(f, 1);
}
DEV void gwait(GAS unsigned* c, unsigned target) {
  for (unsigned it_ = 0; it_ < (1u << 22) && (unsigned)__builtin_amdgcn_readfirstlane(__hip_atomic_load(c, __ATOMIC_RELAXED, __HIP_MEMORY_SCOPE_AGENT)) < target; ++it_) __builtin_amdgcn_s_sleep(2);
  asm volatile("" ::: "memory");
}

#define STMP(kid, idx)
#define STMC(kid, idx)
#define WSS ws
DEV float wave_sum(float v) {
  v += __int_as_float(__builtin_amdgcn_update_dpp(0, __float_as_int(v), 0x111, 0xf, 0xf, false));
  v += __int_as_float(__builtin_amdgcn_update_dpp(0, __float_as_int(v), 0x112, 0xf, 0xf, false));
  v += __int_as_float(__builtin_amdgcn_update_dpp(0, __float_as_int(v), 0x114, 0xf, 0xf, false));
  v += __int_as_float(__builtin_amdgcn_update_dpp(0, __float_as_int(v), 0x118, 0xf, 0xf, false));
  v += __int_as_float(__builtin_amdgcn_update_dpp(0, __float_as_int(v), 0x142, 0xa, 0xf, false));
  v += __int_as_float(__builtin_amdgcn_update_dpp(0, __float_as_int(v), 0x143, 0xc, 0xf, false));
  return __int_as_float(__builtin_amdgcn_readlane(__float_as_int(v), 63));
}
__global__ __launch_bounds__(256) void k1_ln(const u16* __restrict__ x, const u16* __restrict__ wr, char* __restrict__ ws, const u16* __restrict__ wqkv, const u16* __restrict__ wo) {
  const unsigned long long ta_ = __builtin_amdgcn_s_memrealtime();
  { u4 v_[14]; unsigned x_ = 0;
#pragma unroll
    for (int i = 0; i < 7; ++i) { v_[i] = *(const u4*)((const char*)wqkv + (size_t)blockIdx.x * 28672 + i * 4096 + threadIdx.x * 16); v_[7 + i] = *(const u4*)((const char*)wo + (size_t)blockIdx.x * 28672 + i * 4096 + threadIdx.x * 16); }
#pragma unroll
    for (int i = 0; i < 14; ++i) x_ ^= v_[i][0] ^ v_[i][1] ^ v_[i][2] ^ v_[i][3];
    asm volatile("" ::"v"(x_));
    __syncthreads();
    if (threadIdx.x == 0) ((unsigned long long*)(ws + (34u<<20)))[blockIdx.x] = __builtin_amdgcn_s_memrealtime() - ta_;
  }
  u16* xn = (u16*)(ws + WS_XN);
  float* r = (float*)(ws + WS_R);
  int w = threadIdx.x >> 6, l = threadIdx.x & 63;
  if (blockIdx.x == 0 && threadIdx.x == 0) *(unsigned long long*)(ws + (33u<<20)) = __builtin_amdgcn_s_memrealtime();
  if (blockIdx.x == 0 && threadIdx.x < 128) ((unsigned*)(ws + WS_CNT))[threadIdx.x * 32] = 0u;
  u4 a[2], b[2];
#pragma unroll
  for (int i = 0; i < 2; ++i) {
    int row = blockIdx.x * 8 + w * 2 + i;
    a[i] = *(const u4*)(x + row * D + l * 8); b[i] = *(const u4*)(x + row * D + 512 + l * 8);
  }
  u4 wv0 = *(const u4*)(wr + l * 8), wv1 = *(const u4*)(wr + 512 + l * 8);
#pragma unroll
  for (int i = 0; i < 2; ++i) {
    int row = blockIdx.x * 8 + w * 2 + i;
    float v[16];
#pragma unroll
    for (int j = 0; j < 4; ++j) { v[2*j] = lo(a[i][j]); v[2*j+1] = hi(a[i][j]); v[8+2*j] = lo(b[i][j]); v[9+2*j] = hi(b[i][j]); }
    float s = 0;
#pragma unroll
    for (int j = 0; j < 16; ++j) s += v[j];
    s = wave_sum(s);
    float m = s * (1.f / D), q = 0;
#pragma unroll
    for (int j = 0; j < 16; ++j) { v[j] -= m; q += v[j] * v[j]; }
    q = wave_sum(q);
    float rs = 1.f / sqrtf(q * (1.f / D) + 1e-5f);
    u4 oa, ob; float d = 0;
#pragma unroll
    for (int j = 0; j < 4; ++j) {
      oa[j] = pk2(v[2*j] * rs, v[2*j+1] * rs); ob[j] = pk2(v[8+2*j] * rs, v[9+2*j] * rs);
      d += lo(oa[j]) * lo(wv0[j]) + hi(oa[j]) * hi(wv0[j]) + lo(ob[j]) * lo(wv1[j]) + hi(ob[j]) * hi(wv1[j]);
    }
    d = wave_sum(d);
    *(u4*)(xn + row * D + l * 8) = oa; *(u4*)(xn + row * D + 512 + l * 8) = ob;
    if (l == 0) r[row] = rbf(d);
  }
  if (w == 0) STMP(0, 1);
}

constexpr int K2_STG = 40960;
constexpr int L_KVX = 3 * K2_STG;
constexpr int L_RED1 = L_KVX + 17408;
constexpr int L_TOP = L_RED1 + 16384;
constexpr int L_BAR = L_TOP + 256;
constexpr int K2_LDS = L_BAR + 64;

DEV unsigned wave_max(unsigned v) {
  v = max(v, (unsigned)__builtin_amdgcn_update_dpp(0, (int)v, 0x111, 0xf, 0xf, false));
  v = max(v, (unsigned)__builtin_amdgcn_update_dpp(0, (int)v, 0x112, 0xf, 0xf, false));
  v = max(v, (unsigned)__builtin_amdgcn_update_dpp(0, (int)v, 0x114, 0xf, 0xf, false));
  v = max(v, (unsigned)__builtin_amdgcn_update_dpp(0, (int)v, 0x118, 0xf, 0xf, false));
  v = max(v, (unsigned)__builtin_amdgcn_update_dpp(0, (int)v, 0x142, 0xa, 0xf, false));
  v = max(v, (unsigned)__builtin_amdgcn_update_dpp(0, (int)v, 0x143, 0xc, 0xf, false));
  return (unsigned)__builtin_amdgcn_readlane((int)v, 63);
}

struct FR { s8 a0, a1; s4 b[6]; };
#define LWM lds_wait
#define NTL(p) __builtin_nontemporal_load(p)

DEV void k2_moe(char* lds, char* ws, const u16* __restrict__ wg, const u16* __restrict__ wu, const u16* __restrict__ wd, int mw, int l, const u16* __restrict__ wq_) {
  if (blockIdx.x >= 224) return;
  const int u = blockIdx.x;
  const u16* xn = (const u16*)(ws + WS_XN);
  const float* r = (const float*)(ws + WS_R);
  LAS unsigned* barm = (LAS unsigned*)(lds + L_BAR + 4);
  const int np = l & 15, kq = l >> 4;
  const int chunk = u >> 2, ksp = u & 3;
  float rv[8];
#pragma unroll
  for (int i = 0; i < 8; ++i) rv[i] = r[mw * 512 + i * 64 + l];
  u4 gw[2][8];
  {
    const u16* wp = ((np >> 3) ? wu : wg) + (size_t)(256 * ksp + 64 * mw + 8 * kq) * F + 64 * chunk + 8 * (np & 7);
#pragma unroll
    for (int it = 0; it < 2; ++it)
#pragma unroll
      for (int rr = 0; rr < 8; ++rr) gw[it][rr] = NTL((const u4*)(wp + (size_t)(32 * it + rr) * F));
  }
  const int ks = u >> 4, cc = u & 15, kset = np >> 3;
  u4 dw[8];
  {
    const u16* dp = wd + (size_t)(256 * ks + 64 * mw + 32 * kset + 8 * kq) * D + 64 * cc + 8 * (np & 7);
#pragma unroll
    for (int rr = 0; rr < 8; ++rr) dw[rr] = NTL((const u4*)(dp + (size_t)rr * D));
  }
  if (mw == 0) STMP(1, 8);
  unsigned key[8];
#pragma unroll
  for (int i = 0; i < 8; ++i) {
    unsigned b = __float_as_uint(rv[i]) >> 16;
    unsigned mono = (b & 0x8000u) ? (~b & 0xFFFFu) : (b | 0x8000u);
    key[i] = (mono << 16) | (unsigned)(0xFFFF - (mw * 512 + i * 64 + l));
  }
  unsigned* cand = (unsigned*)(lds + L_TOP);
  int* sel = (int*)(lds + L_TOP + 128) + mw * 8;
#pragma unroll 1
  for (int it = 0; it < 8; ++it) {
    unsigned m = max(max(max(key[0], key[1]), max(key[2], key[3])), max(max(key[4], key[5]), max(key[6], key[7])));
    m = wave_max(m);
#pragma unroll
    for (int i = 0; i < 8; ++i) key[i] = key[i] == m ? 0u : key[i];
    if (l == 0) cand[mw * 8 + it] = m;
  }
  asm volatile("s_waitcnt lgkmcnt(0)" ::: "memory");
  lds_arrive(barm, l); lds_wait_s(barm, 4);
  {
    unsigned mine = cand[l & 31];
    int rank = 0;
#pragma unroll
    for (int j = 0; j < 32; ++j) rank += ((unsigned)__builtin_amdgcn_readlane((int)mine, j) > mine) ? 1 : 0;
    if (l < 32 && rank < 8) sel[rank] = 0xFFFF - (int)(mine & 0xFFFF);
  }
  int idx = sel[l & 7];
  if (mw == 0) STMP(1, 9);
  if (u == 0 && mw == 0) {
    if (l < 8) {
      float rr = r[idx];
      float sg = rbf(1.f / (1.f + __expf(-rr)));
      __hip_atomic_store((GAS unsigned long long*)(ws + WS_SEL) + l, ((unsigned long long)__float_as_uint(sg) << 32) | (unsigned)idx, __ATOMIC_RELAXED, __HIP_MEMORY_SCOPE_AGENT);
    }
    asm volatile("s_waitcnt vmcnt(0)" ::: "memory");
    if (l == 0) __hip_atomic_fetch_add((GAS unsigned*)(ws + WS_CNT) + 120 * 32, 1u, __ATOMIC_RELAXED, __HIP_MEMORY_SCOPE_AGENT);
  }
  f4 acc[8];
#pragma unroll
  for (int c = 0; c < 8; ++c) acc[c] = (f4)(0.f);
  {
    const u16* xp = xn + (size_t)idx * D + 256 * ksp + 64 * mw + 8 * kq;
    u4 xa[2];
#pragma unroll
    for (int it = 0; it < 2; ++it) xa[it] = *(const u4*)(xp + 32 * it);
#pragma unroll
    for (int it = 0; it < 2; ++it) {
#pragma unroll
      for (int c = 0; c < 8; ++c) {
        u4 f;
#pragma unroll
        for (int q = 0; q < 4; ++q) {
          unsigned e = gw[it][2 * q][c >> 1], o = gw[it][2 * q + 1][c >> 1];
          f[q] = (c & 1) ? __builtin_amdgcn_perm(o, e, 0x07060302u) : __builtin_amdgcn_perm(o, e, 0x05040100u);
        }
        acc[c] = MFMA16(__builtin_bit_cast(s8, xa[it]), __builtin_bit_cast(s8, f), acc[c]);
      }
    }
  }
  if (mw == 0) STMP(1, 10);
  float* red1 = (float*)(lds + L_RED1);
  if (kq < 2) {
#pragma unroll
    for (int e = 0; e < 4; ++e) {
      float* p = red1 + ((mw * 8 + 4 * kq + e) * 2 + (np >> 3)) * 64 + 8 * (np & 7);
      *(f4*)p = (f4){acc[0][e], acc[1][e], acc[2][e], acc[3][e]};
      *(f4*)(p + 4) = (f4){acc[4][e], acc[5][e], acc[6][e], acc[7][e]};
    }
  }
  asm volatile("s_waitcnt lgkmcnt(0)" ::: "memory");
  lds_arrive(barm, l); lds_wait_s(barm, 8);
  GAS unsigned* gcnt = (GAS unsigned*)(ws + WS_CNT) + (32 + chunk) * 32;
  GAS unsigned* hcnt = (GAS unsigned*)(ws + WS_CNT) + (16 + (chunk >> 2)) * 32;
  float* gpart = (float*)(ws + WS_GP) + (size_t)chunk * 4096;
  __amdgpu_buffer_rsrc_t grs = __builtin_amdgcn_make_buffer_rsrc(gpart, 0, 16384, 0x00020000);
  unsigned* tick = (unsigned*)(lds + L_TOP + 192);
  {
    int tm = mw * 64 + l;
    f4 sacc = *(const f4*)(red1 + tm * 4) + *(const f4*)(red1 + 1024 + tm * 4) + *(const f4*)(red1 + 2048 + tm * 4) + *(const f4*)(red1 + 3072 + tm * 4);
    __builtin_amdgcn_raw_buffer_store_b128(__builtin_bit_cast(u4, sacc), grs, (ksp * 1024 + tm * 4) * 4, 0, 16);
    asm volatile("s_waitcnt vmcnt(0)" ::: "memory");
  }
  lds_arrive(barm, l);
  if (mw == 0) {
    lds_wait_s(barm, 12);
    unsigned tk = 0;
    if (l == 0) tk = __hip_atomic_fetch_add(gcnt, 1u, __ATOMIC_RELAXED, __HIP_MEMORY_SCOPE_AGENT);
    tk = __builtin_amdgcn_readfirstlane(tk);
    if (tk == 3) {
      int tok = l >> 3, c8 = (l & 7) * 8;
      u4 pv[4][4];
#pragma unroll
      for (int p = 0; p < 4; ++p)
#pragma unroll
        for (int q = 0; q < 4; ++q)
          pv[p][q] = __builtin_amdgcn_raw_buffer_load_b128(grs, (p * 1024 + (tok * 2 + (q >> 1)) * 64 + c8 + (q & 1) * 4) * 4, 0, 16);
      f4 gq[4];
#pragma unroll
      for (int q = 0; q < 4; ++q) gq[q] = (__builtin_bit_cast(f4, pv[0][q]) + __builtin_bit_cast(f4, pv[1][q])) + (__builtin_bit_cast(f4, pv[2][q]) + __builtin_bit_cast(f4, pv[3][q]));
      float hv[8];
#pragma unroll
      for (int e = 0; e < 8; ++e) {
        float gg = rbf(gq[e >> 2][e & 3]), u1 = rbf(gq[2 + (e >> 2)][e & 3]);
        float sg = rbf(gg / (1.f + __expf(-gg)));
        hv[e] = sg * u1;
      }
      u4 ho; ho[0] = pk2(hv[0], hv[1]); ho[1] = pk2(hv[2], hv[3]); ho[2] = pk2(hv[4], hv[5]); ho[3] = pk2(hv[6], hv[7]);
      __amdgpu_buffer_rsrc_t hrs = __builtin_amdgcn_make_buffer_rsrc(ws + WS_H, 0, 8 * F * 2, 0x00020000);
      __builtin_amdgcn_raw_buffer_store_b128(ho, hrs, (tok * F + 64 * chunk + c8) * 2, 0, 16);
      asm volatile("s_waitcnt vmcnt(0)" ::: "memory");
      if (l == 0) __hip_atomic_fetch_add(hcnt, 1u, __ATOMIC_RELAXED, __HIP_MEMORY_SCOPE_AGENT);
    }
  }
  if (mw == 0) STMP(1, 11);
  GAS unsigned* hcnt2 = (GAS unsigned*)(ws + WS_CNT) + (16 + ks) * 32;
  bwait(mw == 0, hcnt2, 4, (LAS unsigned*)(lds + L_BAR + 24), l);
  if (mw == 0) STMP(1, 12);
  f4 acd[8];
  {
    __amdgpu_buffer_rsrc_t hrs = __builtin_amdgcn_make_buffer_rsrc(ws + WS_H, 0, 8 * F * 2, 0x00020000);
    u4 ha = __builtin_amdgcn_raw_buffer_load_b128(hrs, ((l & 7) * F + 256 * ks + 64 * mw + 32 * ((l >> 3) & 1) + 8 * kq) * 2, 0, 16);
#pragma unroll
    for (int c = 0; c < 8; ++c) {
      u4 f;
#pragma unroll
      for (int q = 0; q < 4; ++q) {
        unsigned e = dw[2 * q][c >> 1], o = dw[2 * q + 1][c >> 1];
        f[q] = (c & 1) ? __builtin_amdgcn_perm(o, e, 0x07060302u) : __builtin_amdgcn_perm(o, e, 0x05040100u);
      }
      acd[c] = MFMA16(__builtin_bit_cast(s8, ha), __builtin_bit_cast(s8, f), (f4)(0.f));
    }
  }
  float* red2 = (float*)(lds + L_RED1);
  lds_wait_s(barm, 12);
  if ((kq >> 1) == kset) {
#pragma unroll
    for (int e = 0; e < 4; ++e) {
      float* p = red2 + ((mw * 2 + kset) * 8 + 4 * (kq & 1) + e) * 64 + 8 * (np & 7);
      *(f4*)p = (f4){acd[0][e], acd[1][e], acd[2][e], acd[3][e]};
      *(f4*)(p + 4) = (f4){acd[4][e], acd[5][e], acd[6][e], acd[7][e]};
    }
  }
  asm volatile("s_waitcnt lgkmcnt(0)" ::: "memory");
  lds_arrive(barm, l); lds_wait_s(barm, 16);
  {
    int tm = mw * 64 + l, tok = tm >> 5, c2 = (tm & 31) * 2;
    f2 sacc = (f2)(0.f);
#pragma unroll
    for (int p = 0; p < 8; ++p) sacc += *(const f2*)(red2 + (p * 8 + tok) * 64 + c2);
    __hip_atomic_store((GAS unsigned long long*)((float*)(ws + WS_YP) + (size_t)(ks * 8 + tok) * D + 64 * cc + c2), __builtin_bit_cast(unsigned long long, sacc), __ATOMIC_RELAXED, __HIP_MEMORY_SCOPE_AGENT);
    asm volatile("s_waitcnt vmcnt(0)" ::: "memory");
    if (l == 0) __hip_atomic_fetch_add((GAS unsigned*)(ws + WS_CNT) + (104 + cc) * 32, 1u, __ATOMIC_RELAXED, __HIP_MEMORY_SCOPE_AGENT);
  }
  if (mw == 0) STMP(1, 13);
  asm volatile(".Lk2_moe_end:");
}

__global__ __launch_bounds__(512) void k2(const u16* __restrict__ wqkv, const u16* __restrict__ wg, const u16* __restrict__ wu,
                                          const u16* __restrict__ wd, char* __restrict__ ws, const u16* __restrict__ x,
                                          const u16* __restrict__ wo, u16* __restrict__ out) {
  extern __shared__ __attribute__((aligned(16))) char lds[];
  const int t0 = threadIdx.x, w = __builtin_amdgcn_readfirstlane(t0 >> 6), l = t0 & 63;
  if (t0 < 16) ((unsigned*)(lds + L_BAR))[t0] = 0u;
  __syncthreads();
  if (w == 0) STMP(1, 0);
  asm volatile(".Lk2_code_beg:");
  if (w == 7) {
    unsigned long long pc; unsigned len, off;
    asm volatile("s_getpc_b64 %0\n.Lk2_here:" : "=s"(pc));
    asm volatile("s_mov_b32 %0, (.Lk2_here - .Lk2_code_beg)" : "=s"(off));
    asm volatile("s_mov_b32 %0, (.Lk2_code_end - .Lk2_code_beg)" : "=s"(len));
    int len2;
    asm volatile("s_mov_b32 %0, (.Lk2_moe_end - .Lk2_code_beg)" : "=s"(len2));
    if (len2 > (int)len) len = (unsigned)len2;
    pc -= off;
    unsigned acc_ = 0;
    for (unsigned o = l * 64; o < len; o += 4096) acc_ ^= *(const unsigned*)((const char*)pc + o);
    asm volatile("" ::"v"(acc_));
  }
  if (w >= 4) {
    k2_moe(lds, ws, wg, wu, wd, w - 4, l, wqkv);
    return;
  }
  const int t = t0, wr = w >> 1, wc = w & 1, r31 = l & 31, h = l >> 5;
  const int g = blockIdx.x, xcd = g & 7, j = g >> 3;
  const int mb = (xcd & 1) * 8 + (j >> 2), hp = (xcd >> 1) * 4 + (j & 3);
  LAS unsigned* barc = (LAS unsigned*)(lds + L_BAR);
  unsigned aoffv, boffv;
  { int row = t >> 3, slot = t & 7; aoffv = row * 2048 + ((slot ^ ((row >> 1) & 7)) * 16); boffv = row * 6144 + ((slot ^ (((row >> 1) & 1) << 2)) * 16); }
  const char* Ab = ws + WS_XN + (size_t)mb * 128 * 2048;
  const char* Bb = (const char*)wqkv + hp * 128;
  const unsigned ldsb = (unsigned)(size_t)(LAS char*)lds;
  const unsigned m0base = ldsb + w * 1024;
  const int rot = 0;
  auto gl = [&](int kt_, int buf, int i) {
    const int kt = (kt_ + rot) & 15;
    unsigned m0v = m0base + buf * K2_STG;
    if (i < 4) { const char* sb = Ab + kt * 128 + i * 65536; GLDS(aoffv, sb, m0v + i * 4096); }
    else { int ii = i - 4; const char* sb = Bb + (size_t)(kt * 64 + (ii & 1) * 32) * 6144 + (ii >> 1) * 2048; GLDS(boffv, sb, m0v + 16384 + ii * 4096); }
  };
  unsigned aad[4];
  { int aoff = (wr * 64 + r31) * 128, asw = (r31 >> 1) & 7;
#pragma unroll
    for (int s = 0; s < 4; ++s) aad[s] = ldsb + aoff + (((2 * s + h) ^ asw) * 16); }
  const int q = (l & 15) >> 2, p = l & 3;
  const unsigned bad = ldsb + 16384 + (8 * h + q) * 128 + ((wc ^ (q >> 1)) * 64) + ((l >> 4) & 1) * 32 + p * 8;
  f16v acc[2][3];
#pragma unroll
  for (int m = 0; m < 2; ++m)
#pragma unroll
    for (int n = 0; n < 3; ++n) acc[m][n] = (f16v)(0.f);
#define MF(m, n, Fq) do { if (n == 0) acc[m][0] = MFMA32(CAT(Fq.b[0], Fq.b[1]), (m ? Fq.a1 : Fq.a0), acc[m][0]); \
     else acc[m][n] = MFMA32((m ? Fq.a1 : Fq.a0), CAT(Fq.b[2 * n], Fq.b[2 * n + 1]), acc[m][n]); } while (0)
#define GL(i) do { if (dost_) gl(kt + 2, sbuf, (i)); } while (0)
#define ARRIVE(addr) asm volatile("s_mov_b64 exec, 1\n ds_add_u32 %0, %1\n s_mov_b64 exec, -1" ::"v"(addr), "v"(onev) : "memory")
#define POLL(dst, addr) asm volatile("ds_read_b32 %0, %1" : "=v"(dst) : "v"(addr) : "memory")
#define KEEP(v) asm volatile("" ::"v"(v))
#define CHECK(v, c, target) do { if ((unsigned)__builtin_amdgcn_readfirstlane(v) < (unsigned)(target)) lds_wait(c, target); } while (0)
#define SUB(Fq, G, va, vb, SS, G0, CHK) do { \
    WAITL(5); SBAR(); \
    MF(0, 0, Fq); SBAR(); RD128(G.a0, va, 0); RDTR(G.b[0], vb, SS * 2048); RDTR(G.b[1], vb, SS * 2048 + 512); RDTR(G.b[2], vb, 8192 + SS * 2048); RDTR(G.b[3], vb, 8192 + SS * 2048 + 512); SBAR(); \
    WAITL(8); SBAR(); \
    MF(0, 1, Fq); SBAR(); RD128(G.a1, va, 4096); RDTR(G.b[4], vb, 16384 + SS * 2048); RDTR(G.b[5], vb, 16384 + SS * 2048 + 512); SBAR(); \
    WAITL(8); SBAR(); \
    MF(0, 2, Fq); SBAR(); if (CHK) { KEEP(vB); if (dost_) CHECK(vB, barB, 4 * kt); } GL(G0); SBAR(); \
    MF(1, 0, Fq); SBAR(); GL(G0 + 1); SBAR(); MF(1, 1, Fq); SBAR(); GL(G0 + 2); SBAR(); MF(1, 2, Fq); SBAR(); } while (0)
#define KSTEP(DOST, LAST) do { \
    constexpr bool dost_ = DOST; \
    int nbuf = buf == 2 ? 0 : buf + 1, sbuf = buf == 0 ? 2 : buf - 1; \
    unsigned bo = buf * K2_STG, nbo = nbuf * K2_STG; \
    unsigned vb = bad + bo; \
    { unsigned va = aad[1] + bo; SUB(f0, f1, va, vb, 1, 0, true); } \
    { unsigned va = aad[2] + bo; SUB(f1, f0, va, vb, 2, 3, false); } \
    if (!LAST) { if (dost_) WAITV(6); else WAITV(0); ARRIVE(adA); SBAR(); } \
    { unsigned va = aad[3] + bo; SUB(f0, f1, va, vb, 3, 6, false); } \
    GL(9); \
    if (!LAST) { \
      POLL(vA, adA); \
      WAITL(0); SBAR(); \
      ARRIVE(adB); \
      MF(0, 0, f1); SBAR(); \
      CHECK(vA, barc, 4 * (kt + 2)); \
      SBAR(); \
      unsigned va = aad[0] + nbo, vn = bad + nbo; \
      RD128(f0.a0, va, 0); RDTR(f0.b[0], vn, 0); RDTR(f0.b[1], vn, 512); RDTR(f0.b[2], vn, 8192); RDTR(f0.b[3], vn, 8192 + 512); SBAR(); \
      MF(0, 1, f1); SBAR(); \
      RD128(f0.a1, va, 4096); RDTR(f0.b[4], vn, 16384); RDTR(f0.b[5], vn, 16384 + 512); SBAR(); \
      MF(0, 2, f1); MF(1, 0, f1); MF(1, 1, f1); SBAR(); \
      POLL(vB, adB); SBAR(); \
      MF(1, 2, f1); SBAR(); \
    } else { WAITL(0); SBAR(); MF(0, 0, f1); MF(0, 1, f1); MF(0, 2, f1); MF(1, 0, f1); MF(1, 1, f1); MF(1, 2, f1); } \
    buf = nbuf; } while (0)
  const unsigned adA = ldsb + L_BAR, adB = ldsb + L_BAR + 8, onev = 1u;
  LAS unsigned* barB = (LAS unsigned*)(lds + L_BAR + 8);
  unsigned vA, vB = 0;
#pragma unroll
  for (int s = 0; s < 2; ++s)
#pragma unroll
    for (int i = 0; i < 10; ++i) gl(s, s, i);
  WAITV(10);
  lds_arrive(barc, l); lds_wait(barc, 4);
  if (w == 0) { STMP(1, 1); STMC(1, 40); }
  FR f0, f1;
  { unsigned va = aad[0], vb = bad;
    RD128(f0.a0, va, 0); RDTR(f0.b[0], vb, 0); RDTR(f0.b[1], vb, 512); RDTR(f0.b[2], vb, 8192); RDTR(f0.b[3], vb, 8192 + 512); RD128(f0.a1, va, 4096); RDTR(f0.b[4], vb, 16384); RDTR(f0.b[5], vb, 16384 + 512);
    SBAR(); }
  int buf = 0;
  int kt = 0;
#pragma unroll 1
  for (; kt < 14; ++kt) KSTEP(true, false);
  KSTEP(false, false);
  ++kt;
  KSTEP(false, true);
  if (w == 0) { STMP(1, 2); STMC(1, 41); }
  const int head = hp * 2 + wc;
  f16v kv = (f16v)(0.f), zz16 = (f16v)(0.f);
  const u4 ones = (u4){0x3F803F80u, 0x3F803F80u, 0x3F803F80u, 0x3F803F80u};
#pragma unroll
  for (int m = 0; m < 2; ++m) {
#pragma unroll
    for (int s2 = 0; s2 < 2; ++s2) {
      u4 kf, vf;
#pragma unroll
      for (int e = 0; e < 4; ++e) {
        kf[e] = pk2(phif(acc[m][1][8 * s2 + 2 * e]), phif(acc[m][1][8 * s2 + 2 * e + 1]));
        vf[e] = pk2(acc[m][2][8 * s2 + 2 * e], acc[m][2][8 * s2 + 2 * e + 1]);
      }
      kv = MFMA32(__builtin_bit_cast(s8, kf), __builtin_bit_cast(s8, vf), kv);
      zz16 = MFMA32(__builtin_bit_cast(s8, kf), __builtin_bit_cast(s8, ones), zz16);
    }
  }
  float z;
  { float t0_ = zz16[0];
#pragma unroll
    for (int i = 1; i < 16; ++i) t0_ = (r31 == i) ? zz16[i] : t0_;
    z = t0_; }
  float* cb = (float*)(lds + L_KVX) + wc * 1088;
  if (wr) {
#pragma unroll
    for (int i4 = 0; i4 < 4; ++i4) *(f4*)(cb + i4 * 256 + l * 4) = (f4){kv[4 * i4], kv[4 * i4 + 1], kv[4 * i4 + 2], kv[4 * i4 + 3]};
    cb[1024 + l] = z;
  }
  asm volatile("s_waitcnt lgkmcnt(0)" ::: "memory");
  lds_arrive(barc, l);
  GAS unsigned* kcnt = (GAS unsigned*)(ws + WS_CNT) + hp * 32;
  if (wr == 0) {
    lds_wait(barc, 4 * 17);
    float* part = (float*)(ws + WS_KVP) + (size_t)(mb * 16 + hp) * 2176;
    __amdgpu_buffer_rsrc_t rs = __builtin_amdgcn_make_buffer_rsrc(part, 0, 2176 * 4, 0x00020000);
#pragma unroll
    for (int i4 = 0; i4 < 4; ++i4) {
      f4 o = *(const f4*)(cb + i4 * 256 + l * 4);
      f4 v = (f4){kv[4 * i4] + o[0], kv[4 * i4 + 1] + o[1], kv[4 * i4 + 2] + o[2], kv[4 * i4 + 3] + o[3]};
      __builtin_amdgcn_raw_buffer_store_b128(__builtin_bit_cast(u4, v), rs, (wc * 1088 + i4 * 256 + l * 4) * 4, 0, 16);
    }
    float zz = z + cb[1024 + l];
    __builtin_amdgcn_raw_buffer_store_b32(__float_as_uint(zz), rs, (wc * 1088 + 1024 + l) * 4, 0, 16);
    asm volatile("s_waitcnt vmcnt(0)" ::: "memory");
    if (l == 0) __hip_atomic_fetch_add(kcnt, 1u, __ATOMIC_RELAXED, __HIP_MEMORY_SCOPE_AGENT);
  }
  unsigned wovoff; { int row = l >> 3, slot = l & 7; wovoff = row * 2048 + ((slot ^ (((row >> 1) & 1) << 2)) * 16); }
  const char* wob = (const char*)wo + hp * 128;
  if (wr == 1) {
    lds_wait(barc, 4 * 17);
#pragma unroll
    for (int c = 0; c < 7; ++c)
#pragma unroll
      for (int pc = 0; pc < 8; ++pc)
        GLDS(wovoff, wob + (size_t)(wc * 512 + c * 64 + pc * 8) * 2048, ldsb + c * 16384 + wc * 8192 + pc * 1024);
  }
  if (w == 0) STMP(1, 3);
  u4 pf[2][2];
#pragma unroll
  for (int m = 0; m < 2; ++m)
#pragma unroll
    for (int s2 = 0; s2 < 2; ++s2)
#pragma unroll
      for (int e = 0; e < 4; ++e) pf[m][s2][e] = pk2(phif(acc[m][0][8 * s2 + 2 * e]), phif(acc[m][0][8 * s2 + 2 * e + 1]));
  bwait(w == 0, kcnt, 32, (LAS unsigned*)(lds + L_BAR + 28), l);
  if (w == 0) STMP(1, 4);
  f4 ks4[4]; float zs;
  {
    const float* src = (const float*)(ws + WS_KVP) + (size_t)hp * 2176 + wc * 1088;
    __amdgpu_buffer_rsrc_t rr = __builtin_amdgcn_make_buffer_rsrc((void*)src, 0, (15u * 16 * 2176 + 1088) * 4, 0x00020000);
    u4 v[8][4]; unsigned zv[8];
#pragma unroll
    for (int pp = 0; pp < 8; ++pp) {
#pragma unroll
      for (int i4 = 0; i4 < 4; ++i4) v[pp][i4] = __builtin_amdgcn_raw_buffer_load_b128(rr, ((wr * 8 + pp) * 16 * 2176 + i4 * 256 + l * 4) * 4, 0, 16);
      zv[pp] = __builtin_amdgcn_raw_buffer_load_b32(rr, ((wr * 8 + pp) * 16 * 2176 + 1024 + l) * 4, 0, 16);
    }
#pragma unroll
    for (int i4 = 0; i4 < 4; ++i4) {
      ks4[i4] = __builtin_bit_cast(f4, v[0][i4]);
#pragma unroll
      for (int pp = 1; pp < 8; ++pp) ks4[i4] += __builtin_bit_cast(f4, v[pp][i4]);
    }
    zs = __uint_as_float(zv[0]);
#pragma unroll
    for (int pp = 1; pp < 8; ++pp) zs += __uint_as_float(zv[pp]);
  }
  float* xb = (float*)(lds + L_KVX) + (wr * 2 + wc) * 1088;
#pragma unroll
  for (int i4 = 0; i4 < 4; ++i4) *(f4*)(xb + i4 * 256 + l * 4) = ks4[i4];
  xb[1024 + l] = zs;
  asm volatile("s_waitcnt lgkmcnt(0)" ::: "memory");
  lds_arrive(barc, l); lds_wait(barc, 4 * 18);
  u4 kA[2], zf[2];
  {
    const float* ob = (const float*)(lds + L_KVX) + ((wr ^ 1) * 2 + wc) * 1088;
#pragma unroll
    for (int i4 = 0; i4 < 4; ++i4) {
      f4 o = *(const f4*)(ob + i4 * 256 + l * 4);
      f4 a = wr ? o + ks4[i4] : ks4[i4] + o;
      kA[i4 >> 1][(i4 & 1) * 2] = pk2(a[0], a[1]); kA[i4 >> 1][(i4 & 1) * 2 + 1] = pk2(a[2], a[3]);
    }
    const float* z0p = (const float*)(lds + L_KVX) + (0 * 2 + wc) * 1088 + 1024 + h * 32;
    const float* z1p = (const float*)(lds + L_KVX) + (1 * 2 + wc) * 1088 + 1024 + h * 32;
#pragma unroll
    for (int q4 = 0; q4 < 4; ++q4) {
      f4 a = *(const f4*)(z0p + q4 * 4) + *(const f4*)(z1p + q4 * 4);
      zf[q4 >> 1][(q4 & 1) * 2] = pk2(a[0], a[1]); zf[q4 >> 1][(q4 & 1) * 2 + 1] = pk2(a[2], a[3]);
    }
  }
  asm volatile("s_waitcnt lgkmcnt(0)" ::: "memory");
  lds_arrive(barc, l);
  __amdgpu_buffer_rsrc_t ors = __builtin_amdgcn_make_buffer_rsrc(ws + WS_PQF, 0, 4u << 20, 0x00020000);
#pragma unroll
  for (int m = 0; m < 2; ++m) {
    f16v X = MFMA32(__builtin_bit_cast(s8, kA[0]), __builtin_bit_cast(s8, pf[m][0]), (f16v)(0.f));
    X = MFMA32(__builtin_bit_cast(s8, kA[1]), __builtin_bit_cast(s8, pf[m][1]), X);
    float den = 0;
#pragma unroll
    for (int e = 0; e < 4; ++e) {
      unsigned a0 = pf[m][0][e], a1 = pf[m][1][e], c0 = zf[0][e], c1 = zf[1][e];
      den = __builtin_amdgcn_fdot2_f32_bf16(__builtin_bit_cast(bf2, a0), __builtin_bit_cast(bf2, c0), den, false);
      den = __builtin_amdgcn_fdot2_f32_bf16(__builtin_bit_cast(bf2, a1), __builtin_bit_cast(bf2, c1), den, false);
    }
    auto rr = __builtin_amdgcn_permlane32_swap(__float_as_uint(den), __float_as_uint(den), false, false);
    den = __uint_as_float(rr[0]) + __uint_as_float(rr[1]);
    den = rbf(rbf(den) + 1e-6f);
    float inv = __builtin_amdgcn_rcpf(den);
    u4 o0, o1;
#pragma unroll
    for (int e = 0; e < 4; ++e) { o0[e] = pk2(X[2 * e] * inv, X[2 * e + 1] * inv); o1[e] = pk2(X[8 + 2 * e] * inv, X[9 + 2 * e] * inv); }
    unsigned dofs = (((mb * 4 + wr * 2 + m) * 32 + head) * 2) * 1024 + l * 16;
    __builtin_amdgcn_raw_buffer_store_b128(o0, ors, dofs, 0, 16);
    __builtin_amdgcn_raw_buffer_store_b128(o1, ors, dofs + 1024, 0, 16);
  }
  asm volatile("s_waitcnt vmcnt(0)" ::: "memory");
  GAS unsigned* ocnt = (GAS unsigned*)(ws + WS_CNT) + (88 + mb) * 32;
  if (l == 0) __hip_atomic_fetch_add(ocnt, 1u, __ATOMIC_RELAXED, __HIP_MEMORY_SCOPE_AGENT);
  if (w == 0) STMP(1, 5);
  lds_wait(barc, 4 * 19);
  {
#pragma unroll
    for (int pc = 0; pc < 4; ++pc)
      GLDS(wovoff, wob + (size_t)((w >> 1) * 512 + 7 * 64 + ((w & 1) * 4 + pc) * 8) * 2048, ldsb + 7 * 16384 + (w >> 1) * 8192 + ((w & 1) * 4 + pc) * 1024);
  }
  const int nb = hp, rt = w;
  const int tok = mb * 128 + rt * 32 + r31;
  const u16* xn = (const u16*)(ws + WS_XN);
  u2 xv[8], nv[8];
#pragma unroll
  for (int i8 = 0; i8 < 8; ++i8) {
    int col = nb * 64 + (i8 >> 2) * 32 + 8 * (i8 & 3) + 4 * h;
    xv[i8] = *(const u2*)(x + (size_t)tok * D + col); nv[i8] = *(const u2*)(xn + (size_t)tok * D + col);
  }
  WAITV(16);
  lds_arrive(barc, l);
  bwait(w == 0, (GAS unsigned*)(ws + WS_CNT) + 120 * 32, 1, (LAS unsigned*)(lds + L_BAR + 36), l);
  int sel = -1; float sg = 0.f;
#pragma unroll
  for (int i = 0; i < 8; ++i) {
    unsigned long long e = __hip_atomic_load((GAS unsigned long long*)(ws + WS_SEL) + i, __ATOMIC_RELAXED, __HIP_MEMORY_SCOPE_AGENT);
    if ((int)(unsigned)e == tok) { sel = i; sg = __uint_as_float((unsigned)(e >> 32)); }
  }
  bwait(w == 0, ocnt, 64, (LAS unsigned*)(lds + L_BAR + 32), l);
  if (w == 0) STMP(1, 6);
  const unsigned abase = ((mb * 4 + rt) * 32) * 2048 + l * 16;
  u4 a[4][8];
#define K3LDA(c) do { _Pragma("unroll") for (int f = 0; f < 8; ++f) \
    a[(c) & 3][f] = __builtin_amdgcn_raw_buffer_load_b128(ors, abase + ((f >> 2) * 16 + 2 * (c) + ((f >> 1) & 1)) * 2048 + (f & 1) * 1024, 0, 16); } while (0)
  K3LDA(0); K3LDA(1); K3LDA(2); K3LDA(3);
  lds_wait(barc, 4 * 20);
  const unsigned woad0 = ldsb + (4 * h + q) * 128 + (((q >> 1) & 1) * 64) + ((l >> 4) & 1) * 32 + p * 8;
  const unsigned woad1 = woad0 ^ 64;
  f16v oc[2];
  oc[0] = (f16v)(0.f); oc[1] = (f16v)(0.f);
#define K3HALF(c, kh_) do { \
    s4 b[16]; \
    const unsigned w0_ = woad0 + ((c) >> 2) * 65536, w1_ = woad1 + ((c) >> 2) * 65536; \
    _Pragma("unroll") for (int hh = 0; hh < 2; ++hh) { \
      RDTR(b[hh * 8 + 0], w0_, ((c) & 3) * 16384 + (kh_) * 8192 + hh * 4096); RDTR(b[hh * 8 + 1], w0_, ((c) & 3) * 16384 + (kh_) * 8192 + hh * 4096 + 1024); \
      RDTR(b[hh * 8 + 2], w1_, ((c) & 3) * 16384 + (kh_) * 8192 + hh * 4096); RDTR(b[hh * 8 + 3], w1_, ((c) & 3) * 16384 + (kh_) * 8192 + hh * 4096 + 1024); \
      RDTR(b[hh * 8 + 4], w0_, ((c) & 3) * 16384 + (kh_) * 8192 + hh * 4096 + 2048); RDTR(b[hh * 8 + 5], w0_, ((c) & 3) * 16384 + (kh_) * 8192 + hh * 4096 + 3072); \
      RDTR(b[hh * 8 + 6], w1_, ((c) & 3) * 16384 + (kh_) * 8192 + hh * 4096 + 2048); RDTR(b[hh * 8 + 7], w1_, ((c) & 3) * 16384 + (kh_) * 8192 + hh * 4096 + 3072); } \
    WAITL(12); SBAR(); \
    oc[0] = MFMA32(CAT(b[0], b[1]), __builtin_bit_cast(s8, a[(c) & 3][(kh_) * 4 + 0]), oc[0]); \
    oc[1] = MFMA32(CAT(b[2], b[3]), __builtin_bit_cast(s8, a[(c) & 3][(kh_) * 4 + 0]), oc[1]); SBAR(); \
    WAITL(8); SBAR(); \
    oc[0] = MFMA32(CAT(b[4], b[5]), __builtin_bit_cast(s8, a[(c) & 3][(kh_) * 4 + 1]), oc[0]); \
    oc[1] = MFMA32(CAT(b[6], b[7]), __builtin_bit_cast(s8, a[(c) & 3][(kh_) * 4 + 1]), oc[1]); SBAR(); \
    WAITL(4); SBAR(); \
    oc[0] = MFMA32(CAT(b[8], b[9]), __builtin_bit_cast(s8, a[(c) & 3][(kh_) * 4 + 2]), oc[0]); \
    oc[1] = MFMA32(CAT(b[10], b[11]), __builtin_bit_cast(s8, a[(c) & 3][(kh_) * 4 + 2]), oc[1]); SBAR(); \
    WAITL(0); SBAR(); \
    oc[0] = MFMA32(CAT(b[12], b[13]), __builtin_bit_cast(s8, a[(c) & 3][(kh_) * 4 + 3]), oc[0]); \
    oc[1] = MFMA32(CAT(b[14], b[15]), __builtin_bit_cast(s8, a[(c) & 3][(kh_) * 4 + 3]), oc[1]); SBAR(); } while (0)
#define K3C(c) do { K3HALF(c, 0); K3HALF(c, 1); if ((c) < 4) { K3LDA((c) + 4); } SBAR(); } while (0)
  K3C(0); K3C(1); K3C(2); K3C(3); K3C(4); K3C(5); K3C(6); K3C(7);
  if (w == 0) STMP(1, 7);
  const bool anysel = __ballot(sel >= 0) != 0ull;
  if (anysel) gwait((GAS unsigned*)(ws + WS_CNT) + (104 + nb) * 32, 56);
  __amdgpu_buffer_rsrc_t yrs = __builtin_amdgcn_make_buffer_rsrc(ws + WS_YP, 0, 14 * 8 * D * 4, 0x00020000);
  u2 ovs[8];
#pragma unroll
  for (int i8 = 0; i8 < 8; ++i8) {
    const f16v& oo = oc[i8 >> 2];
    const int i4 = i8 & 3;
    int col = nb * 64 + (i8 >> 2) * 32 + 8 * i4 + 4 * h;
    float av[4] = {oo[4 * i4], oo[4 * i4 + 1], oo[4 * i4 + 2], oo[4 * i4 + 3]};
    float b[4] = {lo(nv[i8][0]), hi(nv[i8][0]), lo(nv[i8][1]), hi(nv[i8][1])};
    float xx[4] = {lo(xv[i8][0]), hi(xv[i8][0]), lo(xv[i8][1]), hi(xv[i8][1])};
    if (sel >= 0) {
      f4 y = (f4)(0.f);
#pragma unroll
      for (int ks = 0; ks < 14; ++ks) y += __builtin_bit_cast(f4, __builtin_amdgcn_raw_buffer_load_b128(yrs, ((ks * 8 + sel) * D + col) * 4, 0, 16));
#pragma unroll
      for (int e = 0; e < 4; ++e) b[e] = rbf(b[e] + rbf(rbf(y[e]) * sg));
    }
    u2 ov;
#pragma unroll
    for (int e = 0; e < 4; ++e) av[e] = rbf(xx[e] + rbf(av[e])) + b[e];
    ov[0] = pk2(av[0], av[1]); ov[1] = pk2(av[2], av[3]);
    ovs[i8] = ov;
  }
#pragma unroll
  for (int k2 = 0; k2 < 4; ++k2) {
    auto r0 = __builtin_amdgcn_permlane32_swap(ovs[2 * k2][0], ovs[2 * k2 + 1][0], false, false);
    auto r1 = __builtin_amdgcn_permlane32_swap(ovs[2 * k2][1], ovs[2 * k2 + 1][1], false, false);
    u4 o4; o4[0] = r0[0]; o4[1] = r1[0]; o4[2] = r0[1]; o4[3] = r1[1];
    *(u4*)(out + (size_t)tok * D + nb * 64 + 16 * k2 + 8 * h) = o4;
  }
  { unsigned long long t0_ = *(const unsigned long long*)(ws + (33u<<20)); unsigned long long d_ = ((const unsigned long long*)(ws + (34u<<20)))[blockIdx.x]; while (__builtin_amdgcn_s_memrealtime() - t0_ < 6000ull + 4ull * d_) __builtin_amdgcn_s_sleep(1); }
  asm volatile(".Lk2_code_end:");
}

extern "C" void kernel_launch(void* const* d_in, const int* in_sizes, int n_in, void* d_out, int out_size,
                              void* d_ws, size_t ws_size, hipStream_t stream) {
  const u16* x = (const u16*)d_in[0];
  const u16* wqkv = (const u16*)d_in[1];
  const u16* wo = (const u16*)d_in[2];
  const u16* wr = (const u16*)d_in[4];
  const u16* wg = (const u16*)d_in[5];
  const u16* wu = (const u16*)d_in[6];
  const u16* wd = (const u16*)d_in[7];
  char* ws = (char*)d_ws;
  (void)hipFuncSetAttribute((const void*)k2, hipFuncAttributeMaxDynamicSharedMemorySize, K2_LDS);
  k1_ln<<<256, 256, 0, stream>>>(x, wr, ws, wg, wu);
  k2<<<256, 512, K2_LDS, stream>>>(wqkv, wg, wu, wd, ws, x, wo, (u16*)d_out);
}
```

```cpp
#include <hip/hip_runtime.h>
#include <stdint.h>

typedef unsigned short u16;
typedef short s4 __attribute__((ext_vector_type(4)));
typedef short s8 __attribute__((ext_vector_type(8)));
typedef float f4 __attribute__((ext_vector_type(4)));
typedef float f2 __attribute__((ext_vector_type(2)));
typedef unsigned u4 __attribute__((ext_vector_type(4)));
typedef unsigned u2 __attribute__((ext_vector_type(2)));
typedef __bf16 bf2 __attribute__((ext_vector_type(2)));
#define LAS __attribute__((address_space(3)))
#define DEV __device__ __forceinline__

constexpr int D = 1024, F = 3584;
#define NOMOE 0
#define WS0 4
#define HROT 1

constexpr unsigned WS_E = 0;
constexpr unsigned WS_YF = 8192;
constexpr unsigned WS_CAND = 12288;
constexpr unsigned WS_OFL = 16384;
constexpr unsigned WS_GF = 32768;
constexpr unsigned WS_RG = 49152;
constexpr unsigned WS_ST = 65536;
constexpr unsigned WS_ZP = 131072;
constexpr unsigned WS_CS = 196608;
constexpr unsigned WS_KVP = 1u << 20;
constexpr unsigned WS_GP = 2u << 20;
constexpr unsigned WS_YP = 3u << 20;
constexpr unsigned WS_OF = 5u << 20;
constexpr unsigned WS_SZ = 9u << 20;

constexpr int STG = 40960;
constexpr int L_KVX = 40960;
constexpr int L_PQ = 53248;
constexpr int L_M = 159744, L_RS = L_M + 512, L_CS = L_RS + 512, L_SEL = L_CS + 768, L_FL = L_SEL + 64;
constexpr int LDS_SZ = 163840;
constexpr int NBAR = 15;
constexpr unsigned POIS = 0xFFFFFFFFu;
constexpr int wo_off(int pos) { return pos < 9 ? 122880 + 4096 * pos : pos < 22 ? 69632 + 4096 * (pos - 9) : 4096 * (pos - 22); }

DEV unsigned pk2(float a, float b) { bf2 v; v[0] = (__bf16)a; v[1] = (__bf16)b; return __builtin_bit_cast(unsigned, v); }
DEV float lo(unsigned u) { return __uint_as_float(u << 16); }
DEV float hi(unsigned u) { return __uint_as_float(u & 0xFFFF0000u); }
DEV float rbf(float a) { return lo(pk2(a, a)); }
DEV float dot2(unsigned a, unsigned b, float c) { return __builtin_amdgcn_fdot2_f32_bf16(__builtin_bit_cast(bf2, a), __builtin_bit_cast(bf2, b), c, false); }
#define RD128(dst, addr, OFF) asm volatile("ds_read_b128 %0, %1 offset:%2" : "=v"(dst) : "v"(addr), "i"(OFF) : "memory")
#define RDTR(dst, addr, OFF) asm volatile("ds_read_b64_tr_b16 %0, %1 offset:%2" : "=v"(dst) : "v"(addr), "i"(OFF) : "memory")
#define SBAR() __builtin_amdgcn_sched_barrier(0)
#define CAT(x, y) __builtin_shufflevector(x, y, 0, 1, 2, 3, 4, 5, 6, 7)
#define WAITV(n) asm volatile("s_waitcnt vmcnt(%0)" ::"i"(n) : "memory")
#define WAITL(n) asm volatile("s_waitcnt lgkmcnt(%0)" ::"i"(n) : "memory")
#define MEMB() asm volatile("" ::: "memory")
#define SLEEP(n) asm volatile("s_sleep %0" ::"i"(n) : "memory")
#define BAR() asm volatile("s_barrier" ::: "memory")
#define GL0(vo, sb, m0v) asm volatile("s_mov_b32 m0, %2\n s_nop 0\n global_load_lds_dwordx4 %0, %1" ::"v"(vo), "s"(sb), "s"(m0v) : "memory")
#define GLI(vo, sb, OFF) asm volatile("global_load_lds_dwordx4 %0, %1 offset:%2" ::"v"(vo), "s"(sb), "i"(OFF) : "memory")
#define MFMA16(a, b, c) __builtin_amdgcn_mfma_f32_16x16x32_bf16(a, b, c, 0, 0, 0)
#define S8(x) __builtin_bit_cast(s8, x)
#define LD128(off) __builtin_amdgcn_raw_buffer_load_b128(wsr, (off), 0, 16)
#define LD64(off) __builtin_amdgcn_raw_buffer_load_b64(wsr, (off), 0, 16)
#define ST128(v, off) __builtin_amdgcn_raw_buffer_store_b128((v), wsr, (off), 0, 16)
#define ST64(v, off) __builtin_amdgcn_raw_buffer_store_b64((v), wsr, (off), 0, 16)
#define NTL(p) __builtin_nontemporal_load(p)
constexpr unsigned SPIN = 1u << 21;
DEV const char* uptr(const char* p) {
  unsigned long long v = (unsigned long long)p;
  unsigned lo_ = __builtin_amdgcn_readfirstlane((unsigned)v), hi_ = __builtin_amdgcn_readfirstlane((unsigned)(v >> 32));
  return (const char*)(((unsigned long long)hi_ << 32) | lo_);
}
DEV void lsig(unsigned addr, unsigned val) { asm volatile("s_waitcnt lgkmcnt(0)\n ds_write_b32 %0, %1" ::"v"(addr), "v"(val) : "memory"); }
template <int SL = 0> DEV void lwait(unsigned addr, unsigned mask, unsigned target) {
  _Pragma("unroll 1") for (unsigned it = 0; it < SPIN; ++it) {
    unsigned v;
    asm volatile("ds_read_b32 %0, %1\n s_waitcnt lgkmcnt(0)" : "=v"(v) : "v"(addr) : "memory");
    if (((unsigned)__ballot(v < target) & mask) == 0u) break;
    if (SL) SLEEP(SL);
  }
}
DEV unsigned umax3(unsigned a, unsigned b, unsigned c) { return max(max(a, b), c); }
template <int C> DEV float dppf(float v) { return __int_as_float(__builtin_amdgcn_update_dpp(0, __float_as_int(v), C, 0xf, 0xf, false)); }
DEV float wave_sum(float v) {
  v += dppf<0x111>(v); v += dppf<0x112>(v); v += dppf<0x114>(v); v += dppf<0x118>(v);
  v += __int_as_float(__builtin_amdgcn_update_dpp(0, __float_as_int(v), 0x142, 0xa, 0xf, false));
  v += __int_as_float(__builtin_amdgcn_update_dpp(0, __float_as_int(v), 0x143, 0xc, 0xf, false));
  return __int_as_float(__builtin_amdgcn_readlane(__float_as_int(v), 63));
}
DEV unsigned wave_max(unsigned v) {
  v = max(v, (unsigned)__builtin_amdgcn_update_dpp(0, (int)v, 0x111, 0xf, 0xf, false));
  v = max(v, (unsigned)__builtin_amdgcn_update_dpp(0, (int)v, 0x112, 0xf, 0xf, false));
  v = max(v, (unsigned)__builtin_amdgcn_update_dpp(0, (int)v, 0x114, 0xf, 0xf, false));
  v = max(v, (unsigned)__builtin_amdgcn_update_dpp(0, (int)v, 0x118, 0xf, 0xf, false));
  v = max(v, (unsigned)__builtin_amdgcn_update_dpp(0, (int)v, 0x142, 0xa, 0xf, false));
  v = max(v, (unsigned)__builtin_amdgcn_update_dpp(0, (int)v, 0x143, 0xc, 0xf, false));
  return (unsigned)__builtin_amdgcn_readlane((int)v, 63);
}
DEV unsigned phi2(f2 u, f2 rs) {
  f2 t = u * rs;
  f2 a = t * 1.44269504f, p = t + 1.f, r;
  r[0] = __builtin_amdgcn_fmed3f(__builtin_amdgcn_exp2f(a[0]), p[0], 1.f);
  r[1] = __builtin_amdgcn_fmed3f(__builtin_amdgcn_exp2f(a[1]), p[1], 1.f);
  r += 1e-6f;
  return pk2(r[0], r[1]);
}

#define STDECL
#define STMP(i)
#define BST(i)
#define STFLUSH()

DEV u4 row_stats(u4 a, u4 b, u4 w0, u4 w1, int row, unsigned tag, __amdgpu_buffer_rsrc_t wsr, int l) {
  float s = 0;
#pragma unroll
  for (int j = 0; j < 4; ++j) { unsigned x0 = a[j], x1 = b[j]; s = dot2(x0, 0x3F803F80u, s); s = dot2(x1, 0x3F803F80u, s); }
  const float m = wave_sum(s) * (1.f / D);
  f2 v[8], q2 = (f2)(0.f);
#pragma unroll
  for (int j = 0; j < 4; ++j) { v[j] = (f2){lo(a[j]), hi(a[j])} - m; v[4 + j] = (f2){lo(b[j]), hi(b[j])} - m; }
#pragma unroll
  for (int j = 0; j < 8; ++j) q2 += v[j] * v[j];
  const float q = wave_sum(q2[0] + q2[1]);
  const float rs = 1.f / sqrtf(q * (1.f / D) + 1e-5f);
  float d = 0;
#pragma unroll
  for (int j = 0; j < 4; ++j) {
    f2 n0 = v[j] * rs, n1 = v[4 + j] * rs;
    unsigned c0 = w0[j], c1 = w1[j];
    d = dot2(pk2(n0[0], n0[1]), c0, d); d = dot2(pk2(n1[0], n1[1]), c1, d);
  }
  d = wave_sum(d);
  const unsigned rb = pk2(d, d) & 0xFFFFu;
  const unsigned mono = (rb & 0x8000u) ? (~rb & 0xFFFFu) : (rb | 0x8000u);
  const unsigned key = (mono << 16) | (unsigned)(0xFFFF - row);
  if (l == 0) ST64(((u2){key, tag}), WS_RG + row * 8);
  return (u4){__float_as_uint(m), tag, __float_as_uint(rs), tag};
}

struct FQ { s8 a[4]; s4 b[12]; };

__global__ __launch_bounds__(512) void fused(const u16* __restrict__ x, const u16* __restrict__ wqkv, const u16* __restrict__ wo,
                                             const u16* __restrict__ wrt, const u16* __restrict__ wg, const u16* __restrict__ wu,
                                             const u16* __restrict__ wd, char* __restrict__ ws, u16* __restrict__ out) {
  extern __shared__ __attribute__((aligned(16))) char lds[];
  asm volatile(".Lcode_beg:");
  const int w = __builtin_amdgcn_readfirstlane(threadIdx.x >> 6), l = threadIdx.x & 63;
  STDECL;
  STMP(0);
  const int g = blockIdx.x, xcd = g & 7, j = g >> 3, ii = j >> 2;
  const int mb = (xcd & 1) * 8 + ii, hp = (xcd >> 1) * 4 + (j & 3);
  const int rot = 2 * xcd + 4 * (ii & 3);
  const int nb = hp;
  const int hrot = HROT == 0 ? 0 : HROT == 1 ? ((2 * (j & 3) + ii) & 7) * 4 : ((2 * (j & 3)) & 7) * 4;
  const unsigned ldsb = (unsigned)(size_t)(LAS char*)lds;
  const unsigned adFL = ldsb + L_FL + 4 * (l & 7), myFL = ldsb + L_FL + 4 * w;
  const __amdgpu_buffer_rsrc_t wsr = __builtin_amdgcn_make_buffer_rsrc(ws, 0, WS_SZ, 0x00020000);
  const int r15 = l & 15, g4 = l >> 4, q = (l & 15) >> 2, p = l & 3;
  const char* wob = uptr((const char*)wo + nb * 128);
  const unsigned wov = (l >> 3) * 2048 + (((l & 7) ^ (((l >> 4) & 3) << 1)) * 16);
#define WODMA(pos_lds, head) do { const char* sb_ = wob + (size_t)(head) * 65536; unsigned m_ = ldsb + (pos_lds); \
    GL0(wv0, sb_, m_); GLI(wv1, sb_, 1024); GLI(wv2, sb_, 2048); GLI(wv3, sb_, 3072); } while (0)

  if (w == 4 || w == 5) {
    const int lw = w - 4;
    unsigned ev;
    asm volatile("global_load_dword %0, %1, %2 sc1" : "=v"(ev) : "v"(0u), "s"(ws) : "memory");
    unsigned vfa[8], vfb[12];
#pragma unroll
    for (int i = 0; i < 8; ++i) { int row = 8 * (lw * 8 + i) + (l >> 3); vfa[i] = row * 2048 + (((l & 7) ^ ((row >> 1) & 7)) * 16) - (i & 3) * 1024; }
#pragma unroll
    for (int i = 0; i < 12; ++i) {
      const int pc = lw * 12 + i, row = (pc & 7) * 8 + (l >> 3);
      vfb[i] = 4096 + row * 6144 + (pc >> 3) * 2048 + (((l & 7) ^ ((((row >> 1) & 1) << 2) | (((row >> 3) & 1) << 1))) * 16) - (i & 3) * 1024;
    }
    const char* Ab = uptr((const char*)x + (size_t)mb * 128 * 2048);
    const char* Bb = uptr((const char*)wqkv + hp * 128) - 4096;
    auto issue = [&](int s, int buf) {
      const int kt = (s + rot) & 15;
      const char* sa = Ab + kt * 128; const char* sb = Bb + (size_t)(kt * 64) * 6144;
      const unsigned ma = ldsb + buf * STG + lw * 8192, mq = ldsb + buf * STG + 16384 + lw * 12288;
#pragma unroll
      for (int i = 0; i < 8; ++i) { if ((i & 3) == 0) { unsigned mm = ma + i * 1024; GL0(vfa[i], sa, mm); } else GLI(vfa[i], sa, (i & 3) * 1024); }
#pragma unroll
      for (int i = 0; i < 12; ++i) { if ((i & 3) == 0) { unsigned mm = mq + i * 1024; GL0(vfb[i], sb, mm); } else GLI(vfb[i], sb, (i & 3) * 1024); }
    };
    issue(0, 0); issue(1, 1);
    const unsigned wva = wov + lw * 32768, wvb = wva + 15360;
    BST(32);
    WAITV(20); BST(0); BAR(); BST(16);
    asm volatile("" : "+v"(ev));
    const unsigned tag = __builtin_amdgcn_readfirstlane(ev) + 1u;
    issue(2, 2);
    int buf = 0;
#pragma unroll 1
    for (int s = 0; s < 13; ++s) {
      if (s > WS0 && s <= WS0 + 9) WAITV(22); else WAITV(20);
      BST(s + 1);
      BAR();
      BST(s + 17);
      issue(s + 3, buf);
      BST(s + 34);
      if (s >= WS0 && s < WS0 + 9) {
        const char* sb = wob + (size_t)((s - WS0 + hrot) & 31) * 65536;
        const unsigned mm = ldsb + 122880 + (s - WS0) * 4096 + lw * 2048;
        GL0(wva, sb, mm); GLI(wvb, sb, 1024);
      }
      buf = buf == 2 ? 0 : buf + 1;
    }
    __builtin_amdgcn_s_waitcnt(0x0F70); WAITV(0); BST(14); BAR(); BST(30);
    STMP(1);
    const int hd = hp * 2 + lw;
    u4 sel[2];
#pragma unroll
    for (int e = 0; e < 2; ++e) {
      const unsigned v = (r15 >> 2) == g4 ? (((r15 & 3) == 2 * e ? 0x3F80u : 0u) | ((r15 & 3) == 2 * e + 1 ? 0x3F800000u : 0u)) : 0u;
      sel[0][e] = v; sel[0][2 + e] = 0u; sel[1][e] = 0u; sel[1][2 + e] = v;
    }
    u4 pv[16][2], zv0, zv1, pq[8]; unsigned pr;
#pragma unroll
    for (int pp = 0; pp < 16; ++pp) asm volatile("" : "=v"(pv[pp][0]), "=v"(pv[pp][1]));
    asm volatile("" : "=v"(zv0), "=v"(zv1), "=v"(pr));
    const char* kvb = uptr(ws + WS_KVP + (size_t)hd * 32768);
    const char* zb = uptr(ws + WS_ZP + (size_t)hd * 2048);
    const unsigned lv16 = l * 16, zoff = r15 * 128 + g4 * 32, poff = (l & 31) * 1024;
    lwait<1>(adFL, lw ? 0x0Au : 0x05u, 2);
    __builtin_amdgcn_s_setprio(3);
    STMP(2);
    unsigned ok = 0, done = 0, prdy = 15u; bool first = true;
    _Pragma("unroll 1") for (unsigned it = 0; it < SPIN; ++it) {
      const unsigned miss = 15u & ~ok;
      const bool last1 = (miss & (miss - 1u)) == 0u;
      const unsigned cand = last1 ? miss : (prdy & miss);
#pragma unroll
      for (int gq = 0; gq < 4; ++gq)
        if ((cand >> gq) & 1) {
#pragma unroll
          for (int i = 0; i < 4; ++i) {
            const char* sb = kvb + (gq * 4 + (i & 2)) * 2048;
            asm volatile("global_load_dwordx4 %0, %1, %2 offset:%3 sc1" : "+v"(pv[gq * 4 + i][0]) : "v"(lv16), "s"(sb), "i"((i & 1) * 2048) : "memory");
            asm volatile("global_load_dwordx4 %0, %1, %2 offset:%3 sc1" : "+v"(pv[gq * 4 + i][1]) : "v"(lv16), "s"(sb), "i"((i & 1) * 2048 + 1024) : "memory");
          }
        }
      if (cand == miss) {
        asm volatile("global_load_dwordx4 %0, %1, %2 sc1" : "+v"(zv0) : "v"(zoff), "s"(zb) : "memory");
        asm volatile("global_load_dwordx4 %0, %1, %2 offset:16 sc1" : "+v"(zv1) : "v"(zoff), "s"(zb) : "memory");
      }
      if (!last1) asm volatile("global_load_dword %0, %1, %2 sc1" : "+v"(pr) : "v"(poff), "s"(kvb) : "memory");
      if (first) {
        const unsigned pa = ldsb + L_PQ + lw * 4096 + l * 16;
#pragma unroll
        for (int i = 0; i < 8; ++i) RD128(pq[i], pa, (i >> 2) * 8192 + (i & 3) * 1024);
        WAITL(0);
      }
#define GSUM(msk) _Pragma("unroll") for (int gq = 0; gq < 4; ++gq) if (((msk) >> gq) & 1) { \
          f4 ga[4]; \
          _Pragma("unroll") for (int i = 0; i < 4; ++i) \
            _Pragma("unroll") for (int t = 0; t < 4; ++t) ga[t] = MFMA16(S8(sel[t & 1]), S8(pv[gq * 4 + i][t >> 1]), i ? ga[t] : (f4)(0.f)); \
          _Pragma("unroll") for (int t = 0; t < 4; ++t) pv[gq * 4 + (t >> 1)][t & 1] = __builtin_bit_cast(u4, ga[t]); }
      GSUM(ok & ~done);
      done = ok;
      WAITV(0);
#pragma unroll
      for (int gq = 0; gq < 4; ++gq)
        if ((cand >> gq) & 1) {
#pragma unroll
          for (int i = 0; i < 4; ++i) asm volatile("" : "+v"(pv[gq * 4 + i][0]), "+v"(pv[gq * 4 + i][1]));
          unsigned m = umax3(pv[gq * 4][0][0], pv[gq * 4][1][0], pv[gq * 4 + 1][0][0]);
          m = umax3(m, pv[gq * 4 + 1][1][0], pv[gq * 4 + 2][0][0]);
          m = umax3(m, pv[gq * 4 + 2][1][0], pv[gq * 4 + 3][0][0]);
          m = max(m, pv[gq * 4 + 3][1][0]);
          if (__ballot(m == POIS) == 0ull) ok |= 1u << gq;
        }
      asm volatile("" : "+v"(zv0), "+v"(zv1), "+v"(pr));
      if (ok == 15u && cand == miss) { if (__ballot(max(zv0[0], zv1[0]) == POIS) == 0ull) break; }
      if (first) { first = false; const unsigned m2 = 15u & ~ok; if (m2 & (m2 - 1u)) lsig(myFL, 1); }
      if (!last1) {
        const unsigned b = (unsigned)__ballot(pr != POIS);
        prdy = 0;
#pragma unroll
        for (int gq = 0; gq < 4; ++gq) prdy |= (((b >> (8 * gq)) & 255u) == 255u ? 1u : 0u) << gq;
        if (!(prdy & ~ok)) SLEEP(2);
      }
    }
    STMP(3);
    GSUM(15u & ~done);
    u4 kA[2], zA;
    {
      float z[8];
#pragma unroll
      for (int e = 0; e < 4; ++e) { z[e] = __uint_as_float(zv0[e]); z[4 + e] = __uint_as_float(zv1[e]); }
#define ZR(n) "v_add_f32_dpp %0, %0, %0 row_ror:" #n " row_mask:0xf bank_mask:0xf\n v_add_f32_dpp %1, %1, %1 row_ror:" #n " row_mask:0xf bank_mask:0xf\n" \
              "v_add_f32_dpp %2, %2, %2 row_ror:" #n " row_mask:0xf bank_mask:0xf\n v_add_f32_dpp %3, %3, %3 row_ror:" #n " row_mask:0xf bank_mask:0xf\n" \
              "v_add_f32_dpp %4, %4, %4 row_ror:" #n " row_mask:0xf bank_mask:0xf\n v_add_f32_dpp %5, %5, %5 row_ror:" #n " row_mask:0xf bank_mask:0xf\n" \
              "v_add_f32_dpp %6, %6, %6 row_ror:" #n " row_mask:0xf bank_mask:0xf\n v_add_f32_dpp %7, %7, %7 row_ror:" #n " row_mask:0xf bank_mask:0xf\n"
      asm volatile("s_nop 1\n" ZR(8) ZR(4) ZR(2) ZR(1) : "+v"(z[0]), "+v"(z[1]), "+v"(z[2]), "+v"(z[3]), "+v"(z[4]), "+v"(z[5]), "+v"(z[6]), "+v"(z[7]));
#pragma unroll
      for (int e = 0; e < 4; ++e) zA[e] = pk2(z[2 * e], z[2 * e + 1]);
    }
    float inv[8];
#pragma unroll
    for (int i = 0; i < 8; ++i) { f4 dn = MFMA16(S8(zA), S8(pq[i]), (f4)(0.f)); inv[i] = dn[0]; }
#pragma unroll
    for (int t = 0; t < 4; ++t) {
#define PF(gq) __builtin_bit_cast(f4, pv[(gq) * 4 + (t >> 1)][t & 1])
      f4 s = (PF(0) + PF(1)) + (PF(2) + PF(3));
      kA[t >> 1][(t & 1) * 2] = pk2(s[0], s[1]); kA[t >> 1][(t & 1) * 2 + 1] = pk2(s[2], s[3]);
    }
    f4 o0[8], o1[8];
#pragma unroll
    for (int i = 0; i < 8; ++i) {
      o0[i] = MFMA16(S8(kA[0]), S8(pq[i]), (f4)(0.f)); o1[i] = MFMA16(S8(kA[1]), S8(pq[i]), (f4)(0.f));
      inv[i] = __builtin_amdgcn_rcpf(inv[i] + 1e-6f);
    }
#pragma unroll
    for (int i = 0; i < 8; ++i) {
      o0[i] *= inv[i]; o1[i] *= inv[i];
      u4 o; o[0] = min(pk2(o0[i][0], o0[i][1]), POIS - 1u); o[1] = pk2(o0[i][2], o0[i][3]); o[2] = pk2(o1[i][0], o1[i][1]); o[3] = pk2(o1[i][2], o1[i][3]);
      ST128(o, WS_OF + ((mb * 8 + i) * 32 + hd) * 1024 + l * 16);
    }
    if (l < 4) ST64(((u2){tag, tag}), WS_OFL + (l * 16 + mb) * 256 + hd * 8);
    lsig(myFL, 1);
    STMP(4);
    STFLUSH();
    asm volatile(".Lend_l:");
    return;
  }

  const unsigned ev0 = __builtin_amdgcn_raw_buffer_load_b32(wsr, WS_E, 0, 16);
  if (w >= 6) {
    const int mw = w - 6;
    if (mw == 1 && l < 8) *(LAS unsigned*)(lds + L_FL + 4 * l) = 0u;
    const int row0 = mb * 128 + hp * 8 + 4 + 2 * mw;
    u4 xa0 = *(const u4*)(x + row0 * D + l * 8), xb0 = *(const u4*)(x + row0 * D + 512 + l * 8);
    u4 rw0 = *(const u4*)(wrt + l * 8), rw1 = *(const u4*)(wrt + 512 + l * 8);
    u4 xa1 = *(const u4*)(x + row0 * D + D + l * 8), xb1 = *(const u4*)(x + row0 * D + D + 512 + l * 8);
    unsigned cw = 0;
    if (mw == 1) {
      unsigned long long pc; unsigned off, len;
      asm volatile("s_getpc_b64 %0\n.Lcode_here:" : "=s"(pc));
      asm volatile("s_mov_b32 %0, (.Lcode_here - .Lcode_beg)" : "=s"(off));
      unsigned l1, l2;
      asm volatile("s_mov_b32 %0, (.Lend_c - .Lcode_beg)" : "=s"(len));
      asm volatile("s_mov_b32 %0, (.Lend_l - .Lcode_beg)" : "=s"(l1));
      asm volatile("s_mov_b32 %0, (.Lend_h - .Lcode_beg)" : "=s"(l2));
      len = max(len, max(l1, l2));
      const unsigned o = min((unsigned)(l * 32 + j) * 64u, len - 4u);
      asm volatile("global_load_dword %0, %1, %2" : "+v"(cw) : "v"(o), "s"(pc - off) : "memory");
    }
    {
      const u4 pz = (u4)(POIS);
      const int hd = hp * 2 + mw;
#pragma unroll
      for (int i = 0; i < 8; ++i) ST128(pz, WS_OF + ((mb * 8 + i) * 32 + hd) * 1024 + l * 16);
      ST128(pz, WS_KVP + (hd * 16 + mb) * 2048 + l * 16);
      ST128(pz, WS_KVP + (hd * 16 + mb) * 2048 + 1024 + l * 16);
      if (l < 8) ST128(pz, WS_ZP + (hd * 16 + mb) * 128 + l * 16);
    }
    const unsigned tag = __builtin_amdgcn_readfirstlane(ev0) + 1u, tag2 = tag * 0x9E3779B1u;
    int nbar = 0;
#define TICK() do { if (nbar < NBAR) { BST(nbar); BAR(); ++nbar; } else SLEEP(2); } while (0)
    { const u4 st0 = row_stats(xa0, xb0, rw0, rw1, row0, tag, wsr, l); if (l == 0) ST128(st0, WS_ST + row0 * 16); }
    BST(0);
    BAR();
    unsigned wt0, wt1;
    { const char* tb = wob + (size_t)(ii * 128 + mw * 64) * 2048; const unsigned to = (l >> 1) * 2048 + (l & 1) * 64;
      asm volatile("global_load_dword %0, %1, %2" : "=v"(wt0) : "v"(to), "s"(tb) : "memory");
      asm volatile("global_load_dword %0, %1, %2" : "=v"(wt1) : "v"(to), "s"(tb + 65536) : "memory"); }
    const u4 st1 = row_stats(xa1, xb1, rw0, rw1, row0 + 1, tag, wsr, l);
    WAITL(0);
    BST(1);
    BAR();
    nbar = 2;
    if (ii >> 2) { BST(2); BAR(); nbar = 3; }
    STMP(1);
    {
      const unsigned bo = ldsb + (nbar - 1) * STG + 16384 + (8 * g4 + q) * 128 + (p & 1) * 8;
      const int sw = ((q >> 1) << 2) | ((g4 & 1) << 1);
      const unsigned b0 = bo + (((mw * 4 + (p >> 1)) ^ sw) * 16), b1 = bo + (((mw * 4 + 2 + (p >> 1)) ^ sw) * 16);
      const u4 ones = (u4)(0x3F803F80u);
      f4 c[6];
#pragma unroll
      for (int ss = 0; ss < 2; ++ss) {
        s4 b[12];
#pragma unroll
        for (int n = 0; n < 6; ++n) { RDTR(b[2 * n], (n & 1) ? b1 : b0, (n >> 1) * 8192 + ss * 4096); RDTR(b[2 * n + 1], (n & 1) ? b1 : b0, (n >> 1) * 8192 + ss * 4096 + 512); }
        WAITL(0); SBAR();
#pragma unroll
        for (int n = 0; n < 6; ++n) c[n] = MFMA16(S8(ones), CAT(b[2 * n], b[2 * n + 1]), ss ? c[n] : (f4)(0.f));
        SBAR();
      }
      const float v0 = g4 == 0 ? c[0][0] : g4 == 1 ? c[1][0] : g4 == 2 ? c[2][0] : c[3][0];
      const float v1 = g4 == 0 ? c[4][0] : c[5][0];
      const unsigned cb = WS_CS + ((hp * 2 + mw) * 16 + mb) * 768;
      TICK();
      WAITV(0);
      asm volatile("" ::"v"(wt0), "v"(wt1), "v"(cw));
      if (l == 0) ST128(st1, WS_ST + (row0 + 1) * 16);
      ST64(((u2){__float_as_uint(v0), tag}), cb + l * 8);
      if (l < 32) ST64(((u2){__float_as_uint(v1), tag}), cb + 512 + l * 8);
    }
    if (g >= 224) {
      unsigned key[16];
      _Pragma("unroll 1") for (unsigned it = 0; it < SPIN; ++it) {
        u4 kk[8];
#pragma unroll
        for (int i = 0; i < 8; ++i) kk[i] = LD128(WS_RG + mw * 8192 + i * 1024 + l * 16);
        TICK();
        bool ok = true;
#pragma unroll
        for (int i = 0; i < 8; ++i) { ok = ok && kk[i][1] == tag && kk[i][3] == tag; key[2 * i] = kk[i][0]; key[2 * i + 1] = kk[i][2]; }
        if (__ballot(!ok) == 0ull) break;
      }
      unsigned base = 0, mine = 0;
#pragma unroll 1
      for (int it = 0; it < 8; ++it) {
        unsigned m = key[0];
#pragma unroll
        for (int i = 1; i < 16; ++i) m = max(m, key[i]);
        m = wave_max(m);
#pragma unroll
        for (int i = 0; i < 16; ++i) key[i] -= m;
        base += m;
        mine = l == it ? base : mine;
        if (it & 1) TICK();
      }
      if (l < 8) ST64(((u2){mine, tag}), WS_CAND + (g - 224) * 128 + mw * 64 + l * 8);
      STMP(2);
    }
    u4 stv; float cs0, cs1;
    _Pragma("unroll 1") for (unsigned it = 0; it < SPIN; ++it) {
      u4 cv[16];
      stv = LD128(WS_ST + (mb * 128 + mw * 64 + l) * 16);
      const unsigned cb = WS_CS + (hp * 2 + mw) * 12288 + (l < 48 ? l : 0) * 16;
#pragma unroll
      for (int i = 0; i < 16; ++i) cv[i] = LD128(cb + i * 768);
      TICK(); TICK();
      bool ok = stv[1] == tag && stv[3] == tag;
      cs0 = 0.f; cs1 = 0.f;
#pragma unroll
      for (int i = 0; i < 16; ++i) { ok = ok && cv[i][1] == tag && cv[i][3] == tag; cs0 += __uint_as_float(cv[i][0]); cs1 += __uint_as_float(cv[i][2]); }
      if (__ballot(!ok) == 0ull) break;
    }
    asm volatile("" : "+v"(cs0), "+v"(cs1));
    STMP(3);
    const int u = NOMOE ? g + (tag ? 999 : 0) : g, chunk = u >> 2, ksp = u & 3;
    const int np = l & 7, kset = (l >> 3) & 1;
    u4 gw[4][8], dw[2][8];
    auto wload = [&]() {
      if (u < 224) {
        const u16* wp = (mw ? wu : wg) + (size_t)(256 * ksp + 32 * kset + 8 * g4) * F + 64 * chunk + 8 * np;
#pragma unroll
        for (int it = 0; it < 4; ++it)
#pragma unroll
          for (int rr = 0; rr < 8; ++rr) gw[it][rr] = NTL((const u4*)(wp + (size_t)(64 * it + rr) * F));
        const u16* dp = wd + (size_t)(64 * chunk + 8 * g4) * D + 256 * ksp + 128 * mw + 8 * r15;
#pragma unroll
        for (int it = 0; it < 2; ++it)
#pragma unroll
          for (int rr = 0; rr < 8; ++rr) dw[it][rr] = NTL((const u4*)(dp + (size_t)(32 * it + rr) * D));
      }
    };
    unsigned cand = 0; bool have = false;
    auto pollc = [&]() { u2 c = LD64(WS_CAND + (g & 31) * 128 + r15 * 8); TICK(); cand = c[0]; have = __ballot(c[1] != tag) == 0ull; };
    int idx = 0; float sg = 0.f;
    auto rank = [&]() {
      int rk = 0;
#pragma unroll
      for (int i = 0; i < 16; ++i) rk += ((unsigned)__builtin_amdgcn_readlane((int)cand, i) > cand) ? 1 : 0;
      unsigned k = 0;
#pragma unroll
      for (int i = 0; i < 16; ++i) { const int ri = __builtin_amdgcn_readlane(rk, i); const unsigned ci = (unsigned)__builtin_amdgcn_readlane((int)cand, i); k = (ri == (l & 7)) ? ci : k; }
      idx = (0xFFFF - (int)(k & 0xFFFFu)) & 2047;
      const unsigned mono = k >> 16, rb = (mono & 0x8000u) ? (mono & 0x7FFFu) : (~mono & 0xFFFFu);
      sg = rbf(1.f / (1.f + __expf(-lo(rb))));
    };
    u4 xs[4];
    auto xload = [&]() {
      u4 sv;
      const u16* xp = x + (size_t)idx * D + 256 * ksp + 32 * kset + 8 * g4;
#pragma unroll
      for (int it = 0; it < 4; ++it) xs[it] = *(const u4*)(xp + 64 * it);
      _Pragma("unroll 1") for (unsigned it = 0; it < SPIN; ++it) { sv = LD128(WS_ST + idx * 16); TICK(); if (__ballot(sv[1] != tag || sv[3] != tag) == 0ull) break; }
      const float m = __uint_as_float(sv[0]), rs = __uint_as_float(sv[2]);
#pragma unroll
      for (int it = 0; it < 4; ++it)
#pragma unroll
        for (int e = 0; e < 4; ++e) xs[it][e] = pk2((lo(xs[it][e]) - m) * rs, (hi(xs[it][e]) - m) * rs);
    };
    while (nbar < NBAR - 2 && !have) pollc();
    const bool early = have;
    if (early) { rank(); xload(); }
    while (nbar < NBAR) TICK();
    STMP(4);
    *(LAS float*)(lds + L_M + (mw * 64 + l) * 4) = __uint_as_float(stv[0]);
    *(LAS float*)(lds + L_RS + (mw * 64 + l) * 4) = __uint_as_float(stv[2]);
    if (l < 48) *(LAS f2*)(lds + L_CS + mw * 384 + l * 8) = (f2){cs0, cs1};
    if (early && mw == 0 && l < 8) *(LAS int*)(lds + L_SEL + l * 4) = idx;
    lsig(myFL, early ? 2u : 1u);
    wload();
    if (!early) {
      _Pragma("unroll 1") for (unsigned it = 0; it < SPIN && !have; ++it) pollc();
      rank();
      if (mw == 0 && l < 8) *(LAS int*)(lds + L_SEL + l * 4) = idx;
      lsig(myFL, 2u);
      xload();
    }
    if (u < 224) {
      STMP(5);
      f4 acc[8];
#pragma unroll
      for (int it = 0; it < 4; ++it)
#pragma unroll
        for (int c = 0; c < 8; ++c) {
          u4 f;
#pragma unroll
          for (int e = 0; e < 4; ++e) {
            const unsigned ev = gw[it][2 * e][c >> 1], od = gw[it][2 * e + 1][c >> 1];
            f[e] = (c & 1) ? __builtin_amdgcn_perm(od, ev, 0x07060302u) : __builtin_amdgcn_perm(od, ev, 0x05040100u);
          }
          acc[c] = MFMA16(S8(xs[it]), S8(f), it ? acc[c] : (f4)(0.f));
        }
#pragma unroll
      for (int i = 0; i < 4; ++i) {
        u4 s;
#pragma unroll
        for (int c = 0; c < 4; ++c) {
          auto r = __builtin_amdgcn_permlane32_swap(__float_as_uint(acc[c][i]), __float_as_uint(acc[c + 4][i]), false, false);
          s[c] = __float_as_uint(__uint_as_float(r[0]) + dppf<0x128>(__uint_as_float(r[1])));
        }
        if (r15 < 8) ST128(s, WS_GP + (((chunk * 4 + ksp) * 2 + mw) * 8 + 4 * (g4 & 1) + i) * 256 + r15 * 32 + (g4 >> 1) * 16);
      }
      WAITV(0);
      if (l == 0) ST64(((u2){tag, tag2}), WS_GF + chunk * 128 + (ksp * 2 + mw) * 8);
      STMP(6);
      _Pragma("unroll 1") for (unsigned it = 0; it < SPIN; ++it) {
        u2 f = LD64(WS_GF + chunk * 128 + (l & 7) * 8);
        if (__ballot(f[0] != tag || f[1] != tag2) == 0ull) break;
        SLEEP(1);
      }
      STMP(7);
      u4 ha[2];
      {
        u4 pg[4][4];
#pragma unroll
        for (int ks = 0; ks < 4; ++ks)
#pragma unroll
          for (int t = 0; t < 4; ++t)
            pg[ks][t] = LD128(WS_GP + (((chunk * 4 + ks) * 2 + (t >> 1)) * 8 + np) * 256 + (32 * kset + 8 * g4) * 4 + (t & 1) * 16);
        f4 gq[4];
#pragma unroll
        for (int t = 0; t < 4; ++t)
          gq[t] = (__builtin_bit_cast(f4, pg[0][t]) + __builtin_bit_cast(f4, pg[1][t])) + (__builtin_bit_cast(f4, pg[2][t]) + __builtin_bit_cast(f4, pg[3][t]));
        float hv[8];
#pragma unroll
        for (int e = 0; e < 8; ++e) {
          const float gg = rbf(gq[e >> 2][e & 3]), uu = rbf(gq[2 + (e >> 2)][e & 3]);
          hv[e] = rbf(gg / (1.f + __expf(-gg))) * uu;
        }
#pragma unroll
        for (int e = 0; e < 4; ++e) { ha[0][e] = pk2(hv[2 * e], hv[2 * e + 1]); ha[1][e] = (unsigned)__builtin_amdgcn_update_dpp(0, (int)ha[0][e], 0x128, 0xf, 0xf, false); }
      }
#pragma unroll
      for (int it = 0; it < 2; ++it)
#pragma unroll
        for (int c = 0; c < 8; ++c) {
          u4 f;
#pragma unroll
          for (int e = 0; e < 4; ++e) {
            const unsigned ev = dw[it][2 * e][c >> 1], od = dw[it][2 * e + 1][c >> 1];
            f[e] = (c & 1) ? __builtin_amdgcn_perm(od, ev, 0x07060302u) : __builtin_amdgcn_perm(od, ev, 0x05040100u);
          }
          acc[c] = MFMA16(S8(ha[it]), S8(f), it ? acc[c] : (f4)(0.f));
        }
      if (g4 < 2) {
#pragma unroll
        for (int i = 0; i < 4; ++i) {
          const unsigned yo = WS_YP + ((chunk * 8 + 4 * g4 + i) * D + 256 * ksp + 128 * mw + 8 * r15) * 4;
          ST128(__builtin_bit_cast(u4, ((f4){acc[0][i], acc[1][i], acc[2][i], acc[3][i]})), yo);
          ST128(__builtin_bit_cast(u4, ((f4){acc[4][i], acc[5][i], acc[6][i], acc[7][i]})), yo + 16);
        }
      }
      WAITV(0);
      if (l == 0) ST64(((u2){tag, tag2}), WS_YF + (ksp * 2 + mw) * 512 + chunk * 8);
      STMP(8);
      asm volatile(".Lend_h:");
    }
    unsigned mine = (unsigned)__ballot(!NOMOE && l < 8 && (idx >> 7) == mb && (l & 1) == mw);
    if (mine) {
      _Pragma("unroll 1") for (unsigned it = 0; it < SPIN; ++it) {
        u2 f = LD64(WS_YF + (nb >> 1) * 512 + (l < 56 ? l : 0) * 8);
        if (__ballot(f[0] != tag || f[1] != tag2) == 0ull) break;
        SLEEP(1);
      }
      lwait<1>(adFL, 0x0Fu, 2);
      while (mine) {
        const int t = __builtin_ctz(mine); mine &= mine - 1;
        u4 yv[14];
#pragma unroll
        for (int i = 0; i < 14; ++i) yv[i] = LD128(WS_YP + (((i * 4 + g4) * 8 + t) * D + nb * 64 + r15 * 4) * 4);
        f4 y = __builtin_bit_cast(f4, yv[0]);
#pragma unroll
        for (int i = 1; i < 14; ++i) y += __builtin_bit_cast(f4, yv[i]);
        const float gt = __int_as_float(__builtin_amdgcn_readlane(__float_as_int(sg), t));
#pragma unroll
        for (int e = 0; e < 4; ++e) {
          auto r = __builtin_amdgcn_permlane32_swap(__float_as_uint(y[e]), __float_as_uint(y[e]), false, false);
          const float a = __uint_as_float(r[0]) + __uint_as_float(r[1]);
          auto r2 = __builtin_amdgcn_permlane16_swap(__float_as_uint(a), __float_as_uint(a), false, false);
          y[e] = rbf(rbf(__uint_as_float(r2[0]) + __uint_as_float(r2[1])) * gt);
        }
        if (l < 16) *(LAS f4*)(lds + L_KVX + t * 256 + l * 16) = y;
      }
    }
    lsig(myFL, 3u);
    STMP(9);
    STFLUSH();
    return;
  }

  const int wr = w >> 1, wc = w & 1;
  unsigned tag;
  {
    const int row = mb * 128 + hp * 8 + w;
    u4 xa = *(const u4*)(x + row * D + l * 8), xb = *(const u4*)(x + row * D + 512 + l * 8);
    u4 rw0 = *(const u4*)(wrt + l * 8), rw1 = *(const u4*)(wrt + 512 + l * 8);
    tag = __builtin_amdgcn_readfirstlane(ev0) + 1u;
    const u4 st0 = row_stats(xa, xb, rw0, rw1, row, tag, wsr, l);
    if (l == 0) ST128(st0, WS_ST + row * 16);
  }
  unsigned aad[2], bad[2];
#pragma unroll
  for (int s = 0; s < 2; ++s) aad[s] = ldsb + (wr * 64 + r15) * 128 + (((4 * s + g4) ^ ((r15 >> 1) & 7)) * 16);
#pragma unroll
  for (int nh = 0; nh < 2; ++nh) bad[nh] = ldsb + 16384 + (8 * g4 + q) * 128 + (((wc * 4 + 2 * nh + (p >> 1)) ^ (((q >> 1) << 2) | ((g4 & 1) << 1))) * 16) + (p & 1) * 8;
  f4 acc[4][6];
#pragma unroll
  for (int m = 0; m < 4; ++m)
#pragma unroll
    for (int n = 0; n < 6; ++n) acc[m][n] = (f4)(0.f);
#define MF(m, n, Fq) do { if (n < 2) acc[m][n] = MFMA16(CAT(Fq.b[2 * n], Fq.b[2 * n + 1]), Fq.a[m], acc[m][n]); \
     else acc[m][n] = MFMA16(Fq.a[m], CAT(Fq.b[2 * n], Fq.b[2 * n + 1]), acc[m][n]); SBAR(); } while (0)
#define RB(G, n, vb0, vb1, SS, hf) RDTR(G.b[2 * (n) + (hf)], (((n) & 1) ? vb1 : vb0), ((n) >> 1) * 8192 + (SS) * 4096 + (hf) * 512)
#define RA(G, m, va) RD128(G.a[m], va, (m) * 2048)
#define SUB(Fq, G, va, vb0, vb1, SS, PRE) do { \
    WAITL(13); SBAR(); \
    MF(0, 0, Fq); PRE; RA(G, 0, va); SBAR(); \
    WAITL(11); SBAR(); \
    MF(1, 0, Fq); RB(G, 0, vb0, vb1, SS, 0); SBAR(); MF(2, 0, Fq); RB(G, 0, vb0, vb1, SS, 1); SBAR(); MF(3, 0, Fq); RA(G, 1, va); SBAR(); \
    WAITL(12); SBAR(); \
    MF(0, 1, Fq); RA(G, 2, va); SBAR(); MF(1, 1, Fq); RA(G, 3, va); SBAR(); MF(2, 1, Fq); RB(G, 1, vb0, vb1, SS, 0); SBAR(); MF(3, 1, Fq); RB(G, 1, vb0, vb1, SS, 1); SBAR(); \
    WAITL(8); SBAR(); \
    MF(0, 2, Fq); RB(G, 2, vb0, vb1, SS, 0); SBAR(); MF(1, 2, Fq); RB(G, 2, vb0, vb1, SS, 1); SBAR(); MF(2, 2, Fq); RB(G, 3, vb0, vb1, SS, 0); SBAR(); MF(3, 2, Fq); RB(G, 3, vb0, vb1, SS, 1); SBAR(); \
    MF(0, 3, Fq); RB(G, 4, vb0, vb1, SS, 0); SBAR(); MF(1, 3, Fq); RB(G, 4, vb0, vb1, SS, 1); SBAR(); MF(2, 3, Fq); RB(G, 5, vb0, vb1, SS, 0); SBAR(); MF(3, 3, Fq); RB(G, 5, vb0, vb1, SS, 1); SBAR(); \
    MF(0, 4, Fq); MF(1, 4, Fq); MF(2, 4, Fq); MF(3, 4, Fq); MF(0, 5, Fq); MF(1, 5, Fq); MF(2, 5, Fq); MF(3, 5, Fq); } while (0)
#define KSTEP(MODE) do { \
    const int nbuf = buf == 2 ? 0 : buf + 1; \
    const unsigned bo = buf * STG, nbo = nbuf * STG; \
    { unsigned va = aad[1] + bo, vb0 = bad[0] + bo, vb1 = bad[1] + bo; SUB(f0, f1, va, vb0, vb1, 1, ); } \
    if (MODE < 2) { \
      unsigned va = aad[0] + nbo, vb0 = bad[0] + nbo, vb1 = bad[1] + nbo; \
      SUB(f1, f0, va, vb0, vb1, 0, if (MODE == 0) { WAITL(0); BAR(); SBAR(); }); \
    } else { WAITL(0); SBAR(); \
      MF(0, 2, f1); MF(1, 2, f1); MF(2, 2, f1); MF(3, 2, f1); MF(0, 3, f1); MF(1, 3, f1); MF(2, 3, f1); MF(3, 3, f1); \
      MF(0, 4, f1); MF(1, 4, f1); MF(2, 4, f1); MF(3, 4, f1); MF(0, 5, f1); MF(1, 5, f1); MF(2, 5, f1); MF(3, 5, f1); \
      MF(0, 0, f1); MF(1, 0, f1); MF(2, 0, f1); MF(3, 0, f1); MF(0, 1, f1); MF(1, 1, f1); MF(2, 1, f1); MF(3, 1, f1); } \
    buf = nbuf; } while (0)
  STMP(1);
  WAITL(0);
  BAR();
  STMP(2);
  WAITL(0);
  FQ f0, f1;
  { unsigned va = aad[0], vb0 = bad[0], vb1 = bad[1];
    RA(f0, 0, va); RB(f0, 0, vb0, vb1, 0, 0); RB(f0, 0, vb0, vb1, 0, 1); RA(f0, 1, va); RA(f0, 2, va); RA(f0, 3, va);
    RB(f0, 1, vb0, vb1, 0, 0); RB(f0, 1, vb0, vb1, 0, 1); RB(f0, 2, vb0, vb1, 0, 0); RB(f0, 2, vb0, vb1, 0, 1); RB(f0, 3, vb0, vb1, 0, 0); RB(f0, 3, vb0, vb1, 0, 1);
    RB(f0, 4, vb0, vb1, 0, 0); RB(f0, 4, vb0, vb1, 0, 1); RB(f0, 5, vb0, vb1, 0, 0); RB(f0, 5, vb0, vb1, 0, 1);
    WAITL(0); SBAR(); }
  int buf = 0;
#pragma unroll 1
  for (int kt = 0; kt < 14; ++kt) KSTEP(0);
  KSTEP(1);
  KSTEP(2);
  STMP(3);
  __builtin_amdgcn_s_setprio(2);
  lwait(adFL, 0xC0u, 1);
  const LAS float* Lm = (const LAS float*)(lds + L_M) + wr * 64;
  const LAS float* Lr = (const LAS float*)(lds + L_RS) + wr * 64;
  const LAS float* Lc = (const LAS float*)(lds + L_CS) + wc * 96;
  f4 kv[4], zt[2];
  {
    float cs[4];
#pragma unroll
    for (int n = 0; n < 4; ++n) cs[n] = Lc[32 + 16 * n + r15];
    const u4 ones = (u4)(0x3F803F80u);
#pragma unroll
    for (int t = 0; t < 2; ++t) {
      u4 kf[2], vf[2];
#pragma unroll
      for (int mm = 0; mm < 2; ++mm) {
        const int m = 2 * t + mm;
        const f4 mv = *(const LAS f4*)(Lm + m * 16 + 4 * g4), rv = *(const LAS f4*)(Lr + m * 16 + 4 * g4);
#pragma unroll
        for (int a = 0; a < 2; ++a) {
          const f4 uk = acc[m][2 + a] - mv * cs[a];
          const f4 uv = (acc[m][4 + a] - mv * cs[2 + a]) * rv;
          kf[a][2 * mm] = phi2((f2){uk[0], uk[1]}, (f2){rv[0], rv[1]}); kf[a][2 * mm + 1] = phi2((f2){uk[2], uk[3]}, (f2){rv[2], rv[3]});
          vf[a][2 * mm] = pk2(uv[0], uv[1]); vf[a][2 * mm + 1] = pk2(uv[2], uv[3]);
        }
      }
#pragma unroll
      for (int tt = 0; tt < 4; ++tt) kv[tt] = MFMA16(S8(kf[tt & 1]), S8(vf[tt >> 1]), t ? kv[tt] : (f4)(0.f));
#pragma unroll
      for (int a = 0; a < 2; ++a) zt[a] = MFMA16(S8(kf[a]), S8(ones), t ? zt[a] : (f4)(0.f));
    }
  }
  const int hd = hp * 2 + wc;
  {
    LAS f4* ks = (LAS f4*)(lds + L_KVX + w * 3072) + l;
    const LAS f4* kr = (const LAS f4*)(lds + L_KVX + (w ^ 2) * 3072) + l;
    ks[0] = wr ? kv[0] : kv[2]; ks[64] = wr ? kv[1] : kv[3]; ks[128] = wr ? zt[0] : zt[1];
    lsig(myFL, 1);
    const f4 m0 = wr ? kv[2] : kv[0], m1 = wr ? kv[3] : kv[1], mz = wr ? zt[1] : zt[0];
    lwait(adFL, 1u << (w ^ 2), 1);
    const f4 s0 = m0 + kr[0], s1 = m1 + kr[64], sz = mz + kr[128];
    u4 o; o[0] = min(pk2(s0[0], s0[1]), POIS - 1u); o[1] = pk2(s0[2], s0[3]); o[2] = pk2(s1[0], s1[1]); o[3] = pk2(s1[2], s1[3]);
    ST128(o, WS_KVP + (hd * 16 + mb) * 2048 + wr * 1024 + l * 16);
    u4 zo = __builtin_bit_cast(u4, sz); zo[0] = min(zo[0], POIS - 1u);
    if (r15 == 0) ST128(zo, WS_ZP + (hd * 16 + mb) * 128 + g4 * 32 + wr * 16);
  }
  STMP(4);
  {
    const f4 c0 = *(const LAS f4*)(Lc + 4 * g4), c1 = *(const LAS f4*)(Lc + 16 + 4 * g4);
#pragma unroll
    for (int m = 0; m < 4; ++m) {
      const float mv = Lm[m * 16 + r15], rv = Lr[m * 16 + r15];
      const f4 u0 = acc[m][0] - c0 * mv, u1 = acc[m][1] - c1 * mv;
      u4 pf;
      pf[0] = phi2((f2){u0[0], u0[1]}, (f2)(rv)); pf[1] = phi2((f2){u0[2], u0[3]}, (f2)(rv));
      pf[2] = phi2((f2){u1[0], u1[1]}, (f2)(rv)); pf[3] = phi2((f2){u1[2], u1[3]}, (f2)(rv));
      *(LAS u4*)(lds + L_PQ + wr * 8192 + wc * 4096 + m * 1024 + l * 16) = pf;
    }
  }
  lsig(myFL, 2);
  __builtin_amdgcn_s_setprio(0);
  STMP(5);
  u2 xv[2][4];
  const int trow = mb * 128 + w * 32 + r15;
#pragma unroll
  for (int rg = 0; rg < 2; ++rg)
#pragma unroll
    for (int t = 0; t < 4; ++t) xv[rg][t] = *(const u2*)(x + (size_t)(trow + 16 * rg) * D + nb * 64 + 16 * t + 4 * g4);
  lwait<1>(adFL, 0x40u, 2);
  int sel[2] = {-1, -1};
  {
    const u4 s0 = *(const LAS u4*)(lds + L_SEL), s1 = *(const LAS u4*)(lds + L_SEL + 16);
#pragma unroll
    for (int i = 0; i < 8; ++i) { const int v = (int)(i < 4 ? s0[i & 3] : s1[i & 3]); sel[0] = v == trow ? i : sel[0]; sel[1] = v == trow + 16 ? i : sel[1]; }
  }
  const bool anysel = __ballot(sel[0] >= 0 || sel[1] >= 0) != 0ull;
  f4 oc[2][4];
#pragma unroll
  for (int rg = 0; rg < 2; ++rg) {
    const float m = *(const LAS float*)(lds + L_M + (w * 32 + 16 * rg + r15) * 4), rs = *(const LAS float*)(lds + L_RS + (w * 32 + 16 * rg + r15) * 4);
#pragma unroll
    for (int t = 0; t < 4; ++t) {
      const f4 xf = (f4){lo(xv[rg][t][0]), hi(xv[rg][t][0]), lo(xv[rg][t][1]), hi(xv[rg][t][1])};
      oc[rg][t] = (xf - m) * rs + xf;
      asm volatile("" : "+v"(oc[rg][t]));
    }
  }
  unsigned wa[4];
#pragma unroll
  for (int t = 0; t < 4; ++t) wa[t] = ldsb + wo_off(0) + (4 * g4 + q) * 128 + (((2 * t + (p >> 1)) ^ (((2 * g4 + (q >> 1)) & 3) << 1)) * 16) + (p & 1) * 8;
  u4 a[4][8];
#pragma unroll
  for (int k = 0; k < 4; ++k)
#pragma unroll
    for (int f = 0; f < 8; f += 2) asm volatile("" : "=v"(a[k][f]), "=v"(a[k][f + 1]));
  const char* ob = uptr(ws + WS_OF + (size_t)(mb * 8 + w * 2) * 32768);
  const unsigned ov0 = l * 16, ov1 = l * 16 + 32768;
  const unsigned wv0 = wov, wv1 = wov + 15360, wv2 = wov + 30720, wv3 = wov + 46080;
  lwait<1>(adFL, 0x0Fu, 2);
  STMP(6);
  lwait<1>(adFL, 0x30u, 1);
  __builtin_amdgcn_s_setprio(2);
  STMP(7);
#pragma unroll
  for (int i = 0; i < 6; ++i) {
    const int pos = 9 + 4 * i + w;
    if (pos < 32) { const int po = pos < 22 ? 69632 + 4096 * (pos - 9) : 4096 * (pos - 22); WODMA(po, (pos + hrot) & 31); }
  }
  STMP(15);
  unsigned rdy = 0;
  const unsigned flo = (w * 16 + mb) * 256 + (((l & 31) + hrot) & 31) * 8;
  const char* fb = uptr(ws + WS_OFL);
  auto need = [&](unsigned mask) {
    if ((rdy & mask) != mask) {
      _Pragma("unroll 1") for (unsigned it = 0; it < SPIN; ++it) {
        unsigned f;
        asm volatile("global_load_dword %0, %1, %2 sc1\n s_waitcnt vmcnt(0)" : "=v"(f) : "v"(flo), "s"(fb) : "memory");
        rdy = (unsigned)__ballot(f == tag);
        if ((rdy & mask) == mask) break;
        SLEEP(1);
      }
    }
  };
#define OLD(c) do { const char* sb_ = ob + ((4 * (c) + hrot) & 31) * 1024; _Pragma("unroll") for (int f = 0; f < 8; ++f) \
    asm volatile("global_load_dwordx4 %0, %1, %2 offset:%3 sc1" : "+v"(a[(c) & 3][f]) : "v"((f & 1) ? ov1 : ov0), "s"(sb_), "i"((f >> 1) * 1024) : "memory"); } while (0)
  auto isbad = [&](u4(&v)[8]) {
#pragma unroll
    for (int f = 0; f < 8; f += 2) asm volatile("" : "+v"(v[f]), "+v"(v[f + 1]));
    unsigned m = umax3(v[0][0], v[1][0], v[2][0]); m = umax3(m, v[3][0], v[4][0]); m = umax3(m, v[5][0], v[6][0]); m = max(m, v[7][0]);
    return __ballot(m == POIS) != 0ull;
  };
#define OCHK(c) do { WAITV(((c) < 5 ? 3 : 7 - (c)) * 8); \
    if (isbad(a[(c) & 3])) { _Pragma("unroll 1") for (unsigned it = 0; it < SPIN; ++it) { OLD(c); WAITV(0); if (!isbad(a[(c) & 3])) break; } } } while (0)
#define OLN(c) do { need(15u << (4 * (c))); OLD(c); } while (0)
  need(15u);
  lsig(myFL, 3);
  OLD(0); STMP(14); OLN(1); OLN(2); OLN(3);
  STMP(10);
  s4 b[2][8];
#define ORD1(pos, t, h) do { constexpr int o_ = wo_off(pos) - wo_off((pos) < 9 ? 0 : (pos) < 22 ? 9 : 22); \
    if ((h) == 0 && ((pos) == 9 || (pos) == 22)) { wa[t] -= (pos) == 9 ? wo_off(0) - wo_off(9) : wo_off(9) - wo_off(22); asm volatile("" : "+v"(wa[t])); } \
    RDTR(b[(pos) & 1][2 * (t) + (h)], wa[t], o_ + 2048 * (h)); } while (0)
#define OMF(pos, rg, t) asm volatile("v_mfma_f32_16x16x32_bf16 %0, %1, %2, %0" : "+v"(oc[rg][t]) \
    : "v"(CAT(b[(pos) & 1][2 * (t)], b[(pos) & 1][2 * (t) + 1])), "v"(a[((pos) >> 2) & 3][2 * ((pos) & 3) + (rg)]))
#define OT(pos, t) do { WAITL((pos) + 1 < 32 ? 6 : 6 - 2 * (t)); SBAR(); \
    OMF(pos, 0, t); SBAR(); if ((pos) + 1 < 32) ORD1(((pos) + 1) & 31, t, 0); SBAR(); \
    OMF(pos, 1, t); SBAR(); if ((pos) + 1 < 32) ORD1(((pos) + 1) & 31, t, 1); SBAR(); } while (0)
#define OHEAD(pos) do { OT(pos, 0); OT(pos, 1); OT(pos, 2); OT(pos, 3); } while (0)
#define OGRP(c) do { OCHK(c); SBAR(); OHEAD(4 * (c)); OHEAD(4 * (c) + 1); OHEAD(4 * (c) + 2); OHEAD(4 * (c) + 3); if ((c) < 4) { OLN((c) + 4); } SBAR(); } while (0)
  ORD1(0, 0, 0); ORD1(0, 0, 1); ORD1(0, 1, 0); ORD1(0, 1, 1); ORD1(0, 2, 0); ORD1(0, 2, 1); ORD1(0, 3, 0); ORD1(0, 3, 1);
  OGRP(0); STMP(11); OGRP(1);
  lwait(adFL, 0x0Fu, 3);
  OGRP(2); OGRP(3); STMP(12); OGRP(4); STMP(13); OGRP(5); OGRP(6); OGRP(7);
  asm volatile("s_waitcnt vmcnt(0)\n s_nop 7\n s_nop 7" : "+v"(oc[0][0]), "+v"(oc[0][1]), "+v"(oc[0][2]), "+v"(oc[0][3]), "+v"(oc[1][0]), "+v"(oc[1][1]), "+v"(oc[1][2]), "+v"(oc[1][3])::"memory");
  STMP(8);
  if (anysel) {
    lwait(adFL, 0xC0u, 3);
#pragma unroll
    for (int rg = 0; rg < 2; ++rg)
      if (sel[rg] >= 0) {
#pragma unroll
        for (int t = 0; t < 4; ++t) oc[rg][t] += *(const LAS f4*)(lds + L_KVX + sel[rg] * 256 + (16 * t + 4 * g4) * 4);
      }
  }
  const __amdgpu_buffer_rsrc_t outr = __builtin_amdgcn_make_buffer_rsrc(out, 0, 2048 * D * 2, 0x00020000);
#pragma unroll
  for (int rg = 0; rg < 2; ++rg)
#pragma unroll
    for (int hh = 0; hh < 2; ++hh) {
      const f4 ta = oc[rg][2 * hh], tb = oc[rg][2 * hh + 1];
      auto r0 = __builtin_amdgcn_permlane16_swap(pk2(ta[0], ta[1]), pk2(tb[0], tb[1]), false, false);
      auto r1 = __builtin_amdgcn_permlane16_swap(pk2(ta[2], ta[3]), pk2(tb[2], tb[3]), false, false);
      const int col = 32 * hh + 16 * (g4 & 1) + 8 * (g4 >> 1);
      __builtin_amdgcn_raw_buffer_store_b128(((u4){r0[0], r1[0], r0[1], r1[1]}), outr, ((trow + 16 * rg) * D + nb * 64 + col) * 2, 0, 16);
    }
  if (g == 0 && w == 0 && l == 0) __builtin_amdgcn_raw_buffer_store_b32(tag, wsr, WS_E, 0, 16);
  STMP(9);
  STFLUSH();
  asm volatile(".Lend_c:");
}

extern "C" void kernel_launch(void* const* d_in, const int* in_sizes, int n_in, void* d_out, int out_size,
                              void* d_ws, size_t ws_size, hipStream_t stream) {
  (void)hipFuncSetAttribute((const void*)fused, hipFuncAttributeMaxDynamicSharedMemorySize, LDS_SZ);
  fused<<<256, 512, LDS_SZ, stream>>>((const u16*)d_in[0], (const u16*)d_in[1], (const u16*)d_in[2], (const u16*)d_in[4],
                                      (const u16*)d_in[5], (const u16*)d_in[6], (const u16*)d_in[7], (char*)d_ws, (u16*)d_out);
}
```

```cpp
#include <hip/hip_runtime.h>
#include <stdint.h>

typedef unsigned short u16;
typedef short s4 __attribute__((ext_vector_type(4)));
typedef short s8 __attribute__((ext_vector_type(8)));
typedef float f4 __attribute__((ext_vector_type(4)));
typedef float f16v __attribute__((ext_vector_type(16)));
typedef float f2 __attribute__((ext_vector_type(2)));
typedef unsigned u4 __attribute__((ext_vector_type(4)));
typedef unsigned u2 __attribute__((ext_vector_type(2)));
typedef __bf16 bf2 __attribute__((ext_vector_type(2)));
#define LAS __attribute__((address_space(3)))
#define DEV __device__ __forceinline__

constexpr int D = 1024, F = 3584;
#define NOMOE 0
#define KD 235
#define OD 190
#define HPRIO 0
#define OWN 1
#define NOPOIS 0
#define CAP 7
#define RD 7
#define DYN 1
#define JX 0
#define NOSKIP 0
#define GRP 8
#define ROT 0

constexpr unsigned WS_E = 0;
constexpr unsigned WS_YF = 8192;
constexpr unsigned WS_CAND = 12288;
constexpr unsigned WS_GF = 32768;
constexpr unsigned WS_RG = 49152;
constexpr unsigned WS_ST = 65536;
constexpr unsigned WS_HK = 98304;
constexpr unsigned WS_ZP = 131072;
constexpr unsigned WS_CS = 196608;
constexpr unsigned WS_OH = 458752;
constexpr unsigned WS_KVP = 1u << 20;
constexpr unsigned WS_GP = 2u << 20;
constexpr unsigned WS_YP = 3u << 20;
constexpr unsigned WS_OF = 5u << 20;
constexpr unsigned WS_SZ = 9u << 20;
#define NR 4
#define RGO(tag) ((64u << 20) + ((tag) & (NR - 1u)) * (10u << 20))
#define MKWS(tag) char* const wsx = ws + RGO(tag); const __amdgpu_buffer_rsrc_t wsr = __builtin_amdgcn_make_buffer_rsrc(wsx, 0, WS_SZ, 0x00020000)

constexpr int STG = 40960;
constexpr int L_KVX = 0;
constexpr int L_PQ = 12288;
constexpr int L_M = 28672, L_RS = L_M + 512, L_CS = L_RS + 512, L_SEL = L_CS + 768, L_FL = L_SEL + 64;
constexpr int L_WO = 32768;
constexpr int LDS_SZ = 163840;
constexpr unsigned POIS = 0xFFFFFFFFu;

DEV unsigned pk2(float a, float b) { bf2 v; v[0] = (__bf16)a; v[1] = (__bf16)b; return __builtin_bit_cast(unsigned, v); }
DEV float lo(unsigned u) { return __uint_as_float(u << 16); }
DEV float hi(unsigned u) { return __uint_as_float(u & 0xFFFF0000u); }
DEV float rbf(float a) { return lo(pk2(a, a)); }
DEV float dot2(unsigned a, unsigned b, float c) { return __builtin_amdgcn_fdot2_f32_bf16(__builtin_bit_cast(bf2, a), __builtin_bit_cast(bf2, b), c, false); }
#define RD128(dst, addr, OFF) asm volatile("ds_read_b128 %0, %1 offset:%2" : "=v"(dst) : "v"(addr), "i"(OFF) : "memory")
#define RDTR(dst, addr, OFF) asm volatile("ds_read_b64_tr_b16 %0, %1 offset:%2" : "=v"(dst) : "v"(addr), "i"(OFF) : "memory")
#define SBAR() __builtin_amdgcn_sched_barrier(0)
#define CAT(x, y) __builtin_shufflevector(x, y, 0, 1, 2, 3, 4, 5, 6, 7)
#define WAITV(n) asm volatile("s_waitcnt vmcnt(%0)" ::"i"(n) : "memory")
#define WAITL(n) asm volatile("s_waitcnt lgkmcnt(%0)" ::"i"(n) : "memory")
#define MEMB() asm volatile("" ::: "memory")
#define SLEEP(n) asm volatile("s_sleep %0" ::"i"(n) : "memory")
#define BAR() asm volatile("s_barrier" ::: "memory")
#define GL0(vo, sb, m0v) asm volatile("s_mov_b32 m0, %2\n s_nop 0\n global_load_lds_dwordx4 %0, %1" ::"v"(vo), "s"(sb), "s"(m0v) : "memory")
#define GLI(vo, sb, OFF) asm volatile("global_load_lds_dwordx4 %0, %1 offset:%2" ::"v"(vo), "s"(sb), "i"(OFF) : "memory")
#define MFMA16(a, b, c) __builtin_amdgcn_mfma_f32_16x16x32_bf16(a, b, c, 0, 0, 0)
#define S8(x) __builtin_bit_cast(s8, x)
#define LD128(off) __builtin_amdgcn_raw_buffer_load_b128(wsr, (off), 0, 16)
#define LD64(off) __builtin_amdgcn_raw_buffer_load_b64(wsr, (off), 0, 16)
#define ST128(v, off) __builtin_amdgcn_raw_buffer_store_b128((v), wsr, (off), 0, 16)
#define ST64(v, off) __builtin_amdgcn_raw_buffer_store_b64((v), wsr, (off), 0, 16)
#define OFA(i) (WS_OF + ((mb * 32 + hd) * 8 + (i)) * 1024 + l * 16)
#define NOW() ((unsigned)__builtin_amdgcn_s_memrealtime())
constexpr unsigned SPIN = 1u << 21;
DEV const char* uptr(const char* p) {
  unsigned long long v = (unsigned long long)p;
  unsigned lo_ = __builtin_amdgcn_readfirstlane((unsigned)v), hi_ = __builtin_amdgcn_readfirstlane((unsigned)(v >> 32));
  return (const char*)(((unsigned long long)hi_ << 32) | lo_);
}
DEV void lsig(unsigned addr, unsigned val) { asm volatile("s_waitcnt lgkmcnt(0)\n ds_write_b32 %0, %1" ::"v"(addr), "v"(val) : "memory"); }
template <int SL = 0> DEV void lwait(unsigned addr, unsigned mask, unsigned target) {
  _Pragma("unroll 1") for (unsigned it = 0; it < SPIN; ++it) {
    unsigned v;
    asm volatile("ds_read_b32 %0, %1\n s_waitcnt lgkmcnt(0)" : "=v"(v) : "v"(addr) : "memory");
    if (((unsigned)__ballot(v < target) & mask) == 0u) break;
    if (SL) SLEEP(SL);
  }
}
DEV unsigned umax3(unsigned a, unsigned b, unsigned c) { return max(max(a, b), c); }
template <int C> DEV float dppf(float v) { return __int_as_float(__builtin_amdgcn_update_dpp(0, __float_as_int(v), C, 0xf, 0xf, false)); }
DEV unsigned wave_max(unsigned v) {
  v = max(v, (unsigned)__builtin_amdgcn_update_dpp(0, (int)v, 0x111, 0xf, 0xf, false));
  v = max(v, (unsigned)__builtin_amdgcn_update_dpp(0, (int)v, 0x112, 0xf, 0xf, false));
  v = max(v, (unsigned)__builtin_amdgcn_update_dpp(0, (int)v, 0x114, 0xf, 0xf, false));
  v = max(v, (unsigned)__builtin_amdgcn_update_dpp(0, (int)v, 0x118, 0xf, 0xf, false));
  v = max(v, (unsigned)__builtin_amdgcn_update_dpp(0, (int)v, 0x142, 0xa, 0xf, false));
  v = max(v, (unsigned)__builtin_amdgcn_update_dpp(0, (int)v, 0x143, 0xc, 0xf, false));
  return (unsigned)__builtin_amdgcn_readlane((int)v, 63);
}
DEV unsigned phi2(f2 u, f2 rs) {
  f2 t = u * rs;
  f2 a = t * 1.44269504f, p = t + 1.f, r;
  r[0] = __builtin_amdgcn_fmed3f(__builtin_amdgcn_exp2f(a[0]), p[0], 1.f);
  r[1] = __builtin_amdgcn_fmed3f(__builtin_amdgcn_exp2f(a[1]), p[1], 1.f);
  r += 1e-6f;
  return pk2(r[0], r[1]);
}

#define STDECL
#define STMP(i)
#define BST(i)
#define STFLUSH()
#define RETRY()

DEV float half_sum(float v) {
  asm volatile("s_nop 1\n v_add_f32_dpp %0, %0, %0 row_ror:8 row_mask:0xf bank_mask:0xf\n s_nop 1\n v_add_f32_dpp %0, %0, %0 row_ror:4 row_mask:0xf bank_mask:0xf\n"
               "s_nop 1\n v_add_f32_dpp %0, %0, %0 row_ror:2 row_mask:0xf bank_mask:0xf\n s_nop 1\n v_add_f32_dpp %0, %0, %0 row_ror:1 row_mask:0xf bank_mask:0xf\n s_nop 1" : "+v"(v));
  auto r = __builtin_amdgcn_permlane16_swap(__float_as_uint(v), __float_as_uint(v), false, false);
  return __uint_as_float(r[0]) + __uint_as_float(r[1]);
}

constexpr int PP = 8 - RD < 1 ? 1 : 8 - RD;
#define ISPP(pos) ((pos) < 24 && ((pos) & 7) == PP)
constexpr int oyoung(int pos) {
  int n = 0;
  for (int i = pos + 1; i < pos + RD - 1 + (pos == 0) && i < 32; ++i) n += 2 + (ISPP(i) ? 1 : 0);
  return n;
}
struct FQ { s8 a[4]; s4 b[12]; };
template <bool B> struct BC { static constexpr bool v = B; };

__global__ __launch_bounds__(512) void fused(const u16* __restrict__ x, const u16* __restrict__ wqkv, const u16* __restrict__ wo,
                                             const u16* __restrict__ wrt, const u16* __restrict__ wg, const u16* __restrict__ wu,
                                             const u16* __restrict__ wd, char* __restrict__ ws, u16* __restrict__ out) {
  extern __shared__ __attribute__((aligned(16))) char lds[];
  asm volatile(".Lcode_beg:");
  const int w = __builtin_amdgcn_readfirstlane(threadIdx.x >> 6), l = threadIdx.x & 63;
  STDECL;
  STMP(0);
  const int g = blockIdx.x, xcd = g & 7, j = g >> 3, ii = j >> 2;
  unsigned cw = 0;
  if (w == 7) {
    unsigned long long pc; unsigned off, len, l1, l2, l3;
    asm volatile("s_getpc_b64 %0\n.Lcode_here:" : "=s"(pc));
    asm volatile("s_mov_b32 %0, (.Lcode_here - .Lcode_beg)" : "=s"(off));
    asm volatile("s_mov_b32 %0, (.Lend_c - .Lcode_beg)" : "=s"(len));
    asm volatile("s_mov_b32 %0, (.Lend_l - .Lcode_beg)" : "=s"(l1));
    asm volatile("s_mov_b32 %0, (.Lend_h - .Lcode_beg)" : "=s"(l2));
    asm volatile("s_mov_b32 %0, (.Lend_s - .Lcode_beg)" : "=s"(l3));
    len = max(max(len, l3), max(l1, l2));
    const unsigned o = min((unsigned)(l * 32 + j) * 64u, len - 4u);
    asm volatile("global_load_dword %0, %1, %2" : "+v"(cw) : "v"(o), "s"(pc - off) : "memory");
  }
  const int mb = (xcd & 1) * 8 + ii, hp = (xcd >> 1) * 4 + ((j + JX) & 3);
#define JR 0
  const int rot = 2 * xcd + 4 * ((ii + (JR == 1 ? j : JR == 2 ? (j & 2) : 0)) & 3);
  const int nb = hp;
  const int hr = ROT * (GRP / 4) * (j & 3);
#define HEADAT(p) (((p) & ~(GRP - 1)) | (((p) + hr) & (GRP - 1)))
  const int ph = ii >= 6 ? ii - 6 : ii >= 3 ? ii - 3 : ii;
  const unsigned ldsb = (unsigned)(size_t)(LAS char*)lds;
  const unsigned adFL = ldsb + L_FL + 4 * (l & 7), myFL = ldsb + L_FL + 4 * w;
  const __amdgpu_buffer_rsrc_t wse = __builtin_amdgcn_make_buffer_rsrc(ws, 0, 64, 0x00020000);
  const int r15 = l & 15, g4 = l >> 4, q = (l & 15) >> 2, p = l & 3;
  const char* wob = uptr((const char*)wo + nb * 128);

  if (w != 4 && w != 5) {
  const unsigned ev0 = __builtin_amdgcn_raw_buffer_load_b32(wse, WS_E, 0, 16);
  if (w >= 6) {
    const int mw = w - 6;
    const unsigned tag = __builtin_amdgcn_readfirstlane(ev0) + 1u, tag2 = tag * 0x9E3779B1u;
    MKWS(tag);
    auto helper = [&](auto SP) {
    constexpr bool spare = decltype(SP)::v, moe = !spare && !NOMOE;
    int nbar = 0;
#define TICK() do { BST(nbar); SBAR(); BAR(); SBAR(); ++nbar; } while (0)
#define DTICK(lim) do { if (nbar < (lim)) TICK(); else SLEEP(2); } while (0)
    const int hd = hp * 2 + mw;
    const u4 pz = (u4)(POIS);
    const int chunk = g >> 2, ksp = g & 3;
    const int np = l & 7, kset = (l >> 3) & 1;
    u4 gw[4][8], dw[2][8];
    const __amdgpu_buffer_rsrc_t wgr = __builtin_amdgcn_make_buffer_rsrc((void*)(mw ? wu : wg), 0, D * F * 2, 0x00020000);
    const __amdgpu_buffer_rsrc_t wdr = __builtin_amdgcn_make_buffer_rsrc((void*)wd, 0, D * F * 2, 0x00020000);
    const __amdgpu_buffer_rsrc_t wor = __builtin_amdgcn_make_buffer_rsrc((void*)wo, 0, D * D * 2, 0x00020000);
    const unsigned gvo = ((256 * ksp + 32 * kset + 8 * g4) * F + 64 * chunk + 8 * np) * 2;
    const unsigned dvo = ((64 * chunk + 8 * g4) * D + 256 * ksp + 128 * mw + 8 * r15) * 2;
    auto burst = [&](int k) {
      if (moe) {
#pragma unroll
        for (int i = 0; i < 16; ++i) {
          if (k < 2) gw[(2 * k + (i >> 3)) & 3][i & 7] = __builtin_amdgcn_raw_buffer_load_b128(wgr, gvo, (128 * k + 64 * (i >> 3) + (i & 7)) * F * 2, 2);
          else dw[i >> 3][i & 7] = __builtin_amdgcn_raw_buffer_load_b128(wdr, dvo, (32 * (i >> 3) + (i & 7)) * D * 2, 2);
        }
      }
    };
    auto transp = [&](u4(&v)[8]) {
      if (moe) {
        u4 f[8];
#pragma unroll
        for (int c = 0; c < 8; ++c)
#pragma unroll
          for (int e = 0; e < 4; ++e) {
            const unsigned ev = v[2 * e][c >> 1], od = v[2 * e + 1][c >> 1];
            f[c][e] = (c & 1) ? __builtin_amdgcn_perm(od, ev, 0x07060302u) : __builtin_amdgcn_perm(od, ev, 0x05040100u);
          }
#pragma unroll
        for (int c = 0; c < 8; ++c) { v[c] = f[c]; asm volatile("" : "+v"(v[c])); }
      }
    };
    auto pois = [&](int i) {
      if (i < 2) ST128(pz, WS_KVP + (hd * 16 + mb) * 2048 + i * 1024 + l * 16);
      else if (i == 2) ST128(pz, WS_ZP + (hd * 16 + mb) * 128 + (l & 7) * 16);
      else if (!NOPOIS) ST128(pz, OFA(i - 3));
    };
    unsigned wt0, wt1;
    auto touch = [&]() {
      const unsigned to = (ii * 128 + mw * 64 + (l >> 1)) * 2048 + nb * 128 + (l & 1) * 64;
      wt0 = __builtin_amdgcn_raw_buffer_load_b32(wor, to, 0, 0); wt1 = __builtin_amdgcn_raw_buffer_load_b32(wor, to, 65536, 0);
    };
    auto colsum = [&](int h) {
      const unsigned bo = ldsb + (1 + h) * STG + 16384 + (8 * g4 + q) * 128 + (p & 1) * 8;
      const int sw = ((q >> 1) << 2) | ((g4 & 1) << 1);
      const unsigned b0 = bo + (((mw * 4 + (p >> 1)) ^ sw) * 16), b1 = bo + (((mw * 4 + 2 + (p >> 1)) ^ sw) * 16);
      const u4 ones = (u4)(0x3F803F80u);
      f4 c[6]; s4 b[24];
#pragma unroll
      for (int n = 0; n < 12; ++n) { RDTR(b[2 * n], (n & 1) ? b1 : b0, ((n % 6) >> 1) * 8192 + (n / 6) * 4096); RDTR(b[2 * n + 1], (n & 1) ? b1 : b0, ((n % 6) >> 1) * 8192 + (n / 6) * 4096 + 512); }
      WAITL(0);
      TICK();
#pragma unroll
      for (int n = 0; n < 12; ++n) c[n % 6] = MFMA16(S8(ones), CAT(b[2 * n], b[2 * n + 1]), n >= 6 ? c[n % 6] : (f4)(0.f));
      const u4 csg = (u4){__float_as_uint(g4 ? c[3][0] : c[0][0]), __float_as_uint(g4 ? c[4][0] : c[1][0]), __float_as_uint(g4 ? c[5][0] : c[2][0]), tag};
      ST128(csg, l < 32 ? WS_CS + (hd * 16 + mb) * 512 + l * 16 : WS_SZ);
    };
    u4 stv, cv[8], cd; float cs[3];
    auto gload = [&]() {
      stv = LD128(WS_ST + (mb * 128 + mw * 64 + l) * 16);
      cd = LD128(WS_CAND + (g & 31) * 128 + (l & 7) * 16);
#pragma unroll
      for (int i = 0; i < 8; ++i) cv[i] = LD128(WS_CS + hd * 8192 + i * 1024 + l * 16);
    };
    auto gbad = [&]() {
      unsigned bad = (stv[1] ^ tag) | (stv[3] ^ tag) | (cd[1] ^ tag) | (cd[3] ^ tag);
#pragma unroll
      for (int i = 0; i < 8; ++i) bad |= cv[i][3] ^ tag;
      return __ballot(bad != 0u) != 0ull;
    };
    int idx;
    auto gfin = [&]() {
      if (gbad()) { RETRY(); _Pragma("unroll 1") for (unsigned it = 0; it < SPIN; ++it) { SLEEP(2); gload(); if (!gbad()) break; } }
#pragma unroll
      for (int e = 0; e < 3; ++e) {
        float a = __uint_as_float(cv[0][e]);
#pragma unroll
        for (int i = 1; i < 8; ++i) a += __uint_as_float(cv[i][e]);
        auto r = __builtin_amdgcn_permlane32_swap(__float_as_uint(a), __float_as_uint(a), false, false);
        cs[e] = __uint_as_float(r[0]) + __uint_as_float(r[1]);
      }
      asm volatile("" : "+v"(cs[0]), "+v"(cs[1]), "+v"(cs[2]) : "v"(wt0), "v"(wt1));
      cd[0] = max(cd[0], cd[2]);
      idx = (int)(cd[0] & 2047u);
    };
    auto lpub = [&]() {
      *(LAS float*)(lds + L_M + (mw * 64 + l) * 4) = __uint_as_float(stv[0]);
      *(LAS float*)(lds + L_RS + (mw * 64 + l) * 4) = __uint_as_float(stv[2]);
      if (l < 32) {
#pragma unroll
        for (int e = 0; e < 3; ++e) *(LAS float*)(lds + L_CS + mw * 384 + ((3 * g4 + e) * 16 + r15) * 4) = cs[e];
      }
      if (mw == 0 && l < 8) { *(LAS int*)(lds + L_SEL + l * 4) = idx; *(LAS unsigned*)(lds + L_FL + 4 * l) = 0u; }
      WAITL(0);
    };
    u4 xs[4];
    asm volatile("" ::"s"(wg), "s"(wu), "s"(wd), "v"(cw));
    if (HPRIO) __builtin_amdgcn_s_setprio(HPRIO);
    TICK();
    if (spare) {
#pragma unroll
      for (int i = 0; i < 6; ++i) pois(i);
      TICK();
#pragma unroll
      for (int i = 6; i < 11; ++i) pois(i);
      TICK();
      u4 kk[8];
      auto kload = [&]() {
#pragma unroll
        for (int i = 0; i < 8; ++i) kk[i] = LD128(WS_RG + mw * 8192 + i * 1024 + l * 16);
      };
      kload();
      colsum(1);
      _Pragma("unroll 1") for (unsigned it = 0; it < SPIN; ++it) {
        while (nbar < 10 && (__builtin_amdgcn_s_getreg(7 | (31 << 11)) & 0xC000000Eu)) TICK();
        unsigned bad = 0;
#pragma unroll
        for (int i = 0; i < 8; ++i) bad |= (kk[i][1] ^ tag) | (kk[i][3] ^ tag);
        if (__ballot(bad != 0u) == 0ull) break;
        kload();
        DTICK(10);
      }
      __builtin_amdgcn_s_setprio(3);
      unsigned base = 0, mine8 = 0;
#pragma unroll 1
      for (int it = 0; it < 8; ++it) {
        unsigned m = 0;
#pragma unroll
        for (int i = 0; i < 8; ++i) m = umax3(m, kk[i][0], kk[i][2]);
        m = wave_max(m);
#pragma unroll
        for (int i = 0; i < 8; ++i) { kk[i][0] -= m; kk[i][2] -= m; }
        base += m;
        mine8 = l == it ? base : mine8;
        if ((it == 1 || it == 4) && nbar < 10) TICK();
      }
      if (l < 8) ST64(((u2){mine8, tag}), WS_CAND + (g - 224) * 128 + (mw ? 7 - l : l) * 16 + mw * 8);
      __builtin_amdgcn_s_setprio(HPRIO);
      STMP(2);
      touch();
      while (nbar < 10) TICK();
      gload();
      TICK(); TICK();
      WAITV(0);
      gfin();
      STMP(3);
      TICK(); TICK();
      lpub();
    } else {
      TICK();
      if (ii >> 2) TICK();
      colsum(ii >> 2);
      if (!(ii >> 2)) TICK();
      STMP(1);
      pois(0); pois(1); pois(2);
      TICK();
      pois(3); pois(4);
      TICK();
      for (int i = 0; i < ph; ++i) TICK();
      burst(0); touch();
      TICK();
      pois(5); pois(6);
      TICK();
      pois(7); pois(8);
      transp(gw[0]);
      TICK();
      pois(9); pois(10);
      gload();
      transp(gw[1]);
      TICK();
      burst(1);
      TICK();
      WAITV(moe ? 16 : 0);
      gfin();
      u4 sv;
      if (moe) {
        const u16* xp = x + (size_t)idx * D + 256 * ksp + 32 * kset + 8 * g4;
#pragma unroll
        for (int it = 0; it < 4; ++it) xs[it] = *(const u4*)(xp + 64 * it);
        sv = LD128(WS_ST + idx * 16);
      }
      STMP(3);
      TICK();
      transp(gw[2]);
      TICK();
      transp(gw[3]);
      TICK();
      lpub();
      if (moe) {
        if (__ballot(sv[1] != tag || sv[3] != tag) != 0ull) {
          _Pragma("unroll 1") for (unsigned it = 0; it < SPIN; ++it) { SLEEP(1); sv = LD128(WS_ST + idx * 16); if (__ballot(sv[1] != tag || sv[3] != tag) == 0ull) break; }
        }
        asm volatile("" : "+v"(xs[0]), "+v"(xs[1]), "+v"(xs[2]), "+v"(xs[3]));
      }
      burst(2);
      TICK();
      if (moe) {
        const float m = __uint_as_float(sv[0]), rs = __uint_as_float(sv[2]);
#pragma unroll
        for (int it = 0; it < 4; ++it) {
#pragma unroll
          for (int e = 0; e < 4; ++e) { const unsigned d = pk2(lo(xs[it][e]) - m, hi(xs[it][e]) - m); xs[it][e] = pk2(lo(d) * rs, hi(d) * rs); }
          asm volatile("" : "+v"(xs[it]));
        }
      }
    }
    while (nbar < 17) TICK();
    if (HPRIO) __builtin_amdgcn_s_setprio(0);
    STMP(4);
    float sg;
    {
      const unsigned mono = cd[0] >> 16, rb = (mono & 0x8000u) ? (mono & 0x7FFFu) : (~mono & 0xFFFFu);
      sg = rbf(1.f / (1.f + __expf(-lo(rb))));
    }
    if (moe) {
      STMP(5);
      f4 acc[8];
#pragma unroll
      for (int it = 0; it < 4; ++it)
#pragma unroll
        for (int c = 0; c < 8; ++c) acc[c] = MFMA16(S8(xs[it]), S8(gw[it][c]), it ? acc[c] : (f4)(0.f));
#pragma unroll
      for (int i = 0; i < 4; ++i) {
        u4 s;
#pragma unroll
        for (int c = 0; c < 4; ++c) {
          auto r = __builtin_amdgcn_permlane32_swap(__float_as_uint(acc[c][i]), __float_as_uint(acc[c + 4][i]), false, false);
          s[c] = __float_as_uint(__uint_as_float(r[0]) + dppf<0x128>(__uint_as_float(r[1])));
        }
        if (r15 < 8) ST128(s, WS_GP + (((chunk * 4 + ksp) * 2 + mw) * 8 + 4 * (g4 & 1) + i) * 256 + r15 * 32 + (g4 >> 1) * 16);
      }
      WAITV(0);
      if (l == 0) ST64(((u2){tag, tag2}), WS_GF + chunk * 128 + (ksp * 2 + mw) * 8);
      STMP(6);
      transp(dw[0]); transp(dw[1]);
      _Pragma("unroll 1") for (unsigned it = 0; it < SPIN; ++it) {
        u2 f = LD64(WS_GF + chunk * 128 + (l & 7) * 8);
        if (__ballot(f[0] != tag || f[1] != tag2) == 0ull) break;
        SLEEP(1);
      }
      STMP(7);
      u4 ha[2];
      {
        u4 pg[4][4];
#pragma unroll
        for (int ks = 0; ks < 4; ++ks)
#pragma unroll
          for (int t = 0; t < 4; ++t)
            pg[ks][t] = LD128(WS_GP + (((chunk * 4 + ks) * 2 + (t >> 1)) * 8 + np) * 256 + (32 * kset + 8 * g4) * 4 + (t & 1) * 16);
        f4 gq[4];
#pragma unroll
        for (int t = 0; t < 4; ++t)
          gq[t] = (__builtin_bit_cast(f4, pg[0][t]) + __builtin_bit_cast(f4, pg[1][t])) + (__builtin_bit_cast(f4, pg[2][t]) + __builtin_bit_cast(f4, pg[3][t]));
        float hv[8];
#pragma unroll
        for (int e = 0; e < 8; ++e) {
          const float gg = rbf(gq[e >> 2][e & 3]), uu = rbf(gq[2 + (e >> 2)][e & 3]);
          hv[e] = rbf(gg / (1.f + __expf(-gg))) * uu;
        }
#pragma unroll
        for (int e = 0; e < 4; ++e) { ha[0][e] = pk2(hv[2 * e], hv[2 * e + 1]); ha[1][e] = (unsigned)__builtin_amdgcn_update_dpp(0, (int)ha[0][e], 0x128, 0xf, 0xf, false); }
      }
#pragma unroll
      for (int it = 0; it < 2; ++it)
#pragma unroll
        for (int c = 0; c < 8; ++c) acc[c] = MFMA16(S8(ha[it]), S8(dw[it][c]), it ? acc[c] : (f4)(0.f));
      if (g4 < 2) {
#pragma unroll
        for (int i = 0; i < 4; ++i) {
          const unsigned yo = WS_YP + ((chunk * 8 + 4 * g4 + i) * D + 256 * ksp + 128 * mw + 8 * r15) * 4;
          ST128(__builtin_bit_cast(u4, ((f4){acc[0][i], acc[1][i], acc[2][i], acc[3][i]})), yo);
          ST128(__builtin_bit_cast(u4, ((f4){acc[4][i], acc[5][i], acc[6][i], acc[7][i]})), yo + 16);
        }
      }
      WAITV(0);
      if (l == 0) ST64(((u2){tag, tag2}), WS_YF + (ksp * 2 + mw) * 512 + chunk * 8);
      STMP(8);
    }
    unsigned mine = (unsigned)__ballot(!NOMOE && l < 8 && (idx >> 7) == mb && (l & 1) == mw);
    if (mine) {
      _Pragma("unroll 1") for (unsigned it = 0; it < SPIN; ++it) {
        u2 f = LD64(WS_YF + (nb >> 1) * 512 + (l < 56 ? l : 0) * 8);
        if (__ballot(f[0] != tag || f[1] != tag2) == 0ull) break;
        SLEEP(1);
      }
      lwait<1>(adFL, 0x0Fu, 2);
      while (mine) {
        const int t = __builtin_ctz(mine); mine &= mine - 1;
        u4 yv[14];
#pragma unroll
        for (int i = 0; i < 14; ++i) yv[i] = LD128(WS_YP + (((i * 4 + g4) * 8 + t) * D + nb * 64 + r15 * 4) * 4);
        f4 y = __builtin_bit_cast(f4, yv[0]);
#pragma unroll
        for (int i = 1; i < 14; ++i) y += __builtin_bit_cast(f4, yv[i]);
        const float gt = __int_as_float(__builtin_amdgcn_readlane(__float_as_int(sg), t));
#pragma unroll
        for (int e = 0; e < 4; ++e) {
          auto r = __builtin_amdgcn_permlane32_swap(__float_as_uint(y[e]), __float_as_uint(y[e]), false, false);
          const float a = __uint_as_float(r[0]) + __uint_as_float(r[1]);
          auto r2 = __builtin_amdgcn_permlane16_swap(__float_as_uint(a), __float_as_uint(a), false, false);
          y[e] = rbf(rbf(__uint_as_float(r2[0]) + __uint_as_float(r2[1])) * gt);
        }
        if (l < 16) *(LAS f4*)(lds + L_KVX + t * 256 + l * 16) = y;
      }
    }
    lsig(myFL, 3u);
    STMP(9);
    STFLUSH();
    if (spare) asm volatile(".Lend_s:"); else asm volatile(".Lend_h:");
    };
    if (g >= 224) helper(BC<true>()); else helper(BC<false>());
    return;
  }

  const int wr = w >> 1, wc = w & 1;
  unsigned tag; u2 kg; u4 sg4;
  const int srow = mb * 128 + hp * 8 + 2 * w + (l >> 5);
  {
    const int row = srow;
    u4 xa[4], rw[4];
#pragma unroll
    for (int c = 0; c < 4; ++c) xa[c] = *(const u4*)(x + row * D + (l & 31) * 8 + 256 * c);
#pragma unroll
    for (int c = 0; c < 4; ++c) rw[c] = *(const u4*)(wrt + (l & 31) * 8 + 256 * c);
    float s0 = 0, s1 = 0;
#pragma unroll
    for (int c = 0; c < 4; ++c)
#pragma unroll
      for (int e = 0; e < 4; e += 2) { unsigned x0 = xa[c][e], x1 = xa[c][e + 1]; s0 = dot2(x0, 0x3F803F80u, s0); s1 = dot2(x1, 0x3F803F80u, s1); }
    const float m = half_sum(s0 + s1) * (1.f / D);
    f2 v[16], q2 = (f2)(0.f), q3 = (f2)(0.f);
#pragma unroll
    for (int c = 0; c < 16; ++c) v[c] = (f2){lo(xa[c >> 2][c & 3]), hi(xa[c >> 2][c & 3])} - m;
#pragma unroll
    for (int c = 0; c < 16; c += 2) { q2 += v[c] * v[c]; q3 += v[c + 1] * v[c + 1]; }
    q2 += q3;
    const float mr = rbf(m);
    const float rs = 1.f / rbf(sqrtf(rbf(rbf(half_sum(q2[0] + q2[1]) * (1.f / D)) + 1e-5f)));
    float d0 = 0, d1 = 0;
#pragma unroll
    for (int c = 0; c < 16; c += 2) {
      const f2 e0 = (f2){lo(xa[c >> 2][c & 3]), hi(xa[c >> 2][c & 3])} - mr, e1 = (f2){lo(xa[c >> 2][(c & 3) + 1]), hi(xa[c >> 2][(c & 3) + 1])} - mr;
      const unsigned p0 = pk2(e0[0], e0[1]), p1 = pk2(e1[0], e1[1]);
      const f2 n0 = (f2){lo(p0), hi(p0)} * rs, n1 = (f2){lo(p1), hi(p1)} * rs;
      unsigned c0 = rw[c >> 2][c & 3], c1 = rw[c >> 2][(c & 3) + 1];
      d0 = dot2(pk2(n0[0], n0[1]), c0, d0); d1 = dot2(pk2(n1[0], n1[1]), c1, d1);
    }
    const float d = half_sum(d0 + d1);
    const unsigned rb = pk2(d, d) & 0xFFFFu;
    const unsigned mono = (rb & 0x8000u) ? (~rb & 0xFFFFu) : (rb | 0x8000u);
    tag = __builtin_amdgcn_readfirstlane(ev0) + 1u;
    kg = (u2){(mono << 16) | (unsigned)row, tag};
    sg4 = (u4){__float_as_uint(mr), tag, __float_as_uint(rs), tag};
  }
  MKWS(tag);
  if ((l & 31) == 0) { ST64(kg, WS_RG + srow * 8); ST128(sg4, WS_ST + srow * 16); }
  unsigned aad[2], bad[2];
#pragma unroll
  for (int s = 0; s < 2; ++s) aad[s] = ldsb + (wr * 64 + r15) * 128 + (((4 * s + g4) ^ ((r15 >> 1) & 7)) * 16);
#pragma unroll
  for (int nh = 0; nh < 2; ++nh) bad[nh] = ldsb + 16384 + (8 * g4 + q) * 128 + (((wc * 4 + 2 * nh + (p >> 1)) ^ (((q >> 1) << 2) | ((g4 & 1) << 1))) * 16) + (p & 1) * 8;
  f4 acc[4][6];
#pragma unroll
  for (int m = 0; m < 4; ++m)
#pragma unroll
    for (int n = 0; n < 6; ++n) { acc[m][n] = (f4)(0.f); asm volatile("" : "+v"(acc[m][n])); }
#define MF(m, n, Fq) do { if (n < 2) acc[m][n] = MFMA16(CAT(Fq.b[2 * n], Fq.b[2 * n + 1]), Fq.a[m], acc[m][n]); \
     else acc[m][n] = MFMA16(Fq.a[m], CAT(Fq.b[2 * n], Fq.b[2 * n + 1]), acc[m][n]); SBAR(); } while (0)
#define RB(G, n, vb0, vb1, SS, hf) RDTR(G.b[2 * (n) + (hf)], (((n) & 1) ? vb1 : vb0), ((n) >> 1) * 8192 + (SS) * 4096 + (hf) * 512)
#define RA(G, m, va) RD128(G.a[m], va, (m) * 2048)
#define SUB(Fq, G, va, vb0, vb1, SS, PRE) do { \
    WAITL(13); SBAR(); \
    MF(0, 0, Fq); PRE; RA(G, 0, va); SBAR(); \
    WAITL(11); SBAR(); \
    MF(1, 0, Fq); RB(G, 0, vb0, vb1, SS, 0); SBAR(); MF(2, 0, Fq); RB(G, 0, vb0, vb1, SS, 1); SBAR(); MF(3, 0, Fq); RA(G, 1, va); SBAR(); \
    WAITL(12); SBAR(); \
    MF(0, 1, Fq); RA(G, 2, va); SBAR(); MF(1, 1, Fq); RA(G, 3, va); SBAR(); MF(2, 1, Fq); RB(G, 1, vb0, vb1, SS, 0); SBAR(); MF(3, 1, Fq); RB(G, 1, vb0, vb1, SS, 1); SBAR(); \
    WAITL(8); SBAR(); \
    MF(0, 2, Fq); RB(G, 2, vb0, vb1, SS, 0); SBAR(); MF(1, 2, Fq); RB(G, 2, vb0, vb1, SS, 1); SBAR(); MF(2, 2, Fq); RB(G, 3, vb0, vb1, SS, 0); SBAR(); MF(3, 2, Fq); RB(G, 3, vb0, vb1, SS, 1); SBAR(); \
    MF(0, 3, Fq); RB(G, 4, vb0, vb1, SS, 0); SBAR(); MF(1, 3, Fq); RB(G, 4, vb0, vb1, SS, 1); SBAR(); MF(2, 3, Fq); RB(G, 5, vb0, vb1, SS, 0); SBAR(); MF(3, 3, Fq); RB(G, 5, vb0, vb1, SS, 1); SBAR(); \
    MF(0, 4, Fq); MF(1, 4, Fq); MF(2, 4, Fq); MF(3, 4, Fq); MF(0, 5, Fq); MF(1, 5, Fq); MF(2, 5, Fq); MF(3, 5, Fq); } while (0)
#define KSTEP(MODE) do { \
    if (kt == sx) { const unsigned xo = xad + buf * STG; \
      _Pragma("unroll") for (int t = 0; t < 8; ++t) { const unsigned a_ = xo ^ (t * 16); \
        asm volatile("ds_read_b64 %0, %1" : "+v"(xv[t]) : "v"(a_) : "memory"); } \
      WAITL(0); SBAR(); } \
    const int nbuf = (buf + 1) & 3; \
    const unsigned bo = buf * STG, nbo = nbuf * STG; \
    { unsigned va = aad[1] + bo, vb0 = bad[0] + bo, vb1 = bad[1] + bo; SUB(f0, f1, va, vb0, vb1, 1, ); } \
    if (MODE < 2) { \
      unsigned va = aad[0] + nbo, vb0 = bad[0] + nbo, vb1 = bad[1] + nbo; \
      SUB(f1, f0, va, vb0, vb1, 0, if (MODE == 0) { WAITL(0); BAR(); SBAR(); }); \
    } else { WAITL(0); BAR(); SBAR(); \
      MF(0, 2, f1); MF(1, 2, f1); MF(2, 2, f1); MF(3, 2, f1); MF(0, 3, f1); MF(1, 3, f1); MF(2, 3, f1); MF(3, 3, f1); \
      MF(0, 4, f1); MF(1, 4, f1); MF(2, 4, f1); MF(3, 4, f1); MF(0, 5, f1); MF(1, 5, f1); MF(2, 5, f1); MF(3, 5, f1); \
      MF(0, 0, f1); MF(1, 0, f1); MF(2, 0, f1); MF(3, 0, f1); MF(0, 1, f1); MF(1, 1, f1); MF(2, 1, f1); MF(3, 1, f1); } \
    buf = nbuf; } while (0)
  u2 xv[8];
#pragma unroll
  for (int t = 0; t < 8; ++t) asm volatile("" : "=v"(xv[t]));
  const int sx = (nb - rot) & 15;
  const unsigned xad = ldsb + (w * 32 + (l & 31)) * 128 + ((r15 >> 1) & 7) * 16 + (l >> 5) * 8;
  STMP(1);
  WAITL(0);
  BAR();
  STMP(2);
  WAITL(0);
  FQ f0, f1;
  { unsigned va = aad[0], vb0 = bad[0], vb1 = bad[1];
    RA(f0, 0, va); RB(f0, 0, vb0, vb1, 0, 0); RB(f0, 0, vb0, vb1, 0, 1); RA(f0, 1, va); RA(f0, 2, va); RA(f0, 3, va);
    RB(f0, 1, vb0, vb1, 0, 0); RB(f0, 1, vb0, vb1, 0, 1); RB(f0, 2, vb0, vb1, 0, 0); RB(f0, 2, vb0, vb1, 0, 1); RB(f0, 3, vb0, vb1, 0, 0); RB(f0, 3, vb0, vb1, 0, 1);
    RB(f0, 4, vb0, vb1, 0, 0); RB(f0, 4, vb0, vb1, 0, 1); RB(f0, 5, vb0, vb1, 0, 0); RB(f0, 5, vb0, vb1, 0, 1);
    WAITL(0); SBAR(); }
  int buf = 0;
#pragma unroll 1
  for (int kt = 0; kt < 15; ++kt) KSTEP(0);
  { const int kt = 15; KSTEP(2); }
  STMP(3);
  __builtin_amdgcn_s_setprio(2);
  const LAS float* Lm = (const LAS float*)(lds + L_M) + wr * 64;
  const LAS float* Lr = (const LAS float*)(lds + L_RS) + wr * 64;
  const LAS float* Lc = (const LAS float*)(lds + L_CS) + wc * 96;
  f4 kv[4], zt[2];
  {
    float cs[4];
#pragma unroll
    for (int n = 0; n < 4; ++n) cs[n] = Lc[32 + 16 * n + r15];
    const u4 ones = (u4)(0x3F803F80u);
#pragma unroll
    for (int t = 0; t < 2; ++t) {
      u4 kf[2], vf[2];
#pragma unroll
      for (int mm = 0; mm < 2; ++mm) {
        const int m = 2 * t + mm;
        const f4 mv = *(const LAS f4*)(Lm + m * 16 + 4 * g4), rv = *(const LAS f4*)(Lr + m * 16 + 4 * g4);
#pragma unroll
        for (int a = 0; a < 2; ++a) {
          const f4 uk = acc[m][2 + a] - mv * cs[a];
          const f4 uv = (acc[m][4 + a] - mv * cs[2 + a]) * rv;
          kf[a][2 * mm] = phi2((f2){uk[0], uk[1]}, (f2){rv[0], rv[1]}); kf[a][2 * mm + 1] = phi2((f2){uk[2], uk[3]}, (f2){rv[2], rv[3]});
          vf[a][2 * mm] = pk2(uv[0], uv[1]); vf[a][2 * mm + 1] = pk2(uv[2], uv[3]);
        }
      }
#pragma unroll
      for (int tt = 0; tt < 4; ++tt) kv[tt] = MFMA16(S8(kf[tt & 1]), S8(vf[tt >> 1]), t ? kv[tt] : (f4)(0.f));
#pragma unroll
      for (int a = 0; a < 2; ++a) zt[a] = MFMA16(S8(kf[a]), S8(ones), t ? zt[a] : (f4)(0.f));
    }
  }
  const int hd = hp * 2 + wc;
  {
    LAS f4* ks = (LAS f4*)(lds + L_KVX + w * 3072) + l;
    const LAS f4* kr = (const LAS f4*)(lds + L_KVX + (w ^ 2) * 3072) + l;
    ks[0] = wr ? kv[0] : kv[2]; ks[64] = wr ? kv[1] : kv[3]; ks[128] = wr ? zt[0] : zt[1];
    lsig(myFL, 1);
    const f4 m0 = wr ? kv[2] : kv[0], m1 = wr ? kv[3] : kv[1], mz = wr ? zt[1] : zt[0];
    lwait(adFL, 1u << (w ^ 2), 1);
    const f4 s0 = m0 + kr[0], s1 = m1 + kr[64], sz = mz + kr[128];
    u4 o; o[0] = min(pk2(s0[0], s0[1]), POIS - 1u); o[1] = pk2(s0[2], s0[3]); o[2] = pk2(s1[0], s1[1]); o[3] = pk2(s1[2], s1[3]);
    ST128(o, WS_KVP + (hd * 16 + mb) * 2048 + wr * 1024 + l * 16);
    u4 zo = __builtin_bit_cast(u4, sz); zo[0] = min(zo[0], POIS - 1u);
    if (r15 == 0) ST128(zo, WS_ZP + (hd * 16 + mb) * 128 + g4 * 32 + wr * 16);
  }
  STMP(4);
  {
    const f4 c0 = *(const LAS f4*)(Lc + 4 * g4), c1 = *(const LAS f4*)(Lc + 16 + 4 * g4);
#pragma unroll
    for (int m = 0; m < 4; ++m) {
      const float mv = Lm[m * 16 + r15], rv = Lr[m * 16 + r15];
      const f4 u0 = acc[m][0] - c0 * mv, u1 = acc[m][1] - c1 * mv;
      u4 pf;
      pf[0] = phi2((f2){u0[0], u0[1]}, (f2)(rv)); pf[1] = phi2((f2){u0[2], u0[3]}, (f2)(rv));
      pf[2] = phi2((f2){u1[0], u1[1]}, (f2)(rv)); pf[3] = phi2((f2){u1[2], u1[3]}, (f2)(rv));
      *(LAS u4*)(lds + L_PQ + wr * 8192 + wc * 4096 + m * 1024 + l * 16) = pf;
    }
  }
  lsig(myFL, 2);
  __builtin_amdgcn_s_setprio(0);
  STMP(5);
  const int trow = mb * 128 + w * 32 + (l & 31);
  u2 oh;
  { const char* hb = uptr(wsx + WS_HK); const unsigned ho = ((l >> 1) & 15) * 2048 + (mb ^ ((l & 1) * 8)) * 128;
    asm volatile("global_load_dwordx2 %0, %1, %2 sc1" : "=v"(oh) : "v"(ho), "s"(hb) : "memory"); }
  int sel = -1;
  {
    const u4 s0 = *(const LAS u4*)(lds + L_SEL), s1 = *(const LAS u4*)(lds + L_SEL + 16);
#pragma unroll
    for (int i = 0; i < 8; ++i) { const int v = (int)(i < 4 ? s0[i & 3] : s1[i & 3]); sel = v == trow ? i : sel; }
  }
  const bool anysel = __ballot(sel >= 0) != 0ull;
  f4 xi[8];
  {
    const float m = *(const LAS float*)(lds + L_M + (w * 32 + (l & 31)) * 4), rs = *(const LAS float*)(lds + L_RS + (w * 32 + (l & 31)) * 4);
#pragma unroll
    for (int t = 0; t < 8; ++t) {
      const f4 xf = (f4){lo(xv[t][0]), hi(xv[t][0]), lo(xv[t][1]), hi(xv[t][1])};
      xi[t] = (xf - m) * rs + xf;
      asm volatile("" : "+v"(xi[t]));
    }
  }
  unsigned wa[2];
#pragma unroll
  for (int t = 0; t < 2; ++t) wa[t] = ldsb + L_WO + (4 * (g4 >> 1) + q) * 128 + (((4 * t + 2 * (g4 & 1) + (p >> 1)) ^ ((q >> 1) << 2)) * 16) + (p & 1) * 8;
  int qo[4];
  {
    WAITV(0);
    asm volatile("" : "+v"(oh));
    unsigned rel = oh[1] - NOW() + 1000u; rel = oh[0] == tag ? rel : ~0u;
    rel = max(rel, (unsigned)__builtin_amdgcn_update_dpp(0, (int)rel, 0xB1, 0xf, 0xf, false));
    rel = max(rel, (unsigned)__builtin_amdgcn_update_dpp(0, (int)rel, 0x4E, 0xf, 0xf, false));
    rel = max(rel, (unsigned)__builtin_amdgcn_update_dpp(0, (int)rel, 0x141, 0xf, 0xf, false));
    const unsigned r0 = __builtin_amdgcn_readlane(rel, 0), r1 = __builtin_amdgcn_readlane(rel, 8), r2 = __builtin_amdgcn_readlane(rel, 16), r3 = __builtin_amdgcn_readlane(rel, 24);
    const int a = GRP == 8 && r0 > r1 ? 1 : 0, b = GRP == 8 && r2 > r3 ? 3 : 2; const bool sw = GRP <= 16 && max(r0, r1) > max(r2, r3);
    qo[0] = DYN ? (sw ? b : a) : 0; qo[1] = qo[0] ^ 1; qo[2] = DYN ? (sw ? a : b) : 2; qo[3] = qo[2] ^ 1;
  }
  u4 a[RD][2];
#pragma unroll
  for (int k = 0; k < RD; ++k) asm volatile("" : "=v"(a[k][0]), "=v"(a[k][1]));
  const char* ob = uptr(wsx + WS_OF + mb * 262144 + w * 2048);
  const unsigned ov0 = l * 16;
  STMP(6);
  lwait<1>(adFL, 0x30u, 1);
  STMP(7);
  unsigned seen = 0, go = 0; u2 oh2;
  asm volatile("" : "=v"(oh2));
  const int lq = (l >> 3) & 3;
  const unsigned flo = HEADAT((lq == 0 ? qo[0] : lq == 1 ? qo[1] : lq == 2 ? qo[2] : qo[3]) * 8 + (l & 7)) * 128;
  const char* fb = uptr(wsx + WS_OH + mb * 4096);
  auto need = [&](unsigned mask, bool first) {
    if (__builtin_expect((go & mask) != mask, 0)) {
      _Pragma("unroll 1") for (unsigned it = 0; it < SPIN; ++it) {
        bool poll = (seen & mask) != mask;
        if (first && seen != ~0u && !poll) poll = ((unsigned)__ballot(oh[1] - NOW() - 60u < 1000u) & mask) != 0u;
        if (poll) {
          asm volatile("global_load_dwordx2 %0, %1, %2 sc1\n s_waitcnt vmcnt(0)" : "+v"(oh) : "v"(flo), "s"(fb) : "memory");
          seen = (unsigned)__ballot(oh[0] == tag);
        }
        go = seen & (unsigned)__ballot(oh[1] - NOW() - 1u >= 1000u);
        if ((go & mask) == mask) break;
        SLEEP(1);
      }
    }
  };
#define OX 0
#define OLD(pos) do { if (OX == 2 && (pos) >= RD) break; const char* sb_ = ob + (OX == 1 ? ((pos) & 3) : HEADAT(qo[(pos) >> 3] * 8 + ((pos) & 7))) * 8192; \
    asm volatile("global_load_dwordx4 %0, %1, %2 sc1" : "+v"(a[(pos) % RD][0]) : "v"(ov0), "s"(sb_) : "memory"); \
    asm volatile("global_load_dwordx4 %0, %1, %2 offset:1024 sc1" : "+v"(a[(pos) % RD][1]) : "v"(ov0), "s"(sb_) : "memory"); } while (0)
#define OLP(pos) do { if (ISPP(pos)) asm volatile("global_load_dwordx2 %0, %1, %2 sc1" : "+v"(oh2) : "v"(flo), "s"(fb) : "memory"); \
    OLD(pos); } while (0)
  auto isbad = [&](u4(&v)[2]) {
    asm volatile("" : "+v"(v[0]), "+v"(v[1]));
    return __ballot(max(v[0][0], v[1][0]) == POIS) != 0ull;
  };
#define OCHK(pos) do { WAITV(oyoung(pos)); \
    if (__builtin_expect(isbad(a[(pos) % RD]), 0)) { RETRY(); _Pragma("unroll 1") for (unsigned it = 0; it < SPIN; ++it) { SLEEP(4); OLD(pos); WAITV(0); if (!isbad(a[(pos) % RD])) break; } } \
    if (ISPP(pos)) { asm volatile("" : "+v"(oh2)); oh = oh2; seen = (unsigned)__ballot(oh[0] == tag); } } while (0)
  need(1u, true);
  if (OWN) lwait<1>(adFL, 0x30u, 2);
  __builtin_amdgcn_s_setprio(2);
  STMP(15);
  OLD(0); STMP(14);
#pragma unroll
  for (int i = 1; i < RD; ++i) { need(1u << i, false); OLP(i); }
  STMP(10);
  s4 b[2][8];
  f16v oc[3][2];
#define ORD(pos, f) do { if ((f) < 2 && ((pos) & 7) == 0) { wa[(f) & 1] += (qo[(pos) >> 3] - ((pos) ? qo[((pos) >> 3) - ((pos) ? 1 : 0)] : 0)) * 32768; asm volatile("" : "+v"(wa[(f) & 1])); } \
    RDTR(b[(pos) & 1][2 * (f)], wa[(f) & 1], ((pos) & 7) * 4096 + ((f) >> 1) * 1024); RDTR(b[(pos) & 1][2 * (f) + 1], wa[(f) & 1], ((pos) & 7) * 4096 + ((f) >> 1) * 1024 + 2048); } while (0)
#define OSET(pos) (GRP == 8 ? ((pos) < 8 ? 0 : (pos) < 24 ? 1 : 2) : (pos) >> 4)
#define OMF(pos, f) do { if ((f) < 2 && ((pos) & (GRP == 8 ? 7 : 15)) == 0) asm volatile("v_mfma_f32_32x32x16_bf16 %0, %1, %2, 0" : "=&v"(oc[OSET(pos)][(f) & 1]) \
      : "v"(CAT(b[(pos) & 1][2 * (f)], b[(pos) & 1][2 * (f) + 1])), "v"(a[(pos) % RD][(f) >> 1])); \
    else asm volatile("v_mfma_f32_32x32x16_bf16 %0, %1, %2, %0" : "+v"(oc[OSET(pos)][(f) & 1]) \
      : "v"(CAT(b[(pos) & 1][2 * (f)], b[(pos) & 1][2 * (f) + 1])), "v"(a[(pos) % RD][(f) >> 1])); \
    SBAR(); if ((pos) + 1 < 32) ORD(((pos) + 1) & 31, f); SBAR(); } while (0)
#define OFENCE(k) asm volatile("s_nop 7\n s_nop 7\n s_nop 7" : "+v"(oc[k][0]), "+v"(oc[k][1]))
#define OADD(d, k) do { OFENCE(k); oc[d][0] += oc[k][0]; oc[d][1] += oc[k][1]; asm volatile("s_nop 1" : "+v"(oc[d][0]), "+v"(oc[d][1])); SBAR(); } while (0)
#define ONX(pos) ((pos) > 0 && (pos) + RD - 1 < 32)
#define OHEAD(pos) do { \
    if (GRP == 8 && (pos) == 16) OADD(0, 1); \
    WAITL(4); SBAR(); \
    OMF(pos, 0); if (ONX(pos)) need(1u << (((pos) + RD - 1) & 31), false); SBAR(); \
    OMF(pos, 1); if (ONX(pos)) OLP(((pos) + RD - 1) & 31); \
    WAITL((pos) + 1 < 32 ? 4 : 0); SBAR(); \
    OMF(pos, 2); OMF(pos, 3); \
    if ((pos) + 1 < 32) OCHK(((pos) + 1) & 31); SBAR(); } while (0)
#define OGRP(c) do { OHEAD(4 * (c)); OHEAD(4 * (c) + 1); OHEAD(4 * (c) + 2); OHEAD(4 * (c) + 3); } while (0)
  ORD(0, 0); ORD(0, 1); ORD(0, 2); ORD(0, 3);
  OCHK(0); SBAR();
  OGRP(0); STMP(11); OGRP(1);
  OGRP(2); OGRP(3); STMP(12); OGRP(4); STMP(13); OGRP(5); OGRP(6); OGRP(7);
  if (GRP == 8) OADD(1, 2); else OFENCE(1);
#pragma unroll
  for (int t = 0; t < 8; ++t)
#pragma unroll
    for (int i = 0; i < 4; ++i) xi[t][i] += oc[0][t >> 2][(t & 3) * 4 + i] + oc[1][t >> 2][(t & 3) * 4 + i];
  STMP(8);
  if (anysel) {
    lwait(adFL, 0xC0u, 3);
    if (sel >= 0) {
#pragma unroll
      for (int t = 0; t < 8; ++t) xi[t] += *(const LAS f4*)(lds + L_KVX + sel * 256 + (8 * t + 4 * (l >> 5)) * 4);
    }
  }
  const __amdgpu_buffer_rsrc_t outr = __builtin_amdgcn_make_buffer_rsrc(out, 0, 2048 * D * 2, 0x00020000);
#pragma unroll
  for (int hh = 0; hh < 4; ++hh) {
    const f4 ta = xi[2 * hh], tb = xi[2 * hh + 1];
    auto r0 = __builtin_amdgcn_permlane32_swap(pk2(ta[0], ta[1]), pk2(tb[0], tb[1]), false, false);
    auto r1 = __builtin_amdgcn_permlane32_swap(pk2(ta[2], ta[3]), pk2(tb[2], tb[3]), false, false);
    __builtin_amdgcn_raw_buffer_store_b128(((u4){r0[0], r1[0], r0[1], r1[1]}), outr, (trow * D + nb * 64 + 16 * hh + 8 * (l >> 5)) * 2, 0, 16);
  }
  if (g == 0 && w == 0 && l == 0) __builtin_amdgcn_raw_buffer_store_b32(tag, wse, WS_E, 0, 16);
  STMP(9);
  STFLUSH();
  asm volatile(".Lend_c:");
  return;
  }
  {
    const int lw = w - 4;
    unsigned ev;
    asm volatile("global_load_dword %0, %1, %2 sc1" : "=v"(ev) : "v"(0u), "s"(ws) : "memory");
    unsigned vf[20], vfw[4];
#pragma unroll
    for (int i = 0; i < 8; ++i) { const int row = 8 * (lw * 8 + i) + (l >> 3); vf[i] = row * 2048 + (((l & 7) ^ ((row >> 1) & 7)) * 16) + (3 - (i & 3)) * 1024; }
#pragma unroll
    for (int i = 0; i < 12; ++i) {
      const int pc = lw * 12 + i, row = (pc & 7) * 8 + (l >> 3);
      vf[8 + i] = row * 6144 + (pc >> 3) * 2048 + (((l & 7) ^ ((((row >> 1) & 1) << 2) | (((row >> 3) & 1) << 1))) * 16) + (3 - (i & 3)) * 1024;
    }
#pragma unroll
    for (int i = 0; i < 4; ++i) vfw[i] = (8 * i + (l >> 3)) * 2048 + (((l & 7) ^ (((l >> 4) & 1) << 2)) * 16) + (3 - i) * 1024;
    const char* Ab = uptr((const char*)x + (size_t)mb * 128 * 2048) - 3072;
    const char* Bb = uptr((const char*)wqkv + hp * 128) - 3072;
    const unsigned mba = ldsb + lw * 8192, mbb = ldsb + 16384 + lw * 12288;
#define GL4(v, sb, mm, MO) do { asm volatile("s_add_i32 m0, %2, %3\n s_nop 0\n global_load_lds_dwordx4 %0, %1" ::"v"((v)[0]), "s"(sb), "s"(mm), "i"(MO) : "memory", "scc"); \
      GLI((v)[1], sb, 1024); GLI((v)[2], sb, 2048); GLI((v)[3], sb, 3072); } while (0)
    auto issue = [&](int s, int u0, int u1) {
      const int kt = (s + rot) & 15;
      const char* sa = Ab + kt * 128; const char* sb = Bb + kt * (64 * 6144);
      const unsigned so = (s & 3) * STG, ma = mba + so, mq = mbb + so;
#pragma unroll
      for (int u = u0; u < u1; ++u) { if (u < 2) GL4(vf + 4 * u, sa, ma, u * 4096); else GL4(vf + 4 * u, sb, mq, (u - 2) * 4096); }
    };
    int tk = 0;
#define LBAR() do { BST(tk + 1); BAR(); BST(tk + 21); ++tk; } while (0)
    issue(0, 0, 5); issue(1, 0, 5);
    BST(60);
    WAITV(20); BST(0); BAR(); BST(20);
    asm volatile("" : "+v"(ev));
    const unsigned tag = __builtin_amdgcn_readfirstlane(ev) + 1u;
    MKWS(tag);
    issue(2, 0, 5);
    int s0 = 3;
#pragma unroll 1
    for (; s0 < (NOSKIP ? 16 : 8 + ph); ++s0) { issue(s0, 0, 5); BST(tk + 40); WAITV(40); LBAR(); }
#pragma unroll 1
    for (; s0 < 16; s0 += 4) {
      WAITV(20); LBAR();
      issue(s0, 0, 5); issue(s0 + 1, 0, 2); BST(tk + 40); WAITV(28); LBAR();
      issue(s0 + 1, 2, 5);
      if (s0 + 2 >= 16) { WAITV(20); LBAR(); break; }
      issue(s0 + 2, 0, 4); BST(tk + 40); WAITV(36); LBAR();
      issue(s0 + 2, 4, 5);
      if (s0 + 3 >= 16) { WAITV(20); LBAR(); break; }
      issue(s0 + 3, 0, 5); BST(tk + 40); WAITV(40); LBAR();
    }
    int wq = lw;
    auto wo_upto = [&](int lim) {
      _Pragma("unroll 1") for (; wq < lim; wq += 2) { const char* wp = wob - 3072 + HEADAT(wq) * 65536; const unsigned wm = ldsb + wq * 4096; GL4(vfw, wp, wm, L_WO); }
    };
    if (ph == 0 || NOSKIP) { WAITV(20); LBAR(); }
    if (ph == 1 && !NOSKIP) WAITV(0); else { wo_upto(12); WAITV(24); }
    LBAR();
    if (lw == 0) ST64(((u2){tag, NOW() + KD}), l == 0 ? WS_HK + (hp * 16 + mb) * 128 : WS_SZ);
    if (NOSKIP) wo_upto(22); else if (ph != 2) wo_upto(ph ? 14 : 22);
    LBAR();
    wo_upto(32);
    STMP(1);
    const int hd = hp * 2 + lw;
    u4 sel[2];
#pragma unroll
    for (int e = 0; e < 2; ++e) {
      const unsigned v = (r15 >> 2) == g4 ? (((r15 & 3) == 2 * e ? 0x3F80u : 0u) | ((r15 & 3) == 2 * e + 1 ? 0x3F800000u : 0u)) : 0u;
      sel[0][e] = v; sel[0][2 + e] = 0u; sel[1][e] = 0u; sel[1][2 + e] = v;
    }
    u4 pv[16][2], zv0, zv1, pq[8];
    const char* kvb = uptr(wsx + WS_KVP + (size_t)hd * 32768);
    const char* zb = uptr(wsx + WS_ZP + (size_t)hd * 2048);
    const char* hb = uptr(wsx + WS_HK + hp * 2048);
    const unsigned lv16 = l * 16, zoff = r15 * 128 + g4 * 32;
    const unsigned pa = ldsb + L_PQ + lw * 4096 + l * 16, omask = lw ? 0x0Au : 0x05u;
    u2 hk;
    _Pragma("unroll 1") for (unsigned it = 0; it < SPIN; ++it) {
      asm volatile("global_load_dwordx2 %0, %1, %2 sc1\n s_waitcnt vmcnt(0)" : "=v"(hk) : "v"(r15 * 128), "s"(hb) : "memory");
      if (it == 0) lsig(myFL, 1);
      if (__ballot(hk[0] != tag) == 0ull) break;
      SLEEP(2);
    }
    __builtin_amdgcn_s_setprio(3);
    STMP(2);
    const unsigned t0 = NOW();
    unsigned rel = hk[1] - t0; rel = rel < 1000u ? rel : 0u;
    rel = max(rel, (unsigned)__builtin_amdgcn_update_dpp(0, (int)rel, 0xB1, 0xf, 0xf, false));
    rel = max(rel, (unsigned)__builtin_amdgcn_update_dpp(0, (int)rel, 0x4E, 0xf, 0xf, false));
    int gx;
    { const unsigned r0 = __builtin_amdgcn_readlane(rel, 0), r1 = __builtin_amdgcn_readlane(rel, 4), r2 = __builtin_amdgcn_readlane(rel, 8), r3 = __builtin_amdgcn_readlane(rel, 12);
      gx = max(r0, r1) > max(r2, r3) ? (r0 > r1 ? 3 : 2) : (r2 > r3 ? 1 : 0); }
#define GLD(gq) do { _Pragma("unroll") for (int i = 0; i < 4; ++i) { const char* sb = kvb + (((gq) ^ gx) * 4 + (i & 2)) * 2048; \
      asm volatile("global_load_dwordx4 %0, %1, %2 offset:%3 sc1" : "=v"(pv[(gq) * 4 + i][0]) : "v"(lv16), "s"(sb), "i"((i & 1) * 2048) : "memory"); \
      asm volatile("global_load_dwordx4 %0, %1, %2 offset:%3 sc1" : "=v"(pv[(gq) * 4 + i][1]) : "v"(lv16), "s"(sb), "i"((i & 1) * 2048 + 1024) : "memory"); } } while (0)
#define ZLD() do { asm volatile("global_load_dwordx4 %0, %1, %2 sc1" : "=v"(zv0) : "v"(zoff), "s"(zb) : "memory"); \
      asm volatile("global_load_dwordx4 %0, %1, %2 offset:16 sc1" : "=v"(zv1) : "v"(zoff), "s"(zb) : "memory"); } while (0)
#pragma unroll
    for (int gq = 0; gq < 4; ++gq) {
      const unsigned rq = __builtin_amdgcn_readlane(rel, 4 * (gq ^ gx));
      _Pragma("unroll 1") for (unsigned it = 0; it < SPIN; ++it) if (NOW() - t0 >= rq) break;
      if (gq == 3) ZLD();
      GLD(gq);
    }
    ST64(((u2){tag, NOW() + OD}), l == 0 ? WS_OH + (mb * 32 + hd) * 128 : WS_SZ);
    STMP(5);
    lwait(adFL, omask, 2);
#pragma unroll
    for (int i = 0; i < 8; ++i) RD128(pq[i], pa, (i >> 2) * 8192 + (i & 3) * 1024);
#pragma unroll
    for (int gq = 0; gq < 4; ++gq) {
      SBAR(); WAITV(gq == 0 ? 27 : gq == 1 ? 19 : gq == 2 ? 11 : 1);
      _Pragma("unroll 1") for (unsigned it = 0; it < SPIN; ++it) {
#pragma unroll
        for (int i = 0; i < 4; ++i) asm volatile("" : "+v"(pv[gq * 4 + i][0]), "+v"(pv[gq * 4 + i][1]));
        unsigned m = umax3(pv[gq * 4][0][0], pv[gq * 4][1][0], pv[gq * 4 + 1][0][0]);
        m = umax3(m, pv[gq * 4 + 1][1][0], pv[gq * 4 + 2][0][0]);
        m = umax3(m, pv[gq * 4 + 2][1][0], pv[gq * 4 + 3][0][0]);
        m = max(m, pv[gq * 4 + 3][1][0]);
        if (gq == 3) { asm volatile("" : "+v"(zv0), "+v"(zv1)); m = umax3(m, zv0[0], zv1[0]); }
        if (__ballot(m == POIS) == 0ull) break;
        RETRY();
        SLEEP(2);
        if (gq == 3) ZLD();
        GLD(gq);
        WAITV(0);
      }
      f4 ga[4];
#pragma unroll
      for (int i = 0; i < 4; ++i)
#pragma unroll
        for (int t = 0; t < 4; ++t) ga[t] = MFMA16(S8(sel[t & 1]), S8(pv[gq * 4 + i][t >> 1]), i ? ga[t] : (f4)(0.f));
#pragma unroll
      for (int t = 0; t < 4; ++t) pv[gq * 4 + (t >> 1)][t & 1] = __builtin_bit_cast(u4, ga[t]);
    }
    STMP(3);
    WAITL(0);
#pragma unroll
    for (int i = 0; i < 8; i += 2) asm volatile("" : "+v"(pq[i]), "+v"(pq[i + 1]));
    u4 kA[2], zA;
    {
      float z[8];
#pragma unroll
      for (int e = 0; e < 4; ++e) { z[e] = __uint_as_float(zv0[e]); z[4 + e] = __uint_as_float(zv1[e]); }
#define ZR(n) "v_add_f32_dpp %0, %0, %0 row_ror:" #n " row_mask:0xf bank_mask:0xf\n v_add_f32_dpp %1, %1, %1 row_ror:" #n " row_mask:0xf bank_mask:0xf\n" \
              "v_add_f32_dpp %2, %2, %2 row_ror:" #n " row_mask:0xf bank_mask:0xf\n v_add_f32_dpp %3, %3, %3 row_ror:" #n " row_mask:0xf bank_mask:0xf\n" \
              "v_add_f32_dpp %4, %4, %4 row_ror:" #n " row_mask:0xf bank_mask:0xf\n v_add_f32_dpp %5, %5, %5 row_ror:" #n " row_mask:0xf bank_mask:0xf\n" \
              "v_add_f32_dpp %6, %6, %6 row_ror:" #n " row_mask:0xf bank_mask:0xf\n v_add_f32_dpp %7, %7, %7 row_ror:" #n " row_mask:0xf bank_mask:0xf\n"
      asm volatile("s_nop 1\n" ZR(8) ZR(4) ZR(2) ZR(1) : "+v"(z[0]), "+v"(z[1]), "+v"(z[2]), "+v"(z[3]), "+v"(z[4]), "+v"(z[5]), "+v"(z[6]), "+v"(z[7]));
#pragma unroll
      for (int e = 0; e < 4; ++e) zA[e] = pk2(z[2 * e], z[2 * e + 1]);
    }
    float inv[8];
#pragma unroll
    for (int i = 0; i < 8; ++i) { f4 dn = MFMA16(S8(zA), S8(pq[i]), (f4)(0.f)); inv[i] = dn[0]; }
#pragma unroll
    for (int t = 0; t < 4; ++t) {
#define PF(gq) __builtin_bit_cast(f4, pv[(gq) * 4 + (t >> 1)][t & 1])
      f4 s = (PF(0) + PF(1)) + (PF(2) + PF(3));
      kA[t >> 1][(t & 1) * 2] = pk2(s[0], s[1]); kA[t >> 1][(t & 1) * 2 + 1] = pk2(s[2], s[3]);
    }
    f4 o0[8], o1[8];
#pragma unroll
    for (int i = 0; i < 8; ++i) {
      o0[i] = MFMA16(S8(kA[0]), S8(pq[i]), (f4)(0.f)); o1[i] = MFMA16(S8(kA[1]), S8(pq[i]), (f4)(0.f));
      inv[i] = __builtin_amdgcn_rcpf(inv[i] + 1e-6f);
    }
#pragma unroll
    for (int i = 0; i < 8; ++i) {
      o0[i] *= inv[i]; o1[i] *= inv[i];
      u4 o; o[0] = min(pk2(o0[i][0], o0[i][1]), POIS - 1u); o[1] = pk2(o0[i][2], o0[i][3]); o[2] = pk2(o1[i][0], o1[i][1]); o[3] = pk2(o1[i][2], o1[i][3]);
      ST128(o, WS_OF + (mb * 32 + hd) * 8192 + (i >> 1) * 2048 + (i & 1) * 256 + g4 * 512 + r15 * 16);
    }
    lsig(myFL, 2);
    STMP(4);
    STFLUSH();
    asm volatile(".Lend_l:");
  }

}

extern "C" void kernel_launch(void* const* d_in, const int* in_sizes, int n_in, void* d_out, int out_size,
                              void* d_ws, size_t ws_size, hipStream_t stream) {
  (void)hipFuncSetAttribute((const void*)fused, hipFuncAttributeMaxDynamicSharedMemorySize, LDS_SZ);
  fused<<<256, 512, LDS_SZ, stream>>>((const u16*)d_in[0], (const u16*)d_in[1], (const u16*)d_in[2], (const u16*)d_in[4],
                                      (const u16*)d_in[5], (const u16*)d_in[6], (const u16*)d_in[7], (char*)d_ws, (u16*)d_out);
}
```

```cpp
#define EVW 0
#define EVT 0
#define EVG b_==0
#include <hip/hip_runtime.h>
#include <stdint.h>

typedef unsigned short u16;
typedef short s4 __attribute__((ext_vector_type(4)));
typedef short s8 __attribute__((ext_vector_type(8)));
typedef float f4 __attribute__((ext_vector_type(4)));
typedef float f16v __attribute__((ext_vector_type(16)));
typedef float f2 __attribute__((ext_vector_type(2)));
typedef unsigned u4 __attribute__((ext_vector_type(4)));
typedef unsigned u2 __attribute__((ext_vector_type(2)));
typedef __bf16 bf2 __attribute__((ext_vector_type(2)));
#define LAS __attribute__((address_space(3)))
#define DEV __device__ __forceinline__

constexpr int D = 1024, F = 3584;
#define NOMOE 0
#define KD 235
#define OD 190
#define HPRIO 0
#define OWN 1
#define NOPOIS 0
#define CAP 7
#define RD 7
#define DYN 1
#define JX 0
#define NOSKIP 0
#define GRP 8
#define ROT 0

constexpr unsigned WS_E = 0;
constexpr unsigned WS_YF = 8192;
constexpr unsigned WS_CAND = 12288;
constexpr unsigned WS_GF = 32768;
constexpr unsigned WS_RG = 49152;
constexpr unsigned WS_ST = 65536;
constexpr unsigned WS_HK = 98304;
constexpr unsigned WS_ZP = 131072;
constexpr unsigned WS_CS = 196608;
constexpr unsigned WS_OH = 458752;
constexpr unsigned WS_KVP = 1u << 20;
constexpr unsigned WS_GP = 2u << 20;
constexpr unsigned WS_YP = 3u << 20;
constexpr unsigned WS_OF = 5u << 20;
constexpr unsigned WS_SZ = 9u << 20;
#define NR 4
#define RGO(tag) ((64u << 20) + ((tag) & (NR - 1u)) * (10u << 20))
#define MKWS(tag) char* const wsx = ws + RGO(tag); const __amdgpu_buffer_rsrc_t wsr = __builtin_amdgcn_make_buffer_rsrc(wsx, 0, WS_SZ, 0x00020000)

constexpr int STG = 40960;
constexpr int L_KVX = 0;
constexpr int L_PQ = 12288;
constexpr int L_M = 28672, L_RS = L_M + 512, L_CS = L_RS + 512, L_SEL = L_CS + 768, L_FL = L_SEL + 64;
constexpr int L_WO = 32768;
constexpr int LDS_SZ = 163840;
constexpr unsigned POIS = 0xFFFFFFFFu;

DEV unsigned pk2(float a, float b) { bf2 v; v[0] = (__bf16)a; v[1] = (__bf16)b; return __builtin_bit_cast(unsigned, v); }
DEV float lo(unsigned u) { return __uint_as_float(u << 16); }
DEV float hi(unsigned u) { return __uint_as_float(u & 0xFFFF0000u); }
DEV float rbf(float a) { return lo(pk2(a, a)); }
DEV float dot2(unsigned a, unsigned b, float c) { return __builtin_amdgcn_fdot2_f32_bf16(__builtin_bit_cast(bf2, a), __builtin_bit_cast(bf2, b), c, false); }
#define RD128(dst, addr, OFF) asm volatile("ds_read_b128 %0, %1 offset:%2" : "=v"(dst) : "v"(addr), "i"(OFF) : "memory")
#define RDTR(dst, addr, OFF) asm volatile("ds_read_b64_tr_b16 %0, %1 offset:%2" : "=v"(dst) : "v"(addr), "i"(OFF) : "memory")
#define SBAR() __builtin_amdgcn_sched_barrier(0)
#define CAT(x, y) __builtin_shufflevector(x, y, 0, 1, 2, 3, 4, 5, 6, 7)
#define WAITV(n) asm volatile("s_waitcnt vmcnt(%0)" ::"i"(n) : "memory")
#define WAITL(n) asm volatile("s_waitcnt lgkmcnt(%0)" ::"i"(n) : "memory")
#define MEMB() asm volatile("" ::: "memory")
#define SLEEP(n) asm volatile("s_sleep %0" ::"i"(n) : "memory")
#define BAR() asm volatile("s_barrier" ::: "memory")
#define GL0(vo, sb, m0v) asm volatile("s_mov_b32 m0, %2\n s_nop 0\n global_load_lds_dwordx4 %0, %1" ::"v"(vo), "s"(sb), "s"(m0v) : "memory")
#define GLI(vo, sb, OFF) asm volatile("global_load_lds_dwordx4 %0, %1 offset:%2" ::"v"(vo), "s"(sb), "i"(OFF) : "memory")
#define MFMA16(a, b, c) __builtin_amdgcn_mfma_f32_16x16x32_bf16(a, b, c, 0, 0, 0)
#define S8(x) __builtin_bit_cast(s8, x)
#define LD128(off) __builtin_amdgcn_raw_buffer_load_b128(wsr, (off), 0, 16)
#define LD64(off) __builtin_amdgcn_raw_buffer_load_b64(wsr, (off), 0, 16)
#define ST128(v, off) __builtin_amdgcn_raw_buffer_store_b128((v), wsr, (off), 0, 16)
#define ST64(v, off) __builtin_amdgcn_raw_buffer_store_b64((v), wsr, (off), 0, 16)
#define OFA(i) (WS_OF + ((mb * 32 + hd) * 8 + (i)) * 1024 + l * 16)
#define NOW() ((unsigned)__builtin_amdgcn_s_memrealtime())
constexpr unsigned SPIN = 1u << 21;
DEV const char* uptr(const char* p) {
  unsigned long long v = (unsigned long long)p;
  unsigned lo_ = __builtin_amdgcn_readfirstlane((unsigned)v), hi_ = __builtin_amdgcn_readfirstlane((unsigned)(v >> 32));
  return (const char*)(((unsigned long long)hi_ << 32) | lo_);
}
DEV void lsig(unsigned addr, unsigned val) { asm volatile("s_waitcnt lgkmcnt(0)\n ds_write_b32 %0, %1" ::"v"(addr), "v"(val) : "memory"); }
template <int SL = 0> DEV void lwait(unsigned addr, unsigned mask, unsigned target) {
  _Pragma("unroll 1") for (unsigned it = 0; it < SPIN; ++it) {
    unsigned v;
    asm volatile("ds_read_b32 %0, %1\n s_waitcnt lgkmcnt(0)" : "=v"(v) : "v"(addr) : "memory");
    if (((unsigned)__ballot(v < target) & mask) == 0u) break;
    if (SL) SLEEP(SL);
  }
}
DEV unsigned umax3(unsigned a, unsigned b, unsigned c) { return max(max(a, b), c); }
template <int C> DEV float dppf(float v) { return __int_as_float(__builtin_amdgcn_update_dpp(0, __float_as_int(v), C, 0xf, 0xf, false)); }
DEV unsigned wave_max(unsigned v) {
  v = max(v, (unsigned)__builtin_amdgcn_update_dpp(0, (int)v, 0x111, 0xf, 0xf, false));
  v = max(v, (unsigned)__builtin_amdgcn_update_dpp(0, (int)v, 0x112, 0xf, 0xf, false));
  v = max(v, (unsigned)__builtin_amdgcn_update_dpp(0, (int)v, 0x114, 0xf, 0xf, false));
  v = max(v, (unsigned)__builtin_amdgcn_update_dpp(0, (int)v, 0x118, 0xf, 0xf, false));
  v = max(v, (unsigned)__builtin_amdgcn_update_dpp(0, (int)v, 0x142, 0xa, 0xf, false));
  v = max(v, (unsigned)__builtin_amdgcn_update_dpp(0, (int)v, 0x143, 0xc, 0xf, false));
  return (unsigned)__builtin_amdgcn_readlane((int)v, 63);
}
DEV unsigned phi2(f2 u, f2 rs) {
  f2 t = u * rs;
  f2 a = t * 1.44269504f, p = t + 1.f, r;
  r[0] = __builtin_amdgcn_fmed3f(__builtin_amdgcn_exp2f(a[0]), p[0], 1.f);
  r[1] = __builtin_amdgcn_fmed3f(__builtin_amdgcn_exp2f(a[1]), p[1], 1.f);
  r += 1e-6f;
  return pk2(r[0], r[1]);
}

#define STDECL unsigned st_[16] = {0, 0, 0, 0, 0, 0, 0, 0, 0, 0, 0, 0, 0, 0, 0, 0}
#define STMP(i) st_[i] = (unsigned)__builtin_amdgcn_s_memrealtime()
#define BST(i)
#define RETRY()
#define STFLUSH() do { const int b_ = blockIdx.x; if (w == EVW && (EVG) && st_[EVT]) { _Pragma("unroll 1") for (unsigned it = 0; it < SPIN; ++it) { if (NOW() - st_[EVT] >= 4500u) break; SLEEP(1); } } } while (0)

DEV float half_sum(float v) {
  asm volatile("s_nop 1\n v_add_f32_dpp %0, %0, %0 row_ror:8 row_mask:0xf bank_mask:0xf\n s_nop 1\n v_add_f32_dpp %0, %0, %0 row_ror:4 row_mask:0xf bank_mask:0xf\n"
               "s_nop 1\n v_add_f32_dpp %0, %0, %0 row_ror:2 row_mask:0xf bank_mask:0xf\n s_nop 1\n v_add_f32_dpp %0, %0, %0 row_ror:1 row_mask:0xf bank_mask:0xf\n s_nop 1" : "+v"(v));
  auto r = __builtin_amdgcn_permlane16_swap(__float_as_uint(v), __float_as_uint(v), false, false);
  return __uint_as_float(r[0]) + __uint_as_float(r[1]);
}

constexpr int PP = 8 - RD < 1 ? 1 : 8 - RD;
#define ISPP(pos) ((pos) < 24 && ((pos) & 7) == PP)
constexpr int oyoung(int pos) {
  int n = 0;
  for (int i = pos + 1; i < pos + RD - 1 + (pos == 0) && i < 32; ++i) n += 2 + (ISPP(i) ? 1 : 0);
  return n;
}
struct FQ { s8 a[4]; s4 b[12]; };
template <bool B> struct BC { static constexpr bool v = B; };

__global__ __launch_bounds__(512) void fused(const u16* __restrict__ x, const u16* __restrict__ wqkv, const u16* __restrict__ wo,
                                             const u16* __restrict__ wrt, const u16* __restrict__ wg, const u16* __restrict__ wu,
                                             const u16* __restrict__ wd, char* __restrict__ ws, u16* __restrict__ out) {
  extern __shared__ __attribute__((aligned(16))) char lds[];
  asm volatile(".Lcode_beg:");
  const int w = __builtin_amdgcn_readfirstlane(threadIdx.x >> 6), l = threadIdx.x & 63;
  STDECL;
  STMP(0);
  const int g = blockIdx.x, xcd = g & 7, j = g >> 3, ii = j >> 2;
  unsigned cw = 0;
  if (w == 7) {
    unsigned long long pc; unsigned off, len, l1, l2, l3;
    asm volatile("s_getpc_b64 %0\n.Lcode_here:" : "=s"(pc));
    asm volatile("s_mov_b32 %0, (.Lcode_here - .Lcode_beg)" : "=s"(off));
    asm volatile("s_mov_b32 %0, (.Lend_c - .Lcode_beg)" : "=s"(len));
    asm volatile("s_mov_b32 %0, (.Lend_l - .Lcode_beg)" : "=s"(l1));
    asm volatile("s_mov_b32 %0, (.Lend_h - .Lcode_beg)" : "=s"(l2));
    asm volatile("s_mov_b32 %0, (.Lend_s - .Lcode_beg)" : "=s"(l3));
    len = max(max(len, l3), max(l1, l2));
    const unsigned o = min((unsigned)(l * 32 + j) * 64u, len - 4u);
    asm volatile("global_load_dword %0, %1, %2" : "+v"(cw) : "v"(o), "s"(pc - off) : "memory");
  }
  const int mb = (xcd & 1) * 8 + ii, hp = (xcd >> 1) * 4 + ((j + JX) & 3);
#define JR 0
  const int rot = 2 * xcd + 4 * ((ii + (JR == 1 ? j : JR == 2 ? (j & 2) : 0)) & 3);
  const int nb = hp;
  const int hr = ROT * (GRP / 4) * (j & 3);
#define HEADAT(p) (((p) & ~(GRP - 1)) | (((p) + hr) & (GRP - 1)))
  const int ph = ii >= 6 ? ii - 6 : ii >= 3 ? ii - 3 : ii;
  const unsigned ldsb = (unsigned)(size_t)(LAS char*)lds;
  const unsigned adFL = ldsb + L_FL + 4 * (l & 7), myFL = ldsb + L_FL + 4 * w;
  const __amdgpu_buffer_rsrc_t wse = __builtin_amdgcn_make_buffer_rsrc(ws, 0, 64, 0x00020000);
  const int r15 = l & 15, g4 = l >> 4, q = (l & 15) >> 2, p = l & 3;
  const char* wob = uptr((const char*)wo + nb * 128);

  if (w != 4 && w != 5) {
  const unsigned ev0 = __builtin_amdgcn_raw_buffer_load_b32(wse, WS_E, 0, 16);
  if (w >= 6) {
    const int mw = w - 6;
    const unsigned tag = __builtin_amdgcn_readfirstlane(ev0) + 1u, tag2 = tag * 0x9E3779B1u;
    MKWS(tag);
    auto helper = [&](auto SP) {
    constexpr bool spare = decltype(SP)::v, moe = !spare && !NOMOE;
    int nbar = 0;
#define TICK() do { BST(nbar); SBAR(); BAR(); SBAR(); ++nbar; } while (0)
#define DTICK(lim) do { if (nbar < (lim)) TICK(); else SLEEP(2); } while (0)
    const int hd = hp * 2 + mw;
    const u4 pz = (u4)(POIS);
    const int chunk = g >> 2, ksp = g & 3;
    const int np = l & 7, kset = (l >> 3) & 1;
    u4 gw[4][8], dw[2][8];
    const __amdgpu_buffer_rsrc_t wgr = __builtin_amdgcn_make_buffer_rsrc((void*)(mw ? wu : wg), 0, D * F * 2, 0x00020000);
    const __amdgpu_buffer_rsrc_t wdr = __builtin_amdgcn_make_buffer_rsrc((void*)wd, 0, D * F * 2, 0x00020000);
    const __amdgpu_buffer_rsrc_t wor = __builtin_amdgcn_make_buffer_rsrc((void*)wo, 0, D * D * 2, 0x00020000);
    const unsigned gvo = ((256 * ksp + 32 * kset + 8 * g4) * F + 64 * chunk + 8 * np) * 2;
    const unsigned dvo = ((64 * chunk + 8 * g4) * D + 256 * ksp + 128 * mw + 8 * r15) * 2;
    auto burst = [&](int k) {
      if (moe) {
#pragma unroll
        for (int i = 0; i < 16; ++i) {
          if (k < 2) gw[(2 * k + (i >> 3)) & 3][i & 7] = __builtin_amdgcn_raw_buffer_load_b128(wgr, gvo, (128 * k + 64 * (i >> 3) + (i & 7)) * F * 2, 2);
          else dw[i >> 3][i & 7] = __builtin_amdgcn_raw_buffer_load_b128(wdr, dvo, (32 * (i >> 3) + (i & 7)) * D * 2, 2);
        }
      }
    };
    auto transp = [&](u4(&v)[8]) {
      if (moe) {
        u4 f[8];
#pragma unroll
        for (int c = 0; c < 8; ++c)
#pragma unroll
          for (int e = 0; e < 4; ++e) {
            const unsigned ev = v[2 * e][c >> 1], od = v[2 * e + 1][c >> 1];
            f[c][e] = (c & 1) ? __builtin_amdgcn_perm(od, ev, 0x07060302u) : __builtin_amdgcn_perm(od, ev, 0x05040100u);
          }
#pragma unroll
        for (int c = 0; c < 8; ++c) { v[c] = f[c]; asm volatile("" : "+v"(v[c])); }
      }
    };
    auto pois = [&](int i) {
      if (i < 2) ST128(pz, WS_KVP + (hd * 16 + mb) * 2048 + i * 1024 + l * 16);
      else if (i == 2) ST128(pz, WS_ZP + (hd * 16 + mb) * 128 + (l & 7) * 16);
      else if (!NOPOIS) ST128(pz, OFA(i - 3));
    };
    unsigned wt0, wt1;
    auto touch = [&]() {
      const unsigned to = (ii * 128 + mw * 64 + (l >> 1)) * 2048 + nb * 128 + (l & 1) * 64;
      wt0 = __builtin_amdgcn_raw_buffer_load_b32(wor, to, 0, 0); wt1 = __builtin_amdgcn_raw_buffer_load_b32(wor, to, 65536, 0);
    };
    auto colsum = [&](int h) {
      const unsigned bo = ldsb + (1 + h) * STG + 16384 + (8 * g4 + q) * 128 + (p & 1) * 8;
      const int sw = ((q >> 1) << 2) | ((g4 & 1) << 1);
      const unsigned b0 = bo + (((mw * 4 + (p >> 1)) ^ sw) * 16), b1 = bo + (((mw * 4 + 2 + (p >> 1)) ^ sw) * 16);
      const u4 ones = (u4)(0x3F803F80u);
      f4 c[6]; s4 b[24];
#pragma unroll
      for (int n = 0; n < 12; ++n) { RDTR(b[2 * n], (n & 1) ? b1 : b0, ((n % 6) >> 1) * 8192 + (n / 6) * 4096); RDTR(b[2 * n + 1], (n & 1) ? b1 : b0, ((n % 6) >> 1) * 8192 + (n / 6) * 4096 + 512); }
      WAITL(0);
      TICK();
#pragma unroll
      for (int n = 0; n < 12; ++n) c[n % 6] = MFMA16(S8(ones), CAT(b[2 * n], b[2 * n + 1]), n >= 6 ? c[n % 6] : (f4)(0.f));
      const u4 csg = (u4){__float_as_uint(g4 ? c[3][0] : c[0][0]), __float_as_uint(g4 ? c[4][0] : c[1][0]), __float_as_uint(g4 ? c[5][0] : c[2][0]), tag};
      ST128(csg, l < 32 ? WS_CS + (hd * 16 + mb) * 512 + l * 16 : WS_SZ);
    };
    u4 stv, cv[8], cd; float cs[3];
    auto gload = [&]() {
      stv = LD128(WS_ST + (mb * 128 + mw * 64 + l) * 16);
      cd = LD128(WS_CAND + (g & 31) * 128 + (l & 7) * 16);
#pragma unroll
      for (int i = 0; i < 8; ++i) cv[i] = LD128(WS_CS + hd * 8192 + i * 1024 + l * 16);
    };
    auto gbad = [&]() {
      unsigned bad = (stv[1] ^ tag) | (stv[3] ^ tag) | (cd[1] ^ tag) | (cd[3] ^ tag);
#pragma unroll
      for (int i = 0; i < 8; ++i) bad |= cv[i][3] ^ tag;
      return __ballot(bad != 0u) != 0ull;
    };
    int idx;
    auto gfin = [&]() {
      if (gbad()) { RETRY(); _Pragma("unroll 1") for (unsigned it = 0; it < SPIN; ++it) { SLEEP(2); gload(); if (!gbad()) break; } }
#pragma unroll
      for (int e = 0; e < 3; ++e) {
        float a = __uint_as_float(cv[0][e]);
#pragma unroll
        for (int i = 1; i < 8; ++i) a += __uint_as_float(cv[i][e]);
        auto r = __builtin_amdgcn_permlane32_swap(__float_as_uint(a), __float_as_uint(a), false, false);
        cs[e] = __uint_as_float(r[0]) + __uint_as_float(r[1]);
      }
      asm volatile("" : "+v"(cs[0]), "+v"(cs[1]), "+v"(cs[2]) : "v"(wt0), "v"(wt1));
      cd[0] = max(cd[0], cd[2]);
      idx = (int)(cd[0] & 2047u);
    };
    auto lpub = [&]() {
      *(LAS float*)(lds + L_M + (mw * 64 + l) * 4) = __uint_as_float(stv[0]);
      *(LAS float*)(lds + L_RS + (mw * 64 + l) * 4) = __uint_as_float(stv[2]);
      if (l < 32) {
#pragma unroll
        for (int e = 0; e < 3; ++e) *(LAS float*)(lds + L_CS + mw * 384 + ((3 * g4 + e) * 16 + r15) * 4) = cs[e];
      }
      if (mw == 0 && l < 8) { *(LAS int*)(lds + L_SEL + l * 4) = idx; *(LAS unsigned*)(lds + L_FL + 4 * l) = 0u; }
      WAITL(0);
    };
    u4 xs[4];
    asm volatile("" ::"s"(wg), "s"(wu), "s"(wd), "v"(cw));
    if (HPRIO) __builtin_amdgcn_s_setprio(HPRIO);
    TICK();
    if (spare) {
#pragma unroll
      for (int i = 0; i < 6; ++i) pois(i);
      TICK();
#pragma unroll
      for (int i = 6; i < 11; ++i) pois(i);
      TICK();
      u4 kk[8];
      auto kload = [&]() {
#pragma unroll
        for (int i = 0; i < 8; ++i) kk[i] = LD128(WS_RG + mw * 8192 + i * 1024 + l * 16);
      };
      kload();
      colsum(1);
      _Pragma("unroll 1") for (unsigned it = 0; it < SPIN; ++it) {
        while (nbar < 10 && (__builtin_amdgcn_s_getreg(7 | (31 << 11)) & 0xC000000Eu)) TICK();
        unsigned bad = 0;
#pragma unroll
        for (int i = 0; i < 8; ++i) bad |= (kk[i][1] ^ tag) | (kk[i][3] ^ tag);
        if (__ballot(bad != 0u) == 0ull) break;
        kload();
        DTICK(10);
      }
      __builtin_amdgcn_s_setprio(3);
      unsigned base = 0, mine8 = 0;
#pragma unroll 1
      for (int it = 0; it < 8; ++it) {
        unsigned m = 0;
#pragma unroll
        for (int i = 0; i < 8; ++i) m = umax3(m, kk[i][0], kk[i][2]);
        m = wave_max(m);
#pragma unroll
        for (int i = 0; i < 8; ++i) { kk[i][0] -= m; kk[i][2] -= m; }
        base += m;
        mine8 = l == it ? base : mine8;
        if ((it == 1 || it == 4) && nbar < 10) TICK();
      }
      if (l < 8) ST64(((u2){mine8, tag}), WS_CAND + (g - 224) * 128 + (mw ? 7 - l : l) * 16 + mw * 8);
      __builtin_amdgcn_s_setprio(HPRIO);
      STMP(2);
      touch();
      while (nbar < 10) TICK();
      gload();
      TICK(); TICK();
      WAITV(0);
      gfin();
      STMP(3);
      TICK(); TICK();
      lpub();
    } else {
      TICK();
      if (ii >> 2) TICK();
      colsum(ii >> 2);
      if (!(ii >> 2)) TICK();
      STMP(1);
      pois(0); pois(1); pois(2);
      TICK();
      pois(3); pois(4);
      TICK();
      for (int i = 0; i < ph; ++i) TICK();
      burst(0); touch();
      TICK();
      pois(5); pois(6);
      TICK();
      pois(7); pois(8);
      transp(gw[0]);
      TICK();
      pois(9); pois(10);
      gload();
      transp(gw[1]);
      TICK();
      burst(1);
      TICK();
      WAITV(moe ? 16 : 0);
      gfin();
      u4 sv;
      if (moe) {
        const u16* xp = x + (size_t)idx * D + 256 * ksp + 32 * kset + 8 * g4;
#pragma unroll
        for (int it = 0; it < 4; ++it) xs[it] = *(const u4*)(xp + 64 * it);
        sv = LD128(WS_ST + idx * 16);
      }
      STMP(3);
      TICK();
      transp(gw[2]);
      TICK();
      transp(gw[3]);
      TICK();
      lpub();
      if (moe) {
        if (__ballot(sv[1] != tag || sv[3] != tag) != 0ull) {
          _Pragma("unroll 1") for (unsigned it = 0; it < SPIN; ++it) { SLEEP(1); sv = LD128(WS_ST + idx * 16); if (__ballot(sv[1] != tag || sv[3] != tag) == 0ull) break; }
        }
        asm volatile("" : "+v"(xs[0]), "+v"(xs[1]), "+v"(xs[2]), "+v"(xs[3]));
      }
      burst(2);
      TICK();
      if (moe) {
        const float m = __uint_as_float(sv[0]), rs = __uint_as_float(sv[2]);
#pragma unroll
        for (int it = 0; it < 4; ++it) {
#pragma unroll
          for (int e = 0; e < 4; ++e) { const unsigned d = pk2(lo(xs[it][e]) - m, hi(xs[it][e]) - m); xs[it][e] = pk2(lo(d) * rs, hi(d) * rs); }
          asm volatile("" : "+v"(xs[it]));
        }
      }
    }
    while (nbar < 17) TICK();
    if (HPRIO) __builtin_amdgcn_s_setprio(0);
    STMP(4);
    float sg;
    {
      const unsigned mono = cd[0] >> 16, rb = (mono & 0x8000u) ? (mono & 0x7FFFu) : (~mono & 0xFFFFu);
      sg = rbf(1.f / (1.f + __expf(-lo(rb))));
    }
    if (moe) {
      STMP(5);
      f4 acc[8];
#pragma unroll
      for (int it = 0; it < 4; ++it)
#pragma unroll
        for (int c = 0; c < 8; ++c) acc[c] = MFMA16(S8(xs[it]), S8(gw[it][c]), it ? acc[c] : (f4)(0.f));
#pragma unroll
      for (int i = 0; i < 4; ++i) {
        u4 s;
#pragma unroll
        for (int c = 0; c < 4; ++c) {
          auto r = __builtin_amdgcn_permlane32_swap(__float_as_uint(acc[c][i]), __float_as_uint(acc[c + 4][i]), false, false);
          s[c] = __float_as_uint(__uint_as_float(r[0]) + dppf<0x128>(__uint_as_float(r[1])));
        }
        if (r15 < 8) ST128(s, WS_GP + (((chunk * 4 + ksp) * 2 + mw) * 8 + 4 * (g4 & 1) + i) * 256 + r15 * 32 + (g4 >> 1) * 16);
      }
      WAITV(0);
      if (l == 0) ST64(((u2){tag, tag2}), WS_GF + chunk * 128 + (ksp * 2 + mw) * 8);
      STMP(6);
      transp(dw[0]); transp(dw[1]);
      _Pragma("unroll 1") for (unsigned it = 0; it < SPIN; ++it) {
        u2 f = LD64(WS_GF + chunk * 128 + (l & 7) * 8);
        if (__ballot(f[0] != tag || f[1] != tag2) == 0ull) break;
        SLEEP(1);
      }
      STMP(7);
      u4 ha[2];
      {
        u4 pg[4][4];
#pragma unroll
        for (int ks = 0; ks < 4; ++ks)
#pragma unroll
          for (int t = 0; t < 4; ++t)
            pg[ks][t] = LD128(WS_GP + (((chunk * 4 + ks) * 2 + (t >> 1)) * 8 + np) * 256 + (32 * kset + 8 * g4) * 4 + (t & 1) * 16);
        f4 gq[4];
#pragma unroll
        for (int t = 0; t < 4; ++t)
          gq[t] = (__builtin_bit_cast(f4, pg[0][t]) + __builtin_bit_cast(f4, pg[1][t])) + (__builtin_bit_cast(f4, pg[2][t]) + __builtin_bit_cast(f4, pg[3][t]));
        float hv[8];
#pragma unroll
        for (int e = 0; e < 8; ++e) {
          const float gg = rbf(gq[e >> 2][e & 3]), uu = rbf(gq[2 + (e >> 2)][e & 3]);
          hv[e] = rbf(gg / (1.f + __expf(-gg))) * uu;
        }
#pragma unroll
        for (int e = 0; e < 4; ++e) { ha[0][e] = pk2(hv[2 * e], hv[2 * e + 1]); ha[1][e] = (unsigned)__builtin_amdgcn_update_dpp(0, (int)ha[0][e], 0x128, 0xf, 0xf, false); }
      }
#pragma unroll
      for (int it = 0; it < 2; ++it)
#pragma unroll
        for (int c = 0; c < 8; ++c) acc[c] = MFMA16(S8(ha[it]), S8(dw[it][c]), it ? acc[c] : (f4)(0.f));
      if (g4 < 2) {
#pragma unroll
        for (int i = 0; i < 4; ++i) {
          const unsigned yo = WS_YP + ((chunk * 8 + 4 * g4 + i) * D + 256 * ksp + 128 * mw + 8 * r15) * 4;
          ST128(__builtin_bit_cast(u4, ((f4){acc[0][i], acc[1][i], acc[2][i], acc[3][i]})), yo);
          ST128(__builtin_bit_cast(u4, ((f4){acc[4][i], acc[5][i], acc[6][i], acc[7][i]})), yo + 16);
        }
      }
      WAITV(0);
      if (l == 0) ST64(((u2){tag, tag2}), WS_YF + (ksp * 2 + mw) * 512 + chunk * 8);
      STMP(8);
    }
    unsigned mine = (unsigned)__ballot(!NOMOE && l < 8 && (idx >> 7) == mb && (l & 1) == mw);
    if (mine) {
      _Pragma("unroll 1") for (unsigned it = 0; it < SPIN; ++it) {
        u2 f = LD64(WS_YF + (nb >> 1) * 512 + (l < 56 ? l : 0) * 8);
        if (__ballot(f[0] != tag || f[1] != tag2) == 0ull) break;
        SLEEP(1);
      }
      lwait<1>(adFL, 0x0Fu, 2);
      while (mine) {
        const int t = __builtin_ctz(mine); mine &= mine - 1;
        u4 yv[14];
#pragma unroll
        for (int i = 0; i < 14; ++i) yv[i] = LD128(WS_YP + (((i * 4 + g4) * 8 + t) * D + nb * 64 + r15 * 4) * 4);
        f4 y = __builtin_bit_cast(f4, yv[0]);
#pragma unroll
        for (int i = 1; i < 14; ++i) y += __builtin_bit_cast(f4, yv[i]);
        const float gt = __int_as_float(__builtin_amdgcn_readlane(__float_as_int(sg), t));
#pragma unroll
        for (int e = 0; e < 4; ++e) {
          auto r = __builtin_amdgcn_permlane32_swap(__float_as_uint(y[e]), __float_as_uint(y[e]), false, false);
          const float a = __uint_as_float(r[0]) + __uint_as_float(r[1]);
          auto r2 = __builtin_amdgcn_permlane16_swap(__float_as_uint(a), __float_as_uint(a), false, false);
          y[e] = rbf(rbf(__uint_as_float(r2[0]) + __uint_as_float(r2[1])) * gt);
        }
        if (l < 16) *(LAS f4*)(lds + L_KVX + t * 256 + l * 16) = y;
      }
    }
    lsig(myFL, 3u);
    STMP(9);
    STFLUSH();
    if (spare) asm volatile(".Lend_s:"); else asm volatile(".Lend_h:");
    };
    if (g >= 224) helper(BC<true>()); else helper(BC<false>());
    return;
  }

  const int wr = w >> 1, wc = w & 1;
  unsigned tag; u2 kg; u4 sg4;
  const int srow = mb * 128 + hp * 8 + 2 * w + (l >> 5);
  {
    const int row = srow;
    u4 xa[4], rw[4];
#pragma unroll
    for (int c = 0; c < 4; ++c) xa[c] = *(const u4*)(x + row * D + (l & 31) * 8 + 256 * c);
#pragma unroll
    for (int c = 0; c < 4; ++c) rw[c] = *(const u4*)(wrt + (l & 31) * 8 + 256 * c);
    float s0 = 0, s1 = 0;
#pragma unroll
    for (int c = 0; c < 4; ++c)
#pragma unroll
      for (int e = 0; e < 4; e += 2) { unsigned x0 = xa[c][e], x1 = xa[c][e + 1]; s0 = dot2(x0, 0x3F803F80u, s0); s1 = dot2(x1, 0x3F803F80u, s1); }
    const float m = half_sum(s0 + s1) * (1.f / D);
    f2 v[16], q2 = (f2)(0.f), q3 = (f2)(0.f);
#pragma unroll
    for (int c = 0; c < 16; ++c) v[c] = (f2){lo(xa[c >> 2][c & 3]), hi(xa[c >> 2][c & 3])} - m;
#pragma unroll
    for (int c = 0; c < 16; c += 2) { q2 += v[c] * v[c]; q3 += v[c + 1] * v[c + 1]; }
    q2 += q3;
    const float mr = rbf(m);
    const float rs = 1.f / rbf(sqrtf(rbf(rbf(half_sum(q2[0] + q2[1]) * (1.f / D)) + 1e-5f)));
    float d0 = 0, d1 = 0;
#pragma unroll
    for (int c = 0; c < 16; c += 2) {
      const f2 e0 = (f2){lo(xa[c >> 2][c & 3]), hi(xa[c >> 2][c & 3])} - mr, e1 = (f2){lo(xa[c >> 2][(c & 3) + 1]), hi(xa[c >> 2][(c & 3) + 1])} - mr;
      const unsigned p0 = pk2(e0[0], e0[1]), p1 = pk2(e1[0], e1[1]);
      const f2 n0 = (f2){lo(p0), hi(p0)} * rs, n1 = (f2){lo(p1), hi(p1)} * rs;
      unsigned c0 = rw[c >> 2][c & 3], c1 = rw[c >> 2][(c & 3) + 1];
      d0 = dot2(pk2(n0[0], n0[1]), c0, d0); d1 = dot2(pk2(n1[0], n1[1]), c1, d1);
    }
    const float d = half_sum(d0 + d1);
    const unsigned rb = pk2(d, d) & 0xFFFFu;
    const unsigned mono = (rb & 0x8000u) ? (~rb & 0xFFFFu) : (rb | 0x8000u);
    tag = __builtin_amdgcn_readfirstlane(ev0) + 1u;
    kg = (u2){(mono << 16) | (unsigned)row, tag};
    sg4 = (u4){__float_as_uint(mr), tag, __float_as_uint(rs), tag};
  }
  MKWS(tag);
  if ((l & 31) == 0) { ST64(kg, WS_RG + srow * 8); ST128(sg4, WS_ST + srow * 16); }
  unsigned aad[2], bad[2];
#pragma unroll
  for (int s = 0; s < 2; ++s) aad[s] = ldsb + (wr * 64 + r15) * 128 + (((4 * s + g4) ^ ((r15 >> 1) & 7)) * 16);
#pragma unroll
  for (int nh = 0; nh < 2; ++nh) bad[nh] = ldsb + 16384 + (8 * g4 + q) * 128 + (((wc * 4 + 2 * nh + (p >> 1)) ^ (((q >> 1) << 2) | ((g4 & 1) << 1))) * 16) + (p & 1) * 8;
  f4 acc[4][6];
#pragma unroll
  for (int m = 0; m < 4; ++m)
#pragma unroll
    for (int n = 0; n < 6; ++n) { acc[m][n] = (f4)(0.f); asm volatile("" : "+v"(acc[m][n])); }
#define MF(m, n, Fq) do { if (n < 2) acc[m][n] = MFMA16(CAT(Fq.b[2 * n], Fq.b[2 * n + 1]), Fq.a[m], acc[m][n]); \
     else acc[m][n] = MFMA16(Fq.a[m], CAT(Fq.b[2 * n], Fq.b[2 * n + 1]), acc[m][n]); SBAR(); } while (0)
#define RB(G, n, vb0, vb1, SS, hf) RDTR(G.b[2 * (n) + (hf)], (((n) & 1) ? vb1 : vb0), ((n) >> 1) * 8192 + (SS) * 4096 + (hf) * 512)
#define RA(G, m, va) RD128(G.a[m], va, (m) * 2048)
#define SUB(Fq, G, va, vb0, vb1, SS, PRE) do { \
    WAITL(13); SBAR(); \
    MF(0, 0, Fq); PRE; RA(G, 0, va); SBAR(); \
    WAITL(11); SBAR(); \
    MF(1, 0, Fq); RB(G, 0, vb0, vb1, SS, 0); SBAR(); MF(2, 0, Fq); RB(G, 0, vb0, vb1, SS, 1); SBAR(); MF(3, 0, Fq); RA(G, 1, va); SBAR(); \
    WAITL(12); SBAR(); \
    MF(0, 1, Fq); RA(G, 2, va); SBAR(); MF(1, 1, Fq); RA(G, 3, va); SBAR(); MF(2, 1, Fq); RB(G, 1, vb0, vb1, SS, 0); SBAR(); MF(3, 1, Fq); RB(G, 1, vb0, vb1, SS, 1); SBAR(); \
    WAITL(8); SBAR(); \
    MF(0, 2, Fq); RB(G, 2, vb0, vb1, SS, 0); SBAR(); MF(1, 2, Fq); RB(G, 2, vb0, vb1, SS, 1); SBAR(); MF(2, 2, Fq); RB(G, 3, vb0, vb1, SS, 0); SBAR(); MF(3, 2, Fq); RB(G, 3, vb0, vb1, SS, 1); SBAR(); \
    MF(0, 3, Fq); RB(G, 4, vb0, vb1, SS, 0); SBAR(); MF(1, 3, Fq); RB(G, 4, vb0, vb1, SS, 1); SBAR(); MF(2, 3, Fq); RB(G, 5, vb0, vb1, SS, 0); SBAR(); MF(3, 3, Fq); RB(G, 5, vb0, vb1, SS, 1); SBAR(); \
    MF(0, 4, Fq); MF(1, 4, Fq); MF(2, 4, Fq); MF(3, 4, Fq); MF(0, 5, Fq); MF(1, 5, Fq); MF(2, 5, Fq); MF(3, 5, Fq); } while (0)
#define KSTEP(MODE) do { \
    if (kt == sx) { const unsigned xo = xad + buf * STG; \
      _Pragma("unroll") for (int t = 0; t < 8; ++t) { const unsigned a_ = xo ^ (t * 16); \
        asm volatile("ds_read_b64 %0, %1" : "+v"(xv[t]) : "v"(a_) : "memory"); } \
      WAITL(0); SBAR(); } \
    const int nbuf = (buf + 1) & 3; \
    const unsigned bo = buf * STG, nbo = nbuf * STG; \
    { unsigned va = aad[1] + bo, vb0 = bad[0] + bo, vb1 = bad[1] + bo; SUB(f0, f1, va, vb0, vb1, 1, ); } \
    if (MODE < 2) { \
      unsigned va = aad[0] + nbo, vb0 = bad[0] + nbo, vb1 = bad[1] + nbo; \
      SUB(f1, f0, va, vb0, vb1, 0, if (MODE == 0) { WAITL(0); BAR(); SBAR(); }); \
    } else { WAITL(0); BAR(); SBAR(); \
      MF(0, 2, f1); MF(1, 2, f1); MF(2, 2, f1); MF(3, 2, f1); MF(0, 3, f1); MF(1, 3, f1); MF(2, 3, f1); MF(3, 3, f1); \
      MF(0, 4, f1); MF(1, 4, f1); MF(2, 4, f1); MF(3, 4, f1); MF(0, 5, f1); MF(1, 5, f1); MF(2, 5, f1); MF(3, 5, f1); \
      MF(0, 0, f1); MF(1, 0, f1); MF(2, 0, f1); MF(3, 0, f1); MF(0, 1, f1); MF(1, 1, f1); MF(2, 1, f1); MF(3, 1, f1); } \
    buf = nbuf; } while (0)
  u2 xv[8];
#pragma unroll
  for (int t = 0; t < 8; ++t) asm volatile("" : "=v"(xv[t]));
  const int sx = (nb - rot) & 15;
  const unsigned xad = ldsb + (w * 32 + (l & 31)) * 128 + ((r15 >> 1) & 7) * 16 + (l >> 5) * 8;
  STMP(1);
  WAITL(0);
  BAR();
  STMP(2);
  WAITL(0);
  FQ f0, f1;
  { unsigned va = aad[0], vb0 = bad[0], vb1 = bad[1];
    RA(f0, 0, va); RB(f0, 0, vb0, vb1, 0, 0); RB(f0, 0, vb0, vb1, 0, 1); RA(f0, 1, va); RA(f0, 2, va); RA(f0, 3, va);
    RB(f0, 1, vb0, vb1, 0, 0); RB(f0, 1, vb0, vb1, 0, 1); RB(f0, 2, vb0, vb1, 0, 0); RB(f0, 2, vb0, vb1, 0, 1); RB(f0, 3, vb0, vb1, 0, 0); RB(f0, 3, vb0, vb1, 0, 1);
    RB(f0, 4, vb0, vb1, 0, 0); RB(f0, 4, vb0, vb1, 0, 1); RB(f0, 5, vb0, vb1, 0, 0); RB(f0, 5, vb0, vb1, 0, 1);
    WAITL(0); SBAR(); }
  int buf = 0;
#pragma unroll 1
  for (int kt = 0; kt < 15; ++kt) KSTEP(0);
  { const int kt = 15; KSTEP(2); }
  STMP(3);
  __builtin_amdgcn_s_setprio(2);
  const LAS float* Lm = (const LAS float*)(lds + L_M) + wr * 64;
  const LAS float* Lr = (const LAS float*)(lds + L_RS) + wr * 64;
  const LAS float* Lc = (const LAS float*)(lds + L_CS) + wc * 96;
  f4 kv[4], zt[2];
  {
    float cs[4];
#pragma unroll
    for (int n = 0; n < 4; ++n) cs[n] = Lc[32 + 16 * n + r15];
    const u4 ones = (u4)(0x3F803F80u);
#pragma unroll
    for (int t = 0; t < 2; ++t) {
      u4 kf[2], vf[2];
#pragma unroll
      for (int mm = 0; mm < 2; ++mm) {
        const int m = 2 * t + mm;
        const f4 mv = *(const LAS f4*)(Lm + m * 16 + 4 * g4), rv = *(const LAS f4*)(Lr + m * 16 + 4 * g4);
#pragma unroll
        for (int a = 0; a < 2; ++a) {
          const f4 uk = acc[m][2 + a] - mv * cs[a];
          const f4 uv = (acc[m][4 + a] - mv * cs[2 + a]) * rv;
          kf[a][2 * mm] = phi2((f2){uk[0], uk[1]}, (f2){rv[0], rv[1]}); kf[a][2 * mm + 1] = phi2((f2){uk[2], uk[3]}, (f2){rv[2], rv[3]});
          vf[a][2 * mm] = pk2(uv[0], uv[1]); vf[a][2 * mm + 1] = pk2(uv[2], uv[3]);
        }
      }
#pragma unroll
      for (int tt = 0; tt < 4; ++tt) kv[tt] = MFMA16(S8(kf[tt & 1]), S8(vf[tt >> 1]), t ? kv[tt] : (f4)(0.f));
#pragma unroll
      for (int a = 0; a < 2; ++a) zt[a] = MFMA16(S8(kf[a]), S8(ones), t ? zt[a] : (f4)(0.f));
    }
  }
  const int hd = hp * 2 + wc;
  {
    LAS f4* ks = (LAS f4*)(lds + L_KVX + w * 3072) + l;
    const LAS f4* kr = (const LAS f4*)(lds + L_KVX + (w ^ 2) * 3072) + l;
    ks[0] = wr ? kv[0] : kv[2]; ks[64] = wr ? kv[1] : kv[3]; ks[128] = wr ? zt[0] : zt[1];
    lsig(myFL, 1);
    const f4 m0 = wr ? kv[2] : kv[0], m1 = wr ? kv[3] : kv[1], mz = wr ? zt[1] : zt[0];
    lwait(adFL, 1u << (w ^ 2), 1);
    const f4 s0 = m0 + kr[0], s1 = m1 + kr[64], sz = mz + kr[128];
    u4 o; o[0] = min(pk2(s0[0], s0[1]), POIS - 1u); o[1] = pk2(s0[2], s0[3]); o[2] = pk2(s1[0], s1[1]); o[3] = pk2(s1[2], s1[3]);
    ST128(o, WS_KVP + (hd * 16 + mb) * 2048 + wr * 1024 + l * 16);
    u4 zo = __builtin_bit_cast(u4, sz); zo[0] = min(zo[0], POIS - 1u);
    if (r15 == 0) ST128(zo, WS_ZP + (hd * 16 + mb) * 128 + g4 * 32 + wr * 16);
  }
  STMP(4);
  {
    const f4 c0 = *(const LAS f4*)(Lc + 4 * g4), c1 = *(const LAS f4*)(Lc + 16 + 4 * g4);
#pragma unroll
    for (int m = 0; m < 4; ++m) {
      const float mv = Lm[m * 16 + r15], rv = Lr[m * 16 + r15];
      const f4 u0 = acc[m][0] - c0 * mv, u1 = acc[m][1] - c1 * mv;
      u4 pf;
      pf[0] = phi2((f2){u0[0], u0[1]}, (f2)(rv)); pf[1] = phi2((f2){u0[2], u0[3]}, (f2)(rv));
      pf[2] = phi2((f2){u1[0], u1[1]}, (f2)(rv)); pf[3] = phi2((f2){u1[2], u1[3]}, (f2)(rv));
      *(LAS u4*)(lds + L_PQ + wr * 8192 + wc * 4096 + m * 1024 + l * 16) = pf;
    }
  }
  lsig(myFL, 2);
  __builtin_amdgcn_s_setprio(0);
  STMP(5);
  const int trow = mb * 128 + w * 32 + (l & 31);
  u2 oh;
  { const char* hb = uptr(wsx + WS_HK); const unsigned ho = ((l >> 1) & 15) * 2048 + (mb ^ ((l & 1) * 8)) * 128;
    asm volatile("global_load_dwordx2 %0, %1, %2 sc1" : "=v"(oh) : "v"(ho), "s"(hb) : "memory"); }
  int sel = -1;
  {
    const u4 s0 = *(const LAS u4*)(lds + L_SEL), s1 = *(const LAS u4*)(lds + L_SEL + 16);
#pragma unroll
    for (int i = 0; i < 8; ++i) { const int v = (int)(i < 4 ? s0[i & 3] : s1[i & 3]); sel = v == trow ? i : sel; }
  }
  const bool anysel = __ballot(sel >= 0) != 0ull;
  f4 xi[8];
  {
    const float m = *(const LAS float*)(lds + L_M + (w * 32 + (l & 31)) * 4), rs = *(const LAS float*)(lds + L_RS + (w * 32 + (l & 31)) * 4);
#pragma unroll
    for (int t = 0; t < 8; ++t) {
      const f4 xf = (f4){lo(xv[t][0]), hi(xv[t][0]), lo(xv[t][1]), hi(xv[t][1])};
      xi[t] = (xf - m) * rs + xf;
      asm volatile("" : "+v"(xi[t]));
    }
  }
  unsigned wa[2];
#pragma unroll
  for (int t = 0; t < 2; ++t) wa[t] = ldsb + L_WO + (4 * (g4 >> 1) + q) * 128 + (((4 * t + 2 * (g4 & 1) + (p >> 1)) ^ ((q >> 1) << 2)) * 16) + (p & 1) * 8;
  int qo[4];
  {
    WAITV(0);
    asm volatile("" : "+v"(oh));
    unsigned rel = oh[1] - NOW() + 1000u; rel = oh[0] == tag ? rel : ~0u;
    rel = max(rel, (unsigned)__builtin_amdgcn_update_dpp(0, (int)rel, 0xB1, 0xf, 0xf, false));
    rel = max(rel, (unsigned)__builtin_amdgcn_update_dpp(0, (int)rel, 0x4E, 0xf, 0xf, false));
    rel = max(rel, (unsigned)__builtin_amdgcn_update_dpp(0, (int)rel, 0x141, 0xf, 0xf, false));
    const unsigned r0 = __builtin_amdgcn_readlane(rel, 0), r1 = __builtin_amdgcn_readlane(rel, 8), r2 = __builtin_amdgcn_readlane(rel, 16), r3 = __builtin_amdgcn_readlane(rel, 24);
    const int a = GRP == 8 && r0 > r1 ? 1 : 0, b = GRP == 8 && r2 > r3 ? 3 : 2; const bool sw = GRP <= 16 && max(r0, r1) > max(r2, r3);
    qo[0] = DYN ? (sw ? b : a) : 0; qo[1] = qo[0] ^ 1; qo[2] = DYN ? (sw ? a : b) : 2; qo[3] = qo[2] ^ 1;
  }
  u4 a[RD][2];
#pragma unroll
  for (int k = 0; k < RD; ++k) asm volatile("" : "=v"(a[k][0]), "=v"(a[k][1]));
  const char* ob = uptr(wsx + WS_OF + mb * 262144 + w * 2048);
  const unsigned ov0 = l * 16;
  STMP(6);
  lwait<1>(adFL, 0x30u, 1);
  STMP(7);
  unsigned seen = 0, go = 0; u2 oh2;
  asm volatile("" : "=v"(oh2));
  const int lq = (l >> 3) & 3;
  const unsigned flo = HEADAT((lq == 0 ? qo[0] : lq == 1 ? qo[1] : lq == 2 ? qo[2] : qo[3]) * 8 + (l & 7)) * 128;
  const char* fb = uptr(wsx + WS_OH + mb * 4096);
  auto need = [&](unsigned mask, bool first) {
    if (__builtin_expect((go & mask) != mask, 0)) {
      _Pragma("unroll 1") for (unsigned it = 0; it < SPIN; ++it) {
        bool poll = (seen & mask) != mask;
        if (first && seen != ~0u && !poll) poll = ((unsigned)__ballot(oh[1] - NOW() - 60u < 1000u) & mask) != 0u;
        if (poll) {
          asm volatile("global_load_dwordx2 %0, %1, %2 sc1\n s_waitcnt vmcnt(0)" : "+v"(oh) : "v"(flo), "s"(fb) : "memory");
          seen = (unsigned)__ballot(oh[0] == tag);
        }
        go = seen & (unsigned)__ballot(oh[1] - NOW() - 1u >= 1000u);
        if ((go & mask) == mask) break;
        SLEEP(1);
      }
    }
  };
#define OX 0
#define OLD(pos) do { if (OX == 2 && (pos) >= RD) break; const char* sb_ = ob + (OX == 1 ? ((pos) & 3) : HEADAT(qo[(pos) >> 3] * 8 + ((pos) & 7))) * 8192; \
    asm volatile("global_load_dwordx4 %0, %1, %2 sc1" : "+v"(a[(pos) % RD][0]) : "v"(ov0), "s"(sb_) : "memory"); \
    asm volatile("global_load_dwordx4 %0, %1, %2 offset:1024 sc1" : "+v"(a[(pos) % RD][1]) : "v"(ov0), "s"(sb_) : "memory"); } while (0)
#define OLP(pos) do { if (ISPP(pos)) asm volatile("global_load_dwordx2 %0, %1, %2 sc1" : "+v"(oh2) : "v"(flo), "s"(fb) : "memory"); \
    OLD(pos); } while (0)
  auto isbad = [&](u4(&v)[2]) {
    asm volatile("" : "+v"(v[0]), "+v"(v[1]));
    return __ballot(max(v[0][0], v[1][0]) == POIS) != 0ull;
  };
#define OCHK(pos) do { WAITV(oyoung(pos)); \
    if (__builtin_expect(isbad(a[(pos) % RD]), 0)) { RETRY(); _Pragma("unroll 1") for (unsigned it = 0; it < SPIN; ++it) { SLEEP(4); OLD(pos); WAITV(0); if (!isbad(a[(pos) % RD])) break; } } \
    if (ISPP(pos)) { asm volatile("" : "+v"(oh2)); oh = oh2; seen = (unsigned)__ballot(oh[0] == tag); } } while (0)
  need(1u, true);
  if (OWN) lwait<1>(adFL, 0x30u, 2);
  __builtin_amdgcn_s_setprio(2);
  STMP(15);
  OLD(0); STMP(14);
#pragma unroll
  for (int i = 1; i < RD; ++i) { need(1u << i, false); OLP(i); }
  STMP(10);
  s4 b[2][8];
  f16v oc[3][2];
#define ORD(pos, f) do { if ((f) < 2 && ((pos) & 7) == 0) { wa[(f) & 1] += (qo[(pos) >> 3] - ((pos) ? qo[((pos) >> 3) - ((pos) ? 1 : 0)] : 0)) * 32768; asm volatile("" : "+v"(wa[(f) & 1])); } \
    RDTR(b[(pos) & 1][2 * (f)], wa[(f) & 1], ((pos) & 7) * 4096 + ((f) >> 1) * 1024); RDTR(b[(pos) & 1][2 * (f) + 1], wa[(f) & 1], ((pos) & 7) * 4096 + ((f) >> 1) * 1024 + 2048); } while (0)
#define OSET(pos) (GRP == 8 ? ((pos) < 8 ? 0 : (pos) < 24 ? 1 : 2) : (pos) >> 4)
#define OMF(pos, f) do { if ((f) < 2 && ((pos) & (GRP == 8 ? 7 : 15)) == 0) asm volatile("v_mfma_f32_32x32x16_bf16 %0, %1, %2, 0" : "=&v"(oc[OSET(pos)][(f) & 1]) \
      : "v"(CAT(b[(pos) & 1][2 * (f)], b[(pos) & 1][2 * (f) + 1])), "v"(a[(pos) % RD][(f) >> 1])); \
    else asm volatile("v_mfma_f32_32x32x16_bf16 %0, %1, %2, %0" : "+v"(oc[OSET(pos)][(f) & 1]) \
      : "v"(CAT(b[(pos) & 1][2 * (f)], b[(pos) & 1][2 * (f) + 1])), "v"(a[(pos) % RD][(f) >> 1])); \
    SBAR(); if ((pos) + 1 < 32) ORD(((pos) + 1) & 31, f); SBAR(); } while (0)
#define OFENCE(k) asm volatile("s_nop 7\n s_nop 7\n s_nop 7" : "+v"(oc[k][0]), "+v"(oc[k][1]))
#define OADD(d, k) do { OFENCE(k); oc[d][0] += oc[k][0]; oc[d][1] += oc[k][1]; asm volatile("s_nop 1" : "+v"(oc[d][0]), "+v"(oc[d][1])); SBAR(); } while (0)
#define ONX(pos) ((pos) > 0 && (pos) + RD - 1 < 32)
#define OHEAD(pos) do { \
    if (GRP == 8 && (pos) == 16) OADD(0, 1); \
    WAITL(4); SBAR(); \
    OMF(pos, 0); if (ONX(pos)) need(1u << (((pos) + RD - 1) & 31), false); SBAR(); \
    OMF(pos, 1); if (ONX(pos)) OLP(((pos) + RD - 1) & 31); \
    WAITL((pos) + 1 < 32 ? 4 : 0); SBAR(); \
    OMF(pos, 2); OMF(pos, 3); \
    if ((pos) + 1 < 32) OCHK(((pos) + 1) & 31); SBAR(); } while (0)
#define OGRP(c) do { OHEAD(4 * (c)); OHEAD(4 * (c) + 1); OHEAD(4 * (c) + 2); OHEAD(4 * (c) + 3); } while (0)
  ORD(0, 0); ORD(0, 1); ORD(0, 2); ORD(0, 3);
  OCHK(0); SBAR();
  OGRP(0); STMP(11); OGRP(1);
  OGRP(2); OGRP(3); STMP(12); OGRP(4); STMP(13); OGRP(5); OGRP(6); OGRP(7);
  if (GRP == 8) OADD(1, 2); else OFENCE(1);
#pragma unroll
  for (int t = 0; t < 8; ++t)
#pragma unroll
    for (int i = 0; i < 4; ++i) xi[t][i] += oc[0][t >> 2][(t & 3) * 4 + i] + oc[1][t >> 2][(t & 3) * 4 + i];
  STMP(8);
  if (anysel) {
    lwait(adFL, 0xC0u, 3);
    if (sel >= 0) {
#pragma unroll
      for (int t = 0; t < 8; ++t) xi[t] += *(const LAS f4*)(lds + L_KVX + sel * 256 + (8 * t + 4 * (l >> 5)) * 4);
    }
  }
  const __amdgpu_buffer_rsrc_t outr = __builtin_amdgcn_make_buffer_rsrc(out, 0, 2048 * D * 2, 0x00020000);
#pragma unroll
  for (int hh = 0; hh < 4; ++hh) {
    const f4 ta = xi[2 * hh], tb = xi[2 * hh + 1];
    auto r0 = __builtin_amdgcn_permlane32_swap(pk2(ta[0], ta[1]), pk2(tb[0], tb[1]), false, false);
    auto r1 = __builtin_amdgcn_permlane32_swap(pk2(ta[2], ta[3]), pk2(tb[2], tb[3]), false, false);
    __builtin_amdgcn_raw_buffer_store_b128(((u4){r0[0], r1[0], r0[1], r1[1]}), outr, (trow * D + nb * 64 + 16 * hh + 8 * (l >> 5)) * 2, 0, 16);
  }
  if (g == 0 && w == 0 && l == 0) __builtin_amdgcn_raw_buffer_store_b32(tag, wse, WS_E, 0, 16);
  STMP(9);
  STFLUSH();
  asm volatile(".Lend_c:");
  return;
  }
  {
    const int lw = w - 4;
    unsigned ev;
    asm volatile("global_load_dword %0, %1, %2 sc1" : "=v"(ev) : "v"(0u), "s"(ws) : "memory");
    unsigned vf[20], vfw[4];
#pragma unroll
    for (int i = 0; i < 8; ++i) { const int row = 8 * (lw * 8 + i) + (l >> 3); vf[i] = row * 2048 + (((l & 7) ^ ((row >> 1) & 7)) * 16) + (3 - (i & 3)) * 1024; }
#pragma unroll
    for (int i = 0; i < 12; ++i) {
      const int pc = lw * 12 + i, row = (pc & 7) * 8 + (l >> 3);
      vf[8 + i] = row * 6144 + (pc >> 3) * 2048 + (((l & 7) ^ ((((row >> 1) & 1) << 2) | (((row >> 3) & 1) << 1))) * 16) + (3 - (i & 3)) * 1024;
    }
#pragma unroll
    for (int i = 0; i < 4; ++i) vfw[i] = (8 * i + (l >> 3)) * 2048 + (((l & 7) ^ (((l >> 4) & 1) << 2)) * 16) + (3 - i) * 1024;
    const char* Ab = uptr((const char*)x + (size_t)mb * 128 * 2048) - 3072;
    const char* Bb = uptr((const char*)wqkv + hp * 128) - 3072;
    const unsigned mba = ldsb + lw * 8192, mbb = ldsb + 16384 + lw * 12288;
#define GL4(v, sb, mm, MO) do { asm volatile("s_add_i32 m0, %2, %3\n s_nop 0\n global_load_lds_dwordx4 %0, %1" ::"v"((v)[0]), "s"(sb), "s"(mm), "i"(MO) : "memory", "scc"); \
      GLI((v)[1], sb, 1024); GLI((v)[2], sb, 2048); GLI((v)[3], sb, 3072); } while (0)
    auto issue = [&](int s, int u0, int u1) {
      const int kt = (s + rot) & 15;
      const char* sa = Ab + kt * 128; const char* sb = Bb + kt * (64 * 6144);
      const unsigned so = (s & 3) * STG, ma = mba + so, mq = mbb + so;
#pragma unroll
      for (int u = u0; u < u1; ++u) { if (u < 2) GL4(vf + 4 * u, sa, ma, u * 4096); else GL4(vf + 4 * u, sb, mq, (u - 2) * 4096); }
    };
    int tk = 0;
#define LBAR() do { BST(tk + 1); BAR(); BST(tk + 21); ++tk; } while (0)
    issue(0, 0, 5); issue(1, 0, 5);
    BST(60);
    WAITV(20); BST(0); BAR(); BST(20);
    asm volatile("" : "+v"(ev));
    const unsigned tag = __builtin_amdgcn_readfirstlane(ev) + 1u;
    MKWS(tag);
    issue(2, 0, 5);
    int s0 = 3;
#pragma unroll 1
    for (; s0 < (NOSKIP ? 16 : 8 + ph); ++s0) { issue(s0, 0, 5); BST(tk + 40); WAITV(40); LBAR(); }
#pragma unroll 1
    for (; s0 < 16; s0 += 4) {
      WAITV(20); LBAR();
      issue(s0, 0, 5); issue(s0 + 1, 0, 2); BST(tk + 40); WAITV(28); LBAR();
      issue(s0 + 1, 2, 5);
      if (s0 + 2 >= 16) { WAITV(20); LBAR(); break; }
      issue(s0 + 2, 0, 4); BST(tk + 40); WAITV(36); LBAR();
      issue(s0 + 2, 4, 5);
      if (s0 + 3 >= 16) { WAITV(20); LBAR(); break; }
      issue(s0 + 3, 0, 5); BST(tk + 40); WAITV(40); LBAR();
    }
    int wq = lw;
    auto wo_upto = [&](int lim) {
      _Pragma("unroll 1") for (; wq < lim; wq += 2) { const char* wp = wob - 3072 + HEADAT(wq) * 65536; const unsigned wm = ldsb + wq * 4096; GL4(vfw, wp, wm, L_WO); }
    };
    if (ph == 0 || NOSKIP) { WAITV(20); LBAR(); }
    if (ph == 1 && !NOSKIP) WAITV(0); else { wo_upto(12); WAITV(24); }
    LBAR();
    if (lw == 0) ST64(((u2){tag, NOW() + KD}), l == 0 ? WS_HK + (hp * 16 + mb) * 128 : WS_SZ);
    if (NOSKIP) wo_upto(22); else if (ph != 2) wo_upto(ph ? 14 : 22);
    LBAR();
    wo_upto(32);
    STMP(1);
    const int hd = hp * 2 + lw;
    u4 sel[2];
#pragma unroll
    for (int e = 0; e < 2; ++e) {
      const unsigned v = (r15 >> 2) == g4 ? (((r15 & 3) == 2 * e ? 0x3F80u : 0u) | ((r15 & 3) == 2 * e + 1 ? 0x3F800000u : 0u)) : 0u;
      sel[0][e] = v; sel[0][2 + e] = 0u; sel[1][e] = 0u; sel[1][2 + e] = v;
    }
    u4 pv[16][2], zv0, zv1, pq[8];
    const char* kvb = uptr(wsx + WS_KVP + (size_t)hd * 32768);
    const char* zb = uptr(wsx + WS_ZP + (size_t)hd * 2048);
    const char* hb = uptr(wsx + WS_HK + hp * 2048);
    const unsigned lv16 = l * 16, zoff = r15 * 128 + g4 * 32;
    const unsigned pa = ldsb + L_PQ + lw * 4096 + l * 16, omask = lw ? 0x0Au : 0x05u;
    u2 hk;
    _Pragma("unroll 1") for (unsigned it = 0; it < SPIN; ++it) {
      asm volatile("global_load_dwordx2 %0, %1, %2 sc1\n s_waitcnt vmcnt(0)" : "=v"(hk) : "v"(r15 * 128), "s"(hb) : "memory");
      if (it == 0) lsig(myFL, 1);
      if (__ballot(hk[0] != tag) == 0ull) break;
      SLEEP(2);
    }
    __builtin_amdgcn_s_setprio(3);
    STMP(2);
    const unsigned t0 = NOW();
    unsigned rel = hk[1] - t0; rel = rel < 1000u ? rel : 0u;
    rel = max(rel, (unsigned)__builtin_amdgcn_update_dpp(0, (int)rel, 0xB1, 0xf, 0xf, false));
    rel = max(rel, (unsigned)__builtin_amdgcn_update_dpp(0, (int)rel, 0x4E, 0xf, 0xf, false));
    int gx;
    { const unsigned r0 = __builtin_amdgcn_readlane(rel, 0), r1 = __builtin_amdgcn_readlane(rel, 4), r2 = __builtin_amdgcn_readlane(rel, 8), r3 = __builtin_amdgcn_readlane(rel, 12);
      gx = max(r0, r1) > max(r2, r3) ? (r0 > r1 ? 3 : 2) : (r2 > r3 ? 1 : 0); }
#define GLD(gq) do { _Pragma("unroll") for (int i = 0; i < 4; ++i) { const char* sb = kvb + (((gq) ^ gx) * 4 + (i & 2)) * 2048; \
      asm volatile("global_load_dwordx4 %0, %1, %2 offset:%3 sc1" : "=v"(pv[(gq) * 4 + i][0]) : "v"(lv16), "s"(sb), "i"((i & 1) * 2048) : "memory"); \
      asm volatile("global_load_dwordx4 %0, %1, %2 offset:%3 sc1" : "=v"(pv[(gq) * 4 + i][1]) : "v"(lv16), "s"(sb), "i"((i & 1) * 2048 + 1024) : "memory"); } } while (0)
#define ZLD() do { asm volatile("global_load_dwordx4 %0, %1, %2 sc1" : "=v"(zv0) : "v"(zoff), "s"(zb) : "memory"); \
      asm volatile("global_load_dwordx4 %0, %1, %2 offset:16 sc1" : "=v"(zv1) : "v"(zoff), "s"(zb) : "memory"); } while (0)
#pragma unroll
    for (int gq = 0; gq < 4; ++gq) {
      const unsigned rq = __builtin_amdgcn_readlane(rel, 4 * (gq ^ gx));
      _Pragma("unroll 1") for (unsigned it = 0; it < SPIN; ++it) if (NOW() - t0 >= rq) break;
      if (gq == 3) ZLD();
      GLD(gq);
    }
    ST64(((u2){tag, NOW() + OD}), l == 0 ? WS_OH + (mb * 32 + hd) * 128 : WS_SZ);
    STMP(5);
    lwait(adFL, omask, 2);
#pragma unroll
    for (int i = 0; i < 8; ++i) RD128(pq[i], pa, (i >> 2) * 8192 + (i & 3) * 1024);
#pragma unroll
    for (int gq = 0; gq < 4; ++gq) {
      SBAR(); WAITV(gq == 0 ? 27 : gq == 1 ? 19 : gq == 2 ? 11 : 1);
      _Pragma("unroll 1") for (unsigned it = 0; it < SPIN; ++it) {
#pragma unroll
        for (int i = 0; i < 4; ++i) asm volatile("" : "+v"(pv[gq * 4 + i][0]), "+v"(pv[gq * 4 + i][1]));
        unsigned m = umax3(pv[gq * 4][0][0], pv[gq * 4][1][0], pv[gq * 4 + 1][0][0]);
        m = umax3(m, pv[gq * 4 + 1][1][0], pv[gq * 4 + 2][0][0]);
        m = umax3(m, pv[gq * 4 + 2][1][0], pv[gq * 4 + 3][0][0]);
        m = max(m, pv[gq * 4 + 3][1][0]);
        if (gq == 3) { asm volatile("" : "+v"(zv0), "+v"(zv1)); m = umax3(m, zv0[0], zv1[0]); }
        if (__ballot(m == POIS) == 0ull) break;
        RETRY();
        SLEEP(2);
        if (gq == 3) ZLD();
        GLD(gq);
        WAITV(0);
      }
      f4 ga[4];
#pragma unroll
      for (int i = 0; i < 4; ++i)
#pragma unroll
        for (int t = 0; t < 4; ++t) ga[t] = MFMA16(S8(sel[t & 1]), S8(pv[gq * 4 + i][t >> 1]), i ? ga[t] : (f4)(0.f));
#pragma unroll
      for (int t = 0; t < 4; ++t) pv[gq * 4 + (t >> 1)][t & 1] = __builtin_bit_cast(u4, ga[t]);
    }
    STMP(3);
    WAITL(0);
#pragma unroll
    for (int i = 0; i < 8; i += 2) asm volatile("" : "+v"(pq[i]), "+v"(pq[i + 1]));
    u4 kA[2], zA;
    {
      float z[8];
#pragma unroll
      for (int e = 0; e < 4; ++e) { z[e] = __uint_as_float(zv0[e]); z[4 + e] = __uint_as_float(zv1[e]); }
#define ZR(n) "v_add_f32_dpp %0, %0, %0 row_ror:" #n " row_mask:0xf bank_mask:0xf\n v_add_f32_dpp %1, %1, %1 row_ror:" #n " row_mask:0xf bank_mask:0xf\n" \
              "v_add_f32_dpp %2, %2, %2 row_ror:" #n " row_mask:0xf bank_mask:0xf\n v_add_f32_dpp %3, %3, %3 row_ror:" #n " row_mask:0xf bank_mask:0xf\n" \
              "v_add_f32_dpp %4, %4, %4 row_ror:" #n " row_mask:0xf bank_mask:0xf\n v_add_f32_dpp %5, %5, %5 row_ror:" #n " row_mask:0xf bank_mask:0xf\n" \
              "v_add_f32_dpp %6, %6, %6 row_ror:" #n " row_mask:0xf bank_mask:0xf\n v_add_f32_dpp %7, %7, %7 row_ror:" #n " row_mask:0xf bank_mask:0xf\n"
      asm volatile("s_nop 1\n" ZR(8) ZR(4) ZR(2) ZR(1) : "+v"(z[0]), "+v"(z[1]), "+v"(z[2]), "+v"(z[3]), "+v"(z[4]), "+v"(z[5]), "+v"(z[6]), "+v"(z[7]));
#pragma unroll
      for (int e = 0; e < 4; ++e) zA[e] = pk2(z[2 * e], z[2 * e + 1]);
    }
    float inv[8];
#pragma unroll
    for (int i = 0; i < 8; ++i) { f4 dn = MFMA16(S8(zA), S8(pq[i]), (f4)(0.f)); inv[i] = dn[0]; }
#pragma unroll
    for (int t = 0; t < 4; ++t) {
#define PF(gq) __builtin_bit_cast(f4, pv[(gq) * 4 + (t >> 1)][t & 1])
      f4 s = (PF(0) + PF(1)) + (PF(2) + PF(3));
      kA[t >> 1][(t & 1) * 2] = pk2(s[0], s[1]); kA[t >> 1][(t & 1) * 2 + 1] = pk2(s[2], s[3]);
    }
    f4 o0[8], o1[8];
#pragma unroll
    for (int i = 0; i < 8; ++i) {
      o0[i] = MFMA16(S8(kA[0]), S8(pq[i]), (f4)(0.f)); o1[i] = MFMA16(S8(kA[1]), S8(pq[i]), (f4)(0.f));
      inv[i] = __builtin_amdgcn_rcpf(inv[i] + 1e-6f);
    }
#pragma unroll
    for (int i = 0; i < 8; ++i) {
      o0[i] *= inv[i]; o1[i] *= inv[i];
      u4 o; o[0] = min(pk2(o0[i][0], o0[i][1]), POIS - 1u); o[1] = pk2(o0[i][2], o0[i][3]); o[2] = pk2(o1[i][0], o1[i][1]); o[3] = pk2(o1[i][2], o1[i][3]);
      ST128(o, WS_OF + (mb * 32 + hd) * 8192 + (i >> 1) * 2048 + (i & 1) * 256 + g4 * 512 + r15 * 16);
    }
    lsig(myFL, 2);
    STMP(4);
    STFLUSH();
    asm volatile(".Lend_l:");
  }

}

extern "C" void kernel_launch(void* const* d_in, const int* in_sizes, int n_in, void* d_out, int out_size,
                              void* d_ws, size_t ws_size, hipStream_t stream) {
  (void)hipFuncSetAttribute((const void*)fused, hipFuncAttributeMaxDynamicSharedMemorySize, LDS_SZ);
  fused<<<256, 512, LDS_SZ, stream>>>((const u16*)d_in[0], (const u16*)d_in[1], (const u16*)d_in[2], (const u16*)d_in[4],
                                      (const u16*)d_in[5], (const u16*)d_in[6], (const u16*)d_in[7], (char*)d_ws, (u16*)d_out);
}
```

```cpp
#include <hip/hip_runtime.h>
#include <stdint.h>

typedef unsigned short u16;
typedef short s4 __attribute__((ext_vector_type(4)));
typedef short s8 __attribute__((ext_vector_type(8)));
typedef float f4 __attribute__((ext_vector_type(4)));
typedef float f16v __attribute__((ext_vector_type(16)));
typedef float f2 __attribute__((ext_vector_type(2)));
typedef unsigned u4 __attribute__((ext_vector_type(4)));
typedef unsigned u2 __attribute__((ext_vector_type(2)));
typedef __bf16 bf2 __attribute__((ext_vector_type(2)));
#define LAS __attribute__((address_space(3)))
#define DEV __device__ __forceinline__

constexpr int D = 1024, F = 3584;
#define NOMOE 0
#define KD 235
#define OD 190
#define OHP 1
#define FS 128
#define GSL 8
#define YSL 16
#define TP1 0
#define TP2 3
#define TLBW 0
#define FULL 1
#define HPRIO 0
#define OWN 1
#define NOPOIS 0
#define CAP 7
#define RD 7
#define DYN 1
#define JX 0
#define NOSKIP 0
#define GRP 8
#define ROT 0

constexpr unsigned WS_E = 0;
constexpr unsigned WS_YF = 524288;
constexpr unsigned WS_CAND = 12288;
constexpr unsigned WS_GF = 589824;
constexpr unsigned WS_RG = 49152;
constexpr unsigned WS_ST = 65536;
constexpr unsigned WS_HK = 98304;
constexpr unsigned WS_ZP = 131072;
constexpr unsigned WS_CS = 196608;
constexpr unsigned WS_OH = 458752;
constexpr unsigned WS_KVP = 1u << 20;
constexpr unsigned WS_GP = 2u << 20;
constexpr unsigned WS_YP = 3u << 20;
constexpr unsigned WS_OF = 5u << 20;
constexpr unsigned WS_SZ = 9u << 20;
#define NR 4
#define RGO(tag) ((64u << 20) + ((tag) & (NR - 1u)) * (10u << 20))
#define MKWS(tag) char* const wsx = ws + RGO(tag); const __amdgpu_buffer_rsrc_t wsr = __builtin_amdgcn_make_buffer_rsrc(wsx, 0, WS_SZ, 0x00020000)

constexpr int STG = 40960;
constexpr int L_KVX = 0;
constexpr int L_PQ = 12288;
constexpr int L_M = 28672, L_RS = L_M + 512, L_CS = L_RS + 512, L_SEL = L_CS + 768, L_FL = L_SEL + 64;
constexpr int L_WO = 32768;
constexpr int LDS_SZ = 163840;
constexpr unsigned POIS = 0xFFFFFFFFu;

DEV unsigned pk2(float a, float b) { bf2 v; v[0] = (__bf16)a; v[1] = (__bf16)b; return __builtin_bit_cast(unsigned, v); }
DEV float lo(unsigned u) { return __uint_as_float(u << 16); }
DEV float hi(unsigned u) { return __uint_as_float(u & 0xFFFF0000u); }
DEV float rbf(float a) { return lo(pk2(a, a)); }
DEV float dot2(unsigned a, unsigned b, float c) { return __builtin_amdgcn_fdot2_f32_bf16(__builtin_bit_cast(bf2, a), __builtin_bit_cast(bf2, b), c, false); }
#define RD128(dst, addr, OFF) asm volatile("ds_read_b128 %0, %1 offset:%2" : "=v"(dst) : "v"(addr), "i"(OFF) : "memory")
#define RDTR(dst, addr, OFF) asm volatile("ds_read_b64_tr_b16 %0, %1 offset:%2" : "=v"(dst) : "v"(addr), "i"(OFF) : "memory")
#define SBAR() __builtin_amdgcn_sched_barrier(0)
#define CAT(x, y) __builtin_shufflevector(x, y, 0, 1, 2, 3, 4, 5, 6, 7)
#define WAITV(n) asm volatile("s_waitcnt vmcnt(%0)" ::"i"(n) : "memory")
#define WAITL(n) asm volatile("s_waitcnt lgkmcnt(%0)" ::"i"(n) : "memory")
#define MEMB() asm volatile("" ::: "memory")
#define SLEEP(n) asm volatile("s_sleep %0" ::"i"(n) : "memory")
#define BAR() asm volatile("s_barrier" ::: "memory")
#define GL0(vo, sb, m0v) asm volatile("s_mov_b32 m0, %2\n s_nop 0\n global_load_lds_dwordx4 %0, %1" ::"v"(vo), "s"(sb), "s"(m0v) : "memory")
#define GLI(vo, sb, OFF) asm volatile("global_load_lds_dwordx4 %0, %1 offset:%2" ::"v"(vo), "s"(sb), "i"(OFF) : "memory")
#define MFMA16(a, b, c) __builtin_amdgcn_mfma_f32_16x16x32_bf16(a, b, c, 0, 0, 0)
#define S8(x) __builtin_bit_cast(s8, x)
#define LD128(off) __builtin_amdgcn_raw_buffer_load_b128(wsr, (off), 0, 16)
#define LD64(off) __builtin_amdgcn_raw_buffer_load_b64(wsr, (off), 0, 16)
#define ST128(v, off) __builtin_amdgcn_raw_buffer_store_b128((v), wsr, (off), 0, 16)
#define ST64(v, off) __builtin_amdgcn_raw_buffer_store_b64((v), wsr, (off), 0, 16)
#define OFA(i) (WS_OF + ((mb * 32 + hd) * 8 + (i)) * 1024 + l * 16)
#define NOW() ((unsigned)__builtin_amdgcn_s_memrealtime())
constexpr unsigned SPIN = 1u << 21;
DEV const char* uptr(const char* p) {
  unsigned long long v = (unsigned long long)p;
  unsigned lo_ = __builtin_amdgcn_readfirstlane((unsigned)v), hi_ = __builtin_amdgcn_readfirstlane((unsigned)(v >> 32));
  return (const char*)(((unsigned long long)hi_ << 32) | lo_);
}
DEV void lsig(unsigned addr, unsigned val) { asm volatile("s_waitcnt lgkmcnt(0)\n ds_write_b32 %0, %1" ::"v"(addr), "v"(val) : "memory"); }
template <int SL = 0> DEV void lwait(unsigned addr, unsigned mask, unsigned target) {
  _Pragma("unroll 1") for (unsigned it = 0; it < SPIN; ++it) {
    unsigned v;
    asm volatile("ds_read_b32 %0, %1\n s_waitcnt lgkmcnt(0)" : "=v"(v) : "v"(addr) : "memory");
    if (((unsigned)__ballot(v < target) & mask) == 0u) break;
    if (SL) SLEEP(SL);
  }
}
DEV unsigned umax3(unsigned a, unsigned b, unsigned c) { return max(max(a, b), c); }
template <int C> DEV float dppf(float v) { return __int_as_float(__builtin_amdgcn_update_dpp(0, __float_as_int(v), C, 0xf, 0xf, false)); }
DEV unsigned wave_max(unsigned v) {
  v = max(v, (unsigned)__builtin_amdgcn_update_dpp(0, (int)v, 0x111, 0xf, 0xf, false));
  v = max(v, (unsigned)__builtin_amdgcn_update_dpp(0, (int)v, 0x112, 0xf, 0xf, false));
  v = max(v, (unsigned)__builtin_amdgcn_update_dpp(0, (int)v, 0x114, 0xf, 0xf, false));
  v = max(v, (unsigned)__builtin_amdgcn_update_dpp(0, (int)v, 0x118, 0xf, 0xf, false));
  v = max(v, (unsigned)__builtin_amdgcn_update_dpp(0, (int)v, 0x142, 0xa, 0xf, false));
  v = max(v, (unsigned)__builtin_amdgcn_update_dpp(0, (int)v, 0x143, 0xc, 0xf, false));
  return (unsigned)__builtin_amdgcn_readlane((int)v, 63);
}
DEV unsigned phi2(f2 u, f2 rs) {
  f2 t = u * rs;
  f2 a = t * 1.44269504f, p = t + 1.f, r;
  r[0] = __builtin_amdgcn_fmed3f(__builtin_amdgcn_exp2f(a[0]), p[0], 1.f);
  r[1] = __builtin_amdgcn_fmed3f(__builtin_amdgcn_exp2f(a[1]), p[1], 1.f);
  r += 1e-6f;
  return pk2(r[0], r[1]);
}

#define STDECL
#define STMP(i)
#define BST(i)
#define STFLUSH()
#define RETRY()

DEV float half_sum(float v) {
  asm volatile("s_nop 1\n v_add_f32_dpp %0, %0, %0 row_ror:8 row_mask:0xf bank_mask:0xf\n s_nop 1\n v_add_f32_dpp %0, %0, %0 row_ror:4 row_mask:0xf bank_mask:0xf\n"
               "s_nop 1\n v_add_f32_dpp %0, %0, %0 row_ror:2 row_mask:0xf bank_mask:0xf\n s_nop 1\n v_add_f32_dpp %0, %0, %0 row_ror:1 row_mask:0xf bank_mask:0xf\n s_nop 1" : "+v"(v));
  auto r = __builtin_amdgcn_permlane16_swap(__float_as_uint(v), __float_as_uint(v), false, false);
  return __uint_as_float(r[0]) + __uint_as_float(r[1]);
}

constexpr int PP = 8 - RD < 1 ? 1 : 8 - RD;
#define ISPP(pos) ((pos) < 24 && ((pos) & 7) == PP)
constexpr int oyoung(int pos) {
  int n = 0;
  for (int i = pos + 1; i < pos + RD - 1 + (pos == 0) && i < 32; ++i) n += 2 + (ISPP(i) ? 1 : 0);
  return n;
}
struct FQ { s8 a[4]; s4 b[12]; };
template <bool B> struct BC { static constexpr bool v = B; };

__global__ __launch_bounds__(512) void fused(const u16* __restrict__ x, const u16* __restrict__ wqkv, const u16* __restrict__ wo,
                                             const u16* __restrict__ wrt, const u16* __restrict__ wg, const u16* __restrict__ wu,
                                             const u16* __restrict__ wd, char* __restrict__ ws, u16* __restrict__ out) {
  extern __shared__ __attribute__((aligned(16))) char lds[];
  asm volatile(".Lcode_beg:");
  const int w = __builtin_amdgcn_readfirstlane(threadIdx.x >> 6), l = threadIdx.x & 63;
  STDECL;
  STMP(0);
  const int g = blockIdx.x, xcd = g & 7, j = g >> 3, ii = j >> 2;
  unsigned cw = 0;
  if (w == 7) {
    unsigned long long pc; unsigned off, len, l1, l2, l3;
    asm volatile("s_getpc_b64 %0\n.Lcode_here:" : "=s"(pc));
    asm volatile("s_mov_b32 %0, (.Lcode_here - .Lcode_beg)" : "=s"(off));
    asm volatile("s_mov_b32 %0, (.Lend_c - .Lcode_beg)" : "=s"(len));
    asm volatile("s_mov_b32 %0, (.Lend_l - .Lcode_beg)" : "=s"(l1));
    asm volatile("s_mov_b32 %0, (.Lend_h - .Lcode_beg)" : "=s"(l2));
    asm volatile("s_mov_b32 %0, (.Lend_s - .Lcode_beg)" : "=s"(l3));
    len = max(max(len, l3), max(l1, l2));
    const unsigned o = min((unsigned)(l * 32 + j) * 64u, len - 4u);
    asm volatile("global_load_dword %0, %1, %2" : "+v"(cw) : "v"(o), "s"(pc - off) : "memory");
  }
  const int mb = (xcd & 1) * 8 + ii, hp = (xcd >> 1) * 4 + ((j + JX) & 3);
#define JR 0
  const int rot = 2 * xcd + 4 * ((ii + (JR == 1 ? j : JR == 2 ? (j & 2) : 0)) & 3);
  const int nb = hp;
  const int hr = ROT * (GRP / 4) * (j & 3);
#define HEADAT(p) (((p) & ~(GRP - 1)) | (((p) + hr) & (GRP - 1)))
  const int ph = ii >= 6 ? ii - 6 : ii >= 3 ? ii - 3 : ii;
  const unsigned ldsb = (unsigned)(size_t)(LAS char*)lds;
  const unsigned adFL = ldsb + L_FL + 4 * (l & 7), myFL = ldsb + L_FL + 4 * w;
  const __amdgpu_buffer_rsrc_t wse = __builtin_amdgcn_make_buffer_rsrc(ws, 0, 64, 0x00020000);
  const int r15 = l & 15, g4 = l >> 4, q = (l & 15) >> 2, p = l & 3;
  const char* wob = uptr((const char*)wo + nb * 128);

  if (w != 4 && w != 5) {
  const unsigned ev0 = __builtin_amdgcn_raw_buffer_load_b32(wse, WS_E, 0, 16);
  if (w >= 6) {
    const int mw = w - 6;
    const unsigned tag = __builtin_amdgcn_readfirstlane(ev0) + 1u, tag2 = tag * 0x9E3779B1u;
    MKWS(tag);
    auto helper = [&](auto SP) {
    constexpr bool spare = decltype(SP)::v, moe = !spare && !NOMOE;
    int nbar = 0;
#define TICK() do { BST(nbar); SBAR(); BAR(); SBAR(); ++nbar; } while (0)
#define DTICK(lim) do { if (nbar < (lim)) TICK(); else SLEEP(2); } while (0)
    const int hd = hp * 2 + mw;
    const u4 pz = (u4)(POIS);
    const int chunk = g >> 2, ksp = g & 3;
    const int np = l & 7, kset = (l >> 3) & 1;
    u4 gw[4][8], dw[2][8];
    const __amdgpu_buffer_rsrc_t wgr = __builtin_amdgcn_make_buffer_rsrc((void*)(mw ? wu : wg), 0, D * F * 2, 0x00020000);
    const __amdgpu_buffer_rsrc_t wdr = __builtin_amdgcn_make_buffer_rsrc((void*)wd, 0, D * F * 2, 0x00020000);
    const __amdgpu_buffer_rsrc_t wor = __builtin_amdgcn_make_buffer_rsrc((void*)wo, 0, D * D * 2, 0x00020000);
    const unsigned gvo = ((256 * ksp + 32 * kset + 8 * g4) * F + 64 * chunk + 8 * np) * 2;
    const unsigned dvo = ((64 * chunk + 8 * g4) * D + 256 * ksp + 128 * mw + 8 * r15) * 2;
    auto burst = [&](int k) {
      if (moe) {
#pragma unroll
        for (int i = 0; i < 16; ++i) {
          if (k < 2) gw[(2 * k + (i >> 3)) & 3][i & 7] = __builtin_amdgcn_raw_buffer_load_b128(wgr, gvo, (128 * k + 64 * (i >> 3) + (i & 7)) * F * 2, 2);
          else dw[i >> 3][i & 7] = __builtin_amdgcn_raw_buffer_load_b128(wdr, dvo, (32 * (i >> 3) + (i & 7)) * D * 2, 2);
        }
      }
    };
    auto transp = [&](u4(&v)[8]) {
      if (moe) {
        u4 f[8];
#pragma unroll
        for (int c = 0; c < 8; ++c)
#pragma unroll
          for (int e = 0; e < 4; ++e) {
            const unsigned ev = v[2 * e][c >> 1], od = v[2 * e + 1][c >> 1];
            f[c][e] = (c & 1) ? __builtin_amdgcn_perm(od, ev, 0x07060302u) : __builtin_amdgcn_perm(od, ev, 0x05040100u);
          }
#pragma unroll
        for (int c = 0; c < 8; ++c) { v[c] = f[c]; asm volatile("" : "+v"(v[c])); }
      }
    };
    auto pois = [&](int i) {
      if (i < 2) ST128(pz, WS_KVP + (hd * 16 + mb) * 2048 + i * 1024 + l * 16);
      else if (i == 2) ST128(pz, WS_ZP + (hd * 16 + mb) * 128 + (l & 7) * 16);
      else if (!NOPOIS) ST128(pz, OFA(i - 3));
    };
    unsigned wt0, wt1, wt2 = 0, wt3 = 0;
    auto touch = [&]() {
      const unsigned to = (ii * 128 + mw * 64 + (l >> 1)) * 2048 + nb * 128 + (l & 1) * 64;
      wt0 = __builtin_amdgcn_raw_buffer_load_b32(wor, to, 0, 0); wt1 = __builtin_amdgcn_raw_buffer_load_b32(wor, to, 65536, 0);
      if (TLBW) {
        const __amdgpu_buffer_rsrc_t outr = __builtin_amdgcn_make_buffer_rsrc(out, 0, 2048 * D * 2, 0x00020000);
        wt2 = __builtin_amdgcn_raw_buffer_load_b32(outr, (mb * 128 + mw * 64 + (l & 3) * 16) * 2048 + nb * 128, 0, 0);
        wt3 = __builtin_amdgcn_raw_buffer_load_b32(wsr, (l & 1) ? WS_GP + g * 4096 + mw * 2048 : WS_YP + (g >> 2) * 32768 + (l & 2) * 8192, 0, 0);
      }
    };
    auto colsum = [&](int h) {
      const unsigned bo = ldsb + (1 + h) * STG + 16384 + (8 * g4 + q) * 128 + (p & 1) * 8;
      const int sw = ((q >> 1) << 2) | ((g4 & 1) << 1);
      const unsigned b0 = bo + (((mw * 4 + (p >> 1)) ^ sw) * 16), b1 = bo + (((mw * 4 + 2 + (p >> 1)) ^ sw) * 16);
      const u4 ones = (u4)(0x3F803F80u);
      f4 c[6]; s4 b[24];
#pragma unroll
      for (int n = 0; n < 12; ++n) { RDTR(b[2 * n], (n & 1) ? b1 : b0, ((n % 6) >> 1) * 8192 + (n / 6) * 4096); RDTR(b[2 * n + 1], (n & 1) ? b1 : b0, ((n % 6) >> 1) * 8192 + (n / 6) * 4096 + 512); }
      WAITL(0);
      TICK();
#pragma unroll
      for (int n = 0; n < 12; ++n) c[n % 6] = MFMA16(S8(ones), CAT(b[2 * n], b[2 * n + 1]), n >= 6 ? c[n % 6] : (f4)(0.f));
      const u4 csg = (u4){__float_as_uint(g4 ? c[3][0] : c[0][0]), __float_as_uint(g4 ? c[4][0] : c[1][0]), __float_as_uint(g4 ? c[5][0] : c[2][0]), tag};
      ST128(csg, l < 32 ? WS_CS + (hd * 16 + mb) * 512 + l * 16 : WS_SZ);
    };
    u4 stv, cv[8], cd; float cs[3];
    auto gload = [&]() {
      stv = LD128(WS_ST + (mb * 128 + mw * 64 + l) * 16);
      cd = LD128(WS_CAND + (g & 31) * 128 + (l & 7) * 16);
#pragma unroll
      for (int i = 0; i < 8; ++i) cv[i] = LD128(WS_CS + hd * 8192 + i * 1024 + l * 16);
    };
    auto gbad = [&]() {
      unsigned bad = (stv[1] ^ tag) | (stv[3] ^ tag) | (cd[1] ^ tag) | (cd[3] ^ tag);
#pragma unroll
      for (int i = 0; i < 8; ++i) bad |= cv[i][3] ^ tag;
      return __ballot(bad != 0u) != 0ull;
    };
    int idx;
    auto gfin = [&]() {
      if (gbad()) { RETRY(); _Pragma("unroll 1") for (unsigned it = 0; it < SPIN; ++it) { SLEEP(2); gload(); if (!gbad()) break; } }
#pragma unroll
      for (int e = 0; e < 3; ++e) {
        float a = __uint_as_float(cv[0][e]);
#pragma unroll
        for (int i = 1; i < 8; ++i) a += __uint_as_float(cv[i][e]);
        auto r = __builtin_amdgcn_permlane32_swap(__float_as_uint(a), __float_as_uint(a), false, false);
        cs[e] = __uint_as_float(r[0]) + __uint_as_float(r[1]);
      }
      asm volatile("" : "+v"(cs[0]), "+v"(cs[1]), "+v"(cs[2]) : "v"(wt0), "v"(wt1), "v"(wt2), "v"(wt3));
      cd[0] = max(cd[0], cd[2]);
      idx = (int)(cd[0] & 2047u);
    };
    auto lpub = [&]() {
      *(LAS float*)(lds + L_M + (mw * 64 + l) * 4) = __uint_as_float(stv[0]);
      *(LAS float*)(lds + L_RS + (mw * 64 + l) * 4) = __uint_as_float(stv[2]);
      if (l < 32) {
#pragma unroll
        for (int e = 0; e < 3; ++e) *(LAS float*)(lds + L_CS + mw * 384 + ((3 * g4 + e) * 16 + r15) * 4) = cs[e];
      }
      if (mw == 0 && l < 8) { *(LAS int*)(lds + L_SEL + l * 4) = idx; *(LAS unsigned*)(lds + L_FL + 4 * l) = 0u; }
      WAITL(0);
    };
    u4 xs[4];
    asm volatile("" ::"s"(wg), "s"(wu), "s"(wd), "v"(cw));
    if (HPRIO) __builtin_amdgcn_s_setprio(HPRIO);
    TICK();
    if (spare) {
#pragma unroll
      for (int i = 0; i < 6; ++i) pois(i);
      TICK();
#pragma unroll
      for (int i = 6; i < 11; ++i) pois(i);
      TICK();
      u4 kk[8];
      auto kload = [&]() {
#pragma unroll
        for (int i = 0; i < 8; ++i) kk[i] = LD128(WS_RG + mw * 8192 + i * 1024 + l * 16);
      };
      kload();
      colsum(1);
      _Pragma("unroll 1") for (unsigned it = 0; it < SPIN; ++it) {
        while (nbar < 10 && (__builtin_amdgcn_s_getreg(7 | (31 << 11)) & 0xC000000Eu)) TICK();
        unsigned bad = 0;
#pragma unroll
        for (int i = 0; i < 8; ++i) bad |= (kk[i][1] ^ tag) | (kk[i][3] ^ tag);
        if (__ballot(bad != 0u) == 0ull) break;
        kload();
        DTICK(10);
      }
      __builtin_amdgcn_s_setprio(3);
      unsigned base = 0, mine8 = 0;
#pragma unroll 1
      for (int it = 0; it < 8; ++it) {
        unsigned m = 0;
#pragma unroll
        for (int i = 0; i < 8; ++i) m = umax3(m, kk[i][0], kk[i][2]);
        m = wave_max(m);
#pragma unroll
        for (int i = 0; i < 8; ++i) { kk[i][0] -= m; kk[i][2] -= m; }
        base += m;
        mine8 = l == it ? base : mine8;
        if ((it == 1 || it == 4) && nbar < 10) TICK();
      }
      if (l < 8) ST64(((u2){mine8, tag}), WS_CAND + (g - 224) * 128 + (mw ? 7 - l : l) * 16 + mw * 8);
      __builtin_amdgcn_s_setprio(HPRIO);
      STMP(2);
      touch();
      while (nbar < 10) TICK();
      gload();
      TICK(); TICK();
      WAITV(0);
      gfin();
      STMP(3);
      TICK(); TICK();
      lpub();
    } else {
      TICK();
      if (ii >> 2) TICK();
      colsum(ii >> 2);
      if (!(ii >> 2)) TICK();
      STMP(1);
      pois(0); pois(1); pois(2);
      TICK();
      pois(3); pois(4);
      TICK();
      for (int i = 0; i < ph; ++i) TICK();
      burst(0); touch();
      TICK();
      pois(5); pois(6);
      TICK();
      pois(7); pois(8);
      transp(gw[0]);
      TICK();
      pois(9); pois(10);
      gload();
      transp(gw[1]);
      TICK();
      burst(1);
      TICK();
      WAITV(moe ? 16 : 0);
      gfin();
      u4 sv;
      if (moe) {
        const u16* xp = x + (size_t)idx * D + 256 * ksp + 32 * kset + 8 * g4;
#pragma unroll
        for (int it = 0; it < 4; ++it) xs[it] = *(const u4*)(xp + 64 * it);
        sv = LD128(WS_ST + idx * 16);
      }
      STMP(3);
      TICK();
      transp(gw[2]);
      TICK();
      transp(gw[3]);
      TICK();
      lpub();
      if (moe) {
        if (__ballot(sv[1] != tag || sv[3] != tag) != 0ull) {
          _Pragma("unroll 1") for (unsigned it = 0; it < SPIN; ++it) { SLEEP(1); sv = LD128(WS_ST + idx * 16); if (__ballot(sv[1] != tag || sv[3] != tag) == 0ull) break; }
        }
        asm volatile("" : "+v"(xs[0]), "+v"(xs[1]), "+v"(xs[2]), "+v"(xs[3]));
      }
      burst(2);
      TICK();
      if (moe) {
        const float m = __uint_as_float(sv[0]), rs = __uint_as_float(sv[2]);
#pragma unroll
        for (int it = 0; it < 4; ++it) {
#pragma unroll
          for (int e = 0; e < 4; ++e) { const unsigned d = pk2(lo(xs[it][e]) - m, hi(xs[it][e]) - m); xs[it][e] = pk2(lo(d) * rs, hi(d) * rs); }
          asm volatile("" : "+v"(xs[it]));
        }
      }
    }
    while (nbar < 17) TICK();
    __builtin_amdgcn_s_setprio(TP1);
    STMP(4);
    float sg;
    {
      const unsigned mono = cd[0] >> 16, rb = (mono & 0x8000u) ? (mono & 0x7FFFu) : (~mono & 0xFFFFu);
      sg = rbf(1.f / (1.f + __expf(-lo(rb))));
    }
    if (moe) {
      STMP(5);
      f4 acc[8];
#pragma unroll
      for (int it = 0; it < 4; ++it)
#pragma unroll
        for (int c = 0; c < 8; ++c) acc[c] = MFMA16(S8(gw[it][c]), S8(xs[it]), it ? acc[c] : (f4)(0.f));
#pragma unroll
      for (int i = 0; i < 4; ++i) {
        u4 s;
#pragma unroll
        for (int c = 0; c < 4; ++c) {
          auto r = __builtin_amdgcn_permlane32_swap(__float_as_uint(acc[c][i]), __float_as_uint(acc[c + 4][i]), false, false);
          s[c] = __float_as_uint(__uint_as_float(r[0]) + dppf<0x128>(__uint_as_float(r[1])));
        }
        if (r15 < 8) ST128(s, WS_GP + ((chunk * 4 + ksp) * 2 + mw) * 2048 + (g4 >> 1) * 1024 + (4 * (g4 & 1) + i) * 128 + r15 * 16);
      }
      WAITV(0);
      if (l == 0) ST64(((u2){tag, tag2}), WS_GF + chunk * 1024 + (ksp * 2 + mw) * FS);
      STMP(6);
      transp(dw[0]); transp(dw[1]);
      _Pragma("unroll 1") for (unsigned it = 0; it < SPIN; ++it) {
        u2 f = LD64(WS_GF + chunk * 1024 + (l & 7) * FS);
        if (__ballot(f[0] != tag || f[1] != tag2) == 0ull) break;
        SLEEP(GSL);
      }
      STMP(7);
      __builtin_amdgcn_s_setprio(TP2);
      u4 ha[2];
      {
        u4 pg[4][4];
#pragma unroll
        for (int ks = 0; ks < 4; ++ks)
#pragma unroll
          for (int t = 0; t < 4; ++t)
            pg[ks][t] = LD128(WS_GP + ((chunk * 4 + ks) * 4 + t) * 1024 + (4 * kset + g4) * 128 + np * 16);
        f4 gq[4];
#pragma unroll
        for (int t = 0; t < 4; ++t)
          gq[t] = (__builtin_bit_cast(f4, pg[0][t]) + __builtin_bit_cast(f4, pg[1][t])) + (__builtin_bit_cast(f4, pg[2][t]) + __builtin_bit_cast(f4, pg[3][t]));
        float hv[8];
#pragma unroll
        for (int e = 0; e < 8; ++e) {
          const float gg = rbf(gq[e >> 2][e & 3]), uu = rbf(gq[2 + (e >> 2)][e & 3]);
          hv[e] = rbf(gg / (1.f + __expf(-gg))) * uu;
        }
#pragma unroll
        for (int e = 0; e < 4; ++e) { ha[0][e] = pk2(hv[2 * e], hv[2 * e + 1]); ha[1][e] = (unsigned)__builtin_amdgcn_update_dpp(0, (int)ha[0][e], 0x128, 0xf, 0xf, false); }
      }
#pragma unroll
      for (int it = 0; it < 2; ++it)
#pragma unroll
        for (int c = 0; c < 8; ++c) acc[c] = MFMA16(S8(ha[it]), S8(dw[it][c]), it ? acc[c] : (f4)(0.f));
      if (g4 < 2) {
#pragma unroll
        for (int i = 0; i < 4; ++i) {
          const unsigned yo = WS_YP + ((chunk * 8 + 4 * g4 + i) * D + 256 * ksp + 128 * mw + 4 * r15) * 4;
          ST128(__builtin_bit_cast(u4, ((f4){acc[0][i], acc[1][i], acc[2][i], acc[3][i]})), yo);
          ST128(__builtin_bit_cast(u4, ((f4){acc[4][i], acc[5][i], acc[6][i], acc[7][i]})), yo + 256);
        }
      }
      WAITV(0);
      if (l == 0) ST64(((u2){tag, tag2}), WS_YF + (ksp * 2 + mw) * 8192 + chunk * FS);
      STMP(8);
    }
    unsigned mine = (unsigned)__ballot(!NOMOE && l < 8 && (idx >> 7) == mb && (l & 1) == mw);
    if (mine) {
      if (spare) lwait<4>(adFL, 0x30u, 2);
      _Pragma("unroll 1") for (unsigned it = 0; it < SPIN; ++it) {
        u2 f = LD64(WS_YF + (nb >> 1) * 8192 + (l < 56 ? l : 0) * FS);
        if (__ballot(f[0] != tag || f[1] != tag2) == 0ull) break;
        SLEEP(YSL);
      }
      lwait<1>(adFL, 0x0Fu, 2);
      __builtin_amdgcn_s_setprio(TP2);
      while (mine) {
        const int t = __builtin_ctz(mine); mine &= mine - 1;
        u4 yv[14];
#pragma unroll
        for (int i = 0; i < 14; ++i) yv[i] = LD128(WS_YP + (((i * 4 + g4) * 8 + t) * D + (nb >> 1) * 128 + (r15 >> 3) * 64 + (nb & 1) * 32 + (r15 & 7) * 4) * 4);
        f4 y = __builtin_bit_cast(f4, yv[0]);
#pragma unroll
        for (int i = 1; i < 14; ++i) y += __builtin_bit_cast(f4, yv[i]);
        const float gt = __int_as_float(__builtin_amdgcn_readlane(__float_as_int(sg), t));
#pragma unroll
        for (int e = 0; e < 4; ++e) {
          auto r = __builtin_amdgcn_permlane32_swap(__float_as_uint(y[e]), __float_as_uint(y[e]), false, false);
          const float a = __uint_as_float(r[0]) + __uint_as_float(r[1]);
          auto r2 = __builtin_amdgcn_permlane16_swap(__float_as_uint(a), __float_as_uint(a), false, false);
          y[e] = rbf(rbf(__uint_as_float(r2[0]) + __uint_as_float(r2[1])) * gt);
        }
        if (l < 16) *(LAS f4*)(lds + L_KVX + t * 256 + (l & 7) * 32 + (l >> 3) * 16) = y;
      }
    }
    lsig(myFL, 3u);
    STMP(9);
    STFLUSH();
    if (spare) asm volatile(".Lend_s:"); else asm volatile(".Lend_h:");
    };
    if (g >= 224) helper(BC<true>()); else helper(BC<false>());
    return;
  }

  const int wr = w >> 1, wc = w & 1;
  unsigned tag; u2 kg; u4 sg4;
  const int srow = mb * 128 + hp * 8 + 2 * w + (l >> 5);
  {
    const int row = srow;
    u4 xa[4], rw[4];
#pragma unroll
    for (int c = 0; c < 4; ++c) xa[c] = *(const u4*)(x + row * D + (l & 31) * 8 + 256 * c);
#pragma unroll
    for (int c = 0; c < 4; ++c) rw[c] = *(const u4*)(wrt + (l & 31) * 8 + 256 * c);
    float s0 = 0, s1 = 0;
#pragma unroll
    for (int c = 0; c < 4; ++c)
#pragma unroll
      for (int e = 0; e < 4; e += 2) { unsigned x0 = xa[c][e], x1 = xa[c][e + 1]; s0 = dot2(x0, 0x3F803F80u, s0); s1 = dot2(x1, 0x3F803F80u, s1); }
    const float m = half_sum(s0 + s1) * (1.f / D);
    f2 v[16], q2 = (f2)(0.f), q3 = (f2)(0.f);
#pragma unroll
    for (int c = 0; c < 16; ++c) v[c] = (f2){lo(xa[c >> 2][c & 3]), hi(xa[c >> 2][c & 3])} - m;
#pragma unroll
    for (int c = 0; c < 16; c += 2) { q2 += v[c] * v[c]; q3 += v[c + 1] * v[c + 1]; }
    q2 += q3;
    const float mr = rbf(m);
    const float rs = 1.f / rbf(sqrtf(rbf(rbf(half_sum(q2[0] + q2[1]) * (1.f / D)) + 1e-5f)));
    float d0 = 0, d1 = 0;
#pragma unroll
    for (int c = 0; c < 16; c += 2) {
      const f2 e0 = (f2){lo(xa[c >> 2][c & 3]), hi(xa[c >> 2][c & 3])} - mr, e1 = (f2){lo(xa[c >> 2][(c & 3) + 1]), hi(xa[c >> 2][(c & 3) + 1])} - mr;
      const unsigned p0 = pk2(e0[0], e0[1]), p1 = pk2(e1[0], e1[1]);
      const f2 n0 = (f2){lo(p0), hi(p0)} * rs, n1 = (f2){lo(p1), hi(p1)} * rs;
      unsigned c0 = rw[c >> 2][c & 3], c1 = rw[c >> 2][(c & 3) + 1];
      d0 = dot2(pk2(n0[0], n0[1]), c0, d0); d1 = dot2(pk2(n1[0], n1[1]), c1, d1);
    }
    const float d = half_sum(d0 + d1);
    const unsigned rb = pk2(d, d) & 0xFFFFu;
    const unsigned mono = (rb & 0x8000u) ? (~rb & 0xFFFFu) : (rb | 0x8000u);
    tag = __builtin_amdgcn_readfirstlane(ev0) + 1u;
    kg = (u2){(mono << 16) | (unsigned)row, tag};
    sg4 = (u4){__float_as_uint(mr), tag, __float_as_uint(rs), tag};
  }
  MKWS(tag);
  if ((l & 31) == 0) { ST64(kg, WS_RG + srow * 8); ST128(sg4, WS_ST + srow * 16); }
  unsigned aad[2], bad[2];
#pragma unroll
  for (int s = 0; s < 2; ++s) aad[s] = ldsb + (wr * 64 + r15) * 128 + (((4 * s + g4) ^ ((r15 >> 1) & 7)) * 16);
#pragma unroll
  for (int nh = 0; nh < 2; ++nh) bad[nh] = ldsb + 16384 + (8 * g4 + q) * 128 + (((wc * 4 + 2 * nh + (p >> 1)) ^ (((q >> 1) << 2) | ((g4 & 1) << 1))) * 16) + (p & 1) * 8;
  f4 acc[4][6];
#pragma unroll
  for (int m = 0; m < 4; ++m)
#pragma unroll
    for (int n = 0; n < 6; ++n) { acc[m][n] = (f4)(0.f); asm volatile("" : "+v"(acc[m][n])); }
#define MF(m, n, Fq) do { if (n < 2) acc[m][n] = MFMA16(CAT(Fq.b[2 * n], Fq.b[2 * n + 1]), Fq.a[m], acc[m][n]); \
     else acc[m][n] = MFMA16(Fq.a[m], CAT(Fq.b[2 * n], Fq.b[2 * n + 1]), acc[m][n]); SBAR(); } while (0)
#define RB(G, n, vb0, vb1, SS, hf) RDTR(G.b[2 * (n) + (hf)], (((n) & 1) ? vb1 : vb0), ((n) >> 1) * 8192 + (SS) * 4096 + (hf) * 512)
#define RA(G, m, va) RD128(G.a[m], va, (m) * 2048)
#define SUB(Fq, G, va, vb0, vb1, SS, PRE) do { \
    WAITL(13); SBAR(); \
    MF(0, 0, Fq); PRE; RA(G, 0, va); SBAR(); \
    WAITL(11); SBAR(); \
    MF(1, 0, Fq); RB(G, 0, vb0, vb1, SS, 0); SBAR(); MF(2, 0, Fq); RB(G, 0, vb0, vb1, SS, 1); SBAR(); MF(3, 0, Fq); RA(G, 1, va); SBAR(); \
    WAITL(12); SBAR(); \
    MF(0, 1, Fq); RA(G, 2, va); SBAR(); MF(1, 1, Fq); RA(G, 3, va); SBAR(); MF(2, 1, Fq); RB(G, 1, vb0, vb1, SS, 0); SBAR(); MF(3, 1, Fq); RB(G, 1, vb0, vb1, SS, 1); SBAR(); \
    WAITL(8); SBAR(); \
    MF(0, 2, Fq); RB(G, 2, vb0, vb1, SS, 0); SBAR(); MF(1, 2, Fq); RB(G, 2, vb0, vb1, SS, 1); SBAR(); MF(2, 2, Fq); RB(G, 3, vb0, vb1, SS, 0); SBAR(); MF(3, 2, Fq); RB(G, 3, vb0, vb1, SS, 1); SBAR(); \
    MF(0, 3, Fq); RB(G, 4, vb0, vb1, SS, 0); SBAR(); MF(1, 3, Fq); RB(G, 4, vb0, vb1, SS, 1); SBAR(); MF(2, 3, Fq); RB(G, 5, vb0, vb1, SS, 0); SBAR(); MF(3, 3, Fq); RB(G, 5, vb0, vb1, SS, 1); SBAR(); \
    MF(0, 4, Fq); MF(1, 4, Fq); MF(2, 4, Fq); MF(3, 4, Fq); MF(0, 5, Fq); MF(1, 5, Fq); MF(2, 5, Fq); MF(3, 5, Fq); } while (0)
#define KSTEP(MODE) do { \
    if (kt == sx) { const unsigned xo = xad + buf * STG; \
      _Pragma("unroll") for (int t = 0; t < 8; ++t) { const unsigned a_ = xo ^ (t * 16); \
        asm volatile("ds_read_b64 %0, %1" : "+v"(xv[t]) : "v"(a_) : "memory"); } \
      WAITL(0); SBAR(); } \
    const int nbuf = (buf + 1) & 3; \
    const unsigned bo = buf * STG, nbo = nbuf * STG; \
    { unsigned va = aad[1] + bo, vb0 = bad[0] + bo, vb1 = bad[1] + bo; SUB(f0, f1, va, vb0, vb1, 1, ); } \
    if (MODE < 2) { \
      unsigned va = aad[0] + nbo, vb0 = bad[0] + nbo, vb1 = bad[1] + nbo; \
      SUB(f1, f0, va, vb0, vb1, 0, if (MODE == 0) { WAITL(0); BAR(); SBAR(); }); \
    } else { WAITL(0); BAR(); SBAR(); \
      MF(0, 2, f1); MF(1, 2, f1); MF(2, 2, f1); MF(3, 2, f1); MF(0, 3, f1); MF(1, 3, f1); MF(2, 3, f1); MF(3, 3, f1); \
      MF(0, 4, f1); MF(1, 4, f1); MF(2, 4, f1); MF(3, 4, f1); MF(0, 5, f1); MF(1, 5, f1); MF(2, 5, f1); MF(3, 5, f1); \
      MF(0, 0, f1); MF(1, 0, f1); MF(2, 0, f1); MF(3, 0, f1); MF(0, 1, f1); MF(1, 1, f1); MF(2, 1, f1); MF(3, 1, f1); } \
    buf = nbuf; } while (0)
  u2 xv[8];
#pragma unroll
  for (int t = 0; t < 8; ++t) asm volatile("" : "=v"(xv[t]));
  const int sx = (nb - rot) & 15;
  const unsigned xad = ldsb + (w * 32 + (l & 31)) * 128 + ((r15 >> 1) & 7) * 16 + (l >> 5) * 8;
  STMP(1);
  WAITL(0);
  BAR();
  STMP(2);
  WAITL(0);
  FQ f0, f1;
  { unsigned va = aad[0], vb0 = bad[0], vb1 = bad[1];
    RA(f0, 0, va); RB(f0, 0, vb0, vb1, 0, 0); RB(f0, 0, vb0, vb1, 0, 1); RA(f0, 1, va); RA(f0, 2, va); RA(f0, 3, va);
    RB(f0, 1, vb0, vb1, 0, 0); RB(f0, 1, vb0, vb1, 0, 1); RB(f0, 2, vb0, vb1, 0, 0); RB(f0, 2, vb0, vb1, 0, 1); RB(f0, 3, vb0, vb1, 0, 0); RB(f0, 3, vb0, vb1, 0, 1);
    RB(f0, 4, vb0, vb1, 0, 0); RB(f0, 4, vb0, vb1, 0, 1); RB(f0, 5, vb0, vb1, 0, 0); RB(f0, 5, vb0, vb1, 0, 1);
    WAITL(0); SBAR(); }
  int buf = 0;
#pragma unroll 1
  for (int kt = 0; kt < 15; ++kt) KSTEP(0);
  { const int kt = 15; KSTEP(2); }
  STMP(3);
  __builtin_amdgcn_s_setprio(2);
  const LAS float* Lm = (const LAS float*)(lds + L_M) + wr * 64;
  const LAS float* Lr = (const LAS float*)(lds + L_RS) + wr * 64;
  const LAS float* Lc = (const LAS float*)(lds + L_CS) + wc * 96;
  f4 kv[4], zt[2];
  {
    float cs[4];
#pragma unroll
    for (int n = 0; n < 4; ++n) cs[n] = Lc[32 + 16 * n + r15];
    const u4 ones = (u4)(0x3F803F80u);
#pragma unroll
    for (int t = 0; t < 2; ++t) {
      u4 kf[2], vf[2];
#pragma unroll
      for (int mm = 0; mm < 2; ++mm) {
        const int m = 2 * t + mm;
        const f4 mv = *(const LAS f4*)(Lm + m * 16 + 4 * g4), rv = *(const LAS f4*)(Lr + m * 16 + 4 * g4);
#pragma unroll
        for (int a = 0; a < 2; ++a) {
          const f4 uk = acc[m][2 + a] - mv * cs[a];
          const f4 uv = (acc[m][4 + a] - mv * cs[2 + a]) * rv;
          kf[a][2 * mm] = phi2((f2){uk[0], uk[1]}, (f2){rv[0], rv[1]}); kf[a][2 * mm + 1] = phi2((f2){uk[2], uk[3]}, (f2){rv[2], rv[3]});
          vf[a][2 * mm] = pk2(uv[0], uv[1]); vf[a][2 * mm + 1] = pk2(uv[2], uv[3]);
        }
      }
#pragma unroll
      for (int tt = 0; tt < 4; ++tt) kv[tt] = MFMA16(S8(kf[tt & 1]), S8(vf[tt >> 1]), t ? kv[tt] : (f4)(0.f));
#pragma unroll
      for (int a = 0; a < 2; ++a) zt[a] = MFMA16(S8(kf[a]), S8(ones), t ? zt[a] : (f4)(0.f));
    }
  }
  const int hd = hp * 2 + wc;
  {
    LAS f4* ks = (LAS f4*)(lds + L_KVX + w * 3072) + l;
    const LAS f4* kr = (const LAS f4*)(lds + L_KVX + (w ^ 2) * 3072) + l;
    ks[0] = wr ? kv[0] : kv[2]; ks[64] = wr ? kv[1] : kv[3]; ks[128] = wr ? zt[0] : zt[1];
    lsig(myFL, 1);
    const f4 m0 = wr ? kv[2] : kv[0], m1 = wr ? kv[3] : kv[1], mz = wr ? zt[1] : zt[0];
    lwait(adFL, 1u << (w ^ 2), 1);
    const f4 s0 = m0 + kr[0], s1 = m1 + kr[64], sz = mz + kr[128];
    u4 o; o[0] = min(pk2(s0[0], s0[1]), POIS - 1u); o[1] = pk2(s0[2], s0[3]); o[2] = pk2(s1[0], s1[1]); o[3] = pk2(s1[2], s1[3]);
    ST128(o, WS_KVP + (hd * 16 + mb) * 2048 + wr * 1024 + l * 16);
    u4 zo = __builtin_bit_cast(u4, sz); zo[0] = min(zo[0], POIS - 1u);
    if (r15 == 0) ST128(zo, WS_ZP + (hd * 16 + mb) * 128 + g4 * 32 + wr * 16);
  }
  STMP(4);
  {
    const f4 c0 = *(const LAS f4*)(Lc + 4 * g4), c1 = *(const LAS f4*)(Lc + 16 + 4 * g4);
#pragma unroll
    for (int m = 0; m < 4; ++m) {
      const float mv = Lm[m * 16 + r15], rv = Lr[m * 16 + r15];
      const f4 u0 = acc[m][0] - c0 * mv, u1 = acc[m][1] - c1 * mv;
      u4 pf;
      pf[0] = phi2((f2){u0[0], u0[1]}, (f2)(rv)); pf[1] = phi2((f2){u0[2], u0[3]}, (f2)(rv));
      pf[2] = phi2((f2){u1[0], u1[1]}, (f2)(rv)); pf[3] = phi2((f2){u1[2], u1[3]}, (f2)(rv));
      *(LAS u4*)(lds + L_PQ + wr * 8192 + wc * 4096 + m * 1024 + l * 16) = pf;
    }
  }
  lsig(myFL, 2);
  __builtin_amdgcn_s_setprio(0);
  STMP(5);
  const int trow = mb * 128 + w * 32 + (l & 31);
  u2 oh;
  { const char* hb = uptr(wsx + WS_HK); const unsigned ho = ((l >> 1) & 15) * 2048 + (mb ^ ((l & 1) * 8)) * 128;
    asm volatile("global_load_dwordx2 %0, %1, %2 sc1" : "=v"(oh) : "v"(ho), "s"(hb) : "memory"); }
  int sel = -1;
  {
    const u4 s0 = *(const LAS u4*)(lds + L_SEL), s1 = *(const LAS u4*)(lds + L_SEL + 16);
#pragma unroll
    for (int i = 0; i < 8; ++i) { const int v = (int)(i < 4 ? s0[i & 3] : s1[i & 3]); sel = v == trow ? i : sel; }
  }
  const bool anysel = __ballot(sel >= 0) != 0ull;
  f4 xi[8];
  {
    const float m = *(const LAS float*)(lds + L_M + (w * 32 + (l & 31)) * 4), rs = *(const LAS float*)(lds + L_RS + (w * 32 + (l & 31)) * 4);
#pragma unroll
    for (int t = 0; t < 8; ++t) {
      const f4 xf = (f4){lo(xv[t][0]), hi(xv[t][0]), lo(xv[t][1]), hi(xv[t][1])};
      xi[t] = (xf - m) * rs + xf;
      asm volatile("" : "+v"(xi[t]));
    }
  }
  unsigned wa[2];
#pragma unroll
  for (int t = 0; t < 2; ++t) wa[t] = ldsb + L_WO + (4 * (g4 >> 1) + q) * 128 + (((4 * t + 2 * (g4 & 1) + (p >> 1)) ^ ((q >> 1) << 2)) * 16) + (p & 1) * 8;
  int qo[4];
  {
    WAITV(0);
    asm volatile("" : "+v"(oh));
    unsigned rel = oh[1] - NOW() + 1000u; rel = oh[0] == tag ? rel : ~0u;
    rel = max(rel, (unsigned)__builtin_amdgcn_update_dpp(0, (int)rel, 0xB1, 0xf, 0xf, false));
    rel = max(rel, (unsigned)__builtin_amdgcn_update_dpp(0, (int)rel, 0x4E, 0xf, 0xf, false));
    rel = max(rel, (unsigned)__builtin_amdgcn_update_dpp(0, (int)rel, 0x141, 0xf, 0xf, false));
    const unsigned r0 = __builtin_amdgcn_readlane(rel, 0), r1 = __builtin_amdgcn_readlane(rel, 8), r2 = __builtin_amdgcn_readlane(rel, 16), r3 = __builtin_amdgcn_readlane(rel, 24);
    const int a = GRP == 8 && r0 > r1 ? 1 : 0, b = GRP == 8 && r2 > r3 ? 3 : 2; const bool sw = GRP <= 16 && max(r0, r1) > max(r2, r3);
    qo[0] = DYN ? (sw ? b : a) : 0; qo[1] = qo[0] ^ 1; qo[2] = DYN ? (sw ? a : b) : 2; qo[3] = qo[2] ^ 1;
  }
  u4 a[RD][2];
#pragma unroll
  for (int k = 0; k < RD; ++k) asm volatile("" : "=v"(a[k][0]), "=v"(a[k][1]));
  const char* ob = uptr(wsx + WS_OF + mb * 262144 + w * 2048);
  const unsigned ov0 = l * 16;
  STMP(6);
  lwait<1>(adFL, 0x30u, 1);
  STMP(7);
  unsigned seen = 0, go = 0; u2 oh2;
  asm volatile("" : "=v"(oh2));
  const int lq = (l >> 3) & 3;
  const unsigned flo = HEADAT((lq == 0 ? qo[0] : lq == 1 ? qo[1] : lq == 2 ? qo[2] : qo[3]) * 8 + (l & 7)) * 128;
  const char* fb = uptr(wsx + WS_OH + mb * 4096);
  auto need = [&](unsigned mask, bool first) {
    if (__builtin_expect((go & mask) != mask, 0)) {
      _Pragma("unroll 1") for (unsigned it = 0; it < SPIN; ++it) {
        bool poll = (seen & mask) != mask;
        if (first && seen != ~0u && !poll) poll = ((unsigned)__ballot(oh[1] - NOW() - 60u < 1000u) & mask) != 0u;
        if (poll) {
          asm volatile("global_load_dwordx2 %0, %1, %2 sc1\n s_waitcnt vmcnt(0)" : "+v"(oh) : "v"(flo), "s"(fb) : "memory");
          seen = (unsigned)__ballot(oh[0] == tag);
        }
        go = seen & (unsigned)__ballot(oh[1] - NOW() - 1u >= 1000u);
        if ((go & mask) == mask) break;
        SLEEP(1);
      }
    }
  };
#define OX 0
#define OLD(pos) do { if (OX == 2 && (pos) >= RD) break; const char* sb_ = ob + (OX == 1 ? ((pos) & 3) : HEADAT(qo[(pos) >> 3] * 8 + ((pos) & 7))) * 8192; \
    asm volatile("global_load_dwordx4 %0, %1, %2 sc1" : "+v"(a[(pos) % RD][0]) : "v"(ov0), "s"(sb_) : "memory"); \
    asm volatile("global_load_dwordx4 %0, %1, %2 offset:1024 sc1" : "+v"(a[(pos) % RD][1]) : "v"(ov0), "s"(sb_) : "memory"); } while (0)
#define OLP(pos) do { if (ISPP(pos)) asm volatile("global_load_dwordx2 %0, %1, %2 sc1" : "+v"(oh2) : "v"(flo), "s"(fb) : "memory"); \
    OLD(pos); } while (0)
  auto isbad = [&](u4(&v)[2]) {
    asm volatile("" : "+v"(v[0]), "+v"(v[1]));
    return __ballot(max(v[0][0], v[1][0]) == POIS) != 0ull;
  };
#define OCHK(pos) do { WAITV(oyoung(pos)); \
    if (__builtin_expect(isbad(a[(pos) % RD]), 0)) { RETRY(); _Pragma("unroll 1") for (unsigned it = 0; it < SPIN; ++it) { SLEEP(4); OLD(pos); WAITV(0); if (!isbad(a[(pos) % RD])) break; } } \
    if (ISPP(pos)) { asm volatile("" : "+v"(oh2)); oh = oh2; seen = (unsigned)__ballot(oh[0] == tag); } } while (0)
  need(1u, true);
  if (OWN) lwait<1>(adFL, 0x30u, 2);
  __builtin_amdgcn_s_setprio(2);
  STMP(15);
  OLD(0); STMP(14);
#pragma unroll
  for (int i = 1; i < RD; ++i) { need(1u << i, false); OLP(i); }
  STMP(10);
  s4 b[2][8];
  f16v oc[3][2];
#define ORD(pos, f) do { if ((f) < 2 && ((pos) & 7) == 0) { wa[(f) & 1] += (qo[(pos) >> 3] - ((pos) ? qo[((pos) >> 3) - ((pos) ? 1 : 0)] : 0)) * 32768; asm volatile("" : "+v"(wa[(f) & 1])); } \
    RDTR(b[(pos) & 1][2 * (f)], wa[(f) & 1], ((pos) & 7) * 4096 + ((f) >> 1) * 1024); RDTR(b[(pos) & 1][2 * (f) + 1], wa[(f) & 1], ((pos) & 7) * 4096 + ((f) >> 1) * 1024 + 2048); } while (0)
#define OSET(pos) (GRP == 8 ? ((pos) < 8 ? 0 : (pos) < 24 ? 1 : 2) : (pos) >> 4)
#define OMF(pos, f) do { if ((f) < 2 && ((pos) & (GRP == 8 ? 7 : 15)) == 0) asm volatile("v_mfma_f32_32x32x16_bf16 %0, %1, %2, 0" : "=&v"(oc[OSET(pos)][(f) & 1]) \
      : "v"(CAT(b[(pos) & 1][2 * (f)], b[(pos) & 1][2 * (f) + 1])), "v"(a[(pos) % RD][(f) >> 1])); \
    else asm volatile("v_mfma_f32_32x32x16_bf16 %0, %1, %2, %0" : "+v"(oc[OSET(pos)][(f) & 1]) \
      : "v"(CAT(b[(pos) & 1][2 * (f)], b[(pos) & 1][2 * (f) + 1])), "v"(a[(pos) % RD][(f) >> 1])); \
    SBAR(); if ((pos) + 1 < 32) ORD(((pos) + 1) & 31, f); SBAR(); } while (0)
#define OFENCE(k) asm volatile("s_nop 7\n s_nop 7\n s_nop 7" : "+v"(oc[k][0]), "+v"(oc[k][1]))
#define OADD(d, k) do { OFENCE(k); oc[d][0] += oc[k][0]; oc[d][1] += oc[k][1]; asm volatile("s_nop 1" : "+v"(oc[d][0]), "+v"(oc[d][1])); SBAR(); } while (0)
#define ONX(pos) ((pos) > 0 && (pos) + RD - 1 < 32)
#define OHEAD(pos) do { \
    if (GRP == 8 && (pos) == 16) OADD(0, 1); \
    WAITL(4); SBAR(); \
    OMF(pos, 0); if (ONX(pos)) need(1u << (((pos) + RD - 1) & 31), false); SBAR(); \
    OMF(pos, 1); if (ONX(pos)) OLP(((pos) + RD - 1) & 31); \
    WAITL((pos) + 1 < 32 ? 4 : 0); SBAR(); \
    OMF(pos, 2); OMF(pos, 3); \
    if ((pos) + 1 < 32) OCHK(((pos) + 1) & 31); SBAR(); } while (0)
#define OGRP(c) do { OHEAD(4 * (c)); OHEAD(4 * (c) + 1); OHEAD(4 * (c) + 2); OHEAD(4 * (c) + 3); } while (0)
  ORD(0, 0); ORD(0, 1); ORD(0, 2); ORD(0, 3);
  OCHK(0); SBAR();
  OGRP(0); STMP(11); OGRP(1);
  OGRP(2); OGRP(3); STMP(12); OGRP(4); STMP(13); OGRP(5); OGRP(6); OGRP(7);
  if (GRP == 8) OADD(1, 2); else OFENCE(1);
#pragma unroll
  for (int t = 0; t < 8; ++t)
#pragma unroll
    for (int i = 0; i < 4; ++i) xi[t][i] += oc[0][t >> 2][(t & 3) * 4 + i] + oc[1][t >> 2][(t & 3) * 4 + i];
  STMP(8);
  if (anysel) {
    lwait(adFL, 0xC0u, 3);
    if (sel >= 0) {
#pragma unroll
      for (int t = 0; t < 8; ++t) xi[t] += *(const LAS f4*)(lds + L_KVX + sel * 256 + (8 * t + 4 * (l >> 5)) * 4);
    }
  }
  const __amdgpu_buffer_rsrc_t outr = __builtin_amdgcn_make_buffer_rsrc(out, 0, 2048 * D * 2, 0x00020000);
  if (FULL) {
    LAS char* tb = (LAS char*)lds + L_PQ + w * 4096;
#pragma unroll
    for (int t = 0; t < 8; ++t) *(LAS u2*)(tb + (l & 31) * 128 + ((t ^ (l & 7)) * 16) + (l >> 5) * 8) = (u2){pk2(xi[t][0], xi[t][1]), pk2(xi[t][2], xi[t][3])};
#pragma unroll
    for (int i = 0; i < 4; ++i) {
      const u4 v = *(const LAS u4*)(tb + i * 1024 + (l >> 3) * 128 + (((l ^ (l >> 3)) & 7) * 16));
      __builtin_amdgcn_raw_buffer_store_b128(v, outr, (mb * 128 + w * 32 + i * 8 + (l >> 3)) * 2048 + nb * 128 + (l & 7) * 16, 0, 16);
    }
  } else {
#pragma unroll
  for (int hh = 0; hh < 4; ++hh) {
    const f4 ta = xi[2 * hh], tb = xi[2 * hh + 1];
    auto r0 = __builtin_amdgcn_permlane32_swap(pk2(ta[0], ta[1]), pk2(tb[0], tb[1]), false, false);
    auto r1 = __builtin_amdgcn_permlane32_swap(pk2(ta[2], ta[3]), pk2(tb[2], tb[3]), false, false);
    __builtin_amdgcn_raw_buffer_store_b128(((u4){r0[0], r1[0], r0[1], r1[1]}), outr, (trow * D + nb * 64 + 16 * hh + 8 * (l >> 5)) * 2, 0, 16);
  }
  }
  if (g == 0 && w == 0 && l == 0) __builtin_amdgcn_raw_buffer_store_b32(tag, wse, WS_E, 0, 16);
  STMP(9);
  STFLUSH();
  asm volatile(".Lend_c:");
  return;
  }
  {
    const int lw = w - 4;
    unsigned ev;
    asm volatile("global_load_dword %0, %1, %2 sc1" : "=v"(ev) : "v"(0u), "s"(ws) : "memory");
    unsigned vf[20], vfw[4];
#pragma unroll
    for (int i = 0; i < 8; ++i) { const int row = 8 * (lw * 8 + i) + (l >> 3); vf[i] = row * 2048 + (((l & 7) ^ ((row >> 1) & 7)) * 16) + (3 - (i & 3)) * 1024; }
#pragma unroll
    for (int i = 0; i < 12; ++i) {
      const int pc = lw * 12 + i, row = (pc & 7) * 8 + (l >> 3);
      vf[8 + i] = row * 6144 + (pc >> 3) * 2048 + (((l & 7) ^ ((((row >> 1) & 1) << 2) | (((row >> 3) & 1) << 1))) * 16) + (3 - (i & 3)) * 1024;
    }
#pragma unroll
    for (int i = 0; i < 4; ++i) vfw[i] = (8 * i + (l >> 3)) * 2048 + (((l & 7) ^ (((l >> 4) & 1) << 2)) * 16) + (3 - i) * 1024;
    const char* Ab = uptr((const char*)x + (size_t)mb * 128 * 2048) - 3072;
    const char* Bb = uptr((const char*)wqkv + hp * 128) - 3072;
    const unsigned mba = ldsb + lw * 8192, mbb = ldsb + 16384 + lw * 12288;
#define GL4(v, sb, mm, MO) do { asm volatile("s_add_i32 m0, %2, %3\n s_nop 0\n global_load_lds_dwordx4 %0, %1" ::"v"((v)[0]), "s"(sb), "s"(mm), "i"(MO) : "memory", "scc"); \
      GLI((v)[1], sb, 1024); GLI((v)[2], sb, 2048); GLI((v)[3], sb, 3072); } while (0)
    auto issue = [&](int s, int u0, int u1) {
      const int kt = (s + rot) & 15;
      const char* sa = Ab + kt * 128; const char* sb = Bb + kt * (64 * 6144);
      const unsigned so = (s & 3) * STG, ma = mba + so, mq = mbb + so;
#pragma unroll
      for (int u = u0; u < u1; ++u) { if (u < 2) GL4(vf + 4 * u, sa, ma, u * 4096); else GL4(vf + 4 * u, sb, mq, (u - 2) * 4096); }
    };
    int tk = 0;
#define LBAR() do { BST(tk + 1); BAR(); BST(tk + 21); ++tk; } while (0)
    issue(0, 0, 5); issue(1, 0, 5);
    BST(60);
    WAITV(20); BST(0); BAR(); BST(20);
    asm volatile("" : "+v"(ev));
    const unsigned tag = __builtin_amdgcn_readfirstlane(ev) + 1u;
    MKWS(tag);
    issue(2, 0, 5);
    int s0 = 3;
#pragma unroll 1
    for (; s0 < (NOSKIP ? 16 : 8 + ph); ++s0) { issue(s0, 0, 5); BST(tk + 40); WAITV(40); LBAR(); }
#pragma unroll 1
    for (; s0 < 16; s0 += 4) {
      WAITV(20); LBAR();
      issue(s0, 0, 5); issue(s0 + 1, 0, 2); BST(tk + 40); WAITV(28); LBAR();
      issue(s0 + 1, 2, 5);
      if (s0 + 2 >= 16) { WAITV(20); LBAR(); break; }
      issue(s0 + 2, 0, 4); BST(tk + 40); WAITV(36); LBAR();
      issue(s0 + 2, 4, 5);
      if (s0 + 3 >= 16) { WAITV(20); LBAR(); break; }
      issue(s0 + 3, 0, 5); BST(tk + 40); WAITV(40); LBAR();
    }
    int wq = lw;
    auto wo_upto = [&](int lim) {
      _Pragma("unroll 1") for (; wq < lim; wq += 2) { const char* wp = wob - 3072 + HEADAT(wq) * 65536; const unsigned wm = ldsb + wq * 4096; GL4(vfw, wp, wm, L_WO); }
    };
    if (ph == 0 || NOSKIP) { WAITV(20); LBAR(); }
    if (ph == 1 && !NOSKIP) WAITV(0); else { wo_upto(12); WAITV(24); }
    LBAR();
    if (lw == 0) ST64(((u2){tag, NOW() + KD}), l == 0 ? WS_HK + (hp * 16 + mb) * 128 : WS_SZ);
    if (NOSKIP) wo_upto(22); else if (ph != 2) wo_upto(ph ? 14 : 22);
    LBAR();
    wo_upto(32);
    STMP(1);
    const int hd = hp * 2 + lw;
    u4 sel[2];
#pragma unroll
    for (int e = 0; e < 2; ++e) {
      const unsigned v = (r15 >> 2) == g4 ? (((r15 & 3) == 2 * e ? 0x3F80u : 0u) | ((r15 & 3) == 2 * e + 1 ? 0x3F800000u : 0u)) : 0u;
      sel[0][e] = v; sel[0][2 + e] = 0u; sel[1][e] = 0u; sel[1][2 + e] = v;
    }
    u4 pv[16][2], zv0, zv1, pq[8];
    const char* kvb = uptr(wsx + WS_KVP + (size_t)hd * 32768);
    const char* zb = uptr(wsx + WS_ZP + (size_t)hd * 2048);
    const char* hb = uptr(wsx + WS_HK + hp * 2048);
    const unsigned lv16 = l * 16, zoff = r15 * 128 + g4 * 32;
    const unsigned pa = ldsb + L_PQ + lw * 4096 + l * 16, omask = lw ? 0x0Au : 0x05u;
    u2 hk;
    _Pragma("unroll 1") for (unsigned it = 0; it < SPIN; ++it) {
      asm volatile("global_load_dwordx2 %0, %1, %2 sc1\n s_waitcnt vmcnt(0)" : "=v"(hk) : "v"(r15 * 128), "s"(hb) : "memory");
      if (it == 0) lsig(myFL, 1);
      if (__ballot(hk[0] != tag) == 0ull) break;
      SLEEP(2);
    }
    __builtin_amdgcn_s_setprio(3);
    STMP(2);
    const unsigned t0 = NOW();
    unsigned rel = hk[1] - t0; rel = rel < 1000u ? rel : 0u;
    rel = max(rel, (unsigned)__builtin_amdgcn_update_dpp(0, (int)rel, 0xB1, 0xf, 0xf, false));
    rel = max(rel, (unsigned)__builtin_amdgcn_update_dpp(0, (int)rel, 0x4E, 0xf, 0xf, false));
    int gx;
    { const unsigned r0 = __builtin_amdgcn_readlane(rel, 0), r1 = __builtin_amdgcn_readlane(rel, 4), r2 = __builtin_amdgcn_readlane(rel, 8), r3 = __builtin_amdgcn_readlane(rel, 12);
      gx = max(r0, r1) > max(r2, r3) ? (r0 > r1 ? 3 : 2) : (r2 > r3 ? 1 : 0);
      if (OHP) ST64(((u2){tag, t0 + max(max(r0, r1), max(r2, r3)) + OD}), l == 0 ? WS_OH + (mb * 32 + hd) * 128 : WS_SZ); }
#define GLD(gq) do { _Pragma("unroll") for (int i = 0; i < 4; ++i) { const char* sb = kvb + (((gq) ^ gx) * 4 + (i & 2)) * 2048; \
      asm volatile("global_load_dwordx4 %0, %1, %2 offset:%3 sc1" : "=v"(pv[(gq) * 4 + i][0]) : "v"(lv16), "s"(sb), "i"((i & 1) * 2048) : "memory"); \
      asm volatile("global_load_dwordx4 %0, %1, %2 offset:%3 sc1" : "=v"(pv[(gq) * 4 + i][1]) : "v"(lv16), "s"(sb), "i"((i & 1) * 2048 + 1024) : "memory"); } } while (0)
#define ZLD() do { asm volatile("global_load_dwordx4 %0, %1, %2 sc1" : "=v"(zv0) : "v"(zoff), "s"(zb) : "memory"); \
      asm volatile("global_load_dwordx4 %0, %1, %2 offset:16 sc1" : "=v"(zv1) : "v"(zoff), "s"(zb) : "memory"); } while (0)
#pragma unroll
    for (int gq = 0; gq < 4; ++gq) {
      const unsigned rq = __builtin_amdgcn_readlane(rel, 4 * (gq ^ gx));
      _Pragma("unroll 1") for (unsigned it = 0; it < SPIN; ++it) if (NOW() - t0 >= rq) break;
      if (gq == 3) ZLD();
      GLD(gq);
    }
    if (!OHP) ST64(((u2){tag, NOW() + OD}), l == 0 ? WS_OH + (mb * 32 + hd) * 128 : WS_SZ);
    STMP(5);
    lwait(adFL, omask, 2);
#pragma unroll
    for (int i = 0; i < 8; ++i) RD128(pq[i], pa, (i >> 2) * 8192 + (i & 3) * 1024);
#pragma unroll
    for (int gq = 0; gq < 4; ++gq) {
      SBAR(); WAITV((gq == 0 ? 27 : gq == 1 ? 19 : gq == 2 ? 11 : 1) - OHP);
      _Pragma("unroll 1") for (unsigned it = 0; it < SPIN; ++it) {
#pragma unroll
        for (int i = 0; i < 4; ++i) asm volatile("" : "+v"(pv[gq * 4 + i][0]), "+v"(pv[gq * 4 + i][1]));
        unsigned m = umax3(pv[gq * 4][0][0], pv[gq * 4][1][0], pv[gq * 4 + 1][0][0]);
        m = umax3(m, pv[gq * 4 + 1][1][0], pv[gq * 4 + 2][0][0]);
        m = umax3(m, pv[gq * 4 + 2][1][0], pv[gq * 4 + 3][0][0]);
        m = max(m, pv[gq * 4 + 3][1][0]);
        if (gq == 3) { asm volatile("" : "+v"(zv0), "+v"(zv1)); m = umax3(m, zv0[0], zv1[0]); }
        if (__ballot(m == POIS) == 0ull) break;
        RETRY();
        SLEEP(2);
        if (gq == 3) ZLD();
        GLD(gq);
        WAITV(0);
      }
      f4 ga[4];
#pragma unroll
      for (int i = 0; i < 4; ++i)
#pragma unroll
        for (int t = 0; t < 4; ++t) ga[t] = MFMA16(S8(sel[t & 1]), S8(pv[gq * 4 + i][t >> 1]), i ? ga[t] : (f4)(0.f));
#pragma unroll
      for (int t = 0; t < 4; ++t) pv[gq * 4 + (t >> 1)][t & 1] = __builtin_bit_cast(u4, ga[t]);
    }
    STMP(3);
    WAITL(0);
#pragma unroll
    for (int i = 0; i < 8; i += 2) asm volatile("" : "+v"(pq[i]), "+v"(pq[i + 1]));
    u4 kA[2], zA;
    {
      float z[8];
#pragma unroll
      for (int e = 0; e < 4; ++e) { z[e] = __uint_as_float(zv0[e]); z[4 + e] = __uint_as_float(zv1[e]); }
#define ZR(n) "v_add_f32_dpp %0, %0, %0 row_ror:" #n " row_mask:0xf bank_mask:0xf\n v_add_f32_dpp %1, %1, %1 row_ror:" #n " row_mask:0xf bank_mask:0xf\n" \
              "v_add_f32_dpp %2, %2, %2 row_ror:" #n " row_mask:0xf bank_mask:0xf\n v_add_f32_dpp %3, %3, %3 row_ror:" #n " row_mask:0xf bank_mask:0xf\n" \
              "v_add_f32_dpp %4, %4, %4 row_ror:" #n " row_mask:0xf bank_mask:0xf\n v_add_f32_dpp %5, %5, %5 row_ror:" #n " row_mask:0xf bank_mask:0xf\n" \
              "v_add_f32_dpp %6, %6, %6 row_ror:" #n " row_mask:0xf bank_mask:0xf\n v_add_f32_dpp %7, %7, %7 row_ror:" #n " row_mask:0xf bank_mask:0xf\n"
      asm volatile("s_nop 1\n" ZR(8) ZR(4) ZR(2) ZR(1) : "+v"(z[0]), "+v"(z[1]), "+v"(z[2]), "+v"(z[3]), "+v"(z[4]), "+v"(z[5]), "+v"(z[6]), "+v"(z[7]));
#pragma unroll
      for (int e = 0; e < 4; ++e) zA[e] = pk2(z[2 * e], z[2 * e + 1]);
    }
    float inv[8];
#pragma unroll
    for (int i = 0; i < 8; ++i) { f4 dn = MFMA16(S8(zA), S8(pq[i]), (f4)(0.f)); inv[i] = dn[0]; }
#pragma unroll
    for (int t = 0; t < 4; ++t) {
#define PF(gq) __builtin_bit_cast(f4, pv[(gq) * 4 + (t >> 1)][t & 1])
      f4 s = (PF(0) + PF(1)) + (PF(2) + PF(3));
      kA[t >> 1][(t & 1) * 2] = pk2(s[0], s[1]); kA[t >> 1][(t & 1) * 2 + 1] = pk2(s[2], s[3]);
    }
    f4 o0[8], o1[8];
#pragma unroll
    for (int i = 0; i < 8; ++i) {
      o0[i] = MFMA16(S8(kA[0]), S8(pq[i]), (f4)(0.f)); o1[i] = MFMA16(S8(kA[1]), S8(pq[i]), (f4)(0.f));
      inv[i] = __builtin_amdgcn_rcpf(inv[i] + 1e-6f);
    }
#pragma unroll
    for (int i = 0; i < 8; ++i) {
      o0[i] *= inv[i]; o1[i] *= inv[i];
      u4 o; o[0] = min(pk2(o0[i][0], o0[i][1]), POIS - 1u); o[1] = pk2(o0[i][2], o0[i][3]); o[2] = pk2(o1[i][0], o1[i][1]); o[3] = pk2(o1[i][2], o1[i][3]);
      ST128(o, WS_OF + (mb * 32 + hd) * 8192 + (i >> 1) * 2048 + (i & 1) * 256 + g4 * 512 + r15 * 16);
    }
    lsig(myFL, 2);
    STMP(4);
    STFLUSH();
    asm volatile(".Lend_l:");
  }

}

extern "C" void kernel_launch(void* const* d_in, const int* in_sizes, int n_in, void* d_out, int out_size,
                              void* d_ws, size_t ws_size, hipStream_t stream) {
  (void)hipFuncSetAttribute((const void*)fused, hipFuncAttributeMaxDynamicSharedMemorySize, LDS_SZ);
  fused<<<256, 512, LDS_SZ, stream>>>((const u16*)d_in[0], (const u16*)d_in[1], (const u16*)d_in[2], (const u16*)d_in[4],
                                      (const u16*)d_in[5], (const u16*)d_in[6], (const u16*)d_in[7], (char*)d_ws, (u16*)d_out);
}
```

```cpp
#include <hip/hip_runtime.h>
#include <stdint.h>

typedef unsigned short u16;
typedef short s4 __attribute__((ext_vector_type(4)));
typedef short s8 __attribute__((ext_vector_type(8)));
typedef float f4 __attribute__((ext_vector_type(4)));
typedef float f16v __attribute__((ext_vector_type(16)));
typedef float f2 __attribute__((ext_vector_type(2)));
typedef unsigned u4 __attribute__((ext_vector_type(4)));
typedef unsigned u2 __attribute__((ext_vector_type(2)));
typedef __bf16 bf2 __attribute__((ext_vector_type(2)));
#define LAS __attribute__((address_space(3)))
#define DEV __device__ __forceinline__

constexpr int D = 1024, F = 3584;
#define NOMOE 0
#define KD 290
#define OD 190
#define OHP 1
#define FS 128
#define GSL 8
#define YSL 16
#define TP1 0
#define TP2 3
#define TLBW 0
#define FULL 1
#define HPRIO 1
#define OWN 1
#define NOPOIS 0
#define CAP 7
#define RD 4
#define DYN 1
#define JX 0
#define NOSKIP 0
#define GRP 8
#define PV 4
#define U1 4
#define U2 2
#define ROT 0

constexpr unsigned WS_E = 0;
constexpr unsigned WS_YF = 524288;
constexpr unsigned WS_CAND = 12288;
constexpr unsigned WS_GF = 589824;
constexpr unsigned WS_RG = 49152;
constexpr unsigned WS_ST = 65536;
constexpr unsigned WS_HK = 98304;
constexpr unsigned WS_ZP = 131072;
constexpr unsigned WS_CS = 196608;
constexpr unsigned WS_OH = 458752;
constexpr unsigned WS_KVP = 1u << 20;
constexpr unsigned WS_GP = 2u << 20;
constexpr unsigned WS_YP = 3u << 20;
constexpr unsigned WS_OF = 5u << 20;
constexpr unsigned WS_SZ = 9u << 20;
#define NR 4
#define RGO(tag) ((64u << 20) + ((tag) & (NR - 1u)) * (10u << 20))
#define MKWS(tag) char* const wsx = ws + RGO(tag); const __amdgpu_buffer_rsrc_t wsr = __builtin_amdgcn_make_buffer_rsrc(wsx, 0, WS_SZ, 0x00020000)

constexpr int STG = 40960;
constexpr int L_KVX = 0;
constexpr int L_PQ = 12288;
constexpr int L_M = 28672, L_RS = L_M + 512, L_CS = L_RS + 512, L_SEL = L_CS + 768, L_FL = L_SEL + 64;
constexpr int L_WO = 32768;
constexpr int LDS_SZ = 163840;
constexpr unsigned POIS = 0xFFFFFFFFu;

DEV unsigned pk2(float a, float b) { bf2 v; v[0] = (__bf16)a; v[1] = (__bf16)b; return __builtin_bit_cast(unsigned, v); }
DEV float lo(unsigned u) { return __uint_as_float(u << 16); }
DEV float hi(unsigned u) { return __uint_as_float(u & 0xFFFF0000u); }
DEV float rbf(float a) { return lo(pk2(a, a)); }
DEV float dot2(unsigned a, unsigned b, float c) { return __builtin_amdgcn_fdot2_f32_bf16(__builtin_bit_cast(bf2, a), __builtin_bit_cast(bf2, b), c, false); }
#define RD128(dst, addr, OFF) asm volatile("ds_read_b128 %0, %1 offset:%2" : "=v"(dst) : "v"(addr), "i"(OFF) : "memory")
#define RDTR(dst, addr, OFF) asm volatile("ds_read_b64_tr_b16 %0, %1 offset:%2" : "=v"(dst) : "v"(addr), "i"(OFF) : "memory")
#define SBAR() __builtin_amdgcn_sched_barrier(0)
#define CAT(x, y) __builtin_shufflevector(x, y, 0, 1, 2, 3, 4, 5, 6, 7)
#define WAITV(n) asm volatile("s_waitcnt vmcnt(%0)" ::"i"(n) : "memory")
#define WAITL(n) asm volatile("s_waitcnt lgkmcnt(%0)" ::"i"(n) : "memory")
#define MEMB() asm volatile("" ::: "memory")
#define SLEEP(n) asm volatile("s_sleep %0" ::"i"(n) : "memory")
#define BAR() asm volatile("s_barrier" ::: "memory")
#define GL0(vo, sb, m0v) asm volatile("s_mov_b32 m0, %2\n s_nop 0\n global_load_lds_dwordx4 %0, %1" ::"v"(vo), "s"(sb), "s"(m0v) : "memory")
#define GLI(vo, sb, OFF) asm volatile("global_load_lds_dwordx4 %0, %1 offset:%2" ::"v"(vo), "s"(sb), "i"(OFF) : "memory")
#define MFMA16(a, b, c) __builtin_amdgcn_mfma_f32_16x16x32_bf16(a, b, c, 0, 0, 0)
#define S8(x) __builtin_bit_cast(s8, x)
#define LD128(off) __builtin_amdgcn_raw_buffer_load_b128(wsr, (off), 0, 16)
#define LD64(off) __builtin_amdgcn_raw_buffer_load_b64(wsr, (off), 0, 16)
#define ST128(v, off) __builtin_amdgcn_raw_buffer_store_b128((v), wsr, (off), 0, 16)
#define ST64(v, off) __builtin_amdgcn_raw_buffer_store_b64((v), wsr, (off), 0, 16)
#define OFA(i) (WS_OF + ((mb * 32 + hd) * 8 + (i)) * 1024 + l * 16)
#define NOW() ((unsigned)__builtin_amdgcn_s_memrealtime())
constexpr unsigned SPIN = 1u << 21;
DEV const char* uptr(const char* p) {
  unsigned long long v = (unsigned long long)p;
  unsigned lo_ = __builtin_amdgcn_readfirstlane((unsigned)v), hi_ = __builtin_amdgcn_readfirstlane((unsigned)(v >> 32));
  return (const char*)(((unsigned long long)hi_ << 32) | lo_);
}
DEV void lsig(unsigned addr, unsigned val) { asm volatile("s_waitcnt lgkmcnt(0)\n ds_write_b32 %0, %1" ::"v"(addr), "v"(val) : "memory"); }
template <int SL = 0> DEV void lwait(unsigned addr, unsigned mask, unsigned target) {
  _Pragma("unroll 1") for (unsigned it = 0; it < SPIN; ++it) {
    unsigned v;
    asm volatile("ds_read_b32 %0, %1\n s_waitcnt lgkmcnt(0)" : "=v"(v) : "v"(addr) : "memory");
    if (((unsigned)__ballot(v < target) & mask) == 0u) break;
    if (SL) SLEEP(SL);
  }
}
DEV unsigned umax3(unsigned a, unsigned b, unsigned c) { return max(max(a, b), c); }
template <int C> DEV float dppf(float v) { return __int_as_float(__builtin_amdgcn_update_dpp(0, __float_as_int(v), C, 0xf, 0xf, false)); }
DEV unsigned wave_max(unsigned v) {
  v = max(v, (unsigned)__builtin_amdgcn_update_dpp(0, (int)v, 0x111, 0xf, 0xf, false));
  v = max(v, (unsigned)__builtin_amdgcn_update_dpp(0, (int)v, 0x112, 0xf, 0xf, false));
  v = max(v, (unsigned)__builtin_amdgcn_update_dpp(0, (int)v, 0x114, 0xf, 0xf, false));
  v = max(v, (unsigned)__builtin_amdgcn_update_dpp(0, (int)v, 0x118, 0xf, 0xf, false));
  v = max(v, (unsigned)__builtin_amdgcn_update_dpp(0, (int)v, 0x142, 0xa, 0xf, false));
  v = max(v, (unsigned)__builtin_amdgcn_update_dpp(0, (int)v, 0x143, 0xc, 0xf, false));
  return (unsigned)__builtin_amdgcn_readlane((int)v, 63);
}
DEV unsigned phi2(f2 u, f2 rs) {
  f2 t = u * rs;
  f2 a = t * 1.44269504f, p = t + 1.f, r;
  r[0] = __builtin_amdgcn_fmed3f(__builtin_amdgcn_exp2f(a[0]), p[0], 1.f);
  r[1] = __builtin_amdgcn_fmed3f(__builtin_amdgcn_exp2f(a[1]), p[1], 1.f);
  r += 1e-6f;
  return pk2(r[0], r[1]);
}

#define STDECL
#define STMP(i)
#define BST(i)
#define STFLUSH()
#define RETRY()
#define RETRY2()

DEV float half_sum(float v) {
  asm volatile("s_nop 1\n v_add_f32_dpp %0, %0, %0 row_ror:8 row_mask:0xf bank_mask:0xf\n s_nop 1\n v_add_f32_dpp %0, %0, %0 row_ror:4 row_mask:0xf bank_mask:0xf\n"
               "s_nop 1\n v_add_f32_dpp %0, %0, %0 row_ror:2 row_mask:0xf bank_mask:0xf\n s_nop 1\n v_add_f32_dpp %0, %0, %0 row_ror:1 row_mask:0xf bank_mask:0xf\n s_nop 1" : "+v"(v));
  auto r = __builtin_amdgcn_permlane16_swap(__float_as_uint(v), __float_as_uint(v), false, false);
  return __uint_as_float(r[0]) + __uint_as_float(r[1]);
}

struct FQ { s8 a[4]; s4 b[12]; };
template <bool B> struct BC { static constexpr bool v = B; };

__global__ __launch_bounds__(512) void fused(const u16* __restrict__ x, const u16* __restrict__ wqkv, const u16* __restrict__ wo,
                                             const u16* __restrict__ wrt, const u16* __restrict__ wg, const u16* __restrict__ wu,
                                             const u16* __restrict__ wd, char* __restrict__ ws, u16* __restrict__ out) {
  extern __shared__ __attribute__((aligned(16))) char lds[];
  asm volatile(".Lcode_beg:");
  const int w = __builtin_amdgcn_readfirstlane(threadIdx.x >> 6), l = threadIdx.x & 63;
  STDECL;
  STMP(0);
  const int g = blockIdx.x, xcd = g & 7, j = g >> 3, ii = j >> 2;
  unsigned cw = 0;
  if (w == 7) {
    unsigned long long pc; unsigned off, len, l1, l2, l3;
    asm volatile("s_getpc_b64 %0\n.Lcode_here:" : "=s"(pc));
    asm volatile("s_mov_b32 %0, (.Lcode_here - .Lcode_beg)" : "=s"(off));
    asm volatile("s_mov_b32 %0, (.Lend_c - .Lcode_beg)" : "=s"(len));
    asm volatile("s_mov_b32 %0, (.Lend_l - .Lcode_beg)" : "=s"(l1));
    asm volatile("s_mov_b32 %0, (.Lend_h - .Lcode_beg)" : "=s"(l2));
    asm volatile("s_mov_b32 %0, (.Lend_s - .Lcode_beg)" : "=s"(l3));
    len = max(max(len, l3), max(l1, l2));
    const unsigned o = min((unsigned)(l * 32 + j) * 64u, len - 4u);
    asm volatile("global_load_dword %0, %1, %2" : "+v"(cw) : "v"(o), "s"(pc - off) : "memory");
  }
#define PTAB 0xA98BFEDC76542103ull
#define PHT 0x6624924999924924ull
#define CX 4
  const int mb = (xcd & 1) * 8 + ii, hp = (xcd >> 1) * 4 + (j & 3);
  const bool sp = (hp & 7) == 3;
  const int rot = 2 * xcd + 4 * (ii & 3);
  const int nb = hp;
#define PAIRAT(p) ((int)(PTAB >> (4 * (p))) & 15)
#define HEADAT(s) (2 * PAIRAT((s) >> 1) + ((s) & 1))
  const int ph = (int)(PHT >> (2 * j)) & 3;
  const unsigned ldsb = (unsigned)(size_t)(LAS char*)lds;
  const unsigned adFL = ldsb + L_FL + 4 * (l & 7), myFL = ldsb + L_FL + 4 * w;
  const __amdgpu_buffer_rsrc_t wse = __builtin_amdgcn_make_buffer_rsrc(ws, 0, 64, 0x00020000);
  const int r15 = l & 15, g4 = l >> 4, q = (l & 15) >> 2, p = l & 3;
  const char* wob = uptr((const char*)wo + nb * 128);

  if (w != 4 && w != 5) {
  const unsigned ev0 = __builtin_amdgcn_raw_buffer_load_b32(wse, WS_E, 0, 16);
  if (w >= 6) {
    const int mw = w - 6;
    const unsigned tag = __builtin_amdgcn_readfirstlane(ev0) + 1u, tag2 = tag * 0x9E3779B1u;
    MKWS(tag);
    auto helper = [&](auto SP) {
    constexpr bool spare = decltype(SP)::v, moe = !spare && !NOMOE;
    int nbar = 0;
#define TICK() do { BST(nbar); SBAR(); BAR(); SBAR(); ++nbar; } while (0)
#define DTICK(lim) do { if (nbar < (lim)) TICK(); else SLEEP(2); } while (0)
    const int hd = hp * 2 + mw;
    const u4 pz = (u4)(POIS);
    const int chunk = 4 * (hp - (hp > 3) - (hp > 11)) + (ii & 3), ksp = (xcd & 1) * 2 + (ii >> 2), chg = chunk ^ (mw * CX);
    const int np = l & 7, kset = (l >> 3) & 1;
    u4 gw[4][8], dw[2][8];
    const __amdgpu_buffer_rsrc_t wgr = __builtin_amdgcn_make_buffer_rsrc((void*)(mw ? wu : wg), 0, D * F * 2, 0x00020000);
    const __amdgpu_buffer_rsrc_t wdr = __builtin_amdgcn_make_buffer_rsrc((void*)wd, 0, D * F * 2, 0x00020000);
    const __amdgpu_buffer_rsrc_t wor = __builtin_amdgcn_make_buffer_rsrc((void*)wo, 0, D * D * 2, 0x00020000);
    const unsigned gvo = ((256 * ksp + 32 * kset + 8 * g4) * F + 64 * chg + 8 * np) * 2;
    const unsigned dvo = ((64 * chunk + 8 * g4) * D + 256 * ksp + 128 * mw + 8 * r15) * 2;
    auto burst = [&](int k) {
      if (moe) {
#pragma unroll
        for (int i = 0; i < 16; ++i) {
          if (k < 2) gw[(2 * k + (i >> 3)) & 3][i & 7] = __builtin_amdgcn_raw_buffer_load_b128(wgr, gvo, (128 * k + 64 * (i >> 3) + (i & 7)) * F * 2, 2);
          else dw[i >> 3][i & 7] = __builtin_amdgcn_raw_buffer_load_b128(wdr, dvo, (32 * (i >> 3) + (i & 7)) * D * 2, 2);
        }
      }
    };
    auto transp = [&](u4(&v)[8]) {
      if (moe) {
        u4 f[8];
#pragma unroll
        for (int c = 0; c < 8; ++c)
#pragma unroll
          for (int e = 0; e < 4; ++e) {
            const unsigned ev = v[2 * e][c >> 1], od = v[2 * e + 1][c >> 1];
            f[c][e] = (c & 1) ? __builtin_amdgcn_perm(od, ev, 0x07060302u) : __builtin_amdgcn_perm(od, ev, 0x05040100u);
          }
#pragma unroll
        for (int c = 0; c < 8; ++c) { v[c] = f[c]; asm volatile("" : "+v"(v[c])); }
      }
    };
    auto pois = [&](int i) {
      if (i < 2) ST128(pz, WS_KVP + (hd * 16 + mb) * 2048 + i * 1024 + l * 16);
      else if (i == 2) ST128(pz, WS_ZP + (hd * 16 + mb) * 128 + (l & 7) * 16);
      else if (!NOPOIS) ST128(pz, OFA(i - 3));
    };
    unsigned wt0, wt1, wt2 = 0, wt3 = 0;
    auto touch = [&]() {
      const unsigned to = (j * 32 + mw * 16 + (l >> 3)) * 2048 + (xcd >> 1) * 512 + (l & 7) * 64;
      wt0 = __builtin_amdgcn_raw_buffer_load_b32(wor, to, 0, 0); wt1 = __builtin_amdgcn_raw_buffer_load_b32(wor, to, 16384, 0);
      if (TLBW) {
        const __amdgpu_buffer_rsrc_t outr = __builtin_amdgcn_make_buffer_rsrc(out, 0, 2048 * D * 2, 0x00020000);
        wt2 = __builtin_amdgcn_raw_buffer_load_b32(outr, (mb * 128 + mw * 64 + (l & 3) * 16) * 2048 + nb * 128, 0, 0);
        wt3 = __builtin_amdgcn_raw_buffer_load_b32(wsr, (l & 1) ? WS_GP + g * 4096 + mw * 2048 : WS_YP + (g >> 2) * 32768 + (l & 2) * 8192, 0, 0);
      }
    };
    s4 sb[24];
    auto colrd = [&](int h) {
      const unsigned bo = ldsb + (1 + h) * STG + 16384 + (8 * g4 + q) * 128 + (p & 1) * 8;
      const int sw = ((q >> 1) << 2) | ((g4 & 1) << 1);
      const unsigned b0 = bo + (((mw * 4 + (p >> 1)) ^ sw) * 16), b1 = bo + (((mw * 4 + 2 + (p >> 1)) ^ sw) * 16);
#pragma unroll
      for (int n = 0; n < 12; ++n) { RDTR(sb[2 * n], (n & 1) ? b1 : b0, ((n % 6) >> 1) * 8192 + (n / 6) * 4096); RDTR(sb[2 * n + 1], (n & 1) ? b1 : b0, ((n % 6) >> 1) * 8192 + (n / 6) * 4096 + 512); }
      WAITL(0);
      SBAR();
    };
    auto colsum = [&]() {
      const u4 ones = (u4)(0x3F803F80u);
      f4 c[6];
#pragma unroll
      for (int n = 0; n < 12; ++n) c[n % 6] = MFMA16(S8(ones), CAT(sb[2 * n], sb[2 * n + 1]), n >= 6 ? c[n % 6] : (f4)(0.f));
      const u4 csg = (u4){__float_as_uint(g4 ? c[3][0] : c[0][0]), __float_as_uint(g4 ? c[4][0] : c[1][0]), __float_as_uint(g4 ? c[5][0] : c[2][0]), tag};
      ST128(csg, l < 32 ? WS_CS + (hd * 16 + mb) * 512 + l * 16 : WS_SZ);
    };
    u4 stv, cv[8], cd; float cs[3];
    auto gload = [&]() {
      stv = LD128(WS_ST + (mb * 128 + mw * 64 + l) * 16);
      cd = LD128(WS_CAND + (g & 31) * 128 + (l & 7) * 16);
#pragma unroll
      for (int i = 0; i < 8; ++i) cv[i] = LD128(WS_CS + hd * 8192 + i * 1024 + l * 16);
    };
    auto gbad = [&]() {
      unsigned bad = (stv[1] ^ tag) | (stv[3] ^ tag) | (cd[1] ^ tag) | (cd[3] ^ tag);
#pragma unroll
      for (int i = 0; i < 8; ++i) bad |= cv[i][3] ^ tag;
      return __ballot(bad != 0u) != 0ull;
    };
    int idx;
    auto gfin = [&]() {
      if (gbad()) { RETRY(); _Pragma("unroll 1") for (unsigned it = 0; it < SPIN; ++it) { SLEEP(2); gload(); if (!gbad()) break; } }
#pragma unroll
      for (int e = 0; e < 3; ++e) {
        float a = __uint_as_float(cv[0][e]);
#pragma unroll
        for (int i = 1; i < 8; ++i) a += __uint_as_float(cv[i][e]);
        auto r = __builtin_amdgcn_permlane32_swap(__float_as_uint(a), __float_as_uint(a), false, false);
        cs[e] = __uint_as_float(r[0]) + __uint_as_float(r[1]);
      }
      asm volatile("" : "+v"(cs[0]), "+v"(cs[1]), "+v"(cs[2]) : "v"(wt0), "v"(wt1), "v"(wt2), "v"(wt3));
      cd[0] = max(cd[0], cd[2]);
      idx = (int)(cd[0] & 2047u);
    };
    auto lpub = [&]() {
      *(LAS float*)(lds + L_M + (mw * 64 + l) * 4) = __uint_as_float(stv[0]);
      *(LAS float*)(lds + L_RS + (mw * 64 + l) * 4) = __uint_as_float(stv[2]);
      if (l < 32) {
#pragma unroll
        for (int e = 0; e < 3; ++e) *(LAS float*)(lds + L_CS + mw * 384 + ((3 * g4 + e) * 16 + r15) * 4) = cs[e];
      }
      if (mw == 0 && l < 8) *(LAS int*)(lds + L_SEL + l * 4) = idx;
      lsig(myFL, 1u);
    };
    u4 xs[4];
    asm volatile("" ::"s"(wg), "s"(wu), "s"(wd), "v"(cw));
    if (HPRIO) __builtin_amdgcn_s_setprio(HPRIO);
    TICK();
    if (spare) {
      TICK();
      if (ii >> 2) TICK();
      colrd(ii >> 2);
      if (!(ii >> 2)) TICK();
      u4 kk[8];
      auto kload = [&]() {
#pragma unroll
        for (int i = 0; i < 8; ++i) kk[i] = LD128(WS_RG + mw * 8192 + i * 1024 + l * 16);
      };
      kload();
      TICK();
      _Pragma("unroll 1") for (unsigned it = 0; it < SPIN; ++it) {
        unsigned bad = 0;
#pragma unroll
        for (int i = 0; i < 8; ++i) { bad |= (kk[i][1] ^ tag) | (kk[i][3] ^ tag); asm volatile("" : "+v"(bad)); }
        if (__ballot(bad != 0u) == 0ull) break;
        RETRY();
        kload();
        DTICK(7);
      }
      __builtin_amdgcn_s_setprio(3);
      unsigned m1 = max(kk[0][0], kk[0][2]), m2 = min(kk[0][0], kk[0][2]), m3 = 0;
#pragma unroll
      for (int i = 2; i < 16; ++i) {
        const unsigned a = kk[i >> 1][(i & 1) * 2];
        asm("v_med3_u32 %0, %1, %2, %0" : "+v"(m3) : "v"(m2), "v"(a));
        asm("v_med3_u32 %0, %1, %2, %0" : "+v"(m2) : "v"(m1), "v"(a));
        m1 = max(m1, a);
      }
      unsigned mine8 = 0;
#pragma unroll
      for (int it = 0; it < 8; ++it) {
        const unsigned km = wave_max(m1);
        mine8 = l == it ? km : mine8;
        const bool win = m1 == km;
        if (it < 7) { m1 = win ? m2 : m1; m2 = win ? m3 : m2; m3 = win ? 0u : m3; }
        if (it == 3 && nbar < 7) TICK();
      }
      if (__builtin_expect(__ballot(m1 == 0u) != 0ull, 0)) {
        unsigned lim = 0;
#pragma unroll 1
        for (int it = 0; it < 8; ++it) {
          unsigned m = 0;
#pragma unroll
          for (int i = 0; i < 8; ++i) m = umax3(m, kk[i][0] - lim, kk[i][2] - lim);
          lim += wave_max(m);
          mine8 = l == it ? lim : mine8;
        }
      }
      ST64(((u2){mine8, tag}), l < 8 ? WS_CAND + ((hp >> 3) * 16 + mb) * 128 + (mw ? 7 - l : l) * 16 + mw * 8 : WS_SZ);
      __builtin_amdgcn_s_setprio(HPRIO);
      STMP(2);
      TICK();
      colsum();
#pragma unroll
      for (int i = 0; i < 11; ++i) { if (i == 5) DTICK(9); pois(i); }
      touch();
      while (nbar < 10) TICK();
      gload();
      TICK(); TICK();
      WAITV(0);
      gfin();
      STMP(3);
      TICK(); TICK(); TICK();
      lpub();
    } else {
      if (PV == 5) { pois(0); pois(1); }
      TICK();
      if (PV == 4) { pois(0); pois(1); pois(2); }
      if (PV == 5) { pois(2); pois(3); }
      if (ii >> 2) { TICK(); if (PV == 4) pois(3); if (PV >= 4) pois(4); }
      colrd(ii >> 2);
      TICK();
      if (!(ii >> 2)) { if (PV == 4) pois(3); if (PV >= 4) pois(4); }
      colsum();
      if (!(ii >> 2)) TICK();
      STMP(1);
      for (int i = 0; i < ph; ++i) TICK();
      burst(0); touch();
      TICK();
      if (PV == 2) { pois(0); pois(1); pois(2); pois(3); pois(4); }
      TICK(); TICK();
      transp(gw[0]);
      TICK();
      transp(gw[1]);
      TICK();
      if (PV == 3) { pois(0); pois(1); pois(2); pois(3); pois(4); SBAR(); }
      burst(1);
      SBAR();
      if (PV == 0) { pois(0); pois(1); pois(2); pois(3); pois(4); }
      TICK();
      if (PV == 1) { pois(0); pois(1); pois(2); pois(3); pois(4); }
      pois(5); pois(6); pois(7); pois(8); pois(9); pois(10);
      TICK(); TICK();
      transp(gw[2]);
      WAITV(0);
      if (ph < 2) TICK();
      transp(gw[3]);
      if (ph < 1) TICK();
      SBAR();
      gload();
      TICK();
      gfin();
      u4 sv;
      if (moe) {
        const u16* xp = x + (size_t)idx * D + 256 * ksp + 32 * kset + 8 * g4;
#pragma unroll
        for (int it = 0; it < 4; ++it) xs[it] = *(const u4*)(xp + 64 * it);
        sv = LD128(WS_ST + idx * 16);
      }
      SBAR();
      lpub();
      SBAR();
      burst(2);
      STMP(3);
      SBAR();
      if (moe) {
        if (__builtin_expect(__ballot(sv[1] != tag || sv[3] != tag) != 0ull, 0)) {
          _Pragma("unroll 1") for (unsigned it = 0; it < SPIN; ++it) { SLEEP(1); sv = LD128(WS_ST + idx * 16); if (__ballot(sv[1] != tag || sv[3] != tag) == 0ull) break; }
        }
        const float m = __uint_as_float(sv[0]), rs = __uint_as_float(sv[2]);
#pragma unroll
        for (int it = 0; it < 4; ++it) {
#pragma unroll
          for (int e = 0; e < 4; ++e) { const unsigned d = pk2(lo(xs[it][e]) - m, hi(xs[it][e]) - m); xs[it][e] = pk2(lo(d) * rs, hi(d) * rs); }
          asm volatile("" : "+v"(xs[it]));
        }
      }
    }
    __builtin_amdgcn_s_setprio(TP1);
    STMP(4);
    float sg;
    {
      const unsigned mono = cd[0] >> 16, rb = (mono & 0x8000u) ? (mono & 0x7FFFu) : (~mono & 0xFFFFu);
      sg = rbf(1.f / (1.f + __expf(-lo(rb))));
    }
    if (moe) {
      STMP(5);
      f4 acc[8];
#pragma unroll
      for (int it = 0; it < 4; ++it)
#pragma unroll
        for (int c = 0; c < 8; ++c) acc[c] = MFMA16(S8(gw[it][c]), S8(xs[it]), it ? acc[c] : (f4)(0.f));
#pragma unroll
      for (int i = 0; i < 4; ++i) {
        u4 s;
#pragma unroll
        for (int c = 0; c < 4; ++c) {
          auto r = __builtin_amdgcn_permlane32_swap(__float_as_uint(acc[c][i]), __float_as_uint(acc[c + 4][i]), false, false);
          s[c] = __float_as_uint(__uint_as_float(r[0]) + dppf<0x128>(__uint_as_float(r[1])));
        }
        if (r15 < 8) ST128(s, WS_GP + ((chg * 4 + ksp) * 2 + mw) * 2048 + (g4 >> 1) * 1024 + (4 * (g4 & 1) + i) * 128 + r15 * 16);
      }
      WAITV(0);
      if (l == 0) ST64(((u2){tag, tag2}), WS_GF + chg * 1024 + (ksp * 2 + mw) * FS);
      STMP(6);
      transp(dw[0]); transp(dw[1]);
      _Pragma("unroll 1") for (unsigned it = 0; it < SPIN; ++it) {
        u2 f = LD64(WS_GF + chunk * 1024 + (l & 7) * FS);
        if (__ballot(f[0] != tag || f[1] != tag2) == 0ull) break;
        SLEEP(GSL);
      }
      STMP(7);
      __builtin_amdgcn_s_setprio(TP2);
      u4 ha[2];
      {
        u4 pg[4][4];
#pragma unroll
        for (int ks = 0; ks < 4; ++ks)
#pragma unroll
          for (int t = 0; t < 4; ++t)
            pg[ks][t] = LD128(WS_GP + ((chunk * 4 + ks) * 4 + t) * 1024 + (4 * kset + g4) * 128 + np * 16);
        f4 gq[4];
#pragma unroll
        for (int t = 0; t < 4; ++t)
          gq[t] = (__builtin_bit_cast(f4, pg[0][t]) + __builtin_bit_cast(f4, pg[1][t])) + (__builtin_bit_cast(f4, pg[2][t]) + __builtin_bit_cast(f4, pg[3][t]));
        float hv[8];
#pragma unroll
        for (int e = 0; e < 8; ++e) {
          const float gg = rbf(gq[e >> 2][e & 3]), uu = rbf(gq[2 + (e >> 2)][e & 3]);
          hv[e] = rbf(gg / (1.f + __expf(-gg))) * uu;
        }
#pragma unroll
        for (int e = 0; e < 4; ++e) { ha[0][e] = pk2(hv[2 * e], hv[2 * e + 1]); ha[1][e] = (unsigned)__builtin_amdgcn_update_dpp(0, (int)ha[0][e], 0x128, 0xf, 0xf, false); }
      }
#pragma unroll
      for (int it = 0; it < 2; ++it)
#pragma unroll
        for (int c = 0; c < 8; ++c) acc[c] = MFMA16(S8(ha[it]), S8(dw[it][c]), it ? acc[c] : (f4)(0.f));
      if (g4 < 2) {
#pragma unroll
        for (int i = 0; i < 4; ++i) {
          const unsigned yo = WS_YP + ((chunk * 8 + 4 * g4 + i) * D + 256 * ksp + 128 * mw + 4 * r15) * 4;
          ST128(__builtin_bit_cast(u4, ((f4){acc[0][i], acc[1][i], acc[2][i], acc[3][i]})), yo);
          ST128(__builtin_bit_cast(u4, ((f4){acc[4][i], acc[5][i], acc[6][i], acc[7][i]})), yo + 256);
        }
      }
      WAITV(0);
      if (l == 0) ST64(((u2){tag, tag2}), WS_YF + (ksp * 2 + mw) * 8192 + chunk * FS);
      STMP(8);
    }
    unsigned mine = (unsigned)__ballot(!NOMOE && l < 8 && (idx >> 7) == mb && (l & 1) == mw);
    if (mine) {
      if (spare) lwait<4>(adFL, 0x30u, 2);
      _Pragma("unroll 1") for (unsigned it = 0; it < SPIN; ++it) {
        u2 f = LD64(WS_YF + (nb >> 1) * 8192 + (l < 56 ? l : 0) * FS);
        if (__ballot(f[0] != tag || f[1] != tag2) == 0ull) break;
        SLEEP(YSL);
      }
      lwait<1>(adFL, 0x0Fu, 4);
      __builtin_amdgcn_s_setprio(TP2);
      while (mine) {
        const int t = __builtin_ctz(mine); mine &= mine - 1;
        u4 yv[14];
#pragma unroll
        for (int i = 0; i < 14; ++i) yv[i] = LD128(WS_YP + (((i * 4 + g4) * 8 + t) * D + (nb >> 1) * 128 + (r15 >> 3) * 64 + (nb & 1) * 32 + (r15 & 7) * 4) * 4);
        f4 y = __builtin_bit_cast(f4, yv[0]);
#pragma unroll
        for (int i = 1; i < 14; ++i) y += __builtin_bit_cast(f4, yv[i]);
        const float gt = __int_as_float(__builtin_amdgcn_readlane(__float_as_int(sg), t));
#pragma unroll
        for (int e = 0; e < 4; ++e) {
          auto r = __builtin_amdgcn_permlane32_swap(__float_as_uint(y[e]), __float_as_uint(y[e]), false, false);
          const float a = __uint_as_float(r[0]) + __uint_as_float(r[1]);
          auto r2 = __builtin_amdgcn_permlane16_swap(__float_as_uint(a), __float_as_uint(a), false, false);
          y[e] = rbf(rbf(__uint_as_float(r2[0]) + __uint_as_float(r2[1])) * gt);
        }
        if (l < 16) *(LAS f4*)(lds + L_KVX + t * 256 + (l & 7) * 32 + (l >> 3) * 16) = y;
      }
    }
    lsig(myFL, 3u);
    STMP(9);
    STFLUSH();
    if (spare) asm volatile(".Lend_s:"); else asm volatile(".Lend_h:");
    };
    if (sp) helper(BC<true>()); else helper(BC<false>());
    return;
  }

  const int wr = w >> 1, wc = w & 1;
  unsigned tag; u2 kg; u4 sg4;
  const int srow = mb * 128 + hp * 8 + 2 * w + (l >> 5);
  {
    const int row = srow;
    u4 xa[4], rw[4];
#pragma unroll
    for (int c = 0; c < 4; ++c) xa[c] = *(const u4*)(x + row * D + (l & 31) * 8 + 256 * c);
#pragma unroll
    for (int c = 0; c < 4; ++c) rw[c] = *(const u4*)(wrt + (l & 31) * 8 + 256 * c);
    float s0 = 0, s1 = 0;
#pragma unroll
    for (int c = 0; c < 4; ++c)
#pragma unroll
      for (int e = 0; e < 4; e += 2) { unsigned x0 = xa[c][e], x1 = xa[c][e + 1]; s0 = dot2(x0, 0x3F803F80u, s0); s1 = dot2(x1, 0x3F803F80u, s1); }
    const float m = half_sum(s0 + s1) * (1.f / D);
    f2 v[16], q2 = (f2)(0.f), q3 = (f2)(0.f);
#pragma unroll
    for (int c = 0; c < 16; ++c) v[c] = (f2){lo(xa[c >> 2][c & 3]), hi(xa[c >> 2][c & 3])} - m;
#pragma unroll
    for (int c = 0; c < 16; c += 2) { q2 += v[c] * v[c]; q3 += v[c + 1] * v[c + 1]; }
    q2 += q3;
    const float mr = rbf(m);
    const float rs = 1.f / rbf(sqrtf(rbf(rbf(half_sum(q2[0] + q2[1]) * (1.f / D)) + 1e-5f)));
    float d0 = 0, d1 = 0;
#pragma unroll
    for (int c = 0; c < 16; c += 2) {
      const f2 e0 = (f2){lo(xa[c >> 2][c & 3]), hi(xa[c >> 2][c & 3])} - mr, e1 = (f2){lo(xa[c >> 2][(c & 3) + 1]), hi(xa[c >> 2][(c & 3) + 1])} - mr;
      const unsigned p0 = pk2(e0[0], e0[1]), p1 = pk2(e1[0], e1[1]);
      const f2 n0 = (f2){lo(p0), hi(p0)} * rs, n1 = (f2){lo(p1), hi(p1)} * rs;
      unsigned c0 = rw[c >> 2][c & 3], c1 = rw[c >> 2][(c & 3) + 1];
      d0 = dot2(pk2(n0[0], n0[1]), c0, d0); d1 = dot2(pk2(n1[0], n1[1]), c1, d1);
    }
    const float d = half_sum(d0 + d1);
    const unsigned rb = pk2(d, d) & 0xFFFFu;
    const unsigned mono = (rb & 0x8000u) ? (~rb & 0xFFFFu) : (rb | 0x8000u);
    tag = __builtin_amdgcn_readfirstlane(ev0) + 1u;
    kg = (u2){(mono << 16) | (unsigned)row, tag};
    sg4 = (u4){__float_as_uint(mr), tag, __float_as_uint(rs), tag};
  }
  MKWS(tag);
  if ((l & 31) == 0) { ST64(kg, WS_RG + srow * 8); ST128(sg4, WS_ST + srow * 16); }
  unsigned aad[2], bad[2];
#pragma unroll
  for (int s = 0; s < 2; ++s) aad[s] = ldsb + (wr * 64 + r15) * 128 + (((4 * s + g4) ^ ((r15 >> 1) & 7)) * 16);
#pragma unroll
  for (int nh = 0; nh < 2; ++nh) bad[nh] = ldsb + 16384 + (8 * g4 + q) * 128 + (((wc * 4 + 2 * nh + (p >> 1)) ^ (((q >> 1) << 2) | ((g4 & 1) << 1))) * 16) + (p & 1) * 8;
  f4 acc[4][6];
#pragma unroll
  for (int m = 0; m < 4; ++m)
#pragma unroll
    for (int n = 0; n < 6; ++n) { acc[m][n] = (f4)(0.f); asm volatile("" : "+v"(acc[m][n])); }
#define MF(m, n, Fq) do { if (n < 2) acc[m][n] = MFMA16(CAT(Fq.b[2 * n], Fq.b[2 * n + 1]), Fq.a[m], acc[m][n]); \
     else acc[m][n] = MFMA16(Fq.a[m], CAT(Fq.b[2 * n], Fq.b[2 * n + 1]), acc[m][n]); SBAR(); } while (0)
#define RB(G, n, vb0, vb1, SS, hf) RDTR(G.b[2 * (n) + (hf)], (((n) & 1) ? vb1 : vb0), ((n) >> 1) * 8192 + (SS) * 4096 + (hf) * 512)
#define RA(G, m, va) RD128(G.a[m], va, (m) * 2048)
#define SUB(Fq, G, va, vb0, vb1, SS, PRE) do { \
    WAITL(13); SBAR(); \
    MF(0, 0, Fq); PRE; RA(G, 0, va); SBAR(); \
    WAITL(11); SBAR(); \
    MF(1, 0, Fq); RB(G, 0, vb0, vb1, SS, 0); SBAR(); MF(2, 0, Fq); RB(G, 0, vb0, vb1, SS, 1); SBAR(); MF(3, 0, Fq); RA(G, 1, va); SBAR(); \
    WAITL(12); SBAR(); \
    MF(0, 1, Fq); RA(G, 2, va); SBAR(); MF(1, 1, Fq); RA(G, 3, va); SBAR(); MF(2, 1, Fq); RB(G, 1, vb0, vb1, SS, 0); SBAR(); MF(3, 1, Fq); RB(G, 1, vb0, vb1, SS, 1); SBAR(); \
    WAITL(8); SBAR(); \
    MF(0, 2, Fq); RB(G, 2, vb0, vb1, SS, 0); SBAR(); MF(1, 2, Fq); RB(G, 2, vb0, vb1, SS, 1); SBAR(); MF(2, 2, Fq); RB(G, 3, vb0, vb1, SS, 0); SBAR(); MF(3, 2, Fq); RB(G, 3, vb0, vb1, SS, 1); SBAR(); \
    MF(0, 3, Fq); RB(G, 4, vb0, vb1, SS, 0); SBAR(); MF(1, 3, Fq); RB(G, 4, vb0, vb1, SS, 1); SBAR(); MF(2, 3, Fq); RB(G, 5, vb0, vb1, SS, 0); SBAR(); MF(3, 3, Fq); RB(G, 5, vb0, vb1, SS, 1); SBAR(); \
    MF(0, 4, Fq); MF(1, 4, Fq); MF(2, 4, Fq); MF(3, 4, Fq); MF(0, 5, Fq); MF(1, 5, Fq); MF(2, 5, Fq); MF(3, 5, Fq); } while (0)
#define KSTEP(MODE) do { \
    if (kt == sx) { const unsigned xo = xad + buf * STG; \
      _Pragma("unroll") for (int t = 0; t < 8; ++t) { const unsigned a_ = xo ^ (t * 16); \
        asm volatile("ds_read_b64 %0, %1" : "+v"(xv[t]) : "v"(a_) : "memory"); } \
      WAITL(0); SBAR(); } \
    const int nbuf = (buf + 1) & 3; \
    const unsigned bo = buf * STG, nbo = nbuf * STG; \
    { unsigned va = aad[1] + bo, vb0 = bad[0] + bo, vb1 = bad[1] + bo; SUB(f0, f1, va, vb0, vb1, 1, ); } \
    if (MODE < 2) { \
      unsigned va = aad[0] + nbo, vb0 = bad[0] + nbo, vb1 = bad[1] + nbo; \
      SUB(f1, f0, va, vb0, vb1, 0, if (MODE == 0) { WAITL(0); BAR(); } else asm volatile("ds_write_b32 %0, %1" ::"v"(myFL), "v"(1u) : "memory"); SBAR()); \
    } else { asm volatile("ds_write_b32 %1, %2\n ds_read_b32 %0, %3\n s_waitcnt lgkmcnt(2)" : "=&v"(fl0) : "v"(myFL), "v"(2u), "v"(adFL) : "memory"); SBAR(); \
      MF(0, 2, f1); MF(1, 2, f1); MF(2, 2, f1); MF(3, 2, f1); MF(0, 3, f1); MF(1, 3, f1); MF(2, 3, f1); MF(3, 3, f1); \
      MF(0, 4, f1); MF(1, 4, f1); MF(2, 4, f1); MF(3, 4, f1); MF(0, 5, f1); MF(1, 5, f1); MF(2, 5, f1); MF(3, 5, f1); \
      MF(0, 0, f1); MF(1, 0, f1); MF(2, 0, f1); MF(3, 0, f1); MF(0, 1, f1); MF(1, 1, f1); MF(2, 1, f1); MF(3, 1, f1); } \
    buf = nbuf; } while (0)
  u2 xv[8];
#pragma unroll
  for (int t = 0; t < 8; ++t) asm volatile("" : "=v"(xv[t]));
  const int sx = (nb - rot) & 15;
  const unsigned xad = ldsb + (w * 32 + (l & 31)) * 128 + ((r15 >> 1) & 7) * 16 + (l >> 5) * 8;
  STMP(1);
  WAITL(0);
  BAR();
  STMP(2);
  WAITL(0);
  FQ f0, f1;
  { unsigned va = aad[0], vb0 = bad[0], vb1 = bad[1];
    RA(f0, 0, va); RB(f0, 0, vb0, vb1, 0, 0); RB(f0, 0, vb0, vb1, 0, 1); RA(f0, 1, va); RA(f0, 2, va); RA(f0, 3, va);
    RB(f0, 1, vb0, vb1, 0, 0); RB(f0, 1, vb0, vb1, 0, 1); RB(f0, 2, vb0, vb1, 0, 0); RB(f0, 2, vb0, vb1, 0, 1); RB(f0, 3, vb0, vb1, 0, 0); RB(f0, 3, vb0, vb1, 0, 1);
    RB(f0, 4, vb0, vb1, 0, 0); RB(f0, 4, vb0, vb1, 0, 1); RB(f0, 5, vb0, vb1, 0, 0); RB(f0, 5, vb0, vb1, 0, 1);
    WAITL(0); SBAR(); }
  int buf = 0; unsigned fl0;
#pragma unroll 1
  for (int kt = 0; kt < 14; ++kt) KSTEP(0);
  { const int kt = 14; KSTEP(1); }
  { const int kt = 15; KSTEP(2); }
  asm volatile("s_waitcnt lgkmcnt(0)" : "+v"(fl0) : : "memory");
  if (__builtin_expect(((unsigned)__ballot(fl0 < 1u) & 0xC0u) != 0u, 0)) { RETRY2(); lwait(adFL, 0xC0u, 1); }
  STMP(3);
  __builtin_amdgcn_s_setprio(2);
  const LAS float* Lm = (const LAS float*)(lds + L_M) + wr * 64;
  const LAS float* Lr = (const LAS float*)(lds + L_RS) + wr * 64;
  const LAS float* Lc = (const LAS float*)(lds + L_CS) + wc * 96;
  f4 kv[4], zt[2];
  {
    float cs[4];
#pragma unroll
    for (int n = 0; n < 4; ++n) cs[n] = Lc[32 + 16 * n + r15];
    const u4 ones = (u4)(0x3F803F80u);
#pragma unroll
    for (int t = 0; t < 2; ++t) {
      u4 kf[2], vf[2];
#pragma unroll
      for (int mm = 0; mm < 2; ++mm) {
        const int m = 2 * t + mm;
        const f4 mv = *(const LAS f4*)(Lm + m * 16 + 4 * g4), rv = *(const LAS f4*)(Lr + m * 16 + 4 * g4);
#pragma unroll
        for (int a = 0; a < 2; ++a) {
          const f4 uk = acc[m][2 + a] - mv * cs[a];
          const f4 uv = (acc[m][4 + a] - mv * cs[2 + a]) * rv;
          kf[a][2 * mm] = phi2((f2){uk[0], uk[1]}, (f2){rv[0], rv[1]}); kf[a][2 * mm + 1] = phi2((f2){uk[2], uk[3]}, (f2){rv[2], rv[3]});
          vf[a][2 * mm] = pk2(uv[0], uv[1]); vf[a][2 * mm + 1] = pk2(uv[2], uv[3]);
        }
      }
#pragma unroll
      for (int tt = 0; tt < 4; ++tt) kv[tt] = MFMA16(S8(kf[tt & 1]), S8(vf[tt >> 1]), t ? kv[tt] : (f4)(0.f));
#pragma unroll
      for (int a = 0; a < 2; ++a) zt[a] = MFMA16(S8(kf[a]), S8(ones), t ? zt[a] : (f4)(0.f));
    }
  }
  const int hd = hp * 2 + wc;
  {
    LAS f4* ks = (LAS f4*)(lds + L_KVX + w * 3072) + l;
    const LAS f4* kr = (const LAS f4*)(lds + L_KVX + (w ^ 2) * 3072) + l;
    ks[0] = wr ? kv[0] : kv[2]; ks[64] = wr ? kv[1] : kv[3]; ks[128] = wr ? zt[0] : zt[1];
    lsig(myFL, 3);
    const f4 m0 = wr ? kv[2] : kv[0], m1 = wr ? kv[3] : kv[1], mz = wr ? zt[1] : zt[0];
    lwait(adFL, 1u << (w ^ 2), 3);
    const f4 s0 = m0 + kr[0], s1 = m1 + kr[64], sz = mz + kr[128];
    u4 o; o[0] = min(pk2(s0[0], s0[1]), POIS - 1u); o[1] = pk2(s0[2], s0[3]); o[2] = pk2(s1[0], s1[1]); o[3] = pk2(s1[2], s1[3]);
    ST128(o, WS_KVP + (hd * 16 + mb) * 2048 + wr * 1024 + l * 16);
    u4 zo = __builtin_bit_cast(u4, sz); zo[0] = min(zo[0], POIS - 1u);
    if (r15 == 0) ST128(zo, WS_ZP + (hd * 16 + mb) * 128 + g4 * 32 + wr * 16);
  }
  STMP(4);
  {
    const f4 c0 = *(const LAS f4*)(Lc + 4 * g4), c1 = *(const LAS f4*)(Lc + 16 + 4 * g4);
#pragma unroll
    for (int m = 0; m < 4; ++m) {
      const float mv = Lm[m * 16 + r15], rv = Lr[m * 16 + r15];
      const f4 u0 = acc[m][0] - c0 * mv, u1 = acc[m][1] - c1 * mv;
      u4 pf;
      pf[0] = phi2((f2){u0[0], u0[1]}, (f2)(rv)); pf[1] = phi2((f2){u0[2], u0[3]}, (f2)(rv));
      pf[2] = phi2((f2){u1[0], u1[1]}, (f2)(rv)); pf[3] = phi2((f2){u1[2], u1[3]}, (f2)(rv));
      *(LAS u4*)(lds + L_PQ + wr * 8192 + wc * 4096 + m * 1024 + l * 16) = pf;
    }
  }
  lsig(myFL, 4);
  __builtin_amdgcn_s_setprio(0);
  STMP(5);
  const int trow = mb * 128 + w * 32 + (l & 31);
  u2 oh;
  const unsigned flo = (2 * PAIRAT(r15) + wc) * 128;
  const char* fb = uptr(wsx + WS_OH + mb * 4096);
  asm volatile("global_load_dwordx2 %0, %1, %2 sc1" : "=v"(oh) : "v"(flo), "s"(fb) : "memory");
  int sel = -1;
  {
    const u4 s0 = *(const LAS u4*)(lds + L_SEL), s1 = *(const LAS u4*)(lds + L_SEL + 16);
#pragma unroll
    for (int i = 0; i < 8; ++i) { const int v = (int)(i < 4 ? s0[i & 3] : s1[i & 3]); sel = v == trow ? i : sel; }
  }
  const bool anysel = __ballot(sel >= 0) != 0ull;
  f16v oc[2][2];
  {
    const float m = *(const LAS float*)(lds + L_M + (w * 32 + (l & 31)) * 4), rs = *(const LAS float*)(lds + L_RS + (w * 32 + (l & 31)) * 4);
#pragma unroll
    for (int t = 0; t < 8; ++t) {
      const f4 xf = (f4){lo(xv[t][0]), hi(xv[t][0]), lo(xv[t][1]), hi(xv[t][1])};
      const f4 y = (xf - m) * rs + xf;
#pragma unroll
      for (int i = 0; i < 4; ++i) oc[0][t >> 2][(t & 3) * 4 + i] = y[i];
    }
    asm volatile("" : "+v"(oc[0][0]), "+v"(oc[0][1]));
  }
  unsigned wa[2];
#pragma unroll
  for (int t = 0; t < 2; ++t) wa[t] = ldsb + L_WO + wc * 4096 + (4 * (g4 >> 1) + q) * 128 + (((4 * t + 2 * (g4 & 1) + (p >> 1)) ^ ((q >> 1) << 2)) * 16) + (p & 1) * 8;
  u4 a[RD][4];
#pragma unroll
  for (int k = 0; k < RD; ++k) asm volatile("" : "=v"(a[k][0]), "=v"(a[k][1]), "=v"(a[k][2]), "=v"(a[k][3]));
  const char* ob = uptr(wsx + WS_OF + mb * 262144 + wc * 8192 + wr * 4096);
  const unsigned ov0 = l * 16 + wc * 2048, ov1 = ov0 ^ 2048;
  STMP(6);
  lwait<1>(adFL, 0x30u, 1);
  STMP(7);
  WAITV(0);
  asm volatile("" : "+v"(oh));
  unsigned seen = (unsigned)__ballot(oh[0] == tag), go = 0;
  auto need = [&](unsigned mask, bool first) {
    if (__builtin_expect((go & mask) != mask, 0)) {
      _Pragma("unroll 1") for (unsigned it = 0; it < SPIN; ++it) {
        bool poll = (seen & mask) != mask;
        if (first && seen != ~0u && !poll) poll = ((unsigned)__ballot(oh[1] - NOW() - 80u < 1000u) & mask) != 0u;
        if (poll) {
          if (first) SLEEP(4);
          asm volatile("global_load_dwordx2 %0, %1, %2 sc1\n s_waitcnt vmcnt(0)" : "+v"(oh) : "v"(flo), "s"(fb) : "memory");
          seen = (unsigned)__ballot(oh[0] == tag);
        }
        go = seen & (unsigned)__ballot(oh[1] - NOW() - 1u >= 1000u);
        if ((go & mask) == mask) break;
        SLEEP(1);
      }
    }
  };
#define OX 0
#define OL1(pos, i) do { if (OX == 2 && (pos) >= RD) break; \
    asm volatile("global_load_dwordx4 %0, %1, %2 offset:%3 sc1" : "+v"(a[(pos) % RD][i]) : "v"((i) & 1 ? ov1 : ov0), "s"(ob + (OX == 1 ? ((pos) & 1) : PAIRAT(pos)) * 16384), "i"(((i) >> 1) * 1024) : "memory"); } while (0)
#define OLD(pos) do { OL1(pos, 0); OL1(pos, 1); OL1(pos, 2); OL1(pos, 3); } while (0)
  auto omax = [&](u4(&v)[4]) {
    asm volatile("" : "+v"(v[0]), "+v"(v[1]), "+v"(v[2]), "+v"(v[3]));
    return max(umax3(v[0][0], v[1][0], v[2][0]), v[3][0]);
  };
#define OFIX(pos, m) do { \
    if (__builtin_expect(__ballot((m) == POIS) != 0ull, 0)) { RETRY(); _Pragma("unroll 1") for (unsigned it = 0; it < SPIN; ++it) { SLEEP(4); OLD(pos); WAITV(0); if (__ballot(omax(a[(pos) % RD]) == POIS) == 0ull) break; } } } while (0)
#define OS1(i)
#define OS2(i)
  need(1u, true);
  if (OWN) lwait<1>(adFL, 0x30u, 2);
  __builtin_amdgcn_s_setprio(2);
  STMP(15);
  OS1(0); OLD(0); STMP(14);
#pragma unroll
  for (int i = 1; i < RD; ++i) { need(1u << i, false); OS1(i); OLD(i); }
  STMP(10);
  s4 b[8];
#define ORD(pos, f) do { if ((f) < 2 && (pos) == 8) { wa[(f) & 1] += 65536; asm volatile("" : "+v"(wa[(f) & 1])); } \
    RDTR(b[2 * (f)], wa[(f) & 1], ((pos) & 7) * 8192 + ((f) >> 1) * 1024); RDTR(b[2 * (f) + 1], wa[(f) & 1], ((pos) & 7) * 8192 + ((f) >> 1) * 1024 + 2048); } while (0)
#define OMF(pos, f, r) do { if ((f) < 2 && (pos) == 0 && (r) == 1) asm volatile("v_mfma_f32_32x32x16_bf16 %0, %1, %2, 0" : "=&v"(oc[r][(f) & 1]) \
      : "v"(CAT(b[2 * (f)], b[2 * (f) + 1])), "v"(a[(pos) % RD][((f) & 2) + (r)])); \
    else asm volatile("v_mfma_f32_32x32x16_bf16 %0, %1, %2, %0" : "+v"(oc[r][(f) & 1]) \
      : "v"(CAT(b[2 * (f)], b[2 * (f) + 1])), "v"(a[(pos) % RD][((f) & 2) + (r)])); \
    SBAR(); if ((r) == 1 && (pos) < 15) ORD(((pos) + 1) & 15, f); SBAR(); } while (0)
#define ONX(pos) ((pos) > 0 && (pos) + RD - 1 < 16)
#define ONL(pos, i) do { if (ONX(pos)) OL1(((pos) + RD - 1) & 15, i); SBAR(); } while (0)
#define OWL(pos, f) do { WAITL((pos) < 15 ? 6 : 6 - 2 * (f)); SBAR(); } while (0)
#define OHEAD(pos) do { unsigned m_ = 0; \
    OWL(pos, 0); OMF(pos, 0, 0); if (ONX(pos)) { need(1u << (((pos) + RD - 1) & 15), false); OS1(((pos) + RD - 1) & 15); } SBAR(); \
    OMF(pos, 0, 1); ONL(pos, 0); \
    OWL(pos, 1); OMF(pos, 1, 0); ONL(pos, 1); \
    OMF(pos, 1, 1); ONL(pos, 2); \
    OWL(pos, 2); OMF(pos, 2, 0); ONL(pos, 3); \
    if ((pos) == 4) { asm volatile("ds_write_b32 %0, %1" ::"v"(myFL), "v"(5u) : "memory"); SBAR(); } \
    if ((pos) == 13) { asm volatile("ds_read_b32 %0, %1" : "=v"(fl3) : "v"(adFL) : "memory"); SBAR(); } \
    OMF(pos, 2, 1); \
    OWL(pos, 3); OMF(pos, 3, 0); \
    if ((pos) < 15) { WAITV(4 * ((pos) + RD - 1 < 16 ? RD - 2 : 14 - (pos))); m_ = omax(a[((pos) + 1) % RD]); asm volatile("" : "+v"(m_)); SBAR(); } \
    OMF(pos, 3, 1); \
    if ((pos) < 15) { OFIX(((pos) + 1) & 15, m_); OS2(((pos) + 1) & 15); } SBAR(); } while (0)
  unsigned fl3;
  ORD(0, 0); ORD(0, 1); ORD(0, 2); ORD(0, 3);
  { WAITV(4 * (RD - 1)); const unsigned m_ = omax(a[0]); OFIX(0, m_); OS2(0); SBAR(); }
  OHEAD(0); OHEAD(1); STMP(11); OHEAD(2); OHEAD(3); OHEAD(4); OHEAD(5); OHEAD(6); OHEAD(7); STMP(12);
  OHEAD(8); OHEAD(9); STMP(13); OHEAD(10); OHEAD(11); OHEAD(12); OHEAD(13); OHEAD(14);
  f4 xi[8];
  {
    asm volatile("" : "+v"(fl3));
    if (__builtin_expect(((unsigned)__ballot(fl3 < 5u) >> (w ^ 2)) & 1u, 0)) lwait(adFL, 1u << (w ^ 2), 5);
    const unsigned xw = ldsb + L_WO + (4 * wr + wc) * 4096 + l * 16, xr = xw ^ 4096;
#define XW(t) asm volatile("ds_write_b128 %0, %1 offset:%2" ::"v"(xw), "v"(__builtin_shufflevector(oc[1][(t) >> 2], oc[1][(t) >> 2], ((t) & 3) * 4, ((t) & 3) * 4 + 1, ((t) & 3) * 4 + 2, ((t) & 3) * 4 + 3)), \
      "i"(((t) >> 2) * 8192 + ((t) & 3) * 1024) : "memory")
    SBAR();
    OWL(15, 0); OMF(15, 0, 1); OWL(15, 1); OMF(15, 1, 1); OWL(15, 2); OMF(15, 2, 1); OWL(15, 3); OMF(15, 3, 1);
    OMF(15, 0, 0); OMF(15, 1, 0); XW(0); XW(1); XW(2); XW(3); SBAR();
    OMF(15, 2, 0); XW(4); XW(5); XW(6); XW(7); SBAR();
    OMF(15, 3, 0);
    asm volatile("ds_write_b32 %0, %1" ::"v"(myFL), "v"(6u) : "memory");
    _Pragma("unroll 1") for (unsigned it = 0; it < SPIN; ++it) {
      unsigned v;
      asm volatile("ds_read_b32 %0, %1" : "=v"(v) : "v"(adFL) : "memory");
#pragma unroll
      for (int t = 0; t < 8; ++t) RD128(xi[t], xr, (t >> 2) * 8192 + (t & 3) * 1024);
      WAITL(0);
      asm volatile("" : "+v"(v), "+v"(xi[0]), "+v"(xi[1]), "+v"(xi[2]), "+v"(xi[3]), "+v"(xi[4]), "+v"(xi[5]), "+v"(xi[6]), "+v"(xi[7]));
      if ((((unsigned)__ballot(v < 6u) >> (w ^ 1)) & 1u) == 0u) break;
    }
    asm volatile("" : "+v"(oc[0][0]), "+v"(oc[0][1]));
#pragma unroll
    for (int t = 0; t < 8; ++t)
#pragma unroll
      for (int i = 0; i < 4; ++i) xi[t][i] += oc[0][t >> 2][(t & 3) * 4 + i];
  }
  STMP(8);
  if (anysel) {
    lwait(adFL, 0xC0u, 3);
    if (sel >= 0) {
#pragma unroll
      for (int t = 0; t < 8; ++t) xi[t] += *(const LAS f4*)(lds + L_KVX + sel * 256 + (8 * t + 4 * (l >> 5)) * 4);
    }
  }
  const __amdgpu_buffer_rsrc_t outr = __builtin_amdgcn_make_buffer_rsrc(out, 0, 2048 * D * 2, 0x00020000);
  if (FULL) {
    LAS char* tb = (LAS char*)lds + L_PQ + w * 4096;
#pragma unroll
    for (int t = 0; t < 8; ++t) *(LAS u2*)(tb + (l & 31) * 128 + ((t ^ (l & 7)) * 16) + (l >> 5) * 8) = (u2){pk2(xi[t][0], xi[t][1]), pk2(xi[t][2], xi[t][3])};
#pragma unroll
    for (int i = 0; i < 4; ++i) {
      const u4 v = *(const LAS u4*)(tb + i * 1024 + (l >> 3) * 128 + (((l ^ (l >> 3)) & 7) * 16));
      __builtin_amdgcn_raw_buffer_store_b128(v, outr, (mb * 128 + w * 32 + i * 8 + (l >> 3)) * 2048 + nb * 128 + (l & 7) * 16, 0, 16);
    }
  } else {
#pragma unroll
  for (int hh = 0; hh < 4; ++hh) {
    const f4 ta = xi[2 * hh], tb = xi[2 * hh + 1];
    auto r0 = __builtin_amdgcn_permlane32_swap(pk2(ta[0], ta[1]), pk2(tb[0], tb[1]), false, false);
    auto r1 = __builtin_amdgcn_permlane32_swap(pk2(ta[2], ta[3]), pk2(tb[2], tb[3]), false, false);
    __builtin_amdgcn_raw_buffer_store_b128(((u4){r0[0], r1[0], r0[1], r1[1]}), outr, (trow * D + nb * 64 + 16 * hh + 8 * (l >> 5)) * 2, 0, 16);
  }
  }
  if (g == 0 && w == 0 && l == 0) __builtin_amdgcn_raw_buffer_store_b32(tag, wse, WS_E, 0, 16);
  STMP(9);
  STFLUSH();
  asm volatile(".Lend_c:");
  return;
  }
  {
    const int lw = w - 4;
    unsigned ev;
    asm volatile("global_load_dword %0, %1, %2 sc1" : "=v"(ev) : "v"(0u), "s"(ws) : "memory");
    unsigned vf[20], vfw[4];
#pragma unroll
    for (int i = 0; i < 8; ++i) { const int row = 8 * (lw * 8 + i) + (l >> 3); vf[i] = row * 2048 + (((l & 7) ^ ((row >> 1) & 7)) * 16) + (3 - (i & 3)) * 1024; }
#pragma unroll
    for (int i = 0; i < 12; ++i) {
      const int pc = lw * 12 + i, row = (pc & 7) * 8 + (l >> 3);
      vf[8 + i] = row * 6144 + (pc >> 3) * 2048 + (((l & 7) ^ ((((row >> 1) & 1) << 2) | (((row >> 3) & 1) << 1))) * 16) + (3 - (i & 3)) * 1024;
    }
#pragma unroll
    for (int i = 0; i < 4; ++i) vfw[i] = (8 * i + (l >> 3)) * 2048 + (((l & 7) ^ (((l >> 4) & 1) << 2)) * 16) + (3 - i) * 1024;
    const char* Ab = uptr((const char*)x + (size_t)mb * 128 * 2048) - 3072;
    const char* Bb = uptr((const char*)wqkv + hp * 128) - 3072;
    const unsigned mba = ldsb + lw * 8192, mbb = ldsb + 16384 + lw * 12288;
#define GL4(v, sb, mm, MO) do { asm volatile("s_add_i32 m0, %2, %3\n s_nop 0\n global_load_lds_dwordx4 %0, %1" ::"v"((v)[0]), "s"(sb), "s"(mm), "i"(MO) : "memory", "scc"); \
      GLI((v)[1], sb, 1024); GLI((v)[2], sb, 2048); GLI((v)[3], sb, 3072); } while (0)
    auto issue = [&](int s, int u0, int u1) {
      const int kt = (s + rot) & 15;
      const char* sa = Ab + kt * 128; const char* sb = Bb + kt * (64 * 6144);
      const unsigned so = (s & 3) * STG, ma = mba + so, mq = mbb + so;
#pragma unroll
      for (int u = u0; u < u1; ++u) { if (u < 2) GL4(vf + 4 * u, sa, ma, u * 4096); else GL4(vf + 4 * u, sb, mq, (u - 2) * 4096); }
    };
    int tk = 0;
#define LBAR() do { BST(tk + 1); BAR(); BST(tk + 21); ++tk; } while (0)
    issue(0, 0, 5); issue(1, 0, 5);
    BST(60);
    WAITV(20); BST(0); BAR(); BST(20);
    asm volatile("" : "+v"(ev));
    const unsigned tag = __builtin_amdgcn_readfirstlane(ev) + 1u;
    MKWS(tag);
    issue(2, 0, 5);
    int s0 = 3;
#pragma unroll 1
    for (; s0 < (sp ? 16 : 6 + ph); ++s0) { issue(s0, 0, 5); BST(tk + 40); WAITV(40); LBAR(); }
#pragma unroll 1
    for (; s0 < 16; s0 += 5) {
      WAITV(20); LBAR();
      WAITV(0); LBAR();
      issue(s0, 0, 5); issue(s0 + 1, 0, U1); BST(tk + 40); WAITV(4 * U1); LBAR();
      issue(s0 + 1, U1, 5); issue(s0 + 2, 0, 5);
      if (s0 + 3 >= 16) break;
      issue(s0 + 3, 0, U2); BST(tk + 40); WAITV(20 + 4 * U2); LBAR();
      issue(s0 + 3, U2, 5);
      if (s0 + 4 >= 16) break;
      issue(s0 + 4, 0, 5); BST(tk + 40); WAITV(40); LBAR();
    }
    int wq = lw;
    auto wo_upto = [&](int lim) {
      _Pragma("unroll 1") for (; wq < lim; wq += 2) { const char* wp = wob - 3072 + HEADAT(wq) * 65536; const unsigned wm = ldsb + wq * 4096; GL4(vfw, wp, wm, L_WO); }
    };
    asm volatile("ds_write_b32 %0, %1" ::"v"(adFL), "v"(0u) : "memory");
    WAITV(0); WAITL(0); LBAR();
    if (lw == 0) ST64(((u2){tag, NOW() + KD}), l == 0 ? WS_HK + (hp * 16 + mb) * 128 : WS_SZ);
    wo_upto(12);
    BST(15); lwait<1>(adFL, 0x0Fu, 1); BST(35);
    wo_upto(22);
    BST(16); lwait<1>(adFL, 0x0Fu, 2); BST(36);
    wo_upto(32);
    STMP(1);
    const int hd = hp * 2 + lw;
    u4 sel[2];
#pragma unroll
    for (int e = 0; e < 2; ++e) {
      const unsigned v = (r15 >> 2) == g4 ? (((r15 & 3) == 2 * e ? 0x3F80u : 0u) | ((r15 & 3) == 2 * e + 1 ? 0x3F800000u : 0u)) : 0u;
      sel[0][e] = v; sel[0][2 + e] = 0u; sel[1][e] = 0u; sel[1][2 + e] = v;
    }
    u4 pv[16][2], zv0, zv1, pq[8];
    const char* kvb = uptr(wsx + WS_KVP + (size_t)hd * 32768);
    const char* zb = uptr(wsx + WS_ZP + (size_t)hd * 2048);
    const char* hb = uptr(wsx + WS_HK + hp * 2048);
    const unsigned lv16 = l * 16, zoff = r15 * 128 + g4 * 32;
    const unsigned pa = ldsb + L_PQ + lw * 4096 + l * 16, omask = lw ? 0x0Au : 0x05u;
    u2 hk;
    _Pragma("unroll 1") for (unsigned it = 0; it < SPIN; ++it) {
      asm volatile("global_load_dwordx2 %0, %1, %2 sc1\n s_waitcnt vmcnt(0)" : "=v"(hk) : "v"(r15 * 128), "s"(hb) : "memory");
      if (it == 0) lsig(myFL, 1);
      if (__ballot(hk[0] != tag) == 0ull) break;
      SLEEP(2);
    }
    __builtin_amdgcn_s_setprio(3);
    STMP(2);
    const unsigned t0 = NOW();
    unsigned rel = hk[1] - t0; rel = rel < 1000u ? rel : 0u;
    rel = max(rel, (unsigned)__builtin_amdgcn_update_dpp(0, (int)rel, 0xB1, 0xf, 0xf, false));
    rel = max(rel, (unsigned)__builtin_amdgcn_update_dpp(0, (int)rel, 0x4E, 0xf, 0xf, false));
    int gx;
    { const unsigned r0 = __builtin_amdgcn_readlane(rel, 0), r1 = __builtin_amdgcn_readlane(rel, 4), r2 = __builtin_amdgcn_readlane(rel, 8), r3 = __builtin_amdgcn_readlane(rel, 12);
      gx = max(r0, r1) > max(r2, r3) ? (r0 > r1 ? 3 : 2) : (r2 > r3 ? 1 : 0);
      if (OHP) ST64(((u2){tag, t0 + max(max(r0, r1), max(r2, r3)) + OD}), l == 0 ? WS_OH + (mb * 32 + hd) * 128 : WS_SZ); }
#define GLD(gq) do { _Pragma("unroll") for (int i = 0; i < 4; ++i) { const char* sb = kvb + (((gq) ^ gx) * 4 + (i & 2)) * 2048; \
      asm volatile("global_load_dwordx4 %0, %1, %2 offset:%3 sc1" : "=v"(pv[(gq) * 4 + i][0]) : "v"(lv16), "s"(sb), "i"((i & 1) * 2048) : "memory"); \
      asm volatile("global_load_dwordx4 %0, %1, %2 offset:%3 sc1" : "=v"(pv[(gq) * 4 + i][1]) : "v"(lv16), "s"(sb), "i"((i & 1) * 2048 + 1024) : "memory"); } } while (0)
#define ZLD() do { asm volatile("global_load_dwordx4 %0, %1, %2 sc1" : "=v"(zv0) : "v"(zoff), "s"(zb) : "memory"); \
      asm volatile("global_load_dwordx4 %0, %1, %2 offset:16 sc1" : "=v"(zv1) : "v"(zoff), "s"(zb) : "memory"); } while (0)
#pragma unroll
    for (int gq = 0; gq < 4; ++gq) {
      const unsigned rq = __builtin_amdgcn_readlane(rel, 4 * (gq ^ gx));
      _Pragma("unroll 1") for (unsigned it = 0; it < SPIN; ++it) if (NOW() - t0 >= rq) break;
      if (gq == 3) ZLD();
      GLD(gq);
    }
    if (!OHP) ST64(((u2){tag, NOW() + OD}), l == 0 ? WS_OH + (mb * 32 + hd) * 128 : WS_SZ);
    STMP(5);
    lwait(adFL, omask, 4);
#pragma unroll
    for (int i = 0; i < 8; ++i) RD128(pq[i], pa, (i >> 2) * 8192 + (i & 3) * 1024);
#pragma unroll
    for (int gq = 0; gq < 4; ++gq) {
      SBAR(); WAITV((gq == 0 ? 27 : gq == 1 ? 19 : gq == 2 ? 11 : 1) - OHP);
      _Pragma("unroll 1") for (unsigned it = 0; it < SPIN; ++it) {
#pragma unroll
        for (int i = 0; i < 4; ++i) asm volatile("" : "+v"(pv[gq * 4 + i][0]), "+v"(pv[gq * 4 + i][1]));
        unsigned m = umax3(pv[gq * 4][0][0], pv[gq * 4][1][0], pv[gq * 4 + 1][0][0]);
        m = umax3(m, pv[gq * 4 + 1][1][0], pv[gq * 4 + 2][0][0]);
        m = umax3(m, pv[gq * 4 + 2][1][0], pv[gq * 4 + 3][0][0]);
        m = max(m, pv[gq * 4 + 3][1][0]);
        if (gq == 3) { asm volatile("" : "+v"(zv0), "+v"(zv1)); m = umax3(m, zv0[0], zv1[0]); }
        if (__ballot(m == POIS) == 0ull) break;
        RETRY();
        SLEEP(2);
        if (gq == 3) ZLD();
        GLD(gq);
        WAITV(0);
      }
      f4 ga[4];
#pragma unroll
      for (int i = 0; i < 4; ++i)
#pragma unroll
        for (int t = 0; t < 4; ++t) ga[t] = MFMA16(S8(sel[t & 1]), S8(pv[gq * 4 + i][t >> 1]), i ? ga[t] : (f4)(0.f));
#pragma unroll
      for (int t = 0; t < 4; ++t) pv[gq * 4 + (t >> 1)][t & 1] = __builtin_bit_cast(u4, ga[t]);
    }
    STMP(3);
    WAITL(0);
#pragma unroll
    for (int i = 0; i < 8; i += 2) asm volatile("" : "+v"(pq[i]), "+v"(pq[i + 1]));
    u4 kA[2], zA;
    {
      float z[8];
#pragma unroll
      for (int e = 0; e < 4; ++e) { z[e] = __uint_as_float(zv0[e]); z[4 + e] = __uint_as_float(zv1[e]); }
#define ZR(n) "v_add_f32_dpp %0, %0, %0 row_ror:" #n " row_mask:0xf bank_mask:0xf\n v_add_f32_dpp %1, %1, %1 row_ror:" #n " row_mask:0xf bank_mask:0xf\n" \
              "v_add_f32_dpp %2, %2, %2 row_ror:" #n " row_mask:0xf bank_mask:0xf\n v_add_f32_dpp %3, %3, %3 row_ror:" #n " row_mask:0xf bank_mask:0xf\n" \
              "v_add_f32_dpp %4, %4, %4 row_ror:" #n " row_mask:0xf bank_mask:0xf\n v_add_f32_dpp %5, %5, %5 row_ror:" #n " row_mask:0xf bank_mask:0xf\n" \
              "v_add_f32_dpp %6, %6, %6 row_ror:" #n " row_mask:0xf bank_mask:0xf\n v_add_f32_dpp %7, %7, %7 row_ror:" #n " row_mask:0xf bank_mask:0xf\n"
      asm volatile("s_nop 1\n" ZR(8) ZR(4) ZR(2) ZR(1) : "+v"(z[0]), "+v"(z[1]), "+v"(z[2]), "+v"(z[3]), "+v"(z[4]), "+v"(z[5]), "+v"(z[6]), "+v"(z[7]));
#pragma unroll
      for (int e = 0; e < 4; ++e) zA[e] = pk2(z[2 * e], z[2 * e + 1]);
    }
    float inv[8];
#pragma unroll
    for (int i = 0; i < 8; ++i) { f4 dn = MFMA16(S8(zA), S8(pq[i]), (f4)(0.f)); inv[i] = dn[0]; }
#pragma unroll
    for (int t = 0; t < 4; ++t) {
#define PF(gq) __builtin_bit_cast(f4, pv[(gq) * 4 + (t >> 1)][t & 1])
      f4 s = (PF(0) + PF(1)) + (PF(2) + PF(3));
      kA[t >> 1][(t & 1) * 2] = pk2(s[0], s[1]); kA[t >> 1][(t & 1) * 2 + 1] = pk2(s[2], s[3]);
    }
    f4 o0[8], o1[8];
#pragma unroll
    for (int i = 0; i < 8; ++i) {
      o0[i] = MFMA16(S8(kA[0]), S8(pq[i]), (f4)(0.f)); o1[i] = MFMA16(S8(kA[1]), S8(pq[i]), (f4)(0.f));
      inv[i] = __builtin_amdgcn_rcpf(inv[i] + 1e-6f);
    }
#pragma unroll
    for (int i = 0; i < 8; ++i) {
      o0[i] *= inv[i]; o1[i] *= inv[i];
      u4 o; o[0] = min(pk2(o0[i][0], o0[i][1]), POIS - 1u); o[1] = pk2(o0[i][2], o0[i][3]); o[2] = pk2(o1[i][0], o1[i][1]); o[3] = pk2(o1[i][2], o1[i][3]);
      ST128(o, WS_OF + (mb * 32 + hd) * 8192 + (i >> 1) * 2048 + (i & 1) * 256 + g4 * 512 + r15 * 16);
    }
    lsig(myFL, 2);
    STMP(4);
    STFLUSH();
    asm volatile(".Lend_l:");
  }

}

extern "C" void kernel_launch(void* const* d_in, const int* in_sizes, int n_in, void* d_out, int out_size,
                              void* d_ws, size_t ws_size, hipStream_t stream) {
  (void)hipFuncSetAttribute((const void*)fused, hipFuncAttributeMaxDynamicSharedMemorySize, LDS_SZ);
  fused<<<256, 512, LDS_SZ, stream>>>((const u16*)d_in[0], (const u16*)d_in[1], (const u16*)d_in[2], (const u16*)d_in[4],
                                      (const u16*)d_in[5], (const u16*)d_in[6], (const u16*)d_in[7], (char*)d_ws, (u16*)d_out);
}
```

```cpp
#define EVW 0
#define EVG b_==0
#define EVX 0
#include <hip/hip_runtime.h>
#include <stdint.h>

typedef unsigned short u16;
typedef short s4 __attribute__((ext_vector_type(4)));
typedef short s8 __attribute__((ext_vector_type(8)));
typedef float f4 __attribute__((ext_vector_type(4)));
typedef float f16v __attribute__((ext_vector_type(16)));
typedef float f2 __attribute__((ext_vector_type(2)));
typedef unsigned u4 __attribute__((ext_vector_type(4)));
typedef unsigned u2 __attribute__((ext_vector_type(2)));
typedef __bf16 bf2 __attribute__((ext_vector_type(2)));
#define LAS __attribute__((address_space(3)))
#define DEV __device__ __forceinline__

constexpr int D = 1024, F = 3584;
#define NOMOE 0
#define KD 290
#define OD 190
#define OHP 1
#define FS 128
#define GSL 8
#define YSL 16
#define TP1 0
#define TP2 3
#define TLBW 0
#define FULL 1
#define HPRIO 1
#define OWN 1
#define NOPOIS 0
#define CAP 7
#define RD 4
#define DYN 1
#define JX 0
#define NOSKIP 0
#define GRP 8
#define PV 4
#define U1 4
#define U2 2
#define ROT 0

constexpr unsigned WS_E = 0;
constexpr unsigned WS_YF = 524288;
constexpr unsigned WS_CAND = 12288;
constexpr unsigned WS_GF = 589824;
constexpr unsigned WS_RG = 49152;
constexpr unsigned WS_ST = 65536;
constexpr unsigned WS_HK = 98304;
constexpr unsigned WS_ZP = 131072;
constexpr unsigned WS_CS = 196608;
constexpr unsigned WS_OH = 458752;
constexpr unsigned WS_KVP = 1u << 20;
constexpr unsigned WS_GP = 2u << 20;
constexpr unsigned WS_YP = 3u << 20;
constexpr unsigned WS_OF = 5u << 20;
constexpr unsigned WS_SZ = 9u << 20;
#define NR 4
#define RGO(tag) ((64u << 20) + ((tag) & (NR - 1u)) * (10u << 20))
#define MKWS(tag) char* const wsx = ws + RGO(tag); const __amdgpu_buffer_rsrc_t wsr = __builtin_amdgcn_make_buffer_rsrc(wsx, 0, WS_SZ, 0x00020000)

constexpr int STG = 40960;
constexpr int L_KVX = 0;
constexpr int L_PQ = 12288;
constexpr int L_M = 28672, L_RS = L_M + 512, L_CS = L_RS + 512, L_SEL = L_CS + 768, L_FL = L_SEL + 64;
constexpr int L_WO = 32768;
constexpr int LDS_SZ = 163840;
constexpr unsigned POIS = 0xFFFFFFFFu;

DEV unsigned pk2(float a, float b) { bf2 v; v[0] = (__bf16)a; v[1] = (__bf16)b; return __builtin_bit_cast(unsigned, v); }
DEV float lo(unsigned u) { return __uint_as_float(u << 16); }
DEV float hi(unsigned u) { return __uint_as_float(u & 0xFFFF0000u); }
DEV float rbf(float a) { return lo(pk2(a, a)); }
DEV float dot2(unsigned a, unsigned b, float c) { return __builtin_amdgcn_fdot2_f32_bf16(__builtin_bit_cast(bf2, a), __builtin_bit_cast(bf2, b), c, false); }
#define RD128(dst, addr, OFF) asm volatile("ds_read_b128 %0, %1 offset:%2" : "=v"(dst) : "v"(addr), "i"(OFF) : "memory")
#define RDTR(dst, addr, OFF) asm volatile("ds_read_b64_tr_b16 %0, %1 offset:%2" : "=v"(dst) : "v"(addr), "i"(OFF) : "memory")
#define SBAR() __builtin_amdgcn_sched_barrier(0)
#define CAT(x, y) __builtin_shufflevector(x, y, 0, 1, 2, 3, 4, 5, 6, 7)
#define WAITV(n) asm volatile("s_waitcnt vmcnt(%0)" ::"i"(n) : "memory")
#define WAITL(n) asm volatile("s_waitcnt lgkmcnt(%0)" ::"i"(n) : "memory")
#define MEMB() asm volatile("" ::: "memory")
#define SLEEP(n) asm volatile("s_sleep %0" ::"i"(n) : "memory")
#define BAR() asm volatile("s_barrier" ::: "memory")
#define GL0(vo, sb, m0v) asm volatile("s_mov_b32 m0, %2\n s_nop 0\n global_load_lds_dwordx4 %0, %1" ::"v"(vo), "s"(sb), "s"(m0v) : "memory")
#define GLI(vo, sb, OFF) asm volatile("global_load_lds_dwordx4 %0, %1 offset:%2" ::"v"(vo), "s"(sb), "i"(OFF) : "memory")
#define MFMA16(a, b, c) __builtin_amdgcn_mfma_f32_16x16x32_bf16(a, b, c, 0, 0, 0)
#define S8(x) __builtin_bit_cast(s8, x)
#define LD128(off) __builtin_amdgcn_raw_buffer_load_b128(wsr, (off), 0, 16)
#define LD64(off) __builtin_amdgcn_raw_buffer_load_b64(wsr, (off), 0, 16)
#define ST128(v, off) __builtin_amdgcn_raw_buffer_store_b128((v), wsr, (off), 0, 16)
#define ST64(v, off) __builtin_amdgcn_raw_buffer_store_b64((v), wsr, (off), 0, 16)
#define OFA(i) (WS_OF + ((mb * 32 + hd) * 8 + (i)) * 1024 + l * 16)
#define NOW() ((unsigned)__builtin_amdgcn_s_memrealtime())
constexpr unsigned SPIN = 1u << 21;
DEV const char* uptr(const char* p) {
  unsigned long long v = (unsigned long long)p;
  unsigned lo_ = __builtin_amdgcn_readfirstlane((unsigned)v), hi_ = __builtin_amdgcn_readfirstlane((unsigned)(v >> 32));
  return (const char*)(((unsigned long long)hi_ << 32) | lo_);
}
DEV void lsig(unsigned addr, unsigned val) { asm volatile("s_waitcnt lgkmcnt(0)\n ds_write_b32 %0, %1" ::"v"(addr), "v"(val) : "memory"); }
template <int SL = 0> DEV void lwait(unsigned addr, unsigned mask, unsigned target) {
  _Pragma("unroll 1") for (unsigned it = 0; it < SPIN; ++it) {
    unsigned v;
    asm volatile("ds_read_b32 %0, %1\n s_waitcnt lgkmcnt(0)" : "=v"(v) : "v"(addr) : "memory");
    if (((unsigned)__ballot(v < target) & mask) == 0u) break;
    if (SL) SLEEP(SL);
  }
}
DEV unsigned umax3(unsigned a, unsigned b, unsigned c) { return max(max(a, b), c); }
template <int C> DEV float dppf(float v) { return __int_as_float(__builtin_amdgcn_update_dpp(0, __float_as_int(v), C, 0xf, 0xf, false)); }
DEV unsigned wave_max(unsigned v) {
  v = max(v, (unsigned)__builtin_amdgcn_update_dpp(0, (int)v, 0x111, 0xf, 0xf, false));
  v = max(v, (unsigned)__builtin_amdgcn_update_dpp(0, (int)v, 0x112, 0xf, 0xf, false));
  v = max(v, (unsigned)__builtin_amdgcn_update_dpp(0, (int)v, 0x114, 0xf, 0xf, false));
  v = max(v, (unsigned)__builtin_amdgcn_update_dpp(0, (int)v, 0x118, 0xf, 0xf, false));
  v = max(v, (unsigned)__builtin_amdgcn_update_dpp(0, (int)v, 0x142, 0xa, 0xf, false));
  v = max(v, (unsigned)__builtin_amdgcn_update_dpp(0, (int)v, 0x143, 0xc, 0xf, false));
  return (unsigned)__builtin_amdgcn_readlane((int)v, 63);
}
DEV unsigned phi2(f2 u, f2 rs) {
  f2 t = u * rs;
  f2 a = t * 1.44269504f, p = t + 1.f, r;
  r[0] = __builtin_amdgcn_fmed3f(__builtin_amdgcn_exp2f(a[0]), p[0], 1.f);
  r[1] = __builtin_amdgcn_fmed3f(__builtin_amdgcn_exp2f(a[1]), p[1], 1.f);
  r += 1e-6f;
  return pk2(r[0], r[1]);
}

#define STDECL unsigned st_[16] = {0, 0, 0, 0, 0, 0, 0, 0, 0, 0, 0, 0, 0, 0, 0, 0}
#define STMP(i) st_[i] = (unsigned)__builtin_amdgcn_s_memrealtime()
#define BST(i)
#define RETRY()
#define RETRY2()
#define CL(p) ((unsigned)((size_t)(p) >> 7) & 7u)
#define STFLUSH() do { const int b_ = blockIdx.x; if (w == EVW && (EVG)) { const unsigned v_ = min((unsigned)(EVX), 300000u); _Pragma("unroll 1") for (unsigned it = 0; it < SPIN; ++it) { if (NOW() - st_[0] >= 4500u + v_) break; SLEEP(1); } } } while (0)

DEV float half_sum(float v) {
  asm volatile("s_nop 1\n v_add_f32_dpp %0, %0, %0 row_ror:8 row_mask:0xf bank_mask:0xf\n s_nop 1\n v_add_f32_dpp %0, %0, %0 row_ror:4 row_mask:0xf bank_mask:0xf\n"
               "s_nop 1\n v_add_f32_dpp %0, %0, %0 row_ror:2 row_mask:0xf bank_mask:0xf\n s_nop 1\n v_add_f32_dpp %0, %0, %0 row_ror:1 row_mask:0xf bank_mask:0xf\n s_nop 1" : "+v"(v));
  auto r = __builtin_amdgcn_permlane16_swap(__float_as_uint(v), __float_as_uint(v), false, false);
  return __uint_as_float(r[0]) + __uint_as_float(r[1]);
}

struct FQ { s8 a[4]; s4 b[12]; };
template <bool B> struct BC { static constexpr bool v = B; };

__global__ __launch_bounds__(512) void fused(const u16* __restrict__ x, const u16* __restrict__ wqkv, const u16* __restrict__ wo,
                                             const u16* __restrict__ wrt, const u16* __restrict__ wg, const u16* __restrict__ wu,
                                             const u16* __restrict__ wd, char* __restrict__ ws, u16* __restrict__ out) {
  extern __shared__ __attribute__((aligned(16))) char lds[];
  asm volatile(".Lcode_beg:");
  const int w = __builtin_amdgcn_readfirstlane(threadIdx.x >> 6), l = threadIdx.x & 63;
  STDECL;
  STMP(0);
  const int g = blockIdx.x, xcd = g & 7, j = g >> 3, ii = j >> 2;
  unsigned cw = 0;
  if (w == 7) {
    unsigned long long pc; unsigned off, len, l1, l2, l3;
    asm volatile("s_getpc_b64 %0\n.Lcode_here:" : "=s"(pc));
    asm volatile("s_mov_b32 %0, (.Lcode_here - .Lcode_beg)" : "=s"(off));
    asm volatile("s_mov_b32 %0, (.Lend_c - .Lcode_beg)" : "=s"(len));
    asm volatile("s_mov_b32 %0, (.Lend_l - .Lcode_beg)" : "=s"(l1));
    asm volatile("s_mov_b32 %0, (.Lend_h - .Lcode_beg)" : "=s"(l2));
    asm volatile("s_mov_b32 %0, (.Lend_s - .Lcode_beg)" : "=s"(l3));
    len = max(max(len, l3), max(l1, l2));
    const unsigned o = min((unsigned)(l * 32 + j) * 64u, len - 4u);
    asm volatile("global_load_dword %0, %1, %2" : "+v"(cw) : "v"(o), "s"(pc - off) : "memory");
  }
#define PTAB 0xA98BFEDC76542103ull
#define PHT 0x6624924999924924ull
#define CX 4
  const int mb = (xcd & 1) * 8 + ii, hp = (xcd >> 1) * 4 + (j & 3);
  const bool sp = (hp & 7) == 3;
  const int rot = 2 * xcd + 4 * (ii & 3);
  const int nb = hp;
#define PAIRAT(p) ((int)(PTAB >> (4 * (p))) & 15)
#define HEADAT(s) (2 * PAIRAT((s) >> 1) + ((s) & 1))
  const int ph = (int)(PHT >> (2 * j)) & 3;
  const unsigned ldsb = (unsigned)(size_t)(LAS char*)lds;
  const unsigned adFL = ldsb + L_FL + 4 * (l & 7), myFL = ldsb + L_FL + 4 * w;
  const __amdgpu_buffer_rsrc_t wse = __builtin_amdgcn_make_buffer_rsrc(ws, 0, 64, 0x00020000);
  const int r15 = l & 15, g4 = l >> 4, q = (l & 15) >> 2, p = l & 3;
  const char* wob = uptr((const char*)wo + nb * 128);

  if (w != 4 && w != 5) {
  const unsigned ev0 = __builtin_amdgcn_raw_buffer_load_b32(wse, WS_E, 0, 16);
  if (w >= 6) {
    const int mw = w - 6;
    const unsigned tag = __builtin_amdgcn_readfirstlane(ev0) + 1u, tag2 = tag * 0x9E3779B1u;
    MKWS(tag);
    auto helper = [&](auto SP) {
    constexpr bool spare = decltype(SP)::v, moe = !spare && !NOMOE;
    int nbar = 0;
#define TICK() do { BST(nbar); SBAR(); BAR(); SBAR(); ++nbar; } while (0)
#define DTICK(lim) do { if (nbar < (lim)) TICK(); else SLEEP(2); } while (0)
    const int hd = hp * 2 + mw;
    const u4 pz = (u4)(POIS);
    const int chunk = 4 * (hp - (hp > 3) - (hp > 11)) + (ii & 3), ksp = (xcd & 1) * 2 + (ii >> 2), chg = chunk ^ (mw * CX);
    const int np = l & 7, kset = (l >> 3) & 1;
    u4 gw[4][8], dw[2][8];
    const __amdgpu_buffer_rsrc_t wgr = __builtin_amdgcn_make_buffer_rsrc((void*)(mw ? wu : wg), 0, D * F * 2, 0x00020000);
    const __amdgpu_buffer_rsrc_t wdr = __builtin_amdgcn_make_buffer_rsrc((void*)wd, 0, D * F * 2, 0x00020000);
    const __amdgpu_buffer_rsrc_t wor = __builtin_amdgcn_make_buffer_rsrc((void*)wo, 0, D * D * 2, 0x00020000);
    const unsigned gvo = ((256 * ksp + 32 * kset + 8 * g4) * F + 64 * chg + 8 * np) * 2;
    const unsigned dvo = ((64 * chunk + 8 * g4) * D + 256 * ksp + 128 * mw + 8 * r15) * 2;
    auto burst = [&](int k) {
      if (moe) {
#pragma unroll
        for (int i = 0; i < 16; ++i) {
          if (k < 2) gw[(2 * k + (i >> 3)) & 3][i & 7] = __builtin_amdgcn_raw_buffer_load_b128(wgr, gvo, (128 * k + 64 * (i >> 3) + (i & 7)) * F * 2, 2);
          else dw[i >> 3][i & 7] = __builtin_amdgcn_raw_buffer_load_b128(wdr, dvo, (32 * (i >> 3) + (i & 7)) * D * 2, 2);
        }
      }
    };
    auto transp = [&](u4(&v)[8]) {
      if (moe) {
        u4 f[8];
#pragma unroll
        for (int c = 0; c < 8; ++c)
#pragma unroll
          for (int e = 0; e < 4; ++e) {
            const unsigned ev = v[2 * e][c >> 1], od = v[2 * e + 1][c >> 1];
            f[c][e] = (c & 1) ? __builtin_amdgcn_perm(od, ev, 0x07060302u) : __builtin_amdgcn_perm(od, ev, 0x05040100u);
          }
#pragma unroll
        for (int c = 0; c < 8; ++c) { v[c] = f[c]; asm volatile("" : "+v"(v[c])); }
      }
    };
    auto pois = [&](int i) {
      if (i < 2) ST128(pz, WS_KVP + (hd * 16 + mb) * 2048 + i * 1024 + l * 16);
      else if (i == 2) ST128(pz, WS_ZP + (hd * 16 + mb) * 128 + (l & 7) * 16);
      else if (!NOPOIS) ST128(pz, OFA(i - 3));
    };
    unsigned wt0, wt1, wt2 = 0, wt3 = 0;
    auto touch = [&]() {
      const unsigned to = (j * 32 + mw * 16 + (l >> 3)) * 2048 + (xcd >> 1) * 512 + (l & 7) * 64;
      wt0 = __builtin_amdgcn_raw_buffer_load_b32(wor, to, 0, 0); wt1 = __builtin_amdgcn_raw_buffer_load_b32(wor, to, 16384, 0);
      if (TLBW) {
        const __amdgpu_buffer_rsrc_t outr = __builtin_amdgcn_make_buffer_rsrc(out, 0, 2048 * D * 2, 0x00020000);
        wt2 = __builtin_amdgcn_raw_buffer_load_b32(outr, (mb * 128 + mw * 64 + (l & 3) * 16) * 2048 + nb * 128, 0, 0);
        wt3 = __builtin_amdgcn_raw_buffer_load_b32(wsr, (l & 1) ? WS_GP + g * 4096 + mw * 2048 : WS_YP + (g >> 2) * 32768 + (l & 2) * 8192, 0, 0);
      }
    };
    s4 sb[24];
    auto colrd = [&](int h) {
      const unsigned bo = ldsb + (1 + h) * STG + 16384 + (8 * g4 + q) * 128 + (p & 1) * 8;
      const int sw = ((q >> 1) << 2) | ((g4 & 1) << 1);
      const unsigned b0 = bo + (((mw * 4 + (p >> 1)) ^ sw) * 16), b1 = bo + (((mw * 4 + 2 + (p >> 1)) ^ sw) * 16);
#pragma unroll
      for (int n = 0; n < 12; ++n) { RDTR(sb[2 * n], (n & 1) ? b1 : b0, ((n % 6) >> 1) * 8192 + (n / 6) * 4096); RDTR(sb[2 * n + 1], (n & 1) ? b1 : b0, ((n % 6) >> 1) * 8192 + (n / 6) * 4096 + 512); }
      WAITL(0);
      SBAR();
    };
    auto colsum = [&]() {
      const u4 ones = (u4)(0x3F803F80u);
      f4 c[6];
#pragma unroll
      for (int n = 0; n < 12; ++n) c[n % 6] = MFMA16(S8(ones), CAT(sb[2 * n], sb[2 * n + 1]), n >= 6 ? c[n % 6] : (f4)(0.f));
      const u4 csg = (u4){__float_as_uint(g4 ? c[3][0] : c[0][0]), __float_as_uint(g4 ? c[4][0] : c[1][0]), __float_as_uint(g4 ? c[5][0] : c[2][0]), tag};
      ST128(csg, l < 32 ? WS_CS + (hd * 16 + mb) * 512 + l * 16 : WS_SZ);
    };
    u4 stv, cv[8], cd; float cs[3];
    auto gload = [&]() {
      stv = LD128(WS_ST + (mb * 128 + mw * 64 + l) * 16);
      cd = LD128(WS_CAND + (g & 31) * 128 + (l & 7) * 16);
#pragma unroll
      for (int i = 0; i < 8; ++i) cv[i] = LD128(WS_CS + hd * 8192 + i * 1024 + l * 16);
    };
    auto gbad = [&]() {
      unsigned bad = (stv[1] ^ tag) | (stv[3] ^ tag) | (cd[1] ^ tag) | (cd[3] ^ tag);
#pragma unroll
      for (int i = 0; i < 8; ++i) bad |= cv[i][3] ^ tag;
      return __ballot(bad != 0u) != 0ull;
    };
    int idx;
    auto gfin = [&]() {
      if (gbad()) { RETRY(); _Pragma("unroll 1") for (unsigned it = 0; it < SPIN; ++it) { SLEEP(2); gload(); if (!gbad()) break; } }
#pragma unroll
      for (int e = 0; e < 3; ++e) {
        float a = __uint_as_float(cv[0][e]);
#pragma unroll
        for (int i = 1; i < 8; ++i) a += __uint_as_float(cv[i][e]);
        auto r = __builtin_amdgcn_permlane32_swap(__float_as_uint(a), __float_as_uint(a), false, false);
        cs[e] = __uint_as_float(r[0]) + __uint_as_float(r[1]);
      }
      asm volatile("" : "+v"(cs[0]), "+v"(cs[1]), "+v"(cs[2]) : "v"(wt0), "v"(wt1), "v"(wt2), "v"(wt3));
      cd[0] = max(cd[0], cd[2]);
      idx = (int)(cd[0] & 2047u);
    };
    auto lpub = [&]() {
      *(LAS float*)(lds + L_M + (mw * 64 + l) * 4) = __uint_as_float(stv[0]);
      *(LAS float*)(lds + L_RS + (mw * 64 + l) * 4) = __uint_as_float(stv[2]);
      if (l < 32) {
#pragma unroll
        for (int e = 0; e < 3; ++e) *(LAS float*)(lds + L_CS + mw * 384 + ((3 * g4 + e) * 16 + r15) * 4) = cs[e];
      }
      if (mw == 0 && l < 8) *(LAS int*)(lds + L_SEL + l * 4) = idx;
      lsig(myFL, 1u);
    };
    u4 xs[4];
    asm volatile("" ::"s"(wg), "s"(wu), "s"(wd), "v"(cw));
    if (HPRIO) __builtin_amdgcn_s_setprio(HPRIO);
    TICK();
    if (spare) {
      TICK();
      if (ii >> 2) TICK();
      colrd(ii >> 2);
      if (!(ii >> 2)) TICK();
      u4 kk[8];
      auto kload = [&]() {
#pragma unroll
        for (int i = 0; i < 8; ++i) kk[i] = LD128(WS_RG + mw * 8192 + i * 1024 + l * 16);
      };
      kload();
      TICK();
      _Pragma("unroll 1") for (unsigned it = 0; it < SPIN; ++it) {
        unsigned bad = 0;
#pragma unroll
        for (int i = 0; i < 8; ++i) { bad |= (kk[i][1] ^ tag) | (kk[i][3] ^ tag); asm volatile("" : "+v"(bad)); }
        if (__ballot(bad != 0u) == 0ull) break;
        RETRY();
        kload();
        DTICK(7);
      }
      __builtin_amdgcn_s_setprio(3);
      unsigned m1 = max(kk[0][0], kk[0][2]), m2 = min(kk[0][0], kk[0][2]), m3 = 0;
#pragma unroll
      for (int i = 2; i < 16; ++i) {
        const unsigned a = kk[i >> 1][(i & 1) * 2];
        asm("v_med3_u32 %0, %1, %2, %0" : "+v"(m3) : "v"(m2), "v"(a));
        asm("v_med3_u32 %0, %1, %2, %0" : "+v"(m2) : "v"(m1), "v"(a));
        m1 = max(m1, a);
      }
      unsigned mine8 = 0;
#pragma unroll
      for (int it = 0; it < 8; ++it) {
        const unsigned km = wave_max(m1);
        mine8 = l == it ? km : mine8;
        const bool win = m1 == km;
        if (it < 7) { m1 = win ? m2 : m1; m2 = win ? m3 : m2; m3 = win ? 0u : m3; }
        if (it == 3 && nbar < 7) TICK();
      }
      if (__builtin_expect(__ballot(m1 == 0u) != 0ull, 0)) {
        unsigned lim = 0;
#pragma unroll 1
        for (int it = 0; it < 8; ++it) {
          unsigned m = 0;
#pragma unroll
          for (int i = 0; i < 8; ++i) m = umax3(m, kk[i][0] - lim, kk[i][2] - lim);
          lim += wave_max(m);
          mine8 = l == it ? lim : mine8;
        }
      }
      ST64(((u2){mine8, tag}), l < 8 ? WS_CAND + ((hp >> 3) * 16 + mb) * 128 + (mw ? 7 - l : l) * 16 + mw * 8 : WS_SZ);
      __builtin_amdgcn_s_setprio(HPRIO);
      STMP(2);
      TICK();
      colsum();
#pragma unroll
      for (int i = 0; i < 11; ++i) { if (i == 5) DTICK(9); pois(i); }
      touch();
      while (nbar < 10) TICK();
      gload();
      TICK(); TICK();
      WAITV(0);
      gfin();
      STMP(3);
      TICK(); TICK(); TICK();
      lpub();
    } else {
      if (PV == 5) { pois(0); pois(1); }
      TICK();
      if (PV == 4) { pois(0); pois(1); pois(2); }
      if (PV == 5) { pois(2); pois(3); }
      if (ii >> 2) { TICK(); if (PV == 4) pois(3); if (PV >= 4) pois(4); }
      colrd(ii >> 2);
      TICK();
      if (!(ii >> 2)) { if (PV == 4) pois(3); if (PV >= 4) pois(4); }
      colsum();
      if (!(ii >> 2)) TICK();
      STMP(1);
      for (int i = 0; i < ph; ++i) TICK();
      burst(0); touch();
      TICK();
      if (PV == 2) { pois(0); pois(1); pois(2); pois(3); pois(4); }
      TICK(); TICK();
      transp(gw[0]);
      TICK();
      transp(gw[1]);
      TICK();
      if (PV == 3) { pois(0); pois(1); pois(2); pois(3); pois(4); SBAR(); }
      burst(1);
      SBAR();
      if (PV == 0) { pois(0); pois(1); pois(2); pois(3); pois(4); }
      TICK();
      if (PV == 1) { pois(0); pois(1); pois(2); pois(3); pois(4); }
      pois(5); pois(6); pois(7); pois(8); pois(9); pois(10);
      TICK(); TICK();
      transp(gw[2]);
      WAITV(0);
      if (ph < 2) TICK();
      transp(gw[3]);
      if (ph < 1) TICK();
      SBAR();
      gload();
      TICK();
      gfin();
      u4 sv;
      if (moe) {
        const u16* xp = x + (size_t)idx * D + 256 * ksp + 32 * kset + 8 * g4;
#pragma unroll
        for (int it = 0; it < 4; ++it) xs[it] = *(const u4*)(xp + 64 * it);
        sv = LD128(WS_ST + idx * 16);
      }
      SBAR();
      lpub();
      SBAR();
      burst(2);
      STMP(3);
      SBAR();
      if (moe) {
        if (__builtin_expect(__ballot(sv[1] != tag || sv[3] != tag) != 0ull, 0)) {
          _Pragma("unroll 1") for (unsigned it = 0; it < SPIN; ++it) { SLEEP(1); sv = LD128(WS_ST + idx * 16); if (__ballot(sv[1] != tag || sv[3] != tag) == 0ull) break; }
        }
        const float m = __uint_as_float(sv[0]), rs = __uint_as_float(sv[2]);
#pragma unroll
        for (int it = 0; it < 4; ++it) {
#pragma unroll
          for (int e = 0; e < 4; ++e) { const unsigned d = pk2(lo(xs[it][e]) - m, hi(xs[it][e]) - m); xs[it][e] = pk2(lo(d) * rs, hi(d) * rs); }
          asm volatile("" : "+v"(xs[it]));
        }
      }
    }
    __builtin_amdgcn_s_setprio(TP1);
    STMP(4);
    float sg;
    {
      const unsigned mono = cd[0] >> 16, rb = (mono & 0x8000u) ? (mono & 0x7FFFu) : (~mono & 0xFFFFu);
      sg = rbf(1.f / (1.f + __expf(-lo(rb))));
    }
    if (moe) {
      STMP(5);
      f4 acc[8];
#pragma unroll
      for (int it = 0; it < 4; ++it)
#pragma unroll
        for (int c = 0; c < 8; ++c) acc[c] = MFMA16(S8(gw[it][c]), S8(xs[it]), it ? acc[c] : (f4)(0.f));
#pragma unroll
      for (int i = 0; i < 4; ++i) {
        u4 s;
#pragma unroll
        for (int c = 0; c < 4; ++c) {
          auto r = __builtin_amdgcn_permlane32_swap(__float_as_uint(acc[c][i]), __float_as_uint(acc[c + 4][i]), false, false);
          s[c] = __float_as_uint(__uint_as_float(r[0]) + dppf<0x128>(__uint_as_float(r[1])));
        }
        if (r15 < 8) ST128(s, WS_GP + ((chg * 4 + ksp) * 2 + mw) * 2048 + (g4 >> 1) * 1024 + (4 * (g4 & 1) + i) * 128 + r15 * 16);
      }
      WAITV(0);
      if (l == 0) ST64(((u2){tag, tag2}), WS_GF + chg * 1024 + (ksp * 2 + mw) * FS);
      STMP(6);
      transp(dw[0]); transp(dw[1]);
      _Pragma("unroll 1") for (unsigned it = 0; it < SPIN; ++it) {
        u2 f = LD64(WS_GF + chunk * 1024 + (l & 7) * FS);
        if (__ballot(f[0] != tag || f[1] != tag2) == 0ull) break;
        SLEEP(GSL);
      }
      STMP(7);
      __builtin_amdgcn_s_setprio(TP2);
      u4 ha[2];
      {
        u4 pg[4][4];
#pragma unroll
        for (int ks = 0; ks < 4; ++ks)
#pragma unroll
          for (int t = 0; t < 4; ++t)
            pg[ks][t] = LD128(WS_GP + ((chunk * 4 + ks) * 4 + t) * 1024 + (4 * kset + g4) * 128 + np * 16);
        f4 gq[4];
#pragma unroll
        for (int t = 0; t < 4; ++t)
          gq[t] = (__builtin_bit_cast(f4, pg[0][t]) + __builtin_bit_cast(f4, pg[1][t])) + (__builtin_bit_cast(f4, pg[2][t]) + __builtin_bit_cast(f4, pg[3][t]));
        float hv[8];
#pragma unroll
        for (int e = 0; e < 8; ++e) {
          const float gg = rbf(gq[e >> 2][e & 3]), uu = rbf(gq[2 + (e >> 2)][e & 3]);
          hv[e] = rbf(gg / (1.f + __expf(-gg))) * uu;
        }
#pragma unroll
        for (int e = 0; e < 4; ++e) { ha[0][e] = pk2(hv[2 * e], hv[2 * e + 1]); ha[1][e] = (unsigned)__builtin_amdgcn_update_dpp(0, (int)ha[0][e], 0x128, 0xf, 0xf, false); }
      }
#pragma unroll
      for (int it = 0; it < 2; ++it)
#pragma unroll
        for (int c = 0; c < 8; ++c) acc[c] = MFMA16(S8(ha[it]), S8(dw[it][c]), it ? acc[c] : (f4)(0.f));
      if (g4 < 2) {
#pragma unroll
        for (int i = 0; i < 4; ++i) {
          const unsigned yo = WS_YP + ((chunk * 8 + 4 * g4 + i) * D + 256 * ksp + 128 * mw + 4 * r15) * 4;
          ST128(__builtin_bit_cast(u4, ((f4){acc[0][i], acc[1][i], acc[2][i], acc[3][i]})), yo);
          ST128(__builtin_bit_cast(u4, ((f4){acc[4][i], acc[5][i], acc[6][i], acc[7][i]})), yo + 256);
        }
      }
      WAITV(0);
      if (l == 0) ST64(((u2){tag, tag2}), WS_YF + (ksp * 2 + mw) * 8192 + chunk * FS);
      STMP(8);
    }
    unsigned mine = (unsigned)__ballot(!NOMOE && l < 8 && (idx >> 7) == mb && (l & 1) == mw);
    if (mine) {
      if (spare) lwait<4>(adFL, 0x30u, 2);
      _Pragma("unroll 1") for (unsigned it = 0; it < SPIN; ++it) {
        u2 f = LD64(WS_YF + (nb >> 1) * 8192 + (l < 56 ? l : 0) * FS);
        if (__ballot(f[0] != tag || f[1] != tag2) == 0ull) break;
        SLEEP(YSL);
      }
      lwait<1>(adFL, 0x0Fu, 4);
      __builtin_amdgcn_s_setprio(TP2);
      while (mine) {
        const int t = __builtin_ctz(mine); mine &= mine - 1;
        u4 yv[14];
#pragma unroll
        for (int i = 0; i < 14; ++i) yv[i] = LD128(WS_YP + (((i * 4 + g4) * 8 + t) * D + (nb >> 1) * 128 + (r15 >> 3) * 64 + (nb & 1) * 32 + (r15 & 7) * 4) * 4);
        f4 y = __builtin_bit_cast(f4, yv[0]);
#pragma unroll
        for (int i = 1; i < 14; ++i) y += __builtin_bit_cast(f4, yv[i]);
        const float gt = __int_as_float(__builtin_amdgcn_readlane(__float_as_int(sg), t));
#pragma unroll
        for (int e = 0; e < 4; ++e) {
          auto r = __builtin_amdgcn_permlane32_swap(__float_as_uint(y[e]), __float_as_uint(y[e]), false, false);
          const float a = __uint_as_float(r[0]) + __uint_as_float(r[1]);
          auto r2 = __builtin_amdgcn_permlane16_swap(__float_as_uint(a), __float_as_uint(a), false, false);
          y[e] = rbf(rbf(__uint_as_float(r2[0]) + __uint_as_float(r2[1])) * gt);
        }
        if (l < 16) *(LAS f4*)(lds + L_KVX + t * 256 + (l & 7) * 32 + (l >> 3) * 16) = y;
      }
    }
    lsig(myFL, 3u);
    STMP(9);
    STFLUSH();
    if (spare) asm volatile(".Lend_s:"); else asm volatile(".Lend_h:");
    };
    if (sp) helper(BC<true>()); else helper(BC<false>());
    return;
  }

  const int wr = w >> 1, wc = w & 1;
  unsigned tag; u2 kg; u4 sg4;
  const int srow = mb * 128 + hp * 8 + 2 * w + (l >> 5);
  {
    const int row = srow;
    u4 xa[4], rw[4];
#pragma unroll
    for (int c = 0; c < 4; ++c) xa[c] = *(const u4*)(x + row * D + (l & 31) * 8 + 256 * c);
#pragma unroll
    for (int c = 0; c < 4; ++c) rw[c] = *(const u4*)(wrt + (l & 31) * 8 + 256 * c);
    float s0 = 0, s1 = 0;
#pragma unroll
    for (int c = 0; c < 4; ++c)
#pragma unroll
      for (int e = 0; e < 4; e += 2) { unsigned x0 = xa[c][e], x1 = xa[c][e + 1]; s0 = dot2(x0, 0x3F803F80u, s0); s1 = dot2(x1, 0x3F803F80u, s1); }
    const float m = half_sum(s0 + s1) * (1.f / D);
    f2 v[16], q2 = (f2)(0.f), q3 = (f2)(0.f);
#pragma unroll
    for (int c = 0; c < 16; ++c) v[c] = (f2){lo(xa[c >> 2][c & 3]), hi(xa[c >> 2][c & 3])} - m;
#pragma unroll
    for (int c = 0; c < 16; c += 2) { q2 += v[c] * v[c]; q3 += v[c + 1] * v[c + 1]; }
    q2 += q3;
    const float mr = rbf(m);
    const float rs = 1.f / rbf(sqrtf(rbf(rbf(half_sum(q2[0] + q2[1]) * (1.f / D)) + 1e-5f)));
    float d0 = 0, d1 = 0;
#pragma unroll
    for (int c = 0; c < 16; c += 2) {
      const f2 e0 = (f2){lo(xa[c >> 2][c & 3]), hi(xa[c >> 2][c & 3])} - mr, e1 = (f2){lo(xa[c >> 2][(c & 3) + 1]), hi(xa[c >> 2][(c & 3) + 1])} - mr;
      const unsigned p0 = pk2(e0[0], e0[1]), p1 = pk2(e1[0], e1[1]);
      const f2 n0 = (f2){lo(p0), hi(p0)} * rs, n1 = (f2){lo(p1), hi(p1)} * rs;
      unsigned c0 = rw[c >> 2][c & 3], c1 = rw[c >> 2][(c & 3) + 1];
      d0 = dot2(pk2(n0[0], n0[1]), c0, d0); d1 = dot2(pk2(n1[0], n1[1]), c1, d1);
    }
    const float d = half_sum(d0 + d1);
    const unsigned rb = pk2(d, d) & 0xFFFFu;
    const unsigned mono = (rb & 0x8000u) ? (~rb & 0xFFFFu) : (rb | 0x8000u);
    tag = __builtin_amdgcn_readfirstlane(ev0) + 1u;
    kg = (u2){(mono << 16) | (unsigned)row, tag};
    sg4 = (u4){__float_as_uint(mr), tag, __float_as_uint(rs), tag};
  }
  MKWS(tag);
  if ((l & 31) == 0) { ST64(kg, WS_RG + srow * 8); ST128(sg4, WS_ST + srow * 16); }
  unsigned aad[2], bad[2];
#pragma unroll
  for (int s = 0; s < 2; ++s) aad[s] = ldsb + (wr * 64 + r15) * 128 + (((4 * s + g4) ^ ((r15 >> 1) & 7)) * 16);
#pragma unroll
  for (int nh = 0; nh < 2; ++nh) bad[nh] = ldsb + 16384 + (8 * g4 + q) * 128 + (((wc * 4 + 2 * nh + (p >> 1)) ^ (((q >> 1) << 2) | ((g4 & 1) << 1))) * 16) + (p & 1) * 8;
  f4 acc[4][6];
#pragma unroll
  for (int m = 0; m < 4; ++m)
#pragma unroll
    for (int n = 0; n < 6; ++n) { acc[m][n] = (f4)(0.f); asm volatile("" : "+v"(acc[m][n])); }
#define MF(m, n, Fq) do { if (n < 2) acc[m][n] = MFMA16(CAT(Fq.b[2 * n], Fq.b[2 * n + 1]), Fq.a[m], acc[m][n]); \
     else acc[m][n] = MFMA16(Fq.a[m], CAT(Fq.b[2 * n], Fq.b[2 * n + 1]), acc[m][n]); SBAR(); } while (0)
#define RB(G, n, vb0, vb1, SS, hf) RDTR(G.b[2 * (n) + (hf)], (((n) & 1) ? vb1 : vb0), ((n) >> 1) * 8192 + (SS) * 4096 + (hf) * 512)
#define RA(G, m, va) RD128(G.a[m], va, (m) * 2048)
#define SUB(Fq, G, va, vb0, vb1, SS, PRE) do { \
    WAITL(13); SBAR(); \
    MF(0, 0, Fq); PRE; RA(G, 0, va); SBAR(); \
    WAITL(11); SBAR(); \
    MF(1, 0, Fq); RB(G, 0, vb0, vb1, SS, 0); SBAR(); MF(2, 0, Fq); RB(G, 0, vb0, vb1, SS, 1); SBAR(); MF(3, 0, Fq); RA(G, 1, va); SBAR(); \
    WAITL(12); SBAR(); \
    MF(0, 1, Fq); RA(G, 2, va); SBAR(); MF(1, 1, Fq); RA(G, 3, va); SBAR(); MF(2, 1, Fq); RB(G, 1, vb0, vb1, SS, 0); SBAR(); MF(3, 1, Fq); RB(G, 1, vb0, vb1, SS, 1); SBAR(); \
    WAITL(8); SBAR(); \
    MF(0, 2, Fq); RB(G, 2, vb0, vb1, SS, 0); SBAR(); MF(1, 2, Fq); RB(G, 2, vb0, vb1, SS, 1); SBAR(); MF(2, 2, Fq); RB(G, 3, vb0, vb1, SS, 0); SBAR(); MF(3, 2, Fq); RB(G, 3, vb0, vb1, SS, 1); SBAR(); \
    MF(0, 3, Fq); RB(G, 4, vb0, vb1, SS, 0); SBAR(); MF(1, 3, Fq); RB(G, 4, vb0, vb1, SS, 1); SBAR(); MF(2, 3, Fq); RB(G, 5, vb0, vb1, SS, 0); SBAR(); MF(3, 3, Fq); RB(G, 5, vb0, vb1, SS, 1); SBAR(); \
    MF(0, 4, Fq); MF(1, 4, Fq); MF(2, 4, Fq); MF(3, 4, Fq); MF(0, 5, Fq); MF(1, 5, Fq); MF(2, 5, Fq); MF(3, 5, Fq); } while (0)
#define KSTEP(MODE) do { \
    if (kt == sx) { const unsigned xo = xad + buf * STG; \
      _Pragma("unroll") for (int t = 0; t < 8; ++t) { const unsigned a_ = xo ^ (t * 16); \
        asm volatile("ds_read_b64 %0, %1" : "+v"(xv[t]) : "v"(a_) : "memory"); } \
      WAITL(0); SBAR(); } \
    const int nbuf = (buf + 1) & 3; \
    const unsigned bo = buf * STG, nbo = nbuf * STG; \
    { unsigned va = aad[1] + bo, vb0 = bad[0] + bo, vb1 = bad[1] + bo; SUB(f0, f1, va, vb0, vb1, 1, ); } \
    if (MODE < 2) { \
      unsigned va = aad[0] + nbo, vb0 = bad[0] + nbo, vb1 = bad[1] + nbo; \
      SUB(f1, f0, va, vb0, vb1, 0, if (MODE == 0) { WAITL(0); BAR(); } else asm volatile("ds_write_b32 %0, %1" ::"v"(myFL), "v"(1u) : "memory"); SBAR()); \
    } else { asm volatile("ds_write_b32 %1, %2\n ds_read_b32 %0, %3\n s_waitcnt lgkmcnt(2)" : "=&v"(fl0) : "v"(myFL), "v"(2u), "v"(adFL) : "memory"); SBAR(); \
      MF(0, 2, f1); MF(1, 2, f1); MF(2, 2, f1); MF(3, 2, f1); MF(0, 3, f1); MF(1, 3, f1); MF(2, 3, f1); MF(3, 3, f1); \
      MF(0, 4, f1); MF(1, 4, f1); MF(2, 4, f1); MF(3, 4, f1); MF(0, 5, f1); MF(1, 5, f1); MF(2, 5, f1); MF(3, 5, f1); \
      MF(0, 0, f1); MF(1, 0, f1); MF(2, 0, f1); MF(3, 0, f1); MF(0, 1, f1); MF(1, 1, f1); MF(2, 1, f1); MF(3, 1, f1); } \
    buf = nbuf; } while (0)
  u2 xv[8];
#pragma unroll
  for (int t = 0; t < 8; ++t) asm volatile("" : "=v"(xv[t]));
  const int sx = (nb - rot) & 15;
  const unsigned xad = ldsb + (w * 32 + (l & 31)) * 128 + ((r15 >> 1) & 7) * 16 + (l >> 5) * 8;
  STMP(1);
  WAITL(0);
  BAR();
  STMP(2);
  WAITL(0);
  FQ f0, f1;
  { unsigned va = aad[0], vb0 = bad[0], vb1 = bad[1];
    RA(f0, 0, va); RB(f0, 0, vb0, vb1, 0, 0); RB(f0, 0, vb0, vb1, 0, 1); RA(f0, 1, va); RA(f0, 2, va); RA(f0, 3, va);
    RB(f0, 1, vb0, vb1, 0, 0); RB(f0, 1, vb0, vb1, 0, 1); RB(f0, 2, vb0, vb1, 0, 0); RB(f0, 2, vb0, vb1, 0, 1); RB(f0, 3, vb0, vb1, 0, 0); RB(f0, 3, vb0, vb1, 0, 1);
    RB(f0, 4, vb0, vb1, 0, 0); RB(f0, 4, vb0, vb1, 0, 1); RB(f0, 5, vb0, vb1, 0, 0); RB(f0, 5, vb0, vb1, 0, 1);
    WAITL(0); SBAR(); }
  int buf = 0; unsigned fl0;
#pragma unroll 1
  for (int kt = 0; kt < 14; ++kt) KSTEP(0);
  { const int kt = 14; KSTEP(1); }
  { const int kt = 15; KSTEP(2); }
  asm volatile("s_waitcnt lgkmcnt(0)" : "+v"(fl0) : : "memory");
  if (__builtin_expect(((unsigned)__ballot(fl0 < 1u) & 0xC0u) != 0u, 0)) { RETRY2(); lwait(adFL, 0xC0u, 1); }
  STMP(3);
  __builtin_amdgcn_s_setprio(2);
  const LAS float* Lm = (const LAS float*)(lds + L_M) + wr * 64;
  const LAS float* Lr = (const LAS float*)(lds + L_RS) + wr * 64;
  const LAS float* Lc = (const LAS float*)(lds + L_CS) + wc * 96;
  f4 kv[4], zt[2];
  {
    float cs[4];
#pragma unroll
    for (int n = 0; n < 4; ++n) cs[n] = Lc[32 + 16 * n + r15];
    const u4 ones = (u4)(0x3F803F80u);
#pragma unroll
    for (int t = 0; t < 2; ++t) {
      u4 kf[2], vf[2];
#pragma unroll
      for (int mm = 0; mm < 2; ++mm) {
        const int m = 2 * t + mm;
        const f4 mv = *(const LAS f4*)(Lm + m * 16 + 4 * g4), rv = *(const LAS f4*)(Lr + m * 16 + 4 * g4);
#pragma unroll
        for (int a = 0; a < 2; ++a) {
          const f4 uk = acc[m][2 + a] - mv * cs[a];
          const f4 uv = (acc[m][4 + a] - mv * cs[2 + a]) * rv;
          kf[a][2 * mm] = phi2((f2){uk[0], uk[1]}, (f2){rv[0], rv[1]}); kf[a][2 * mm + 1] = phi2((f2){uk[2], uk[3]}, (f2){rv[2], rv[3]});
          vf[a][2 * mm] = pk2(uv[0], uv[1]); vf[a][2 * mm + 1] = pk2(uv[2], uv[3]);
        }
      }
#pragma unroll
      for (int tt = 0; tt < 4; ++tt) kv[tt] = MFMA16(S8(kf[tt & 1]), S8(vf[tt >> 1]), t ? kv[tt] : (f4)(0.f));
#pragma unroll
      for (int a = 0; a < 2; ++a) zt[a] = MFMA16(S8(kf[a]), S8(ones), t ? zt[a] : (f4)(0.f));
    }
  }
  const int hd = hp * 2 + wc;
  {
    LAS f4* ks = (LAS f4*)(lds + L_KVX + w * 3072) + l;
    const LAS f4* kr = (const LAS f4*)(lds + L_KVX + (w ^ 2) * 3072) + l;
    ks[0] = wr ? kv[0] : kv[2]; ks[64] = wr ? kv[1] : kv[3]; ks[128] = wr ? zt[0] : zt[1];
    lsig(myFL, 3);
    const f4 m0 = wr ? kv[2] : kv[0], m1 = wr ? kv[3] : kv[1], mz = wr ? zt[1] : zt[0];
    lwait(adFL, 1u << (w ^ 2), 3);
    const f4 s0 = m0 + kr[0], s1 = m1 + kr[64], sz = mz + kr[128];
    u4 o; o[0] = min(pk2(s0[0], s0[1]), POIS - 1u); o[1] = pk2(s0[2], s0[3]); o[2] = pk2(s1[0], s1[1]); o[3] = pk2(s1[2], s1[3]);
    ST128(o, WS_KVP + (hd * 16 + mb) * 2048 + wr * 1024 + l * 16);
    u4 zo = __builtin_bit_cast(u4, sz); zo[0] = min(zo[0], POIS - 1u);
    if (r15 == 0) ST128(zo, WS_ZP + (hd * 16 + mb) * 128 + g4 * 32 + wr * 16);
  }
  STMP(4);
  {
    const f4 c0 = *(const LAS f4*)(Lc + 4 * g4), c1 = *(const LAS f4*)(Lc + 16 + 4 * g4);
#pragma unroll
    for (int m = 0; m < 4; ++m) {
      const float mv = Lm[m * 16 + r15], rv = Lr[m * 16 + r15];
      const f4 u0 = acc[m][0] - c0 * mv, u1 = acc[m][1] - c1 * mv;
      u4 pf;
      pf[0] = phi2((f2){u0[0], u0[1]}, (f2)(rv)); pf[1] = phi2((f2){u0[2], u0[3]}, (f2)(rv));
      pf[2] = phi2((f2){u1[0], u1[1]}, (f2)(rv)); pf[3] = phi2((f2){u1[2], u1[3]}, (f2)(rv));
      *(LAS u4*)(lds + L_PQ + wr * 8192 + wc * 4096 + m * 1024 + l * 16) = pf;
    }
  }
  lsig(myFL, 4);
  __builtin_amdgcn_s_setprio(0);
  STMP(5);
  const int trow = mb * 128 + w * 32 + (l & 31);
  u2 oh;
  const unsigned flo = (2 * PAIRAT(r15) + wc) * 128;
  const char* fb = uptr(wsx + WS_OH + mb * 4096);
  asm volatile("global_load_dwordx2 %0, %1, %2 sc1" : "=v"(oh) : "v"(flo), "s"(fb) : "memory");
  int sel = -1;
  {
    const u4 s0 = *(const LAS u4*)(lds + L_SEL), s1 = *(const LAS u4*)(lds + L_SEL + 16);
#pragma unroll
    for (int i = 0; i < 8; ++i) { const int v = (int)(i < 4 ? s0[i & 3] : s1[i & 3]); sel = v == trow ? i : sel; }
  }
  const bool anysel = __ballot(sel >= 0) != 0ull;
  f16v oc[2][2];
  {
    const float m = *(const LAS float*)(lds + L_M + (w * 32 + (l & 31)) * 4), rs = *(const LAS float*)(lds + L_RS + (w * 32 + (l & 31)) * 4);
#pragma unroll
    for (int t = 0; t < 8; ++t) {
      const f4 xf = (f4){lo(xv[t][0]), hi(xv[t][0]), lo(xv[t][1]), hi(xv[t][1])};
      const f4 y = (xf - m) * rs + xf;
#pragma unroll
      for (int i = 0; i < 4; ++i) oc[0][t >> 2][(t & 3) * 4 + i] = y[i];
    }
    asm volatile("" : "+v"(oc[0][0]), "+v"(oc[0][1]));
  }
  unsigned wa[2];
#pragma unroll
  for (int t = 0; t < 2; ++t) wa[t] = ldsb + L_WO + wc * 4096 + (4 * (g4 >> 1) + q) * 128 + (((4 * t + 2 * (g4 & 1) + (p >> 1)) ^ ((q >> 1) << 2)) * 16) + (p & 1) * 8;
  u4 a[RD][4];
#pragma unroll
  for (int k = 0; k < RD; ++k) asm volatile("" : "=v"(a[k][0]), "=v"(a[k][1]), "=v"(a[k][2]), "=v"(a[k][3]));
  const char* ob = uptr(wsx + WS_OF + mb * 262144 + wc * 8192 + wr * 4096);
  const unsigned ov0 = l * 16 + wc * 2048, ov1 = ov0 ^ 2048;
  STMP(6);
  lwait<1>(adFL, 0x30u, 1);
  STMP(7);
  WAITV(0);
  asm volatile("" : "+v"(oh));
  unsigned seen = (unsigned)__ballot(oh[0] == tag), go = 0;
  auto need = [&](unsigned mask, bool first) {
    if (__builtin_expect((go & mask) != mask, 0)) {
      _Pragma("unroll 1") for (unsigned it = 0; it < SPIN; ++it) {
        bool poll = (seen & mask) != mask;
        if (first && seen != ~0u && !poll) poll = ((unsigned)__ballot(oh[1] - NOW() - 80u < 1000u) & mask) != 0u;
        if (poll) {
          if (first) SLEEP(4);
          asm volatile("global_load_dwordx2 %0, %1, %2 sc1\n s_waitcnt vmcnt(0)" : "+v"(oh) : "v"(flo), "s"(fb) : "memory");
          seen = (unsigned)__ballot(oh[0] == tag);
        }
        go = seen & (unsigned)__ballot(oh[1] - NOW() - 1u >= 1000u);
        if ((go & mask) == mask) break;
        SLEEP(1);
      }
    }
  };
#define OX 0
#define OL1(pos, i) do { if (OX == 2 && (pos) >= RD) break; \
    asm volatile("global_load_dwordx4 %0, %1, %2 offset:%3 sc1" : "+v"(a[(pos) % RD][i]) : "v"((i) & 1 ? ov1 : ov0), "s"(ob + (OX == 1 ? ((pos) & 1) : PAIRAT(pos)) * 16384), "i"(((i) >> 1) * 1024) : "memory"); } while (0)
#define OLD(pos) do { OL1(pos, 0); OL1(pos, 1); OL1(pos, 2); OL1(pos, 3); } while (0)
  auto omax = [&](u4(&v)[4]) {
    asm volatile("" : "+v"(v[0]), "+v"(v[1]), "+v"(v[2]), "+v"(v[3]));
    return max(umax3(v[0][0], v[1][0], v[2][0]), v[3][0]);
  };
#define OFIX(pos, m) do { \
    if (__builtin_expect(__ballot((m) == POIS) != 0ull, 0)) { RETRY(); _Pragma("unroll 1") for (unsigned it = 0; it < SPIN; ++it) { SLEEP(4); OLD(pos); WAITV(0); if (__ballot(omax(a[(pos) % RD]) == POIS) == 0ull) break; } } } while (0)
#define OS1(i)
#define OS2(i)
  need(1u, true);
  if (OWN) lwait<1>(adFL, 0x30u, 2);
  __builtin_amdgcn_s_setprio(2);
  STMP(15);
  OS1(0); OLD(0); STMP(14);
#pragma unroll
  for (int i = 1; i < RD; ++i) { need(1u << i, false); OS1(i); OLD(i); }
  STMP(10);
  s4 b[8];
#define ORD(pos, f) do { if ((f) < 2 && (pos) == 8) { wa[(f) & 1] += 65536; asm volatile("" : "+v"(wa[(f) & 1])); } \
    RDTR(b[2 * (f)], wa[(f) & 1], ((pos) & 7) * 8192 + ((f) >> 1) * 1024); RDTR(b[2 * (f) + 1], wa[(f) & 1], ((pos) & 7) * 8192 + ((f) >> 1) * 1024 + 2048); } while (0)
#define OMF(pos, f, r) do { if ((f) < 2 && (pos) == 0 && (r) == 1) asm volatile("v_mfma_f32_32x32x16_bf16 %0, %1, %2, 0" : "=&v"(oc[r][(f) & 1]) \
      : "v"(CAT(b[2 * (f)], b[2 * (f) + 1])), "v"(a[(pos) % RD][((f) & 2) + (r)])); \
    else asm volatile("v_mfma_f32_32x32x16_bf16 %0, %1, %2, %0" : "+v"(oc[r][(f) & 1]) \
      : "v"(CAT(b[2 * (f)], b[2 * (f) + 1])), "v"(a[(pos) % RD][((f) & 2) + (r)])); \
    SBAR(); if ((r) == 1 && (pos) < 15) ORD(((pos) + 1) & 15, f); SBAR(); } while (0)
#define ONX(pos) ((pos) > 0 && (pos) + RD - 1 < 16)
#define ONL(pos, i) do { if (ONX(pos)) OL1(((pos) + RD - 1) & 15, i); SBAR(); } while (0)
#define OWL(pos, f) do { WAITL((pos) < 15 ? 6 : 6 - 2 * (f)); SBAR(); } while (0)
#define OHEAD(pos) do { unsigned m_ = 0; \
    OWL(pos, 0); OMF(pos, 0, 0); if (ONX(pos)) { need(1u << (((pos) + RD - 1) & 15), false); OS1(((pos) + RD - 1) & 15); } SBAR(); \
    OMF(pos, 0, 1); ONL(pos, 0); \
    OWL(pos, 1); OMF(pos, 1, 0); ONL(pos, 1); \
    OMF(pos, 1, 1); ONL(pos, 2); \
    OWL(pos, 2); OMF(pos, 2, 0); ONL(pos, 3); \
    if ((pos) == 4) { asm volatile("ds_write_b32 %0, %1" ::"v"(myFL), "v"(5u) : "memory"); SBAR(); } \
    if ((pos) == 13) { asm volatile("ds_read_b32 %0, %1" : "=v"(fl3) : "v"(adFL) : "memory"); SBAR(); } \
    OMF(pos, 2, 1); \
    OWL(pos, 3); OMF(pos, 3, 0); \
    if ((pos) < 15) { WAITV(4 * ((pos) + RD - 1 < 16 ? RD - 2 : 14 - (pos))); m_ = omax(a[((pos) + 1) % RD]); asm volatile("" : "+v"(m_)); SBAR(); } \
    OMF(pos, 3, 1); \
    if ((pos) < 15) { OFIX(((pos) + 1) & 15, m_); OS2(((pos) + 1) & 15); } SBAR(); } while (0)
  unsigned fl3;
  ORD(0, 0); ORD(0, 1); ORD(0, 2); ORD(0, 3);
  { WAITV(4 * (RD - 1)); const unsigned m_ = omax(a[0]); OFIX(0, m_); OS2(0); SBAR(); }
  OHEAD(0); OHEAD(1); STMP(11); OHEAD(2); OHEAD(3); OHEAD(4); OHEAD(5); OHEAD(6); OHEAD(7); STMP(12);
  OHEAD(8); OHEAD(9); STMP(13); OHEAD(10); OHEAD(11); OHEAD(12); OHEAD(13); OHEAD(14);
  f4 xi[8];
  {
    asm volatile("" : "+v"(fl3));
    if (__builtin_expect(((unsigned)__ballot(fl3 < 5u) >> (w ^ 2)) & 1u, 0)) lwait(adFL, 1u << (w ^ 2), 5);
    const unsigned xw = ldsb + L_WO + (4 * wr + wc) * 4096 + l * 16, xr = xw ^ 4096;
#define XW(t) asm volatile("ds_write_b128 %0, %1 offset:%2" ::"v"(xw), "v"(__builtin_shufflevector(oc[1][(t) >> 2], oc[1][(t) >> 2], ((t) & 3) * 4, ((t) & 3) * 4 + 1, ((t) & 3) * 4 + 2, ((t) & 3) * 4 + 3)), \
      "i"(((t) >> 2) * 8192 + ((t) & 3) * 1024) : "memory")
    SBAR();
    OWL(15, 0); OMF(15, 0, 1); OWL(15, 1); OMF(15, 1, 1); OWL(15, 2); OMF(15, 2, 1); OWL(15, 3); OMF(15, 3, 1);
    OMF(15, 0, 0); OMF(15, 1, 0); XW(0); XW(1); XW(2); XW(3); SBAR();
    OMF(15, 2, 0); XW(4); XW(5); XW(6); XW(7); SBAR();
    OMF(15, 3, 0);
    asm volatile("ds_write_b32 %0, %1" ::"v"(myFL), "v"(6u) : "memory");
    _Pragma("unroll 1") for (unsigned it = 0; it < SPIN; ++it) {
      unsigned v;
      asm volatile("ds_read_b32 %0, %1" : "=v"(v) : "v"(adFL) : "memory");
#pragma unroll
      for (int t = 0; t < 8; ++t) RD128(xi[t], xr, (t >> 2) * 8192 + (t & 3) * 1024);
      WAITL(0);
      asm volatile("" : "+v"(v), "+v"(xi[0]), "+v"(xi[1]), "+v"(xi[2]), "+v"(xi[3]), "+v"(xi[4]), "+v"(xi[5]), "+v"(xi[6]), "+v"(xi[7]));
      if ((((unsigned)__ballot(v < 6u) >> (w ^ 1)) & 1u) == 0u) break;
    }
    asm volatile("" : "+v"(oc[0][0]), "+v"(oc[0][1]));
#pragma unroll
    for (int t = 0; t < 8; ++t)
#pragma unroll
      for (int i = 0; i < 4; ++i) xi[t][i] += oc[0][t >> 2][(t & 3) * 4 + i];
  }
  STMP(8);
  if (anysel) {
    lwait(adFL, 0xC0u, 3);
    if (sel >= 0) {
#pragma unroll
      for (int t = 0; t < 8; ++t) xi[t] += *(const LAS f4*)(lds + L_KVX + sel * 256 + (8 * t + 4 * (l >> 5)) * 4);
    }
  }
  const __amdgpu_buffer_rsrc_t outr = __builtin_amdgcn_make_buffer_rsrc(out, 0, 2048 * D * 2, 0x00020000);
  if (FULL) {
    LAS char* tb = (LAS char*)lds + L_PQ + w * 4096;
#pragma unroll
    for (int t = 0; t < 8; ++t) *(LAS u2*)(tb + (l & 31) * 128 + ((t ^ (l & 7)) * 16) + (l >> 5) * 8) = (u2){pk2(xi[t][0], xi[t][1]), pk2(xi[t][2], xi[t][3])};
#pragma unroll
    for (int i = 0; i < 4; ++i) {
      const u4 v = *(const LAS u4*)(tb + i * 1024 + (l >> 3) * 128 + (((l ^ (l >> 3)) & 7) * 16));
      __builtin_amdgcn_raw_buffer_store_b128(v, outr, (mb * 128 + w * 32 + i * 8 + (l >> 3)) * 2048 + nb * 128 + (l & 7) * 16, 0, 16);
    }
  } else {
#pragma unroll
  for (int hh = 0; hh < 4; ++hh) {
    const f4 ta = xi[2 * hh], tb = xi[2 * hh + 1];
    auto r0 = __builtin_amdgcn_permlane32_swap(pk2(ta[0], ta[1]), pk2(tb[0], tb[1]), false, false);
    auto r1 = __builtin_amdgcn_permlane32_swap(pk2(ta[2], ta[3]), pk2(tb[2], tb[3]), false, false);
    __builtin_amdgcn_raw_buffer_store_b128(((u4){r0[0], r1[0], r0[1], r1[1]}), outr, (trow * D + nb * 64 + 16 * hh + 8 * (l >> 5)) * 2, 0, 16);
  }
  }
  if (g == 0 && w == 0 && l == 0) __builtin_amdgcn_raw_buffer_store_b32(tag, wse, WS_E, 0, 16);
  STMP(9);
  STFLUSH();
  asm volatile(".Lend_c:");
  return;
  }
  {
    const int lw = w - 4;
    unsigned ev;
    asm volatile("global_load_dword %0, %1, %2 sc1" : "=v"(ev) : "v"(0u), "s"(ws) : "memory");
    unsigned vf[20], vfw[4];
#pragma unroll
    for (int i = 0; i < 8; ++i) { const int row = 8 * (lw * 8 + i) + (l >> 3); vf[i] = row * 2048 + (((l & 7) ^ ((row >> 1) & 7)) * 16) + (3 - (i & 3)) * 1024; }
#pragma unroll
    for (int i = 0; i < 12; ++i) {
      const int pc = lw * 12 + i, row = (pc & 7) * 8 + (l >> 3);
      vf[8 + i] = row * 6144 + (pc >> 3) * 2048 + (((l & 7) ^ ((((row >> 1) & 1) << 2) | (((row >> 3) & 1) << 1))) * 16) + (3 - (i & 3)) * 1024;
    }
#pragma unroll
    for (int i = 0; i < 4; ++i) vfw[i] = (8 * i + (l >> 3)) * 2048 + (((l & 7) ^ (((l >> 4) & 1) << 2)) * 16) + (3 - i) * 1024;
    const char* Ab = uptr((const char*)x + (size_t)mb * 128 * 2048) - 3072;
    const char* Bb = uptr((const char*)wqkv + hp * 128) - 3072;
    const unsigned mba = ldsb + lw * 8192, mbb = ldsb + 16384 + lw * 12288;
#define GL4(v, sb, mm, MO) do { asm volatile("s_add_i32 m0, %2, %3\n s_nop 0\n global_load_lds_dwordx4 %0, %1" ::"v"((v)[0]), "s"(sb), "s"(mm), "i"(MO) : "memory", "scc"); \
      GLI((v)[1], sb, 1024); GLI((v)[2], sb, 2048); GLI((v)[3], sb, 3072); } while (0)
    auto issue = [&](int s, int u0, int u1) {
      const int kt = (s + rot) & 15;
      const char* sa = Ab + kt * 128; const char* sb = Bb + kt * (64 * 6144);
      const unsigned so = (s & 3) * STG, ma = mba + so, mq = mbb + so;
#pragma unroll
      for (int u = u0; u < u1; ++u) { if (u < 2) GL4(vf + 4 * u, sa, ma, u * 4096); else GL4(vf + 4 * u, sb, mq, (u - 2) * 4096); }
    };
    int tk = 0;
#define LBAR() do { BST(tk + 1); BAR(); BST(tk + 21); ++tk; } while (0)
    issue(0, 0, 5); issue(1, 0, 5);
    BST(60);
    WAITV(20); BST(0); BAR(); BST(20);
    asm volatile("" : "+v"(ev));
    const unsigned tag = __builtin_amdgcn_readfirstlane(ev) + 1u;
    MKWS(tag);
    issue(2, 0, 5);
    int s0 = 3;
#pragma unroll 1
    for (; s0 < (sp ? 16 : 6 + ph); ++s0) { issue(s0, 0, 5); BST(tk + 40); WAITV(40); LBAR(); }
#pragma unroll 1
    for (; s0 < 16; s0 += 5) {
      WAITV(20); LBAR();
      WAITV(0); LBAR();
      issue(s0, 0, 5); issue(s0 + 1, 0, U1); BST(tk + 40); WAITV(4 * U1); LBAR();
      issue(s0 + 1, U1, 5); issue(s0 + 2, 0, 5);
      if (s0 + 3 >= 16) break;
      issue(s0 + 3, 0, U2); BST(tk + 40); WAITV(20 + 4 * U2); LBAR();
      issue(s0 + 3, U2, 5);
      if (s0 + 4 >= 16) break;
      issue(s0 + 4, 0, 5); BST(tk + 40); WAITV(40); LBAR();
    }
    int wq = lw;
    auto wo_upto = [&](int lim) {
      _Pragma("unroll 1") for (; wq < lim; wq += 2) { const char* wp = wob - 3072 + HEADAT(wq) * 65536; const unsigned wm = ldsb + wq * 4096; GL4(vfw, wp, wm, L_WO); }
    };
    asm volatile("ds_write_b32 %0, %1" ::"v"(adFL), "v"(0u) : "memory");
    WAITV(0); WAITL(0); LBAR();
    if (lw == 0) ST64(((u2){tag, NOW() + KD}), l == 0 ? WS_HK + (hp * 16 + mb) * 128 : WS_SZ);
    wo_upto(12);
    BST(15); lwait<1>(adFL, 0x0Fu, 1); BST(35);
    wo_upto(22);
    BST(16); lwait<1>(adFL, 0x0Fu, 2); BST(36);
    wo_upto(32);
    STMP(1);
    const int hd = hp * 2 + lw;
    u4 sel[2];
#pragma unroll
    for (int e = 0; e < 2; ++e) {
      const unsigned v = (r15 >> 2) == g4 ? (((r15 & 3) == 2 * e ? 0x3F80u : 0u) | ((r15 & 3) == 2 * e + 1 ? 0x3F800000u : 0u)) : 0u;
      sel[0][e] = v; sel[0][2 + e] = 0u; sel[1][e] = 0u; sel[1][2 + e] = v;
    }
    u4 pv[16][2], zv0, zv1, pq[8];
    const char* kvb = uptr(wsx + WS_KVP + (size_t)hd * 32768);
    const char* zb = uptr(wsx + WS_ZP + (size_t)hd * 2048);
    const char* hb = uptr(wsx + WS_HK + hp * 2048);
    const unsigned lv16 = l * 16, zoff = r15 * 128 + g4 * 32;
    const unsigned pa = ldsb + L_PQ + lw * 4096 + l * 16, omask = lw ? 0x0Au : 0x05u;
    u2 hk;
    _Pragma("unroll 1") for (unsigned it = 0; it < SPIN; ++it) {
      asm volatile("global_load_dwordx2 %0, %1, %2 sc1\n s_waitcnt vmcnt(0)" : "=v"(hk) : "v"(r15 * 128), "s"(hb) : "memory");
      if (it == 0) lsig(myFL, 1);
      if (__ballot(hk[0] != tag) == 0ull) break;
      SLEEP(2);
    }
    __builtin_amdgcn_s_setprio(3);
    STMP(2);
    const unsigned t0 = NOW();
    unsigned rel = hk[1] - t0; rel = rel < 1000u ? rel : 0u;
    rel = max(rel, (unsigned)__builtin_amdgcn_update_dpp(0, (int)rel, 0xB1, 0xf, 0xf, false));
    rel = max(rel, (unsigned)__builtin_amdgcn_update_dpp(0, (int)rel, 0x4E, 0xf, 0xf, false));
    int gx;
    { const unsigned r0 = __builtin_amdgcn_readlane(rel, 0), r1 = __builtin_amdgcn_readlane(rel, 4), r2 = __builtin_amdgcn_readlane(rel, 8), r3 = __builtin_amdgcn_readlane(rel, 12);
      gx = max(r0, r1) > max(r2, r3) ? (r0 > r1 ? 3 : 2) : (r2 > r3 ? 1 : 0);
      if (OHP) ST64(((u2){tag, t0 + max(max(r0, r1), max(r2, r3)) + OD}), l == 0 ? WS_OH + (mb * 32 + hd) * 128 : WS_SZ); }
#define GLD(gq) do { _Pragma("unroll") for (int i = 0; i < 4; ++i) { const char* sb = kvb + (((gq) ^ gx) * 4 + (i & 2)) * 2048; \
      asm volatile("global_load_dwordx4 %0, %1, %2 offset:%3 sc1" : "=v"(pv[(gq) * 4 + i][0]) : "v"(lv16), "s"(sb), "i"((i & 1) * 2048) : "memory"); \
      asm volatile("global_load_dwordx4 %0, %1, %2 offset:%3 sc1" : "=v"(pv[(gq) * 4 + i][1]) : "v"(lv16), "s"(sb), "i"((i & 1) * 2048 + 1024) : "memory"); } } while (0)
#define ZLD() do { asm volatile("global_load_dwordx4 %0, %1, %2 sc1" : "=v"(zv0) : "v"(zoff), "s"(zb) : "memory"); \
      asm volatile("global_load_dwordx4 %0, %1, %2 offset:16 sc1" : "=v"(zv1) : "v"(zoff), "s"(zb) : "memory"); } while (0)
#pragma unroll
    for (int gq = 0; gq < 4; ++gq) {
      const unsigned rq = __builtin_amdgcn_readlane(rel, 4 * (gq ^ gx));
      _Pragma("unroll 1") for (unsigned it = 0; it < SPIN; ++it) if (NOW() - t0 >= rq) break;
      if (gq == 3) ZLD();
      GLD(gq);
    }
    if (!OHP) ST64(((u2){tag, NOW() + OD}), l == 0 ? WS_OH + (mb * 32 + hd) * 128 : WS_SZ);
    STMP(5);
    lwait(adFL, omask, 4);
#pragma unroll
    for (int i = 0; i < 8; ++i) RD128(pq[i], pa, (i >> 2) * 8192 + (i & 3) * 1024);
#pragma unroll
    for (int gq = 0; gq < 4; ++gq) {
      SBAR(); WAITV((gq == 0 ? 27 : gq == 1 ? 19 : gq == 2 ? 11 : 1) - OHP);
      _Pragma("unroll 1") for (unsigned it = 0; it < SPIN; ++it) {
#pragma unroll
        for (int i = 0; i < 4; ++i) asm volatile("" : "+v"(pv[gq * 4 + i][0]), "+v"(pv[gq * 4 + i][1]));
        unsigned m = umax3(pv[gq * 4][0][0], pv[gq * 4][1][0], pv[gq * 4 + 1][0][0]);
        m = umax3(m, pv[gq * 4 + 1][1][0], pv[gq * 4 + 2][0][0]);
        m = umax3(m, pv[gq * 4 + 2][1][0], pv[gq * 4 + 3][0][0]);
        m = max(m, pv[gq * 4 + 3][1][0]);
        if (gq == 3) { asm volatile("" : "+v"(zv0), "+v"(zv1)); m = umax3(m, zv0[0], zv1[0]); }
        if (__ballot(m == POIS) == 0ull) break;
        RETRY();
        SLEEP(2);
        if (gq == 3) ZLD();
        GLD(gq);
        WAITV(0);
      }
      f4 ga[4];
#pragma unroll
      for (int i = 0; i < 4; ++i)
#pragma unroll
        for (int t = 0; t < 4; ++t) ga[t] = MFMA16(S8(sel[t & 1]), S8(pv[gq * 4 + i][t >> 1]), i ? ga[t] : (f4)(0.f));
#pragma unroll
      for (int t = 0; t < 4; ++t) pv[gq * 4 + (t >> 1)][t & 1] = __builtin_bit_cast(u4, ga[t]);
    }
    STMP(3);
    WAITL(0);
#pragma unroll
    for (int i = 0; i < 8; i += 2) asm volatile("" : "+v"(pq[i]), "+v"(pq[i + 1]));
    u4 kA[2], zA;
    {
      float z[8];
#pragma unroll
      for (int e = 0; e < 4; ++e) { z[e] = __uint_as_float(zv0[e]); z[4 + e] = __uint_as_float(zv1[e]); }
#define ZR(n) "v_add_f32_dpp %0, %0, %0 row_ror:" #n " row_mask:0xf bank_mask:0xf\n v_add_f32_dpp %1, %1, %1 row_ror:" #n " row_mask:0xf bank_mask:0xf\n" \
              "v_add_f32_dpp %2, %2, %2 row_ror:" #n " row_mask:0xf bank_mask:0xf\n v_add_f32_dpp %3, %3, %3 row_ror:" #n " row_mask:0xf bank_mask:0xf\n" \
              "v_add_f32_dpp %4, %4, %4 row_ror:" #n " row_mask:0xf bank_mask:0xf\n v_add_f32_dpp %5, %5, %5 row_ror:" #n " row_mask:0xf bank_mask:0xf\n" \
              "v_add_f32_dpp %6, %6, %6 row_ror:" #n " row_mask:0xf bank_mask:0xf\n v_add_f32_dpp %7, %7, %7 row_ror:" #n " row_mask:0xf bank_mask:0xf\n"
      asm volatile("s_nop 1\n" ZR(8) ZR(4) ZR(2) ZR(1) : "+v"(z[0]), "+v"(z[1]), "+v"(z[2]), "+v"(z[3]), "+v"(z[4]), "+v"(z[5]), "+v"(z[6]), "+v"(z[7]));
#pragma unroll
      for (int e = 0; e < 4; ++e) zA[e] = pk2(z[2 * e], z[2 * e + 1]);
    }
    float inv[8];
#pragma unroll
    for (int i = 0; i < 8; ++i) { f4 dn = MFMA16(S8(zA), S8(pq[i]), (f4)(0.f)); inv[i] = dn[0]; }
#pragma unroll
    for (int t = 0; t < 4; ++t) {
#define PF(gq) __builtin_bit_cast(f4, pv[(gq) * 4 + (t >> 1)][t & 1])
      f4 s = (PF(0) + PF(1)) + (PF(2) + PF(3));
      kA[t >> 1][(t & 1) * 2] = pk2(s[0], s[1]); kA[t >> 1][(t & 1) * 2 + 1] = pk2(s[2], s[3]);
    }
    f4 o0[8], o1[8];
#pragma unroll
    for (int i = 0; i < 8; ++i) {
      o0[i] = MFMA16(S8(kA[0]), S8(pq[i]), (f4)(0.f)); o1[i] = MFMA16(S8(kA[1]), S8(pq[i]), (f4)(0.f));
      inv[i] = __builtin_amdgcn_rcpf(inv[i] + 1e-6f);
    }
#pragma unroll
    for (int i = 0; i < 8; ++i) {
      o0[i] *= inv[i]; o1[i] *= inv[i];
      u4 o; o[0] = min(pk2(o0[i][0], o0[i][1]), POIS - 1u); o[1] = pk2(o0[i][2], o0[i][3]); o[2] = pk2(o1[i][0], o1[i][1]); o[3] = pk2(o1[i][2], o1[i][3]);
      ST128(o, WS_OF + (mb * 32 + hd) * 8192 + (i >> 1) * 2048 + (i & 1) * 256 + g4 * 512 + r15 * 16);
    }
    lsig(myFL, 2);
    STMP(4);
    STFLUSH();
    asm volatile(".Lend_l:");
  }

}

extern "C" void kernel_launch(void* const* d_in, const int* in_sizes, int n_in, void* d_out, int out_size,
                              void* d_ws, size_t ws_size, hipStream_t stream) {
  (void)hipFuncSetAttribute((const void*)fused, hipFuncAttributeMaxDynamicSharedMemorySize, LDS_SZ);
  fused<<<256, 512, LDS_SZ, stream>>>((const u16*)d_in[0], (const u16*)d_in[1], (const u16*)d_in[2], (const u16*)d_in[4],
                                      (const u16*)d_in[5], (const u16*)d_in[6], (const u16*)d_in[7], (char*)d_ws, (u16*)d_out);
}
```

```cpp
#include <hip/hip_runtime.h>
#include <stdint.h>

typedef unsigned short u16;
typedef short s4 __attribute__((ext_vector_type(4)));
typedef short s8 __attribute__((ext_vector_type(8)));
typedef float f4 __attribute__((ext_vector_type(4)));
typedef float f16v __attribute__((ext_vector_type(16)));
typedef float f2 __attribute__((ext_vector_type(2)));
typedef unsigned u4 __attribute__((ext_vector_type(4)));
typedef unsigned u2 __attribute__((ext_vector_type(2)));
typedef __bf16 bf2 __attribute__((ext_vector_type(2)));
#define LAS __attribute__((address_space(3)))
#define DEV __device__ __forceinline__

constexpr int D = 1024, F = 3584;
#define NOMOE 0
#define KD 310
#define OD 190
#define OHP 1
#define FS 128
#define GSL 8
#define YSL 16
#define TP1 0
#define TP2 3
#define TLBW 0
#define FULL 1
#define HPRIO 1
#define OWN 1
#define NOPOIS 0
#define CAP 7
#define RD 4
#define DYN 1
#define JX 0
#define NOSKIP 0
#define GRP 8
#define PV 4
#define U1 4
#define U2 2
#define ROT 0

constexpr unsigned WS_E = 0;
constexpr unsigned WS_YF = 524288;
constexpr unsigned WS_CAND = 12288;
constexpr unsigned WS_GF = 589824;
constexpr unsigned WS_RG = 49152;
constexpr unsigned WS_ST = 65536;
constexpr unsigned WS_HK = 98304;
constexpr unsigned WS_ZP = 131072;
constexpr unsigned WS_CS = 196608;
constexpr unsigned WS_OH = 458752;
constexpr unsigned WS_KVP = 1u << 20;
constexpr unsigned WS_GP = 2u << 20;
constexpr unsigned WS_YP = 3u << 20;
constexpr unsigned WS_OF = 5u << 20;
constexpr unsigned WS_SZ = 9u << 20;
#define NR 4
#define RGO(tag) ((64u << 20) + ((tag) & (NR - 1u)) * (10u << 20))
#define MKWS(tag) char* const wsx = ws + RGO(tag); const __amdgpu_buffer_rsrc_t wsr = __builtin_amdgcn_make_buffer_rsrc(wsx, 0, WS_SZ, 0x00020000)

constexpr int STG = 40960;
constexpr int L_KVX = 0;
constexpr int L_PQ = 12288;
constexpr int L_M = 28672, L_RS = L_M + 512, L_CS = L_RS + 512, L_SEL = L_CS + 768, L_FL = L_SEL + 64;
constexpr int L_WO = 32768;
constexpr int LDS_SZ = 163840;
constexpr unsigned POIS = 0xFFFFFFFFu;

DEV unsigned pk2(float a, float b) { bf2 v; v[0] = (__bf16)a; v[1] = (__bf16)b; return __builtin_bit_cast(unsigned, v); }
DEV float lo(unsigned u) { return __uint_as_float(u << 16); }
DEV float hi(unsigned u) { return __uint_as_float(u & 0xFFFF0000u); }
DEV float rbf(float a) { return lo(pk2(a, a)); }
DEV float dot2(unsigned a, unsigned b, float c) { return __builtin_amdgcn_fdot2_f32_bf16(__builtin_bit_cast(bf2, a), __builtin_bit_cast(bf2, b), c, false); }
#define RD128(dst, addr, OFF) asm volatile("ds_read_b128 %0, %1 offset:%2" : "=v"(dst) : "v"(addr), "i"(OFF) : "memory")
#define RDTR(dst, addr, OFF) asm volatile("ds_read_b64_tr_b16 %0, %1 offset:%2" : "=v"(dst) : "v"(addr), "i"(OFF) : "memory")
#define SBAR() __builtin_amdgcn_sched_barrier(0)
#define CAT(x, y) __builtin_shufflevector(x, y, 0, 1, 2, 3, 4, 5, 6, 7)
#define WAITV(n) asm volatile("s_waitcnt vmcnt(%0)" ::"i"(n) : "memory")
#define WAITL(n) asm volatile("s_waitcnt lgkmcnt(%0)" ::"i"(n) : "memory")
#define MEMB() asm volatile("" ::: "memory")
#define SLEEP(n) asm volatile("s_sleep %0" ::"i"(n) : "memory")
#define BAR() asm volatile("s_barrier" ::: "memory")
#define GL0(vo, sb, m0v) asm volatile("s_mov_b32 m0, %2\n s_nop 0\n global_load_lds_dwordx4 %0, %1" ::"v"(vo), "s"(sb), "s"(m0v) : "memory")
#define GLI(vo, sb, OFF) asm volatile("global_load_lds_dwordx4 %0, %1 offset:%2" ::"v"(vo), "s"(sb), "i"(OFF) : "memory")
#define MFMA16(a, b, c) __builtin_amdgcn_mfma_f32_16x16x32_bf16(a, b, c, 0, 0, 0)
#define S8(x) __builtin_bit_cast(s8, x)
#define LD128(off) __builtin_amdgcn_raw_buffer_load_b128(wsr, (off), 0, 16)
#define LD64(off) __builtin_amdgcn_raw_buffer_load_b64(wsr, (off), 0, 16)
#define ST128(v, off) __builtin_amdgcn_raw_buffer_store_b128((v), wsr, (off), 0, 16)
#define ST64(v, off) __builtin_amdgcn_raw_buffer_store_b64((v), wsr, (off), 0, 16)
#define OFA(i) (WS_OF + ((mb * 32 + hd) * 8 + (i)) * 1024 + l * 16)
#define NOW() ((unsigned)__builtin_amdgcn_s_memrealtime())
constexpr unsigned SPIN = 1u << 21;
DEV const char* uptr(const char* p) {
  unsigned long long v = (unsigned long long)p;
  unsigned lo_ = __builtin_amdgcn_readfirstlane((unsigned)v), hi_ = __builtin_amdgcn_readfirstlane((unsigned)(v >> 32));
  return (const char*)(((unsigned long long)hi_ << 32) | lo_);
}
DEV void lsig(unsigned addr, unsigned val) { asm volatile("s_waitcnt lgkmcnt(0)\n ds_write_b32 %0, %1" ::"v"(addr), "v"(val) : "memory"); }
template <int SL = 0> DEV void lwait(unsigned addr, unsigned mask, unsigned target) {
  _Pragma("unroll 1") for (unsigned it = 0; it < SPIN; ++it) {
    unsigned v;
    asm volatile("ds_read_b32 %0, %1\n s_waitcnt lgkmcnt(0)" : "=v"(v) : "v"(addr) : "memory");
    if (((unsigned)__ballot(v < target) & mask) == 0u) break;
    if (SL) SLEEP(SL);
  }
}
DEV unsigned umax3(unsigned a, unsigned b, unsigned c) { return max(max(a, b), c); }
template <int C> DEV float dppf(float v) { return __int_as_float(__builtin_amdgcn_update_dpp(0, __float_as_int(v), C, 0xf, 0xf, false)); }
DEV unsigned wave_max(unsigned v) {
  v = max(v, (unsigned)__builtin_amdgcn_update_dpp(0, (int)v, 0x111, 0xf, 0xf, false));
  v = max(v, (unsigned)__builtin_amdgcn_update_dpp(0, (int)v, 0x112, 0xf, 0xf, false));
  v = max(v, (unsigned)__builtin_amdgcn_update_dpp(0, (int)v, 0x114, 0xf, 0xf, false));
  v = max(v, (unsigned)__builtin_amdgcn_update_dpp(0, (int)v, 0x118, 0xf, 0xf, false));
  v = max(v, (unsigned)__builtin_amdgcn_update_dpp(0, (int)v, 0x142, 0xa, 0xf, false));
  v = max(v, (unsigned)__builtin_amdgcn_update_dpp(0, (int)v, 0x143, 0xc, 0xf, false));
  return (unsigned)__builtin_amdgcn_readlane((int)v, 63);
}
DEV unsigned phi2(f2 u, f2 rs) {
  f2 t = u * rs;
  f2 a = t * 1.44269504f, p = t + 1.f, r;
  r[0] = __builtin_amdgcn_fmed3f(__builtin_amdgcn_exp2f(a[0]), p[0], 1.f);
  r[1] = __builtin_amdgcn_fmed3f(__builtin_amdgcn_exp2f(a[1]), p[1], 1.f);
  r += 1e-6f;
  return pk2(r[0], r[1]);
}

#define STDECL
#define STMP(i)
#define BST(i)
#define STFLUSH()
#define RETRY()
#define RETRY2()

DEV float half_sum(float v) {
  asm volatile("s_nop 1\n v_add_f32_dpp %0, %0, %0 row_ror:8 row_mask:0xf bank_mask:0xf\n s_nop 1\n v_add_f32_dpp %0, %0, %0 row_ror:4 row_mask:0xf bank_mask:0xf\n"
               "s_nop 1\n v_add_f32_dpp %0, %0, %0 row_ror:2 row_mask:0xf bank_mask:0xf\n s_nop 1\n v_add_f32_dpp %0, %0, %0 row_ror:1 row_mask:0xf bank_mask:0xf\n s_nop 1" : "+v"(v));
  auto r = __builtin_amdgcn_permlane16_swap(__float_as_uint(v), __float_as_uint(v), false, false);
  return __uint_as_float(r[0]) + __uint_as_float(r[1]);
}

struct FQ { s8 a[4]; s4 b[12]; };
template <bool B> struct BC { static constexpr bool v = B; };

__global__ __launch_bounds__(512) void fused(const u16* __restrict__ x, const u16* __restrict__ wqkv, const u16* __restrict__ wo,
                                             const u16* __restrict__ wrt, const u16* __restrict__ wg, const u16* __restrict__ wu,
                                             const u16* __restrict__ wd, char* __restrict__ ws, u16* __restrict__ out) {
  extern __shared__ __attribute__((aligned(16))) char lds[];
  asm volatile(".Lcode_beg:");
  const int w = __builtin_amdgcn_readfirstlane(threadIdx.x >> 6), l = threadIdx.x & 63;
  STDECL;
  STMP(0);
  const int g = blockIdx.x, xcd = g & 7, j = g >> 3, ii = j >> 2;
  unsigned cw = 0;
  if (w == 7) {
    unsigned long long pc; unsigned off, len, l1, l2, l3;
    asm volatile("s_getpc_b64 %0\n.Lcode_here:" : "=s"(pc));
    asm volatile("s_mov_b32 %0, (.Lcode_here - .Lcode_beg)" : "=s"(off));
    asm volatile("s_mov_b32 %0, (.Lend_c - .Lcode_beg)" : "=s"(len));
    asm volatile("s_mov_b32 %0, (.Lend_l - .Lcode_beg)" : "=s"(l1));
    asm volatile("s_mov_b32 %0, (.Lend_h - .Lcode_beg)" : "=s"(l2));
    asm volatile("s_mov_b32 %0, (.Lend_s - .Lcode_beg)" : "=s"(l3));
    len = max(max(len, l3), max(l1, l2));
    const unsigned o = min((unsigned)(l * 32 + j) * 64u, len - 4u);
    asm volatile("global_load_dword %0, %1, %2" : "+v"(cw) : "v"(o), "s"(pc - off) : "memory");
  }
#define PTAB 0xA98BFEDC76542103ull
#define PHT 0x6624924999924924ull
#define CX 4
  const int mb = (xcd & 1) * 8 + ii, hp = (xcd >> 1) * 4 + (j & 3);
  const bool sp = (hp & 7) == 3;
  const int rot = 2 * xcd + 4 * (ii & 3);
  const int nb = hp;
#define PAIRAT(p) ((int)(PTAB >> (4 * (p))) & 15)
#define HEADAT(s) (2 * PAIRAT((s) >> 1) + ((s) & 1))
  const int ph = (int)(PHT >> (2 * j)) & 3;
  const unsigned ldsb = (unsigned)(size_t)(LAS char*)lds;
  const unsigned adFL = ldsb + L_FL + 4 * (l & 7), myFL = ldsb + L_FL + 4 * w;
  const __amdgpu_buffer_rsrc_t wse = __builtin_amdgcn_make_buffer_rsrc(ws, 0, 64, 0x00020000);
  const int r15 = l & 15, g4 = l >> 4, q = (l & 15) >> 2, p = l & 3;
  const char* wob = uptr((const char*)wo + nb * 128);

  if (w != 4 && w != 5) {
  const unsigned ev0 = __builtin_amdgcn_raw_buffer_load_b32(wse, WS_E, 0, 16);
  if (w >= 6) {
    const int mw = w - 6;
    const unsigned tag = __builtin_amdgcn_readfirstlane(ev0) + 1u, tag2 = tag * 0x9E3779B1u;
    MKWS(tag);
    auto helper = [&](auto SP) {
    constexpr bool spare = decltype(SP)::v, moe = !spare && !NOMOE;
    int nbar = 0;
#define TICK() do { BST(nbar); SBAR(); BAR(); SBAR(); ++nbar; } while (0)
#define DTICK(lim) do { if (nbar < (lim)) TICK(); else SLEEP(2); } while (0)
    const int hd = hp * 2 + mw;
    const u4 pz = (u4)(POIS);
    const int chunk = 4 * (hp - (hp > 3) - (hp > 11)) + (ii & 3), ksp = (xcd & 1) * 2 + (ii >> 2), chg = chunk ^ (mw * CX);
    const int np = l & 7, kset = (l >> 3) & 1;
    u4 gw[4][8], dw[2][8];
    const __amdgpu_buffer_rsrc_t wgr = __builtin_amdgcn_make_buffer_rsrc((void*)(mw ? wu : wg), 0, D * F * 2, 0x00020000);
    const __amdgpu_buffer_rsrc_t wdr = __builtin_amdgcn_make_buffer_rsrc((void*)wd, 0, D * F * 2, 0x00020000);
    const __amdgpu_buffer_rsrc_t wor = __builtin_amdgcn_make_buffer_rsrc((void*)wo, 0, D * D * 2, 0x00020000);
    const unsigned gvo = ((256 * ksp + 32 * kset + 8 * g4) * F + 64 * chg + 8 * np) * 2;
    const unsigned dvo = ((64 * chunk + 8 * g4) * D + 256 * ksp + 128 * mw + 8 * r15) * 2;
    auto burst = [&](int k) {
      if (moe) {
#pragma unroll
        for (int i = 0; i < 16; ++i) {
          if (k < 2) gw[(2 * k + (i >> 3)) & 3][i & 7] = __builtin_amdgcn_raw_buffer_load_b128(wgr, gvo, (128 * k + 64 * (i >> 3) + (i & 7)) * F * 2, 2);
          else dw[i >> 3][i & 7] = __builtin_amdgcn_raw_buffer_load_b128(wdr, dvo, (32 * (i >> 3) + (i & 7)) * D * 2, 2);
        }
      }
    };
    auto transp = [&](u4(&v)[8]) {
      if (moe) {
        u4 f[8];
#pragma unroll
        for (int c = 0; c < 8; ++c)
#pragma unroll
          for (int e = 0; e < 4; ++e) {
            const unsigned ev = v[2 * e][c >> 1], od = v[2 * e + 1][c >> 1];
            f[c][e] = (c & 1) ? __builtin_amdgcn_perm(od, ev, 0x07060302u) : __builtin_amdgcn_perm(od, ev, 0x05040100u);
          }
#pragma unroll
        for (int c = 0; c < 8; ++c) { v[c] = f[c]; asm volatile("" : "+v"(v[c])); }
      }
    };
    auto pois = [&](int i) {
      if (i < 2) ST128(pz, WS_KVP + (hd * 16 + mb) * 2048 + i * 1024 + l * 16);
      else if (i == 2) ST128(pz, WS_ZP + (hd * 16 + mb) * 128 + (l & 7) * 16);
      else if (!NOPOIS) ST128(pz, OFA(i - 3));
    };
    unsigned wt0, wt1, wt2 = 0, wt3 = 0;
    auto touch = [&]() {
      const unsigned to = (j * 32 + mw * 16 + (l >> 3)) * 2048 + (xcd >> 1) * 512 + (l & 7) * 64;
      wt0 = __builtin_amdgcn_raw_buffer_load_b32(wor, to, 0, 0); wt1 = __builtin_amdgcn_raw_buffer_load_b32(wor, to, 16384, 0);
      if (TLBW) {
        const __amdgpu_buffer_rsrc_t outr = __builtin_amdgcn_make_buffer_rsrc(out, 0, 2048 * D * 2, 0x00020000);
        wt2 = __builtin_amdgcn_raw_buffer_load_b32(outr, (mb * 128 + mw * 64 + (l & 3) * 16) * 2048 + nb * 128, 0, 0);
        wt3 = __builtin_amdgcn_raw_buffer_load_b32(wsr, (l & 1) ? WS_GP + g * 4096 + mw * 2048 : WS_YP + (g >> 2) * 32768 + (l & 2) * 8192, 0, 0);
      }
    };
    s4 sb[24];
    auto colrd = [&](int h) {
      const unsigned bo = ldsb + (1 + h) * STG + 16384 + (8 * g4 + q) * 128 + (p & 1) * 8;
      const int sw = ((q >> 1) << 2) | ((g4 & 1) << 1);
      const unsigned b0 = bo + (((mw * 4 + (p >> 1)) ^ sw) * 16), b1 = bo + (((mw * 4 + 2 + (p >> 1)) ^ sw) * 16);
#pragma unroll
      for (int n = 0; n < 12; ++n) { RDTR(sb[2 * n], (n & 1) ? b1 : b0, ((n % 6) >> 1) * 8192 + (n / 6) * 4096); RDTR(sb[2 * n + 1], (n & 1) ? b1 : b0, ((n % 6) >> 1) * 8192 + (n / 6) * 4096 + 512); }
      WAITL(0);
      SBAR();
    };
    auto colsum = [&]() {
      const u4 ones = (u4)(0x3F803F80u);
      f4 c[6];
#pragma unroll
      for (int n = 0; n < 12; ++n) c[n % 6] = MFMA16(S8(ones), CAT(sb[2 * n], sb[2 * n + 1]), n >= 6 ? c[n % 6] : (f4)(0.f));
      const u4 csg = (u4){__float_as_uint(g4 ? c[3][0] : c[0][0]), __float_as_uint(g4 ? c[4][0] : c[1][0]), __float_as_uint(g4 ? c[5][0] : c[2][0]), tag};
      ST128(csg, l < 32 ? WS_CS + (hd * 16 + mb) * 512 + l * 16 : WS_SZ);
    };
    u4 stv, cv[8], cd; float cs[3];
    auto gload = [&]() {
      stv = LD128(WS_ST + (mb * 128 + mw * 64 + l) * 16);
      cd = LD128(WS_CAND + (g & 31) * 128 + (l & 7) * 16);
#pragma unroll
      for (int i = 0; i < 8; ++i) cv[i] = LD128(WS_CS + hd * 8192 + i * 1024 + l * 16);
    };
    auto gbad = [&]() {
      unsigned bad = (stv[1] ^ tag) | (stv[3] ^ tag) | (cd[1] ^ tag) | (cd[3] ^ tag);
#pragma unroll
      for (int i = 0; i < 8; ++i) bad |= cv[i][3] ^ tag;
      return __ballot(bad != 0u) != 0ull;
    };
    int idx;
    auto gfin = [&]() {
      if (gbad()) { RETRY(); _Pragma("unroll 1") for (unsigned it = 0; it < SPIN; ++it) { SLEEP(2); gload(); if (!gbad()) break; } }
#pragma unroll
      for (int e = 0; e < 3; ++e) {
        float a = __uint_as_float(cv[0][e]);
#pragma unroll
        for (int i = 1; i < 8; ++i) a += __uint_as_float(cv[i][e]);
        auto r = __builtin_amdgcn_permlane32_swap(__float_as_uint(a), __float_as_uint(a), false, false);
        cs[e] = __uint_as_float(r[0]) + __uint_as_float(r[1]);
      }
      asm volatile("" : "+v"(cs[0]), "+v"(cs[1]), "+v"(cs[2]) : "v"(wt0), "v"(wt1), "v"(wt2), "v"(wt3));
      cd[0] = max(cd[0], cd[2]);
      idx = (int)(cd[0] & 2047u);
    };
    auto lpub = [&]() {
      *(LAS float*)(lds + L_M + (mw * 64 + l) * 4) = __uint_as_float(stv[0]);
      *(LAS float*)(lds + L_RS + (mw * 64 + l) * 4) = __uint_as_float(stv[2]);
      if (l < 32) {
#pragma unroll
        for (int e = 0; e < 3; ++e) *(LAS float*)(lds + L_CS + mw * 384 + ((3 * g4 + e) * 16 + r15) * 4) = cs[e];
      }
      if (mw == 0 && l < 8) *(LAS int*)(lds + L_SEL + l * 4) = idx;
      lsig(myFL, 1u);
    };
    u4 xs[4];
    asm volatile("" ::"s"(wg), "s"(wu), "s"(wd), "v"(cw));
    if (HPRIO) __builtin_amdgcn_s_setprio(HPRIO);
    TICK();
    if (spare) {
      TICK();
      if (ii >> 2) TICK();
      colrd(ii >> 2);
      if (!(ii >> 2)) TICK();
      u4 kk[8];
      auto kload = [&]() {
#pragma unroll
        for (int i = 0; i < 8; ++i) kk[i] = LD128(WS_RG + mw * 8192 + i * 1024 + l * 16);
      };
      kload();
      TICK();
      _Pragma("unroll 1") for (unsigned it = 0; it < SPIN; ++it) {
        unsigned bad = 0;
#pragma unroll
        for (int i = 0; i < 8; ++i) { bad |= (kk[i][1] ^ tag) | (kk[i][3] ^ tag); asm volatile("" : "+v"(bad)); }
        if (__ballot(bad != 0u) == 0ull) break;
        RETRY();
        kload();
        DTICK(7);
      }
      __builtin_amdgcn_s_setprio(3);
      unsigned m1 = max(kk[0][0], kk[0][2]), m2 = min(kk[0][0], kk[0][2]), m3 = 0;
#pragma unroll
      for (int i = 2; i < 16; ++i) {
        const unsigned a = kk[i >> 1][(i & 1) * 2];
        asm("v_med3_u32 %0, %1, %2, %0" : "+v"(m3) : "v"(m2), "v"(a));
        asm("v_med3_u32 %0, %1, %2, %0" : "+v"(m2) : "v"(m1), "v"(a));
        m1 = max(m1, a);
      }
      unsigned mine8 = 0;
#pragma unroll
      for (int it = 0; it < 8; ++it) {
        const unsigned km = wave_max(m1);
        mine8 = l == it ? km : mine8;
        const bool win = m1 == km;
        if (it < 7) { m1 = win ? m2 : m1; m2 = win ? m3 : m2; m3 = win ? 0u : m3; }
        if (it == 3 && nbar < 7) TICK();
      }
      if (__builtin_expect(__ballot(m1 == 0u) != 0ull, 0)) {
        unsigned lim = 0;
#pragma unroll 1
        for (int it = 0; it < 8; ++it) {
          unsigned m = 0;
#pragma unroll
          for (int i = 0; i < 8; ++i) m = umax3(m, kk[i][0] - lim, kk[i][2] - lim);
          lim += wave_max(m);
          mine8 = l == it ? lim : mine8;
        }
      }
      ST64(((u2){mine8, tag}), l < 8 ? WS_CAND + ((hp >> 3) * 16 + mb) * 128 + (mw ? 7 - l : l) * 16 + mw * 8 : WS_SZ);
      __builtin_amdgcn_s_setprio(HPRIO);
      STMP(2);
      TICK();
      colsum();
#pragma unroll
      for (int i = 0; i < 11; ++i) { if (i == 5) DTICK(9); pois(i); }
      touch();
      while (nbar < 10) TICK();
      gload();
      TICK(); TICK();
      WAITV(0);
      gfin();
      STMP(3);
      TICK(); TICK(); TICK();
      lpub();
    } else {
      if (PV == 5) { pois(0); pois(1); }
      TICK();
      if (PV == 4) { pois(0); pois(1); pois(2); }
      if (PV == 5) { pois(2); pois(3); }
      if (ii >> 2) { TICK(); if (PV == 4) pois(3); if (PV >= 4) pois(4); }
      colrd(ii >> 2);
      TICK();
      if (!(ii >> 2)) { if (PV == 4) pois(3); if (PV >= 4) pois(4); }
      colsum();
      if (!(ii >> 2)) TICK();
      STMP(1);
      for (int i = 0; i < ph; ++i) TICK();
      burst(0); touch();
      TICK();
      if (PV == 2) { pois(0); pois(1); pois(2); pois(3); pois(4); }
      TICK(); TICK();
      transp(gw[0]);
      TICK();
      transp(gw[1]);
      TICK();
      if (PV == 3) { pois(0); pois(1); pois(2); pois(3); pois(4); SBAR(); }
      burst(1);
      SBAR();
      if (PV == 0) { pois(0); pois(1); pois(2); pois(3); pois(4); }
      TICK();
      if (PV == 1) { pois(0); pois(1); pois(2); pois(3); pois(4); }
      pois(5); pois(6); pois(7); pois(8); pois(9); pois(10);
      TICK(); TICK();
      transp(gw[2]);
      WAITV(0);
      if (ph < 2) TICK();
      transp(gw[3]);
      if (ph < 1) TICK();
      SBAR();
      gload();
      TICK();
      gfin();
      u4 sv;
      if (moe) {
        const u16* xp = x + (size_t)idx * D + 256 * ksp + 32 * kset + 8 * g4;
#pragma unroll
        for (int it = 0; it < 4; ++it) xs[it] = *(const u4*)(xp + 64 * it);
        sv = LD128(WS_ST + idx * 16);
      }
      SBAR();
      lpub();
      SBAR();
      burst(2);
      STMP(3);
      SBAR();
      if (moe) {
        if (__builtin_expect(__ballot(sv[1] != tag || sv[3] != tag) != 0ull, 0)) {
          _Pragma("unroll 1") for (unsigned it = 0; it < SPIN; ++it) { SLEEP(1); sv = LD128(WS_ST + idx * 16); if (__ballot(sv[1] != tag || sv[3] != tag) == 0ull) break; }
        }
        const float m = __uint_as_float(sv[0]), rs = __uint_as_float(sv[2]);
#pragma unroll
        for (int it = 0; it < 4; ++it) {
#pragma unroll
          for (int e = 0; e < 4; ++e) { const unsigned d = pk2(lo(xs[it][e]) - m, hi(xs[it][e]) - m); xs[it][e] = pk2(lo(d) * rs, hi(d) * rs); }
          asm volatile("" : "+v"(xs[it]));
        }
      }
    }
    __builtin_amdgcn_s_setprio(TP1);
    STMP(4);
    float sg;
    {
      const unsigned mono = cd[0] >> 16, rb = (mono & 0x8000u) ? (mono & 0x7FFFu) : (~mono & 0xFFFFu);
      sg = rbf(1.f / (1.f + __expf(-lo(rb))));
    }
    if (moe) {
      STMP(5);
      f4 acc[8];
#pragma unroll
      for (int it = 0; it < 4; ++it)
#pragma unroll
        for (int c = 0; c < 8; ++c) acc[c] = MFMA16(S8(gw[it][c]), S8(xs[it]), it ? acc[c] : (f4)(0.f));
#pragma unroll
      for (int i = 0; i < 4; ++i) {
        u4 s;
#pragma unroll
        for (int c = 0; c < 4; ++c) {
          auto r = __builtin_amdgcn_permlane32_swap(__float_as_uint(acc[c][i]), __float_as_uint(acc[c + 4][i]), false, false);
          s[c] = __float_as_uint(__uint_as_float(r[0]) + dppf<0x128>(__uint_as_float(r[1])));
        }
        if (r15 < 8) ST128(s, WS_GP + ((chg * 4 + ksp) * 2 + mw) * 2048 + (g4 >> 1) * 1024 + (4 * (g4 & 1) + i) * 128 + r15 * 16);
      }
      WAITV(0);
      if (l == 0) ST64(((u2){tag, tag2}), WS_GF + chg * 1024 + (ksp * 2 + mw) * FS);
      STMP(6);
      transp(dw[0]); transp(dw[1]);
      _Pragma("unroll 1") for (unsigned it = 0; it < SPIN; ++it) {
        u2 f = LD64(WS_GF + chunk * 1024 + (l & 7) * FS);
        if (__ballot(f[0] != tag || f[1] != tag2) == 0ull) break;
        SLEEP(GSL);
      }
      STMP(7);
      __builtin_amdgcn_s_setprio(TP2);
      u4 ha[2];
      {
        u4 pg[4][4];
#pragma unroll
        for (int ks = 0; ks < 4; ++ks)
#pragma unroll
          for (int t = 0; t < 4; ++t)
            pg[ks][t] = LD128(WS_GP + ((chunk * 4 + ks) * 4 + t) * 1024 + (4 * kset + g4) * 128 + np * 16);
        f4 gq[4];
#pragma unroll
        for (int t = 0; t < 4; ++t)
          gq[t] = (__builtin_bit_cast(f4, pg[0][t]) + __builtin_bit_cast(f4, pg[1][t])) + (__builtin_bit_cast(f4, pg[2][t]) + __builtin_bit_cast(f4, pg[3][t]));
        float hv[8];
#pragma unroll
        for (int e = 0; e < 8; ++e) {
          const float gg = rbf(gq[e >> 2][e & 3]), uu = rbf(gq[2 + (e >> 2)][e & 3]);
          hv[e] = rbf(gg / (1.f + __expf(-gg))) * uu;
        }
#pragma unroll
        for (int e = 0; e < 4; ++e) { ha[0][e] = pk2(hv[2 * e], hv[2 * e + 1]); ha[1][e] = (unsigned)__builtin_amdgcn_update_dpp(0, (int)ha[0][e], 0x128, 0xf, 0xf, false); }
      }
#pragma unroll
      for (int it = 0; it < 2; ++it)
#pragma unroll
        for (int c = 0; c < 8; ++c) acc[c] = MFMA16(S8(ha[it]), S8(dw[it][c]), it ? acc[c] : (f4)(0.f));
      if (g4 < 2) {
#pragma unroll
        for (int i = 0; i < 4; ++i) {
          const unsigned yo = WS_YP + ((chunk * 8 + 4 * g4 + i) * D + 256 * ksp + 128 * mw + 4 * r15) * 4;
          ST128(__builtin_bit_cast(u4, ((f4){acc[0][i], acc[1][i], acc[2][i], acc[3][i]})), yo);
          ST128(__builtin_bit_cast(u4, ((f4){acc[4][i], acc[5][i], acc[6][i], acc[7][i]})), yo + 256);
        }
      }
      WAITV(0);
      if (l == 0) ST64(((u2){tag, tag2}), WS_YF + (ksp * 2 + mw) * 8192 + chunk * FS);
      STMP(8);
    }
    unsigned mine = (unsigned)__ballot(!NOMOE && l < 8 && (idx >> 7) == mb && (l & 1) == mw);
    if (mine) {
      if (spare) lwait<4>(adFL, 0x30u, 2);
      _Pragma("unroll 1") for (unsigned it = 0; it < SPIN; ++it) {
        u2 f = LD64(WS_YF + (nb >> 1) * 8192 + (l < 56 ? l : 0) * FS);
        if (__ballot(f[0] != tag || f[1] != tag2) == 0ull) break;
        SLEEP(YSL);
      }
      lwait<1>(adFL, 0x0Fu, 4);
      __builtin_amdgcn_s_setprio(TP2);
      while (mine) {
        const int t = __builtin_ctz(mine); mine &= mine - 1;
        u4 yv[14];
#pragma unroll
        for (int i = 0; i < 14; ++i) yv[i] = LD128(WS_YP + (((i * 4 + g4) * 8 + t) * D + (nb >> 1) * 128 + (r15 >> 3) * 64 + (nb & 1) * 32 + (r15 & 7) * 4) * 4);
        f4 y = __builtin_bit_cast(f4, yv[0]);
#pragma unroll
        for (int i = 1; i < 14; ++i) y += __builtin_bit_cast(f4, yv[i]);
        const float gt = __int_as_float(__builtin_amdgcn_readlane(__float_as_int(sg), t));
#pragma unroll
        for (int e = 0; e < 4; ++e) {
          auto r = __builtin_amdgcn_permlane32_swap(__float_as_uint(y[e]), __float_as_uint(y[e]), false, false);
          const float a = __uint_as_float(r[0]) + __uint_as_float(r[1]);
          auto r2 = __builtin_amdgcn_permlane16_swap(__float_as_uint(a), __float_as_uint(a), false, false);
          y[e] = rbf(rbf(__uint_as_float(r2[0]) + __uint_as_float(r2[1])) * gt);
        }
        if (l < 16) *(LAS f4*)(lds + L_KVX + t * 256 + (l & 7) * 32 + (l >> 3) * 16) = y;
      }
    }
    lsig(myFL, 3u);
    STMP(9);
    STFLUSH();
    if (spare) asm volatile(".Lend_s:"); else asm volatile(".Lend_h:");
    };
    if (sp) helper(BC<true>()); else helper(BC<false>());
    return;
  }

  const int wr = w >> 1, wc = w & 1;
  unsigned tag; u2 kg; u4 sg4;
  const int srow = mb * 128 + hp * 8 + 2 * w + (l >> 5);
  {
    const int row = srow;
    u4 xa[4], rw[4];
#pragma unroll
    for (int c = 0; c < 4; ++c) xa[c] = *(const u4*)(x + row * D + (l & 31) * 8 + 256 * c);
#pragma unroll
    for (int c = 0; c < 4; ++c) rw[c] = *(const u4*)(wrt + (l & 31) * 8 + 256 * c);
    float s0 = 0, s1 = 0;
#pragma unroll
    for (int c = 0; c < 4; ++c)
#pragma unroll
      for (int e = 0; e < 4; e += 2) { unsigned x0 = xa[c][e], x1 = xa[c][e + 1]; s0 = dot2(x0, 0x3F803F80u, s0); s1 = dot2(x1, 0x3F803F80u, s1); }
    const float m = half_sum(s0 + s1) * (1.f / D);
    f2 v[16], q2 = (f2)(0.f), q3 = (f2)(0.f);
#pragma unroll
    for (int c = 0; c < 16; ++c) v[c] = (f2){lo(xa[c >> 2][c & 3]), hi(xa[c >> 2][c & 3])} - m;
#pragma unroll
    for (int c = 0; c < 16; c += 2) { q2 += v[c] * v[c]; q3 += v[c + 1] * v[c + 1]; }
    q2 += q3;
    const float mr = rbf(m);
    const float rs = 1.f / rbf(sqrtf(rbf(rbf(half_sum(q2[0] + q2[1]) * (1.f / D)) + 1e-5f)));
    float d0 = 0, d1 = 0;
#pragma unroll
    for (int c = 0; c < 16; c += 2) {
      const f2 e0 = (f2){lo(xa[c >> 2][c & 3]), hi(xa[c >> 2][c & 3])} - mr, e1 = (f2){lo(xa[c >> 2][(c & 3) + 1]), hi(xa[c >> 2][(c & 3) + 1])} - mr;
      const unsigned p0 = pk2(e0[0], e0[1]), p1 = pk2(e1[0], e1[1]);
      const f2 n0 = (f2){lo(p0), hi(p0)} * rs, n1 = (f2){lo(p1), hi(p1)} * rs;
      unsigned c0 = rw[c >> 2][c & 3], c1 = rw[c >> 2][(c & 3) + 1];
      d0 = dot2(pk2(n0[0], n0[1]), c0, d0); d1 = dot2(pk2(n1[0], n1[1]), c1, d1);
    }
    const float d = half_sum(d0 + d1);
    const unsigned rb = pk2(d, d) & 0xFFFFu;
    const unsigned mono = (rb & 0x8000u) ? (~rb & 0xFFFFu) : (rb | 0x8000u);
    tag = __builtin_amdgcn_readfirstlane(ev0) + 1u;
    kg = (u2){(mono << 16) | (unsigned)row, tag};
    sg4 = (u4){__float_as_uint(mr), tag, __float_as_uint(rs), tag};
  }
  MKWS(tag);
  if ((l & 31) == 0) { ST64(kg, WS_RG + srow * 8); ST128(sg4, WS_ST + srow * 16); }
  unsigned aad[2], bad[2];
#pragma unroll
  for (int s = 0; s < 2; ++s) aad[s] = ldsb + (wr * 64 + r15) * 128 + (((4 * s + g4) ^ ((r15 >> 1) & 7)) * 16);
#pragma unroll
  for (int nh = 0; nh < 2; ++nh) bad[nh] = ldsb + 16384 + (8 * g4 + q) * 128 + (((wc * 4 + 2 * nh + (p >> 1)) ^ (((q >> 1) << 2) | ((g4 & 1) << 1))) * 16) + (p & 1) * 8;
  f4 acc[4][6];
#pragma unroll
  for (int m = 0; m < 4; ++m)
#pragma unroll
    for (int n = 0; n < 6; ++n) { acc[m][n] = (f4)(0.f); asm volatile("" : "+v"(acc[m][n])); }
#define MF(m, n, Fq) do { if (n < 2) acc[m][n] = MFMA16(CAT(Fq.b[2 * n], Fq.b[2 * n + 1]), Fq.a[m], acc[m][n]); \
     else acc[m][n] = MFMA16(Fq.a[m], CAT(Fq.b[2 * n], Fq.b[2 * n + 1]), acc[m][n]); SBAR(); } while (0)
#define RB(G, n, vb0, vb1, SS, hf) RDTR(G.b[2 * (n) + (hf)], (((n) & 1) ? vb1 : vb0), ((n) >> 1) * 8192 + (SS) * 4096 + (hf) * 512)
#define RA(G, m, va) RD128(G.a[m], va, (m) * 2048)
#define SUB(Fq, G, va, vb0, vb1, SS, PRE) do { \
    WAITL(13); SBAR(); \
    MF(0, 0, Fq); PRE; RA(G, 0, va); SBAR(); \
    WAITL(11); SBAR(); \
    MF(1, 0, Fq); RB(G, 0, vb0, vb1, SS, 0); SBAR(); MF(2, 0, Fq); RB(G, 0, vb0, vb1, SS, 1); SBAR(); MF(3, 0, Fq); RA(G, 1, va); SBAR(); \
    WAITL(12); SBAR(); \
    MF(0, 1, Fq); RA(G, 2, va); SBAR(); MF(1, 1, Fq); RA(G, 3, va); SBAR(); MF(2, 1, Fq); RB(G, 1, vb0, vb1, SS, 0); SBAR(); MF(3, 1, Fq); RB(G, 1, vb0, vb1, SS, 1); SBAR(); \
    WAITL(8); SBAR(); \
    MF(0, 2, Fq); RB(G, 2, vb0, vb1, SS, 0); SBAR(); MF(1, 2, Fq); RB(G, 2, vb0, vb1, SS, 1); SBAR(); MF(2, 2, Fq); RB(G, 3, vb0, vb1, SS, 0); SBAR(); MF(3, 2, Fq); RB(G, 3, vb0, vb1, SS, 1); SBAR(); \
    MF(0, 3, Fq); RB(G, 4, vb0, vb1, SS, 0); SBAR(); MF(1, 3, Fq); RB(G, 4, vb0, vb1, SS, 1); SBAR(); MF(2, 3, Fq); RB(G, 5, vb0, vb1, SS, 0); SBAR(); MF(3, 3, Fq); RB(G, 5, vb0, vb1, SS, 1); SBAR(); \
    MF(0, 4, Fq); MF(1, 4, Fq); MF(2, 4, Fq); MF(3, 4, Fq); MF(0, 5, Fq); MF(1, 5, Fq); MF(2, 5, Fq); MF(3, 5, Fq); } while (0)
#define KSTEP(MODE) do { \
    if (kt == sx) { const unsigned xo = xad + buf * STG; \
      _Pragma("unroll") for (int t = 0; t < 8; ++t) { const unsigned a_ = xo ^ (t * 16); \
        asm volatile("ds_read_b64 %0, %1" : "+v"(xv[t]) : "v"(a_) : "memory"); } \
      WAITL(0); SBAR(); } \
    const int nbuf = (buf + 1) & 3; \
    const unsigned bo = buf * STG, nbo = nbuf * STG; \
    { unsigned va = aad[1] + bo, vb0 = bad[0] + bo, vb1 = bad[1] + bo; SUB(f0, f1, va, vb0, vb1, 1, ); } \
    if (MODE < 2) { \
      unsigned va = aad[0] + nbo, vb0 = bad[0] + nbo, vb1 = bad[1] + nbo; \
      SUB(f1, f0, va, vb0, vb1, 0, if (MODE == 0) { WAITL(0); BAR(); } else asm volatile("ds_write_b32 %0, %1" ::"v"(myFL), "v"(1u) : "memory"); SBAR()); \
    } else { asm volatile("ds_write_b32 %1, %2\n ds_read_b32 %0, %3\n s_waitcnt lgkmcnt(2)" : "=&v"(fl0) : "v"(myFL), "v"(2u), "v"(adFL) : "memory"); SBAR(); \
      MF(0, 2, f1); MF(1, 2, f1); MF(2, 2, f1); MF(3, 2, f1); MF(0, 3, f1); MF(1, 3, f1); MF(2, 3, f1); MF(3, 3, f1); \
      MF(0, 4, f1); MF(1, 4, f1); MF(2, 4, f1); MF(3, 4, f1); MF(0, 5, f1); MF(1, 5, f1); MF(2, 5, f1); MF(3, 5, f1); \
      MF(0, 0, f1); MF(1, 0, f1); MF(2, 0, f1); MF(3, 0, f1); MF(0, 1, f1); MF(1, 1, f1); MF(2, 1, f1); MF(3, 1, f1); } \
    buf = nbuf; } while (0)
  u2 xv[8];
#pragma unroll
  for (int t = 0; t < 8; ++t) asm volatile("" : "=v"(xv[t]));
  const int sx = (nb - rot) & 15;
  const unsigned xad = ldsb + (w * 32 + (l & 31)) * 128 + ((r15 >> 1) & 7) * 16 + (l >> 5) * 8;
  STMP(1);
  WAITL(0);
  BAR();
  STMP(2);
  WAITL(0);
  FQ f0, f1;
  { unsigned va = aad[0], vb0 = bad[0], vb1 = bad[1];
    RA(f0, 0, va); RB(f0, 0, vb0, vb1, 0, 0); RB(f0, 0, vb0, vb1, 0, 1); RA(f0, 1, va); RA(f0, 2, va); RA(f0, 3, va);
    RB(f0, 1, vb0, vb1, 0, 0); RB(f0, 1, vb0, vb1, 0, 1); RB(f0, 2, vb0, vb1, 0, 0); RB(f0, 2, vb0, vb1, 0, 1); RB(f0, 3, vb0, vb1, 0, 0); RB(f0, 3, vb0, vb1, 0, 1);
    RB(f0, 4, vb0, vb1, 0, 0); RB(f0, 4, vb0, vb1, 0, 1); RB(f0, 5, vb0, vb1, 0, 0); RB(f0, 5, vb0, vb1, 0, 1);
    WAITL(0); SBAR(); }
  int buf = 0; unsigned fl0;
#pragma unroll 1
  for (int kt = 0; kt < 14; ++kt) KSTEP(0);
  { const int kt = 14; KSTEP(1); }
  { const int kt = 15; KSTEP(2); }
  asm volatile("s_waitcnt lgkmcnt(0)" : "+v"(fl0) : : "memory");
  if (__builtin_expect(((unsigned)__ballot(fl0 < 1u) & 0xC0u) != 0u, 0)) { RETRY2(); lwait(adFL, 0xC0u, 1); }
  STMP(3);
  __builtin_amdgcn_s_setprio(2);
  const LAS float* Lm = (const LAS float*)(lds + L_M) + wr * 64;
  const LAS float* Lr = (const LAS float*)(lds + L_RS) + wr * 64;
  const LAS float* Lc = (const LAS float*)(lds + L_CS) + wc * 96;
  f4 kv[4], zt[2];
  {
    float cs[4];
#pragma unroll
    for (int n = 0; n < 4; ++n) cs[n] = Lc[32 + 16 * n + r15];
    const u4 ones = (u4)(0x3F803F80u);
#pragma unroll
    for (int t = 0; t < 2; ++t) {
      u4 kf[2], vf[2];
#pragma unroll
      for (int mm = 0; mm < 2; ++mm) {
        const int m = 2 * t + mm;
        const f4 mv = *(const LAS f4*)(Lm + m * 16 + 4 * g4), rv = *(const LAS f4*)(Lr + m * 16 + 4 * g4);
#pragma unroll
        for (int a = 0; a < 2; ++a) {
          const f4 uk = acc[m][2 + a] - mv * cs[a];
          const f4 uv = (acc[m][4 + a] - mv * cs[2 + a]) * rv;
          kf[a][2 * mm] = phi2((f2){uk[0], uk[1]}, (f2){rv[0], rv[1]}); kf[a][2 * mm + 1] = phi2((f2){uk[2], uk[3]}, (f2){rv[2], rv[3]});
          vf[a][2 * mm] = pk2(uv[0], uv[1]); vf[a][2 * mm + 1] = pk2(uv[2], uv[3]);
        }
      }
#pragma unroll
      for (int tt = 0; tt < 4; ++tt) kv[tt] = MFMA16(S8(kf[tt & 1]), S8(vf[tt >> 1]), t ? kv[tt] : (f4)(0.f));
#pragma unroll
      for (int a = 0; a < 2; ++a) zt[a] = MFMA16(S8(kf[a]), S8(ones), t ? zt[a] : (f4)(0.f));
    }
  }
  const int hd = hp * 2 + wc;
  {
    LAS f4* ks = (LAS f4*)(lds + L_KVX + w * 3072) + l;
    const LAS f4* kr = (const LAS f4*)(lds + L_KVX + (w ^ 2) * 3072) + l;
    ks[0] = wr ? kv[0] : kv[2]; ks[64] = wr ? kv[1] : kv[3]; ks[128] = wr ? zt[0] : zt[1];
    lsig(myFL, 3);
    const f4 m0 = wr ? kv[2] : kv[0], m1 = wr ? kv[3] : kv[1], mz = wr ? zt[1] : zt[0];
    lwait(adFL, 1u << (w ^ 2), 3);
    const f4 s0 = m0 + kr[0], s1 = m1 + kr[64], sz = mz + kr[128];
    u4 o; o[0] = min(pk2(s0[0], s0[1]), POIS - 1u); o[1] = pk2(s0[2], s0[3]); o[2] = pk2(s1[0], s1[1]); o[3] = pk2(s1[2], s1[3]);
    ST128(o, WS_KVP + (hd * 16 + mb) * 2048 + wr * 1024 + l * 16);
    u4 zo = __builtin_bit_cast(u4, sz); zo[0] = min(zo[0], POIS - 1u);
    if (r15 == 0) ST128(zo, WS_ZP + (hd * 16 + mb) * 128 + g4 * 32 + wr * 16);
  }
  STMP(4);
  {
    const f4 c0 = *(const LAS f4*)(Lc + 4 * g4), c1 = *(const LAS f4*)(Lc + 16 + 4 * g4);
#pragma unroll
    for (int m = 0; m < 4; ++m) {
      const float mv = Lm[m * 16 + r15], rv = Lr[m * 16 + r15];
      const f4 u0 = acc[m][0] - c0 * mv, u1 = acc[m][1] - c1 * mv;
      u4 pf;
      pf[0] = phi2((f2){u0[0], u0[1]}, (f2)(rv)); pf[1] = phi2((f2){u0[2], u0[3]}, (f2)(rv));
      pf[2] = phi2((f2){u1[0], u1[1]}, (f2)(rv)); pf[3] = phi2((f2){u1[2], u1[3]}, (f2)(rv));
      *(LAS u4*)(lds + L_PQ + wr * 8192 + wc * 4096 + m * 1024 + l * 16) = pf;
    }
  }
  lsig(myFL, 4);
  __builtin_amdgcn_s_setprio(0);
  STMP(5);
  const int trow = mb * 128 + w * 32 + (l & 31);
  u2 oh;
  const unsigned flo = (2 * PAIRAT(r15) + wc) * 128;
  const char* fb = uptr(wsx + WS_OH + mb * 4096);
  asm volatile("global_load_dwordx2 %0, %1, %2 sc1" : "=v"(oh) : "v"(flo), "s"(fb) : "memory");
  int sel = -1;
  {
    const u4 s0 = *(const LAS u4*)(lds + L_SEL), s1 = *(const LAS u4*)(lds + L_SEL + 16);
#pragma unroll
    for (int i = 0; i < 8; ++i) { const int v = (int)(i < 4 ? s0[i & 3] : s1[i & 3]); sel = v == trow ? i : sel; }
  }
  const bool anysel = __ballot(sel >= 0) != 0ull;
  f16v oc[2][2];
  {
    const float m = *(const LAS float*)(lds + L_M + (w * 32 + (l & 31)) * 4), rs = *(const LAS float*)(lds + L_RS + (w * 32 + (l & 31)) * 4);
#pragma unroll
    for (int t = 0; t < 8; ++t) {
      const f4 xf = (f4){lo(xv[t][0]), hi(xv[t][0]), lo(xv[t][1]), hi(xv[t][1])};
      const f4 y = (xf - m) * rs + xf;
#pragma unroll
      for (int i = 0; i < 4; ++i) oc[0][t >> 2][(t & 3) * 4 + i] = y[i];
    }
    asm volatile("" : "+v"(oc[0][0]), "+v"(oc[0][1]));
  }
  unsigned wa[2];
#pragma unroll
  for (int t = 0; t < 2; ++t) wa[t] = ldsb + L_WO + wc * 4096 + (4 * (g4 >> 1) + q) * 128 + (((4 * t + 2 * (g4 & 1) + (p >> 1)) ^ ((q >> 1) << 2)) * 16) + (p & 1) * 8;
  u4 a[RD][4];
#pragma unroll
  for (int k = 0; k < RD; ++k) asm volatile("" : "=v"(a[k][0]), "=v"(a[k][1]), "=v"(a[k][2]), "=v"(a[k][3]));
  const char* ob = uptr(wsx + WS_OF + mb * 262144 + wc * 8192 + wr * 4096);
  const unsigned ov0 = l * 16 + wc * 2048, ov1 = ov0 ^ 2048;
  STMP(6);
  lwait<1>(adFL, 0x30u, 1);
  STMP(7);
  WAITV(0);
  asm volatile("" : "+v"(oh));
  unsigned seen = (unsigned)__ballot(oh[0] == tag), go = 0;
  auto need = [&](unsigned mask, bool first) {
    if (__builtin_expect((go & mask) != mask, 0)) {
      _Pragma("unroll 1") for (unsigned it = 0; it < SPIN; ++it) {
        bool poll = (seen & mask) != mask;
        if (first && seen != ~0u && !poll) poll = ((unsigned)__ballot(oh[1] - NOW() - 80u < 1000u) & mask) != 0u;
        if (poll) {
          if (first) SLEEP(4);
          asm volatile("global_load_dwordx2 %0, %1, %2 sc1\n s_waitcnt vmcnt(0)" : "+v"(oh) : "v"(flo), "s"(fb) : "memory");
          seen = (unsigned)__ballot(oh[0] == tag);
        }
        go = seen & (unsigned)__ballot(oh[1] - NOW() - 1u >= 1000u);
        if ((go & mask) == mask) break;
        SLEEP(1);
      }
    }
  };
#define OX 0
#define OL1(pos, i) do { if (OX == 2 && (pos) >= RD) break; \
    asm volatile("global_load_dwordx4 %0, %1, %2 offset:%3 sc1" : "+v"(a[(pos) % RD][i]) : "v"((i) & 1 ? ov1 : ov0), "s"(ob + (OX == 1 ? ((pos) & 1) : PAIRAT(pos)) * 16384), "i"(((i) >> 1) * 1024) : "memory"); } while (0)
#define OLD(pos) do { OL1(pos, 0); OL1(pos, 1); OL1(pos, 2); OL1(pos, 3); } while (0)
  auto omax = [&](u4(&v)[4]) {
    asm volatile("" : "+v"(v[0]), "+v"(v[1]), "+v"(v[2]), "+v"(v[3]));
    return max(umax3(v[0][0], v[1][0], v[2][0]), v[3][0]);
  };
#define OFIX(pos, m) do { \
    if (__builtin_expect(__ballot((m) == POIS) != 0ull, 0)) { RETRY(); _Pragma("unroll 1") for (unsigned it = 0; it < SPIN; ++it) { SLEEP(4); OLD(pos); WAITV(0); if (__ballot(omax(a[(pos) % RD]) == POIS) == 0ull) break; } } } while (0)
#define OS1(i)
#define OS2(i)
  need(1u, true);
  if (OWN) lwait<1>(adFL, 0x30u, 2);
  __builtin_amdgcn_s_setprio(2);
  STMP(15);
  OS1(0); OLD(0); STMP(14);
#pragma unroll
  for (int i = 1; i < RD; ++i) { need(1u << i, false); OS1(i); OLD(i); }
  STMP(10);
  s4 b[8];
#define ORD(pos, f) do { if ((f) < 2 && (pos) == 8) { wa[(f) & 1] += 65536; asm volatile("" : "+v"(wa[(f) & 1])); } \
    RDTR(b[2 * (f)], wa[(f) & 1], ((pos) & 7) * 8192 + ((f) >> 1) * 1024); RDTR(b[2 * (f) + 1], wa[(f) & 1], ((pos) & 7) * 8192 + ((f) >> 1) * 1024 + 2048); } while (0)
#define OMF(pos, f, r) do { if ((f) < 2 && (pos) == 0 && (r) == 1) asm volatile("v_mfma_f32_32x32x16_bf16 %0, %1, %2, 0" : "=&v"(oc[r][(f) & 1]) \
      : "v"(CAT(b[2 * (f)], b[2 * (f) + 1])), "v"(a[(pos) % RD][((f) & 2) + (r)])); \
    else asm volatile("v_mfma_f32_32x32x16_bf16 %0, %1, %2, %0" : "+v"(oc[r][(f) & 1]) \
      : "v"(CAT(b[2 * (f)], b[2 * (f) + 1])), "v"(a[(pos) % RD][((f) & 2) + (r)])); \
    SBAR(); if ((r) == 1 && (pos) < 15) ORD(((pos) + 1) & 15, f); SBAR(); } while (0)
#define ONX(pos) ((pos) > 0 && (pos) + RD - 1 < 16)
#define ONL(pos, i) do { if (ONX(pos)) OL1(((pos) + RD - 1) & 15, i); SBAR(); } while (0)
#define OWL(pos, f) do { WAITL((pos) < 15 ? 6 : 6 - 2 * (f)); SBAR(); } while (0)
#define OHEAD(pos) do { unsigned m_ = 0; \
    OWL(pos, 0); OMF(pos, 0, 0); if (ONX(pos)) { need(1u << (((pos) + RD - 1) & 15), false); OS1(((pos) + RD - 1) & 15); } SBAR(); \
    OMF(pos, 0, 1); ONL(pos, 0); \
    OWL(pos, 1); OMF(pos, 1, 0); ONL(pos, 1); \
    OMF(pos, 1, 1); ONL(pos, 2); \
    OWL(pos, 2); OMF(pos, 2, 0); ONL(pos, 3); \
    if ((pos) == 4) { asm volatile("ds_write_b32 %0, %1" ::"v"(myFL), "v"(5u) : "memory"); SBAR(); } \
    if ((pos) == 13) { asm volatile("ds_read_b32 %0, %1" : "=v"(fl3) : "v"(adFL) : "memory"); SBAR(); } \
    OMF(pos, 2, 1); \
    OWL(pos, 3); OMF(pos, 3, 0); \
    if ((pos) < 15) { WAITV(4 * ((pos) + RD - 1 < 16 ? RD - 2 : 14 - (pos))); m_ = omax(a[((pos) + 1) % RD]); asm volatile("" : "+v"(m_)); SBAR(); } \
    OMF(pos, 3, 1); \
    if ((pos) < 15) { OFIX(((pos) + 1) & 15, m_); OS2(((pos) + 1) & 15); } SBAR(); } while (0)
  unsigned fl3;
  ORD(0, 0); ORD(0, 1); ORD(0, 2); ORD(0, 3);
  { WAITV(4 * (RD - 1)); const unsigned m_ = omax(a[0]); OFIX(0, m_); OS2(0); SBAR(); }
  OHEAD(0); OHEAD(1); STMP(11); OHEAD(2); OHEAD(3); OHEAD(4); OHEAD(5); OHEAD(6); OHEAD(7); STMP(12);
  OHEAD(8); OHEAD(9); STMP(13); OHEAD(10); OHEAD(11); OHEAD(12); OHEAD(13); OHEAD(14);
  f4 xi[8];
  {
    asm volatile("" : "+v"(fl3));
    if (__builtin_expect(((unsigned)__ballot(fl3 < 5u) >> (w ^ 2)) & 1u, 0)) lwait(adFL, 1u << (w ^ 2), 5);
    const unsigned xw = ldsb + L_WO + (4 * wr + wc) * 4096 + l * 16, xr = xw ^ 4096;
#define XW(t) asm volatile("ds_write_b128 %0, %1 offset:%2" ::"v"(xw), "v"(__builtin_shufflevector(oc[1][(t) >> 2], oc[1][(t) >> 2], ((t) & 3) * 4, ((t) & 3) * 4 + 1, ((t) & 3) * 4 + 2, ((t) & 3) * 4 + 3)), \
      "i"(((t) >> 2) * 8192 + ((t) & 3) * 1024) : "memory")
    SBAR();
    OWL(15, 0); OMF(15, 0, 1); OWL(15, 1); OMF(15, 1, 1); OWL(15, 2); OMF(15, 2, 1); OWL(15, 3); OMF(15, 3, 1);
    OMF(15, 0, 0); OMF(15, 1, 0); XW(0); XW(1); XW(2); XW(3); SBAR();
    OMF(15, 2, 0); XW(4); XW(5); XW(6); XW(7); SBAR();
    OMF(15, 3, 0);
    asm volatile("ds_write_b32 %0, %1" ::"v"(myFL), "v"(6u) : "memory");
    _Pragma("unroll 1") for (unsigned it = 0; it < SPIN; ++it) {
      unsigned v;
      asm volatile("ds_read_b32 %0, %1" : "=v"(v) : "v"(adFL) : "memory");
#pragma unroll
      for (int t = 0; t < 8; ++t) RD128(xi[t], xr, (t >> 2) * 8192 + (t & 3) * 1024);
      WAITL(0);
      asm volatile("" : "+v"(v), "+v"(xi[0]), "+v"(xi[1]), "+v"(xi[2]), "+v"(xi[3]), "+v"(xi[4]), "+v"(xi[5]), "+v"(xi[6]), "+v"(xi[7]));
      if ((((unsigned)__ballot(v < 6u) >> (w ^ 1)) & 1u) == 0u) break;
    }
    asm volatile("" : "+v"(oc[0][0]), "+v"(oc[0][1]));
#pragma unroll
    for (int t = 0; t < 8; ++t)
#pragma unroll
      for (int i = 0; i < 4; ++i) xi[t][i] += oc[0][t >> 2][(t & 3) * 4 + i];
  }
  STMP(8);
  if (anysel) {
    lwait(adFL, 0xC0u, 3);
    if (sel >= 0) {
#pragma unroll
      for (int t = 0; t < 8; ++t) xi[t] += *(const LAS f4*)(lds + L_KVX + sel * 256 + (8 * t + 4 * (l >> 5)) * 4);
    }
  }
  const __amdgpu_buffer_rsrc_t outr = __builtin_amdgcn_make_buffer_rsrc(out, 0, 2048 * D * 2, 0x00020000);
  if (FULL) {
    LAS char* tb = (LAS char*)lds + L_PQ + w * 4096;
#pragma unroll
    for (int t = 0; t < 8; ++t) *(LAS u2*)(tb + (l & 31) * 128 + ((t ^ (l & 7)) * 16) + (l >> 5) * 8) = (u2){pk2(xi[t][0], xi[t][1]), pk2(xi[t][2], xi[t][3])};
#pragma unroll
    for (int i = 0; i < 4; ++i) {
      const u4 v = *(const LAS u4*)(tb + i * 1024 + (l >> 3) * 128 + (((l ^ (l >> 3)) & 7) * 16));
      __builtin_amdgcn_raw_buffer_store_b128(v, outr, (mb * 128 + w * 32 + i * 8 + (l >> 3)) * 2048 + nb * 128 + (l & 7) * 16, 0, 16);
    }
  } else {
#pragma unroll
  for (int hh = 0; hh < 4; ++hh) {
    const f4 ta = xi[2 * hh], tb = xi[2 * hh + 1];
    auto r0 = __builtin_amdgcn_permlane32_swap(pk2(ta[0], ta[1]), pk2(tb[0], tb[1]), false, false);
    auto r1 = __builtin_amdgcn_permlane32_swap(pk2(ta[2], ta[3]), pk2(tb[2], tb[3]), false, false);
    __builtin_amdgcn_raw_buffer_store_b128(((u4){r0[0], r1[0], r0[1], r1[1]}), outr, (trow * D + nb * 64 + 16 * hh + 8 * (l >> 5)) * 2, 0, 16);
  }
  }
  if (g == 0 && w == 0 && l == 0) __builtin_amdgcn_raw_buffer_store_b32(tag, wse, WS_E, 0, 16);
  STMP(9);
  STFLUSH();
  asm volatile(".Lend_c:");
  return;
  }
  {
    const int lw = w - 4;
    unsigned ev;
    asm volatile("global_load_dword %0, %1, %2 sc1" : "=v"(ev) : "v"(0u), "s"(ws) : "memory");
    unsigned vf[20], vfw[4];
#pragma unroll
    for (int i = 0; i < 8; ++i) { const int row = 8 * (lw * 8 + i) + (l >> 3); vf[i] = row * 2048 + (((l & 7) ^ ((row >> 1) & 7)) * 16) + (3 - (i & 3)) * 1024; }
#pragma unroll
    for (int i = 0; i < 12; ++i) {
      const int pc = lw * 12 + i, row = (pc & 7) * 8 + (l >> 3);
      vf[8 + i] = row * 6144 + (pc >> 3) * 2048 + (((l & 7) ^ ((((row >> 1) & 1) << 2) | (((row >> 3) & 1) << 1))) * 16) + (3 - (i & 3)) * 1024;
    }
#pragma unroll
    for (int i = 0; i < 4; ++i) vfw[i] = (8 * i + (l >> 3)) * 2048 + (((l & 7) ^ (((l >> 4) & 1) << 2)) * 16) + (3 - i) * 1024;
    const char* Ab = uptr((const char*)x + (size_t)mb * 128 * 2048) - 3072;
    const char* Bb = uptr((const char*)wqkv + hp * 128) - 3072;
    const unsigned mba = ldsb + lw * 8192, mbb = ldsb + 16384 + lw * 12288;
#define GL4(v, sb, mm, MO) do { asm volatile("s_add_i32 m0, %2, %3\n s_nop 0\n global_load_lds_dwordx4 %0, %1" ::"v"((v)[0]), "s"(sb), "s"(mm), "i"(MO) : "memory", "scc"); \
      GLI((v)[1], sb, 1024); GLI((v)[2], sb, 2048); GLI((v)[3], sb, 3072); } while (0)
    auto issue = [&](int s, int u0, int u1) {
      const int kt = (s + rot) & 15;
      const char* sa = Ab + kt * 128; const char* sb = Bb + kt * (64 * 6144);
      const unsigned so = (s & 3) * STG, ma = mba + so, mq = mbb + so;
#pragma unroll
      for (int u = u0; u < u1; ++u) { if (u < 2) GL4(vf + 4 * u, sa, ma, u * 4096); else GL4(vf + 4 * u, sb, mq, (u - 2) * 4096); }
    };
    int tk = 0;
#define LBAR() do { BST(tk + 1); BAR(); BST(tk + 21); ++tk; } while (0)
    issue(0, 0, 5); issue(1, 0, 5);
    BST(60);
    WAITV(20); BST(0); BAR(); BST(20);
    asm volatile("" : "+v"(ev));
    const unsigned tag = __builtin_amdgcn_readfirstlane(ev) + 1u;
    MKWS(tag);
    issue(2, 0, 5);
    int s0 = 3;
#pragma unroll 1
    for (; s0 < (sp ? 16 : 6 + ph); ++s0) { issue(s0, 0, 5); BST(tk + 40); WAITV(40); LBAR(); }
#pragma unroll 1
    for (; s0 < 16; s0 += 5) {
      WAITV(20); LBAR();
      WAITV(0); LBAR();
      issue(s0, 0, 5); issue(s0 + 1, 0, U1); BST(tk + 40); WAITV(4 * U1); LBAR();
      issue(s0 + 1, U1, 5); issue(s0 + 2, 0, 5);
      if (s0 + 3 >= 16) break;
      issue(s0 + 3, 0, U2); BST(tk + 40); WAITV(20 + 4 * U2); LBAR();
      issue(s0 + 3, U2, 5);
      if (s0 + 4 >= 16) break;
      issue(s0 + 4, 0, 5); BST(tk + 40); WAITV(40); LBAR();
    }
    int wq = lw;
    auto wo_upto = [&](int lim) {
      _Pragma("unroll 1") for (; wq < lim; wq += 2) { const char* wp = wob - 3072 + HEADAT(wq) * 65536; const unsigned wm = ldsb + wq * 4096; GL4(vfw, wp, wm, L_WO); }
    };
    asm volatile("ds_write_b32 %0, %1" ::"v"(adFL), "v"(0u) : "memory");
    WAITV(0); WAITL(0); LBAR();
    if (lw == 0) ST64(((u2){tag, NOW() + KD}), l == 0 ? WS_HK + (hp * 16 + mb) * 128 : WS_SZ);
    wo_upto(12);
    BST(15); lwait<1>(adFL, 0x0Fu, 1); BST(35);
    wo_upto(22);
    BST(16); lwait<1>(adFL, 0x0Fu, 2); BST(36);
    wo_upto(32);
    STMP(1);
    const int hd = hp * 2 + lw;
    u4 sel[2];
#pragma unroll
    for (int e = 0; e < 2; ++e) {
      const unsigned v = (r15 >> 2) == g4 ? (((r15 & 3) == 2 * e ? 0x3F80u : 0u) | ((r15 & 3) == 2 * e + 1 ? 0x3F800000u : 0u)) : 0u;
      sel[0][e] = v; sel[0][2 + e] = 0u; sel[1][e] = 0u; sel[1][2 + e] = v;
    }
    u4 pv[16][2], zv0, zv1, pq[8];
    const char* kvb = uptr(wsx + WS_KVP + (size_t)hd * 32768);
    const char* zb = uptr(wsx + WS_ZP + (size_t)hd * 2048);
    const char* hb = uptr(wsx + WS_HK + hp * 2048);
    const unsigned lv16 = l * 16, zoff = r15 * 128 + g4 * 32;
    const unsigned pa = ldsb + L_PQ + lw * 4096 + l * 16, omask = lw ? 0x0Au : 0x05u;
    u2 hk;
    _Pragma("unroll 1") for (unsigned it = 0; it < SPIN; ++it) {
      asm volatile("global_load_dwordx2 %0, %1, %2 sc1\n s_waitcnt vmcnt(0)" : "=v"(hk) : "v"(r15 * 128), "s"(hb) : "memory");
      if (it == 0) lsig(myFL, 1);
      if (__ballot(hk[0] != tag) == 0ull) break;
      SLEEP(2);
    }
    __builtin_amdgcn_s_setprio(3);
    STMP(2);
    const unsigned t0 = NOW();
    unsigned rel = hk[1] - t0; rel = rel < 1000u ? rel : 0u;
    rel = max(rel, (unsigned)__builtin_amdgcn_update_dpp(0, (int)rel, 0xB1, 0xf, 0xf, false));
    rel = max(rel, (unsigned)__builtin_amdgcn_update_dpp(0, (int)rel, 0x4E, 0xf, 0xf, false));
    int gx;
    { const unsigned r0 = __builtin_amdgcn_readlane(rel, 0), r1 = __builtin_amdgcn_readlane(rel, 4), r2 = __builtin_amdgcn_readlane(rel, 8), r3 = __builtin_amdgcn_readlane(rel, 12);
      gx = max(r0, r1) > max(r2, r3) ? (r0 > r1 ? 3 : 2) : (r2 > r3 ? 1 : 0);
      if (OHP) ST64(((u2){tag, t0 + max(max(r0, r1), max(r2, r3)) + OD}), l == 0 ? WS_OH + (mb * 32 + hd) * 128 : WS_SZ); }
#define GLD(gq) do { _Pragma("unroll") for (int i = 0; i < 4; ++i) { const char* sb = kvb + (((gq) ^ gx) * 4 + (i & 2)) * 2048; \
      asm volatile("global_load_dwordx4 %0, %1, %2 offset:%3 sc1" : "=v"(pv[(gq) * 4 + i][0]) : "v"(lv16), "s"(sb), "i"((i & 1) * 2048) : "memory"); \
      asm volatile("global_load_dwordx4 %0, %1, %2 offset:%3 sc1" : "=v"(pv[(gq) * 4 + i][1]) : "v"(lv16), "s"(sb), "i"((i & 1) * 2048 + 1024) : "memory"); } } while (0)
#define ZLD() do { asm volatile("global_load_dwordx4 %0, %1, %2 sc1" : "=v"(zv0) : "v"(zoff), "s"(zb) : "memory"); \
      asm volatile("global_load_dwordx4 %0, %1, %2 offset:16 sc1" : "=v"(zv1) : "v"(zoff), "s"(zb) : "memory"); } while (0)
#pragma unroll
    for (int gq = 0; gq < 4; ++gq) {
      const unsigned rq = __builtin_amdgcn_readlane(rel, 4 * (gq ^ gx));
      _Pragma("unroll 1") for (unsigned it = 0; it < SPIN; ++it) if (NOW() - t0 >= rq) break;
      if (gq == 3) ZLD();
      GLD(gq);
    }
    if (!OHP) ST64(((u2){tag, NOW() + OD}), l == 0 ? WS_OH + (mb * 32 + hd) * 128 : WS_SZ);
    STMP(5);
    lwait(adFL, omask, 4);
#pragma unroll
    for (int i = 0; i < 8; ++i) RD128(pq[i], pa, (i >> 2) * 8192 + (i & 3) * 1024);
#pragma unroll
    for (int gq = 0; gq < 4; ++gq) {
      SBAR(); WAITV((gq == 0 ? 27 : gq == 1 ? 19 : gq == 2 ? 11 : 1) - OHP);
      _Pragma("unroll 1") for (unsigned it = 0; it < SPIN; ++it) {
#pragma unroll
        for (int i = 0; i < 4; ++i) asm volatile("" : "+v"(pv[gq * 4 + i][0]), "+v"(pv[gq * 4 + i][1]));
        unsigned m = umax3(pv[gq * 4][0][0], pv[gq * 4][1][0], pv[gq * 4 + 1][0][0]);
        m = umax3(m, pv[gq * 4 + 1][1][0], pv[gq * 4 + 2][0][0]);
        m = umax3(m, pv[gq * 4 + 2][1][0], pv[gq * 4 + 3][0][0]);
        m = max(m, pv[gq * 4 + 3][1][0]);
        if (gq == 3) { asm volatile("" : "+v"(zv0), "+v"(zv1)); m = umax3(m, zv0[0], zv1[0]); }
        if (__ballot(m == POIS) == 0ull) break;
        RETRY();
        SLEEP(2);
        if (gq == 3) ZLD();
        GLD(gq);
        WAITV(0);
      }
      f4 ga[4];
#pragma unroll
      for (int i = 0; i < 4; ++i)
#pragma unroll
        for (int t = 0; t < 4; ++t) ga[t] = MFMA16(S8(sel[t & 1]), S8(pv[gq * 4 + i][t >> 1]), i ? ga[t] : (f4)(0.f));
#pragma unroll
      for (int t = 0; t < 4; ++t) pv[gq * 4 + (t >> 1)][t & 1] = __builtin_bit_cast(u4, ga[t]);
    }
    STMP(3);
    WAITL(0);
#pragma unroll
    for (int i = 0; i < 8; i += 2) asm volatile("" : "+v"(pq[i]), "+v"(pq[i + 1]));
    u4 kA[2], zA;
    {
      float z[8];
#pragma unroll
      for (int e = 0; e < 4; ++e) { z[e] = __uint_as_float(zv0[e]); z[4 + e] = __uint_as_float(zv1[e]); }
#define ZR(n) "v_add_f32_dpp %0, %0, %0 row_ror:" #n " row_mask:0xf bank_mask:0xf\n v_add_f32_dpp %1, %1, %1 row_ror:" #n " row_mask:0xf bank_mask:0xf\n" \
              "v_add_f32_dpp %2, %2, %2 row_ror:" #n " row_mask:0xf bank_mask:0xf\n v_add_f32_dpp %3, %3, %3 row_ror:" #n " row_mask:0xf bank_mask:0xf\n" \
              "v_add_f32_dpp %4, %4, %4 row_ror:" #n " row_mask:0xf bank_mask:0xf\n v_add_f32_dpp %5, %5, %5 row_ror:" #n " row_mask:0xf bank_mask:0xf\n" \
              "v_add_f32_dpp %6, %6, %6 row_ror:" #n " row_mask:0xf bank_mask:0xf\n v_add_f32_dpp %7, %7, %7 row_ror:" #n " row_mask:0xf bank_mask:0xf\n"
      asm volatile("s_nop 1\n" ZR(8) ZR(4) ZR(2) ZR(1) : "+v"(z[0]), "+v"(z[1]), "+v"(z[2]), "+v"(z[3]), "+v"(z[4]), "+v"(z[5]), "+v"(z[6]), "+v"(z[7]));
#pragma unroll
      for (int e = 0; e < 4; ++e) zA[e] = pk2(z[2 * e], z[2 * e + 1]);
    }
    float inv[8];
#pragma unroll
    for (int i = 0; i < 8; ++i) { f4 dn = MFMA16(S8(zA), S8(pq[i]), (f4)(0.f)); inv[i] = dn[0]; }
#pragma unroll
    for (int t = 0; t < 4; ++t) {
#define PF(gq) __builtin_bit_cast(f4, pv[(gq) * 4 + (t >> 1)][t & 1])
      f4 s = (PF(0) + PF(1)) + (PF(2) + PF(3));
      kA[t >> 1][(t & 1) * 2] = pk2(s[0], s[1]); kA[t >> 1][(t & 1) * 2 + 1] = pk2(s[2], s[3]);
    }
    f4 o0[8], o1[8];
#pragma unroll
    for (int i = 0; i < 8; ++i) {
      o0[i] = MFMA16(S8(kA[0]), S8(pq[i]), (f4)(0.f)); o1[i] = MFMA16(S8(kA[1]), S8(pq[i]), (f4)(0.f));
      inv[i] = __builtin_amdgcn_rcpf(inv[i] + 1e-6f);
    }
#pragma unroll
    for (int i = 0; i < 8; ++i) {
      o0[i] *= inv[i]; o1[i] *= inv[i];
      u4 o; o[0] = min(pk2(o0[i][0], o0[i][1]), POIS - 1u); o[1] = pk2(o0[i][2], o0[i][3]); o[2] = pk2(o1[i][0], o1[i][1]); o[3] = pk2(o1[i][2], o1[i][3]);
      ST128(o, WS_OF + (mb * 32 + hd) * 8192 + (i >> 1) * 2048 + (i & 1) * 256 + g4 * 512 + r15 * 16);
    }
    lsig(myFL, 2);
    STMP(4);
    STFLUSH();
    asm volatile(".Lend_l:");
  }

}

extern "C" void kernel_launch(void* const* d_in, const int* in_sizes, int n_in, void* d_out, int out_size,
                              void* d_ws, size_t ws_size, hipStream_t stream) {
  (void)hipFuncSetAttribute((const void*)fused, hipFuncAttributeMaxDynamicSharedMemorySize, LDS_SZ);
  fused<<<256, 512, LDS_SZ, stream>>>((const u16*)d_in[0], (const u16*)d_in[1], (const u16*)d_in[2], (const u16*)d_in[4],
                                      (const u16*)d_in[5], (const u16*)d_in[6], (const u16*)d_in[7], (char*)d_ws, (u16*)d_out);
}
```

```cpp
#define EVW (w&3)
#define EVG 1
#define EVX 20*(st_[4]-st_[3])
#include <hip/hip_runtime.h>
#include <stdint.h>

typedef unsigned short u16;
typedef short s4 __attribute__((ext_vector_type(4)));
typedef short s8 __attribute__((ext_vector_type(8)));
typedef float f4 __attribute__((ext_vector_type(4)));
typedef float f16v __attribute__((ext_vector_type(16)));
typedef float f2 __attribute__((ext_vector_type(2)));
typedef unsigned u4 __attribute__((ext_vector_type(4)));
typedef unsigned u2 __attribute__((ext_vector_type(2)));
typedef __bf16 bf2 __attribute__((ext_vector_type(2)));
#define LAS __attribute__((address_space(3)))
#define DEV __device__ __forceinline__

constexpr int D = 1024, F = 3584;
#define NOMOE 0
#define KD 310
#define OD 190
#define OHP 1
#define FS 128
#define GSL 8
#define YSL 16
#define TP1 0
#define TP2 3
#define TLBW 0
#define FULL 1
#define HPRIO 1
#define OWN 1
#define NOPOIS 0
#define CAP 7
#define RD 4
#define DYN 1
#define JX 0
#define NOSKIP 0
#define GRP 8
#define PV 4
#define U1 4
#define U2 2
#define ROT 0

constexpr unsigned WS_E = 0;
constexpr unsigned WS_YF = 524288;
constexpr unsigned WS_CAND = 12288;
constexpr unsigned WS_GF = 589824;
constexpr unsigned WS_RG = 49152;
constexpr unsigned WS_ST = 65536;
constexpr unsigned WS_HK = 98304;
constexpr unsigned WS_ZP = 131072;
constexpr unsigned WS_CS = 196608;
constexpr unsigned WS_OH = 458752;
constexpr unsigned WS_KVP = 1u << 20;
constexpr unsigned WS_GP = 2u << 20;
constexpr unsigned WS_YP = 3u << 20;
constexpr unsigned WS_OF = 5u << 20;
constexpr unsigned WS_SZ = 9u << 20;
#define NR 4
#define RGO(tag) ((64u << 20) + ((tag) & (NR - 1u)) * (10u << 20))
#define MKWS(tag) char* const wsx = ws + RGO(tag); const __amdgpu_buffer_rsrc_t wsr = __builtin_amdgcn_make_buffer_rsrc(wsx, 0, WS_SZ, 0x00020000)

constexpr int STG = 40960;
constexpr int L_KVX = 0;
constexpr int L_PQ = 12288;
constexpr int L_M = 28672, L_RS = L_M + 512, L_CS = L_RS + 512, L_SEL = L_CS + 768, L_FL = L_SEL + 64;
constexpr int L_WO = 32768;
constexpr int LDS_SZ = 163840;
constexpr unsigned POIS = 0xFFFFFFFFu;

DEV unsigned pk2(float a, float b) { bf2 v; v[0] = (__bf16)a; v[1] = (__bf16)b; return __builtin_bit_cast(unsigned, v); }
DEV float lo(unsigned u) { return __uint_as_float(u << 16); }
DEV float hi(unsigned u) { return __uint_as_float(u & 0xFFFF0000u); }
DEV float rbf(float a) { return lo(pk2(a, a)); }
DEV float dot2(unsigned a, unsigned b, float c) { return __builtin_amdgcn_fdot2_f32_bf16(__builtin_bit_cast(bf2, a), __builtin_bit_cast(bf2, b), c, false); }
#define RD128(dst, addr, OFF) asm volatile("ds_read_b128 %0, %1 offset:%2" : "=v"(dst) : "v"(addr), "i"(OFF) : "memory")
#define RDTR(dst, addr, OFF) asm volatile("ds_read_b64_tr_b16 %0, %1 offset:%2" : "=v"(dst) : "v"(addr), "i"(OFF) : "memory")
#define SBAR() __builtin_amdgcn_sched_barrier(0)
#define CAT(x, y) __builtin_shufflevector(x, y, 0, 1, 2, 3, 4, 5, 6, 7)
#define WAITV(n) asm volatile("s_waitcnt vmcnt(%0)" ::"i"(n) : "memory")
#define WAITL(n) asm volatile("s_waitcnt lgkmcnt(%0)" ::"i"(n) : "memory")
#define MEMB() asm volatile("" ::: "memory")
#define SLEEP(n) asm volatile("s_sleep %0" ::"i"(n) : "memory")
#define BAR() asm volatile("s_barrier" ::: "memory")
#define GL0(vo, sb, m0v) asm volatile("s_mov_b32 m0, %2\n s_nop 0\n global_load_lds_dwordx4 %0, %1" ::"v"(vo), "s"(sb), "s"(m0v) : "memory")
#define GLI(vo, sb, OFF) asm volatile("global_load_lds_dwordx4 %0, %1 offset:%2" ::"v"(vo), "s"(sb), "i"(OFF) : "memory")
#define MFMA16(a, b, c) __builtin_amdgcn_mfma_f32_16x16x32_bf16(a, b, c, 0, 0, 0)
#define S8(x) __builtin_bit_cast(s8, x)
#define LD128(off) __builtin_amdgcn_raw_buffer_load_b128(wsr, (off), 0, 16)
#define LD64(off) __builtin_amdgcn_raw_buffer_load_b64(wsr, (off), 0, 16)
#define ST128(v, off) __builtin_amdgcn_raw_buffer_store_b128((v), wsr, (off), 0, 16)
#define ST64(v, off) __builtin_amdgcn_raw_buffer_store_b64((v), wsr, (off), 0, 16)
#define OFA(i) (WS_OF + ((mb * 32 + hd) * 8 + (i)) * 1024 + l * 16)
#define NOW() ((unsigned)__builtin_amdgcn_s_memrealtime())
constexpr unsigned SPIN = 1u << 21;
DEV const char* uptr(const char* p) {
  unsigned long long v = (unsigned long long)p;
  unsigned lo_ = __builtin_amdgcn_readfirstlane((unsigned)v), hi_ = __builtin_amdgcn_readfirstlane((unsigned)(v >> 32));
  return (const char*)(((unsigned long long)hi_ << 32) | lo_);
}
DEV void lsig(unsigned addr, unsigned val) { asm volatile("s_waitcnt lgkmcnt(0)\n ds_write_b32 %0, %1" ::"v"(addr), "v"(val) : "memory"); }
template <int SL = 0> DEV void lwait(unsigned addr, unsigned mask, unsigned target) {
  _Pragma("unroll 1") for (unsigned it = 0; it < SPIN; ++it) {
    unsigned v;
    asm volatile("ds_read_b32 %0, %1\n s_waitcnt lgkmcnt(0)" : "=v"(v) : "v"(addr) : "memory");
    if (((unsigned)__ballot(v < target) & mask) == 0u) break;
    if (SL) SLEEP(SL);
  }
}
DEV unsigned umax3(unsigned a, unsigned b, unsigned c) { return max(max(a, b), c); }
template <int C> DEV float dppf(float v) { return __int_as_float(__builtin_amdgcn_update_dpp(0, __float_as_int(v), C, 0xf, 0xf, false)); }
DEV unsigned wave_max(unsigned v) {
  v = max(v, (unsigned)__builtin_amdgcn_update_dpp(0, (int)v, 0x111, 0xf, 0xf, false));
  v = max(v, (unsigned)__builtin_amdgcn_update_dpp(0, (int)v, 0x112, 0xf, 0xf, false));
  v = max(v, (unsigned)__builtin_amdgcn_update_dpp(0, (int)v, 0x114, 0xf, 0xf, false));
  v = max(v, (unsigned)__builtin_amdgcn_update_dpp(0, (int)v, 0x118, 0xf, 0xf, false));
  v = max(v, (unsigned)__builtin_amdgcn_update_dpp(0, (int)v, 0x142, 0xa, 0xf, false));
  v = max(v, (unsigned)__builtin_amdgcn_update_dpp(0, (int)v, 0x143, 0xc, 0xf, false));
  return (unsigned)__builtin_amdgcn_readlane((int)v, 63);
}
DEV unsigned phi2(f2 u, f2 rs) {
  f2 t = u * rs;
  f2 a = t * 1.44269504f, p = t + 1.f, r;
  r[0] = __builtin_amdgcn_fmed3f(__builtin_amdgcn_exp2f(a[0]), p[0], 1.f);
  r[1] = __builtin_amdgcn_fmed3f(__builtin_amdgcn_exp2f(a[1]), p[1], 1.f);
  r += 1e-6f;
  return pk2(r[0], r[1]);
}

#define STDECL unsigned st_[16] = {0, 0, 0, 0, 0, 0, 0, 0, 0, 0, 0, 0, 0, 0, 0, 0}
#define STMP(i) st_[i] = (unsigned)__builtin_amdgcn_s_memrealtime()
#define BST(i)
#define RETRY()
#define RETRY2()
#define CL(p) ((unsigned)((size_t)(p) >> 7) & 7u)
#define STFLUSH() do { const int b_ = blockIdx.x; if (w == EVW && (EVG)) { const unsigned v_ = min((unsigned)(EVX), 300000u); _Pragma("unroll 1") for (unsigned it = 0; it < SPIN; ++it) { if (NOW() - st_[0] >= 4500u + v_) break; SLEEP(1); } } } while (0)

DEV float half_sum(float v) {
  asm volatile("s_nop 1\n v_add_f32_dpp %0, %0, %0 row_ror:8 row_mask:0xf bank_mask:0xf\n s_nop 1\n v_add_f32_dpp %0, %0, %0 row_ror:4 row_mask:0xf bank_mask:0xf\n"
               "s_nop 1\n v_add_f32_dpp %0, %0, %0 row_ror:2 row_mask:0xf bank_mask:0xf\n s_nop 1\n v_add_f32_dpp %0, %0, %0 row_ror:1 row_mask:0xf bank_mask:0xf\n s_nop 1" : "+v"(v));
  auto r = __builtin_amdgcn_permlane16_swap(__float_as_uint(v), __float_as_uint(v), false, false);
  return __uint_as_float(r[0]) + __uint_as_float(r[1]);
}

struct FQ { s8 a[4]; s4 b[12]; };
template <bool B> struct BC { static constexpr bool v = B; };

__global__ __launch_bounds__(512) void fused(const u16* __restrict__ x, const u16* __restrict__ wqkv, const u16* __restrict__ wo,
                                             const u16* __restrict__ wrt, const u16* __restrict__ wg, const u16* __restrict__ wu,
                                             const u16* __restrict__ wd, char* __restrict__ ws, u16* __restrict__ out) {
  extern __shared__ __attribute__((aligned(16))) char lds[];
  asm volatile(".Lcode_beg:");
  const int w = __builtin_amdgcn_readfirstlane(threadIdx.x >> 6), l = threadIdx.x & 63;
  STDECL;
  STMP(0);
  const int g = blockIdx.x, xcd = g & 7, j = g >> 3, ii = j >> 2;
  unsigned cw = 0;
  if (w == 7) {
    unsigned long long pc; unsigned off, len, l1, l2, l3;
    asm volatile("s_getpc_b64 %0\n.Lcode_here:" : "=s"(pc));
    asm volatile("s_mov_b32 %0, (.Lcode_here - .Lcode_beg)" : "=s"(off));
    asm volatile("s_mov_b32 %0, (.Lend_c - .Lcode_beg)" : "=s"(len));
    asm volatile("s_mov_b32 %0, (.Lend_l - .Lcode_beg)" : "=s"(l1));
    asm volatile("s_mov_b32 %0, (.Lend_h - .Lcode_beg)" : "=s"(l2));
    asm volatile("s_mov_b32 %0, (.Lend_s - .Lcode_beg)" : "=s"(l3));
    len = max(max(len, l3), max(l1, l2));
    const unsigned o = min((unsigned)(l * 32 + j) * 64u, len - 4u);
    asm volatile("global_load_dword %0, %1, %2" : "+v"(cw) : "v"(o), "s"(pc - off) : "memory");
  }
#define PTAB 0xA98BFEDC76542103ull
#define PHT 0x6624924999924924ull
#define CX 4
  const int mb = (xcd & 1) * 8 + ii, hp = (xcd >> 1) * 4 + (j & 3);
  const bool sp = (hp & 7) == 3;
  const int rot = 2 * xcd + 4 * (ii & 3);
  const int nb = hp;
#define PAIRAT(p) ((int)(PTAB >> (4 * (p))) & 15)
#define HEADAT(s) (2 * PAIRAT((s) >> 1) + ((s) & 1))
  const int ph = (int)(PHT >> (2 * j)) & 3;
  const unsigned ldsb = (unsigned)(size_t)(LAS char*)lds;
  const unsigned adFL = ldsb + L_FL + 4 * (l & 7), myFL = ldsb + L_FL + 4 * w;
  const __amdgpu_buffer_rsrc_t wse = __builtin_amdgcn_make_buffer_rsrc(ws, 0, 64, 0x00020000);
  const int r15 = l & 15, g4 = l >> 4, q = (l & 15) >> 2, p = l & 3;
  const char* wob = uptr((const char*)wo + nb * 128);

  if (w != 4 && w != 5) {
  const unsigned ev0 = __builtin_amdgcn_raw_buffer_load_b32(wse, WS_E, 0, 16);
  if (w >= 6) {
    const int mw = w - 6;
    const unsigned tag = __builtin_amdgcn_readfirstlane(ev0) + 1u, tag2 = tag * 0x9E3779B1u;
    MKWS(tag);
    auto helper = [&](auto SP) {
    constexpr bool spare = decltype(SP)::v, moe = !spare && !NOMOE;
    int nbar = 0;
#define TICK() do { BST(nbar); SBAR(); BAR(); SBAR(); ++nbar; } while (0)
#define DTICK(lim) do { if (nbar < (lim)) TICK(); else SLEEP(2); } while (0)
    const int hd = hp * 2 + mw;
    const u4 pz = (u4)(POIS);
    const int chunk = 4 * (hp - (hp > 3) - (hp > 11)) + (ii & 3), ksp = (xcd & 1) * 2 + (ii >> 2), chg = chunk ^ (mw * CX);
    const int np = l & 7, kset = (l >> 3) & 1;
    u4 gw[4][8], dw[2][8];
    const __amdgpu_buffer_rsrc_t wgr = __builtin_amdgcn_make_buffer_rsrc((void*)(mw ? wu : wg), 0, D * F * 2, 0x00020000);
    const __amdgpu_buffer_rsrc_t wdr = __builtin_amdgcn_make_buffer_rsrc((void*)wd, 0, D * F * 2, 0x00020000);
    const __amdgpu_buffer_rsrc_t wor = __builtin_amdgcn_make_buffer_rsrc((void*)wo, 0, D * D * 2, 0x00020000);
    const unsigned gvo = ((256 * ksp + 32 * kset + 8 * g4) * F + 64 * chg + 8 * np) * 2;
    const unsigned dvo = ((64 * chunk + 8 * g4) * D + 256 * ksp + 128 * mw + 8 * r15) * 2;
    auto burst = [&](int k) {
      if (moe) {
#pragma unroll
        for (int i = 0; i < 16; ++i) {
          if (k < 2) gw[(2 * k + (i >> 3)) & 3][i & 7] = __builtin_amdgcn_raw_buffer_load_b128(wgr, gvo, (128 * k + 64 * (i >> 3) + (i & 7)) * F * 2, 2);
          else dw[i >> 3][i & 7] = __builtin_amdgcn_raw_buffer_load_b128(wdr, dvo, (32 * (i >> 3) + (i & 7)) * D * 2, 2);
        }
      }
    };
    auto transp = [&](u4(&v)[8]) {
      if (moe) {
        u4 f[8];
#pragma unroll
        for (int c = 0; c < 8; ++c)
#pragma unroll
          for (int e = 0; e < 4; ++e) {
            const unsigned ev = v[2 * e][c >> 1], od = v[2 * e + 1][c >> 1];
            f[c][e] = (c & 1) ? __builtin_amdgcn_perm(od, ev, 0x07060302u) : __builtin_amdgcn_perm(od, ev, 0x05040100u);
          }
#pragma unroll
        for (int c = 0; c < 8; ++c) { v[c] = f[c]; asm volatile("" : "+v"(v[c])); }
      }
    };
    auto pois = [&](int i) {
      if (i < 2) ST128(pz, WS_KVP + (hd * 16 + mb) * 2048 + i * 1024 + l * 16);
      else if (i == 2) ST128(pz, WS_ZP + (hd * 16 + mb) * 128 + (l & 7) * 16);
      else if (!NOPOIS) ST128(pz, OFA(i - 3));
    };
    unsigned wt0, wt1, wt2 = 0, wt3 = 0;
    auto touch = [&]() {
      const unsigned to = (j * 32 + mw * 16 + (l >> 3)) * 2048 + (xcd >> 1) * 512 + (l & 7) * 64;
      wt0 = __builtin_amdgcn_raw_buffer_load_b32(wor, to, 0, 0); wt1 = __builtin_amdgcn_raw_buffer_load_b32(wor, to, 16384, 0);
      if (TLBW) {
        const __amdgpu_buffer_rsrc_t outr = __builtin_amdgcn_make_buffer_rsrc(out, 0, 2048 * D * 2, 0x00020000);
        wt2 = __builtin_amdgcn_raw_buffer_load_b32(outr, (mb * 128 + mw * 64 + (l & 3) * 16) * 2048 + nb * 128, 0, 0);
        wt3 = __builtin_amdgcn_raw_buffer_load_b32(wsr, (l & 1) ? WS_GP + g * 4096 + mw * 2048 : WS_YP + (g >> 2) * 32768 + (l & 2) * 8192, 0, 0);
      }
    };
    s4 sb[24];
    auto colrd = [&](int h) {
      const unsigned bo = ldsb + (1 + h) * STG + 16384 + (8 * g4 + q) * 128 + (p & 1) * 8;
      const int sw = ((q >> 1) << 2) | ((g4 & 1) << 1);
      const unsigned b0 = bo + (((mw * 4 + (p >> 1)) ^ sw) * 16), b1 = bo + (((mw * 4 + 2 + (p >> 1)) ^ sw) * 16);
#pragma unroll
      for (int n = 0; n < 12; ++n) { RDTR(sb[2 * n], (n & 1) ? b1 : b0, ((n % 6) >> 1) * 8192 + (n / 6) * 4096); RDTR(sb[2 * n + 1], (n & 1) ? b1 : b0, ((n % 6) >> 1) * 8192 + (n / 6) * 4096 + 512); }
      WAITL(0);
      SBAR();
    };
    auto colsum = [&]() {
      const u4 ones = (u4)(0x3F803F80u);
      f4 c[6];
#pragma unroll
      for (int n = 0; n < 12; ++n) c[n % 6] = MFMA16(S8(ones), CAT(sb[2 * n], sb[2 * n + 1]), n >= 6 ? c[n % 6] : (f4)(0.f));
      const u4 csg = (u4){__float_as_uint(g4 ? c[3][0] : c[0][0]), __float_as_uint(g4 ? c[4][0] : c[1][0]), __float_as_uint(g4 ? c[5][0] : c[2][0]), tag};
      ST128(csg, l < 32 ? WS_CS + (hd * 16 + mb) * 512 + l * 16 : WS_SZ);
    };
    u4 stv, cv[8], cd; float cs[3];
    auto gload = [&]() {
      stv = LD128(WS_ST + (mb * 128 + mw * 64 + l) * 16);
      cd = LD128(WS_CAND + (g & 31) * 128 + (l & 7) * 16);
#pragma unroll
      for (int i = 0; i < 8; ++i) cv[i] = LD128(WS_CS + hd * 8192 + i * 1024 + l * 16);
    };
    auto gbad = [&]() {
      unsigned bad = (stv[1] ^ tag) | (stv[3] ^ tag) | (cd[1] ^ tag) | (cd[3] ^ tag);
#pragma unroll
      for (int i = 0; i < 8; ++i) bad |= cv[i][3] ^ tag;
      return __ballot(bad != 0u) != 0ull;
    };
    int idx;
    auto gfin = [&]() {
      if (gbad()) { RETRY(); _Pragma("unroll 1") for (unsigned it = 0; it < SPIN; ++it) { SLEEP(2); gload(); if (!gbad()) break; } }
#pragma unroll
      for (int e = 0; e < 3; ++e) {
        float a = __uint_as_float(cv[0][e]);
#pragma unroll
        for (int i = 1; i < 8; ++i) a += __uint_as_float(cv[i][e]);
        auto r = __builtin_amdgcn_permlane32_swap(__float_as_uint(a), __float_as_uint(a), false, false);
        cs[e] = __uint_as_float(r[0]) + __uint_as_float(r[1]);
      }
      asm volatile("" : "+v"(cs[0]), "+v"(cs[1]), "+v"(cs[2]) : "v"(wt0), "v"(wt1), "v"(wt2), "v"(wt3));
      cd[0] = max(cd[0], cd[2]);
      idx = (int)(cd[0] & 2047u);
    };
    auto lpub = [&]() {
      *(LAS float*)(lds + L_M + (mw * 64 + l) * 4) = __uint_as_float(stv[0]);
      *(LAS float*)(lds + L_RS + (mw * 64 + l) * 4) = __uint_as_float(stv[2]);
      if (l < 32) {
#pragma unroll
        for (int e = 0; e < 3; ++e) *(LAS float*)(lds + L_CS + mw * 384 + ((3 * g4 + e) * 16 + r15) * 4) = cs[e];
      }
      if (mw == 0 && l < 8) *(LAS int*)(lds + L_SEL + l * 4) = idx;
      lsig(myFL, 1u);
    };
    u4 xs[4];
    asm volatile("" ::"s"(wg), "s"(wu), "s"(wd), "v"(cw));
    if (HPRIO) __builtin_amdgcn_s_setprio(HPRIO);
    TICK();
    if (spare) {
      TICK();
      if (ii >> 2) TICK();
      colrd(ii >> 2);
      if (!(ii >> 2)) TICK();
      u4 kk[8];
      auto kload = [&]() {
#pragma unroll
        for (int i = 0; i < 8; ++i) kk[i] = LD128(WS_RG + mw * 8192 + i * 1024 + l * 16);
      };
      kload();
      TICK();
      _Pragma("unroll 1") for (unsigned it = 0; it < SPIN; ++it) {
        unsigned bad = 0;
#pragma unroll
        for (int i = 0; i < 8; ++i) { bad |= (kk[i][1] ^ tag) | (kk[i][3] ^ tag); asm volatile("" : "+v"(bad)); }
        if (__ballot(bad != 0u) == 0ull) break;
        RETRY();
        kload();
        DTICK(7);
      }
      __builtin_amdgcn_s_setprio(3);
      unsigned m1 = max(kk[0][0], kk[0][2]), m2 = min(kk[0][0], kk[0][2]), m3 = 0;
#pragma unroll
      for (int i = 2; i < 16; ++i) {
        const unsigned a = kk[i >> 1][(i & 1) * 2];
        asm("v_med3_u32 %0, %1, %2, %0" : "+v"(m3) : "v"(m2), "v"(a));
        asm("v_med3_u32 %0, %1, %2, %0" : "+v"(m2) : "v"(m1), "v"(a));
        m1 = max(m1, a);
      }
      unsigned mine8 = 0;
#pragma unroll
      for (int it = 0; it < 8; ++it) {
        const unsigned km = wave_max(m1);
        mine8 = l == it ? km : mine8;
        const bool win = m1 == km;
        if (it < 7) { m1 = win ? m2 : m1; m2 = win ? m3 : m2; m3 = win ? 0u : m3; }
        if (it == 3 && nbar < 7) TICK();
      }
      if (__builtin_expect(__ballot(m1 == 0u) != 0ull, 0)) {
        unsigned lim = 0;
#pragma unroll 1
        for (int it = 0; it < 8; ++it) {
          unsigned m = 0;
#pragma unroll
          for (int i = 0; i < 8; ++i) m = umax3(m, kk[i][0] - lim, kk[i][2] - lim);
          lim += wave_max(m);
          mine8 = l == it ? lim : mine8;
        }
      }
      ST64(((u2){mine8, tag}), l < 8 ? WS_CAND + ((hp >> 3) * 16 + mb) * 128 + (mw ? 7 - l : l) * 16 + mw * 8 : WS_SZ);
      __builtin_amdgcn_s_setprio(HPRIO);
      STMP(2);
      TICK();
      colsum();
#pragma unroll
      for (int i = 0; i < 11; ++i) { if (i == 5) DTICK(9); pois(i); }
      touch();
      while (nbar < 10) TICK();
      gload();
      TICK(); TICK();
      WAITV(0);
      gfin();
      STMP(3);
      TICK(); TICK(); TICK();
      lpub();
    } else {
      if (PV == 5) { pois(0); pois(1); }
      TICK();
      if (PV == 4) { pois(0); pois(1); pois(2); }
      if (PV == 5) { pois(2); pois(3); }
      if (ii >> 2) { TICK(); if (PV == 4) pois(3); if (PV >= 4) pois(4); }
      colrd(ii >> 2);
      TICK();
      if (!(ii >> 2)) { if (PV == 4) pois(3); if (PV >= 4) pois(4); }
      colsum();
      if (!(ii >> 2)) TICK();
      STMP(1);
      for (int i = 0; i < ph; ++i) TICK();
      burst(0); touch();
      TICK();
      if (PV == 2) { pois(0); pois(1); pois(2); pois(3); pois(4); }
      TICK(); TICK();
      transp(gw[0]);
      TICK();
      transp(gw[1]);
      TICK();
      if (PV == 3) { pois(0); pois(1); pois(2); pois(3); pois(4); SBAR(); }
      burst(1);
      SBAR();
      if (PV == 0) { pois(0); pois(1); pois(2); pois(3); pois(4); }
      TICK();
      if (PV == 1) { pois(0); pois(1); pois(2); pois(3); pois(4); }
      pois(5); pois(6); pois(7); pois(8); pois(9); pois(10);
      TICK(); TICK();
      transp(gw[2]);
      WAITV(0);
      if (ph < 2) TICK();
      transp(gw[3]);
      if (ph < 1) TICK();
      SBAR();
      gload();
      TICK();
      gfin();
      u4 sv;
      if (moe) {
        const u16* xp = x + (size_t)idx * D + 256 * ksp + 32 * kset + 8 * g4;
#pragma unroll
        for (int it = 0; it < 4; ++it) xs[it] = *(const u4*)(xp + 64 * it);
        sv = LD128(WS_ST + idx * 16);
      }
      SBAR();
      lpub();
      SBAR();
      burst(2);
      STMP(3);
      SBAR();
      if (moe) {
        if (__builtin_expect(__ballot(sv[1] != tag || sv[3] != tag) != 0ull, 0)) {
          _Pragma("unroll 1") for (unsigned it = 0; it < SPIN; ++it) { SLEEP(1); sv = LD128(WS_ST + idx * 16); if (__ballot(sv[1] != tag || sv[3] != tag) == 0ull) break; }
        }
        const float m = __uint_as_float(sv[0]), rs = __uint_as_float(sv[2]);
#pragma unroll
        for (int it = 0; it < 4; ++it) {
#pragma unroll
          for (int e = 0; e < 4; ++e) { const unsigned d = pk2(lo(xs[it][e]) - m, hi(xs[it][e]) - m); xs[it][e] = pk2(lo(d) * rs, hi(d) * rs); }
          asm volatile("" : "+v"(xs[it]));
        }
      }
    }
    __builtin_amdgcn_s_setprio(TP1);
    STMP(4);
    float sg;
    {
      const unsigned mono = cd[0] >> 16, rb = (mono & 0x8000u) ? (mono & 0x7FFFu) : (~mono & 0xFFFFu);
      sg = rbf(1.f / (1.f + __expf(-lo(rb))));
    }
    if (moe) {
      STMP(5);
      f4 acc[8];
#pragma unroll
      for (int it = 0; it < 4; ++it)
#pragma unroll
        for (int c = 0; c < 8; ++c) acc[c] = MFMA16(S8(gw[it][c]), S8(xs[it]), it ? acc[c] : (f4)(0.f));
#pragma unroll
      for (int i = 0; i < 4; ++i) {
        u4 s;
#pragma unroll
        for (int c = 0; c < 4; ++c) {
          auto r = __builtin_amdgcn_permlane32_swap(__float_as_uint(acc[c][i]), __float_as_uint(acc[c + 4][i]), false, false);
          s[c] = __float_as_uint(__uint_as_float(r[0]) + dppf<0x128>(__uint_as_float(r[1])));
        }
        if (r15 < 8) ST128(s, WS_GP + ((chg * 4 + ksp) * 2 + mw) * 2048 + (g4 >> 1) * 1024 + (4 * (g4 & 1) + i) * 128 + r15 * 16);
      }
      WAITV(0);
      if (l == 0) ST64(((u2){tag, tag2}), WS_GF + chg * 1024 + (ksp * 2 + mw) * FS);
      STMP(6);
      transp(dw[0]); transp(dw[1]);
      _Pragma("unroll 1") for (unsigned it = 0; it < SPIN; ++it) {
        u2 f = LD64(WS_GF + chunk * 1024 + (l & 7) * FS);
        if (__ballot(f[0] != tag || f[1] != tag2) == 0ull) break;
        SLEEP(GSL);
      }
      STMP(7);
      __builtin_amdgcn_s_setprio(TP2);
      u4 ha[2];
      {
        u4 pg[4][4];
#pragma unroll
        for (int ks = 0; ks < 4; ++ks)
#pragma unroll
          for (int t = 0; t < 4; ++t)
            pg[ks][t] = LD128(WS_GP + ((chunk * 4 + ks) * 4 + t) * 1024 + (4 * kset + g4) * 128 + np * 16);
        f4 gq[4];
#pragma unroll
        for (int t = 0; t < 4; ++t)
          gq[t] = (__builtin_bit_cast(f4, pg[0][t]) + __builtin_bit_cast(f4, pg[1][t])) + (__builtin_bit_cast(f4, pg[2][t]) + __builtin_bit_cast(f4, pg[3][t]));
        float hv[8];
#pragma unroll
        for (int e = 0; e < 8; ++e) {
          const float gg = rbf(gq[e >> 2][e & 3]), uu = rbf(gq[2 + (e >> 2)][e & 3]);
          hv[e] = rbf(gg / (1.f + __expf(-gg))) * uu;
        }
#pragma unroll
        for (int e = 0; e < 4; ++e) { ha[0][e] = pk2(hv[2 * e], hv[2 * e + 1]); ha[1][e] = (unsigned)__builtin_amdgcn_update_dpp(0, (int)ha[0][e], 0x128, 0xf, 0xf, false); }
      }
#pragma unroll
      for (int it = 0; it < 2; ++it)
#pragma unroll
        for (int c = 0; c < 8; ++c) acc[c] = MFMA16(S8(ha[it]), S8(dw[it][c]), it ? acc[c] : (f4)(0.f));
      if (g4 < 2) {
#pragma unroll
        for (int i = 0; i < 4; ++i) {
          const unsigned yo = WS_YP + ((chunk * 8 + 4 * g4 + i) * D + 256 * ksp + 128 * mw + 4 * r15) * 4;
          ST128(__builtin_bit_cast(u4, ((f4){acc[0][i], acc[1][i], acc[2][i], acc[3][i]})), yo);
          ST128(__builtin_bit_cast(u4, ((f4){acc[4][i], acc[5][i], acc[6][i], acc[7][i]})), yo + 256);
        }
      }
      WAITV(0);
      if (l == 0) ST64(((u2){tag, tag2}), WS_YF + (ksp * 2 + mw) * 8192 + chunk * FS);
      STMP(8);
    }
    unsigned mine = (unsigned)__ballot(!NOMOE && l < 8 && (idx >> 7) == mb && (l & 1) == mw);
    if (mine) {
      if (spare) lwait<4>(adFL, 0x30u, 2);
      _Pragma("unroll 1") for (unsigned it = 0; it < SPIN; ++it) {
        u2 f = LD64(WS_YF + (nb >> 1) * 8192 + (l < 56 ? l : 0) * FS);
        if (__ballot(f[0] != tag || f[1] != tag2) == 0ull) break;
        SLEEP(YSL);
      }
      lwait<1>(adFL, 0x0Fu, 4);
      __builtin_amdgcn_s_setprio(TP2);
      while (mine) {
        const int t = __builtin_ctz(mine); mine &= mine - 1;
        u4 yv[14];
#pragma unroll
        for (int i = 0; i < 14; ++i) yv[i] = LD128(WS_YP + (((i * 4 + g4) * 8 + t) * D + (nb >> 1) * 128 + (r15 >> 3) * 64 + (nb & 1) * 32 + (r15 & 7) * 4) * 4);
        f4 y = __builtin_bit_cast(f4, yv[0]);
#pragma unroll
        for (int i = 1; i < 14; ++i) y += __builtin_bit_cast(f4, yv[i]);
        const float gt = __int_as_float(__builtin_amdgcn_readlane(__float_as_int(sg), t));
#pragma unroll
        for (int e = 0; e < 4; ++e) {
          auto r = __builtin_amdgcn_permlane32_swap(__float_as_uint(y[e]), __float_as_uint(y[e]), false, false);
          const float a = __uint_as_float(r[0]) + __uint_as_float(r[1]);
          auto r2 = __builtin_amdgcn_permlane16_swap(__float_as_uint(a), __float_as_uint(a), false, false);
          y[e] = rbf(rbf(__uint_as_float(r2[0]) + __uint_as_float(r2[1])) * gt);
        }
        if (l < 16) *(LAS f4*)(lds + L_KVX + t * 256 + (l & 7) * 32 + (l >> 3) * 16) = y;
      }
    }
    lsig(myFL, 3u);
    STMP(9);
    STFLUSH();
    if (spare) asm volatile(".Lend_s:"); else asm volatile(".Lend_h:");
    };
    if (sp) helper(BC<true>()); else helper(BC<false>());
    return;
  }

  const int wr = w >> 1, wc = w & 1;
  unsigned tag; u2 kg; u4 sg4;
  const int srow = mb * 128 + hp * 8 + 2 * w + (l >> 5);
  {
    const int row = srow;
    u4 xa[4], rw[4];
#pragma unroll
    for (int c = 0; c < 4; ++c) xa[c] = *(const u4*)(x + row * D + (l & 31) * 8 + 256 * c);
#pragma unroll
    for (int c = 0; c < 4; ++c) rw[c] = *(const u4*)(wrt + (l & 31) * 8 + 256 * c);
    float s0 = 0, s1 = 0;
#pragma unroll
    for (int c = 0; c < 4; ++c)
#pragma unroll
      for (int e = 0; e < 4; e += 2) { unsigned x0 = xa[c][e], x1 = xa[c][e + 1]; s0 = dot2(x0, 0x3F803F80u, s0); s1 = dot2(x1, 0x3F803F80u, s1); }
    const float m = half_sum(s0 + s1) * (1.f / D);
    f2 v[16], q2 = (f2)(0.f), q3 = (f2)(0.f);
#pragma unroll
    for (int c = 0; c < 16; ++c) v[c] = (f2){lo(xa[c >> 2][c & 3]), hi(xa[c >> 2][c & 3])} - m;
#pragma unroll
    for (int c = 0; c < 16; c += 2) { q2 += v[c] * v[c]; q3 += v[c + 1] * v[c + 1]; }
    q2 += q3;
    const float mr = rbf(m);
    const float rs = 1.f / rbf(sqrtf(rbf(rbf(half_sum(q2[0] + q2[1]) * (1.f / D)) + 1e-5f)));
    float d0 = 0, d1 = 0;
#pragma unroll
    for (int c = 0; c < 16; c += 2) {
      const f2 e0 = (f2){lo(xa[c >> 2][c & 3]), hi(xa[c >> 2][c & 3])} - mr, e1 = (f2){lo(xa[c >> 2][(c & 3) + 1]), hi(xa[c >> 2][(c & 3) + 1])} - mr;
      const unsigned p0 = pk2(e0[0], e0[1]), p1 = pk2(e1[0], e1[1]);
      const f2 n0 = (f2){lo(p0), hi(p0)} * rs, n1 = (f2){lo(p1), hi(p1)} * rs;
      unsigned c0 = rw[c >> 2][c & 3], c1 = rw[c >> 2][(c & 3) + 1];
      d0 = dot2(pk2(n0[0], n0[1]), c0, d0); d1 = dot2(pk2(n1[0], n1[1]), c1, d1);
    }
    const float d = half_sum(d0 + d1);
    const unsigned rb = pk2(d, d) & 0xFFFFu;
    const unsigned mono = (rb & 0x8000u) ? (~rb & 0xFFFFu) : (rb | 0x8000u);
    tag = __builtin_amdgcn_readfirstlane(ev0) + 1u;
    kg = (u2){(mono << 16) | (unsigned)row, tag};
    sg4 = (u4){__float_as_uint(mr), tag, __float_as_uint(rs), tag};
  }
  MKWS(tag);
  if ((l & 31) == 0) { ST64(kg, WS_RG + srow * 8); ST128(sg4, WS_ST + srow * 16); }
  unsigned aad[2], bad[2];
#pragma unroll
  for (int s = 0; s < 2; ++s) aad[s] = ldsb + (wr * 64 + r15) * 128 + (((4 * s + g4) ^ ((r15 >> 1) & 7)) * 16);
#pragma unroll
  for (int nh = 0; nh < 2; ++nh) bad[nh] = ldsb + 16384 + (8 * g4 + q) * 128 + (((wc * 4 + 2 * nh + (p >> 1)) ^ (((q >> 1) << 2) | ((g4 & 1) << 1))) * 16) + (p & 1) * 8;
  f4 acc[4][6];
#pragma unroll
  for (int m = 0; m < 4; ++m)
#pragma unroll
    for (int n = 0; n < 6; ++n) { acc[m][n] = (f4)(0.f); asm volatile("" : "+v"(acc[m][n])); }
#define MF(m, n, Fq) do { if (n < 2) acc[m][n] = MFMA16(CAT(Fq.b[2 * n], Fq.b[2 * n + 1]), Fq.a[m], acc[m][n]); \
     else acc[m][n] = MFMA16(Fq.a[m], CAT(Fq.b[2 * n], Fq.b[2 * n + 1]), acc[m][n]); SBAR(); } while (0)
#define RB(G, n, vb0, vb1, SS, hf) RDTR(G.b[2 * (n) + (hf)], (((n) & 1) ? vb1 : vb0), ((n) >> 1) * 8192 + (SS) * 4096 + (hf) * 512)
#define RA(G, m, va) RD128(G.a[m], va, (m) * 2048)
#define SUB(Fq, G, va, vb0, vb1, SS, PRE) do { \
    WAITL(13); SBAR(); \
    MF(0, 0, Fq); PRE; RA(G, 0, va); SBAR(); \
    WAITL(11); SBAR(); \
    MF(1, 0, Fq); RB(G, 0, vb0, vb1, SS, 0); SBAR(); MF(2, 0, Fq); RB(G, 0, vb0, vb1, SS, 1); SBAR(); MF(3, 0, Fq); RA(G, 1, va); SBAR(); \
    WAITL(12); SBAR(); \
    MF(0, 1, Fq); RA(G, 2, va); SBAR(); MF(1, 1, Fq); RA(G, 3, va); SBAR(); MF(2, 1, Fq); RB(G, 1, vb0, vb1, SS, 0); SBAR(); MF(3, 1, Fq); RB(G, 1, vb0, vb1, SS, 1); SBAR(); \
    WAITL(8); SBAR(); \
    MF(0, 2, Fq); RB(G, 2, vb0, vb1, SS, 0); SBAR(); MF(1, 2, Fq); RB(G, 2, vb0, vb1, SS, 1); SBAR(); MF(2, 2, Fq); RB(G, 3, vb0, vb1, SS, 0); SBAR(); MF(3, 2, Fq); RB(G, 3, vb0, vb1, SS, 1); SBAR(); \
    MF(0, 3, Fq); RB(G, 4, vb0, vb1, SS, 0); SBAR(); MF(1, 3, Fq); RB(G, 4, vb0, vb1, SS, 1); SBAR(); MF(2, 3, Fq); RB(G, 5, vb0, vb1, SS, 0); SBAR(); MF(3, 3, Fq); RB(G, 5, vb0, vb1, SS, 1); SBAR(); \
    MF(0, 4, Fq); MF(1, 4, Fq); MF(2, 4, Fq); MF(3, 4, Fq); MF(0, 5, Fq); MF(1, 5, Fq); MF(2, 5, Fq); MF(3, 5, Fq); } while (0)
#define KSTEP(MODE) do { \
    if (kt == sx) { const unsigned xo = xad + buf * STG; \
      _Pragma("unroll") for (int t = 0; t < 8; ++t) { const unsigned a_ = xo ^ (t * 16); \
        asm volatile("ds_read_b64 %0, %1" : "+v"(xv[t]) : "v"(a_) : "memory"); } \
      WAITL(0); SBAR(); } \
    const int nbuf = (buf + 1) & 3; \
    const unsigned bo = buf * STG, nbo = nbuf * STG; \
    { unsigned va = aad[1] + bo, vb0 = bad[0] + bo, vb1 = bad[1] + bo; SUB(f0, f1, va, vb0, vb1, 1, ); } \
    if (MODE < 2) { \
      unsigned va = aad[0] + nbo, vb0 = bad[0] + nbo, vb1 = bad[1] + nbo; \
      SUB(f1, f0, va, vb0, vb1, 0, if (MODE == 0) { WAITL(0); BAR(); } else asm volatile("ds_write_b32 %0, %1" ::"v"(myFL), "v"(1u) : "memory"); SBAR()); \
    } else { asm volatile("ds_write_b32 %1, %2\n ds_read_b32 %0, %3\n s_waitcnt lgkmcnt(2)" : "=&v"(fl0) : "v"(myFL), "v"(2u), "v"(adFL) : "memory"); SBAR(); \
      MF(0, 2, f1); MF(1, 2, f1); MF(2, 2, f1); MF(3, 2, f1); MF(0, 3, f1); MF(1, 3, f1); MF(2, 3, f1); MF(3, 3, f1); \
      MF(0, 4, f1); MF(1, 4, f1); MF(2, 4, f1); MF(3, 4, f1); MF(0, 5, f1); MF(1, 5, f1); MF(2, 5, f1); MF(3, 5, f1); \
      MF(0, 0, f1); MF(1, 0, f1); MF(2, 0, f1); MF(3, 0, f1); MF(0, 1, f1); MF(1, 1, f1); MF(2, 1, f1); MF(3, 1, f1); } \
    buf = nbuf; } while (0)
  u2 xv[8];
#pragma unroll
  for (int t = 0; t < 8; ++t) asm volatile("" : "=v"(xv[t]));
  const int sx = (nb - rot) & 15;
  const unsigned xad = ldsb + (w * 32 + (l & 31)) * 128 + ((r15 >> 1) & 7) * 16 + (l >> 5) * 8;
  STMP(1);
  WAITL(0);
  BAR();
  STMP(2);
  WAITL(0);
  FQ f0, f1;
  { unsigned va = aad[0], vb0 = bad[0], vb1 = bad[1];
    RA(f0, 0, va); RB(f0, 0, vb0, vb1, 0, 0); RB(f0, 0, vb0, vb1, 0, 1); RA(f0, 1, va); RA(f0, 2, va); RA(f0, 3, va);
    RB(f0, 1, vb0, vb1, 0, 0); RB(f0, 1, vb0, vb1, 0, 1); RB(f0, 2, vb0, vb1, 0, 0); RB(f0, 2, vb0, vb1, 0, 1); RB(f0, 3, vb0, vb1, 0, 0); RB(f0, 3, vb0, vb1, 0, 1);
    RB(f0, 4, vb0, vb1, 0, 0); RB(f0, 4, vb0, vb1, 0, 1); RB(f0, 5, vb0, vb1, 0, 0); RB(f0, 5, vb0, vb1, 0, 1);
    WAITL(0); SBAR(); }
  int buf = 0; unsigned fl0;
#pragma unroll 1
  for (int kt = 0; kt < 14; ++kt) KSTEP(0);
  { const int kt = 14; KSTEP(1); }
  { const int kt = 15; KSTEP(2); }
  asm volatile("s_waitcnt lgkmcnt(0)" : "+v"(fl0) : : "memory");
  if (__builtin_expect(((unsigned)__ballot(fl0 < 1u) & 0xC0u) != 0u, 0)) { RETRY2(); lwait(adFL, 0xC0u, 1); }
  STMP(3);
  __builtin_amdgcn_s_setprio(2);
  const LAS float* Lm = (const LAS float*)(lds + L_M) + wr * 64;
  const LAS float* Lr = (const LAS float*)(lds + L_RS) + wr * 64;
  const LAS float* Lc = (const LAS float*)(lds + L_CS) + wc * 96;
  f4 kv[4], zt[2];
  {
    float cs[4];
#pragma unroll
    for (int n = 0; n < 4; ++n) cs[n] = Lc[32 + 16 * n + r15];
    const u4 ones = (u4)(0x3F803F80u);
#pragma unroll
    for (int t = 0; t < 2; ++t) {
      u4 kf[2], vf[2];
#pragma unroll
      for (int mm = 0; mm < 2; ++mm) {
        const int m = 2 * t + mm;
        const f4 mv = *(const LAS f4*)(Lm + m * 16 + 4 * g4), rv = *(const LAS f4*)(Lr + m * 16 + 4 * g4);
#pragma unroll
        for (int a = 0; a < 2; ++a) {
          const f4 uk = acc[m][2 + a] - mv * cs[a];
          const f4 uv = (acc[m][4 + a] - mv * cs[2 + a]) * rv;
          kf[a][2 * mm] = phi2((f2){uk[0], uk[1]}, (f2){rv[0], rv[1]}); kf[a][2 * mm + 1] = phi2((f2){uk[2], uk[3]}, (f2){rv[2], rv[3]});
          vf[a][2 * mm] = pk2(uv[0], uv[1]); vf[a][2 * mm + 1] = pk2(uv[2], uv[3]);
        }
      }
#pragma unroll
      for (int tt = 0; tt < 4; ++tt) kv[tt] = MFMA16(S8(kf[tt & 1]), S8(vf[tt >> 1]), t ? kv[tt] : (f4)(0.f));
#pragma unroll
      for (int a = 0; a < 2; ++a) zt[a] = MFMA16(S8(kf[a]), S8(ones), t ? zt[a] : (f4)(0.f));
    }
  }
  const int hd = hp * 2 + wc;
  {
    LAS f4* ks = (LAS f4*)(lds + L_KVX + w * 3072) + l;
    const LAS f4* kr = (const LAS f4*)(lds + L_KVX + (w ^ 2) * 3072) + l;
    ks[0] = wr ? kv[0] : kv[2]; ks[64] = wr ? kv[1] : kv[3]; ks[128] = wr ? zt[0] : zt[1];
    lsig(myFL, 3);
    const f4 m0 = wr ? kv[2] : kv[0], m1 = wr ? kv[3] : kv[1], mz = wr ? zt[1] : zt[0];
    lwait(adFL, 1u << (w ^ 2), 3);
    const f4 s0 = m0 + kr[0], s1 = m1 + kr[64], sz = mz + kr[128];
    u4 o; o[0] = min(pk2(s0[0], s0[1]), POIS - 1u); o[1] = pk2(s0[2], s0[3]); o[2] = pk2(s1[0], s1[1]); o[3] = pk2(s1[2], s1[3]);
    ST128(o, WS_KVP + (hd * 16 + mb) * 2048 + wr * 1024 + l * 16);
    u4 zo = __builtin_bit_cast(u4, sz); zo[0] = min(zo[0], POIS - 1u);
    if (r15 == 0) ST128(zo, WS_ZP + (hd * 16 + mb) * 128 + g4 * 32 + wr * 16);
  }
  STMP(4);
  {
    const f4 c0 = *(const LAS f4*)(Lc + 4 * g4), c1 = *(const LAS f4*)(Lc + 16 + 4 * g4);
#pragma unroll
    for (int m = 0; m < 4; ++m) {
      const float mv = Lm[m * 16 + r15], rv = Lr[m * 16 + r15];
      const f4 u0 = acc[m][0] - c0 * mv, u1 = acc[m][1] - c1 * mv;
      u4 pf;
      pf[0] = phi2((f2){u0[0], u0[1]}, (f2)(rv)); pf[1] = phi2((f2){u0[2], u0[3]}, (f2)(rv));
      pf[2] = phi2((f2){u1[0], u1[1]}, (f2)(rv)); pf[3] = phi2((f2){u1[2], u1[3]}, (f2)(rv));
      *(LAS u4*)(lds + L_PQ + wr * 8192 + wc * 4096 + m * 1024 + l * 16) = pf;
    }
  }
  lsig(myFL, 4);
  __builtin_amdgcn_s_setprio(0);
  STMP(5);
  const int trow = mb * 128 + w * 32 + (l & 31);
  u2 oh;
  const unsigned flo = (2 * PAIRAT(r15) + wc) * 128;
  const char* fb = uptr(wsx + WS_OH + mb * 4096);
  asm volatile("global_load_dwordx2 %0, %1, %2 sc1" : "=v"(oh) : "v"(flo), "s"(fb) : "memory");
  int sel = -1;
  {
    const u4 s0 = *(const LAS u4*)(lds + L_SEL), s1 = *(const LAS u4*)(lds + L_SEL + 16);
#pragma unroll
    for (int i = 0; i < 8; ++i) { const int v = (int)(i < 4 ? s0[i & 3] : s1[i & 3]); sel = v == trow ? i : sel; }
  }
  const bool anysel = __ballot(sel >= 0) != 0ull;
  f16v oc[2][2];
  {
    const float m = *(const LAS float*)(lds + L_M + (w * 32 + (l & 31)) * 4), rs = *(const LAS float*)(lds + L_RS + (w * 32 + (l & 31)) * 4);
#pragma unroll
    for (int t = 0; t < 8; ++t) {
      const f4 xf = (f4){lo(xv[t][0]), hi(xv[t][0]), lo(xv[t][1]), hi(xv[t][1])};
      const f4 y = (xf - m) * rs + xf;
#pragma unroll
      for (int i = 0; i < 4; ++i) oc[0][t >> 2][(t & 3) * 4 + i] = y[i];
    }
    asm volatile("" : "+v"(oc[0][0]), "+v"(oc[0][1]));
  }
  unsigned wa[2];
#pragma unroll
  for (int t = 0; t < 2; ++t) wa[t] = ldsb + L_WO + wc * 4096 + (4 * (g4 >> 1) + q) * 128 + (((4 * t + 2 * (g4 & 1) + (p >> 1)) ^ ((q >> 1) << 2)) * 16) + (p & 1) * 8;
  u4 a[RD][4];
#pragma unroll
  for (int k = 0; k < RD; ++k) asm volatile("" : "=v"(a[k][0]), "=v"(a[k][1]), "=v"(a[k][2]), "=v"(a[k][3]));
  const char* ob = uptr(wsx + WS_OF + mb * 262144 + wc * 8192 + wr * 4096);
  const unsigned ov0 = l * 16 + wc * 2048, ov1 = ov0 ^ 2048;
  STMP(6);
  lwait<1>(adFL, 0x30u, 1);
  STMP(7);
  WAITV(0);
  asm volatile("" : "+v"(oh));
  unsigned seen = (unsigned)__ballot(oh[0] == tag), go = 0;
  auto need = [&](unsigned mask, bool first) {
    if (__builtin_expect((go & mask) != mask, 0)) {
      _Pragma("unroll 1") for (unsigned it = 0; it < SPIN; ++it) {
        bool poll = (seen & mask) != mask;
        if (first && seen != ~0u && !poll) poll = ((unsigned)__ballot(oh[1] - NOW() - 80u < 1000u) & mask) != 0u;
        if (poll) {
          if (first) SLEEP(4);
          asm volatile("global_load_dwordx2 %0, %1, %2 sc1\n s_waitcnt vmcnt(0)" : "+v"(oh) : "v"(flo), "s"(fb) : "memory");
          seen = (unsigned)__ballot(oh[0] == tag);
        }
        go = seen & (unsigned)__ballot(oh[1] - NOW() - 1u >= 1000u);
        if ((go & mask) == mask) break;
        SLEEP(1);
      }
    }
  };
#define OX 0
#define OL1(pos, i) do { if (OX == 2 && (pos) >= RD) break; \
    asm volatile("global_load_dwordx4 %0, %1, %2 offset:%3 sc1" : "+v"(a[(pos) % RD][i]) : "v"((i) & 1 ? ov1 : ov0), "s"(ob + (OX == 1 ? ((pos) & 1) : PAIRAT(pos)) * 16384), "i"(((i) >> 1) * 1024) : "memory"); } while (0)
#define OLD(pos) do { OL1(pos, 0); OL1(pos, 1); OL1(pos, 2); OL1(pos, 3); } while (0)
  auto omax = [&](u4(&v)[4]) {
    asm volatile("" : "+v"(v[0]), "+v"(v[1]), "+v"(v[2]), "+v"(v[3]));
    return max(umax3(v[0][0], v[1][0], v[2][0]), v[3][0]);
  };
#define OFIX(pos, m) do { \
    if (__builtin_expect(__ballot((m) == POIS) != 0ull, 0)) { RETRY(); _Pragma("unroll 1") for (unsigned it = 0; it < SPIN; ++it) { SLEEP(4); OLD(pos); WAITV(0); if (__ballot(omax(a[(pos) % RD]) == POIS) == 0ull) break; } } } while (0)
#define OS1(i)
#define OS2(i)
  need(1u, true);
  if (OWN) lwait<1>(adFL, 0x30u, 2);
  __builtin_amdgcn_s_setprio(2);
  STMP(15);
  OS1(0); OLD(0); STMP(14);
#pragma unroll
  for (int i = 1; i < RD; ++i) { need(1u << i, false); OS1(i); OLD(i); }
  STMP(10);
  s4 b[8];
#define ORD(pos, f) do { if ((f) < 2 && (pos) == 8) { wa[(f) & 1] += 65536; asm volatile("" : "+v"(wa[(f) & 1])); } \
    RDTR(b[2 * (f)], wa[(f) & 1], ((pos) & 7) * 8192 + ((f) >> 1) * 1024); RDTR(b[2 * (f) + 1], wa[(f) & 1], ((pos) & 7) * 8192 + ((f) >> 1) * 1024 + 2048); } while (0)
#define OMF(pos, f, r) do { if ((f) < 2 && (pos) == 0 && (r) == 1) asm volatile("v_mfma_f32_32x32x16_bf16 %0, %1, %2, 0" : "=&v"(oc[r][(f) & 1]) \
      : "v"(CAT(b[2 * (f)], b[2 * (f) + 1])), "v"(a[(pos) % RD][((f) & 2) + (r)])); \
    else asm volatile("v_mfma_f32_32x32x16_bf16 %0, %1, %2, %0" : "+v"(oc[r][(f) & 1]) \
      : "v"(CAT(b[2 * (f)], b[2 * (f) + 1])), "v"(a[(pos) % RD][((f) & 2) + (r)])); \
    SBAR(); if ((r) == 1 && (pos) < 15) ORD(((pos) + 1) & 15, f); SBAR(); } while (0)
#define ONX(pos) ((pos) > 0 && (pos) + RD - 1 < 16)
#define ONL(pos, i) do { if (ONX(pos)) OL1(((pos) + RD - 1) & 15, i); SBAR(); } while (0)
#define OWL(pos, f) do { WAITL((pos) < 15 ? 6 : 6 - 2 * (f)); SBAR(); } while (0)
#define OHEAD(pos) do { unsigned m_ = 0; \
    OWL(pos, 0); OMF(pos, 0, 0); if (ONX(pos)) { need(1u << (((pos) + RD - 1) & 15), false); OS1(((pos) + RD - 1) & 15); } SBAR(); \
    OMF(pos, 0, 1); ONL(pos, 0); \
    OWL(pos, 1); OMF(pos, 1, 0); ONL(pos, 1); \
    OMF(pos, 1, 1); ONL(pos, 2); \
    OWL(pos, 2); OMF(pos, 2, 0); ONL(pos, 3); \
    if ((pos) == 4) { asm volatile("ds_write_b32 %0, %1" ::"v"(myFL), "v"(5u) : "memory"); SBAR(); } \
    if ((pos) == 13) { asm volatile("ds_read_b32 %0, %1" : "=v"(fl3) : "v"(adFL) : "memory"); SBAR(); } \
    OMF(pos, 2, 1); \
    OWL(pos, 3); OMF(pos, 3, 0); \
    if ((pos) < 15) { WAITV(4 * ((pos) + RD - 1 < 16 ? RD - 2 : 14 - (pos))); m_ = omax(a[((pos) + 1) % RD]); asm volatile("" : "+v"(m_)); SBAR(); } \
    OMF(pos, 3, 1); \
    if ((pos) < 15) { OFIX(((pos) + 1) & 15, m_); OS2(((pos) + 1) & 15); } SBAR(); } while (0)
  unsigned fl3;
  ORD(0, 0); ORD(0, 1); ORD(0, 2); ORD(0, 3);
  { WAITV(4 * (RD - 1)); const unsigned m_ = omax(a[0]); OFIX(0, m_); OS2(0); SBAR(); }
  OHEAD(0); OHEAD(1); STMP(11); OHEAD(2); OHEAD(3); OHEAD(4); OHEAD(5); OHEAD(6); OHEAD(7); STMP(12);
  OHEAD(8); OHEAD(9); STMP(13); OHEAD(10); OHEAD(11); OHEAD(12); OHEAD(13); OHEAD(14);
  f4 xi[8];
  {
    asm volatile("" : "+v"(fl3));
    if (__builtin_expect(((unsigned)__ballot(fl3 < 5u) >> (w ^ 2)) & 1u, 0)) lwait(adFL, 1u << (w ^ 2), 5);
    const unsigned xw = ldsb + L_WO + (4 * wr + wc) * 4096 + l * 16, xr = xw ^ 4096;
#define XW(t) asm volatile("ds_write_b128 %0, %1 offset:%2" ::"v"(xw), "v"(__builtin_shufflevector(oc[1][(t) >> 2], oc[1][(t) >> 2], ((t) & 3) * 4, ((t) & 3) * 4 + 1, ((t) & 3) * 4 + 2, ((t) & 3) * 4 + 3)), \
      "i"(((t) >> 2) * 8192 + ((t) & 3) * 1024) : "memory")
    SBAR();
    OWL(15, 0); OMF(15, 0, 1); OWL(15, 1); OMF(15, 1, 1); OWL(15, 2); OMF(15, 2, 1); OWL(15, 3); OMF(15, 3, 1);
    OMF(15, 0, 0); OMF(15, 1, 0); XW(0); XW(1); XW(2); XW(3); SBAR();
    OMF(15, 2, 0); XW(4); XW(5); XW(6); XW(7); SBAR();
    OMF(15, 3, 0);
    asm volatile("ds_write_b32 %0, %1" ::"v"(myFL), "v"(6u) : "memory");
    _Pragma("unroll 1") for (unsigned it = 0; it < SPIN; ++it) {
      unsigned v;
      asm volatile("ds_read_b32 %0, %1" : "=v"(v) : "v"(adFL) : "memory");
#pragma unroll
      for (int t = 0; t < 8; ++t) RD128(xi[t], xr, (t >> 2) * 8192 + (t & 3) * 1024);
      WAITL(0);
      asm volatile("" : "+v"(v), "+v"(xi[0]), "+v"(xi[1]), "+v"(xi[2]), "+v"(xi[3]), "+v"(xi[4]), "+v"(xi[5]), "+v"(xi[6]), "+v"(xi[7]));
      if ((((unsigned)__ballot(v < 6u) >> (w ^ 1)) & 1u) == 0u) break;
    }
    asm volatile("" : "+v"(oc[0][0]), "+v"(oc[0][1]));
#pragma unroll
    for (int t = 0; t < 8; ++t)
#pragma unroll
      for (int i = 0; i < 4; ++i) xi[t][i] += oc[0][t >> 2][(t & 3) * 4 + i];
  }
  STMP(8);
  if (anysel) {
    lwait(adFL, 0xC0u, 3);
    if (sel >= 0) {
#pragma unroll
      for (int t = 0; t < 8; ++t) xi[t] += *(const LAS f4*)(lds + L_KVX + sel * 256 + (8 * t + 4 * (l >> 5)) * 4);
    }
  }
  const __amdgpu_buffer_rsrc_t outr = __builtin_amdgcn_make_buffer_rsrc(out, 0, 2048 * D * 2, 0x00020000);
  if (FULL) {
    LAS char* tb = (LAS char*)lds + L_PQ + w * 4096;
#pragma unroll
    for (int t = 0; t < 8; ++t) *(LAS u2*)(tb + (l & 31) * 128 + ((t ^ (l & 7)) * 16) + (l >> 5) * 8) = (u2){pk2(xi[t][0], xi[t][1]), pk2(xi[t][2], xi[t][3])};
#pragma unroll
    for (int i = 0; i < 4; ++i) {
      const u4 v = *(const LAS u4*)(tb + i * 1024 + (l >> 3) * 128 + (((l ^ (l >> 3)) & 7) * 16));
      __builtin_amdgcn_raw_buffer_store_b128(v, outr, (mb * 128 + w * 32 + i * 8 + (l >> 3)) * 2048 + nb * 128 + (l & 7) * 16, 0, 16);
    }
  } else {
#pragma unroll
  for (int hh = 0; hh < 4; ++hh) {
    const f4 ta = xi[2 * hh], tb = xi[2 * hh + 1];
    auto r0 = __builtin_amdgcn_permlane32_swap(pk2(ta[0], ta[1]), pk2(tb[0], tb[1]), false, false);
    auto r1 = __builtin_amdgcn_permlane32_swap(pk2(ta[2], ta[3]), pk2(tb[2], tb[3]), false, false);
    __builtin_amdgcn_raw_buffer_store_b128(((u4){r0[0], r1[0], r0[1], r1[1]}), outr, (trow * D + nb * 64 + 16 * hh + 8 * (l >> 5)) * 2, 0, 16);
  }
  }
  if (g == 0 && w == 0 && l == 0) __builtin_amdgcn_raw_buffer_store_b32(tag, wse, WS_E, 0, 16);
  STMP(9);
  STFLUSH();
  asm volatile(".Lend_c:");
  return;
  }
  {
    const int lw = w - 4;
    unsigned ev;
    asm volatile("global_load_dword %0, %1, %2 sc1" : "=v"(ev) : "v"(0u), "s"(ws) : "memory");
    unsigned vf[20], vfw[4];
#pragma unroll
    for (int i = 0; i < 8; ++i) { const int row = 8 * (lw * 8 + i) + (l >> 3); vf[i] = row * 2048 + (((l & 7) ^ ((row >> 1) & 7)) * 16) + (3 - (i & 3)) * 1024; }
#pragma unroll
    for (int i = 0; i < 12; ++i) {
      const int pc = lw * 12 + i, row = (pc & 7) * 8 + (l >> 3);
      vf[8 + i] = row * 6144 + (pc >> 3) * 2048 + (((l & 7) ^ ((((row >> 1) & 1) << 2) | (((row >> 3) & 1) << 1))) * 16) + (3 - (i & 3)) * 1024;
    }
#pragma unroll
    for (int i = 0; i < 4; ++i) vfw[i] = (8 * i + (l >> 3)) * 2048 + (((l & 7) ^ (((l >> 4) & 1) << 2)) * 16) + (3 - i) * 1024;
    const char* Ab = uptr((const char*)x + (size_t)mb * 128 * 2048) - 3072;
    const char* Bb = uptr((const char*)wqkv + hp * 128) - 3072;
    const unsigned mba = ldsb + lw * 8192, mbb = ldsb + 16384 + lw * 12288;
#define GL4(v, sb, mm, MO) do { asm volatile("s_add_i32 m0, %2, %3\n s_nop 0\n global_load_lds_dwordx4 %0, %1" ::"v"((v)[0]), "s"(sb), "s"(mm), "i"(MO) : "memory", "scc"); \
      GLI((v)[1], sb, 1024); GLI((v)[2], sb, 2048); GLI((v)[3], sb, 3072); } while (0)
    auto issue = [&](int s, int u0, int u1) {
      const int kt = (s + rot) & 15;
      const char* sa = Ab + kt * 128; const char* sb = Bb + kt * (64 * 6144);
      const unsigned so = (s & 3) * STG, ma = mba + so, mq = mbb + so;
#pragma unroll
      for (int u = u0; u < u1; ++u) { if (u < 2) GL4(vf + 4 * u, sa, ma, u * 4096); else GL4(vf + 4 * u, sb, mq, (u - 2) * 4096); }
    };
    int tk = 0;
#define LBAR() do { BST(tk + 1); BAR(); BST(tk + 21); ++tk; } while (0)
    issue(0, 0, 5); issue(1, 0, 5);
    BST(60);
    WAITV(20); BST(0); BAR(); BST(20);
    asm volatile("" : "+v"(ev));
    const unsigned tag = __builtin_amdgcn_readfirstlane(ev) + 1u;
    MKWS(tag);
    issue(2, 0, 5);
    int s0 = 3;
#pragma unroll 1
    for (; s0 < (sp ? 16 : 6 + ph); ++s0) { issue(s0, 0, 5); BST(tk + 40); WAITV(40); LBAR(); }
#pragma unroll 1
    for (; s0 < 16; s0 += 5) {
      WAITV(20); LBAR();
      WAITV(0); LBAR();
      issue(s0, 0, 5); issue(s0 + 1, 0, U1); BST(tk + 40); WAITV(4 * U1); LBAR();
      issue(s0 + 1, U1, 5); issue(s0 + 2, 0, 5);
      if (s0 + 3 >= 16) break;
      issue(s0 + 3, 0, U2); BST(tk + 40); WAITV(20 + 4 * U2); LBAR();
      issue(s0 + 3, U2, 5);
      if (s0 + 4 >= 16) break;
      issue(s0 + 4, 0, 5); BST(tk + 40); WAITV(40); LBAR();
    }
    int wq = lw;
    auto wo_upto = [&](int lim) {
      _Pragma("unroll 1") for (; wq < lim; wq += 2) { const char* wp = wob - 3072 + HEADAT(wq) * 65536; const unsigned wm = ldsb + wq * 4096; GL4(vfw, wp, wm, L_WO); }
    };
    asm volatile("ds_write_b32 %0, %1" ::"v"(adFL), "v"(0u) : "memory");
    WAITV(0); WAITL(0); LBAR();
    if (lw == 0) ST64(((u2){tag, NOW() + KD}), l == 0 ? WS_HK + (hp * 16 + mb) * 128 : WS_SZ);
    wo_upto(12);
    BST(15); lwait<1>(adFL, 0x0Fu, 1); BST(35);
    wo_upto(22);
    BST(16); lwait<1>(adFL, 0x0Fu, 2); BST(36);
    wo_upto(32);
    STMP(1);
    const int hd = hp * 2 + lw;
    u4 sel[2];
#pragma unroll
    for (int e = 0; e < 2; ++e) {
      const unsigned v = (r15 >> 2) == g4 ? (((r15 & 3) == 2 * e ? 0x3F80u : 0u) | ((r15 & 3) == 2 * e + 1 ? 0x3F800000u : 0u)) : 0u;
      sel[0][e] = v; sel[0][2 + e] = 0u; sel[1][e] = 0u; sel[1][2 + e] = v;
    }
    u4 pv[16][2], zv0, zv1, pq[8];
    const char* kvb = uptr(wsx + WS_KVP + (size_t)hd * 32768);
    const char* zb = uptr(wsx + WS_ZP + (size_t)hd * 2048);
    const char* hb = uptr(wsx + WS_HK + hp * 2048);
    const unsigned lv16 = l * 16, zoff = r15 * 128 + g4 * 32;
    const unsigned pa = ldsb + L_PQ + lw * 4096 + l * 16, omask = lw ? 0x0Au : 0x05u;
    u2 hk;
    _Pragma("unroll 1") for (unsigned it = 0; it < SPIN; ++it) {
      asm volatile("global_load_dwordx2 %0, %1, %2 sc1\n s_waitcnt vmcnt(0)" : "=v"(hk) : "v"(r15 * 128), "s"(hb) : "memory");
      if (it == 0) lsig(myFL, 1);
      if (__ballot(hk[0] != tag) == 0ull) break;
      SLEEP(2);
    }
    __builtin_amdgcn_s_setprio(3);
    STMP(2);
    const unsigned t0 = NOW();
    unsigned rel = hk[1] - t0; rel = rel < 1000u ? rel : 0u;
    rel = max(rel, (unsigned)__builtin_amdgcn_update_dpp(0, (int)rel, 0xB1, 0xf, 0xf, false));
    rel = max(rel, (unsigned)__builtin_amdgcn_update_dpp(0, (int)rel, 0x4E, 0xf, 0xf, false));
    int gx;
    { const unsigned r0 = __builtin_amdgcn_readlane(rel, 0), r1 = __builtin_amdgcn_readlane(rel, 4), r2 = __builtin_amdgcn_readlane(rel, 8), r3 = __builtin_amdgcn_readlane(rel, 12);
      gx = max(r0, r1) > max(r2, r3) ? (r0 > r1 ? 3 : 2) : (r2 > r3 ? 1 : 0);
      if (OHP) ST64(((u2){tag, t0 + max(max(r0, r1), max(r2, r3)) + OD}), l == 0 ? WS_OH + (mb * 32 + hd) * 128 : WS_SZ); }
#define GLD(gq) do { _Pragma("unroll") for (int i = 0; i < 4; ++i) { const char* sb = kvb + (((gq) ^ gx) * 4 + (i & 2)) * 2048; \
      asm volatile("global_load_dwordx4 %0, %1, %2 offset:%3 sc1" : "=v"(pv[(gq) * 4 + i][0]) : "v"(lv16), "s"(sb), "i"((i & 1) * 2048) : "memory"); \
      asm volatile("global_load_dwordx4 %0, %1, %2 offset:%3 sc1" : "=v"(pv[(gq) * 4 + i][1]) : "v"(lv16), "s"(sb), "i"((i & 1) * 2048 + 1024) : "memory"); } } while (0)
#define ZLD() do { asm volatile("global_load_dwordx4 %0, %1, %2 sc1" : "=v"(zv0) : "v"(zoff), "s"(zb) : "memory"); \
      asm volatile("global_load_dwordx4 %0, %1, %2 offset:16 sc1" : "=v"(zv1) : "v"(zoff), "s"(zb) : "memory"); } while (0)
#pragma unroll
    for (int gq = 0; gq < 4; ++gq) {
      const unsigned rq = __builtin_amdgcn_readlane(rel, 4 * (gq ^ gx));
      _Pragma("unroll 1") for (unsigned it = 0; it < SPIN; ++it) if (NOW() - t0 >= rq) break;
      if (gq == 3) ZLD();
      GLD(gq);
    }
    if (!OHP) ST64(((u2){tag, NOW() + OD}), l == 0 ? WS_OH + (mb * 32 + hd) * 128 : WS_SZ);
    STMP(5);
    lwait(adFL, omask, 4);
#pragma unroll
    for (int i = 0; i < 8; ++i) RD128(pq[i], pa, (i >> 2) * 8192 + (i & 3) * 1024);
#pragma unroll
    for (int gq = 0; gq < 4; ++gq) {
      SBAR(); WAITV((gq == 0 ? 27 : gq == 1 ? 19 : gq == 2 ? 11 : 1) - OHP);
      _Pragma("unroll 1") for (unsigned it = 0; it < SPIN; ++it) {
#pragma unroll
        for (int i = 0; i < 4; ++i) asm volatile("" : "+v"(pv[gq * 4 + i][0]), "+v"(pv[gq * 4 + i][1]));
        unsigned m = umax3(pv[gq * 4][0][0], pv[gq * 4][1][0], pv[gq * 4 + 1][0][0]);
        m = umax3(m, pv[gq * 4 + 1][1][0], pv[gq * 4 + 2][0][0]);
        m = umax3(m, pv[gq * 4 + 2][1][0], pv[gq * 4 + 3][0][0]);
        m = max(m, pv[gq * 4 + 3][1][0]);
        if (gq == 3) { asm volatile("" : "+v"(zv0), "+v"(zv1)); m = umax3(m, zv0[0], zv1[0]); }
        if (__ballot(m == POIS) == 0ull) break;
        RETRY();
        SLEEP(2);
        if (gq == 3) ZLD();
        GLD(gq);
        WAITV(0);
      }
      f4 ga[4];
#pragma unroll
      for (int i = 0; i < 4; ++i)
#pragma unroll
        for (int t = 0; t < 4; ++t) ga[t] = MFMA16(S8(sel[t & 1]), S8(pv[gq * 4 + i][t >> 1]), i ? ga[t] : (f4)(0.f));
#pragma unroll
      for (int t = 0; t < 4; ++t) pv[gq * 4 + (t >> 1)][t & 1] = __builtin_bit_cast(u4, ga[t]);
    }
    STMP(3);
    WAITL(0);
#pragma unroll
    for (int i = 0; i < 8; i += 2) asm volatile("" : "+v"(pq[i]), "+v"(pq[i + 1]));
    u4 kA[2], zA;
    {
      float z[8];
#pragma unroll
      for (int e = 0; e < 4; ++e) { z[e] = __uint_as_float(zv0[e]); z[4 + e] = __uint_as_float(zv1[e]); }
#define ZR(n) "v_add_f32_dpp %0, %0, %0 row_ror:" #n " row_mask:0xf bank_mask:0xf\n v_add_f32_dpp %1, %1, %1 row_ror:" #n " row_mask:0xf bank_mask:0xf\n" \
              "v_add_f32_dpp %2, %2, %2 row_ror:" #n " row_mask:0xf bank_mask:0xf\n v_add_f32_dpp %3, %3, %3 row_ror:" #n " row_mask:0xf bank_mask:0xf\n" \
              "v_add_f32_dpp %4, %4, %4 row_ror:" #n " row_mask:0xf bank_mask:0xf\n v_add_f32_dpp %5, %5, %5 row_ror:" #n " row_mask:0xf bank_mask:0xf\n" \
              "v_add_f32_dpp %6, %6, %6 row_ror:" #n " row_mask:0xf bank_mask:0xf\n v_add_f32_dpp %7, %7, %7 row_ror:" #n " row_mask:0xf bank_mask:0xf\n"
      asm volatile("s_nop 1\n" ZR(8) ZR(4) ZR(2) ZR(1) : "+v"(z[0]), "+v"(z[1]), "+v"(z[2]), "+v"(z[3]), "+v"(z[4]), "+v"(z[5]), "+v"(z[6]), "+v"(z[7]));
#pragma unroll
      for (int e = 0; e < 4; ++e) zA[e] = pk2(z[2 * e], z[2 * e + 1]);
    }
    float inv[8];
#pragma unroll
    for (int i = 0; i < 8; ++i) { f4 dn = MFMA16(S8(zA), S8(pq[i]), (f4)(0.f)); inv[i] = dn[0]; }
#pragma unroll
    for (int t = 0; t < 4; ++t) {
#define PF(gq) __builtin_bit_cast(f4, pv[(gq) * 4 + (t >> 1)][t & 1])
      f4 s = (PF(0) + PF(1)) + (PF(2) + PF(3));
      kA[t >> 1][(t & 1) * 2] = pk2(s[0], s[1]); kA[t >> 1][(t & 1) * 2 + 1] = pk2(s[2], s[3]);
    }
    f4 o0[8], o1[8];
#pragma unroll
    for (int i = 0; i < 8; ++i) {
      o0[i] = MFMA16(S8(kA[0]), S8(pq[i]), (f4)(0.f)); o1[i] = MFMA16(S8(kA[1]), S8(pq[i]), (f4)(0.f));
      inv[i] = __builtin_amdgcn_rcpf(inv[i] + 1e-6f);
    }
#pragma unroll
    for (int i = 0; i < 8; ++i) {
      o0[i] *= inv[i]; o1[i] *= inv[i];
      u4 o; o[0] = min(pk2(o0[i][0], o0[i][1]), POIS - 1u); o[1] = pk2(o0[i][2], o0[i][3]); o[2] = pk2(o1[i][0], o1[i][1]); o[3] = pk2(o1[i][2], o1[i][3]);
      ST128(o, WS_OF + (mb * 32 + hd) * 8192 + (i >> 1) * 2048 + (i & 1) * 256 + g4 * 512 + r15 * 16);
    }
    lsig(myFL, 2);
    STMP(4);
    STFLUSH();
    asm volatile(".Lend_l:");
  }

}

extern "C" void kernel_launch(void* const* d_in, const int* in_sizes, int n_in, void* d_out, int out_size,
                              void* d_ws, size_t ws_size, hipStream_t stream) {
  (void)hipFuncSetAttribute((const void*)fused, hipFuncAttributeMaxDynamicSharedMemorySize, LDS_SZ);
  fused<<<256, 512, LDS_SZ, stream>>>((const u16*)d_in[0], (const u16*)d_in[1], (const u16*)d_in[2], (const u16*)d_in[4],
                                      (const u16*)d_in[5], (const u16*)d_in[6], (const u16*)d_in[7], (char*)d_ws, (u16*)d_out);
}
```

```cpp
#include <hip/hip_runtime.h>
#include <stdint.h>

typedef unsigned short u16;
typedef short s4 __attribute__((ext_vector_type(4)));
typedef short s8 __attribute__((ext_vector_type(8)));
typedef float f4 __attribute__((ext_vector_type(4)));
typedef float f16v __attribute__((ext_vector_type(16)));
typedef float f2 __attribute__((ext_vector_type(2)));
typedef unsigned u4 __attribute__((ext_vector_type(4)));
typedef unsigned u2 __attribute__((ext_vector_type(2)));
typedef __bf16 bf2 __attribute__((ext_vector_type(2)));
#define LAS __attribute__((address_space(3)))
#define DEV __device__ __forceinline__

constexpr int D = 1024, F = 3584;
#define NOMOE 0
#define KD 305
#define OD 205
#define OHP 1
#define FS 128
#define GSL 8
#define YSL 16
#define TP1 0
#define TP2 3
#define TLBW 0
#define FULL 1
#define HPRIO 1
#define OWN 1
#define NOPOIS 0
#define CAP 7
#define RD 4
#define DYN 1
#define JX 0
#define NOSKIP 0
#define GRP 8
#define PV 4
#define U1 4
#define U2 2
#define ROT 0

constexpr unsigned WS_E = 0;
constexpr unsigned WS_YF = 524288;
constexpr unsigned WS_CAND = 12288;
constexpr unsigned WS_GF = 589824;
constexpr unsigned WS_RG = 49152;
constexpr unsigned WS_ST = 65536;
constexpr unsigned WS_HK = 98304;
constexpr unsigned WS_ZP = 131072;
constexpr unsigned WS_CS = 196608;
constexpr unsigned WS_OH = 458752;
constexpr unsigned WS_KVP = 1u << 20;
constexpr unsigned WS_GP = 2u << 20;
constexpr unsigned WS_YP = 3u << 20;
constexpr unsigned WS_OF = 5u << 20;
constexpr unsigned WS_SZ = 9u << 20;
#define NR 4
#define RGO(tag) ((64u << 20) + ((tag) & (NR - 1u)) * (10u << 20))
#define MKWS(tag) char* const wsx = ws + RGO(tag); const __amdgpu_buffer_rsrc_t wsr = __builtin_amdgcn_make_buffer_rsrc(wsx, 0, WS_SZ, 0x00020000)

constexpr int STG = 40960;
constexpr int L_KVX = 0;
constexpr int L_PQ = 12288;
constexpr int L_M = 28672, L_RS = L_M + 512, L_CS = L_RS + 512, L_SEL = L_CS + 768, L_FL = L_SEL + 64;
constexpr int L_WO = 32768;
constexpr int LDS_SZ = 163840;
constexpr unsigned POIS = 0xFFFFFFFFu;

DEV unsigned pk2(float a, float b) { bf2 v; v[0] = (__bf16)a; v[1] = (__bf16)b; return __builtin_bit_cast(unsigned, v); }
DEV float lo(unsigned u) { return __uint_as_float(u << 16); }
DEV float hi(unsigned u) { return __uint_as_float(u & 0xFFFF0000u); }
DEV float rbf(float a) { return lo(pk2(a, a)); }
DEV float dot2(unsigned a, unsigned b, float c) { return __builtin_amdgcn_fdot2_f32_bf16(__builtin_bit_cast(bf2, a), __builtin_bit_cast(bf2, b), c, false); }
#define RD128(dst, addr, OFF) asm volatile("ds_read_b128 %0, %1 offset:%2" : "=v"(dst) : "v"(addr), "i"(OFF) : "memory")
#define RDTR(dst, addr, OFF) asm volatile("ds_read_b64_tr_b16 %0, %1 offset:%2" : "=v"(dst) : "v"(addr), "i"(OFF) : "memory")
#define SBAR() __builtin_amdgcn_sched_barrier(0)
#define CAT(x, y) __builtin_shufflevector(x, y, 0, 1, 2, 3, 4, 5, 6, 7)
#define WAITV(n) asm volatile("s_waitcnt vmcnt(%0)" ::"i"(n) : "memory")
#define WAITL(n) asm volatile("s_waitcnt lgkmcnt(%0)" ::"i"(n) : "memory")
#define MEMB() asm volatile("" ::: "memory")
#define SLEEP(n) asm volatile("s_sleep %0" ::"i"(n) : "memory")
#define BAR() asm volatile("s_barrier" ::: "memory")
#define GL0(vo, sb, m0v) asm volatile("s_mov_b32 m0, %2\n s_nop 0\n global_load_lds_dwordx4 %0, %1" ::"v"(vo), "s"(sb), "s"(m0v) : "memory")
#define GLI(vo, sb, OFF) asm volatile("global_load_lds_dwordx4 %0, %1 offset:%2" ::"v"(vo), "s"(sb), "i"(OFF) : "memory")
#define MFMA16(a, b, c) __builtin_amdgcn_mfma_f32_16x16x32_bf16(a, b, c, 0, 0, 0)
#define S8(x) __builtin_bit_cast(s8, x)
#define LD128(off) __builtin_amdgcn_raw_buffer_load_b128(wsr, (off), 0, 16)
#define LD64(off) __builtin_amdgcn_raw_buffer_load_b64(wsr, (off), 0, 16)
#define ST128(v, off) __builtin_amdgcn_raw_buffer_store_b128((v), wsr, (off), 0, 16)
#define ST64(v, off) __builtin_amdgcn_raw_buffer_store_b64((v), wsr, (off), 0, 16)
#define OFA(i) (WS_OF + ((mb * 32 + hd) * 8 + (i)) * 1024 + l * 16)
#define NOW() ((unsigned)__builtin_amdgcn_s_memrealtime())
constexpr unsigned SPIN = 1u << 21;
DEV const char* uptr(const char* p) {
  unsigned long long v = (unsigned long long)p;
  unsigned lo_ = __builtin_amdgcn_readfirstlane((unsigned)v), hi_ = __builtin_amdgcn_readfirstlane((unsigned)(v >> 32));
  return (const char*)(((unsigned long long)hi_ << 32) | lo_);
}
DEV void lsig(unsigned addr, unsigned val) { asm volatile("s_waitcnt lgkmcnt(0)\n ds_write_b32 %0, %1" ::"v"(addr), "v"(val) : "memory"); }
template <int SL = 0> DEV void lwait(unsigned addr, unsigned mask, unsigned target) {
  _Pragma("unroll 1") for (unsigned it = 0; it < SPIN; ++it) {
    unsigned v;
    asm volatile("ds_read_b32 %0, %1\n s_waitcnt lgkmcnt(0)" : "=v"(v) : "v"(addr) : "memory");
    if (((unsigned)__ballot(v < target) & mask) == 0u) break;
    if (SL) SLEEP(SL);
  }
}
DEV unsigned umax3(unsigned a, unsigned b, unsigned c) { return max(max(a, b), c); }
template <int C> DEV float dppf(float v) { return __int_as_float(__builtin_amdgcn_update_dpp(0, __float_as_int(v), C, 0xf, 0xf, false)); }
DEV unsigned wave_max(unsigned v) {
  v = max(v, (unsigned)__builtin_amdgcn_update_dpp(0, (int)v, 0x111, 0xf, 0xf, false));
  v = max(v, (unsigned)__builtin_amdgcn_update_dpp(0, (int)v, 0x112, 0xf, 0xf, false));
  v = max(v, (unsigned)__builtin_amdgcn_update_dpp(0, (int)v, 0x114, 0xf, 0xf, false));
  v = max(v, (unsigned)__builtin_amdgcn_update_dpp(0, (int)v, 0x118, 0xf, 0xf, false));
  v = max(v, (unsigned)__builtin_amdgcn_update_dpp(0, (int)v, 0x142, 0xa, 0xf, false));
  v = max(v, (unsigned)__builtin_amdgcn_update_dpp(0, (int)v, 0x143, 0xc, 0xf, false));
  return (unsigned)__builtin_amdgcn_readlane((int)v, 63);
}
DEV unsigned phi2(f2 u, f2 rs) {
  f2 t = u * rs;
  f2 a = t * 1.44269504f, p = t + 1.f, r;
  r[0] = __builtin_amdgcn_fmed3f(__builtin_amdgcn_exp2f(a[0]), p[0], 1.f);
  r[1] = __builtin_amdgcn_fmed3f(__builtin_amdgcn_exp2f(a[1]), p[1], 1.f);
  r += 1e-6f;
  return pk2(r[0], r[1]);
}

#define STDECL
#define STMP(i)
#define BST(i)
#define STFLUSH()
#define RETRY()
#define RETRY2()

DEV float half_sum(float v) {
  asm volatile("s_nop 1\n v_add_f32_dpp %0, %0, %0 row_ror:8 row_mask:0xf bank_mask:0xf\n s_nop 1\n v_add_f32_dpp %0, %0, %0 row_ror:4 row_mask:0xf bank_mask:0xf\n"
               "s_nop 1\n v_add_f32_dpp %0, %0, %0 row_ror:2 row_mask:0xf bank_mask:0xf\n s_nop 1\n v_add_f32_dpp %0, %0, %0 row_ror:1 row_mask:0xf bank_mask:0xf\n s_nop 1" : "+v"(v));
  auto r = __builtin_amdgcn_permlane16_swap(__float_as_uint(v), __float_as_uint(v), false, false);
  return __uint_as_float(r[0]) + __uint_as_float(r[1]);
}

struct FQ { s8 a[4]; s4 b[12]; };
template <bool B> struct BC { static constexpr bool v = B; };

__global__ __launch_bounds__(512) void fused(const u16* __restrict__ x, const u16* __restrict__ wqkv, const u16* __restrict__ wo,
                                             const u16* __restrict__ wrt, const u16* __restrict__ wg, const u16* __restrict__ wu,
                                             const u16* __restrict__ wd, char* __restrict__ ws, u16* __restrict__ out) {
  extern __shared__ __attribute__((aligned(16))) char lds[];
  asm volatile(".Lcode_beg:");
  const int w = __builtin_amdgcn_readfirstlane(threadIdx.x >> 6), l = threadIdx.x & 63;
  STDECL;
  STMP(0);
  const int g = blockIdx.x, xcd = g & 7, j = g >> 3, ii = j >> 2;
  unsigned cw = 0;
  if (w == 7) {
    unsigned long long pc; unsigned off, len, l1, l2, l3;
    asm volatile("s_getpc_b64 %0\n.Lcode_here:" : "=s"(pc));
    asm volatile("s_mov_b32 %0, (.Lcode_here - .Lcode_beg)" : "=s"(off));
    asm volatile("s_mov_b32 %0, (.Lend_c - .Lcode_beg)" : "=s"(len));
    asm volatile("s_mov_b32 %0, (.Lend_l - .Lcode_beg)" : "=s"(l1));
    asm volatile("s_mov_b32 %0, (.Lend_h - .Lcode_beg)" : "=s"(l2));
    asm volatile("s_mov_b32 %0, (.Lend_s - .Lcode_beg)" : "=s"(l3));
    len = max(max(len, l3), max(l1, l2));
    const unsigned o = min((unsigned)(l * 32 + j) * 64u, len - 4u);
    asm volatile("global_load_dword %0, %1, %2" : "+v"(cw) : "v"(o), "s"(pc - off) : "memory");
  }
#define PTAB 0xA98BFEDC76542103ull
#define PHT 0x6624924999924924ull
#define CX 4
  const int mb = (xcd & 1) * 8 + ii, hp = (xcd >> 1) * 4 + (j & 3);
  const bool sp = (hp & 7) == 3;
  const int rot = 2 * xcd + 4 * (ii & 3);
  const int nb = hp;
#define PAIRAT(p) ((int)(PTAB >> (4 * (p))) & 15)
#define HEADAT(s) (2 * PAIRAT((s) >> 1) + ((s) & 1))
  const int ph = (int)(PHT >> (2 * j)) & 3;
  const unsigned ldsb = (unsigned)(size_t)(LAS char*)lds;
  const unsigned adFL = ldsb + L_FL + 4 * (l & 7), myFL = ldsb + L_FL + 4 * w;
  const __amdgpu_buffer_rsrc_t wse = __builtin_amdgcn_make_buffer_rsrc(ws, 0, 64, 0x00020000);
  const int r15 = l & 15, g4 = l >> 4, q = (l & 15) >> 2, p = l & 3;
  const char* wob = uptr((const char*)wo + nb * 128);

  if (w != 4 && w != 5) {
  const unsigned ev0 = __builtin_amdgcn_raw_buffer_load_b32(wse, WS_E, 0, 16);
  if (w >= 6) {
    const int mw = w - 6;
    const unsigned tag = __builtin_amdgcn_readfirstlane(ev0) + 1u, tag2 = tag * 0x9E3779B1u;
    MKWS(tag);
    auto helper = [&](auto SP) {
    constexpr bool spare = decltype(SP)::v, moe = !spare && !NOMOE;
    int nbar = 0;
#define TICK() do { BST(nbar); SBAR(); BAR(); SBAR(); ++nbar; } while (0)
#define DTICK(lim) do { if (nbar < (lim)) TICK(); else SLEEP(2); } while (0)
    const int hd = hp * 2 + mw;
    const u4 pz = (u4)(POIS);
    const int chunk = 4 * (hp - (hp > 3) - (hp > 11)) + (ii & 3), ksp = (xcd & 1) * 2 + (ii >> 2), chg = chunk ^ (mw * CX);
    const int np = l & 7, kset = (l >> 3) & 1;
    u4 gw[4][8], dw[2][8];
    const __amdgpu_buffer_rsrc_t wgr = __builtin_amdgcn_make_buffer_rsrc((void*)(mw ? wu : wg), 0, D * F * 2, 0x00020000);
    const __amdgpu_buffer_rsrc_t wdr = __builtin_amdgcn_make_buffer_rsrc((void*)wd, 0, D * F * 2, 0x00020000);
    const __amdgpu_buffer_rsrc_t wor = __builtin_amdgcn_make_buffer_rsrc((void*)wo, 0, D * D * 2, 0x00020000);
    const unsigned gvo = ((256 * ksp + 32 * kset + 8 * g4) * F + 64 * chg + 8 * np) * 2;
    const unsigned dvo = ((64 * chunk + 8 * g4) * D + 256 * ksp + 128 * mw + 8 * r15) * 2;
    auto burst = [&](int k) {
      if (moe) {
#pragma unroll
        for (int i = 0; i < 16; ++i) {
          if (k < 2) gw[(2 * k + (i >> 3)) & 3][i & 7] = __builtin_amdgcn_raw_buffer_load_b128(wgr, gvo, (128 * k + 64 * (i >> 3) + (i & 7)) * F * 2, 2);
          else dw[i >> 3][i & 7] = __builtin_amdgcn_raw_buffer_load_b128(wdr, dvo, (32 * (i >> 3) + (i & 7)) * D * 2, 2);
        }
      }
    };
    auto transp = [&](u4(&v)[8]) {
      if (moe) {
        u4 f[8];
#pragma unroll
        for (int c = 0; c < 8; ++c)
#pragma unroll
          for (int e = 0; e < 4; ++e) {
            const unsigned ev = v[2 * e][c >> 1], od = v[2 * e + 1][c >> 1];
            f[c][e] = (c & 1) ? __builtin_amdgcn_perm(od, ev, 0x07060302u) : __builtin_amdgcn_perm(od, ev, 0x05040100u);
          }
#pragma unroll
        for (int c = 0; c < 8; ++c) { v[c] = f[c]; asm volatile("" : "+v"(v[c])); }
      }
    };
    auto pois = [&](int i) {
      if (i < 2) ST128(pz, WS_KVP + (hd * 16 + mb) * 2048 + i * 1024 + l * 16);
      else if (i == 2) ST128(pz, WS_ZP + (hd * 16 + mb) * 128 + (l & 7) * 16);
      else if (!NOPOIS) ST128(pz, OFA(i - 3));
    };
    unsigned wt0, wt1, wt2 = 0, wt3 = 0;
    auto touch = [&]() {
      const unsigned to = (j * 32 + mw * 16 + (l >> 3)) * 2048 + (xcd >> 1) * 512 + (l & 7) * 64;
      wt0 = __builtin_amdgcn_raw_buffer_load_b32(wor, to, 0, 0); wt1 = __builtin_amdgcn_raw_buffer_load_b32(wor, to, 16384, 0);
      if (TLBW) {
        const __amdgpu_buffer_rsrc_t outr = __builtin_amdgcn_make_buffer_rsrc(out, 0, 2048 * D * 2, 0x00020000);
        wt2 = __builtin_amdgcn_raw_buffer_load_b32(outr, (mb * 128 + mw * 64 + (l & 3) * 16) * 2048 + nb * 128, 0, 0);
        wt3 = __builtin_amdgcn_raw_buffer_load_b32(wsr, (l & 1) ? WS_GP + g * 4096 + mw * 2048 : WS_YP + (g >> 2) * 32768 + (l & 2) * 8192, 0, 0);
      }
    };
    s4 sb[24];
    auto colrd = [&](int h) {
      const unsigned bo = ldsb + (1 + h) * STG + 16384 + (8 * g4 + q) * 128 + (p & 1) * 8;
      const int sw = ((q >> 1) << 2) | ((g4 & 1) << 1);
      const unsigned b0 = bo + (((mw * 4 + (p >> 1)) ^ sw) * 16), b1 = bo + (((mw * 4 + 2 + (p >> 1)) ^ sw) * 16);
#pragma unroll
      for (int n = 0; n < 12; ++n) { RDTR(sb[2 * n], (n & 1) ? b1 : b0, ((n % 6) >> 1) * 8192 + (n / 6) * 4096); RDTR(sb[2 * n + 1], (n & 1) ? b1 : b0, ((n % 6) >> 1) * 8192 + (n / 6) * 4096 + 512); }
      WAITL(0);
      SBAR();
    };
    auto colsum = [&]() {
      const u4 ones = (u4)(0x3F803F80u);
      f4 c[6];
#pragma unroll
      for (int n = 0; n < 12; ++n) c[n % 6] = MFMA16(S8(ones), CAT(sb[2 * n], sb[2 * n + 1]), n >= 6 ? c[n % 6] : (f4)(0.f));
      const u4 csg = (u4){__float_as_uint(g4 ? c[3][0] : c[0][0]), __float_as_uint(g4 ? c[4][0] : c[1][0]), __float_as_uint(g4 ? c[5][0] : c[2][0]), tag};
      ST128(csg, l < 32 ? WS_CS + (hd * 16 + mb) * 512 + l * 16 : WS_SZ);
    };
    u4 stv, cv[8], cd; float cs[3];
    auto gload = [&]() {
      stv = LD128(WS_ST + (mb * 128 + mw * 64 + l) * 16);
      cd = LD128(WS_CAND + (g & 31) * 128 + (l & 7) * 16);
#pragma unroll
      for (int i = 0; i < 8; ++i) cv[i] = LD128(WS_CS + hd * 8192 + i * 1024 + l * 16);
    };
    auto gbad = [&]() {
      unsigned bad = (stv[1] ^ tag) | (stv[3] ^ tag) | (cd[1] ^ tag) | (cd[3] ^ tag);
#pragma unroll
      for (int i = 0; i < 8; ++i) bad |= cv[i][3] ^ tag;
      return __ballot(bad != 0u) != 0ull;
    };
    int idx;
    auto gfin = [&]() {
      if (gbad()) { RETRY(); _Pragma("unroll 1") for (unsigned it = 0; it < SPIN; ++it) { SLEEP(2); gload(); if (!gbad()) break; } }
#pragma unroll
      for (int e = 0; e < 3; ++e) {
        float a = __uint_as_float(cv[0][e]);
#pragma unroll
        for (int i = 1; i < 8; ++i) a += __uint_as_float(cv[i][e]);
        auto r = __builtin_amdgcn_permlane32_swap(__float_as_uint(a), __float_as_uint(a), false, false);
        cs[e] = __uint_as_float(r[0]) + __uint_as_float(r[1]);
      }
      asm volatile("" : "+v"(cs[0]), "+v"(cs[1]), "+v"(cs[2]) : "v"(wt0), "v"(wt1), "v"(wt2), "v"(wt3));
      cd[0] = max(cd[0], cd[2]);
      idx = (int)(cd[0] & 2047u);
    };
    auto lpub = [&]() {
      *(LAS float*)(lds + L_M + (mw * 64 + l) * 4) = __uint_as_float(stv[0]);
      *(LAS float*)(lds + L_RS + (mw * 64 + l) * 4) = __uint_as_float(stv[2]);
      if (l < 32) {
#pragma unroll
        for (int e = 0; e < 3; ++e) *(LAS float*)(lds + L_CS + mw * 384 + ((3 * g4 + e) * 16 + r15) * 4) = cs[e];
      }
      if (mw == 0 && l < 8) *(LAS int*)(lds + L_SEL + l * 4) = idx;
      lsig(myFL, 1u);
    };
    u4 xs[4];
    asm volatile("" ::"s"(wg), "s"(wu), "s"(wd), "v"(cw));
    if (HPRIO) __builtin_amdgcn_s_setprio(HPRIO);
    TICK();
    if (spare) {
      TICK();
      if (ii >> 2) TICK();
      colrd(ii >> 2);
      if (!(ii >> 2)) TICK();
      u4 kk[8];
      auto kload = [&]() {
#pragma unroll
        for (int i = 0; i < 8; ++i) kk[i] = LD128(WS_RG + mw * 8192 + i * 1024 + l * 16);
      };
      kload();
      TICK();
      _Pragma("unroll 1") for (unsigned it = 0; it < SPIN; ++it) {
        unsigned bad = 0;
#pragma unroll
        for (int i = 0; i < 8; ++i) { bad |= (kk[i][1] ^ tag) | (kk[i][3] ^ tag); asm volatile("" : "+v"(bad)); }
        if (__ballot(bad != 0u) == 0ull) break;
        RETRY();
        kload();
        DTICK(7);
      }
      __builtin_amdgcn_s_setprio(3);
      unsigned m1 = max(kk[0][0], kk[0][2]), m2 = min(kk[0][0], kk[0][2]), m3 = 0;
#pragma unroll
      for (int i = 2; i < 16; ++i) {
        const unsigned a = kk[i >> 1][(i & 1) * 2];
        asm("v_med3_u32 %0, %1, %2, %0" : "+v"(m3) : "v"(m2), "v"(a));
        asm("v_med3_u32 %0, %1, %2, %0" : "+v"(m2) : "v"(m1), "v"(a));
        m1 = max(m1, a);
      }
      unsigned mine8 = 0;
#pragma unroll
      for (int it = 0; it < 8; ++it) {
        const unsigned km = wave_max(m1);
        mine8 = l == it ? km : mine8;
        const bool win = m1 == km;
        if (it < 7) { m1 = win ? m2 : m1; m2 = win ? m3 : m2; m3 = win ? 0u : m3; }
        if (it == 3 && nbar < 7) TICK();
      }
      if (__builtin_expect(__ballot(m1 == 0u) != 0ull, 0)) {
        unsigned lim = 0;
#pragma unroll 1
        for (int it = 0; it < 8; ++it) {
          unsigned m = 0;
#pragma unroll
          for (int i = 0; i < 8; ++i) m = umax3(m, kk[i][0] - lim, kk[i][2] - lim);
          lim += wave_max(m);
          mine8 = l == it ? lim : mine8;
        }
      }
      ST64(((u2){mine8, tag}), l < 8 ? WS_CAND + ((hp >> 3) * 16 + mb) * 128 + (mw ? 7 - l : l) * 16 + mw * 8 : WS_SZ);
      __builtin_amdgcn_s_setprio(HPRIO);
      STMP(2);
      TICK();
      colsum();
#pragma unroll
      for (int i = 0; i < 11; ++i) { if (i == 5) DTICK(9); pois(i); }
      touch();
      while (nbar < 10) TICK();
      gload();
      TICK(); TICK();
      WAITV(0);
      gfin();
      STMP(3);
      TICK(); TICK(); TICK();
      lpub();
    } else {
      if (PV == 5) { pois(0); pois(1); }
      TICK();
      if (PV == 4) { pois(0); pois(1); pois(2); }
      if (PV == 5) { pois(2); pois(3); }
      if (ii >> 2) { TICK(); if (PV == 4) pois(3); if (PV >= 4) pois(4); }
      colrd(ii >> 2);
      TICK();
      if (!(ii >> 2)) { if (PV == 4) pois(3); if (PV >= 4) pois(4); }
      colsum();
      if (!(ii >> 2)) TICK();
      STMP(1);
      for (int i = 0; i < ph; ++i) TICK();
      burst(0); touch();
      TICK();
      if (PV == 2) { pois(0); pois(1); pois(2); pois(3); pois(4); }
      TICK(); TICK();
      transp(gw[0]);
      TICK();
      transp(gw[1]);
      TICK();
      if (PV == 3) { pois(0); pois(1); pois(2); pois(3); pois(4); SBAR(); }
      burst(1);
      SBAR();
      if (PV == 0) { pois(0); pois(1); pois(2); pois(3); pois(4); }
      TICK();
      if (PV == 1) { pois(0); pois(1); pois(2); pois(3); pois(4); }
      pois(5); pois(6); pois(7); pois(8); pois(9); pois(10);
      TICK(); TICK();
      transp(gw[2]);
      WAITV(0);
      if (ph < 2) TICK();
      transp(gw[3]);
      if (ph < 1) TICK();
      SBAR();
      gload();
      TICK();
      gfin();
      u4 sv;
      if (moe) {
        const u16* xp = x + (size_t)idx * D + 256 * ksp + 32 * kset + 8 * g4;
#pragma unroll
        for (int it = 0; it < 4; ++it) xs[it] = *(const u4*)(xp + 64 * it);
        sv = LD128(WS_ST + idx * 16);
      }
      SBAR();
      lpub();
      SBAR();
      burst(2);
      STMP(3);
      SBAR();
      if (moe) {
        if (__builtin_expect(__ballot(sv[1] != tag || sv[3] != tag) != 0ull, 0)) {
          _Pragma("unroll 1") for (unsigned it = 0; it < SPIN; ++it) { SLEEP(1); sv = LD128(WS_ST + idx * 16); if (__ballot(sv[1] != tag || sv[3] != tag) == 0ull) break; }
        }
        const float m = __uint_as_float(sv[0]), rs = __uint_as_float(sv[2]);
#pragma unroll
        for (int it = 0; it < 4; ++it) {
#pragma unroll
          for (int e = 0; e < 4; ++e) { const unsigned d = pk2(lo(xs[it][e]) - m, hi(xs[it][e]) - m); xs[it][e] = pk2(lo(d) * rs, hi(d) * rs); }
          asm volatile("" : "+v"(xs[it]));
        }
      }
    }
    __builtin_amdgcn_s_setprio(TP1);
    STMP(4);
    float sg;
    {
      const unsigned mono = cd[0] >> 16, rb = (mono & 0x8000u) ? (mono & 0x7FFFu) : (~mono & 0xFFFFu);
      sg = rbf(1.f / (1.f + __expf(-lo(rb))));
    }
    if (moe) {
      STMP(5);
      f4 acc[8];
#pragma unroll
      for (int it = 0; it < 4; ++it)
#pragma unroll
        for (int c = 0; c < 8; ++c) acc[c] = MFMA16(S8(gw[it][c]), S8(xs[it]), it ? acc[c] : (f4)(0.f));
#pragma unroll
      for (int i = 0; i < 4; ++i) {
        u4 s;
#pragma unroll
        for (int c = 0; c < 4; ++c) {
          auto r = __builtin_amdgcn_permlane32_swap(__float_as_uint(acc[c][i]), __float_as_uint(acc[c + 4][i]), false, false);
          s[c] = __float_as_uint(__uint_as_float(r[0]) + dppf<0x128>(__uint_as_float(r[1])));
        }
        if (r15 < 8) ST128(s, WS_GP + ((chg * 4 + ksp) * 2 + mw) * 2048 + (g4 >> 1) * 1024 + (4 * (g4 & 1) + i) * 128 + r15 * 16);
      }
      WAITV(0);
      if (l == 0) ST64(((u2){tag, tag2}), WS_GF + chg * 1024 + (ksp * 2 + mw) * FS);
      STMP(6);
      transp(dw[0]); transp(dw[1]);
      _Pragma("unroll 1") for (unsigned it = 0; it < SPIN; ++it) {
        u2 f = LD64(WS_GF + chunk * 1024 + (l & 7) * FS);
        if (__ballot(f[0] != tag || f[1] != tag2) == 0ull) break;
        SLEEP(GSL);
      }
      STMP(7);
      __builtin_amdgcn_s_setprio(TP2);
      u4 ha[2];
      {
        u4 pg[4][4];
#pragma unroll
        for (int ks = 0; ks < 4; ++ks)
#pragma unroll
          for (int t = 0; t < 4; ++t)
            pg[ks][t] = LD128(WS_GP + ((chunk * 4 + ks) * 4 + t) * 1024 + (4 * kset + g4) * 128 + np * 16);
        f4 gq[4];
#pragma unroll
        for (int t = 0; t < 4; ++t)
          gq[t] = (__builtin_bit_cast(f4, pg[0][t]) + __builtin_bit_cast(f4, pg[1][t])) + (__builtin_bit_cast(f4, pg[2][t]) + __builtin_bit_cast(f4, pg[3][t]));
        float hv[8];
#pragma unroll
        for (int e = 0; e < 8; ++e) {
          const float gg = rbf(gq[e >> 2][e & 3]), uu = rbf(gq[2 + (e >> 2)][e & 3]);
          hv[e] = rbf(gg / (1.f + __expf(-gg))) * uu;
        }
#pragma unroll
        for (int e = 0; e < 4; ++e) { ha[0][e] = pk2(hv[2 * e], hv[2 * e + 1]); ha[1][e] = (unsigned)__builtin_amdgcn_update_dpp(0, (int)ha[0][e], 0x128, 0xf, 0xf, false); }
      }
#pragma unroll
      for (int it = 0; it < 2; ++it)
#pragma unroll
        for (int c = 0; c < 8; ++c) acc[c] = MFMA16(S8(ha[it]), S8(dw[it][c]), it ? acc[c] : (f4)(0.f));
      if (g4 < 2) {
#pragma unroll
        for (int i = 0; i < 4; ++i) {
          const unsigned yo = WS_YP + ((chunk * 8 + 4 * g4 + i) * D + 256 * ksp + 128 * mw + 4 * r15) * 4;
          ST128(__builtin_bit_cast(u4, ((f4){acc[0][i], acc[1][i], acc[2][i], acc[3][i]})), yo);
          ST128(__builtin_bit_cast(u4, ((f4){acc[4][i], acc[5][i], acc[6][i], acc[7][i]})), yo + 256);
        }
      }
      WAITV(0);
      if (l == 0) ST64(((u2){tag, tag2}), WS_YF + (ksp * 2 + mw) * 8192 + chunk * FS);
      STMP(8);
    }
    unsigned mine = (unsigned)__ballot(!NOMOE && l < 8 && (idx >> 7) == mb && (l & 1) == mw);
    if (mine) {
      if (spare) lwait<4>(adFL, 0x30u, 2);
      _Pragma("unroll 1") for (unsigned it = 0; it < SPIN; ++it) {
        u2 f = LD64(WS_YF + (nb >> 1) * 8192 + (l < 56 ? l : 0) * FS);
        if (__ballot(f[0] != tag || f[1] != tag2) == 0ull) break;
        SLEEP(YSL);
      }
      lwait<1>(adFL, 0x0Fu, 4);
      __builtin_amdgcn_s_setprio(TP2);
      while (mine) {
        const int t = __builtin_ctz(mine); mine &= mine - 1;
        u4 yv[14];
#pragma unroll
        for (int i = 0; i < 14; ++i) yv[i] = LD128(WS_YP + (((i * 4 + g4) * 8 + t) * D + (nb >> 1) * 128 + (r15 >> 3) * 64 + (nb & 1) * 32 + (r15 & 7) * 4) * 4);
        f4 y = __builtin_bit_cast(f4, yv[0]);
#pragma unroll
        for (int i = 1; i < 14; ++i) y += __builtin_bit_cast(f4, yv[i]);
        const float gt = __int_as_float(__builtin_amdgcn_readlane(__float_as_int(sg), t));
#pragma unroll
        for (int e = 0; e < 4; ++e) {
          auto r = __builtin_amdgcn_permlane32_swap(__float_as_uint(y[e]), __float_as_uint(y[e]), false, false);
          const float a = __uint_as_float(r[0]) + __uint_as_float(r[1]);
          auto r2 = __builtin_amdgcn_permlane16_swap(__float_as_uint(a), __float_as_uint(a), false, false);
          y[e] = rbf(rbf(__uint_as_float(r2[0]) + __uint_as_float(r2[1])) * gt);
        }
        if (l < 16) *(LAS f4*)(lds + L_KVX + t * 256 + (l & 7) * 32 + (l >> 3) * 16) = y;
      }
    }
    lsig(myFL, 3u);
    STMP(9);
    STFLUSH();
    if (spare) asm volatile(".Lend_s:"); else asm volatile(".Lend_h:");
    };
    if (sp) helper(BC<true>()); else helper(BC<false>());
    return;
  }

  const int wr = w >> 1, wc = w & 1;
  unsigned tag; u2 kg; u4 sg4;
  const int srow = mb * 128 + hp * 8 + 2 * w + (l >> 5);
  {
    const int row = srow;
    u4 xa[4], rw[4];
#pragma unroll
    for (int c = 0; c < 4; ++c) xa[c] = *(const u4*)(x + row * D + (l & 31) * 8 + 256 * c);
#pragma unroll
    for (int c = 0; c < 4; ++c) rw[c] = *(const u4*)(wrt + (l & 31) * 8 + 256 * c);
    float s0 = 0, s1 = 0;
#pragma unroll
    for (int c = 0; c < 4; ++c)
#pragma unroll
      for (int e = 0; e < 4; e += 2) { unsigned x0 = xa[c][e], x1 = xa[c][e + 1]; s0 = dot2(x0, 0x3F803F80u, s0); s1 = dot2(x1, 0x3F803F80u, s1); }
    const float m = half_sum(s0 + s1) * (1.f / D);
    f2 v[16], q2 = (f2)(0.f), q3 = (f2)(0.f);
#pragma unroll
    for (int c = 0; c < 16; ++c) v[c] = (f2){lo(xa[c >> 2][c & 3]), hi(xa[c >> 2][c & 3])} - m;
#pragma unroll
    for (int c = 0; c < 16; c += 2) { q2 += v[c] * v[c]; q3 += v[c + 1] * v[c + 1]; }
    q2 += q3;
    const float mr = rbf(m);
    const float rs = 1.f / rbf(sqrtf(rbf(rbf(half_sum(q2[0] + q2[1]) * (1.f / D)) + 1e-5f)));
    float d0 = 0, d1 = 0;
#pragma unroll
    for (int c = 0; c < 16; c += 2) {
      const f2 e0 = (f2){lo(xa[c >> 2][c & 3]), hi(xa[c >> 2][c & 3])} - mr, e1 = (f2){lo(xa[c >> 2][(c & 3) + 1]), hi(xa[c >> 2][(c & 3) + 1])} - mr;
      const unsigned p0 = pk2(e0[0], e0[1]), p1 = pk2(e1[0], e1[1]);
      const f2 n0 = (f2){lo(p0), hi(p0)} * rs, n1 = (f2){lo(p1), hi(p1)} * rs;
      unsigned c0 = rw[c >> 2][c & 3], c1 = rw[c >> 2][(c & 3) + 1];
      d0 = dot2(pk2(n0[0], n0[1]), c0, d0); d1 = dot2(pk2(n1[0], n1[1]), c1, d1);
    }
    const float d = half_sum(d0 + d1);
    const unsigned rb = pk2(d, d) & 0xFFFFu;
    const unsigned mono = (rb & 0x8000u) ? (~rb & 0xFFFFu) : (rb | 0x8000u);
    tag = __builtin_amdgcn_readfirstlane(ev0) + 1u;
    kg = (u2){(mono << 16) | (unsigned)row, tag};
    sg4 = (u4){__float_as_uint(mr), tag, __float_as_uint(rs), tag};
  }
  MKWS(tag);
  if ((l & 31) == 0) { ST64(kg, WS_RG + srow * 8); ST128(sg4, WS_ST + srow * 16); }
  unsigned aad[2], bad[2];
#pragma unroll
  for (int s = 0; s < 2; ++s) aad[s] = ldsb + (wr * 64 + r15) * 128 + (((4 * s + g4) ^ ((r15 >> 1) & 7)) * 16);
#pragma unroll
  for (int nh = 0; nh < 2; ++nh) bad[nh] = ldsb + 16384 + (8 * g4 + q) * 128 + (((wc * 4 + 2 * nh + (p >> 1)) ^ (((q >> 1) << 2) | ((g4 & 1) << 1))) * 16) + (p & 1) * 8;
  f4 acc[4][6];
#pragma unroll
  for (int m = 0; m < 4; ++m)
#pragma unroll
    for (int n = 0; n < 6; ++n) { acc[m][n] = (f4)(0.f); asm volatile("" : "+v"(acc[m][n])); }
#define MF(m, n, Fq) do { if (n < 2) acc[m][n] = MFMA16(CAT(Fq.b[2 * n], Fq.b[2 * n + 1]), Fq.a[m], acc[m][n]); \
     else acc[m][n] = MFMA16(Fq.a[m], CAT(Fq.b[2 * n], Fq.b[2 * n + 1]), acc[m][n]); SBAR(); } while (0)
#define RB(G, n, vb0, vb1, SS, hf) RDTR(G.b[2 * (n) + (hf)], (((n) & 1) ? vb1 : vb0), ((n) >> 1) * 8192 + (SS) * 4096 + (hf) * 512)
#define RA(G, m, va) RD128(G.a[m], va, (m) * 2048)
#define SUB(Fq, G, va, vb0, vb1, SS, PRE) do { \
    WAITL(13); SBAR(); \
    MF(0, 0, Fq); PRE; RA(G, 0, va); SBAR(); \
    WAITL(11); SBAR(); \
    MF(1, 0, Fq); RB(G, 0, vb0, vb1, SS, 0); SBAR(); MF(2, 0, Fq); RB(G, 0, vb0, vb1, SS, 1); SBAR(); MF(3, 0, Fq); RA(G, 1, va); SBAR(); \
    WAITL(12); SBAR(); \
    MF(0, 1, Fq); RA(G, 2, va); SBAR(); MF(1, 1, Fq); RA(G, 3, va); SBAR(); MF(2, 1, Fq); RB(G, 1, vb0, vb1, SS, 0); SBAR(); MF(3, 1, Fq); RB(G, 1, vb0, vb1, SS, 1); SBAR(); \
    WAITL(8); SBAR(); \
    MF(0, 2, Fq); RB(G, 2, vb0, vb1, SS, 0); SBAR(); MF(1, 2, Fq); RB(G, 2, vb0, vb1, SS, 1); SBAR(); MF(2, 2, Fq); RB(G, 3, vb0, vb1, SS, 0); SBAR(); MF(3, 2, Fq); RB(G, 3, vb0, vb1, SS, 1); SBAR(); \
    MF(0, 3, Fq); RB(G, 4, vb0, vb1, SS, 0); SBAR(); MF(1, 3, Fq); RB(G, 4, vb0, vb1, SS, 1); SBAR(); MF(2, 3, Fq); RB(G, 5, vb0, vb1, SS, 0); SBAR(); MF(3, 3, Fq); RB(G, 5, vb0, vb1, SS, 1); SBAR(); \
    MF(0, 4, Fq); MF(1, 4, Fq); MF(2, 4, Fq); MF(3, 4, Fq); MF(0, 5, Fq); MF(1, 5, Fq); MF(2, 5, Fq); MF(3, 5, Fq); } while (0)
#define KSTEP(MODE) do { \
    if (kt == sx) { const unsigned xo = xad + buf * STG; \
      _Pragma("unroll") for (int t = 0; t < 8; ++t) { const unsigned a_ = xo ^ (t * 16); \
        asm volatile("ds_read_b64 %0, %1" : "+v"(xv[t]) : "v"(a_) : "memory"); } \
      WAITL(0); SBAR(); } \
    const int nbuf = (buf + 1) & 3; \
    const unsigned bo = buf * STG, nbo = nbuf * STG; \
    { unsigned va = aad[1] + bo, vb0 = bad[0] + bo, vb1 = bad[1] + bo; SUB(f0, f1, va, vb0, vb1, 1, ); } \
    if (MODE < 2) { \
      unsigned va = aad[0] + nbo, vb0 = bad[0] + nbo, vb1 = bad[1] + nbo; \
      SUB(f1, f0, va, vb0, vb1, 0, if (MODE == 0) { WAITL(0); BAR(); } else asm volatile("ds_write_b32 %0, %1" ::"v"(myFL), "v"(1u) : "memory"); SBAR()); \
    } else { asm volatile("ds_write_b32 %1, %2\n ds_read_b32 %0, %3\n s_waitcnt lgkmcnt(2)" : "=&v"(fl0) : "v"(myFL), "v"(2u), "v"(adFL) : "memory"); SBAR(); \
      MF(0, 2, f1); MF(1, 2, f1); MF(2, 2, f1); MF(3, 2, f1); MF(0, 3, f1); MF(1, 3, f1); MF(2, 3, f1); MF(3, 3, f1); \
      MF(0, 4, f1); MF(1, 4, f1); MF(2, 4, f1); MF(3, 4, f1); MF(0, 5, f1); MF(1, 5, f1); MF(2, 5, f1); MF(3, 5, f1); \
      MF(0, 0, f1); MF(1, 0, f1); MF(2, 0, f1); MF(3, 0, f1); MF(0, 1, f1); MF(1, 1, f1); MF(2, 1, f1); MF(3, 1, f1); } \
    buf = nbuf; } while (0)
  u2 xv[8];
#pragma unroll
  for (int t = 0; t < 8; ++t) asm volatile("" : "=v"(xv[t]));
  const int sx = (nb - rot) & 15;
  const unsigned xad = ldsb + (w * 32 + (l & 31)) * 128 + ((r15 >> 1) & 7) * 16 + (l >> 5) * 8;
  STMP(1);
  WAITL(0);
  BAR();
  STMP(2);
  WAITL(0);
  FQ f0, f1;
  { unsigned va = aad[0], vb0 = bad[0], vb1 = bad[1];
    RA(f0, 0, va); RB(f0, 0, vb0, vb1, 0, 0); RB(f0, 0, vb0, vb1, 0, 1); RA(f0, 1, va); RA(f0, 2, va); RA(f0, 3, va);
    RB(f0, 1, vb0, vb1, 0, 0); RB(f0, 1, vb0, vb1, 0, 1); RB(f0, 2, vb0, vb1, 0, 0); RB(f0, 2, vb0, vb1, 0, 1); RB(f0, 3, vb0, vb1, 0, 0); RB(f0, 3, vb0, vb1, 0, 1);
    RB(f0, 4, vb0, vb1, 0, 0); RB(f0, 4, vb0, vb1, 0, 1); RB(f0, 5, vb0, vb1, 0, 0); RB(f0, 5, vb0, vb1, 0, 1);
    WAITL(0); SBAR(); }
  int buf = 0; unsigned fl0;
#pragma unroll 1
  for (int kt = 0; kt < 14; ++kt) KSTEP(0);
  { const int kt = 14; KSTEP(1); }
  { const int kt = 15; KSTEP(2); }
  asm volatile("s_waitcnt lgkmcnt(0)" : "+v"(fl0) : : "memory");
  if (__builtin_expect(((unsigned)__ballot(fl0 < 1u) & 0xC0u) != 0u, 0)) { RETRY2(); lwait(adFL, 0xC0u, 1); }
  STMP(3);
  __builtin_amdgcn_s_setprio(2);
  const LAS float* Lm = (const LAS float*)(lds + L_M) + wr * 64;
  const LAS float* Lr = (const LAS float*)(lds + L_RS) + wr * 64;
  const LAS float* Lc = (const LAS float*)(lds + L_CS) + wc * 96;
  f4 kv[4], zt[2];
  {
    float cs[4];
#pragma unroll
    for (int n = 0; n < 4; ++n) cs[n] = Lc[32 + 16 * n + r15];
    const u4 ones = (u4)(0x3F803F80u);
#pragma unroll
    for (int t = 0; t < 2; ++t) {
      u4 kf[2], vf[2];
#pragma unroll
      for (int mm = 0; mm < 2; ++mm) {
        const int m = 2 * t + mm;
        const f4 mv = *(const LAS f4*)(Lm + m * 16 + 4 * g4), rv = *(const LAS f4*)(Lr + m * 16 + 4 * g4);
#pragma unroll
        for (int a = 0; a < 2; ++a) {
          const f4 uk = acc[m][2 + a] - mv * cs[a];
          const f4 uv = (acc[m][4 + a] - mv * cs[2 + a]) * rv;
          kf[a][2 * mm] = phi2((f2){uk[0], uk[1]}, (f2){rv[0], rv[1]}); kf[a][2 * mm + 1] = phi2((f2){uk[2], uk[3]}, (f2){rv[2], rv[3]});
          vf[a][2 * mm] = pk2(uv[0], uv[1]); vf[a][2 * mm + 1] = pk2(uv[2], uv[3]);
        }
      }
#pragma unroll
      for (int tt = 0; tt < 4; ++tt) kv[tt] = MFMA16(S8(kf[tt & 1]), S8(vf[tt >> 1]), t ? kv[tt] : (f4)(0.f));
#pragma unroll
      for (int a = 0; a < 2; ++a) zt[a] = MFMA16(S8(kf[a]), S8(ones), t ? zt[a] : (f4)(0.f));
    }
  }
  const int hd = hp * 2 + wc;
  {
    LAS f4* ks = (LAS f4*)(lds + L_KVX + w * 3072) + l;
    const LAS f4* kr = (const LAS f4*)(lds + L_KVX + (w ^ 2) * 3072) + l;
    ks[0] = wr ? kv[0] : kv[2]; ks[64] = wr ? kv[1] : kv[3]; ks[128] = wr ? zt[0] : zt[1];
    lsig(myFL, 3);
    const f4 m0 = wr ? kv[2] : kv[0], m1 = wr ? kv[3] : kv[1], mz = wr ? zt[1] : zt[0];
    lwait(adFL, 1u << (w ^ 2), 3);
    const f4 s0 = m0 + kr[0], s1 = m1 + kr[64], sz = mz + kr[128];
    u4 o; o[0] = min(pk2(s0[0], s0[1]), POIS - 1u); o[1] = pk2(s0[2], s0[3]); o[2] = pk2(s1[0], s1[1]); o[3] = pk2(s1[2], s1[3]);
    ST128(o, WS_KVP + (hd * 16 + mb) * 2048 + wr * 1024 + l * 16);
    u4 zo = __builtin_bit_cast(u4, sz); zo[0] = min(zo[0], POIS - 1u);
    if (r15 == 0) ST128(zo, WS_ZP + (hd * 16 + mb) * 128 + g4 * 32 + wr * 16);
  }
  STMP(4);
  {
    const f4 c0 = *(const LAS f4*)(Lc + 4 * g4), c1 = *(const LAS f4*)(Lc + 16 + 4 * g4);
#pragma unroll
    for (int m = 0; m < 4; ++m) {
      const float mv = Lm[m * 16 + r15], rv = Lr[m * 16 + r15];
      const f4 u0 = acc[m][0] - c0 * mv, u1 = acc[m][1] - c1 * mv;
      u4 pf;
      pf[0] = phi2((f2){u0[0], u0[1]}, (f2)(rv)); pf[1] = phi2((f2){u0[2], u0[3]}, (f2)(rv));
      pf[2] = phi2((f2){u1[0], u1[1]}, (f2)(rv)); pf[3] = phi2((f2){u1[2], u1[3]}, (f2)(rv));
      *(LAS u4*)(lds + L_PQ + wr * 8192 + wc * 4096 + m * 1024 + l * 16) = pf;
    }
  }
  lsig(myFL, 4);
  __builtin_amdgcn_s_setprio(0);
  STMP(5);
  const int trow = mb * 128 + w * 32 + (l & 31);
  u2 oh;
  const unsigned flo = (2 * PAIRAT(r15) + wc) * 128;
  const char* fb = uptr(wsx + WS_OH + mb * 4096);
  asm volatile("global_load_dwordx2 %0, %1, %2 sc1" : "=v"(oh) : "v"(flo), "s"(fb) : "memory");
  int sel = -1;
  {
    const u4 s0 = *(const LAS u4*)(lds + L_SEL), s1 = *(const LAS u4*)(lds + L_SEL + 16);
#pragma unroll
    for (int i = 0; i < 8; ++i) { const int v = (int)(i < 4 ? s0[i & 3] : s1[i & 3]); sel = v == trow ? i : sel; }
  }
  const bool anysel = __ballot(sel >= 0) != 0ull;
  f16v oc[2][2];
  {
    const float m = *(const LAS float*)(lds + L_M + (w * 32 + (l & 31)) * 4), rs = *(const LAS float*)(lds + L_RS + (w * 32 + (l & 31)) * 4);
#pragma unroll
    for (int t = 0; t < 8; ++t) {
      const f4 xf = (f4){lo(xv[t][0]), hi(xv[t][0]), lo(xv[t][1]), hi(xv[t][1])};
      const f4 y = (xf - m) * rs + xf;
#pragma unroll
      for (int i = 0; i < 4; ++i) oc[0][t >> 2][(t & 3) * 4 + i] = y[i];
    }
    asm volatile("" : "+v"(oc[0][0]), "+v"(oc[0][1]));
  }
  unsigned wa[2];
#pragma unroll
  for (int t = 0; t < 2; ++t) wa[t] = ldsb + L_WO + wc * 4096 + (4 * (g4 >> 1) + q) * 128 + (((4 * t + 2 * (g4 & 1) + (p >> 1)) ^ ((q >> 1) << 2)) * 16) + (p & 1) * 8;
  u4 a[RD][4];
#pragma unroll
  for (int k = 0; k < RD; ++k) asm volatile("" : "=v"(a[k][0]), "=v"(a[k][1]), "=v"(a[k][2]), "=v"(a[k][3]));
  const char* ob = uptr(wsx + WS_OF + mb * 262144 + wc * 8192 + wr * 4096);
  const unsigned ov0 = l * 16 + wc * 2048, ov1 = ov0 ^ 2048;
  STMP(6);
  lwait<1>(adFL, 0x30u, 1);
  STMP(7);
  WAITV(0);
  asm volatile("" : "+v"(oh));
  unsigned seen = (unsigned)__ballot(oh[0] == tag), go = 0;
  auto need = [&](unsigned mask, bool first) {
    if (__builtin_expect((go & mask) != mask, 0)) {
      _Pragma("unroll 1") for (unsigned it = 0; it < SPIN; ++it) {
        bool poll = (seen & mask) != mask;
        if (first && seen != ~0u && !poll) poll = ((unsigned)__ballot(oh[1] - NOW() - 80u < 1000u) & mask) != 0u;
        if (poll) {
          if (first) SLEEP(4);
          asm volatile("global_load_dwordx2 %0, %1, %2 sc1\n s_waitcnt vmcnt(0)" : "+v"(oh) : "v"(flo), "s"(fb) : "memory");
          seen = (unsigned)__ballot(oh[0] == tag);
        }
        go = seen & (unsigned)__ballot(oh[1] - NOW() - 1u >= 1000u);
        if ((go & mask) == mask) break;
        SLEEP(1);
      }
    }
  };
#define OX 0
#define OL1(pos, i) do { if (OX == 2 && (pos) >= RD) break; \
    asm volatile("global_load_dwordx4 %0, %1, %2 offset:%3 sc1" : "+v"(a[(pos) % RD][i]) : "v"((i) & 1 ? ov1 : ov0), "s"(ob + (OX == 1 ? ((pos) & 1) : PAIRAT(pos)) * 16384), "i"(((i) >> 1) * 1024) : "memory"); } while (0)
#define OLD(pos) do { OL1(pos, 0); OL1(pos, 1); OL1(pos, 2); OL1(pos, 3); } while (0)
  auto omax = [&](u4(&v)[4]) {
    asm volatile("" : "+v"(v[0]), "+v"(v[1]), "+v"(v[2]), "+v"(v[3]));
    return max(umax3(v[0][0], v[1][0], v[2][0]), v[3][0]);
  };
#define OFIX(pos, m) do { \
    if (__builtin_expect(__ballot((m) == POIS) != 0ull, 0)) { RETRY(); _Pragma("unroll 1") for (unsigned it = 0; it < SPIN; ++it) { SLEEP(4); OLD(pos); WAITV(0); if (__ballot(omax(a[(pos) % RD]) == POIS) == 0ull) break; } } } while (0)
#define OS1(i)
#define OS2(i)
  need(1u, true);
  if (OWN) lwait<1>(adFL, 0x30u, 2);
  __builtin_amdgcn_s_setprio(2);
  STMP(15);
  OS1(0); OLD(0); STMP(14);
#pragma unroll
  for (int i = 1; i < RD; ++i) { need(1u << i, false); OS1(i); OLD(i); }
  STMP(10);
  s4 b[8];
#define ORD(pos, f) do { if ((f) < 2 && (pos) == 8) { wa[(f) & 1] += 65536; asm volatile("" : "+v"(wa[(f) & 1])); } \
    RDTR(b[2 * (f)], wa[(f) & 1], ((pos) & 7) * 8192 + ((f) >> 1) * 1024); RDTR(b[2 * (f) + 1], wa[(f) & 1], ((pos) & 7) * 8192 + ((f) >> 1) * 1024 + 2048); } while (0)
#define OMF(pos, f, r) do { if ((f) < 2 && (pos) == 0 && (r) == 1) asm volatile("v_mfma_f32_32x32x16_bf16 %0, %1, %2, 0" : "=&v"(oc[r][(f) & 1]) \
      : "v"(CAT(b[2 * (f)], b[2 * (f) + 1])), "v"(a[(pos) % RD][((f) & 2) + (r)])); \
    else asm volatile("v_mfma_f32_32x32x16_bf16 %0, %1, %2, %0" : "+v"(oc[r][(f) & 1]) \
      : "v"(CAT(b[2 * (f)], b[2 * (f) + 1])), "v"(a[(pos) % RD][((f) & 2) + (r)])); \
    SBAR(); if ((r) == 1 && (pos) < 15) ORD(((pos) + 1) & 15, f); SBAR(); } while (0)
#define ONX(pos) ((pos) > 0 && (pos) + RD - 1 < 16)
#define ONL(pos, i) do { if (ONX(pos)) OL1(((pos) + RD - 1) & 15, i); SBAR(); } while (0)
#define OWL(pos, f) do { WAITL((pos) < 15 ? 6 : 6 - 2 * (f)); SBAR(); } while (0)
#define OHEAD(pos) do { unsigned m_ = 0; \
    OWL(pos, 0); OMF(pos, 0, 0); if (ONX(pos)) { need(1u << (((pos) + RD - 1) & 15), false); OS1(((pos) + RD - 1) & 15); } SBAR(); \
    OMF(pos, 0, 1); ONL(pos, 0); \
    OWL(pos, 1); OMF(pos, 1, 0); ONL(pos, 1); \
    OMF(pos, 1, 1); ONL(pos, 2); \
    OWL(pos, 2); OMF(pos, 2, 0); ONL(pos, 3); \
    if ((pos) == 4) { asm volatile("ds_write_b32 %0, %1" ::"v"(myFL), "v"(5u) : "memory"); SBAR(); } \
    if ((pos) == 13) { asm volatile("ds_read_b32 %0, %1" : "=v"(fl3) : "v"(adFL) : "memory"); SBAR(); } \
    OMF(pos, 2, 1); \
    OWL(pos, 3); OMF(pos, 3, 0); \
    if ((pos) < 15) { WAITV(4 * ((pos) + RD - 1 < 16 ? RD - 2 : 14 - (pos))); m_ = omax(a[((pos) + 1) % RD]); asm volatile("" : "+v"(m_)); SBAR(); } \
    OMF(pos, 3, 1); \
    if ((pos) < 15) { OFIX(((pos) + 1) & 15, m_); OS2(((pos) + 1) & 15); } SBAR(); } while (0)
  unsigned fl3;
  ORD(0, 0); ORD(0, 1); ORD(0, 2); ORD(0, 3);
  { WAITV(4 * (RD - 1)); const unsigned m_ = omax(a[0]); OFIX(0, m_); OS2(0); SBAR(); }
  OHEAD(0); OHEAD(1); STMP(11); OHEAD(2); OHEAD(3); OHEAD(4); OHEAD(5); OHEAD(6); OHEAD(7); STMP(12);
  OHEAD(8); OHEAD(9); STMP(13); OHEAD(10); OHEAD(11); OHEAD(12); OHEAD(13); OHEAD(14);
  f4 xi[8];
  {
    asm volatile("" : "+v"(fl3));
    if (__builtin_expect(((unsigned)__ballot(fl3 < 5u) >> (w ^ 2)) & 1u, 0)) lwait(adFL, 1u << (w ^ 2), 5);
    const unsigned xw = ldsb + L_WO + (4 * wr + wc) * 4096 + l * 16, xr = xw ^ 4096;
#define XW(t) asm volatile("ds_write_b128 %0, %1 offset:%2" ::"v"(xw), "v"(__builtin_shufflevector(oc[1][(t) >> 2], oc[1][(t) >> 2], ((t) & 3) * 4, ((t) & 3) * 4 + 1, ((t) & 3) * 4 + 2, ((t) & 3) * 4 + 3)), \
      "i"(((t) >> 2) * 8192 + ((t) & 3) * 1024) : "memory")
    SBAR();
    OWL(15, 0); OMF(15, 0, 1); OWL(15, 1); OMF(15, 1, 1); OWL(15, 2); OMF(15, 2, 1); OWL(15, 3); OMF(15, 3, 1);
    OMF(15, 0, 0); OMF(15, 1, 0); XW(0); XW(1); XW(2); XW(3); SBAR();
    OMF(15, 2, 0); XW(4); XW(5); XW(6); XW(7); SBAR();
    OMF(15, 3, 0);
    asm volatile("ds_write_b32 %0, %1" ::"v"(myFL), "v"(6u) : "memory");
    _Pragma("unroll 1") for (unsigned it = 0; it < SPIN; ++it) {
      unsigned v;
      asm volatile("ds_read_b32 %0, %1" : "=v"(v) : "v"(adFL) : "memory");
#pragma unroll
      for (int t = 0; t < 8; ++t) RD128(xi[t], xr, (t >> 2) * 8192 + (t & 3) * 1024);
      WAITL(0);
      asm volatile("" : "+v"(v), "+v"(xi[0]), "+v"(xi[1]), "+v"(xi[2]), "+v"(xi[3]), "+v"(xi[4]), "+v"(xi[5]), "+v"(xi[6]), "+v"(xi[7]));
      if ((((unsigned)__ballot(v < 6u) >> (w ^ 1)) & 1u) == 0u) break;
    }
    asm volatile("" : "+v"(oc[0][0]), "+v"(oc[0][1]));
#pragma unroll
    for (int t = 0; t < 8; ++t)
#pragma unroll
      for (int i = 0; i < 4; ++i) xi[t][i] += oc[0][t >> 2][(t & 3) * 4 + i];
  }
  STMP(8);
  if (anysel) {
    lwait(adFL, 0xC0u, 3);
    if (sel >= 0) {
#pragma unroll
      for (int t = 0; t < 8; ++t) xi[t] += *(const LAS f4*)(lds + L_KVX + sel * 256 + (8 * t + 4 * (l >> 5)) * 4);
    }
  }
  const __amdgpu_buffer_rsrc_t outr = __builtin_amdgcn_make_buffer_rsrc(out, 0, 2048 * D * 2, 0x00020000);
  if (FULL) {
    LAS char* tb = (LAS char*)lds + L_PQ + w * 4096;
#pragma unroll
    for (int t = 0; t < 8; ++t) *(LAS u2*)(tb + (l & 31) * 128 + ((t ^ (l & 7)) * 16) + (l >> 5) * 8) = (u2){pk2(xi[t][0], xi[t][1]), pk2(xi[t][2], xi[t][3])};
#pragma unroll
    for (int i = 0; i < 4; ++i) {
      const u4 v = *(const LAS u4*)(tb + i * 1024 + (l >> 3) * 128 + (((l ^ (l >> 3)) & 7) * 16));
      __builtin_amdgcn_raw_buffer_store_b128(v, outr, (mb * 128 + w * 32 + i * 8 + (l >> 3)) * 2048 + nb * 128 + (l & 7) * 16, 0, 16);
    }
  } else {
#pragma unroll
  for (int hh = 0; hh < 4; ++hh) {
    const f4 ta = xi[2 * hh], tb = xi[2 * hh + 1];
    auto r0 = __builtin_amdgcn_permlane32_swap(pk2(ta[0], ta[1]), pk2(tb[0], tb[1]), false, false);
    auto r1 = __builtin_amdgcn_permlane32_swap(pk2(ta[2], ta[3]), pk2(tb[2], tb[3]), false, false);
    __builtin_amdgcn_raw_buffer_store_b128(((u4){r0[0], r1[0], r0[1], r1[1]}), outr, (trow * D + nb * 64 + 16 * hh + 8 * (l >> 5)) * 2, 0, 16);
  }
  }
  if (g == 0 && w == 0 && l == 0) __builtin_amdgcn_raw_buffer_store_b32(tag, wse, WS_E, 0, 16);
  STMP(9);
  STFLUSH();
  asm volatile(".Lend_c:");
  return;
  }
  {
    const int lw = w - 4;
    unsigned ev;
    asm volatile("global_load_dword %0, %1, %2 sc1" : "=v"(ev) : "v"(0u), "s"(ws) : "memory");
    unsigned vf[20], vfw[4];
#pragma unroll
    for (int i = 0; i < 8; ++i) { const int row = 8 * (lw * 8 + i) + (l >> 3); vf[i] = row * 2048 + (((l & 7) ^ ((row >> 1) & 7)) * 16) + (3 - (i & 3)) * 1024; }
#pragma unroll
    for (int i = 0; i < 12; ++i) {
      const int pc = lw * 12 + i, row = (pc & 7) * 8 + (l >> 3);
      vf[8 + i] = row * 6144 + (pc >> 3) * 2048 + (((l & 7) ^ ((((row >> 1) & 1) << 2) | (((row >> 3) & 1) << 1))) * 16) + (3 - (i & 3)) * 1024;
    }
#pragma unroll
    for (int i = 0; i < 4; ++i) vfw[i] = (8 * i + (l >> 3)) * 2048 + (((l & 7) ^ (((l >> 4) & 1) << 2)) * 16) + (3 - i) * 1024;
    const char* Ab = uptr((const char*)x + (size_t)mb * 128 * 2048) - 3072;
    const char* Bb = uptr((const char*)wqkv + hp * 128) - 3072;
    const unsigned mba = ldsb + lw * 8192, mbb = ldsb + 16384 + lw * 12288;
#define GL4(v, sb, mm, MO) do { asm volatile("s_add_i32 m0, %2, %3\n s_nop 0\n global_load_lds_dwordx4 %0, %1" ::"v"((v)[0]), "s"(sb), "s"(mm), "i"(MO) : "memory", "scc"); \
      GLI((v)[1], sb, 1024); GLI((v)[2], sb, 2048); GLI((v)[3], sb, 3072); } while (0)
    auto issue = [&](int s, int u0, int u1) {
      const int kt = (s + rot) & 15;
      const char* sa = Ab + kt * 128; const char* sb = Bb + kt * (64 * 6144);
      const unsigned so = (s & 3) * STG, ma = mba + so, mq = mbb + so;
#pragma unroll
      for (int u = u0; u < u1; ++u) { if (u < 2) GL4(vf + 4 * u, sa, ma, u * 4096); else GL4(vf + 4 * u, sb, mq, (u - 2) * 4096); }
    };
    int tk = 0;
#define LBAR() do { BST(tk + 1); BAR(); BST(tk + 21); ++tk; } while (0)
    issue(0, 0, 5); issue(1, 0, 5);
    BST(60);
    WAITV(20); BST(0); BAR(); BST(20);
    asm volatile("" : "+v"(ev));
    const unsigned tag = __builtin_amdgcn_readfirstlane(ev) + 1u;
    MKWS(tag);
    issue(2, 0, 5);
    int s0 = 3;
#pragma unroll 1
    for (; s0 < (sp ? 16 : 6 + ph); ++s0) { issue(s0, 0, 5); BST(tk + 40); WAITV(40); LBAR(); }
#pragma unroll 1
    for (; s0 < 16; s0 += 5) {
      WAITV(20); LBAR();
      WAITV(0); LBAR();
      issue(s0, 0, 5); issue(s0 + 1, 0, U1); BST(tk + 40); WAITV(4 * U1); LBAR();
      issue(s0 + 1, U1, 5); issue(s0 + 2, 0, 5);
      if (s0 + 3 >= 16) break;
      issue(s0 + 3, 0, U2); BST(tk + 40); WAITV(20 + 4 * U2); LBAR();
      issue(s0 + 3, U2, 5);
      if (s0 + 4 >= 16) break;
      issue(s0 + 4, 0, 5); BST(tk + 40); WAITV(40); LBAR();
    }
    int wq = lw;
    auto wo_upto = [&](int lim) {
      _Pragma("unroll 1") for (; wq < lim; wq += 2) { const char* wp = wob - 3072 + HEADAT(wq) * 65536; const unsigned wm = ldsb + wq * 4096; GL4(vfw, wp, wm, L_WO); }
    };
    asm volatile("ds_write_b32 %0, %1" ::"v"(adFL), "v"(0u) : "memory");
    WAITV(0); WAITL(0); LBAR();
    if (lw == 0) ST64(((u2){tag, NOW() + KD}), l == 0 ? WS_HK + (hp * 16 + mb) * 128 : WS_SZ);
    wo_upto(12);
    BST(15); lwait<1>(adFL, 0x0Fu, 1); BST(35);
    wo_upto(22);
    BST(16); lwait<1>(adFL, 0x0Fu, 2); BST(36);
    wo_upto(32);
    STMP(1);
    const int hd = hp * 2 + lw;
    u4 sel[2];
#pragma unroll
    for (int e = 0; e < 2; ++e) {
      const unsigned v = (r15 >> 2) == g4 ? (((r15 & 3) == 2 * e ? 0x3F80u : 0u) | ((r15 & 3) == 2 * e + 1 ? 0x3F800000u : 0u)) : 0u;
      sel[0][e] = v; sel[0][2 + e] = 0u; sel[1][e] = 0u; sel[1][2 + e] = v;
    }
    u4 pv[16][2], zv0, zv1, pq[8];
    const char* kvb = uptr(wsx + WS_KVP + (size_t)hd * 32768);
    const char* zb = uptr(wsx + WS_ZP + (size_t)hd * 2048);
    const char* hb = uptr(wsx + WS_HK + hp * 2048);
    const unsigned lv16 = l * 16, zoff = r15 * 128 + g4 * 32;
    const unsigned pa = ldsb + L_PQ + lw * 4096 + l * 16, omask = lw ? 0x0Au : 0x05u;
    u2 hk;
    _Pragma("unroll 1") for (unsigned it = 0; it < SPIN; ++it) {
      asm volatile("global_load_dwordx2 %0, %1, %2 sc1\n s_waitcnt vmcnt(0)" : "=v"(hk) : "v"(r15 * 128), "s"(hb) : "memory");
      if (it == 0) lsig(myFL, 1);
      if (__ballot(hk[0] != tag) == 0ull) break;
      SLEEP(2);
    }
    __builtin_amdgcn_s_setprio(3);
    STMP(2);
    const unsigned t0 = NOW();
    unsigned rel = hk[1] - t0; rel = rel < 1000u ? rel : 0u;
    rel = max(rel, (unsigned)__builtin_amdgcn_update_dpp(0, (int)rel, 0xB1, 0xf, 0xf, false));
    rel = max(rel, (unsigned)__builtin_amdgcn_update_dpp(0, (int)rel, 0x4E, 0xf, 0xf, false));
    int gx;
    { const unsigned r0 = __builtin_amdgcn_readlane(rel, 0), r1 = __builtin_amdgcn_readlane(rel, 4), r2 = __builtin_amdgcn_readlane(rel, 8), r3 = __builtin_amdgcn_readlane(rel, 12);
      gx = max(r0, r1) > max(r2, r3) ? (r0 > r1 ? 3 : 2) : (r2 > r3 ? 1 : 0);
      if (OHP) ST64(((u2){tag, t0 + max(max(r0, r1), max(r2, r3)) + OD}), l == 0 ? WS_OH + (mb * 32 + hd) * 128 : WS_SZ); }
#define GLD(gq) do { _Pragma("unroll") for (int i = 0; i < 4; ++i) { const char* sb = kvb + (((gq) ^ gx) * 4 + (i & 2)) * 2048; \
      asm volatile("global_load_dwordx4 %0, %1, %2 offset:%3 sc1" : "=v"(pv[(gq) * 4 + i][0]) : "v"(lv16), "s"(sb), "i"((i & 1) * 2048) : "memory"); \
      asm volatile("global_load_dwordx4 %0, %1, %2 offset:%3 sc1" : "=v"(pv[(gq) * 4 + i][1]) : "v"(lv16), "s"(sb), "i"((i & 1) * 2048 + 1024) : "memory"); } } while (0)
#define ZLD() do { asm volatile("global_load_dwordx4 %0, %1, %2 sc1" : "=v"(zv0) : "v"(zoff), "s"(zb) : "memory"); \
      asm volatile("global_load_dwordx4 %0, %1, %2 offset:16 sc1" : "=v"(zv1) : "v"(zoff), "s"(zb) : "memory"); } while (0)
#pragma unroll
    for (int gq = 0; gq < 4; ++gq) {
      const unsigned rq = __builtin_amdgcn_readlane(rel, 4 * (gq ^ gx));
      _Pragma("unroll 1") for (unsigned it = 0; it < SPIN; ++it) if (NOW() - t0 >= rq) break;
      if (gq == 3) ZLD();
      GLD(gq);
    }
    if (!OHP) ST64(((u2){tag, NOW() + OD}), l == 0 ? WS_OH + (mb * 32 + hd) * 128 : WS_SZ);
    STMP(5);
    lwait(adFL, omask, 4);
#pragma unroll
    for (int i = 0; i < 8; ++i) RD128(pq[i], pa, (i >> 2) * 8192 + (i & 3) * 1024);
#pragma unroll
    for (int gq = 0; gq < 4; ++gq) {
      SBAR(); WAITV((gq == 0 ? 27 : gq == 1 ? 19 : gq == 2 ? 11 : 1) - OHP);
      _Pragma("unroll 1") for (unsigned it = 0; it < SPIN; ++it) {
#pragma unroll
        for (int i = 0; i < 4; ++i) asm volatile("" : "+v"(pv[gq * 4 + i][0]), "+v"(pv[gq * 4 + i][1]));
        unsigned m = umax3(pv[gq * 4][0][0], pv[gq * 4][1][0], pv[gq * 4 + 1][0][0]);
        m = umax3(m, pv[gq * 4 + 1][1][0], pv[gq * 4 + 2][0][0]);
        m = umax3(m, pv[gq * 4 + 2][1][0], pv[gq * 4 + 3][0][0]);
        m = max(m, pv[gq * 4 + 3][1][0]);
        if (gq == 3) { asm volatile("" : "+v"(zv0), "+v"(zv1)); m = umax3(m, zv0[0], zv1[0]); }
        if (__ballot(m == POIS) == 0ull) break;
        RETRY();
        SLEEP(2);
        if (gq == 3) ZLD();
        GLD(gq);
        WAITV(0);
      }
      f4 ga[4];
#pragma unroll
      for (int i = 0; i < 4; ++i)
#pragma unroll
        for (int t = 0; t < 4; ++t) ga[t] = MFMA16(S8(sel[t & 1]), S8(pv[gq * 4 + i][t >> 1]), i ? ga[t] : (f4)(0.f));
#pragma unroll
      for (int t = 0; t < 4; ++t) pv[gq * 4 + (t >> 1)][t & 1] = __builtin_bit_cast(u4, ga[t]);
    }
    STMP(3);
    WAITL(0);
#pragma unroll
    for (int i = 0; i < 8; i += 2) asm volatile("" : "+v"(pq[i]), "+v"(pq[i + 1]));
    u4 kA[2], zA;
    {
      float z[8];
#pragma unroll
      for (int e = 0; e < 4; ++e) { z[e] = __uint_as_float(zv0[e]); z[4 + e] = __uint_as_float(zv1[e]); }
#define ZR(n) "v_add_f32_dpp %0, %0, %0 row_ror:" #n " row_mask:0xf bank_mask:0xf\n v_add_f32_dpp %1, %1, %1 row_ror:" #n " row_mask:0xf bank_mask:0xf\n" \
              "v_add_f32_dpp %2, %2, %2 row_ror:" #n " row_mask:0xf bank_mask:0xf\n v_add_f32_dpp %3, %3, %3 row_ror:" #n " row_mask:0xf bank_mask:0xf\n" \
              "v_add_f32_dpp %4, %4, %4 row_ror:" #n " row_mask:0xf bank_mask:0xf\n v_add_f32_dpp %5, %5, %5 row_ror:" #n " row_mask:0xf bank_mask:0xf\n" \
              "v_add_f32_dpp %6, %6, %6 row_ror:" #n " row_mask:0xf bank_mask:0xf\n v_add_f32_dpp %7, %7, %7 row_ror:" #n " row_mask:0xf bank_mask:0xf\n"
      asm volatile("s_nop 1\n" ZR(8) ZR(4) ZR(2) ZR(1) : "+v"(z[0]), "+v"(z[1]), "+v"(z[2]), "+v"(z[3]), "+v"(z[4]), "+v"(z[5]), "+v"(z[6]), "+v"(z[7]));
#pragma unroll
      for (int e = 0; e < 4; ++e) zA[e] = pk2(z[2 * e], z[2 * e + 1]);
    }
    float inv[8];
#pragma unroll
    for (int i = 0; i < 8; ++i) { f4 dn = MFMA16(S8(zA), S8(pq[i]), (f4)(0.f)); inv[i] = dn[0]; }
#pragma unroll
    for (int t = 0; t < 4; ++t) {
#define PF(gq) __builtin_bit_cast(f4, pv[(gq) * 4 + (t >> 1)][t & 1])
      f4 s = (PF(0) + PF(1)) + (PF(2) + PF(3));
      kA[t >> 1][(t & 1) * 2] = pk2(s[0], s[1]); kA[t >> 1][(t & 1) * 2 + 1] = pk2(s[2], s[3]);
    }
    f4 o0[8], o1[8];
#pragma unroll
    for (int i = 0; i < 8; ++i) {
      o0[i] = MFMA16(S8(kA[0]), S8(pq[i]), (f4)(0.f)); o1[i] = MFMA16(S8(kA[1]), S8(pq[i]), (f4)(0.f));
      inv[i] = __builtin_amdgcn_rcpf(inv[i] + 1e-6f);
    }
#pragma unroll
    for (int i = 0; i < 8; ++i) {
      o0[i] *= inv[i]; o1[i] *= inv[i];
      u4 o; o[0] = min(pk2(o0[i][0], o0[i][1]), POIS - 1u); o[1] = pk2(o0[i][2], o0[i][3]); o[2] = pk2(o1[i][0], o1[i][1]); o[3] = pk2(o1[i][2], o1[i][3]);
      ST128(o, WS_OF + (mb * 32 + hd) * 8192 + (i >> 1) * 2048 + (i & 1) * 256 + g4 * 512 + r15 * 16);
    }
    lsig(myFL, 2);
    STMP(4);
    STFLUSH();
    asm volatile(".Lend_l:");
  }

}

extern "C" void kernel_launch(void* const* d_in, const int* in_sizes, int n_in, void* d_out, int out_size,
                              void* d_ws, size_t ws_size, hipStream_t stream) {
  (void)hipFuncSetAttribute((const void*)fused, hipFuncAttributeMaxDynamicSharedMemorySize, LDS_SZ);
  fused<<<256, 512, LDS_SZ, stream>>>((const u16*)d_in[0], (const u16*)d_in[1], (const u16*)d_in[2], (const u16*)d_in[4],
                                      (const u16*)d_in[5], (const u16*)d_in[6], (const u16*)d_in[7], (char*)d_ws, (u16*)d_out);
}
```

```cpp
#include <hip/hip_runtime.h>
#include <stdint.h>

typedef unsigned short u16;
typedef short s4 __attribute__((ext_vector_type(4)));
typedef short s8 __attribute__((ext_vector_type(8)));
typedef float f4 __attribute__((ext_vector_type(4)));
typedef float f16v __attribute__((ext_vector_type(16)));
typedef float f2 __attribute__((ext_vector_type(2)));
typedef unsigned u4 __attribute__((ext_vector_type(4)));
typedef unsigned u2 __attribute__((ext_vector_type(2)));
typedef __bf16 bf2 __attribute__((ext_vector_type(2)));
#define LAS __attribute__((address_space(3)))
#define DEV __device__ __forceinline__

constexpr int D = 1024, F = 3584;
#define NOMOE 0
#define KD 305
#define KS 270
#define OD 200
#define OHP 1
#define FS 128
#define GSL 8
#define YSL 16
#define TP1 0
#define TP2 3
#define TLBW 0
#define FULL 1
#define HPRIO 1
#define OWN 1
#define NOPOIS 0
#define CAP 7
#define RD 4
#define DYN 1
#define JX 0
#define NOSKIP 0
#define GRP 8
#define ADV 15
#define HB 1
#define RT 0
#define WD 0
#define WU1 12
#define WU2 22
#define LW1 1
#define HW1 2
#define HS0 0
#define GPV 2
#define XO 1
#define TP0 3
#define G1 0x31
#define G1S 4
#define G2S 0
#define TD 1
#define TL 0
#define MH 0
#define HWT 3
#define YG 0
#define GG 1
#define PV 4
#define U1 4
#define U2 2
#define ROT 0

constexpr unsigned WS_E = 0;
constexpr unsigned WS_YF = 524288;
constexpr unsigned WS_CAND = 12288;
constexpr unsigned WS_GF = 589824;
constexpr unsigned WS_RG = 49152;
constexpr unsigned WS_ST = 65536;
constexpr unsigned WS_HK = 98304;
constexpr unsigned WS_ZP = 131072;
constexpr unsigned WS_CS = 196608;
constexpr unsigned WS_OH = 458752;
constexpr unsigned WS_KVP = 1u << 20;
constexpr unsigned WS_GP = 2u << 20;
constexpr unsigned WS_YP = 3u << 20;
constexpr unsigned WS_OF = 5u << 20;
constexpr unsigned WS_SZ = 9u << 20;
#define NR 4
#define RGO(tag) ((64u << 20) + ((tag) & (NR - 1u)) * (10u << 20))
#define MKWS(tag) char* const wsx = ws + RGO(tag); const __amdgpu_buffer_rsrc_t wsr = __builtin_amdgcn_make_buffer_rsrc(wsx, 0, WS_SZ, 0x00020000)

constexpr int STG = 40960;
constexpr int L_KVX = 0;
constexpr int L_PQ = 12288;
constexpr int L_M = 28672, L_RS = L_M + 512, L_CS = L_RS + 512, L_SEL = L_CS + 768, L_FL = L_SEL + 64;
constexpr int L_HX = 30720, L_WO = 32768;
constexpr int LDS_SZ = 163840;
constexpr unsigned POIS = 0xFFFFFFFFu;

DEV unsigned pk2(float a, float b) { bf2 v; v[0] = (__bf16)a; v[1] = (__bf16)b; return __builtin_bit_cast(unsigned, v); }
DEV float lo(unsigned u) { return __uint_as_float(u << 16); }
DEV float hi(unsigned u) { return __uint_as_float(u & 0xFFFF0000u); }
DEV float rbf(float a) { return lo(pk2(a, a)); }
DEV float dot2(unsigned a, unsigned b, float c) { return __builtin_amdgcn_fdot2_f32_bf16(__builtin_bit_cast(bf2, a), __builtin_bit_cast(bf2, b), c, false); }
#define RD128(dst, addr, OFF) asm volatile("ds_read_b128 %0, %1 offset:%2" : "=v"(dst) : "v"(addr), "i"(OFF) : "memory")
#define RDTR(dst, addr, OFF) asm volatile("ds_read_b64_tr_b16 %0, %1 offset:%2" : "=v"(dst) : "v"(addr), "i"(OFF) : "memory")
#define SBAR() __builtin_amdgcn_sched_barrier(0)
#define CAT(x, y) __builtin_shufflevector(x, y, 0, 1, 2, 3, 4, 5, 6, 7)
#define WAITV(n) asm volatile("s_waitcnt vmcnt(%0)" ::"i"(n) : "memory")
#define WAITL(n) asm volatile("s_waitcnt lgkmcnt(%0)" ::"i"(n) : "memory")
#define MEMB() asm volatile("" ::: "memory")
#define SLEEP(n) asm volatile("s_sleep %0" ::"i"(n) : "memory")
#define BAR() asm volatile("s_barrier" ::: "memory")
#define GL0(vo, sb, m0v) asm volatile("s_mov_b32 m0, %2\n s_nop 0\n global_load_lds_dwordx4 %0, %1" ::"v"(vo), "s"(sb), "s"(m0v) : "memory")
#define GLI(vo, sb, OFF) asm volatile("global_load_lds_dwordx4 %0, %1 offset:%2" ::"v"(vo), "s"(sb), "i"(OFF) : "memory")
#define MFMA16(a, b, c) __builtin_amdgcn_mfma_f32_16x16x32_bf16(a, b, c, 0, 0, 0)
#define S8(x) __builtin_bit_cast(s8, x)
#define LD128(off) __builtin_amdgcn_raw_buffer_load_b128(wsr, (off), 0, 16)
#define LD64(off) __builtin_amdgcn_raw_buffer_load_b64(wsr, (off), 0, 16)
#define ST128(v, off) __builtin_amdgcn_raw_buffer_store_b128((v), wsr, (off), 0, 16)
#define ST64(v, off) __builtin_amdgcn_raw_buffer_store_b64((v), wsr, (off), 0, 16)
#define OFA(i) (WS_OF + ((mb * 32 + hd) * 8 + (i)) * 1024 + l * 16)
#define NOW() ((unsigned)__builtin_amdgcn_s_memrealtime())
constexpr unsigned SPIN = 1u << 21;
DEV const char* uptr(const char* p) {
  unsigned long long v = (unsigned long long)p;
  unsigned lo_ = __builtin_amdgcn_readfirstlane((unsigned)v), hi_ = __builtin_amdgcn_readfirstlane((unsigned)(v >> 32));
  return (const char*)(((unsigned long long)hi_ << 32) | lo_);
}
DEV void lsig(unsigned addr, unsigned val) { asm volatile("s_waitcnt lgkmcnt(0)\n ds_write_b32 %0, %1" ::"v"(addr), "v"(val) : "memory"); }
template <int SL = 0> DEV void lwait(unsigned addr, unsigned mask, unsigned target) {
  _Pragma("unroll 1") for (unsigned it = 0; it < SPIN; ++it) {
    unsigned v;
    asm volatile("ds_read_b32 %0, %1\n s_waitcnt lgkmcnt(0)" : "=v"(v) : "v"(addr) : "memory");
    if (((unsigned)__ballot(v < target) & mask) == 0u) break;
    if (SL) SLEEP(SL);
  }
}
DEV unsigned nopois(unsigned a) { return min(a, POIS - 1u); }
DEV unsigned umax3(unsigned a, unsigned b, unsigned c) { return max(max(a, b), c); }
template <int C> DEV float dppf(float v) { return __int_as_float(__builtin_amdgcn_update_dpp(0, __float_as_int(v), C, 0xf, 0xf, false)); }
DEV unsigned wave_max(unsigned v) {
  v = max(v, (unsigned)__builtin_amdgcn_update_dpp(0, (int)v, 0x111, 0xf, 0xf, false));
  v = max(v, (unsigned)__builtin_amdgcn_update_dpp(0, (int)v, 0x112, 0xf, 0xf, false));
  v = max(v, (unsigned)__builtin_amdgcn_update_dpp(0, (int)v, 0x114, 0xf, 0xf, false));
  v = max(v, (unsigned)__builtin_amdgcn_update_dpp(0, (int)v, 0x118, 0xf, 0xf, false));
  v = max(v, (unsigned)__builtin_amdgcn_update_dpp(0, (int)v, 0x142, 0xa, 0xf, false));
  v = max(v, (unsigned)__builtin_amdgcn_update_dpp(0, (int)v, 0x143, 0xc, 0xf, false));
  return (unsigned)__builtin_amdgcn_readlane((int)v, 63);
}
DEV unsigned phi2(f2 u, f2 rs) {
  f2 t = u * rs;
  f2 a = t * 1.44269504f, p = t + 1.f, r;
  r[0] = __builtin_amdgcn_fmed3f(__builtin_amdgcn_exp2f(a[0]), p[0], 1.f);
  r[1] = __builtin_amdgcn_fmed3f(__builtin_amdgcn_exp2f(a[1]), p[1], 1.f);
  r += 1e-6f;
  return pk2(r[0], r[1]);
}

#define STDECL
#undef HB
#define HB 0
#define BSTV cw
#define STMP(i)
#define BST(i)
#define STFLUSH()
#define RETRY()
#define RETRY2()

DEV float half_sum(float v) {
  asm volatile("s_nop 1\n v_add_f32_dpp %0, %0, %0 row_ror:8 row_mask:0xf bank_mask:0xf\n s_nop 1\n v_add_f32_dpp %0, %0, %0 row_ror:4 row_mask:0xf bank_mask:0xf\n"
               "s_nop 1\n v_add_f32_dpp %0, %0, %0 row_ror:2 row_mask:0xf bank_mask:0xf\n s_nop 1\n v_add_f32_dpp %0, %0, %0 row_ror:1 row_mask:0xf bank_mask:0xf\n s_nop 1" : "+v"(v));
  auto r = __builtin_amdgcn_permlane16_swap(__float_as_uint(v), __float_as_uint(v), false, false);
  return __uint_as_float(r[0]) + __uint_as_float(r[1]);
}

struct FQ { s8 a[4]; s4 b[12]; };
template <bool B> struct BC { static constexpr bool v = B; };
template <int I> struct IC { static constexpr int v = I; };

__global__ __launch_bounds__(512) void fused(const u16* __restrict__ x, const u16* __restrict__ wqkv, const u16* __restrict__ wo,
                                             const u16* __restrict__ wrt, const u16* __restrict__ wg, const u16* __restrict__ wu,
                                             const u16* __restrict__ wd, char* __restrict__ ws, u16* __restrict__ out) {
  extern __shared__ __attribute__((aligned(16))) char lds[];
  asm volatile(".Lcode_beg:");
  const int w = __builtin_amdgcn_readfirstlane(threadIdx.x >> 6), l = threadIdx.x & 63;
  STDECL;
  STMP(0);
  const int g = blockIdx.x, xcd = g & 7, j = g >> 3, ii = j >> 2;
  unsigned cw = 0, cwo = 0; unsigned long long cwb = 0;
  if (w == 7) {
    unsigned long long pc; unsigned off, len, l1, l2, l3;
    asm volatile("s_getpc_b64 %0\n.Lcode_here:" : "=s"(pc));
    asm volatile("s_mov_b32 %0, (.Lcode_here - .Lcode_beg)" : "=s"(off));
    asm volatile("s_mov_b32 %0, (.Lend_c - .Lcode_beg)" : "=s"(len));
    asm volatile("s_mov_b32 %0, (.Lend_l - .Lcode_beg)" : "=s"(l1));
    asm volatile("s_mov_b32 %0, (.Lend_h - .Lcode_beg)" : "=s"(l2));
    asm volatile("s_mov_b32 %0, (.Lend_s - .Lcode_beg)" : "=s"(l3));
    len = max(max(len, l3), max(l1, l2));
    const unsigned o = min((unsigned)(l * 32 + j) * 64u, len - 4u);
    asm volatile("global_load_dword %0, %1, %2" : "+v"(cw) : "v"(o), "s"(pc - off) : "memory");
    cwo = o; cwb = pc - off;
  }
#define PTAB 0xA98FEDC1045B7623ull
#define PHT 0x6624924999924924ull
#define CX 4
  const int mb = (xcd & 1) * 8 + ii, hp = (xcd >> 1) * 4 + (j & 3);
  const bool sp = (hp & 7) == 3;
  const int rot = 2 * xcd + 4 * (ii & 3);
  const int nb = hp;
#define PAIRAT(p) ((int)(PTAB >> (4 * (p))) & 15)
#define HEADAT(s) (2 * PAIRAT((s) >> 1) + ((s) & 1))
  const int ph = (int)(PHT >> (2 * j)) & 3;
  const unsigned ldsb = (unsigned)(size_t)(LAS char*)lds;
  const unsigned adFL = ldsb + L_FL + 4 * (l & 7), myFL = ldsb + L_FL + 4 * w;
  const __amdgpu_buffer_rsrc_t wse = __builtin_amdgcn_make_buffer_rsrc(ws, 0, 64, 0x00020000);
  const int r15 = l & 15, g4 = l >> 4, q = (l & 15) >> 2, p = l & 3;
  const char* wob = uptr((const char*)wo + nb * 128);

  if (w != 4 && w != 5) {
  const unsigned ev0 = __builtin_amdgcn_raw_buffer_load_b32(wse, WS_E, 0, 16);
  if (w >= 6) {
    const int mw = w - 6;
    auto helper = [&](auto SP) {
    constexpr bool spare = decltype(SP)::v, moe = !spare && !NOMOE;
    int nbar = 0;
#define TICK() do { if (HB) { BST(nbar); asm volatile("" : "+v"(BSTV)); } SBAR(); BAR(); SBAR(); ++nbar; } while (0)
#define DTICK(lim) do { if (nbar < (lim)) TICK(); else SLEEP(2); } while (0)
    const int hd = hp * 2 + mw;
    const u4 pz = (u4)(POIS);
#define HQT 0xDCBA098765320410ull
#define CXA 1
    const int bq = ((int)(HQT >> (4 * hp)) & 15) * 4 + (ii & 3), ksp = (xcd & 1) * 2 + (ii >> 2);
#define CHUNK(b) ((b) < 14 ? 4 * (b) + 3 : ((b) - 14) + ((((b) - 14) * 43) >> 7))
    const int chunk = CHUNK(bq), bg = (CXA || bq < 14) ? bq ^ mw : bq, chg = CHUNK(bg);
    const unsigned tag = __builtin_amdgcn_readfirstlane(ev0) + 1u, tag2 = tag * 0x9E3779B1u;
    MKWS(tag);
    const int np = l & 7, kset = (l >> 3) & 1;
    u4 gw[4][8], dw[2][8];
    const __amdgpu_buffer_rsrc_t wgr = __builtin_amdgcn_make_buffer_rsrc((void*)(mw ? wu : wg), 0, D * F * 2, 0x00020000);
    const __amdgpu_buffer_rsrc_t wdr = __builtin_amdgcn_make_buffer_rsrc((void*)wd, 0, D * F * 2, 0x00020000);
    const __amdgpu_buffer_rsrc_t wor = __builtin_amdgcn_make_buffer_rsrc((void*)wo, 0, D * D * 2, 0x00020000);
    const unsigned gvo = ((256 * ksp + 32 * kset + 8 * g4) * F + 64 * chg + 8 * np) * 2;
    const unsigned dvo = ((64 * chunk + 8 * g4) * D + 256 * ksp + 128 * mw + 8 * r15) * 2;
    auto burst = [&](int k) {
      if (moe) {
#pragma unroll
        for (int i = 0; i < 16; ++i) {
          if (k < 2) gw[(2 * k + (i >> 3)) & 3][i & 7] = __builtin_amdgcn_raw_buffer_load_b128(wgr, gvo, (128 * k + 64 * (i >> 3) + (i & 7)) * F * 2, 2);
          else dw[i >> 3][i & 7] = __builtin_amdgcn_raw_buffer_load_b128(wdr, dvo, (32 * (i >> 3) + (i & 7)) * D * 2, 2);
        }
      }
    };
    auto transp = [&](u4(&v)[8]) {
      if (moe) {
        u4 f[8];
#pragma unroll
        for (int c = 0; c < 8; ++c)
#pragma unroll
          for (int e = 0; e < 4; ++e) {
            const unsigned ev = v[2 * e][c >> 1], od = v[2 * e + 1][c >> 1];
            f[c][e] = (c & 1) ? __builtin_amdgcn_perm(od, ev, 0x07060302u) : __builtin_amdgcn_perm(od, ev, 0x05040100u);
          }
#pragma unroll
        for (int c = 0; c < 8; ++c) { v[c] = f[c]; asm volatile("" : "+v"(v[c])); }
      }
    };
    auto pois = [&](int i) {
      if (i < 2) ST128(pz, WS_KVP + (hd * 16 + mb) * 2048 + i * 1024 + l * 16);
      else if (i == 2) ST128(pz, WS_ZP + (hd * 16 + mb) * 128 + (l & 7) * 16);
      else if (i < 11) ST128(pz, OFA(i - 3));
      else ST128(pz, WS_GP + ((chg * 4 + ksp) * 2 + mw) * 2048 + (i - 11) * 1024 + l * 16);
    };
    unsigned wt0, wt1, wt2 = 0, wt3 = 0;
    auto touch = [&]() {
      const unsigned to = (j * 32 + mw * 16 + (l >> 3)) * 2048 + (xcd >> 1) * 512 + (l & 7) * 64;
      wt0 = __builtin_amdgcn_raw_buffer_load_b32(wor, to, 0, 0); wt1 = __builtin_amdgcn_raw_buffer_load_b32(wor, to, 16384, 0);
      if (TLBW) {
        const __amdgpu_buffer_rsrc_t outr = __builtin_amdgcn_make_buffer_rsrc(out, 0, 2048 * D * 2, 0x00020000);
        wt2 = __builtin_amdgcn_raw_buffer_load_b32(outr, (mb * 128 + mw * 64 + (l & 3) * 16) * 2048 + nb * 128, 0, 0);
        wt3 = __builtin_amdgcn_raw_buffer_load_b32(wsr, (l & 1) ? WS_GP + g * 4096 + mw * 2048 : WS_YP + (g >> 2) * 32768 + (l & 2) * 8192, 0, 0);
      }
    };
    s4 sb[24];
    auto colrd = [&](int h) {
      const unsigned bo = ldsb + (1 + h) * STG + 16384 + (8 * g4 + q) * 128 + (p & 1) * 8;
      const int sw = ((q >> 1) << 2) | ((g4 & 1) << 1);
      const unsigned b0 = bo + (((mw * 4 + (p >> 1)) ^ sw) * 16), b1 = bo + (((mw * 4 + 2 + (p >> 1)) ^ sw) * 16);
#pragma unroll
      for (int n = 0; n < 12; ++n) { RDTR(sb[2 * n], (n & 1) ? b1 : b0, ((n % 6) >> 1) * 8192 + (n / 6) * 4096); RDTR(sb[2 * n + 1], (n & 1) ? b1 : b0, ((n % 6) >> 1) * 8192 + (n / 6) * 4096 + 512); }
      WAITL(0);
      SBAR();
    };
    auto colsum = [&]() {
      const u4 ones = (u4)(0x3F803F80u);
      f4 c[6];
#pragma unroll
      for (int n = 0; n < 12; ++n) c[n % 6] = MFMA16(S8(ones), CAT(sb[2 * n], sb[2 * n + 1]), n >= 6 ? c[n % 6] : (f4)(0.f));
      const u4 csg = (u4){__float_as_uint(g4 ? c[3][0] : c[0][0]), __float_as_uint(g4 ? c[4][0] : c[1][0]), __float_as_uint(g4 ? c[5][0] : c[2][0]), tag};
      ST128(csg, l < 32 ? WS_CS + (hd * 16 + mb) * 512 + l * 16 : WS_SZ);
    };
    u4 stv, cv[8], cd; float cs[3];
    auto gload = [&]() {
      stv = LD128(WS_ST + (mb * 128 + mw * 64 + l) * 16);
#pragma unroll
      for (int i = 0; i < 8; ++i) cv[i] = LD128(WS_CS + hd * 8192 + i * 1024 + l * 16);
    };
    auto gbad = [&]() {
      unsigned bad = (stv[1] ^ tag) | (stv[3] ^ tag);
#pragma unroll
      for (int i = 0; i < 8; ++i) bad |= cv[i][3] ^ tag;
      return __ballot(bad != 0u) != 0ull;
    };
    int idx;
    auto gfin = [&]() {
      if (gbad()) { RETRY(); _Pragma("unroll 1") for (unsigned it = 0; it < SPIN; ++it) { SLEEP(2); gload(); if (!gbad()) break; } }
#pragma unroll
      for (int e = 0; e < 3; ++e) {
        float a = __uint_as_float(cv[0][e]);
#pragma unroll
        for (int i = 1; i < 8; ++i) a += __uint_as_float(cv[i][e]);
        auto r = __builtin_amdgcn_permlane32_swap(__float_as_uint(a), __float_as_uint(a), false, false);
        cs[e] = __uint_as_float(r[0]) + __uint_as_float(r[1]);
      }
      asm volatile("" : "+v"(cs[0]), "+v"(cs[1]), "+v"(cs[2]) : "v"(wt0), "v"(wt1), "v"(wt2), "v"(wt3));
    };
    auto cload = [&]() { cd = LD128(WS_CAND + (((hp >> 2) == 2) * 16 + mb) * 128 + (l & 7) * 16); };
    auto cfin = [&]() {
      if (__builtin_expect(__ballot(cd[1] != tag || cd[3] != tag) != 0ull, 0)) {
        RETRY();
        _Pragma("unroll 1") for (unsigned it = 0; it < SPIN; ++it) { SLEEP(4); cload(); if (__ballot(cd[1] != tag || cd[3] != tag) == 0ull) break; }
      }
      cd[0] = max(cd[0], cd[2]);
      idx = (int)(cd[0] & 2047u);
    };
    auto lpub = [&]() {
      *(LAS float*)(lds + L_M + (mw * 64 + l) * 4) = __uint_as_float(stv[0]);
      *(LAS float*)(lds + L_RS + (mw * 64 + l) * 4) = __uint_as_float(stv[2]);
      if (l < 32) {
#pragma unroll
        for (int e = 0; e < 3; ++e) *(LAS float*)(lds + L_CS + mw * 384 + ((3 * g4 + e) * 16 + r15) * 4) = cs[e];
      }
      if (mw == 0 && l < 8) *(LAS int*)(lds + L_SEL + l * 4) = idx;
      lsig(myFL, 1u);
    };
    u4 xs[4]; float xrs;
    asm volatile("" ::"s"(wg), "s"(wu), "s"(wd), "v"(cw));
    if (HPRIO) __builtin_amdgcn_s_setprio(HPRIO);
    TICK();
    if (spare) {
      TICK();
      if (ii >> 2) TICK();
      colrd(ii >> 2);
      if (!(ii >> 2)) TICK();
      u4 kk[8];
      auto kload = [&]() {
#pragma unroll
        for (int i = 0; i < 8; ++i) kk[i] = LD128(WS_RG + mw * 8192 + i * 1024 + l * 16);
      };
      kload();
      TICK();
      _Pragma("unroll 1") for (unsigned it = 0; it < SPIN; ++it) {
        unsigned bad = 0;
#pragma unroll
        for (int i = 0; i < 8; ++i) { bad |= (kk[i][1] ^ tag) | (kk[i][3] ^ tag); asm volatile("" : "+v"(bad)); }
        if (__ballot(bad != 0u) == 0ull) break;
        RETRY();
        kload();
        DTICK(7);
      }
      __builtin_amdgcn_s_setprio(3);
      unsigned m1 = max(kk[0][0], kk[0][2]), m2 = min(kk[0][0], kk[0][2]), m3 = 0;
#pragma unroll
      for (int i = 2; i < 16; ++i) {
        const unsigned a = kk[i >> 1][(i & 1) * 2];
        asm("v_med3_u32 %0, %1, %2, %0" : "+v"(m3) : "v"(m2), "v"(a));
        asm("v_med3_u32 %0, %1, %2, %0" : "+v"(m2) : "v"(m1), "v"(a));
        m1 = max(m1, a);
      }
      unsigned mine8 = 0;
#pragma unroll
      for (int it = 0; it < 8; ++it) {
        const unsigned km = wave_max(m1);
        mine8 = l == it ? km : mine8;
        const bool win = m1 == km;
        if (it < 7) { m1 = win ? m2 : m1; m2 = win ? m3 : m2; m3 = win ? 0u : m3; }
        if (it == 3 && nbar < 7) TICK();
      }
      if (__builtin_expect(__ballot(m1 == 0u) != 0ull, 0)) {
        unsigned lim = 0;
#pragma unroll 1
        for (int it = 0; it < 8; ++it) {
          unsigned m = 0;
#pragma unroll
          for (int i = 0; i < 8; ++i) m = umax3(m, kk[i][0] - lim, kk[i][2] - lim);
          lim += wave_max(m);
          mine8 = l == it ? lim : mine8;
        }
      }
      ST64(((u2){mine8, tag}), l < 8 ? WS_CAND + ((hp >> 3) * 16 + mb) * 128 + (mw ? 7 - l : l) * 16 + mw * 8 : WS_SZ);
      __builtin_amdgcn_s_setprio(HPRIO);
      STMP(2);
      TICK();
      colsum();
#pragma unroll
      for (int i = 0; i < 11; ++i) { if (i == 5) DTICK(9); pois(i); }
      touch();
      while (nbar < 10) TICK();
      gload(); cload();
      if (RT && mw) asm volatile("global_load_dword %0, %1, %2 sc1" : "+v"(cw) : "v"(cwo), "s"(cwb) : "memory");
      TICK(); TICK();
      WAITV(0);
      asm volatile("" : "+v"(cw));
      gfin(); cfin();
      STMP(3);
      TICK(); TICK(); TICK();
      lpub();
    } else {
      if (PV == 5) { pois(0); pois(1); }
      TICK();
      if (PV == 4) { pois(0); pois(1); pois(2); }
      if (PV == 5) { pois(2); pois(3); }
      if (GPV == 0) pois(11);
      if (ii >> 2) { TICK(); pois(3); pois(4); if (GPV == 0) pois(12); }
      colrd(ii >> 2);
      TICK();
      if (!(ii >> 2)) { pois(3); pois(4); if (GPV == 0) pois(12); }
      colsum();
      if (!(ii >> 2)) TICK();
      u4 sv; f4 acc[8];
      auto xload = [&]() {
        const u16* xp = x + (size_t)idx * D + 256 * ksp + 32 * kset + 8 * g4;
        sv = LD128(WS_ST + idx * 16);
#pragma unroll
        for (int it = 0; it < 4; ++it) xs[it] = *(const u4*)(xp + 64 * it);
        SBAR();
      };
      auto xnorm = [&]() {
        if (__builtin_expect(__ballot(sv[1] != tag || sv[3] != tag) != 0ull, 0)) {
          _Pragma("unroll 1") for (unsigned it = 0; it < SPIN; ++it) { SLEEP(1); sv = LD128(WS_ST + idx * 16); if (__ballot(sv[1] != tag || sv[3] != tag) == 0ull) break; }
        }
        const float nm = -__uint_as_float(sv[0]);
        xrs = __uint_as_float(sv[2]);
#pragma unroll
        for (int it = 0; it < 4; ++it) {
          float t0, t1, t2, t3; unsigned x0 = xs[it][0], x1 = xs[it][1], x2 = xs[it][2], x3 = xs[it][3];
          asm volatile("v_dot2_f32_bf16 %4, %0, %8, %10\n v_dot2_f32_bf16 %0, %0, %9, %10\n v_dot2_f32_bf16 %5, %1, %8, %10\n v_dot2_f32_bf16 %1, %1, %9, %10\n"
                       "v_dot2_f32_bf16 %6, %2, %8, %10\n v_dot2_f32_bf16 %2, %2, %9, %10\n v_dot2_f32_bf16 %7, %3, %8, %10\n v_dot2_f32_bf16 %3, %3, %9, %10\n"
                       "v_cvt_pk_bf16_f32 %0, %4, %0\n v_cvt_pk_bf16_f32 %1, %5, %1\n v_cvt_pk_bf16_f32 %2, %6, %2\n s_nop 0\n v_cvt_pk_bf16_f32 %3, %7, %3"
                       : "+v"(x0), "+v"(x1), "+v"(x2), "+v"(x3), "=&v"(t0), "=&v"(t1), "=&v"(t2), "=&v"(t3) : "s"(0x00003F80u), "s"(0x3F800000u), "v"(nm));
          xs[it] = (u4){x0, x1, x2, x3};
        }
        asm volatile("s_nop 1" : "+v"(xs[0]), "+v"(xs[1]), "+v"(xs[2]), "+v"(xs[3]), "+v"(xrs));
        SBAR();
      };
      auto gmul = [&](int i0, int i1) {
#pragma unroll
        for (int it = i0; it < i1; ++it)
#pragma unroll
          for (int c = 0; c < 8; ++c) acc[c] = MFMA16(S8(gw[it][c]), S8(xs[it]), it ? acc[c] : (f4)(0.f));
#pragma unroll
        for (int c = 0; c < 8; ++c) asm volatile("" : "+v"(acc[c]));
        SBAR();
      };
      auto gout = [&]() {
        unsigned go = r15 < 8 ? WS_GP + ((chg * 4 + ksp) * 2 + mw) * 2048 + (g4 >> 1) * 1024 + (g4 & 1) * 512 + r15 * 16 : WS_SZ;
        asm volatile("" : "+v"(go));
#pragma unroll
        for (int i = 0; i < 4; ++i) {
          unsigned a[4], b[4];
#pragma unroll
          for (int c = 0; c < 4; ++c) {
            auto r = __builtin_amdgcn_permlane32_swap(__float_as_uint(acc[c][i]), __float_as_uint(acc[c + 4][i]), false, false);
            a[c] = r[0]; b[c] = r[1];
          }
          unsigned s0, s1, s2, s3;
#define AR(d, x, y) "v_add_f32_dpp %" #d ", %" #x ", %" #y " row_ror:8 row_mask:0xf bank_mask:0xf\n"
          asm volatile("s_nop 1\n" AR(0, 8, 4) AR(1, 9, 5) AR(2, 10, 6) AR(3, 11, 7) : "=&v"(s0), "=&v"(s1), "=&v"(s2), "=&v"(s3)
                       : "v"(a[0]), "v"(a[1]), "v"(a[2]), "v"(a[3]), "v"(b[0]), "v"(b[1]), "v"(b[2]), "v"(b[3]));
          ST128(((u4){nopois(s0), s1, s2, s3}), go + i * 128);
        }
        SBAR();
      };
      for (int i = 0; i < ph; ++i) TICK();
      burst(0); touch();
      TICK();
      if (GPV == 1) { pois(11); pois(12); }
      TICK();
      if (TL) { transp(gw[0]); transp(gw[1]); }
      TICK();
      if (!TL) transp(gw[0]);
      TICK();
      if (!TL) transp(gw[1]);
      TICK();
      if (ph) cload();
      if (RT && mw) asm volatile("global_load_dword %0, %1, %2 sc1" : "+v"(cw) : "v"(cwo), "s"(cwb) : "memory");
      SBAR();
      burst(1);
      TICK();
      pois(5); pois(6); pois(7); pois(8); pois(9); pois(10);
      if (GPV == 2) { pois(11); pois(12); }
      TICK(); TICK();
      transp(gw[2]);
      WAITV(0);
      asm volatile("" : "+v"(cw));
      if (ph < 2) TICK();
      transp(gw[3]);
      if (ph < 1) { SBAR(); cload(); TICK(); }
      SBAR();
      if (XO == 0) { cfin(); xload(); gload(); } else { gload(); SBAR(); cfin(); xload(); }
      TICK();
      gfin();
      lpub();
      SBAR();
      burst(2);
      STMP(3);
      SBAR();
      __builtin_amdgcn_s_setprio(0);
      if (HS0) SLEEP(HS0);
      if (G1 & 1) { lwait<HW1>(adFL, 0x0Fu, G1 >> 4); if (G1S) SLEEP(G1S); __builtin_amdgcn_s_setprio(TP0); }
      xnorm();
      if (G1 & 2) { lwait<2>(adFL, 0x0Fu, G1 >> 4); if (G1S) SLEEP(G1S); __builtin_amdgcn_s_setprio(TP0); }
      STMP(5);
      gmul(0, 4);
      gout();
      STMP(6);
      SBAR();
    }
    __builtin_amdgcn_s_setprio(TP1);
    STMP(4);
    if (moe) {
      if (TD) { transp(dw[0]); transp(dw[1]); SBAR(); }
      lwait<2>(adFL, 0x30u, 1);
      if (G2S) SLEEP(G2S);
      STMP(7);
      f4 acc[8];
      u4 ha[2];
      {
        u4 pg[4][2];
        auto pload = [&]() {
#pragma unroll
          for (int ks = 0; ks < 4; ++ks)
#pragma unroll
            for (int t = 0; t < 2; ++t) pg[ks][t] = LD128(WS_GP + ((chunk * 4 + ks) * 4 + 2 * t + mw) * 1024 + (4 * kset + g4) * 128 + np * 16);
        };
        auto pbad = [&]() { return __ballot(umax3(umax3(pg[0][0][0], pg[0][1][0], pg[1][0][0]), umax3(pg[1][1][0], pg[2][0][0], pg[2][1][0]), umax3(pg[3][0][0], pg[3][1][0], 0u)) == POIS) != 0ull; };
        pload();
        SBAR();
        __builtin_amdgcn_s_setprio(TP2);
        if (!TD) { transp(dw[0]); transp(dw[1]); }
        SBAR();
        if (__builtin_expect(pbad(), 0)) { _Pragma("unroll 1") for (unsigned it = 0; it < SPIN; ++it) { RETRY2(); SLEEP(4); pload(); if (!pbad()) break; } }
        STMP(10);
        f4 gq[2];
#pragma unroll
        for (int t = 0; t < 2; ++t)
          gq[t] = ((__builtin_bit_cast(f4, pg[0][t]) + __builtin_bit_cast(f4, pg[1][t])) + (__builtin_bit_cast(f4, pg[2][t]) + __builtin_bit_cast(f4, pg[3][t]))) * xrs;
        float hv[4];
#pragma unroll
        for (int e = 0; e < 4; ++e) {
          const float gg = rbf(gq[0][e]), uu = rbf(gq[1][e]);
          hv[e] = rbf(gg / (1.f + __expf(-gg))) * uu;
        }
        *(LAS u2*)(lds + L_HX + l * 16 + mw * 8) = (u2){pk2(hv[0], hv[1]), pk2(hv[2], hv[3])};
        lsig(myFL, 2u);
        lwait(adFL, 0xC0u, 2);
        ha[0] = *(const LAS u4*)(lds + L_HX + l * 16); ha[1] = *(const LAS u4*)(lds + L_HX + (l ^ 8) * 16);
      }
#pragma unroll
      for (int it = 0; it < 2; ++it)
#pragma unroll
        for (int c = 0; c < 8; ++c) acc[c] = MFMA16(S8(ha[it]), S8(dw[it][c]), it ? acc[c] : (f4)(0.f));
      if (g4 < 2) {
#pragma unroll
        for (int i = 0; i < 4; ++i) {
          const unsigned yo = WS_YP + ((chunk * 8 + 4 * g4 + i) * D + 256 * ksp + 128 * mw + 4 * r15) * 4;
          ST128(__builtin_bit_cast(u4, ((f4){acc[0][i], acc[1][i], acc[2][i], acc[3][i]})), yo);
          ST128(__builtin_bit_cast(u4, ((f4){acc[4][i], acc[5][i], acc[6][i], acc[7][i]})), yo + 256);
        }
      }
      STMP(11);
      WAITV(0);
      if (l == 0) ST64(((u2){tag, tag2}), WS_YF + (ksp * 2 + mw) * 8192 + chunk * FS);
      STMP(8);
    }
    float sg;
    {
      const unsigned mono = cd[0] >> 16, rb = (mono & 0x8000u) ? (mono & 0x7FFFu) : (~mono & 0xFFFFu);
      sg = rbf(1.f / (1.f + __expf(-lo(rb))));
    }
    unsigned mine = (unsigned)__ballot(!NOMOE && l < 8 && (idx >> 7) == mb && (l & 1) == mw);
    if (mine) {
      if (YG) lwait<4>(adFL, 0x0Fu, YG); else lwait<4>(adFL, 0x30u, 2);
      _Pragma("unroll 1") for (unsigned it = 0; it < SPIN; ++it) {
        u2 f = LD64(WS_YF + (nb >> 1) * 8192 + (l < 56 ? l : 0) * FS);
        if (__ballot(f[0] != tag || f[1] != tag2) == 0ull) break;
        SLEEP(YSL);
      }
      lwait<1>(adFL, 0x0Fu, 4);
      __builtin_amdgcn_s_setprio(TP2);
      while (mine) {
        const int t = __builtin_ctz(mine); mine &= mine - 1;
        u4 yv[14];
#pragma unroll
        for (int i = 0; i < 14; ++i) yv[i] = LD128(WS_YP + (((i * 4 + g4) * 8 + t) * D + (nb >> 1) * 128 + (r15 >> 3) * 64 + (nb & 1) * 32 + (r15 & 7) * 4) * 4);
        f4 y = __builtin_bit_cast(f4, yv[0]);
#pragma unroll
        for (int i = 1; i < 14; ++i) y += __builtin_bit_cast(f4, yv[i]);
        const float gt = __int_as_float(__builtin_amdgcn_readlane(__float_as_int(sg), t));
#pragma unroll
        for (int e = 0; e < 4; ++e) {
          auto r = __builtin_amdgcn_permlane32_swap(__float_as_uint(y[e]), __float_as_uint(y[e]), false, false);
          const float a = __uint_as_float(r[0]) + __uint_as_float(r[1]);
          auto r2 = __builtin_amdgcn_permlane16_swap(__float_as_uint(a), __float_as_uint(a), false, false);
          y[e] = rbf(rbf(__uint_as_float(r2[0]) + __uint_as_float(r2[1])) * gt);
        }
        if (l < 16) *(LAS f4*)(lds + L_KVX + t * 256 + (l & 7) * 32 + (l >> 3) * 16) = y;
      }
    }
    lsig(myFL, 3u);
    STMP(9);
    STFLUSH();
    if (spare) asm volatile(".Lend_s:"); else asm volatile(".Lend_h:");
    };
    if (sp) helper(BC<true>()); else helper(BC<false>());
    return;
  }

  const int wr = w >> 1, wc = w & 1;
  unsigned tag; u2 kg; u4 sg4;
  const int srow = mb * 128 + hp * 8 + 2 * w + (l >> 5);
  {
    const int row = srow;
    u4 xa[4], rw[4];
#pragma unroll
    for (int c = 0; c < 4; ++c) xa[c] = *(const u4*)(x + row * D + (l & 31) * 8 + 256 * c);
#pragma unroll
    for (int c = 0; c < 4; ++c) rw[c] = *(const u4*)(wrt + (l & 31) * 8 + 256 * c);
    float s0 = 0, s1 = 0;
#pragma unroll
    for (int c = 0; c < 4; ++c)
#pragma unroll
      for (int e = 0; e < 4; e += 2) { unsigned x0 = xa[c][e], x1 = xa[c][e + 1]; s0 = dot2(x0, 0x3F803F80u, s0); s1 = dot2(x1, 0x3F803F80u, s1); }
    const float m = half_sum(s0 + s1) * (1.f / D);
    f2 v[16], q2 = (f2)(0.f), q3 = (f2)(0.f);
#pragma unroll
    for (int c = 0; c < 16; ++c) v[c] = (f2){lo(xa[c >> 2][c & 3]), hi(xa[c >> 2][c & 3])} - m;
#pragma unroll
    for (int c = 0; c < 16; c += 2) { q2 += v[c] * v[c]; q3 += v[c + 1] * v[c + 1]; }
    q2 += q3;
    const float mr = rbf(m);
    const float rs = 1.f / rbf(sqrtf(rbf(rbf(half_sum(q2[0] + q2[1]) * (1.f / D)) + 1e-5f)));
    float d0 = 0, d1 = 0;
#pragma unroll
    for (int c = 0; c < 16; c += 2) {
      const f2 e0 = (f2){lo(xa[c >> 2][c & 3]), hi(xa[c >> 2][c & 3])} - mr, e1 = (f2){lo(xa[c >> 2][(c & 3) + 1]), hi(xa[c >> 2][(c & 3) + 1])} - mr;
      const unsigned p0 = pk2(e0[0], e0[1]), p1 = pk2(e1[0], e1[1]);
      const f2 n0 = (f2){lo(p0), hi(p0)} * rs, n1 = (f2){lo(p1), hi(p1)} * rs;
      unsigned c0 = rw[c >> 2][c & 3], c1 = rw[c >> 2][(c & 3) + 1];
      d0 = dot2(pk2(n0[0], n0[1]), c0, d0); d1 = dot2(pk2(n1[0], n1[1]), c1, d1);
    }
    const float d = half_sum(d0 + d1);
    const unsigned rb = pk2(d, d) & 0xFFFFu;
    const unsigned mono = (rb & 0x8000u) ? (~rb & 0xFFFFu) : (rb | 0x8000u);
    tag = __builtin_amdgcn_readfirstlane(ev0) + 1u;
    kg = (u2){(mono << 16) | (unsigned)row, tag};
    sg4 = (u4){__float_as_uint(mr), tag, __float_as_uint(rs), tag};
  }
  MKWS(tag);
  if ((l & 31) == 0) { ST64(kg, WS_RG + srow * 8); ST128(sg4, WS_ST + srow * 16); }
  unsigned aad[2], bad[2];
#pragma unroll
  for (int s = 0; s < 2; ++s) aad[s] = ldsb + (wr * 64 + r15) * 128 + (((4 * s + g4) ^ ((r15 >> 1) & 7)) * 16);
#pragma unroll
  for (int nh = 0; nh < 2; ++nh) bad[nh] = ldsb + 16384 + (8 * g4 + q) * 128 + (((wc * 4 + 2 * nh + (p >> 1)) ^ (((q >> 1) << 2) | ((g4 & 1) << 1))) * 16) + (p & 1) * 8;
  f4 acc[4][6];
#pragma unroll
  for (int m = 0; m < 4; ++m)
#pragma unroll
    for (int n = 0; n < 6; ++n) { acc[m][n] = (f4)(0.f); asm volatile("" : "+v"(acc[m][n])); }
#define MF(m, n, Fq) do { if (n < 2) acc[m][n] = MFMA16(CAT(Fq.b[2 * n], Fq.b[2 * n + 1]), Fq.a[m], acc[m][n]); \
     else acc[m][n] = MFMA16(Fq.a[m], CAT(Fq.b[2 * n], Fq.b[2 * n + 1]), acc[m][n]); SBAR(); } while (0)
#define RB(G, n, vb0, vb1, SS, hf) RDTR(G.b[2 * (n) + (hf)], (((n) & 1) ? vb1 : vb0), ((n) >> 1) * 8192 + (SS) * 4096 + (hf) * 512)
#define RA(G, m, va) RD128(G.a[m], va, (m) * 2048)
#define SUB(Fq, G, va, vb0, vb1, SS, PRE) do { \
    WAITL(13); SBAR(); \
    MF(0, 0, Fq); PRE; RA(G, 0, va); SBAR(); \
    WAITL(11); SBAR(); \
    MF(1, 0, Fq); RB(G, 0, vb0, vb1, SS, 0); SBAR(); MF(2, 0, Fq); RB(G, 0, vb0, vb1, SS, 1); SBAR(); MF(3, 0, Fq); RA(G, 1, va); SBAR(); \
    WAITL(12); SBAR(); \
    MF(0, 1, Fq); RA(G, 2, va); SBAR(); MF(1, 1, Fq); RA(G, 3, va); SBAR(); MF(2, 1, Fq); RB(G, 1, vb0, vb1, SS, 0); SBAR(); MF(3, 1, Fq); RB(G, 1, vb0, vb1, SS, 1); SBAR(); \
    WAITL(8); SBAR(); \
    MF(0, 2, Fq); RB(G, 2, vb0, vb1, SS, 0); SBAR(); MF(1, 2, Fq); RB(G, 2, vb0, vb1, SS, 1); SBAR(); MF(2, 2, Fq); RB(G, 3, vb0, vb1, SS, 0); SBAR(); MF(3, 2, Fq); RB(G, 3, vb0, vb1, SS, 1); SBAR(); \
    MF(0, 3, Fq); RB(G, 4, vb0, vb1, SS, 0); SBAR(); MF(1, 3, Fq); RB(G, 4, vb0, vb1, SS, 1); SBAR(); MF(2, 3, Fq); RB(G, 5, vb0, vb1, SS, 0); SBAR(); MF(3, 3, Fq); RB(G, 5, vb0, vb1, SS, 1); SBAR(); \
    MF(0, 4, Fq); MF(1, 4, Fq); MF(2, 4, Fq); MF(3, 4, Fq); MF(0, 5, Fq); MF(1, 5, Fq); MF(2, 5, Fq); MF(3, 5, Fq); } while (0)
#define KSTEP(MODE) do { \
    if (kt == sx) { const unsigned xo = xad + buf * STG; \
      _Pragma("unroll") for (int t = 0; t < 8; ++t) { const unsigned a_ = xo ^ (t * 16); \
        asm volatile("ds_read_b64 %0, %1" : "+v"(xv[t]) : "v"(a_) : "memory"); } \
      WAITL(0); SBAR(); } \
    const int nbuf = (buf + 1) & 3; \
    const unsigned bo = buf * STG, nbo = nbuf * STG; \
    { unsigned va = aad[1] + bo, vb0 = bad[0] + bo, vb1 = bad[1] + bo; SUB(f0, f1, va, vb0, vb1, 1, ); } \
    if (MODE < 2) { \
      unsigned va = aad[0] + nbo, vb0 = bad[0] + nbo, vb1 = bad[1] + nbo; \
      SUB(f1, f0, va, vb0, vb1, 0, if (MODE == 0) { WAITL(0); BAR(); } else asm volatile("ds_write_b32 %0, %1" ::"v"(myFL), "v"(1u) : "memory"); SBAR()); \
    } else { asm volatile("ds_write_b32 %1, %2\n ds_read_b32 %0, %3\n s_waitcnt lgkmcnt(2)" : "=&v"(fl0) : "v"(myFL), "v"(2u), "v"(adFL) : "memory"); SBAR(); \
      MF(0, 2, f1); MF(1, 2, f1); MF(2, 2, f1); MF(3, 2, f1); MF(0, 3, f1); MF(1, 3, f1); MF(2, 3, f1); MF(3, 3, f1); \
      MF(0, 4, f1); MF(1, 4, f1); MF(2, 4, f1); MF(3, 4, f1); MF(0, 5, f1); MF(1, 5, f1); MF(2, 5, f1); MF(3, 5, f1); \
      MF(0, 0, f1); MF(1, 0, f1); MF(2, 0, f1); MF(3, 0, f1); MF(0, 1, f1); MF(1, 1, f1); MF(2, 1, f1); MF(3, 1, f1); } \
    buf = nbuf; } while (0)
  u2 xv[8];
#pragma unroll
  for (int t = 0; t < 8; ++t) asm volatile("" : "=v"(xv[t]));
  const int sx = (nb - rot) & 15;
  const unsigned xad = ldsb + (w * 32 + (l & 31)) * 128 + ((r15 >> 1) & 7) * 16 + (l >> 5) * 8;
  STMP(1);
  WAITL(0);
  BAR();
  STMP(2);
  WAITL(0);
  FQ f0, f1;
  { unsigned va = aad[0], vb0 = bad[0], vb1 = bad[1];
    RA(f0, 0, va); RB(f0, 0, vb0, vb1, 0, 0); RB(f0, 0, vb0, vb1, 0, 1); RA(f0, 1, va); RA(f0, 2, va); RA(f0, 3, va);
    RB(f0, 1, vb0, vb1, 0, 0); RB(f0, 1, vb0, vb1, 0, 1); RB(f0, 2, vb0, vb1, 0, 0); RB(f0, 2, vb0, vb1, 0, 1); RB(f0, 3, vb0, vb1, 0, 0); RB(f0, 3, vb0, vb1, 0, 1);
    RB(f0, 4, vb0, vb1, 0, 0); RB(f0, 4, vb0, vb1, 0, 1); RB(f0, 5, vb0, vb1, 0, 0); RB(f0, 5, vb0, vb1, 0, 1);
    WAITL(0); SBAR(); }
  int buf = 0; unsigned fl0;
#pragma unroll 1
  for (int kt = 0; kt < 14; ++kt) KSTEP(0);
  STMP(14); SBAR();
  { const int kt = 14; KSTEP(1); }
  STMP(7); SBAR();
  { const int kt = 15; KSTEP(2); }
  asm volatile("s_waitcnt lgkmcnt(0)" : "+v"(fl0) : : "memory");
  if (__builtin_expect(((unsigned)__ballot(fl0 < 1u) & 0xC0u) != 0u, 0)) lwait(adFL, 0xC0u, 1);
  STMP(3);
  __builtin_amdgcn_s_setprio(2);
  const LAS float* Lm = (const LAS float*)(lds + L_M) + wr * 64;
  const LAS float* Lr = (const LAS float*)(lds + L_RS) + wr * 64;
  const LAS float* Lc = (const LAS float*)(lds + L_CS) + wc * 96;
  f4 kv[4], zt[2];
  {
    float cs[4];
#pragma unroll
    for (int n = 0; n < 4; ++n) cs[n] = Lc[32 + 16 * n + r15];
    const u4 ones = (u4)(0x3F803F80u);
#pragma unroll
    for (int t = 0; t < 2; ++t) {
      u4 kf[2], vf[2];
#pragma unroll
      for (int mm = 0; mm < 2; ++mm) {
        const int m = 2 * t + mm;
        const f4 mv = *(const LAS f4*)(Lm + m * 16 + 4 * g4), rv = *(const LAS f4*)(Lr + m * 16 + 4 * g4);
#pragma unroll
        for (int a = 0; a < 2; ++a) {
          const f4 uk = acc[m][2 + a] - mv * cs[a];
          const f4 uv = (acc[m][4 + a] - mv * cs[2 + a]) * rv;
          kf[a][2 * mm] = phi2((f2){uk[0], uk[1]}, (f2){rv[0], rv[1]}); kf[a][2 * mm + 1] = phi2((f2){uk[2], uk[3]}, (f2){rv[2], rv[3]});
          vf[a][2 * mm] = pk2(uv[0], uv[1]); vf[a][2 * mm + 1] = pk2(uv[2], uv[3]);
        }
      }
#pragma unroll
      for (int tt = 0; tt < 4; ++tt) kv[tt] = MFMA16(S8(kf[tt & 1]), S8(vf[tt >> 1]), t ? kv[tt] : (f4)(0.f));
#pragma unroll
      for (int a = 0; a < 2; ++a) zt[a] = MFMA16(S8(kf[a]), S8(ones), t ? zt[a] : (f4)(0.f));
    }
  }
  const int hd = hp * 2 + wc;
  {
    LAS f4* ks = (LAS f4*)(lds + L_KVX + w * 3072) + l;
    const LAS f4* kr = (const LAS f4*)(lds + L_KVX + (w ^ 2) * 3072) + l;
    ks[0] = wr ? kv[0] : kv[2]; ks[64] = wr ? kv[1] : kv[3]; ks[128] = wr ? zt[0] : zt[1];
    lsig(myFL, 3);
    const f4 m0 = wr ? kv[2] : kv[0], m1 = wr ? kv[3] : kv[1], mz = wr ? zt[1] : zt[0];
    lwait(adFL, 1u << (w ^ 2), 3);
    const f4 s0 = m0 + kr[0], s1 = m1 + kr[64], sz = mz + kr[128];
    u4 o; o[0] = min(pk2(s0[0], s0[1]), POIS - 1u); o[1] = pk2(s0[2], s0[3]); o[2] = pk2(s1[0], s1[1]); o[3] = pk2(s1[2], s1[3]);
    ST128(o, WS_KVP + (hd * 16 + mb) * 2048 + wr * 1024 + l * 16);
    u4 zo = __builtin_bit_cast(u4, sz); zo[0] = min(zo[0], POIS - 1u);
    if (r15 == 0) ST128(zo, WS_ZP + (hd * 16 + mb) * 128 + g4 * 32 + wr * 16);
  }
  STMP(4);
  {
    const f4 c0 = *(const LAS f4*)(Lc + 4 * g4), c1 = *(const LAS f4*)(Lc + 16 + 4 * g4);
#pragma unroll
    for (int m = 0; m < 4; ++m) {
      const float mv = Lm[m * 16 + r15], rv = Lr[m * 16 + r15];
      const f4 u0 = acc[m][0] - c0 * mv, u1 = acc[m][1] - c1 * mv;
      u4 pf;
      pf[0] = phi2((f2){u0[0], u0[1]}, (f2)(rv)); pf[1] = phi2((f2){u0[2], u0[3]}, (f2)(rv));
      pf[2] = phi2((f2){u1[0], u1[1]}, (f2)(rv)); pf[3] = phi2((f2){u1[2], u1[3]}, (f2)(rv));
      *(LAS u4*)(lds + L_PQ + wr * 8192 + wc * 4096 + m * 1024 + l * 16) = pf;
    }
  }
  lsig(myFL, 4);
  __builtin_amdgcn_s_setprio(0);
  STMP(5);
  const int trow = mb * 128 + w * 32 + (l & 31);
  u2 oh;
  const unsigned flo = (2 * PAIRAT(r15) + wc) * 128;
  const char* fb = uptr(wsx + WS_OH + mb * 4096);
  int sel = -1;
  {
    const u4 s0 = *(const LAS u4*)(lds + L_SEL), s1 = *(const LAS u4*)(lds + L_SEL + 16);
#pragma unroll
    for (int i = 0; i < 8; ++i) { const int v = (int)(i < 4 ? s0[i & 3] : s1[i & 3]); sel = v == trow ? i : sel; }
  }
  const bool anysel = __ballot(sel >= 0) != 0ull;
  f16v oc[2][2];
  {
    const float m = *(const LAS float*)(lds + L_M + (w * 32 + (l & 31)) * 4), rs = *(const LAS float*)(lds + L_RS + (w * 32 + (l & 31)) * 4);
#pragma unroll
    for (int t = 0; t < 8; ++t) {
      const f4 xf = (f4){lo(xv[t][0]), hi(xv[t][0]), lo(xv[t][1]), hi(xv[t][1])};
      const f4 y = (xf - m) * rs + xf;
#pragma unroll
      for (int i = 0; i < 4; ++i) oc[0][t >> 2][(t & 3) * 4 + i] = y[i];
    }
    asm volatile("" : "+v"(oc[0][0]), "+v"(oc[0][1]));
  }
  unsigned wa[2];
#pragma unroll
  for (int t = 0; t < 2; ++t) wa[t] = ldsb + L_WO + wc * 4096 + (4 * (g4 >> 1) + q) * 128 + (((4 * t + 2 * (g4 & 1) + (p >> 1)) ^ ((q >> 1) << 2)) * 16) + (p & 1) * 8;
  u4 a[RD][4];
#pragma unroll
  for (int k = 0; k < RD; ++k) asm volatile("" : "=v"(a[k][0]), "=v"(a[k][1]), "=v"(a[k][2]), "=v"(a[k][3]));
  const char* ob = uptr(wsx + WS_OF + mb * 262144 + wc * 8192 + wr * 4096);
  const unsigned ov0 = l * 16 + wc * 2048, ov1 = ov0 ^ 2048;
  STMP(6);
  lwait<2>(adFL, 0x30u, 1);
  asm volatile("global_load_dwordx2 %0, %1, %2 sc1\n s_waitcnt vmcnt(0)" : "=&v"(oh) : "v"(flo), "s"(fb) : "memory");
  unsigned seen = (unsigned)__ballot(oh[0] == tag), go = 0;
  auto need = [&](unsigned mask) {
    if (__builtin_expect((go & mask) != mask, 0)) {
      RETRY2();
      _Pragma("unroll 1") for (unsigned it = 0; it < SPIN; ++it) {
        if ((seen & mask) != mask) {
          asm volatile("global_load_dwordx2 %0, %1, %2 sc1\n s_waitcnt vmcnt(0)" : "+v"(oh) : "v"(flo), "s"(fb) : "memory");
          seen = (unsigned)__ballot(oh[0] == tag);
        }
        go = seen & (unsigned)__ballot(oh[1] - NOW() - 1u >= 1000u);
        if ((go & mask) == mask) break;
        SLEEP(1);
      }
    }
  };
#define OX 0
#define OL1(pos, i) do { if (OX == 2 && (pos) >= RD) break; \
    asm volatile("global_load_dwordx4 %0, %1, %2 offset:%3 sc1" : "+v"(a[(pos) % RD][i]) : "v"((i) & 1 ? ov1 : ov0), "s"(ob + (OX == 1 ? ((pos) & 1) : PAIRAT(pos)) * 16384), "i"(((i) >> 1) * 1024) : "memory"); } while (0)
#define OLD(pos) do { OL1(pos, 0); OL1(pos, 1); OL1(pos, 2); OL1(pos, 3); } while (0)
  auto omax = [&](u4(&v)[4]) {
    asm volatile("" : "+v"(v[0]), "+v"(v[1]), "+v"(v[2]), "+v"(v[3]));
    return max(umax3(v[0][0], v[1][0], v[2][0]), v[3][0]);
  };
#define OFIX(pos, m) do { \
    if (__builtin_expect(__ballot((m) == POIS) != 0ull, 0)) { RETRY(); _Pragma("unroll 1") for (unsigned it = 0; it < SPIN; ++it) { SLEEP(4); OLD(pos); WAITV(0); if (__ballot(omax(a[(pos) % RD]) == POIS) == 0ull) break; } } } while (0)
#define OS1(i)
#define OS2(i)
  if (OWN) lwait<1>(adFL, 0x30u, 2);
  __builtin_amdgcn_s_setprio(2);
  go = seen & (unsigned)__ballot(oh[1] - NOW() - 1u >= 1000u);
  need(1u);
  STMP(15);
  u2 oh2 = oh;
  if (__builtin_expect(seen != ~0u, 0)) asm volatile("global_load_dwordx2 %0, %1, %2 sc1" : "+v"(oh2) : "v"(flo), "s"(fb) : "memory");
  OS1(0); OLD(0);
#pragma unroll
  for (int i = 1; i < RD; ++i) { need(1u << i); OS1(i); OLD(i); }
  STMP(10);
  s4 b[8];
#define ORD(pos, f) do { if ((f) < 2 && (pos) == 8) { wa[(f) & 1] += 65536; asm volatile("" : "+v"(wa[(f) & 1])); } \
    RDTR(b[2 * (f)], wa[(f) & 1], ((pos) & 7) * 8192 + ((f) >> 1) * 1024); RDTR(b[2 * (f) + 1], wa[(f) & 1], ((pos) & 7) * 8192 + ((f) >> 1) * 1024 + 2048); } while (0)
#define OMF(pos, f, r) do { if ((f) < 2 && (pos) == 0 && (r) == 1) asm volatile("v_mfma_f32_32x32x16_bf16 %0, %1, %2, 0" : "=&v"(oc[r][(f) & 1]) \
      : "v"(CAT(b[2 * (f)], b[2 * (f) + 1])), "v"(a[(pos) % RD][((f) & 2) + (r)])); \
    else asm volatile("v_mfma_f32_32x32x16_bf16 %0, %1, %2, %0" : "+v"(oc[r][(f) & 1]) \
      : "v"(CAT(b[2 * (f)], b[2 * (f) + 1])), "v"(a[(pos) % RD][((f) & 2) + (r)])); \
    SBAR(); if ((r) == 1 && (pos) < 15) ORD(((pos) + 1) & 15, f); SBAR(); } while (0)
#define ONX(pos) ((pos) > 0 && (pos) + RD - 1 < 16)
#define ONL(pos, i) do { if (ONX(pos)) OL1(((pos) + RD - 1) & 15, i); SBAR(); } while (0)
#define OWL(pos, f) do { WAITL((pos) < 15 ? 6 : 6 - 2 * (f)); SBAR(); } while (0)
#define OHEAD(pos) do { unsigned m_ = 0; \
    OWL(pos, 0); OMF(pos, 0, 0); if (ONX(pos)) { need(1u << (((pos) + RD - 1) & 15)); OS1(((pos) + RD - 1) & 15); } SBAR(); \
    OMF(pos, 0, 1); ONL(pos, 0); \
    OWL(pos, 1); OMF(pos, 1, 0); ONL(pos, 1); \
    OMF(pos, 1, 1); ONL(pos, 2); \
    OWL(pos, 2); OMF(pos, 2, 0); ONL(pos, 3); \
    if ((pos) == 4) { asm volatile("ds_write_b32 %0, %1" ::"v"(myFL), "v"(5u) : "memory"); SBAR(); } \
    if ((pos) == 13) { asm volatile("ds_read_b32 %0, %1" : "=v"(fl3) : "v"(adFL) : "memory"); SBAR(); } \
    OMF(pos, 2, 1); \
    OWL(pos, 3); OMF(pos, 3, 0); \
    if ((pos) < 15) { WAITV(4 * ((pos) + RD - 1 < 16 ? RD - 2 : 14 - (pos))); m_ = omax(a[((pos) + 1) % RD]); asm volatile("" : "+v"(m_)); SBAR(); } \
    OMF(pos, 3, 1); \
    if ((pos) < 15) { OFIX(((pos) + 1) & 15, m_); OS2(((pos) + 1) & 15); } SBAR(); } while (0)
  unsigned fl3;
  ORD(0, 0); ORD(0, 1); ORD(0, 2); ORD(0, 3);
  { WAITV(4 * (RD - 1)); const unsigned m_ = omax(a[0]); OFIX(0, m_); OS2(0);
    asm volatile("" : "+v"(oh2));
    if (oh2[0] == tag) oh = oh2;
    seen = (unsigned)__ballot(oh[0] == tag);
    go = seen & (unsigned)__ballot(oh[1] - NOW() - max(r15 - (RD - 1), 0) * ADV - 1u >= 1000u);
    SBAR(); }
  OHEAD(0); OHEAD(1); STMP(11); OHEAD(2); OHEAD(3); OHEAD(4); OHEAD(5); OHEAD(6); OHEAD(7); STMP(12);
  OHEAD(8); OHEAD(9); STMP(13); OHEAD(10); OHEAD(11); OHEAD(12); OHEAD(13); OHEAD(14);
  f4 xi[8];
  {
    asm volatile("" : "+v"(fl3));
    if (__builtin_expect(((unsigned)__ballot(fl3 < 5u) >> (w ^ 2)) & 1u, 0)) lwait(adFL, 1u << (w ^ 2), 5);
    if (__builtin_expect(((unsigned)__ballot(fl3 < 2u) & 0x30u) != 0u, 0)) lwait(adFL, 0x30u, 2);
    const unsigned xw = ldsb + L_WO + (4 * wr + wc) * 4096 + l * 16, xr = xw ^ 4096;
#define XW(t) asm volatile("ds_write_b128 %0, %1 offset:%2" ::"v"(xw), "v"(__builtin_shufflevector(oc[1][(t) >> 2], oc[1][(t) >> 2], ((t) & 3) * 4, ((t) & 3) * 4 + 1, ((t) & 3) * 4 + 2, ((t) & 3) * 4 + 3)), \
      "i"(((t) >> 2) * 8192 + ((t) & 3) * 1024) : "memory")
    SBAR();
    OWL(15, 0); OMF(15, 0, 1); OWL(15, 1); OMF(15, 1, 1); OWL(15, 2); OMF(15, 2, 1); OWL(15, 3); OMF(15, 3, 1);
    OMF(15, 0, 0); OMF(15, 1, 0); XW(0); XW(1); XW(2); XW(3); SBAR();
    OMF(15, 2, 0); XW(4); XW(5); XW(6); XW(7); SBAR();
    OMF(15, 3, 0);
    asm volatile("ds_write_b32 %0, %1" ::"v"(myFL), "v"(6u) : "memory");
    _Pragma("unroll 1") for (unsigned it = 0; it < SPIN; ++it) {
      unsigned v;
      asm volatile("ds_read_b32 %0, %1" : "=v"(v) : "v"(adFL) : "memory");
#pragma unroll
      for (int t = 0; t < 8; ++t) RD128(xi[t], xr, (t >> 2) * 8192 + (t & 3) * 1024);
      WAITL(0);
      asm volatile("" : "+v"(v), "+v"(xi[0]), "+v"(xi[1]), "+v"(xi[2]), "+v"(xi[3]), "+v"(xi[4]), "+v"(xi[5]), "+v"(xi[6]), "+v"(xi[7]));
      if ((((unsigned)__ballot(v < 6u) >> (w ^ 1)) & 1u) == 0u) break;
    }
    asm volatile("" : "+v"(oc[0][0]), "+v"(oc[0][1]));
#pragma unroll
    for (int t = 0; t < 8; ++t)
#pragma unroll
      for (int i = 0; i < 4; ++i) xi[t][i] += oc[0][t >> 2][(t & 3) * 4 + i];
  }
  STMP(8);
  if (anysel) {
    lwait(adFL, 0xC0u, 3);
    if (sel >= 0) {
#pragma unroll
      for (int t = 0; t < 8; ++t) xi[t] += *(const LAS f4*)(lds + L_KVX + sel * 256 + (8 * t + 4 * (l >> 5)) * 4);
    }
  }
  const __amdgpu_buffer_rsrc_t outr = __builtin_amdgcn_make_buffer_rsrc(out, 0, 2048 * D * 2, 0x00020000);
  if (FULL) {
    LAS char* tb = (LAS char*)lds + L_PQ + w * 4096;
#pragma unroll
    for (int t = 0; t < 8; ++t) *(LAS u2*)(tb + (l & 31) * 128 + ((t ^ (l & 7)) * 16) + (l >> 5) * 8) = (u2){pk2(xi[t][0], xi[t][1]), pk2(xi[t][2], xi[t][3])};
#pragma unroll
    for (int i = 0; i < 4; ++i) {
      const u4 v = *(const LAS u4*)(tb + i * 1024 + (l >> 3) * 128 + (((l ^ (l >> 3)) & 7) * 16));
      __builtin_amdgcn_raw_buffer_store_b128(v, outr, (mb * 128 + w * 32 + i * 8 + (l >> 3)) * 2048 + nb * 128 + (l & 7) * 16, 0, 16);
    }
  } else {
#pragma unroll
  for (int hh = 0; hh < 4; ++hh) {
    const f4 ta = xi[2 * hh], tb = xi[2 * hh + 1];
    auto r0 = __builtin_amdgcn_permlane32_swap(pk2(ta[0], ta[1]), pk2(tb[0], tb[1]), false, false);
    auto r1 = __builtin_amdgcn_permlane32_swap(pk2(ta[2], ta[3]), pk2(tb[2], tb[3]), false, false);
    __builtin_amdgcn_raw_buffer_store_b128(((u4){r0[0], r1[0], r0[1], r1[1]}), outr, (trow * D + nb * 64 + 16 * hh + 8 * (l >> 5)) * 2, 0, 16);
  }
  }
  if (g == 0 && w == 0 && l == 0) __builtin_amdgcn_raw_buffer_store_b32(tag, wse, WS_E, 0, 16);
  STMP(9);
  STFLUSH();
  asm volatile(".Lend_c:");
  return;
  }
  {
    const int lw = w - 4;
    unsigned ev;
    asm volatile("global_load_dword %0, %1, %2 sc1" : "=v"(ev) : "v"(0u), "s"(ws) : "memory");
    unsigned vf[20], vfw[4];
#pragma unroll
    for (int i = 0; i < 8; ++i) { const int row = 8 * (lw * 8 + i) + (l >> 3); vf[i] = row * 2048 + (((l & 7) ^ ((row >> 1) & 7)) * 16) + (3 - (i & 3)) * 1024; }
#pragma unroll
    for (int i = 0; i < 12; ++i) {
      const int pc = lw * 12 + i, row = (pc & 7) * 8 + (l >> 3);
      vf[8 + i] = row * 6144 + (pc >> 3) * 2048 + (((l & 7) ^ ((((row >> 1) & 1) << 2) | (((row >> 3) & 1) << 1))) * 16) + (3 - (i & 3)) * 1024;
    }
#pragma unroll
    for (int i = 0; i < 4; ++i) vfw[i] = (8 * i + (l >> 3)) * 2048 + (((l & 7) ^ (((l >> 4) & 1) << 2)) * 16) + (3 - i) * 1024;
    const char* Ab = uptr((const char*)x + (size_t)mb * 128 * 2048) - 3072;
    const char* Bb = uptr((const char*)wqkv + hp * 128) - 3072;
    const unsigned mba = ldsb + lw * 8192, mbb = ldsb + 16384 + lw * 12288;
#define GL4(v, sb, mm, MO) do { asm volatile("s_add_i32 m0, %2, %3\n s_nop 0\n global_load_lds_dwordx4 %0, %1" ::"v"((v)[0]), "s"(sb), "s"(mm), "i"(MO) : "memory", "scc"); \
      GLI((v)[1], sb, 1024); GLI((v)[2], sb, 2048); GLI((v)[3], sb, 3072); } while (0)
    auto issue = [&](int s, int u0, int u1) {
      const int kt = (s + rot) & 15;
      const char* sa = Ab + kt * 128; const char* sb = Bb + kt * (64 * 6144);
      const unsigned so = (s & 3) * STG, ma = mba + so, mq = mbb + so;
#pragma unroll
      for (int u = u0; u < u1; ++u) { if (u < 2) GL4(vf + 4 * u, sa, ma, u * 4096); else GL4(vf + 4 * u, sb, mq, (u - 2) * 4096); }
    };
    int tk = 0;
#define LBAR() do { BST(tk + 1); BAR(); BST(tk + 21); ++tk; } while (0)
    issue(0, 0, 5); issue(1, 0, 5);
    BST(60);
    WAITV(20); BST(0); BAR(); BST(20);
    asm volatile("" : "+v"(ev));
    const unsigned tag = __builtin_amdgcn_readfirstlane(ev) + 1u;
    MKWS(tag);
    issue(2, 0, 5);
    int s0 = 3;
#pragma unroll 1
    for (; s0 < (sp ? 16 : 6 + ph); ++s0) { issue(s0, 0, 5); BST(tk + 40); WAITV(40); LBAR(); }
#pragma unroll 1
    for (; s0 < 16; s0 += 5) {
      WAITV(20); LBAR();
      WAITV(0); LBAR();
      issue(s0, 0, 5); issue(s0 + 1, 0, U1); BST(tk + 40); WAITV(4 * U1); LBAR();
      issue(s0 + 1, U1, 5); issue(s0 + 2, 0, 5);
      if (s0 + 3 >= 16) break;
      issue(s0 + 3, 0, U2); BST(tk + 40); WAITV(20 + 4 * U2); LBAR();
      issue(s0 + 3, U2, 5);
      if (s0 + 4 >= 16) break;
      issue(s0 + 4, 0, 5); BST(tk + 40); WAITV(40); LBAR();
    }
    int wq = lw;
    auto wo_upto = [&](int lim) {
      _Pragma("unroll 1") for (; wq < lim; wq += 2) { const char* wp = wob - 3072 + HEADAT(wq) * 65536; const unsigned wm = ldsb + wq * 4096; GL4(vfw, wp, wm, L_WO); }
    };
    asm volatile("ds_write_b32 %0, %1" ::"v"(adFL), "v"(0u) : "memory");
    WAITV(0); WAITL(0); LBAR();
    if (lw == 0) ST64(((u2){tag, NOW() + (sp ? KS : KD)}), l == 0 ? WS_HK + (hp * 16 + mb) * 128 : WS_SZ);
    if (WD < 1) wo_upto(WU1);
    BST(15); lwait<LW1>(adFL, 0x0Fu, 1); BST(35);
    if (WD < 2) wo_upto(WU2);
    BST(16); lwait<LW1>(adFL, 0x0Fu, 2); BST(36);
    wo_upto(32);
    STMP(1);
    const int hd = hp * 2 + lw;
    u4 sel[2];
#pragma unroll
    for (int e = 0; e < 2; ++e) {
      const unsigned v = (r15 >> 2) == g4 ? (((r15 & 3) == 2 * e ? 0x3F80u : 0u) | ((r15 & 3) == 2 * e + 1 ? 0x3F800000u : 0u)) : 0u;
      sel[0][e] = v; sel[0][2 + e] = 0u; sel[1][e] = 0u; sel[1][2 + e] = v;
    }
    u4 pv[16][2], zv0, zv1, pq[8];
    const char* kvb = uptr(wsx + WS_KVP + (size_t)hd * 32768);
    const char* zb = uptr(wsx + WS_ZP + (size_t)hd * 2048);
    const char* hb = uptr(wsx + WS_HK + hp * 2048);
    const unsigned lv16 = l * 16, zoff = r15 * 128 + g4 * 32;
    const unsigned pa = ldsb + L_PQ + lw * 4096 + l * 16, omask = lw ? 0x0Au : 0x05u;
    u2 hk;
    _Pragma("unroll 1") for (unsigned it = 0; it < SPIN; ++it) {
      asm volatile("global_load_dwordx2 %0, %1, %2 sc1\n s_waitcnt vmcnt(0)" : "=v"(hk) : "v"(r15 * 128), "s"(hb) : "memory");
      if (__ballot(hk[0] != tag) == 0ull) break;
      SLEEP(2);
    }
    __builtin_amdgcn_s_setprio(3);
    STMP(2);
    const unsigned t0 = NOW();
    unsigned rel = hk[1] - t0; rel = rel < 1000u ? rel : 0u;
    rel = max(rel, (unsigned)__builtin_amdgcn_update_dpp(0, (int)rel, 0xB1, 0xf, 0xf, false));
    rel = max(rel, (unsigned)__builtin_amdgcn_update_dpp(0, (int)rel, 0x4E, 0xf, 0xf, false));
    int gx;
    { const unsigned r0 = __builtin_amdgcn_readlane(rel, 0), r1 = __builtin_amdgcn_readlane(rel, 4), r2 = __builtin_amdgcn_readlane(rel, 8), r3 = __builtin_amdgcn_readlane(rel, 12);
      gx = max(r0, r1) > max(r2, r3) ? (r0 > r1 ? 3 : 2) : (r2 > r3 ? 1 : 0);
      if (OHP) ST64(((u2){tag, t0 + max(max(r0, r1), max(r2, r3)) + OD}), l == 0 ? WS_OH + (mb * 32 + hd) * 128 : WS_SZ); }
#define GLD(gq) do { _Pragma("unroll") for (int i = 0; i < 4; ++i) { const char* sb = kvb + (((gq) ^ gx) * 4 + (i & 2)) * 2048; \
      asm volatile("global_load_dwordx4 %0, %1, %2 offset:%3 sc1" : "=v"(pv[(gq) * 4 + i][0]) : "v"(lv16), "s"(sb), "i"((i & 1) * 2048) : "memory"); \
      asm volatile("global_load_dwordx4 %0, %1, %2 offset:%3 sc1" : "=v"(pv[(gq) * 4 + i][1]) : "v"(lv16), "s"(sb), "i"((i & 1) * 2048 + 1024) : "memory"); } } while (0)
#define ZLD() do { asm volatile("global_load_dwordx4 %0, %1, %2 sc1" : "=v"(zv0) : "v"(zoff), "s"(zb) : "memory"); \
      asm volatile("global_load_dwordx4 %0, %1, %2 offset:16 sc1" : "=v"(zv1) : "v"(zoff), "s"(zb) : "memory"); } while (0)
#pragma unroll
    for (int gq = 0; gq < 4; ++gq) {
      const unsigned rq = __builtin_amdgcn_readlane(rel, 4 * (gq ^ gx));
      _Pragma("unroll 1") for (unsigned it = 0; it < SPIN; ++it) if (NOW() - t0 >= rq) break;
      if (gq == 3) ZLD();
      GLD(gq);
    }
    if (!OHP) ST64(((u2){tag, NOW() + OD}), l == 0 ? WS_OH + (mb * 32 + hd) * 128 : WS_SZ);
    lsig(myFL, 1);
    STMP(5);
    lwait(adFL, omask, 4);
#pragma unroll
    for (int i = 0; i < 8; ++i) RD128(pq[i], pa, (i >> 2) * 8192 + (i & 3) * 1024);
#pragma unroll
    for (int gq = 0; gq < 4; ++gq) {
      SBAR(); WAITV((gq == 0 ? 27 : gq == 1 ? 19 : gq == 2 ? 11 : 1) - OHP);
      _Pragma("unroll 1") for (unsigned it = 0; it < SPIN; ++it) {
#pragma unroll
        for (int i = 0; i < 4; ++i) asm volatile("" : "+v"(pv[gq * 4 + i][0]), "+v"(pv[gq * 4 + i][1]));
        unsigned m = umax3(pv[gq * 4][0][0], pv[gq * 4][1][0], pv[gq * 4 + 1][0][0]);
        m = umax3(m, pv[gq * 4 + 1][1][0], pv[gq * 4 + 2][0][0]);
        m = umax3(m, pv[gq * 4 + 2][1][0], pv[gq * 4 + 3][0][0]);
        m = max(m, pv[gq * 4 + 3][1][0]);
        if (gq == 3) { asm volatile("" : "+v"(zv0), "+v"(zv1)); m = umax3(m, zv0[0], zv1[0]); }
        if (__ballot(m == POIS) == 0ull) break;
        RETRY();
        SLEEP(2);
        if (gq == 3) ZLD();
        GLD(gq);
        WAITV(0);
      }
      f4 ga[4];
#pragma unroll
      for (int i = 0; i < 4; ++i)
#pragma unroll
        for (int t = 0; t < 4; ++t) ga[t] = MFMA16(S8(sel[t & 1]), S8(pv[gq * 4 + i][t >> 1]), i ? ga[t] : (f4)(0.f));
#pragma unroll
      for (int t = 0; t < 4; ++t) pv[gq * 4 + (t >> 1)][t & 1] = __builtin_bit_cast(u4, ga[t]);
    }
    STMP(3);
    WAITL(0);
#pragma unroll
    for (int i = 0; i < 8; i += 2) asm volatile("" : "+v"(pq[i]), "+v"(pq[i + 1]));
    u4 kA[2], zA;
    {
      float z[8];
#pragma unroll
      for (int e = 0; e < 4; ++e) { z[e] = __uint_as_float(zv0[e]); z[4 + e] = __uint_as_float(zv1[e]); }
#define ZR(n) "v_add_f32_dpp %0, %0, %0 row_ror:" #n " row_mask:0xf bank_mask:0xf\n v_add_f32_dpp %1, %1, %1 row_ror:" #n " row_mask:0xf bank_mask:0xf\n" \
              "v_add_f32_dpp %2, %2, %2 row_ror:" #n " row_mask:0xf bank_mask:0xf\n v_add_f32_dpp %3, %3, %3 row_ror:" #n " row_mask:0xf bank_mask:0xf\n" \
              "v_add_f32_dpp %4, %4, %4 row_ror:" #n " row_mask:0xf bank_mask:0xf\n v_add_f32_dpp %5, %5, %5 row_ror:" #n " row_mask:0xf bank_mask:0xf\n" \
              "v_add_f32_dpp %6, %6, %6 row_ror:" #n " row_mask:0xf bank_mask:0xf\n v_add_f32_dpp %7, %7, %7 row_ror:" #n " row_mask:0xf bank_mask:0xf\n"
      asm volatile("s_nop 1\n" ZR(8) ZR(4) ZR(2) ZR(1) : "+v"(z[0]), "+v"(z[1]), "+v"(z[2]), "+v"(z[3]), "+v"(z[4]), "+v"(z[5]), "+v"(z[6]), "+v"(z[7]));
#pragma unroll
      for (int e = 0; e < 4; ++e) zA[e] = pk2(z[2 * e], z[2 * e + 1]);
    }
    float inv[8];
#pragma unroll
    for (int i = 0; i < 8; ++i) { f4 dn = MFMA16(S8(zA), S8(pq[i]), (f4)(0.f)); inv[i] = dn[0]; }
#pragma unroll
    for (int t = 0; t < 4; ++t) {
#define PF(gq) __builtin_bit_cast(f4, pv[(gq) * 4 + (t >> 1)][t & 1])
      f4 s = (PF(0) + PF(1)) + (PF(2) + PF(3));
      kA[t >> 1][(t & 1) * 2] = pk2(s[0], s[1]); kA[t >> 1][(t & 1) * 2 + 1] = pk2(s[2], s[3]);
    }
    f4 o0[8], o1[8];
#pragma unroll
    for (int i = 0; i < 8; ++i) {
      o0[i] = MFMA16(S8(kA[0]), S8(pq[i]), (f4)(0.f)); o1[i] = MFMA16(S8(kA[1]), S8(pq[i]), (f4)(0.f));
      inv[i] = __builtin_amdgcn_rcpf(inv[i] + 1e-6f);
    }
#pragma unroll
    for (int i = 0; i < 8; ++i) {
      o0[i] *= inv[i]; o1[i] *= inv[i];
      u4 o; o[0] = min(pk2(o0[i][0], o0[i][1]), POIS - 1u); o[1] = pk2(o0[i][2], o0[i][3]); o[2] = pk2(o1[i][0], o1[i][1]); o[3] = pk2(o1[i][2], o1[i][3]);
      ST128(o, WS_OF + (mb * 32 + hd) * 8192 + (i >> 1) * 2048 + (i & 1) * 256 + g4 * 512 + r15 * 16);
    }
    lsig(myFL, 2);
    STMP(4);
    STFLUSH();
    asm volatile(".Lend_l:");
  }

}

extern "C" void kernel_launch(void* const* d_in, const int* in_sizes, int n_in, void* d_out, int out_size,
                              void* d_ws, size_t ws_size, hipStream_t stream) {
  (void)hipFuncSetAttribute((const void*)fused, hipFuncAttributeMaxDynamicSharedMemorySize, LDS_SZ);
  fused<<<256, 512, LDS_SZ, stream>>>((const u16*)d_in[0], (const u16*)d_in[1], (const u16*)d_in[2], (const u16*)d_in[4],
                                      (const u16*)d_in[5], (const u16*)d_in[6], (const u16*)d_in[7], (char*)d_ws, (u16*)d_out);
}
```

```cpp
#include <hip/hip_runtime.h>
#include <stdint.h>

typedef unsigned short u16;
typedef short s4 __attribute__((ext_vector_type(4)));
typedef short s8 __attribute__((ext_vector_type(8)));
typedef float f4 __attribute__((ext_vector_type(4)));
typedef float f16v __attribute__((ext_vector_type(16)));
typedef float f2 __attribute__((ext_vector_type(2)));
typedef unsigned u4 __attribute__((ext_vector_type(4)));
typedef unsigned u2 __attribute__((ext_vector_type(2)));
typedef unsigned u8v __attribute__((ext_vector_type(8)));
typedef __bf16 bf2 __attribute__((ext_vector_type(2)));
#define LAS __attribute__((address_space(3)))
#define DEV __device__ __forceinline__

constexpr int D = 1024, F = 3584;
#define NOMOE 0
#define KD 275
#define KS 268
#define OD 195
#define OHP 1
#define FS 128
#define GSL 8
#define YSL 16
#define TP1 0
#define TP2 3
#define TLBW 0
#define FULL 1
#define HPRIO 1
#define OWN 1
#define NOPOIS 0
#define CAP 7
#define RD 4
#define DYN 1
#define JX 0
#define NOSKIP 0
#define GRP 8
#define ADV 15
#define HB 1
#define RT 1
#define WD 0
#define WU1 12
#define WU2 22
#define PL 0
#define PO 0
#define WG 0
#define DS 0
#define D1 16
#define D2 2
#define HKS 2
#define LW1 1
#define HW1 2
#define HS0 0
#define GPV 2
#define XO 1
#define TP0 3
#define G1 0x31
#define G1S 4
#define G2S 8
#define TD 1
#define YD 170
#define WU0 4
#define WE 12
#define HT 14
#define CF 19
#define KL 145
#define OL 115
#define CL 0
#define WS0 0
#define OM 0
#define HG 128
#define U0 0
#define U3 5
#define TL 0
#define MH 0
#define HWT 3
#define YG 0
#define GG 1
#define PV 1
#define U1 4
#define U2 2
#define ROT 0

constexpr unsigned WS_E = 0;
constexpr unsigned WS_YF = 524288;
constexpr unsigned WS_CAND = 12288;
constexpr unsigned WS_RG = 49152;
constexpr unsigned WS_ST = 65536;
constexpr unsigned WS_HK = 589824;
constexpr unsigned WS_ZP = 131072;
constexpr unsigned WS_CS = 196608;
constexpr unsigned WS_OH = 458752;
constexpr unsigned WS_KVP = 1u << 20;
constexpr unsigned WS_GP = 2u << 20;
constexpr unsigned WS_YP = 3u << 20;
constexpr unsigned WS_OF = 5u << 20;
constexpr unsigned WS_SZ = 9u << 20;
#define NR 4
#define RGO(tag) ((64u << 20) + ((tag) & (NR - 1u)) * (10u << 20))
#define MKWS(tag) char* const wsx = ws + RGO(tag); const __amdgpu_buffer_rsrc_t wsr = __builtin_amdgcn_make_buffer_rsrc(wsx, 0, WS_SZ, 0x00020000)

constexpr int STG = 40960;
constexpr int L_KVX = 0;
constexpr int L_PQ = 12288;
constexpr int L_M = 28672, L_RS = L_M + 512, L_CS = L_RS + 512, L_SEL = L_CS + 768, L_FL = L_SEL + 64;
constexpr int L_HX = 30720, L_WO = 32768;
constexpr int LDS_SZ = 163840;
constexpr unsigned POIS = 0xFFFFFFFFu;

DEV unsigned pk2(float a, float b) { bf2 v; v[0] = (__bf16)a; v[1] = (__bf16)b; return __builtin_bit_cast(unsigned, v); }
DEV float lo(unsigned u) { return __uint_as_float(u << 16); }
DEV float hi(unsigned u) { return __uint_as_float(u & 0xFFFF0000u); }
DEV float rbf(float a) { return lo(pk2(a, a)); }
DEV float dot2(unsigned a, unsigned b, float c) { return __builtin_amdgcn_fdot2_f32_bf16(__builtin_bit_cast(bf2, a), __builtin_bit_cast(bf2, b), c, false); }
#define RD128(dst, addr, OFF) asm volatile("ds_read_b128 %0, %1 offset:%2" : "=v"(dst) : "v"(addr), "i"(OFF) : "memory")
#define RDTR(dst, addr, OFF) asm volatile("ds_read_b64_tr_b16 %0, %1 offset:%2" : "=v"(dst) : "v"(addr), "i"(OFF) : "memory")
#define SBAR() __builtin_amdgcn_sched_barrier(0)
#define CAT(x, y) __builtin_shufflevector(x, y, 0, 1, 2, 3, 4, 5, 6, 7)
#define WAITV(n) asm volatile("s_waitcnt vmcnt(%0)" ::"i"(n) : "memory")
#define WAITL(n) asm volatile("s_waitcnt lgkmcnt(%0)" ::"i"(n) : "memory")
#define MEMB() asm volatile("" ::: "memory")
#define SLEEP(n) asm volatile("s_sleep %0" ::"i"(n) : "memory")
#define BAR() asm volatile("s_barrier" ::: "memory")
#define GL0(vo, sb, m0v) asm volatile("s_mov_b32 m0, %2\n s_nop 0\n global_load_lds_dwordx4 %0, %1" ::"v"(vo), "s"(sb), "s"(m0v) : "memory")
#define GLI(vo, sb, OFF) asm volatile("global_load_lds_dwordx4 %0, %1 offset:%2" ::"v"(vo), "s"(sb), "i"(OFF) : "memory")
#define GL4(v, sb, mm, MO) do { asm volatile("s_add_i32 m0, %2, %3\n s_nop 0\n global_load_lds_dwordx4 %0, %1" ::"v"((v)[0]), "s"(sb), "s"(mm), "i"(MO) : "memory", "scc"); \
      GLI((v)[1], sb, 1024); GLI((v)[2], sb, 2048); GLI((v)[3], sb, 3072); } while (0)
#define VFW(i) ((8 * (i) + (l >> 3)) * 2048 + (((l & 7) ^ (((l >> 4) & 1) << 2)) * 16) + (3 - (i)) * 1024)
#define MFMA16(a, b, c) __builtin_amdgcn_mfma_f32_16x16x32_bf16(a, b, c, 0, 0, 0)
#define S8(x) __builtin_bit_cast(s8, x)
#define LD128(off) __builtin_amdgcn_raw_buffer_load_b128(wsr, (off), 0, 16)
#define LD64(off) __builtin_amdgcn_raw_buffer_load_b64(wsr, (off), 0, 16)
#define ST128(v, off) __builtin_amdgcn_raw_buffer_store_b128((v), wsr, (off), 0, 16)
#define ST64(v, off) __builtin_amdgcn_raw_buffer_store_b64((v), wsr, (off), 0, 16)
#define OFA(i) (WS_OF + ((mb * 32 + hd) * 8 + (i)) * 1024 + l * 16)
#define POISON(i) ST128(pz, (i) < 2 ? WS_KVP + (hd * 16 + mb) * 2048 + (i) * 1024 + l * 16 : (i) == 2 ? WS_ZP + (hd * 16 + mb) * 128 + (l & 7) * 16 : OFA((i) - 3))
#define NOW() ((unsigned)__builtin_amdgcn_s_memrealtime())
constexpr unsigned SPIN = 1u << 21;
DEV const char* uptr(const char* p) {
  unsigned long long v = (unsigned long long)p;
  unsigned lo_ = __builtin_amdgcn_readfirstlane((unsigned)v), hi_ = __builtin_amdgcn_readfirstlane((unsigned)(v >> 32));
  return (const char*)(((unsigned long long)hi_ << 32) | lo_);
}
DEV void lsig(unsigned addr, unsigned val) { asm volatile("s_waitcnt lgkmcnt(0)\n ds_write_b32 %0, %1" ::"v"(addr), "v"(val) : "memory"); }
template <int SL = 0> DEV void lwait(unsigned addr, unsigned mask, unsigned target) {
  _Pragma("unroll 1") for (unsigned it = 0; it < SPIN; ++it) {
    unsigned v;
    asm volatile("ds_read_b32 %0, %1\n s_waitcnt lgkmcnt(0)" : "=v"(v) : "v"(addr) : "memory");
    if (((unsigned)__ballot(v < target) & mask) == 0u) break;
    if (SL) SLEEP(SL);
  }
}
DEV unsigned nopois(unsigned a) { return min(a, POIS - 1u); }
DEV u4 tagf(f4 v, unsigned tag) {
  u4 r;
#pragma unroll
  for (int e = 0; e < 4; ++e) r[e] = __builtin_amdgcn_perm(__float_as_uint(v[e]), tag, 0x07060500u + e);
  return r;
}
DEV void untag(u4& v, unsigned tag, unsigned& bad) {
#pragma unroll
  for (int e = 0; e < 4; ++e) v[e] ^= (tag >> (8 * e)) & 255u;
  bad |= v[0] | v[1]; bad |= v[2] | v[3];
}
DEV unsigned umax3(unsigned a, unsigned b, unsigned c) { return max(max(a, b), c); }
template <int C> DEV float dppf(float v) { return __int_as_float(__builtin_amdgcn_update_dpp(0, __float_as_int(v), C, 0xf, 0xf, false)); }
DEV unsigned wave_max(unsigned v) {
  v = max(v, (unsigned)__builtin_amdgcn_update_dpp(0, (int)v, 0x111, 0xf, 0xf, false));
  v = max(v, (unsigned)__builtin_amdgcn_update_dpp(0, (int)v, 0x112, 0xf, 0xf, false));
  v = max(v, (unsigned)__builtin_amdgcn_update_dpp(0, (int)v, 0x114, 0xf, 0xf, false));
  v = max(v, (unsigned)__builtin_amdgcn_update_dpp(0, (int)v, 0x118, 0xf, 0xf, false));
  v = max(v, (unsigned)__builtin_amdgcn_update_dpp(0, (int)v, 0x142, 0xa, 0xf, false));
  v = max(v, (unsigned)__builtin_amdgcn_update_dpp(0, (int)v, 0x143, 0xc, 0xf, false));
  return (unsigned)__builtin_amdgcn_readlane((int)v, 63);
}
DEV unsigned phi2(f2 u, f2 rs) {
  f2 t = u * rs;
  f2 a = t * 1.44269504f, p = t + 1.f, r;
  r[0] = __builtin_amdgcn_fmed3f(__builtin_amdgcn_exp2f(a[0]), p[0], 1.f);
  r[1] = __builtin_amdgcn_fmed3f(__builtin_amdgcn_exp2f(a[1]), p[1], 1.f);
  r += 1e-6f;
  return pk2(r[0], r[1]);
}

#define STDECL
#undef HB
#define HB 0
#define BSTV cw
#define STMP(i)
#define BST(i)
#define STFLUSH()
#define RETRY()
#define RETRY2()

DEV float half_sum(float v) {
  asm volatile("s_nop 1\n v_add_f32_dpp %0, %0, %0 row_ror:8 row_mask:0xf bank_mask:0xf\n s_nop 1\n v_add_f32_dpp %0, %0, %0 row_ror:4 row_mask:0xf bank_mask:0xf\n"
               "s_nop 1\n v_add_f32_dpp %0, %0, %0 row_ror:2 row_mask:0xf bank_mask:0xf\n s_nop 1\n v_add_f32_dpp %0, %0, %0 row_ror:1 row_mask:0xf bank_mask:0xf\n s_nop 1" : "+v"(v));
  auto r = __builtin_amdgcn_permlane16_swap(__float_as_uint(v), __float_as_uint(v), false, false);
  return __uint_as_float(r[0]) + __uint_as_float(r[1]);
}

struct FQ { s8 a[4]; s4 b[12]; };
template <bool B> struct BC { static constexpr bool v = B; };
template <int I> struct IC { static constexpr int v = I; };

__global__ __launch_bounds__(512) void fused(const u16* __restrict__ x, const u16* __restrict__ wqkv, const u16* __restrict__ wo,
                                             const u16* __restrict__ wrt, const u16* __restrict__ wg, const u16* __restrict__ wu,
                                             const u16* __restrict__ wd, char* __restrict__ ws, u16* __restrict__ out) {
  extern __shared__ __attribute__((aligned(16))) char lds[];
  asm volatile(".Lcode_beg:");
  const int w = __builtin_amdgcn_readfirstlane(threadIdx.x >> 6), l = threadIdx.x & 63;
  STDECL;
  STMP(0);
  const int g = blockIdx.x, xcd = g & 7, j = g >> 3, ii = j >> 2;
  unsigned cw = 0, cwo = 0; unsigned long long cwb = 0;
  if (w == 7) {
    unsigned long long pc; unsigned off, len, l1, l2, l3;
    asm volatile("s_getpc_b64 %0\n.Lcode_here:" : "=s"(pc));
    asm volatile("s_mov_b32 %0, (.Lcode_here - .Lcode_beg)" : "=s"(off));
    asm volatile("s_mov_b32 %0, (.Lend_c - .Lcode_beg)" : "=s"(len));
    asm volatile("s_mov_b32 %0, (.Lend_l - .Lcode_beg)" : "=s"(l1));
    asm volatile("s_mov_b32 %0, (.Lend_h - .Lcode_beg)" : "=s"(l2));
    asm volatile("s_mov_b32 %0, (.Lend_s - .Lcode_beg)" : "=s"(l3));
    len = max(max(len, l3), max(l1, l2));
    const unsigned o = min((unsigned)(l * 32 + j) * 64u, len - 4u);
    asm volatile("global_load_dword %0, %1, %2" : "+v"(cw) : "v"(o), "s"(pc - off) : "memory");
    cwo = o; cwb = pc - off;
  }
#define PTAB 0xA98FEDC1045B7623ull
#define PHT 0x6624924999924924ull
#define CX 4
  const int mb = (xcd & 1) * 8 + ii, hp = (xcd >> 1) * 4 + (j & 3);
  const bool sp = (hp & 7) == 3;
  const int rot = 2 * xcd + 4 * (ii & 3);
  const int nb = hp;
#define PAIRAT(p) ((int)(PTAB >> (4 * (p))) & 15)
#define HEADAT(s) (2 * PAIRAT((s) >> 1) + ((s) & 1))
  const int ph = (int)(PHT >> (2 * j)) & 3;
#define HQT 0xDCBA098765320410ull
#define CXA 1
  const int bq = ((int)(HQT >> (4 * hp)) & 15) * 4 + (ii & 3), ksp = (xcd & 1) * 2 + (ii >> 2);
#define CHUNK(b) ((b) < 14 ? 4 * (b) + 3 : ((b) - 14) + ((((b) - 14) * 43) >> 7))
  const int chunk = CHUNK(bq), bg = (CXA || bq < 14) ? bq ^ (w & 1) : bq, chg = CHUNK(bg);
  const unsigned ldsb = (unsigned)(size_t)(LAS char*)lds;
  const unsigned adFL = ldsb + L_FL + 4 * (l & 7), myFL = ldsb + L_FL + 4 * w;
  const __amdgpu_buffer_rsrc_t wse = __builtin_amdgcn_make_buffer_rsrc(ws, 0, 64, 0x00020000);
  const int r15 = l & 15, g4 = l >> 4, q = (l & 15) >> 2, p = l & 3;
  const char* wob = uptr((const char*)wo + nb * 128);

  if (w != 4 && w != 5) {
  const unsigned ev0 = __builtin_amdgcn_raw_buffer_load_b32(wse, WS_E, 0, 16);
  if (w >= 6) {
    const int mw = w - 6;
    auto helper = [&](auto SP) {
    constexpr bool spare = decltype(SP)::v, moe = !spare && !NOMOE;
    int nbar = 0;
#define TICK() do { if (HB) { BST(nbar); asm volatile("" : "+v"(BSTV)); } SBAR(); BAR(); SBAR(); ++nbar; } while (0)
#define DTICK(lim) do { if (nbar < (lim)) TICK(); else SLEEP(2); } while (0)
    const int hd = hp * 2 + mw;
    const u4 pz = (u4)(POIS);
    const unsigned tag = __builtin_amdgcn_readfirstlane(ev0) + 1u;
    MKWS(tag);
    const int np = l & 7, kset = (l >> 3) & 1;
    u4 gw[4][8], dw[2][8];
    const __amdgpu_buffer_rsrc_t wgr = __builtin_amdgcn_make_buffer_rsrc((void*)(mw ? wu : wg), 0, D * F * 2, 0x00020000);
    const __amdgpu_buffer_rsrc_t wdr = __builtin_amdgcn_make_buffer_rsrc((void*)wd, 0, D * F * 2, 0x00020000);
    const __amdgpu_buffer_rsrc_t wor = __builtin_amdgcn_make_buffer_rsrc((void*)wo, 0, D * D * 2, 0x00020000);
    const unsigned gvo = ((256 * ksp + 32 * kset + 8 * g4) * F + 64 * chg + 8 * np) * 2;
    const unsigned dvo = ((64 * chunk + 8 * g4) * D + 256 * ksp + 128 * mw + 8 * r15) * 2;
    auto burst = [&](int k, int i0 = 0, int i1 = 16) {
      if (moe) {
#pragma unroll
        for (int i = i0; i < i1; ++i) {
          if (k < 2) gw[(2 * k + (i >> 3)) & 3][i & 7] = __builtin_amdgcn_raw_buffer_load_b128(wgr, gvo, (128 * k + 64 * (i >> 3) + (i & 7)) * F * 2, 2);
          else dw[i >> 3][i & 7] = __builtin_amdgcn_raw_buffer_load_b128(wdr, dvo, (32 * (i >> 3) + (i & 7)) * D * 2, 2);
        }
      }
    };
    auto transp = [&](u4(&v)[8]) {
      if (moe) {
        u4 f[8];
#pragma unroll
        for (int c = 0; c < 8; ++c)
#pragma unroll
          for (int e = 0; e < 4; ++e) {
            const unsigned ev = v[2 * e][c >> 1], od = v[2 * e + 1][c >> 1];
            f[c][e] = (c & 1) ? __builtin_amdgcn_perm(od, ev, 0x07060302u) : __builtin_amdgcn_perm(od, ev, 0x05040100u);
          }
#pragma unroll
        for (int c = 0; c < 8; ++c) { v[c] = f[c]; asm volatile("" : "+v"(v[c])); }
      }
    };
    auto pois = [&]() {
      ST128(pz, WS_KVP + (hd * 16 + mb) * 2048 + l * 16); ST128(pz, WS_KVP + (hd * 16 + mb) * 2048 + 1024 + l * 16); ST128(pz, WS_ZP + (hd * 16 + mb) * 128 + (l & 7) * 16);
    };
    unsigned wt0, wt1, wt2 = 0, wt3 = 0;
    auto touch = [&]() {
      const unsigned to = (j * 32 + mw * 16 + (l >> 3)) * 2048 + (xcd >> 1) * 512 + (l & 7) * 64;
      wt0 = __builtin_amdgcn_raw_buffer_load_b32(wor, to, 0, 0); wt1 = __builtin_amdgcn_raw_buffer_load_b32(wor, to, 16384, 0);
      if (TLBW) {
        const __amdgpu_buffer_rsrc_t outr = __builtin_amdgcn_make_buffer_rsrc(out, 0, 2048 * D * 2, 0x00020000);
        wt2 = __builtin_amdgcn_raw_buffer_load_b32(outr, (mb * 128 + mw * 64 + (l & 3) * 16) * 2048 + nb * 128, 0, 0);
        wt3 = __builtin_amdgcn_raw_buffer_load_b32(wsr, (l & 1) ? WS_GP + g * 4096 + mw * 2048 : WS_YP + (g >> 2) * 32768 + (l & 2) * 8192, 0, 0);
      }
    };
    s4 sb[24];
    auto colrd = [&](int h) {
      const unsigned bo = ldsb + (1 + h) * STG + 16384 + (8 * g4 + q) * 128 + (p & 1) * 8;
      const int sw = ((q >> 1) << 2) | ((g4 & 1) << 1);
      const unsigned b0 = bo + (((mw * 4 + (p >> 1)) ^ sw) * 16), b1 = bo + (((mw * 4 + 2 + (p >> 1)) ^ sw) * 16);
#pragma unroll
      for (int n = 0; n < 12; ++n) { RDTR(sb[2 * n], (n & 1) ? b1 : b0, ((n % 6) >> 1) * 8192 + (n / 6) * 4096); RDTR(sb[2 * n + 1], (n & 1) ? b1 : b0, ((n % 6) >> 1) * 8192 + (n / 6) * 4096 + 512); }
      WAITL(0);
      SBAR();
    };
    auto colsum = [&]() {
      const u4 ones = (u4)(0x3F803F80u);
      f4 c[6];
#pragma unroll
      for (int n = 0; n < 12; ++n) c[n % 6] = MFMA16(S8(ones), CAT(sb[2 * n], sb[2 * n + 1]), n >= 6 ? c[n % 6] : (f4)(0.f));
      const u4 csg = (u4){__float_as_uint(g4 ? c[3][0] : c[0][0]), __float_as_uint(g4 ? c[4][0] : c[1][0]), __float_as_uint(g4 ? c[5][0] : c[2][0]), tag};
      WAITV(0);
      ST128(csg, l < 32 ? WS_CS + (hd * 16 + mb) * 512 + l * 16 : WS_SZ);
    };
    u4 stv, cv[8], cd; float cs[3];
    auto gload = [&]() {
      stv = LD128(WS_ST + (mb * 128 + mw * 64 + l) * 16);
#pragma unroll
      for (int i = 0; i < 8; ++i) cv[i] = LD128(WS_CS + hd * 8192 + i * 1024 + l * 16);
    };
    auto gbad = [&]() {
      unsigned bad = (stv[1] ^ tag) | (stv[3] ^ tag);
#pragma unroll
      for (int i = 0; i < 8; ++i) bad |= cv[i][3] ^ tag;
      return __ballot(bad != 0u) != 0ull;
    };
    int idx;
    auto gfin = [&]() {
      if (gbad()) { RETRY(); _Pragma("unroll 1") for (unsigned it = 0; it < SPIN; ++it) { SLEEP(2); gload(); if (!gbad()) break; } }
#pragma unroll
      for (int e = 0; e < 3; ++e) {
        float a = __uint_as_float(cv[0][e]);
#pragma unroll
        for (int i = 1; i < 8; ++i) a += __uint_as_float(cv[i][e]);
        auto r = __builtin_amdgcn_permlane32_swap(__float_as_uint(a), __float_as_uint(a), false, false);
        cs[e] = __uint_as_float(r[0]) + __uint_as_float(r[1]);
      }
      asm volatile("" : "+v"(cs[0]), "+v"(cs[1]), "+v"(cs[2]) : "v"(wt0), "v"(wt1), "v"(wt2), "v"(wt3));
    };
    auto cload = [&]() { cd = LD128(WS_CAND + (((hp >> 2) == 2) * 16 + mb) * 128 + (l & 7) * 16); };
    auto cfin = [&]() {
      if (__builtin_expect(__ballot(cd[1] != tag || cd[3] != tag) != 0ull, 0)) {
        RETRY();
        _Pragma("unroll 1") for (unsigned it = 0; it < SPIN; ++it) { SLEEP(4); cload(); if (__ballot(cd[1] != tag || cd[3] != tag) == 0ull) break; }
      }
      cd[0] = max(cd[0], cd[2]);
      idx = (int)(cd[0] & 2047u);
    };
    auto lpub = [&]() {
      *(LAS float*)(lds + L_M + (mw * 64 + l) * 4) = __uint_as_float(stv[0]);
      *(LAS float*)(lds + L_RS + (mw * 64 + l) * 4) = __uint_as_float(stv[2]);
      if (l < 32) {
#pragma unroll
        for (int e = 0; e < 3; ++e) *(LAS float*)(lds + L_CS + mw * 384 + ((3 * g4 + e) * 16 + r15) * 4) = cs[e];
      }
      if (mw == 0 && l < 8) *(LAS int*)(lds + L_SEL + l * 4) = idx;
      lsig(myFL, 1u);
    };
    u4 xs[4]; float xrs;
    asm volatile("" ::"s"(wg), "s"(wu), "s"(wd), "v"(cw));
    if (HPRIO) __builtin_amdgcn_s_setprio(HPRIO);
    TICK();
    if (spare) {
      TICK();
      pois();
      if (ii >> 2) TICK();
      colrd(ii >> 2);
      if (!(ii >> 2)) TICK();
      u4 kk[8];
      auto kload = [&]() {
#pragma unroll
        for (int i = 0; i < 8; ++i) kk[i] = LD128(WS_RG + mw * 8192 + i * 1024 + l * 16);
      };
      kload();
      TICK();
      _Pragma("unroll 1") for (unsigned it = 0; it < SPIN; ++it) {
        unsigned bad = 0;
#pragma unroll
        for (int i = 0; i < 8; ++i) { bad |= (kk[i][1] ^ tag) | (kk[i][3] ^ tag); asm volatile("" : "+v"(bad)); }
        if (__ballot(bad != 0u) == 0ull) break;
        RETRY();
        kload();
        DTICK(7);
      }
      __builtin_amdgcn_s_setprio(3);
      unsigned m1 = max(kk[0][0], kk[0][2]), m2 = min(kk[0][0], kk[0][2]), m3 = 0;
#pragma unroll
      for (int i = 2; i < 16; ++i) {
        const unsigned a = kk[i >> 1][(i & 1) * 2];
        asm("v_med3_u32 %0, %1, %2, %0" : "+v"(m3) : "v"(m2), "v"(a));
        asm("v_med3_u32 %0, %1, %2, %0" : "+v"(m2) : "v"(m1), "v"(a));
        m1 = max(m1, a);
      }
      unsigned mine8 = 0;
#pragma unroll
      for (int it = 0; it < 8; ++it) {
        const unsigned km = wave_max(m1);
        mine8 = l == it ? km : mine8;
        const bool win = m1 == km;
        if (it < 7) { m1 = win ? m2 : m1; m2 = win ? m3 : m2; m3 = win ? 0u : m3; }
        if (it == 3 && nbar < 7) TICK();
      }
      if (__builtin_expect(__ballot(m1 == 0u) != 0ull, 0)) {
        unsigned lim = 0;
#pragma unroll 1
        for (int it = 0; it < 8; ++it) {
          unsigned m = 0;
#pragma unroll
          for (int i = 0; i < 8; ++i) m = umax3(m, kk[i][0] - lim, kk[i][2] - lim);
          lim += wave_max(m);
          mine8 = l == it ? lim : mine8;
        }
      }
      ST64(((u2){mine8, tag}), l < 8 ? WS_CAND + ((hp >> 3) * 16 + mb) * 128 + (mw ? 7 - l : l) * 16 + mw * 8 : WS_SZ);
      __builtin_amdgcn_s_setprio(HPRIO);
      STMP(2);
      TICK();
      colsum();
      DTICK(9);
      touch();
      while (nbar < 10) TICK();
      gload(); cload();
      if (RT && mw) asm volatile("global_load_dword %0, %1, %2 sc1" : "+v"(cw) : "v"(cwo), "s"(cwb) : "memory");
      TICK(); TICK();
      WAITV(0);
      asm volatile("" : "+v"(cw));
      gfin(); cfin();
      STMP(3);
      TICK(); TICK(); TICK();
      lpub();
    } else {
      if (PV == 0) pois();
      TICK();
      if (PV == 1) pois();
      if (ii >> 2) TICK();
      colrd(ii >> 2);
      TICK();
      if (CL && !(ii >> 2)) TICK();
      colsum();
      if (!CL && !(ii >> 2)) TICK();
      u4 sv; f4 acc[8];
      auto xload = [&]() {
        const u16* xp = x + (size_t)idx * D + 256 * ksp + 32 * kset + 8 * g4;
        sv = LD128(WS_ST + idx * 16);
#pragma unroll
        for (int it = 0; it < 4; ++it) xs[it] = *(const u4*)(xp + 64 * it);
        SBAR();
      };
      auto xnorm = [&]() {
        if (__builtin_expect(__ballot(sv[1] != tag || sv[3] != tag) != 0ull, 0)) {
          _Pragma("unroll 1") for (unsigned it = 0; it < SPIN; ++it) { SLEEP(1); sv = LD128(WS_ST + idx * 16); if (__ballot(sv[1] != tag || sv[3] != tag) == 0ull) break; }
        }
        const float nm = -__uint_as_float(sv[0]);
        xrs = __uint_as_float(sv[2]);
#pragma unroll
        for (int it = 0; it < 4; ++it) {
          float t0, t1, t2, t3; unsigned x0 = xs[it][0], x1 = xs[it][1], x2 = xs[it][2], x3 = xs[it][3];
          asm volatile("v_dot2_f32_bf16 %4, %0, %8, %10\n v_dot2_f32_bf16 %0, %0, %9, %10\n v_dot2_f32_bf16 %5, %1, %8, %10\n v_dot2_f32_bf16 %1, %1, %9, %10\n"
                       "v_dot2_f32_bf16 %6, %2, %8, %10\n v_dot2_f32_bf16 %2, %2, %9, %10\n v_dot2_f32_bf16 %7, %3, %8, %10\n v_dot2_f32_bf16 %3, %3, %9, %10\n"
                       "v_cvt_pk_bf16_f32 %0, %4, %0\n v_cvt_pk_bf16_f32 %1, %5, %1\n v_cvt_pk_bf16_f32 %2, %6, %2\n s_nop 0\n v_cvt_pk_bf16_f32 %3, %7, %3"
                       : "+v"(x0), "+v"(x1), "+v"(x2), "+v"(x3), "=&v"(t0), "=&v"(t1), "=&v"(t2), "=&v"(t3) : "s"(0x00003F80u), "s"(0x3F800000u), "v"(nm));
          xs[it] = (u4){x0, x1, x2, x3};
        }
        asm volatile("s_nop 1" : "+v"(xs[0]), "+v"(xs[1]), "+v"(xs[2]), "+v"(xs[3]), "+v"(xrs));
        SBAR();
      };
      auto gmul = [&](int i0, int i1) {
#pragma unroll
        for (int it = i0; it < i1; ++it)
#pragma unroll
          for (int c = 0; c < 8; ++c) acc[c] = MFMA16(S8(gw[it][c]), S8(xs[it]), it ? acc[c] : (f4)(0.f));
#pragma unroll
        for (int c = 0; c < 8; ++c) asm volatile("" : "+v"(acc[c]));
        SBAR();
      };
      auto gout = [&]() {
        unsigned go = r15 < 8 ? WS_GP + ((chg * 4 + ksp) * 2 + mw) * 2048 + (g4 >> 1) * 1024 + (g4 & 1) * 512 + r15 * 16 : WS_SZ;
        asm volatile("" : "+v"(go));
#pragma unroll
        for (int i = 0; i < 4; ++i) {
          unsigned a[4], b[4];
#pragma unroll
          for (int c = 0; c < 4; ++c) {
            auto r = __builtin_amdgcn_permlane32_swap(__float_as_uint(acc[c][i]), __float_as_uint(acc[c + 4][i]), false, false);
            a[c] = r[0]; b[c] = r[1];
          }
          unsigned s0, s1, s2, s3;
#define AR(d, x, y) "v_add_f32_dpp %" #d ", %" #x ", %" #y " row_ror:8 row_mask:0xf bank_mask:0xf\n"
          asm volatile("s_nop 1\n" AR(0, 8, 4) AR(1, 9, 5) AR(2, 10, 6) AR(3, 11, 7) : "=&v"(s0), "=&v"(s1), "=&v"(s2), "=&v"(s3)
                       : "v"(a[0]), "v"(a[1]), "v"(a[2]), "v"(a[3]), "v"(b[0]), "v"(b[1]), "v"(b[2]), "v"(b[3]));
          ST128(tagf(__builtin_bit_cast(f4, ((u4){s0, s1, s2, s3})), tag), go + i * 128);
        }
        SBAR();
      };
      for (int i = 0; i < ph; ++i) TICK();
      burst(0); touch();
      TICK();
      TICK();
      if (TL) { transp(gw[0]); transp(gw[1]); }
      TICK();
      if (!TL) transp(gw[0]);
      TICK();
      if (!TL) transp(gw[1]);
      TICK();
      if (ph) cload();
      if (RT && mw) asm volatile("global_load_dword %0, %1, %2 sc1" : "+v"(cw) : "v"(cwo), "s"(cwb) : "memory");
      SBAR();
      burst(1);
      TICK();
      TICK(); TICK();
      transp(gw[2]);
      WAITV(0);
      asm volatile("" : "+v"(cw));
      if (ph < 2) TICK();
      transp(gw[3]);
      if (ph < 1) { SBAR(); cload(); TICK(); }
      SBAR();
      if (XO == 0) { cfin(); xload(); gload(); } else { gload(); SBAR(); cfin(); xload(); }
      TICK();
      gfin();
      lpub();
      SBAR();
      __builtin_amdgcn_s_setprio(0);
      burst(2, 0, D1);
      STMP(3);
      SBAR();
      if (HS0) SLEEP(HS0);
      if (G1 & 1) { lwait<HW1>(adFL, 0x0Fu, G1 >> 4); if (G1S) SLEEP(G1S); __builtin_amdgcn_s_setprio(TP0); }
      xnorm();
      if (G1 & 2) { lwait<2>(adFL, 0x0Fu, G1 >> 4); if (G1S) SLEEP(G1S); __builtin_amdgcn_s_setprio(TP0); }
      STMP(5);
      gmul(0, 4);
      gout();
      STMP(6);
      SBAR();
    }
    __builtin_amdgcn_s_setprio(TP1);
    STMP(4);
    if (moe) {
      if (D2 == 0) { burst(2, D1, 16); SBAR(); }
      if (D1 >= 8 || D2 == 0) transp(dw[0]); if (D1 == 16 || D2 == 0) transp(dw[1]);
      SBAR();
      lwait<2>(adFL, 0x30u, 3);
      if (G2S) SLEEP(G2S);
      STMP(7);
      if (D2 == 1) { burst(2, D1, 16); SBAR(); }
      f4 acc[8];
      u4 ha[2];
      {
        u4 pg[4][2];
        auto pload = [&]() {
#pragma unroll
          for (int ks = 0; ks < 4; ++ks)
#pragma unroll
            for (int t = 0; t < 2; ++t) pg[ks][t] = LD128(WS_GP + ((chunk * 4 + ks) * 4 + 2 * t + mw) * 1024 + (4 * kset + g4) * 128 + np * 16);
        };
        auto pbad = [&]() {
          unsigned bad = 0;
#pragma unroll
          for (int i = 0; i < 8; ++i) untag(pg[i >> 1][i & 1], tag, bad);
          return __ballot((bad & 255u) != 0u) != 0ull;
        };
        pload();
        SBAR();
        if (D2 == 2) { burst(2, D1, 16); SBAR(); }
        __builtin_amdgcn_s_setprio(TP2);
        if (D1 < 8 && D2 == 1) transp(dw[0]);
        if (D1 < 16 && D2 == 1) transp(dw[1]);
        SBAR();
        if (__builtin_expect(pbad(), 0)) { _Pragma("unroll 1") for (unsigned it = 0; it < SPIN; ++it) { RETRY2(); SLEEP(4); pload(); if (!pbad()) break; } }
        STMP(10);
        f4 gq[2];
#pragma unroll
        for (int t = 0; t < 2; ++t)
          gq[t] = ((__builtin_bit_cast(f4, pg[0][t]) + __builtin_bit_cast(f4, pg[1][t])) + (__builtin_bit_cast(f4, pg[2][t]) + __builtin_bit_cast(f4, pg[3][t]))) * xrs;
        float hv[4];
#pragma unroll
        for (int e = 0; e < 4; ++e) {
          const float gg = rbf(gq[0][e]), uu = rbf(gq[1][e]);
          hv[e] = rbf(gg / (1.f + __expf(-gg))) * uu;
        }
        *(LAS u2*)(lds + L_HX + l * 16 + mw * 8) = (u2){pk2(hv[0], hv[1]), pk2(hv[2], hv[3])};
        lsig(myFL, 2u);
        if (D1 < 16 && D2 == 2) { SBAR(); if (D1 < 8) transp(dw[0]); transp(dw[1]); SBAR(); }
        lwait(adFL, 0xC0u, 2);
        ha[0] = *(const LAS u4*)(lds + L_HX + l * 16); ha[1] = *(const LAS u4*)(lds + L_HX + (l ^ 8) * 16);
      }
#pragma unroll
      for (int it = 0; it < 2; ++it)
#pragma unroll
        for (int c = 0; c < 8; ++c) acc[c] = MFMA16(S8(ha[it]), S8(dw[it][c]), it ? acc[c] : (f4)(0.f));
      if (g4 < 2) {
#pragma unroll
        for (int i = 0; i < 4; ++i) {
          const unsigned yo = WS_YP + ((chunk * 8 + 4 * g4 + i) * D + 256 * ksp + 128 * mw + 4 * r15) * 4;
          ST128(tagf((f4){acc[0][i], acc[1][i], acc[2][i], acc[3][i]}, tag), yo);
          ST128(tagf((f4){acc[4][i], acc[5][i], acc[6][i], acc[7][i]}, tag), yo + 256);
        }
      }
      STMP(11);
    }
    float sg;
    {
      const unsigned mono = cd[0] >> 16, rb = (mono & 0x8000u) ? (mono & 0x7FFFu) : (~mono & 0xFFFFu);
      sg = rbf(1.f / (1.f + __expf(-lo(rb))));
    }
    unsigned mine = (unsigned)__ballot(!NOMOE && l < 8 && (idx >> 7) == mb && (l & 1) == mw);
    if (mine) {
      if (spare) lwait<4>(adFL, 0x30u, 3);
      {
        const u2 f = LD64(WS_OH + (mb * 16 + r15) * 128);
        const unsigned t0 = NOW();
        unsigned rel = f[1] + YD - t0;
        rel = wave_max(f[0] == tag && rel < 600u ? rel : 0u);
        _Pragma("unroll 1") for (unsigned it = 0; it < SPIN; ++it) { if (NOW() - t0 >= rel) break; SLEEP(2); }
      }
      STMP(8);
      lwait<1>(adFL, 0x0Fu, 4);
      __builtin_amdgcn_s_setprio(TP2);
      while (mine) {
        const int t = __builtin_ctz(mine); mine &= mine - 1;
        u4 yv[14];
        _Pragma("unroll 1") for (unsigned it = 0; it < SPIN; ++it) {
          unsigned bad = 0;
#pragma unroll
          for (int i = 0; i < 14; ++i) yv[i] = LD128(WS_YP + (((i * 4 + g4) * 8 + t) * D + (nb >> 1) * 128 + (r15 >> 3) * 64 + (nb & 1) * 32 + (r15 & 7) * 4) * 4);
          SBAR();
#pragma unroll
          for (int i = 0; i < 14; ++i) untag(yv[i], tag, bad);
          if (__ballot((bad & 255u) != 0u) == 0ull) break;
          RETRY();
          SLEEP(YSL);
        }
        f4 y = __builtin_bit_cast(f4, yv[0]);
#pragma unroll
        for (int i = 1; i < 14; ++i) y += __builtin_bit_cast(f4, yv[i]);
        const float gt = __int_as_float(__builtin_amdgcn_readlane(__float_as_int(sg), t));
#pragma unroll
        for (int e = 0; e < 4; ++e) {
          auto r = __builtin_amdgcn_permlane32_swap(__float_as_uint(y[e]), __float_as_uint(y[e]), false, false);
          const float a = __uint_as_float(r[0]) + __uint_as_float(r[1]);
          auto r2 = __builtin_amdgcn_permlane16_swap(__float_as_uint(a), __float_as_uint(a), false, false);
          y[e] = rbf(rbf(__uint_as_float(r2[0]) + __uint_as_float(r2[1])) * gt);
        }
        if (l < 16) *(LAS f4*)(lds + L_KVX + t * 256 + (l & 7) * 32 + (l >> 3) * 16) = y;
      }
    }
    lsig(myFL, 3u);
    STMP(9);
    STFLUSH();
    if (spare) asm volatile(".Lend_s:"); else asm volatile(".Lend_h:");
    };
    if (sp) helper(BC<true>()); else helper(BC<false>());
    return;
  }

  const int wr = w >> 1, wc = w & 1;
  unsigned tag; u2 kg; u4 sg4;
  const int srow = mb * 128 + hp * 8 + 2 * w + (l >> 5);
  {
    const int row = srow;
    u4 xa[4], rw[4];
#pragma unroll
    for (int c = 0; c < 4; ++c) xa[c] = *(const u4*)(x + row * D + (l & 31) * 8 + 256 * c);
#pragma unroll
    for (int c = 0; c < 4; ++c) rw[c] = *(const u4*)(wrt + (l & 31) * 8 + 256 * c);
    float s0 = 0, s1 = 0;
#pragma unroll
    for (int c = 0; c < 4; ++c)
#pragma unroll
      for (int e = 0; e < 4; e += 2) { unsigned x0 = xa[c][e], x1 = xa[c][e + 1]; s0 = dot2(x0, 0x3F803F80u, s0); s1 = dot2(x1, 0x3F803F80u, s1); }
    const float m = half_sum(s0 + s1) * (1.f / D);
    f2 v[16], q2 = (f2)(0.f), q3 = (f2)(0.f);
#pragma unroll
    for (int c = 0; c < 16; ++c) v[c] = (f2){lo(xa[c >> 2][c & 3]), hi(xa[c >> 2][c & 3])} - m;
#pragma unroll
    for (int c = 0; c < 16; c += 2) { q2 += v[c] * v[c]; q3 += v[c + 1] * v[c + 1]; }
    q2 += q3;
    const float mr = rbf(m);
    const float rs = 1.f / rbf(sqrtf(rbf(rbf(half_sum(q2[0] + q2[1]) * (1.f / D)) + 1e-5f)));
    float d0 = 0, d1 = 0;
#pragma unroll
    for (int c = 0; c < 16; c += 2) {
      const f2 e0 = (f2){lo(xa[c >> 2][c & 3]), hi(xa[c >> 2][c & 3])} - mr, e1 = (f2){lo(xa[c >> 2][(c & 3) + 1]), hi(xa[c >> 2][(c & 3) + 1])} - mr;
      const unsigned p0 = pk2(e0[0], e0[1]), p1 = pk2(e1[0], e1[1]);
      const f2 n0 = (f2){lo(p0), hi(p0)} * rs, n1 = (f2){lo(p1), hi(p1)} * rs;
      unsigned c0 = rw[c >> 2][c & 3], c1 = rw[c >> 2][(c & 3) + 1];
      d0 = dot2(pk2(n0[0], n0[1]), c0, d0); d1 = dot2(pk2(n1[0], n1[1]), c1, d1);
    }
    const float d = half_sum(d0 + d1);
    const unsigned rb = pk2(d, d) & 0xFFFFu;
    const unsigned mono = (rb & 0x8000u) ? (~rb & 0xFFFFu) : (rb | 0x8000u);
    tag = __builtin_amdgcn_readfirstlane(ev0) + 1u;
    kg = (u2){(mono << 16) | (unsigned)row, tag};
    sg4 = (u4){__float_as_uint(mr), tag, __float_as_uint(rs), tag};
  }
  MKWS(tag);
  if ((l & 31) == 0) { ST64(kg, WS_RG + srow * 8); ST128(sg4, WS_ST + srow * 16); }
  unsigned aad[2], bad[2];
#pragma unroll
  for (int s = 0; s < 2; ++s) aad[s] = ldsb + (wr * 64 + r15) * 128 + (((4 * s + g4) ^ ((r15 >> 1) & 7)) * 16);
#pragma unroll
  for (int nh = 0; nh < 2; ++nh) bad[nh] = ldsb + 16384 + (8 * g4 + q) * 128 + (((wc * 4 + 2 * nh + (p >> 1)) ^ (((q >> 1) << 2) | ((g4 & 1) << 1))) * 16) + (p & 1) * 8;
  f4 acc[4][6];
#pragma unroll
  for (int m = 0; m < 4; ++m)
#pragma unroll
    for (int n = 0; n < 6; ++n) { acc[m][n] = (f4)(0.f); asm volatile("" : "+v"(acc[m][n])); }
#define MF(m, n, Fq) do { if (n < 2) acc[m][n] = MFMA16(CAT(Fq.b[2 * n], Fq.b[2 * n + 1]), Fq.a[m], acc[m][n]); \
     else acc[m][n] = MFMA16(Fq.a[m], CAT(Fq.b[2 * n], Fq.b[2 * n + 1]), acc[m][n]); SBAR(); } while (0)
#define RB(G, n, vb0, vb1, SS, hf) RDTR(G.b[2 * (n) + (hf)], (((n) & 1) ? vb1 : vb0), ((n) >> 1) * 8192 + (SS) * 4096 + (hf) * 512)
#define RA(G, m, va) RD128(G.a[m], va, (m) * 2048)
#define SUB(Fq, G, va, vb0, vb1, SS, PRE) do { \
    WAITL(13); SBAR(); \
    MF(0, 0, Fq); PRE; RA(G, 0, va); SBAR(); \
    WAITL(11); SBAR(); \
    MF(1, 0, Fq); RB(G, 0, vb0, vb1, SS, 0); SBAR(); MF(2, 0, Fq); RB(G, 0, vb0, vb1, SS, 1); SBAR(); MF(3, 0, Fq); RA(G, 1, va); SBAR(); \
    WAITL(12); SBAR(); \
    MF(0, 1, Fq); RA(G, 2, va); SBAR(); MF(1, 1, Fq); RA(G, 3, va); SBAR(); MF(2, 1, Fq); RB(G, 1, vb0, vb1, SS, 0); SBAR(); MF(3, 1, Fq); RB(G, 1, vb0, vb1, SS, 1); SBAR(); \
    WAITL(8); SBAR(); \
    MF(0, 2, Fq); RB(G, 2, vb0, vb1, SS, 0); SBAR(); MF(1, 2, Fq); RB(G, 2, vb0, vb1, SS, 1); SBAR(); MF(2, 2, Fq); RB(G, 3, vb0, vb1, SS, 0); SBAR(); MF(3, 2, Fq); RB(G, 3, vb0, vb1, SS, 1); SBAR(); \
    MF(0, 3, Fq); RB(G, 4, vb0, vb1, SS, 0); SBAR(); MF(1, 3, Fq); RB(G, 4, vb0, vb1, SS, 1); SBAR(); MF(2, 3, Fq); RB(G, 5, vb0, vb1, SS, 0); SBAR(); MF(3, 3, Fq); RB(G, 5, vb0, vb1, SS, 1); SBAR(); \
    MF(0, 4, Fq); MF(1, 4, Fq); MF(2, 4, Fq); MF(3, 4, Fq); MF(0, 5, Fq); MF(1, 5, Fq); MF(2, 5, Fq); MF(3, 5, Fq); } while (0)
#define KSTEP(MODE) do { \
    if (kt == sx) { const unsigned xo = xad + buf * STG; \
      _Pragma("unroll") for (int t = 0; t < 8; ++t) { const unsigned a_ = xo ^ (t * 16); \
        asm volatile("ds_read_b64 %0, %1" : "+v"(xv[t]) : "v"(a_) : "memory"); } \
      WAITL(0); SBAR(); } \
    const int nbuf = (buf + 1) & 3; \
    const unsigned bo = buf * STG, nbo = nbuf * STG; \
    { unsigned va = aad[1] + bo, vb0 = bad[0] + bo, vb1 = bad[1] + bo; SUB(f0, f1, va, vb0, vb1, 1, ); } \
    if (MODE < 2) { \
      unsigned va = aad[0] + nbo, vb0 = bad[0] + nbo, vb1 = bad[1] + nbo; \
      SUB(f1, f0, va, vb0, vb1, 0, if (MODE == 0) { WAITL(0); BAR(); } else asm volatile("ds_write_b32 %0, %1" ::"v"(myFL), "v"(1u) : "memory"); SBAR()); \
    } else { asm volatile("ds_write_b32 %1, %2\n ds_read_b32 %0, %3\n s_waitcnt lgkmcnt(2)" : "=&v"(fl0) : "v"(myFL), "v"(2u), "v"(adFL) : "memory"); SBAR(); \
      MF(0, 2, f1); MF(1, 2, f1); MF(2, 2, f1); MF(3, 2, f1); MF(0, 3, f1); MF(1, 3, f1); MF(2, 3, f1); MF(3, 3, f1); \
      MF(0, 4, f1); MF(1, 4, f1); MF(2, 4, f1); MF(3, 4, f1); MF(0, 5, f1); MF(1, 5, f1); MF(2, 5, f1); MF(3, 5, f1); \
      MF(0, 0, f1); MF(1, 0, f1); MF(2, 0, f1); MF(3, 0, f1); MF(0, 1, f1); MF(1, 1, f1); MF(2, 1, f1); MF(3, 1, f1); } \
    buf = nbuf; } while (0)
  u2 xv[8];
#pragma unroll
  for (int t = 0; t < 8; ++t) asm volatile("" : "=v"(xv[t]));
  const int sx = (nb - rot) & 15;
  const unsigned xad = ldsb + (w * 32 + (l & 31)) * 128 + ((r15 >> 1) & 7) * 16 + (l >> 5) * 8;
  STMP(1);
  WAITL(0);
  BAR();
  STMP(2);
  WAITL(0);
  FQ f0, f1;
  { unsigned va = aad[0], vb0 = bad[0], vb1 = bad[1];
    RA(f0, 0, va); RB(f0, 0, vb0, vb1, 0, 0); RB(f0, 0, vb0, vb1, 0, 1); RA(f0, 1, va); RA(f0, 2, va); RA(f0, 3, va);
    RB(f0, 1, vb0, vb1, 0, 0); RB(f0, 1, vb0, vb1, 0, 1); RB(f0, 2, vb0, vb1, 0, 0); RB(f0, 2, vb0, vb1, 0, 1); RB(f0, 3, vb0, vb1, 0, 0); RB(f0, 3, vb0, vb1, 0, 1);
    RB(f0, 4, vb0, vb1, 0, 0); RB(f0, 4, vb0, vb1, 0, 1); RB(f0, 5, vb0, vb1, 0, 0); RB(f0, 5, vb0, vb1, 0, 1);
    WAITL(0); SBAR(); }
  int buf = 0; unsigned fl0;
#pragma unroll 1
  for (int kt = 0; kt < 14; ++kt) KSTEP(0);
  STMP(14); SBAR();
  { const int kt = 14; KSTEP(1); }
  STMP(7); SBAR();
  { const int kt = 15; KSTEP(2); }
  asm volatile("s_waitcnt lgkmcnt(0)" : "+v"(fl0) : : "memory");
  if (__builtin_expect(((unsigned)__ballot(fl0 < 1u) & 0xC0u) != 0u, 0)) lwait(adFL, 0xC0u, 1);
  STMP(3);
  __builtin_amdgcn_s_setprio(2);
  const LAS float* Lm = (const LAS float*)(lds + L_M) + wr * 64;
  const LAS float* Lr = (const LAS float*)(lds + L_RS) + wr * 64;
  const LAS float* Lc = (const LAS float*)(lds + L_CS) + wc * 96;
  f4 kv[4], zt[2];
  {
    float cs[4];
#pragma unroll
    for (int n = 0; n < 4; ++n) cs[n] = Lc[32 + 16 * n + r15];
    const u4 ones = (u4)(0x3F803F80u);
#pragma unroll
    for (int t = 0; t < 2; ++t) {
      u4 kf[2], vf[2];
#pragma unroll
      for (int mm = 0; mm < 2; ++mm) {
        const int m = 2 * t + mm;
        const f4 mv = *(const LAS f4*)(Lm + m * 16 + 4 * g4), rv = *(const LAS f4*)(Lr + m * 16 + 4 * g4);
#pragma unroll
        for (int a = 0; a < 2; ++a) {
          const f4 uk = acc[m][2 + a] - mv * cs[a];
          const f4 uv = (acc[m][4 + a] - mv * cs[2 + a]) * rv;
          kf[a][2 * mm] = phi2((f2){uk[0], uk[1]}, (f2){rv[0], rv[1]}); kf[a][2 * mm + 1] = phi2((f2){uk[2], uk[3]}, (f2){rv[2], rv[3]});
          vf[a][2 * mm] = pk2(uv[0], uv[1]); vf[a][2 * mm + 1] = pk2(uv[2], uv[3]);
        }
      }
#pragma unroll
      for (int tt = 0; tt < 4; ++tt) kv[tt] = MFMA16(S8(kf[tt & 1]), S8(vf[tt >> 1]), t ? kv[tt] : (f4)(0.f));
#pragma unroll
      for (int a = 0; a < 2; ++a) zt[a] = MFMA16(S8(kf[a]), S8(ones), t ? zt[a] : (f4)(0.f));
    }
  }
  const int hd = hp * 2 + wc;
  {
    LAS f4* ks = (LAS f4*)(lds + L_KVX + w * 3072) + l;
    const LAS f4* kr = (const LAS f4*)(lds + L_KVX + (w ^ 2) * 3072) + l;
    ks[0] = wr ? kv[0] : kv[2]; ks[64] = wr ? kv[1] : kv[3]; ks[128] = wr ? zt[0] : zt[1];
    lsig(myFL, 3);
    const f4 m0 = wr ? kv[2] : kv[0], m1 = wr ? kv[3] : kv[1], mz = wr ? zt[1] : zt[0];
    lwait(adFL, 1u << (w ^ 2), 3);
    const f4 s0 = m0 + kr[0], s1 = m1 + kr[64], sz = mz + kr[128];
    u4 o; o[0] = min(pk2(s0[0], s0[1]), POIS - 1u); o[1] = pk2(s0[2], s0[3]); o[2] = pk2(s1[0], s1[1]); o[3] = pk2(s1[2], s1[3]);
    ST128(o, WS_KVP + (hd * 16 + mb) * 2048 + wr * 1024 + l * 16);
    u4 zo = __builtin_bit_cast(u4, sz); zo[0] = min(zo[0], POIS - 1u);
    if (r15 == 0) ST128(zo, WS_ZP + (hd * 16 + mb) * 128 + g4 * 32 + wr * 16);
  }
  STMP(4);
  {
    const f4 c0 = *(const LAS f4*)(Lc + 4 * g4), c1 = *(const LAS f4*)(Lc + 16 + 4 * g4);
#pragma unroll
    for (int m = 0; m < 4; ++m) {
      const float mv = Lm[m * 16 + r15], rv = Lr[m * 16 + r15];
      const f4 u0 = acc[m][0] - c0 * mv, u1 = acc[m][1] - c1 * mv;
      u4 pf;
      pf[0] = phi2((f2){u0[0], u0[1]}, (f2)(rv)); pf[1] = phi2((f2){u0[2], u0[3]}, (f2)(rv));
      pf[2] = phi2((f2){u1[0], u1[1]}, (f2)(rv)); pf[3] = phi2((f2){u1[2], u1[3]}, (f2)(rv));
      *(LAS u4*)(lds + L_PQ + wr * 8192 + wc * 4096 + m * 1024 + l * 16) = pf;
    }
  }
  lsig(myFL, 4);
  __builtin_amdgcn_s_setprio(0);
  STMP(5);
  const int trow = mb * 128 + w * 32 + (l & 31);
  u2 oh;
  const unsigned flo = PAIRAT(r15) * 128;
  const char* fb = uptr(wsx + WS_OH + mb * 2048);
  int sel = -1;
  {
    const u4 s0 = *(const LAS u4*)(lds + L_SEL), s1 = *(const LAS u4*)(lds + L_SEL + 16);
#pragma unroll
    for (int i = 0; i < 8; ++i) { const int v = (int)(i < 4 ? s0[i & 3] : s1[i & 3]); sel = v == trow ? i : sel; }
  }
  const bool anysel = __ballot(sel >= 0) != 0ull;
  f16v oc[2][2];
  {
    const float m = *(const LAS float*)(lds + L_M + (w * 32 + (l & 31)) * 4), rs = *(const LAS float*)(lds + L_RS + (w * 32 + (l & 31)) * 4);
#pragma unroll
    for (int t = 0; t < 8; ++t) {
      const f4 xf = (f4){lo(xv[t][0]), hi(xv[t][0]), lo(xv[t][1]), hi(xv[t][1])};
      const f4 y = (xf - m) * rs + xf;
#pragma unroll
      for (int i = 0; i < 4; ++i) oc[0][t >> 2][(t & 3) * 4 + i] = y[i];
    }
    asm volatile("" : "+v"(oc[0][0]), "+v"(oc[0][1]));
  }
  unsigned wa[2];
#pragma unroll
  for (int t = 0; t < 2; ++t) wa[t] = ldsb + L_WO + wc * 4096 + (4 * (g4 >> 1) + q) * 128 + (((4 * t + 2 * (g4 & 1) + (p >> 1)) ^ ((q >> 1) << 2)) * 16) + (p & 1) * 8;
  u4 a[RD][4];
#pragma unroll
  for (int k = 0; k < RD; ++k) asm volatile("" : "=v"(a[k][0]), "=v"(a[k][1]), "=v"(a[k][2]), "=v"(a[k][3]));
  const char* ob = uptr(wsx + WS_OF + mb * 262144 + wc * 8192 + wr * 4096);
  const unsigned ov0 = l * 16 + wc * 2048, ov1 = ov0 ^ 2048;
  STMP(6);
  {
    unsigned vfw[4];
#pragma unroll
    for (int i = 0; i < 4; ++i) { vfw[i] = VFW(i); asm volatile("" : "+v"(vfw[i])); }
    lwait<2>(adFL, 0x3Fu, (l & 4) ? 3u : 4u);
    asm volatile("global_load_dwordx2 %0, %1, %2 sc1" : "=&v"(oh) : "v"(flo), "s"(fb) : "memory");
#pragma unroll
    for (int i = WE / 2; i < 16; i += 2) {
      const int pp = i + wr;
      const char* wp = wob - 3072 + (2 * PAIRAT(pp) + wc) * 65536; const unsigned wm = ldsb + (2 * pp + wc) * 4096;
      GL4(vfw, wp, wm, L_WO);
    }
    WAITV(32 - WE);
    asm volatile("" : "+v"(oh));
  }
  unsigned seen = (unsigned)__ballot(oh[0] == tag), go = 0;
  auto need = [&](unsigned mask) {
    if (__builtin_expect((go & mask) != mask, 0)) {
      RETRY2();
      _Pragma("unroll 1") for (unsigned it = 0; it < SPIN; ++it) {
        if ((seen & mask) != mask) {
          asm volatile("global_load_dwordx2 %0, %1, %2 sc1\n s_waitcnt vmcnt(0)" : "+v"(oh) : "v"(flo), "s"(fb) : "memory");
          seen = (unsigned)__ballot(oh[0] == tag);
        }
        go = seen & (unsigned)__ballot(oh[1] - NOW() - 1u >= 1000u);
        if ((go & mask) == mask) break;
        SLEEP(1);
      }
    }
  };
#define OX 0
#define OL1(pos, i) do { if (OX == 2 && (pos) >= RD) break; \
    asm volatile("global_load_dwordx4 %0, %1, %2 offset:%3 sc1" : "+v"(a[(pos) % RD][i]) : "v"((i) & 1 ? ov1 : ov0), "s"(ob + (OX == 1 ? ((pos) & 1) : PAIRAT(pos)) * 16384), "i"(((i) >> 1) * 1024) : "memory"); } while (0)
#define OLD(pos) do { OL1(pos, 0); OL1(pos, 1); OL1(pos, 2); OL1(pos, 3); } while (0)
  auto omax = [&](u4(&v)[4]) {
    asm volatile("" : "+v"(v[0]), "+v"(v[1]), "+v"(v[2]), "+v"(v[3]));
    return max(umax3(v[0][0], v[1][0], v[2][0]), v[3][0]);
  };
#define OFIX(pos, m) do { \
    if (__builtin_expect(__ballot((m) == POIS) != 0ull, 0)) { RETRY(); _Pragma("unroll 1") for (unsigned it = 0; it < SPIN; ++it) { SLEEP(4); OLD(pos); WAITV(0); if (__ballot(omax(a[(pos) % RD]) == POIS) == 0ull) break; } } } while (0)
#define OS1(i)
#define OS2(i)
  lwait<1>(adFL, 0x30u, 4);
  __builtin_amdgcn_s_setprio(2);
  go = seen & (unsigned)__ballot(oh[1] - NOW() - 1u >= 1000u);
  need(1u);
  STMP(15);
  u2 oh2 = oh;
  if (__builtin_expect(seen != ~0u, 0)) asm volatile("global_load_dwordx2 %0, %1, %2 sc1" : "+v"(oh2) : "v"(flo), "s"(fb) : "memory");
  OS1(0); OLD(0);
#pragma unroll
  for (int i = 1; i < RD; ++i) { need(1u << i); OS1(i); OLD(i); }
  STMP(10);
  s4 b[8];
#define ORD(pos, f) do { if ((f) < 2 && (pos) == 8) { wa[(f) & 1] += 65536; asm volatile("" : "+v"(wa[(f) & 1])); } \
    RDTR(b[2 * (f)], wa[(f) & 1], ((pos) & 7) * 8192 + ((f) >> 1) * 1024); RDTR(b[2 * (f) + 1], wa[(f) & 1], ((pos) & 7) * 8192 + ((f) >> 1) * 1024 + 2048); } while (0)
#define OMF(pos, f, r) do { if ((f) < 2 && (pos) == 0 && (r) == 1) asm volatile("v_mfma_f32_32x32x16_bf16 %0, %1, %2, 0" : "=&v"(oc[r][(f) & 1]) \
      : "v"(CAT(b[2 * (f)], b[2 * (f) + 1])), "v"(a[(pos) % RD][((f) & 2) + (r)])); \
    else asm volatile("v_mfma_f32_32x32x16_bf16 %0, %1, %2, %0" : "+v"(oc[r][(f) & 1]) \
      : "v"(CAT(b[2 * (f)], b[2 * (f) + 1])), "v"(a[(pos) % RD][((f) & 2) + (r)])); \
    SBAR(); if ((r) == 1 && (pos) < 15) ORD(((pos) + 1) & 15, f); SBAR(); } while (0)
#define ONX(pos) ((pos) > 0 && (pos) + RD - 1 < 16)
#define ONL(pos, i) do { if (ONX(pos)) OL1(((pos) + RD - 1) & 15, i); SBAR(); } while (0)
#define OWL(pos, f) do { WAITL((pos) < 15 ? 6 : 6 - 2 * (f)); SBAR(); } while (0)
#define OHEAD(pos) do { unsigned m_ = 0; \
    if ((pos) == WE / 2 - 1 && WE < 32) { asm volatile("" : "+v"(fl3)); if (__builtin_expect(((unsigned)__ballot(fl3 < 5u) >> (w ^ 2)) & 1u, 0)) lwait(adFL, 1u << (w ^ 2), 5); SBAR(); } \
    OWL(pos, 0); OMF(pos, 0, 0); if (ONX(pos)) { need(1u << (((pos) + RD - 1) & 15)); OS1(((pos) + RD - 1) & 15); } SBAR(); \
    OMF(pos, 0, 1); ONL(pos, 0); \
    OWL(pos, 1); OMF(pos, 1, 0); ONL(pos, 1); \
    OMF(pos, 1, 1); ONL(pos, 2); \
    OWL(pos, 2); OMF(pos, 2, 0); ONL(pos, 3); \
    if ((pos) == 0 || (pos) == 4) { asm volatile("ds_write_b32 %0, %1" ::"v"(myFL), "v"((pos) ? 6u : 5u) : "memory"); SBAR(); } \
    if ((pos) == WE / 2 - 3) { asm volatile("ds_read_b32 %0, %1" : "=v"(fl3) : "v"(adFL) : "memory"); SBAR(); } \
    if ((pos) == 13) { asm volatile("ds_read_b32 %0, %1" : "=v"(fl3) : "v"(adFL) : "memory"); SBAR(); } \
    OMF(pos, 2, 1); \
    OWL(pos, 3); OMF(pos, 3, 0); \
    if ((pos) < 15) { WAITV(4 * ((pos) + RD - 1 < 16 ? RD - 2 : 14 - (pos))); m_ = omax(a[((pos) + 1) % RD]); asm volatile("" : "+v"(m_)); SBAR(); } \
    OMF(pos, 3, 1); \
    if ((pos) < 15) { OFIX(((pos) + 1) & 15, m_); OS2(((pos) + 1) & 15); } SBAR(); } while (0)
  unsigned fl3;
  ORD(0, 0); ORD(0, 1); ORD(0, 2); ORD(0, 3);
  { WAITV(4 * (RD - 1)); const unsigned m_ = omax(a[0]); OFIX(0, m_); OS2(0);
    asm volatile("" : "+v"(oh2));
    if (oh2[0] == tag) oh = oh2;
    seen = (unsigned)__ballot(oh[0] == tag);
    go = seen & (unsigned)__ballot(oh[1] - NOW() - max(r15 - (RD - 1), 0) * ADV - 1u >= 1000u);
    SBAR(); }
  OHEAD(0); OHEAD(1); STMP(11); OHEAD(2); OHEAD(3); OHEAD(4); OHEAD(5); OHEAD(6); OHEAD(7); STMP(12);
  OHEAD(8); OHEAD(9); STMP(13); OHEAD(10); OHEAD(11); OHEAD(12); OHEAD(13); OHEAD(14);
  f4 xi[8];
  {
    asm volatile("" : "+v"(fl3));
    if (__builtin_expect(((unsigned)__ballot(fl3 < 6u) >> (w ^ 2)) & 1u, 0)) lwait(adFL, 1u << (w ^ 2), 6);
    if (__builtin_expect(((unsigned)__ballot(fl3 < 4u) & 0x30u) != 0u, 0)) lwait(adFL, 0x30u, 4);
    const unsigned xw = ldsb + L_WO + (4 * wr + wc) * 4096 + l * 16, xr = xw ^ 4096;
#define XW(t) asm volatile("ds_write_b128 %0, %1 offset:%2" ::"v"(xw), "v"(__builtin_shufflevector(oc[1][(t) >> 2], oc[1][(t) >> 2], ((t) & 3) * 4, ((t) & 3) * 4 + 1, ((t) & 3) * 4 + 2, ((t) & 3) * 4 + 3)), \
      "i"(((t) >> 2) * 8192 + ((t) & 3) * 1024) : "memory")
    SBAR();
    OWL(15, 0); OMF(15, 0, 1); OWL(15, 1); OMF(15, 1, 1); OWL(15, 2); OMF(15, 2, 1); OWL(15, 3); OMF(15, 3, 1);
    OMF(15, 0, 0); OMF(15, 1, 0); XW(0); XW(1); XW(2); XW(3); SBAR();
    OMF(15, 2, 0); XW(4); XW(5); XW(6); XW(7); SBAR();
    OMF(15, 3, 0);
    asm volatile("ds_write_b32 %0, %1" ::"v"(myFL), "v"(7u) : "memory");
    _Pragma("unroll 1") for (unsigned it = 0; it < SPIN; ++it) {
      unsigned v;
      asm volatile("ds_read_b32 %0, %1" : "=v"(v) : "v"(adFL) : "memory");
#pragma unroll
      for (int t = 0; t < 8; ++t) RD128(xi[t], xr, (t >> 2) * 8192 + (t & 3) * 1024);
      WAITL(0);
      asm volatile("" : "+v"(v), "+v"(xi[0]), "+v"(xi[1]), "+v"(xi[2]), "+v"(xi[3]), "+v"(xi[4]), "+v"(xi[5]), "+v"(xi[6]), "+v"(xi[7]));
      if ((((unsigned)__ballot(v < 7u) >> (w ^ 1)) & 1u) == 0u) break;
    }
    asm volatile("" : "+v"(oc[0][0]), "+v"(oc[0][1]));
#pragma unroll
    for (int t = 0; t < 8; ++t)
#pragma unroll
      for (int i = 0; i < 4; ++i) xi[t][i] += oc[0][t >> 2][(t & 3) * 4 + i];
  }
  STMP(8);
  if (anysel) {
    lwait(adFL, 0xC0u, 3);
    if (sel >= 0) {
#pragma unroll
      for (int t = 0; t < 8; ++t) xi[t] += *(const LAS f4*)(lds + L_KVX + sel * 256 + (8 * t + 4 * (l >> 5)) * 4);
    }
  }
  const __amdgpu_buffer_rsrc_t outr = __builtin_amdgcn_make_buffer_rsrc(out, 0, 2048 * D * 2, 0x00020000);
  if (FULL) {
    LAS char* tb = (LAS char*)lds + L_PQ + w * 4096;
#pragma unroll
    for (int t = 0; t < 8; ++t) *(LAS u2*)(tb + (l & 31) * 128 + ((t ^ (l & 7)) * 16) + (l >> 5) * 8) = (u2){pk2(xi[t][0], xi[t][1]), pk2(xi[t][2], xi[t][3])};
#pragma unroll
    for (int i = 0; i < 4; ++i) {
      const u4 v = *(const LAS u4*)(tb + i * 1024 + (l >> 3) * 128 + (((l ^ (l >> 3)) & 7) * 16));
      __builtin_amdgcn_raw_buffer_store_b128(v, outr, (mb * 128 + w * 32 + i * 8 + (l >> 3)) * 2048 + nb * 128 + (l & 7) * 16, 0, 16);
    }
  } else {
#pragma unroll
  for (int hh = 0; hh < 4; ++hh) {
    const f4 ta = xi[2 * hh], tb = xi[2 * hh + 1];
    auto r0 = __builtin_amdgcn_permlane32_swap(pk2(ta[0], ta[1]), pk2(tb[0], tb[1]), false, false);
    auto r1 = __builtin_amdgcn_permlane32_swap(pk2(ta[2], ta[3]), pk2(tb[2], tb[3]), false, false);
    __builtin_amdgcn_raw_buffer_store_b128(((u4){r0[0], r1[0], r0[1], r1[1]}), outr, (trow * D + nb * 64 + 16 * hh + 8 * (l >> 5)) * 2, 0, 16);
  }
  }
  if (g == 0 && w == 0 && l == 0) __builtin_amdgcn_raw_buffer_store_b32(tag, wse, WS_E, 0, 16);
  STMP(9);
  STFLUSH();
  asm volatile(".Lend_c:");
  return;
  }
  {
    const int lw = w - 4;
    unsigned ev;
    asm volatile("global_load_dword %0, %1, %2 sc1" : "=v"(ev) : "v"(0u), "s"(ws) : "memory");
    unsigned vf[20], vfw[4];
#pragma unroll
    for (int i = 0; i < 8; ++i) { const int row = 8 * (lw * 8 + i) + (l >> 3); vf[i] = row * 2048 + (((l & 7) ^ ((row >> 1) & 7)) * 16) + (3 - (i & 3)) * 1024; }
#pragma unroll
    for (int i = 0; i < 12; ++i) {
      const int pc = lw * 12 + i, row = (pc & 7) * 8 + (l >> 3);
      vf[8 + i] = row * 6144 + (pc >> 3) * 2048 + (((l & 7) ^ ((((row >> 1) & 1) << 2) | (((row >> 3) & 1) << 1))) * 16) + (3 - (i & 3)) * 1024;
    }
#pragma unroll
    for (int i = 0; i < 4; ++i) vfw[i] = VFW(i);
    const char* Ab = uptr((const char*)x + (size_t)mb * 128 * 2048) - 3072;
    const char* Bb = uptr((const char*)wqkv + hp * 128) - 3072;
    const unsigned mba = ldsb + lw * 8192, mbb = ldsb + 16384 + lw * 12288;
    auto issue = [&](int s, int u0, int u1) {
      const int kt = (s + rot) & 15;
      const char* sa = Ab + kt * 128; const char* sb = Bb + kt * (64 * 6144);
      const unsigned so = (s & 3) * STG, ma = mba + so, mq = mbb + so;
#pragma unroll
      for (int u = u0; u < u1; ++u) { if (u < 2) GL4(vf + 4 * u, sa, ma, u * 4096); else GL4(vf + 4 * u, sb, mq, (u - 2) * 4096); }
    };
    int tk = 0;
#define LBAR() do { BST(tk + 1); BAR(); BST(tk + 21); ++tk; } while (0)
    issue(0, 0, 5); issue(1, 0, 5); issue(2, 0, U0);
    BST(60);
    WAITV(20 + 4 * U0); BST(0); BAR(); BST(20);
    asm volatile("" : "+v"(ev));
    const unsigned tag = __builtin_amdgcn_readfirstlane(ev) + 1u;
    MKWS(tag);
    issue(2, U0, 5);
    const unsigned ck0 = (unsigned)__builtin_amdgcn_s_memtime(), rt0 = NOW();
    int s0 = 3;
    if (U3 < 5) { issue(3, 0, U3); BST(tk + 40); WAITV(20 + 4 * U3); LBAR(); issue(3, U3, 5); s0 = 4; }
#pragma unroll 1
    for (; s0 < (sp ? 16 : 6 + ph); ++s0) { issue(s0, 0, 5); BST(tk + 40); WAITV(40); LBAR(); }
#pragma unroll 1
    for (; s0 < 16; s0 += 5) {
      WAITV(20); LBAR();
      WAITV(0); LBAR();
      issue(s0, 0, 5); issue(s0 + 1, 0, U1); BST(tk + 40); WAITV(4 * U1); LBAR();
      issue(s0 + 1, U1, 5); issue(s0 + 2, 0, 5);
      if (s0 + 3 >= 16) break;
      issue(s0 + 3, 0, U2); BST(tk + 40); WAITV(20 + 4 * U2); LBAR();
      issue(s0 + 3, U2, 5);
      if (s0 + 4 >= 16) break;
      issue(s0 + 4, 0, 5); BST(tk + 40); WAITV(40); LBAR();
    }
    int wq = 0;
    auto wo_upto = [&](int lim) {
      _Pragma("unroll 1") for (; wq < lim; ++wq) { const char* wp = wob - 3072 + HEADAT(wq) * 65536; const unsigned wm = ldsb + wq * 4096; GL4(vfw, wp, wm, L_WO); }
    };
    asm volatile("ds_write_b32 %0, %1" ::"v"(adFL), "v"(0u) : "memory");
    unsigned kd = sp ? KS : KD, od = OD;
    if (CF) {
      const unsigned dc = (unsigned)__builtin_amdgcn_s_memtime() - ck0, dr = NOW() - rt0;
      const float sc = fminf((float)dr * (float)CF * __builtin_amdgcn_rcpf((float)dc), 2.f);
      kd = KL + (unsigned)((float)(kd - KL) * sc); od = OL + (unsigned)((float)(OD - OL) * sc);
      asm volatile("" : "+v"(kd), "+v"(od));
    }
    WAITV(0); WAITL(0); LBAR();
    const int hd = hp * 2 + lw;
    u4 dg;
    if (lw) {
      const u4 pz = (u4)(POIS);
      ST64(((u2){tag, NOW() + kd}), l == 0 ? WS_HK + hp * 512 + (mb >> 2) * 128 + (mb & 3) * 8 : WS_SZ);
#pragma unroll
      for (int i = 0; i < 16; ++i) ST128(pz, WS_OF + (mb * 32 + hp * 2) * 8192 + i * 1024 + l * 16);
      BST(61);
      const char* hb = uptr(wsx + WS_HK + hp * 512);
      lwait<4>(adFL, 0xCFu, (l & 4) ? 1u : 2u);
      if (HT) SLEEP(HT);
      STMP(1);
      unsigned t0, rq[4];
      _Pragma("unroll 1") for (unsigned it = 0; it < 64; ++it) {
        u8v h[4];
        asm volatile("s_load_dwordx8 %0, %4, 0 glc\n s_load_dwordx8 %1, %4, 128 glc\n s_load_dwordx8 %2, %4, 256 glc\n s_load_dwordx8 %3, %4, 384 glc\n s_waitcnt lgkmcnt(0)"
                     : "=&s"(h[0]), "=&s"(h[1]), "=&s"(h[2]), "=&s"(h[3]) : "s"(hb) : "memory");
        t0 = NOW() - 512u;
        BST(16);
        unsigned bad = 0;
#pragma unroll
        for (int i = 0; i < 16; ++i) {
          unsigned e = h[i >> 2][(i & 3) * 2] ^ tag;
          asm("" : "+s"(e));
          bad |= e;
          const unsigned d = h[i >> 2][(i & 3) * 2 + 1] - t0, r = d < 1536u ? d : 0u; rq[i >> 2] = (i & 3) ? max(rq[i >> 2], r) : r;
        }
        if (bad == 0u) break;
        RETRY2();
        SLEEP(HKS);
      }
      const bool up = max(rq[0], rq[1]) > max(rq[2], rq[3]);
      const unsigned a0 = up ? rq[2] : rq[0], a1 = up ? rq[3] : rq[1], a2 = up ? rq[0] : rq[2], a3 = up ? rq[1] : rq[3];
      const bool od2 = a2 > a3;
      dg = (u4){((t0 + (od2 ? a1 : a0) + 3u) & ~3u) | (up ? 2u : 0u) | (od2 ? 1u : 0u), t0 + (od2 ? a0 : a1), t0 + (od2 ? a3 : a2), t0 + (od2 ? a2 : a3)};
      *(LAS u4*)(lds + L_FL + 32) = dg;
      lsig(myFL, 1);
      BST(15);
      WAITV(0);
      ST64(((u2){tag, t0 + max(max(a2, a3), 512u + OM) + od}), l == 0 ? WS_OH + (mb * 16 + hp) * 128 : WS_SZ);
    } else {
      if (WS0) SLEEP(WS0);
      wo_upto(WE < 12 ? WE : 12);
      BST(15);
      if (WE > 12) { lwait<LW1>(adFL, 0x0Fu, 1); wo_upto(WE < 22 ? WE : 22); }
      BST(16);
      if (WE > 22) { lwait<LW1>(adFL, 0x0Fu, 2); wo_upto(WE); }
      STMP(1);
      u4 v;
      _Pragma("unroll 1") for (unsigned it = 0; it < SPIN; ++it) {
        unsigned f;
        asm volatile("ds_read_b32 %0, %2 offset:20\n ds_read_b128 %1, %2 offset:32\n s_waitcnt lgkmcnt(0)" : "=&v"(f), "=&v"(v) : "v"(ldsb + L_FL) : "memory");
        if (__builtin_amdgcn_readfirstlane(f)) break;
        SLEEP(2);
      }
#pragma unroll
      for (int e = 0; e < 4; ++e) dg[e] = __builtin_amdgcn_readfirstlane(v[e]);
    }
    const int gx = dg[0] & 3u;
    __builtin_amdgcn_s_setprio(3);
    u4 sel[2];
#pragma unroll
    for (int e = 0; e < 2; ++e) {
      const unsigned v = (r15 >> 2) == g4 ? (((r15 & 3) == 2 * e ? 0x3F80u : 0u) | ((r15 & 3) == 2 * e + 1 ? 0x3F800000u : 0u)) : 0u;
      sel[0][e] = v; sel[0][2 + e] = 0u; sel[1][e] = 0u; sel[1][2 + e] = v;
    }
    u4 pv[16][2], zv0, zv1, pq[8];
    const char* kvb = uptr(wsx + WS_KVP + (size_t)hd * 32768);
    const char* zb = uptr(wsx + WS_ZP + (size_t)hd * 2048);
    const unsigned lv16 = l * 16, zoff = r15 * 128 + g4 * 32;
    const unsigned pa = ldsb + L_PQ + lw * 4096 + l * 16, omask = lw ? 0x0Au : 0x25u;
    STMP(2);
#define GLD(gq) do { _Pragma("unroll") for (int i = 0; i < 4; ++i) { const char* sb = kvb + (((gq) ^ gx) * 4 + (i & 2)) * 2048; \
      asm volatile("global_load_dwordx4 %0, %1, %2 offset:%3 sc1" : "=v"(pv[(gq) * 4 + i][0]) : "v"(lv16), "s"(sb), "i"((i & 1) * 2048) : "memory"); \
      asm volatile("global_load_dwordx4 %0, %1, %2 offset:%3 sc1" : "=v"(pv[(gq) * 4 + i][1]) : "v"(lv16), "s"(sb), "i"((i & 1) * 2048 + 1024) : "memory"); } } while (0)
#define ZLD() do { asm volatile("global_load_dwordx4 %0, %1, %2 sc1" : "=v"(zv0) : "v"(zoff), "s"(zb) : "memory"); \
      asm volatile("global_load_dwordx4 %0, %1, %2 offset:16 sc1" : "=v"(zv1) : "v"(zoff), "s"(zb) : "memory"); } while (0)
#pragma unroll
    for (int gq = 0; gq < 4; ++gq) {
      _Pragma("unroll 1") for (unsigned it = 0; it < SPIN; ++it) if ((int)(NOW() - dg[gq]) >= 0) break;
      if (gq == 3) ZLD();
      GLD(gq);
    }
    lsig(myFL, 3);
    STMP(5);
    lwait(adFL, omask, (l & 4) ? 3u : 4u);
#pragma unroll
    for (int i = 0; i < 8; ++i) RD128(pq[i], pa, (i >> 2) * 8192 + (i & 3) * 1024);
#pragma unroll
    for (int gq = 0; gq < 4; ++gq) {
      SBAR(); WAITV(gq == 0 ? 26 : gq == 1 ? 18 : gq == 2 ? 10 : 0);
      _Pragma("unroll 1") for (unsigned it = 0; it < SPIN; ++it) {
#pragma unroll
        for (int i = 0; i < 4; ++i) asm volatile("" : "+v"(pv[gq * 4 + i][0]), "+v"(pv[gq * 4 + i][1]));
        unsigned m = umax3(pv[gq * 4][0][0], pv[gq * 4][1][0], pv[gq * 4 + 1][0][0]);
        m = umax3(m, pv[gq * 4 + 1][1][0], pv[gq * 4 + 2][0][0]);
        m = umax3(m, pv[gq * 4 + 2][1][0], pv[gq * 4 + 3][0][0]);
        m = max(m, pv[gq * 4 + 3][1][0]);
        if (gq == 3) { asm volatile("" : "+v"(zv0), "+v"(zv1)); m = umax3(m, zv0[0], zv1[0]); }
        if (__ballot(m == POIS) == 0ull) break;
        RETRY();
        SLEEP(2);
        if (gq == 3) ZLD();
        GLD(gq);
        WAITV(0);
      }
      f4 ga[4];
#pragma unroll
      for (int i = 0; i < 4; ++i)
#pragma unroll
        for (int t = 0; t < 4; ++t) ga[t] = MFMA16(S8(sel[t & 1]), S8(pv[gq * 4 + i][t >> 1]), i ? ga[t] : (f4)(0.f));
#pragma unroll
      for (int t = 0; t < 4; ++t) pv[gq * 4 + (t >> 1)][t & 1] = __builtin_bit_cast(u4, ga[t]);
    }
    STMP(3);
    WAITL(0);
#pragma unroll
    for (int i = 0; i < 8; i += 2) asm volatile("" : "+v"(pq[i]), "+v"(pq[i + 1]));
    u4 kA[2], zA;
    {
      float z[8];
#pragma unroll
      for (int e = 0; e < 4; ++e) { z[e] = __uint_as_float(zv0[e]); z[4 + e] = __uint_as_float(zv1[e]); }
#define ZR(n) "v_add_f32_dpp %0, %0, %0 row_ror:" #n " row_mask:0xf bank_mask:0xf\n v_add_f32_dpp %1, %1, %1 row_ror:" #n " row_mask:0xf bank_mask:0xf\n" \
              "v_add_f32_dpp %2, %2, %2 row_ror:" #n " row_mask:0xf bank_mask:0xf\n v_add_f32_dpp %3, %3, %3 row_ror:" #n " row_mask:0xf bank_mask:0xf\n" \
              "v_add_f32_dpp %4, %4, %4 row_ror:" #n " row_mask:0xf bank_mask:0xf\n v_add_f32_dpp %5, %5, %5 row_ror:" #n " row_mask:0xf bank_mask:0xf\n" \
              "v_add_f32_dpp %6, %6, %6 row_ror:" #n " row_mask:0xf bank_mask:0xf\n v_add_f32_dpp %7, %7, %7 row_ror:" #n " row_mask:0xf bank_mask:0xf\n"
      asm volatile("s_nop 1\n" ZR(8) ZR(4) ZR(2) ZR(1) : "+v"(z[0]), "+v"(z[1]), "+v"(z[2]), "+v"(z[3]), "+v"(z[4]), "+v"(z[5]), "+v"(z[6]), "+v"(z[7]));
#pragma unroll
      for (int e = 0; e < 4; ++e) zA[e] = pk2(z[2 * e], z[2 * e + 1]);
    }
    float inv[8];
#pragma unroll
    for (int i = 0; i < 8; ++i) { f4 dn = MFMA16(S8(zA), S8(pq[i]), (f4)(0.f)); inv[i] = dn[0]; }
#pragma unroll
    for (int t = 0; t < 4; ++t) {
#define PF(gq) __builtin_bit_cast(f4, pv[(gq) * 4 + (t >> 1)][t & 1])
      f4 s = (PF(0) + PF(1)) + (PF(2) + PF(3));
      kA[t >> 1][(t & 1) * 2] = pk2(s[0], s[1]); kA[t >> 1][(t & 1) * 2 + 1] = pk2(s[2], s[3]);
    }
    f4 o0[8], o1[8];
#pragma unroll
    for (int i = 0; i < 8; ++i) {
      o0[i] = MFMA16(S8(kA[0]), S8(pq[i]), (f4)(0.f)); o1[i] = MFMA16(S8(kA[1]), S8(pq[i]), (f4)(0.f));
      inv[i] = __builtin_amdgcn_rcpf(inv[i] + 1e-6f);
    }
#pragma unroll
    for (int i = 0; i < 8; ++i) {
      o0[i] *= inv[i]; o1[i] *= inv[i];
      u4 o; o[0] = min(pk2(o0[i][0], o0[i][1]), POIS - 1u); o[1] = pk2(o0[i][2], o0[i][3]); o[2] = pk2(o1[i][0], o1[i][1]); o[3] = pk2(o1[i][2], o1[i][3]);
      ST128(o, WS_OF + (mb * 32 + hd) * 8192 + (i >> 1) * 2048 + (i & 1) * 256 + g4 * 512 + r15 * 16);
    }
    lsig(myFL, 4);
    STMP(4);
    STFLUSH();
    asm volatile(".Lend_l:");
  }

}

extern "C" void kernel_launch(void* const* d_in, const int* in_sizes, int n_in, void* d_out, int out_size,
                              void* d_ws, size_t ws_size, hipStream_t stream) {
  (void)hipFuncSetAttribute((const void*)fused, hipFuncAttributeMaxDynamicSharedMemorySize, LDS_SZ);
  fused<<<256, 512, LDS_SZ, stream>>>((const u16*)d_in[0], (const u16*)d_in[1], (const u16*)d_in[2], (const u16*)d_in[4],
                                      (const u16*)d_in[5], (const u16*)d_in[6], (const u16*)d_in[7], (char*)d_ws, (u16*)d_out);
}
```

```cpp
#define EVW 0
#define EVG (b_&7)==1
#define EVX 0
#include <hip/hip_runtime.h>
#include <stdint.h>

typedef unsigned short u16;
typedef short s4 __attribute__((ext_vector_type(4)));
typedef short s8 __attribute__((ext_vector_type(8)));
typedef float f4 __attribute__((ext_vector_type(4)));
typedef float f16v __attribute__((ext_vector_type(16)));
typedef float f2 __attribute__((ext_vector_type(2)));
typedef unsigned u4 __attribute__((ext_vector_type(4)));
typedef unsigned u2 __attribute__((ext_vector_type(2)));
typedef unsigned u8v __attribute__((ext_vector_type(8)));
typedef __bf16 bf2 __attribute__((ext_vector_type(2)));
#define LAS __attribute__((address_space(3)))
#define DEV __device__ __forceinline__

constexpr int D = 1024, F = 3584;
#define NOMOE 0
#define KD 275
#define KS 268
#define OD 195
#define OHP 1
#define FS 128
#define GSL 8
#define YSL 16
#define TP1 0
#define TP2 3
#define TLBW 0
#define FULL 1
#define HPRIO 1
#define OWN 1
#define NOPOIS 0
#define CAP 7
#define RD 4
#define DYN 1
#define JX 0
#define NOSKIP 0
#define GRP 8
#define ADV 15
#define HB 1
#define RT 1
#define WD 0
#define WU1 12
#define WU2 22
#define PL 0
#define PO 0
#define WG 0
#define DS 0
#define D1 16
#define D2 2
#define HKS 2
#define LW1 1
#define HW1 2
#define HS0 0
#define GPV 2
#define XO 1
#define TP0 3
#define G1 0x31
#define G1S 4
#define G2S 8
#define TD 1
#define YD 170
#define WU0 4
#define WE 12
#define HT 14
#define CF 19
#define KL 145
#define OL 115
#define CL 0
#define WS0 0
#define OM 0
#define HG 128
#define U0 0
#define U3 5
#define TL 0
#define MH 0
#define HWT 3
#define YG 0
#define GG 1
#define PV 1
#define U1 4
#define U2 2
#define ROT 0

constexpr unsigned WS_E = 0;
constexpr unsigned WS_YF = 524288;
constexpr unsigned WS_CAND = 12288;
constexpr unsigned WS_RG = 49152;
constexpr unsigned WS_ST = 65536;
constexpr unsigned WS_HK = 589824;
constexpr unsigned WS_ZP = 131072;
constexpr unsigned WS_CS = 196608;
constexpr unsigned WS_OH = 458752;
constexpr unsigned WS_KVP = 1u << 20;
constexpr unsigned WS_GP = 2u << 20;
constexpr unsigned WS_YP = 3u << 20;
constexpr unsigned WS_OF = 5u << 20;
constexpr unsigned WS_SZ = 9u << 20;
#define NR 4
#define RGO(tag) ((64u << 20) + ((tag) & (NR - 1u)) * (10u << 20))
#define MKWS(tag) char* const wsx = ws + RGO(tag); const __amdgpu_buffer_rsrc_t wsr = __builtin_amdgcn_make_buffer_rsrc(wsx, 0, WS_SZ, 0x00020000)

constexpr int STG = 40960;
constexpr int L_KVX = 0;
constexpr int L_PQ = 12288;
constexpr int L_M = 28672, L_RS = L_M + 512, L_CS = L_RS + 512, L_SEL = L_CS + 768, L_FL = L_SEL + 64;
constexpr int L_HX = 30720, L_WO = 32768;
constexpr int LDS_SZ = 163840;
constexpr unsigned POIS = 0xFFFFFFFFu;

DEV unsigned pk2(float a, float b) { bf2 v; v[0] = (__bf16)a; v[1] = (__bf16)b; return __builtin_bit_cast(unsigned, v); }
DEV float lo(unsigned u) { return __uint_as_float(u << 16); }
DEV float hi(unsigned u) { return __uint_as_float(u & 0xFFFF0000u); }
DEV float rbf(float a) { return lo(pk2(a, a)); }
DEV float dot2(unsigned a, unsigned b, float c) { return __builtin_amdgcn_fdot2_f32_bf16(__builtin_bit_cast(bf2, a), __builtin_bit_cast(bf2, b), c, false); }
#define RD128(dst, addr, OFF) asm volatile("ds_read_b128 %0, %1 offset:%2" : "=v"(dst) : "v"(addr), "i"(OFF) : "memory")
#define RDTR(dst, addr, OFF) asm volatile("ds_read_b64_tr_b16 %0, %1 offset:%2" : "=v"(dst) : "v"(addr), "i"(OFF) : "memory")
#define SBAR() __builtin_amdgcn_sched_barrier(0)
#define CAT(x, y) __builtin_shufflevector(x, y, 0, 1, 2, 3, 4, 5, 6, 7)
#define WAITV(n) asm volatile("s_waitcnt vmcnt(%0)" ::"i"(n) : "memory")
#define WAITL(n) asm volatile("s_waitcnt lgkmcnt(%0)" ::"i"(n) : "memory")
#define MEMB() asm volatile("" ::: "memory")
#define SLEEP(n) asm volatile("s_sleep %0" ::"i"(n) : "memory")
#define BAR() asm volatile("s_barrier" ::: "memory")
#define GL0(vo, sb, m0v) asm volatile("s_mov_b32 m0, %2\n s_nop 0\n global_load_lds_dwordx4 %0, %1" ::"v"(vo), "s"(sb), "s"(m0v) : "memory")
#define GLI(vo, sb, OFF) asm volatile("global_load_lds_dwordx4 %0, %1 offset:%2" ::"v"(vo), "s"(sb), "i"(OFF) : "memory")
#define GL4(v, sb, mm, MO) do { asm volatile("s_add_i32 m0, %2, %3\n s_nop 0\n global_load_lds_dwordx4 %0, %1" ::"v"((v)[0]), "s"(sb), "s"(mm), "i"(MO) : "memory", "scc"); \
      GLI((v)[1], sb, 1024); GLI((v)[2], sb, 2048); GLI((v)[3], sb, 3072); } while (0)
#define VFW(i) ((8 * (i) + (l >> 3)) * 2048 + (((l & 7) ^ (((l >> 4) & 1) << 2)) * 16) + (3 - (i)) * 1024)
#define MFMA16(a, b, c) __builtin_amdgcn_mfma_f32_16x16x32_bf16(a, b, c, 0, 0, 0)
#define S8(x) __builtin_bit_cast(s8, x)
#define LD128(off) __builtin_amdgcn_raw_buffer_load_b128(wsr, (off), 0, 16)
#define LD64(off) __builtin_amdgcn_raw_buffer_load_b64(wsr, (off), 0, 16)
#define ST128(v, off) __builtin_amdgcn_raw_buffer_store_b128((v), wsr, (off), 0, 16)
#define ST64(v, off) __builtin_amdgcn_raw_buffer_store_b64((v), wsr, (off), 0, 16)
#define OFA(i) (WS_OF + ((mb * 32 + hd) * 8 + (i)) * 1024 + l * 16)
#define POISON(i) ST128(pz, (i) < 2 ? WS_KVP + (hd * 16 + mb) * 2048 + (i) * 1024 + l * 16 : (i) == 2 ? WS_ZP + (hd * 16 + mb) * 128 + (l & 7) * 16 : OFA((i) - 3))
#define NOW() ((unsigned)__builtin_amdgcn_s_memrealtime())
constexpr unsigned SPIN = 1u << 21;
DEV const char* uptr(const char* p) {
  unsigned long long v = (unsigned long long)p;
  unsigned lo_ = __builtin_amdgcn_readfirstlane((unsigned)v), hi_ = __builtin_amdgcn_readfirstlane((unsigned)(v >> 32));
  return (const char*)(((unsigned long long)hi_ << 32) | lo_);
}
DEV void lsig(unsigned addr, unsigned val) { asm volatile("s_waitcnt lgkmcnt(0)\n ds_write_b32 %0, %1" ::"v"(addr), "v"(val) : "memory"); }
template <int SL = 0> DEV void lwait(unsigned addr, unsigned mask, unsigned target) {
  _Pragma("unroll 1") for (unsigned it = 0; it < SPIN; ++it) {
    unsigned v;
    asm volatile("ds_read_b32 %0, %1\n s_waitcnt lgkmcnt(0)" : "=v"(v) : "v"(addr) : "memory");
    if (((unsigned)__ballot(v < target) & mask) == 0u) break;
    if (SL) SLEEP(SL);
  }
}
DEV unsigned nopois(unsigned a) { return min(a, POIS - 1u); }
DEV u4 tagf(f4 v, unsigned tag) {
  u4 r;
#pragma unroll
  for (int e = 0; e < 4; ++e) r[e] = __builtin_amdgcn_perm(__float_as_uint(v[e]), tag, 0x07060500u + e);
  return r;
}
DEV void untag(u4& v, unsigned tag, unsigned& bad) {
#pragma unroll
  for (int e = 0; e < 4; ++e) v[e] ^= (tag >> (8 * e)) & 255u;
  bad |= v[0] | v[1]; bad |= v[2] | v[3];
}
DEV unsigned umax3(unsigned a, unsigned b, unsigned c) { return max(max(a, b), c); }
template <int C> DEV float dppf(float v) { return __int_as_float(__builtin_amdgcn_update_dpp(0, __float_as_int(v), C, 0xf, 0xf, false)); }
DEV unsigned wave_max(unsigned v) {
  v = max(v, (unsigned)__builtin_amdgcn_update_dpp(0, (int)v, 0x111, 0xf, 0xf, false));
  v = max(v, (unsigned)__builtin_amdgcn_update_dpp(0, (int)v, 0x112, 0xf, 0xf, false));
  v = max(v, (unsigned)__builtin_amdgcn_update_dpp(0, (int)v, 0x114, 0xf, 0xf, false));
  v = max(v, (unsigned)__builtin_amdgcn_update_dpp(0, (int)v, 0x118, 0xf, 0xf, false));
  v = max(v, (unsigned)__builtin_amdgcn_update_dpp(0, (int)v, 0x142, 0xa, 0xf, false));
  v = max(v, (unsigned)__builtin_amdgcn_update_dpp(0, (int)v, 0x143, 0xc, 0xf, false));
  return (unsigned)__builtin_amdgcn_readlane((int)v, 63);
}
DEV unsigned phi2(f2 u, f2 rs) {
  f2 t = u * rs;
  f2 a = t * 1.44269504f, p = t + 1.f, r;
  r[0] = __builtin_amdgcn_fmed3f(__builtin_amdgcn_exp2f(a[0]), p[0], 1.f);
  r[1] = __builtin_amdgcn_fmed3f(__builtin_amdgcn_exp2f(a[1]), p[1], 1.f);
  r += 1e-6f;
  return pk2(r[0], r[1]);
}

#define STDECL unsigned st_[16] = {0, 0, 0, 0, 0, 0, 0, 0, 0, 0, 0, 0, 0, 0, 0, 0}
#define STMP(i) st_[i] = (unsigned)__builtin_amdgcn_s_memrealtime()
#define BST(i)
#undef HB
#define HB 0
#define BSTV cw
#define RETRY()
#define RETRY2()
#define STFLUSH() do { const int b_ = blockIdx.x; if (w == EVW && (EVG)) { const unsigned v_ = min((unsigned)(EVX), 300000u); _Pragma("unroll 1") for (unsigned it = 0; it < SPIN; ++it) { if (NOW() - st_[0] >= 4500u + v_) break; SLEEP(1); } } } while (0)

DEV float half_sum(float v) {
  asm volatile("s_nop 1\n v_add_f32_dpp %0, %0, %0 row_ror:8 row_mask:0xf bank_mask:0xf\n s_nop 1\n v_add_f32_dpp %0, %0, %0 row_ror:4 row_mask:0xf bank_mask:0xf\n"
               "s_nop 1\n v_add_f32_dpp %0, %0, %0 row_ror:2 row_mask:0xf bank_mask:0xf\n s_nop 1\n v_add_f32_dpp %0, %0, %0 row_ror:1 row_mask:0xf bank_mask:0xf\n s_nop 1" : "+v"(v));
  auto r = __builtin_amdgcn_permlane16_swap(__float_as_uint(v), __float_as_uint(v), false, false);
  return __uint_as_float(r[0]) + __uint_as_float(r[1]);
}

struct FQ { s8 a[4]; s4 b[12]; };
template <bool B> struct BC { static constexpr bool v = B; };
template <int I> struct IC { static constexpr int v = I; };

__global__ __launch_bounds__(512) void fused(const u16* __restrict__ x, const u16* __restrict__ wqkv, const u16* __restrict__ wo,
                                             const u16* __restrict__ wrt, const u16* __restrict__ wg, const u16* __restrict__ wu,
                                             const u16* __restrict__ wd, char* __restrict__ ws, u16* __restrict__ out) {
  extern __shared__ __attribute__((aligned(16))) char lds[];
  asm volatile(".Lcode_beg:");
  const int w = __builtin_amdgcn_readfirstlane(threadIdx.x >> 6), l = threadIdx.x & 63;
  STDECL;
  STMP(0);
  const int g = blockIdx.x, xcd = g & 7, j = g >> 3, ii = j >> 2;
  unsigned cw = 0, cwo = 0; unsigned long long cwb = 0;
  if (w == 7) {
    unsigned long long pc; unsigned off, len, l1, l2, l3;
    asm volatile("s_getpc_b64 %0\n.Lcode_here:" : "=s"(pc));
    asm volatile("s_mov_b32 %0, (.Lcode_here - .Lcode_beg)" : "=s"(off));
    asm volatile("s_mov_b32 %0, (.Lend_c - .Lcode_beg)" : "=s"(len));
    asm volatile("s_mov_b32 %0, (.Lend_l - .Lcode_beg)" : "=s"(l1));
    asm volatile("s_mov_b32 %0, (.Lend_h - .Lcode_beg)" : "=s"(l2));
    asm volatile("s_mov_b32 %0, (.Lend_s - .Lcode_beg)" : "=s"(l3));
    len = max(max(len, l3), max(l1, l2));
    const unsigned o = min((unsigned)(l * 32 + j) * 64u, len - 4u);
    asm volatile("global_load_dword %0, %1, %2" : "+v"(cw) : "v"(o), "s"(pc - off) : "memory");
    cwo = o; cwb = pc - off;
  }
#define PTAB 0xA98FEDC1045B7623ull
#define PHT 0x6624924999924924ull
#define CX 4
  const int mb = (xcd & 1) * 8 + ii, hp = (xcd >> 1) * 4 + (j & 3);
  const bool sp = (hp & 7) == 3;
  const int rot = 2 * xcd + 4 * (ii & 3);
  const int nb = hp;
#define PAIRAT(p) ((int)(PTAB >> (4 * (p))) & 15)
#define HEADAT(s) (2 * PAIRAT((s) >> 1) + ((s) & 1))
  const int ph = (int)(PHT >> (2 * j)) & 3;
#define HQT 0xDCBA098765320410ull
#define CXA 1
  const int bq = ((int)(HQT >> (4 * hp)) & 15) * 4 + (ii & 3), ksp = (xcd & 1) * 2 + (ii >> 2);
#define CHUNK(b) ((b) < 14 ? 4 * (b) + 3 : ((b) - 14) + ((((b) - 14) * 43) >> 7))
  const int chunk = CHUNK(bq), bg = (CXA || bq < 14) ? bq ^ (w & 1) : bq, chg = CHUNK(bg);
  const unsigned ldsb = (unsigned)(size_t)(LAS char*)lds;
  const unsigned adFL = ldsb + L_FL + 4 * (l & 7), myFL = ldsb + L_FL + 4 * w;
  const __amdgpu_buffer_rsrc_t wse = __builtin_amdgcn_make_buffer_rsrc(ws, 0, 64, 0x00020000);
  const int r15 = l & 15, g4 = l >> 4, q = (l & 15) >> 2, p = l & 3;
  const char* wob = uptr((const char*)wo + nb * 128);

  if (w != 4 && w != 5) {
  const unsigned ev0 = __builtin_amdgcn_raw_buffer_load_b32(wse, WS_E, 0, 16);
  if (w >= 6) {
    const int mw = w - 6;
    auto helper = [&](auto SP) {
    constexpr bool spare = decltype(SP)::v, moe = !spare && !NOMOE;
    int nbar = 0;
#define TICK() do { if (HB) { BST(nbar); asm volatile("" : "+v"(BSTV)); } SBAR(); BAR(); SBAR(); ++nbar; } while (0)
#define DTICK(lim) do { if (nbar < (lim)) TICK(); else SLEEP(2); } while (0)
    const int hd = hp * 2 + mw;
    const u4 pz = (u4)(POIS);
    const unsigned tag = __builtin_amdgcn_readfirstlane(ev0) + 1u;
    MKWS(tag);
    const int np = l & 7, kset = (l >> 3) & 1;
    u4 gw[4][8], dw[2][8];
    const __amdgpu_buffer_rsrc_t wgr = __builtin_amdgcn_make_buffer_rsrc((void*)(mw ? wu : wg), 0, D * F * 2, 0x00020000);
    const __amdgpu_buffer_rsrc_t wdr = __builtin_amdgcn_make_buffer_rsrc((void*)wd, 0, D * F * 2, 0x00020000);
    const __amdgpu_buffer_rsrc_t wor = __builtin_amdgcn_make_buffer_rsrc((void*)wo, 0, D * D * 2, 0x00020000);
    const unsigned gvo = ((256 * ksp + 32 * kset + 8 * g4) * F + 64 * chg + 8 * np) * 2;
    const unsigned dvo = ((64 * chunk + 8 * g4) * D + 256 * ksp + 128 * mw + 8 * r15) * 2;
    auto burst = [&](int k, int i0 = 0, int i1 = 16) {
      if (moe) {
#pragma unroll
        for (int i = i0; i < i1; ++i) {
          if (k < 2) gw[(2 * k + (i >> 3)) & 3][i & 7] = __builtin_amdgcn_raw_buffer_load_b128(wgr, gvo, (128 * k + 64 * (i >> 3) + (i & 7)) * F * 2, 2);
          else dw[i >> 3][i & 7] = __builtin_amdgcn_raw_buffer_load_b128(wdr, dvo, (32 * (i >> 3) + (i & 7)) * D * 2, 2);
        }
      }
    };
    auto transp = [&](u4(&v)[8]) {
      if (moe) {
        u4 f[8];
#pragma unroll
        for (int c = 0; c < 8; ++c)
#pragma unroll
          for (int e = 0; e < 4; ++e) {
            const unsigned ev = v[2 * e][c >> 1], od = v[2 * e + 1][c >> 1];
            f[c][e] = (c & 1) ? __builtin_amdgcn_perm(od, ev, 0x07060302u) : __builtin_amdgcn_perm(od, ev, 0x05040100u);
          }
#pragma unroll
        for (int c = 0; c < 8; ++c) { v[c] = f[c]; asm volatile("" : "+v"(v[c])); }
      }
    };
    auto pois = [&]() {
      ST128(pz, WS_KVP + (hd * 16 + mb) * 2048 + l * 16); ST128(pz, WS_KVP + (hd * 16 + mb) * 2048 + 1024 + l * 16); ST128(pz, WS_ZP + (hd * 16 + mb) * 128 + (l & 7) * 16);
    };
    unsigned wt0, wt1, wt2 = 0, wt3 = 0;
    auto touch = [&]() {
      const unsigned to = (j * 32 + mw * 16 + (l >> 3)) * 2048 + (xcd >> 1) * 512 + (l & 7) * 64;
      wt0 = __builtin_amdgcn_raw_buffer_load_b32(wor, to, 0, 0); wt1 = __builtin_amdgcn_raw_buffer_load_b32(wor, to, 16384, 0);
      if (TLBW) {
        const __amdgpu_buffer_rsrc_t outr = __builtin_amdgcn_make_buffer_rsrc(out, 0, 2048 * D * 2, 0x00020000);
        wt2 = __builtin_amdgcn_raw_buffer_load_b32(outr, (mb * 128 + mw * 64 + (l & 3) * 16) * 2048 + nb * 128, 0, 0);
        wt3 = __builtin_amdgcn_raw_buffer_load_b32(wsr, (l & 1) ? WS_GP + g * 4096 + mw * 2048 : WS_YP + (g >> 2) * 32768 + (l & 2) * 8192, 0, 0);
      }
    };
    s4 sb[24];
    auto colrd = [&](int h) {
      const unsigned bo = ldsb + (1 + h) * STG + 16384 + (8 * g4 + q) * 128 + (p & 1) * 8;
      const int sw = ((q >> 1) << 2) | ((g4 & 1) << 1);
      const unsigned b0 = bo + (((mw * 4 + (p >> 1)) ^ sw) * 16), b1 = bo + (((mw * 4 + 2 + (p >> 1)) ^ sw) * 16);
#pragma unroll
      for (int n = 0; n < 12; ++n) { RDTR(sb[2 * n], (n & 1) ? b1 : b0, ((n % 6) >> 1) * 8192 + (n / 6) * 4096); RDTR(sb[2 * n + 1], (n & 1) ? b1 : b0, ((n % 6) >> 1) * 8192 + (n / 6) * 4096 + 512); }
      WAITL(0);
      SBAR();
    };
    auto colsum = [&]() {
      const u4 ones = (u4)(0x3F803F80u);
      f4 c[6];
#pragma unroll
      for (int n = 0; n < 12; ++n) c[n % 6] = MFMA16(S8(ones), CAT(sb[2 * n], sb[2 * n + 1]), n >= 6 ? c[n % 6] : (f4)(0.f));
      const u4 csg = (u4){__float_as_uint(g4 ? c[3][0] : c[0][0]), __float_as_uint(g4 ? c[4][0] : c[1][0]), __float_as_uint(g4 ? c[5][0] : c[2][0]), tag};
      WAITV(0);
      ST128(csg, l < 32 ? WS_CS + (hd * 16 + mb) * 512 + l * 16 : WS_SZ);
    };
    u4 stv, cv[8], cd; float cs[3];
    auto gload = [&]() {
      stv = LD128(WS_ST + (mb * 128 + mw * 64 + l) * 16);
#pragma unroll
      for (int i = 0; i < 8; ++i) cv[i] = LD128(WS_CS + hd * 8192 + i * 1024 + l * 16);
    };
    auto gbad = [&]() {
      unsigned bad = (stv[1] ^ tag) | (stv[3] ^ tag);
#pragma unroll
      for (int i = 0; i < 8; ++i) bad |= cv[i][3] ^ tag;
      return __ballot(bad != 0u) != 0ull;
    };
    int idx;
    auto gfin = [&]() {
      if (gbad()) { RETRY(); _Pragma("unroll 1") for (unsigned it = 0; it < SPIN; ++it) { SLEEP(2); gload(); if (!gbad()) break; } }
#pragma unroll
      for (int e = 0; e < 3; ++e) {
        float a = __uint_as_float(cv[0][e]);
#pragma unroll
        for (int i = 1; i < 8; ++i) a += __uint_as_float(cv[i][e]);
        auto r = __builtin_amdgcn_permlane32_swap(__float_as_uint(a), __float_as_uint(a), false, false);
        cs[e] = __uint_as_float(r[0]) + __uint_as_float(r[1]);
      }
      asm volatile("" : "+v"(cs[0]), "+v"(cs[1]), "+v"(cs[2]) : "v"(wt0), "v"(wt1), "v"(wt2), "v"(wt3));
    };
    auto cload = [&]() { cd = LD128(WS_CAND + (((hp >> 2) == 2) * 16 + mb) * 128 + (l & 7) * 16); };
    auto cfin = [&]() {
      if (__builtin_expect(__ballot(cd[1] != tag || cd[3] != tag) != 0ull, 0)) {
        RETRY();
        _Pragma("unroll 1") for (unsigned it = 0; it < SPIN; ++it) { SLEEP(4); cload(); if (__ballot(cd[1] != tag || cd[3] != tag) == 0ull) break; }
      }
      cd[0] = max(cd[0], cd[2]);
      idx = (int)(cd[0] & 2047u);
    };
    auto lpub = [&]() {
      *(LAS float*)(lds + L_M + (mw * 64 + l) * 4) = __uint_as_float(stv[0]);
      *(LAS float*)(lds + L_RS + (mw * 64 + l) * 4) = __uint_as_float(stv[2]);
      if (l < 32) {
#pragma unroll
        for (int e = 0; e < 3; ++e) *(LAS float*)(lds + L_CS + mw * 384 + ((3 * g4 + e) * 16 + r15) * 4) = cs[e];
      }
      if (mw == 0 && l < 8) *(LAS int*)(lds + L_SEL + l * 4) = idx;
      lsig(myFL, 1u);
    };
    u4 xs[4]; float xrs;
    asm volatile("" ::"s"(wg), "s"(wu), "s"(wd), "v"(cw));
    if (HPRIO) __builtin_amdgcn_s_setprio(HPRIO);
    TICK();
    if (spare) {
      TICK();
      pois();
      if (ii >> 2) TICK();
      colrd(ii >> 2);
      if (!(ii >> 2)) TICK();
      u4 kk[8];
      auto kload = [&]() {
#pragma unroll
        for (int i = 0; i < 8; ++i) kk[i] = LD128(WS_RG + mw * 8192 + i * 1024 + l * 16);
      };
      kload();
      TICK();
      _Pragma("unroll 1") for (unsigned it = 0; it < SPIN; ++it) {
        unsigned bad = 0;
#pragma unroll
        for (int i = 0; i < 8; ++i) { bad |= (kk[i][1] ^ tag) | (kk[i][3] ^ tag); asm volatile("" : "+v"(bad)); }
        if (__ballot(bad != 0u) == 0ull) break;
        RETRY();
        kload();
        DTICK(7);
      }
      __builtin_amdgcn_s_setprio(3);
      unsigned m1 = max(kk[0][0], kk[0][2]), m2 = min(kk[0][0], kk[0][2]), m3 = 0;
#pragma unroll
      for (int i = 2; i < 16; ++i) {
        const unsigned a = kk[i >> 1][(i & 1) * 2];
        asm("v_med3_u32 %0, %1, %2, %0" : "+v"(m3) : "v"(m2), "v"(a));
        asm("v_med3_u32 %0, %1, %2, %0" : "+v"(m2) : "v"(m1), "v"(a));
        m1 = max(m1, a);
      }
      unsigned mine8 = 0;
#pragma unroll
      for (int it = 0; it < 8; ++it) {
        const unsigned km = wave_max(m1);
        mine8 = l == it ? km : mine8;
        const bool win = m1 == km;
        if (it < 7) { m1 = win ? m2 : m1; m2 = win ? m3 : m2; m3 = win ? 0u : m3; }
        if (it == 3 && nbar < 7) TICK();
      }
      if (__builtin_expect(__ballot(m1 == 0u) != 0ull, 0)) {
        unsigned lim = 0;
#pragma unroll 1
        for (int it = 0; it < 8; ++it) {
          unsigned m = 0;
#pragma unroll
          for (int i = 0; i < 8; ++i) m = umax3(m, kk[i][0] - lim, kk[i][2] - lim);
          lim += wave_max(m);
          mine8 = l == it ? lim : mine8;
        }
      }
      ST64(((u2){mine8, tag}), l < 8 ? WS_CAND + ((hp >> 3) * 16 + mb) * 128 + (mw ? 7 - l : l) * 16 + mw * 8 : WS_SZ);
      __builtin_amdgcn_s_setprio(HPRIO);
      STMP(2);
      TICK();
      colsum();
      DTICK(9);
      touch();
      while (nbar < 10) TICK();
      gload(); cload();
      if (RT && mw) asm volatile("global_load_dword %0, %1, %2 sc1" : "+v"(cw) : "v"(cwo), "s"(cwb) : "memory");
      TICK(); TICK();
      WAITV(0);
      asm volatile("" : "+v"(cw));
      gfin(); cfin();
      STMP(3);
      TICK(); TICK(); TICK();
      lpub();
    } else {
      if (PV == 0) pois();
      TICK();
      if (PV == 1) pois();
      if (ii >> 2) TICK();
      colrd(ii >> 2);
      TICK();
      if (CL && !(ii >> 2)) TICK();
      colsum();
      if (!CL && !(ii >> 2)) TICK();
      u4 sv; f4 acc[8];
      auto xload = [&]() {
        const u16* xp = x + (size_t)idx * D + 256 * ksp + 32 * kset + 8 * g4;
        sv = LD128(WS_ST + idx * 16);
#pragma unroll
        for (int it = 0; it < 4; ++it) xs[it] = *(const u4*)(xp + 64 * it);
        SBAR();
      };
      auto xnorm = [&]() {
        if (__builtin_expect(__ballot(sv[1] != tag || sv[3] != tag) != 0ull, 0)) {
          _Pragma("unroll 1") for (unsigned it = 0; it < SPIN; ++it) { SLEEP(1); sv = LD128(WS_ST + idx * 16); if (__ballot(sv[1] != tag || sv[3] != tag) == 0ull) break; }
        }
        const float nm = -__uint_as_float(sv[0]);
        xrs = __uint_as_float(sv[2]);
#pragma unroll
        for (int it = 0; it < 4; ++it) {
          float t0, t1, t2, t3; unsigned x0 = xs[it][0], x1 = xs[it][1], x2 = xs[it][2], x3 = xs[it][3];
          asm volatile("v_dot2_f32_bf16 %4, %0, %8, %10\n v_dot2_f32_bf16 %0, %0, %9, %10\n v_dot2_f32_bf16 %5, %1, %8, %10\n v_dot2_f32_bf16 %1, %1, %9, %10\n"
                       "v_dot2_f32_bf16 %6, %2, %8, %10\n v_dot2_f32_bf16 %2, %2, %9, %10\n v_dot2_f32_bf16 %7, %3, %8, %10\n v_dot2_f32_bf16 %3, %3, %9, %10\n"
                       "v_cvt_pk_bf16_f32 %0, %4, %0\n v_cvt_pk_bf16_f32 %1, %5, %1\n v_cvt_pk_bf16_f32 %2, %6, %2\n s_nop 0\n v_cvt_pk_bf16_f32 %3, %7, %3"
                       : "+v"(x0), "+v"(x1), "+v"(x2), "+v"(x3), "=&v"(t0), "=&v"(t1), "=&v"(t2), "=&v"(t3) : "s"(0x00003F80u), "s"(0x3F800000u), "v"(nm));
          xs[it] = (u4){x0, x1, x2, x3};
        }
        asm volatile("s_nop 1" : "+v"(xs[0]), "+v"(xs[1]), "+v"(xs[2]), "+v"(xs[3]), "+v"(xrs));
        SBAR();
      };
      auto gmul = [&](int i0, int i1) {
#pragma unroll
        for (int it = i0; it < i1; ++it)
#pragma unroll
          for (int c = 0; c < 8; ++c) acc[c] = MFMA16(S8(gw[it][c]), S8(xs[it]), it ? acc[c] : (f4)(0.f));
#pragma unroll
        for (int c = 0; c < 8; ++c) asm volatile("" : "+v"(acc[c]));
        SBAR();
      };
      auto gout = [&]() {
        unsigned go = r15 < 8 ? WS_GP + ((chg * 4 + ksp) * 2 + mw) * 2048 + (g4 >> 1) * 1024 + (g4 & 1) * 512 + r15 * 16 : WS_SZ;
        asm volatile("" : "+v"(go));
#pragma unroll
        for (int i = 0; i < 4; ++i) {
          unsigned a[4], b[4];
#pragma unroll
          for (int c = 0; c < 4; ++c) {
            auto r = __builtin_amdgcn_permlane32_swap(__float_as_uint(acc[c][i]), __float_as_uint(acc[c + 4][i]), false, false);
            a[c] = r[0]; b[c] = r[1];
          }
          unsigned s0, s1, s2, s3;
#define AR(d, x, y) "v_add_f32_dpp %" #d ", %" #x ", %" #y " row_ror:8 row_mask:0xf bank_mask:0xf\n"
          asm volatile("s_nop 1\n" AR(0, 8, 4) AR(1, 9, 5) AR(2, 10, 6) AR(3, 11, 7) : "=&v"(s0), "=&v"(s1), "=&v"(s2), "=&v"(s3)
                       : "v"(a[0]), "v"(a[1]), "v"(a[2]), "v"(a[3]), "v"(b[0]), "v"(b[1]), "v"(b[2]), "v"(b[3]));
          ST128(tagf(__builtin_bit_cast(f4, ((u4){s0, s1, s2, s3})), tag), go + i * 128);
        }
        SBAR();
      };
      for (int i = 0; i < ph; ++i) TICK();
      burst(0); touch();
      TICK();
      TICK();
      if (TL) { transp(gw[0]); transp(gw[1]); }
      TICK();
      if (!TL) transp(gw[0]);
      TICK();
      if (!TL) transp(gw[1]);
      TICK();
      if (ph) cload();
      if (RT && mw) asm volatile("global_load_dword %0, %1, %2 sc1" : "+v"(cw) : "v"(cwo), "s"(cwb) : "memory");
      SBAR();
      burst(1);
      TICK();
      TICK(); TICK();
      transp(gw[2]);
      WAITV(0);
      asm volatile("" : "+v"(cw));
      if (ph < 2) TICK();
      transp(gw[3]);
      if (ph < 1) { SBAR(); cload(); TICK(); }
      SBAR();
      if (XO == 0) { cfin(); xload(); gload(); } else { gload(); SBAR(); cfin(); xload(); }
      TICK();
      gfin();
      lpub();
      SBAR();
      __builtin_amdgcn_s_setprio(0);
      burst(2, 0, D1);
      STMP(3);
      SBAR();
      if (HS0) SLEEP(HS0);
      if (G1 & 1) { lwait<HW1>(adFL, 0x0Fu, G1 >> 4); if (G1S) SLEEP(G1S); __builtin_amdgcn_s_setprio(TP0); }
      xnorm();
      if (G1 & 2) { lwait<2>(adFL, 0x0Fu, G1 >> 4); if (G1S) SLEEP(G1S); __builtin_amdgcn_s_setprio(TP0); }
      STMP(5);
      gmul(0, 4);
      gout();
      STMP(6);
      SBAR();
    }
    __builtin_amdgcn_s_setprio(TP1);
    STMP(4);
    if (moe) {
      if (D2 == 0) { burst(2, D1, 16); SBAR(); }
      if (D1 >= 8 || D2 == 0) transp(dw[0]); if (D1 == 16 || D2 == 0) transp(dw[1]);
      SBAR();
      lwait<2>(adFL, 0x30u, 3);
      if (G2S) SLEEP(G2S);
      STMP(7);
      if (D2 == 1) { burst(2, D1, 16); SBAR(); }
      f4 acc[8];
      u4 ha[2];
      {
        u4 pg[4][2];
        auto pload = [&]() {
#pragma unroll
          for (int ks = 0; ks < 4; ++ks)
#pragma unroll
            for (int t = 0; t < 2; ++t) pg[ks][t] = LD128(WS_GP + ((chunk * 4 + ks) * 4 + 2 * t + mw) * 1024 + (4 * kset + g4) * 128 + np * 16);
        };
        auto pbad = [&]() {
          unsigned bad = 0;
#pragma unroll
          for (int i = 0; i < 8; ++i) untag(pg[i >> 1][i & 1], tag, bad);
          return __ballot((bad & 255u) != 0u) != 0ull;
        };
        pload();
        SBAR();
        if (D2 == 2) { burst(2, D1, 16); SBAR(); }
        __builtin_amdgcn_s_setprio(TP2);
        if (D1 < 8 && D2 == 1) transp(dw[0]);
        if (D1 < 16 && D2 == 1) transp(dw[1]);
        SBAR();
        if (__builtin_expect(pbad(), 0)) { _Pragma("unroll 1") for (unsigned it = 0; it < SPIN; ++it) { RETRY2(); SLEEP(4); pload(); if (!pbad()) break; } }
        STMP(10);
        f4 gq[2];
#pragma unroll
        for (int t = 0; t < 2; ++t)
          gq[t] = ((__builtin_bit_cast(f4, pg[0][t]) + __builtin_bit_cast(f4, pg[1][t])) + (__builtin_bit_cast(f4, pg[2][t]) + __builtin_bit_cast(f4, pg[3][t]))) * xrs;
        float hv[4];
#pragma unroll
        for (int e = 0; e < 4; ++e) {
          const float gg = rbf(gq[0][e]), uu = rbf(gq[1][e]);
          hv[e] = rbf(gg / (1.f + __expf(-gg))) * uu;
        }
        *(LAS u2*)(lds + L_HX + l * 16 + mw * 8) = (u2){pk2(hv[0], hv[1]), pk2(hv[2], hv[3])};
        lsig(myFL, 2u);
        if (D1 < 16 && D2 == 2) { SBAR(); if (D1 < 8) transp(dw[0]); transp(dw[1]); SBAR(); }
        lwait(adFL, 0xC0u, 2);
        ha[0] = *(const LAS u4*)(lds + L_HX + l * 16); ha[1] = *(const LAS u4*)(lds + L_HX + (l ^ 8) * 16);
      }
#pragma unroll
      for (int it = 0; it < 2; ++it)
#pragma unroll
        for (int c = 0; c < 8; ++c) acc[c] = MFMA16(S8(ha[it]), S8(dw[it][c]), it ? acc[c] : (f4)(0.f));
      if (g4 < 2) {
#pragma unroll
        for (int i = 0; i < 4; ++i) {
          const unsigned yo = WS_YP + ((chunk * 8 + 4 * g4 + i) * D + 256 * ksp + 128 * mw + 4 * r15) * 4;
          ST128(tagf((f4){acc[0][i], acc[1][i], acc[2][i], acc[3][i]}, tag), yo);
          ST128(tagf((f4){acc[4][i], acc[5][i], acc[6][i], acc[7][i]}, tag), yo + 256);
        }
      }
      STMP(11);
    }
    float sg;
    {
      const unsigned mono = cd[0] >> 16, rb = (mono & 0x8000u) ? (mono & 0x7FFFu) : (~mono & 0xFFFFu);
      sg = rbf(1.f / (1.f + __expf(-lo(rb))));
    }
    unsigned mine = (unsigned)__ballot(!NOMOE && l < 8 && (idx >> 7) == mb && (l & 1) == mw);
    if (mine) {
      if (spare) lwait<4>(adFL, 0x30u, 3);
      {
        const u2 f = LD64(WS_OH + (mb * 16 + r15) * 128);
        const unsigned t0 = NOW();
        unsigned rel = f[1] + YD - t0;
        rel = wave_max(f[0] == tag && rel < 600u ? rel : 0u);
        _Pragma("unroll 1") for (unsigned it = 0; it < SPIN; ++it) { if (NOW() - t0 >= rel) break; SLEEP(2); }
      }
      STMP(8);
      lwait<1>(adFL, 0x0Fu, 4);
      __builtin_amdgcn_s_setprio(TP2);
      while (mine) {
        const int t = __builtin_ctz(mine); mine &= mine - 1;
        u4 yv[14];
        _Pragma("unroll 1") for (unsigned it = 0; it < SPIN; ++it) {
          unsigned bad = 0;
#pragma unroll
          for (int i = 0; i < 14; ++i) yv[i] = LD128(WS_YP + (((i * 4 + g4) * 8 + t) * D + (nb >> 1) * 128 + (r15 >> 3) * 64 + (nb & 1) * 32 + (r15 & 7) * 4) * 4);
          SBAR();
#pragma unroll
          for (int i = 0; i < 14; ++i) untag(yv[i], tag, bad);
          if (__ballot((bad & 255u) != 0u) == 0ull) break;
          RETRY();
          SLEEP(YSL);
        }
        f4 y = __builtin_bit_cast(f4, yv[0]);
#pragma unroll
        for (int i = 1; i < 14; ++i) y += __builtin_bit_cast(f4, yv[i]);
        const float gt = __int_as_float(__builtin_amdgcn_readlane(__float_as_int(sg), t));
#pragma unroll
        for (int e = 0; e < 4; ++e) {
          auto r = __builtin_amdgcn_permlane32_swap(__float_as_uint(y[e]), __float_as_uint(y[e]), false, false);
          const float a = __uint_as_float(r[0]) + __uint_as_float(r[1]);
          auto r2 = __builtin_amdgcn_permlane16_swap(__float_as_uint(a), __float_as_uint(a), false, false);
          y[e] = rbf(rbf(__uint_as_float(r2[0]) + __uint_as_float(r2[1])) * gt);
        }
        if (l < 16) *(LAS f4*)(lds + L_KVX + t * 256 + (l & 7) * 32 + (l >> 3) * 16) = y;
      }
    }
    lsig(myFL, 3u);
    STMP(9);
    STFLUSH();
    if (spare) asm volatile(".Lend_s:"); else asm volatile(".Lend_h:");
    };
    if (sp) helper(BC<true>()); else helper(BC<false>());
    return;
  }

  const int wr = w >> 1, wc = w & 1;
  unsigned tag; u2 kg; u4 sg4;
  const int srow = mb * 128 + hp * 8 + 2 * w + (l >> 5);
  {
    const int row = srow;
    u4 xa[4], rw[4];
#pragma unroll
    for (int c = 0; c < 4; ++c) xa[c] = *(const u4*)(x + row * D + (l & 31) * 8 + 256 * c);
#pragma unroll
    for (int c = 0; c < 4; ++c) rw[c] = *(const u4*)(wrt + (l & 31) * 8 + 256 * c);
    float s0 = 0, s1 = 0;
#pragma unroll
    for (int c = 0; c < 4; ++c)
#pragma unroll
      for (int e = 0; e < 4; e += 2) { unsigned x0 = xa[c][e], x1 = xa[c][e + 1]; s0 = dot2(x0, 0x3F803F80u, s0); s1 = dot2(x1, 0x3F803F80u, s1); }
    const float m = half_sum(s0 + s1) * (1.f / D);
    f2 v[16], q2 = (f2)(0.f), q3 = (f2)(0.f);
#pragma unroll
    for (int c = 0; c < 16; ++c) v[c] = (f2){lo(xa[c >> 2][c & 3]), hi(xa[c >> 2][c & 3])} - m;
#pragma unroll
    for (int c = 0; c < 16; c += 2) { q2 += v[c] * v[c]; q3 += v[c + 1] * v[c + 1]; }
    q2 += q3;
    const float mr = rbf(m);
    const float rs = 1.f / rbf(sqrtf(rbf(rbf(half_sum(q2[0] + q2[1]) * (1.f / D)) + 1e-5f)));
    float d0 = 0, d1 = 0;
#pragma unroll
    for (int c = 0; c < 16; c += 2) {
      const f2 e0 = (f2){lo(xa[c >> 2][c & 3]), hi(xa[c >> 2][c & 3])} - mr, e1 = (f2){lo(xa[c >> 2][(c & 3) + 1]), hi(xa[c >> 2][(c & 3) + 1])} - mr;
      const unsigned p0 = pk2(e0[0], e0[1]), p1 = pk2(e1[0], e1[1]);
      const f2 n0 = (f2){lo(p0), hi(p0)} * rs, n1 = (f2){lo(p1), hi(p1)} * rs;
      unsigned c0 = rw[c >> 2][c & 3], c1 = rw[c >> 2][(c & 3) + 1];
      d0 = dot2(pk2(n0[0], n0[1]), c0, d0); d1 = dot2(pk2(n1[0], n1[1]), c1, d1);
    }
    const float d = half_sum(d0 + d1);
    const unsigned rb = pk2(d, d) & 0xFFFFu;
    const unsigned mono = (rb & 0x8000u) ? (~rb & 0xFFFFu) : (rb | 0x8000u);
    tag = __builtin_amdgcn_readfirstlane(ev0) + 1u;
    kg = (u2){(mono << 16) | (unsigned)row, tag};
    sg4 = (u4){__float_as_uint(mr), tag, __float_as_uint(rs), tag};
  }
  MKWS(tag);
  if ((l & 31) == 0) { ST64(kg, WS_RG + srow * 8); ST128(sg4, WS_ST + srow * 16); }
  unsigned aad[2], bad[2];
#pragma unroll
  for (int s = 0; s < 2; ++s) aad[s] = ldsb + (wr * 64 + r15) * 128 + (((4 * s + g4) ^ ((r15 >> 1) & 7)) * 16);
#pragma unroll
  for (int nh = 0; nh < 2; ++nh) bad[nh] = ldsb + 16384 + (8 * g4 + q) * 128 + (((wc * 4 + 2 * nh + (p >> 1)) ^ (((q >> 1) << 2) | ((g4 & 1) << 1))) * 16) + (p & 1) * 8;
  f4 acc[4][6];
#pragma unroll
  for (int m = 0; m < 4; ++m)
#pragma unroll
    for (int n = 0; n < 6; ++n) { acc[m][n] = (f4)(0.f); asm volatile("" : "+v"(acc[m][n])); }
#define MF(m, n, Fq) do { if (n < 2) acc[m][n] = MFMA16(CAT(Fq.b[2 * n], Fq.b[2 * n + 1]), Fq.a[m], acc[m][n]); \
     else acc[m][n] = MFMA16(Fq.a[m], CAT(Fq.b[2 * n], Fq.b[2 * n + 1]), acc[m][n]); SBAR(); } while (0)
#define RB(G, n, vb0, vb1, SS, hf) RDTR(G.b[2 * (n) + (hf)], (((n) & 1) ? vb1 : vb0), ((n) >> 1) * 8192 + (SS) * 4096 + (hf) * 512)
#define RA(G, m, va) RD128(G.a[m], va, (m) * 2048)
#define SUB(Fq, G, va, vb0, vb1, SS, PRE) do { \
    WAITL(13); SBAR(); \
    MF(0, 0, Fq); PRE; RA(G, 0, va); SBAR(); \
    WAITL(11); SBAR(); \
    MF(1, 0, Fq); RB(G, 0, vb0, vb1, SS, 0); SBAR(); MF(2, 0, Fq); RB(G, 0, vb0, vb1, SS, 1); SBAR(); MF(3, 0, Fq); RA(G, 1, va); SBAR(); \
    WAITL(12); SBAR(); \
    MF(0, 1, Fq); RA(G, 2, va); SBAR(); MF(1, 1, Fq); RA(G, 3, va); SBAR(); MF(2, 1, Fq); RB(G, 1, vb0, vb1, SS, 0); SBAR(); MF(3, 1, Fq); RB(G, 1, vb0, vb1, SS, 1); SBAR(); \
    WAITL(8); SBAR(); \
    MF(0, 2, Fq); RB(G, 2, vb0, vb1, SS, 0); SBAR(); MF(1, 2, Fq); RB(G, 2, vb0, vb1, SS, 1); SBAR(); MF(2, 2, Fq); RB(G, 3, vb0, vb1, SS, 0); SBAR(); MF(3, 2, Fq); RB(G, 3, vb0, vb1, SS, 1); SBAR(); \
    MF(0, 3, Fq); RB(G, 4, vb0, vb1, SS, 0); SBAR(); MF(1, 3, Fq); RB(G, 4, vb0, vb1, SS, 1); SBAR(); MF(2, 3, Fq); RB(G, 5, vb0, vb1, SS, 0); SBAR(); MF(3, 3, Fq); RB(G, 5, vb0, vb1, SS, 1); SBAR(); \
    MF(0, 4, Fq); MF(1, 4, Fq); MF(2, 4, Fq); MF(3, 4, Fq); MF(0, 5, Fq); MF(1, 5, Fq); MF(2, 5, Fq); MF(3, 5, Fq); } while (0)
#define KSTEP(MODE) do { \
    if (kt == sx) { const unsigned xo = xad + buf * STG; \
      _Pragma("unroll") for (int t = 0; t < 8; ++t) { const unsigned a_ = xo ^ (t * 16); \
        asm volatile("ds_read_b64 %0, %1" : "+v"(xv[t]) : "v"(a_) : "memory"); } \
      WAITL(0); SBAR(); } \
    const int nbuf = (buf + 1) & 3; \
    const unsigned bo = buf * STG, nbo = nbuf * STG; \
    { unsigned va = aad[1] + bo, vb0 = bad[0] + bo, vb1 = bad[1] + bo; SUB(f0, f1, va, vb0, vb1, 1, ); } \
    if (MODE < 2) { \
      unsigned va = aad[0] + nbo, vb0 = bad[0] + nbo, vb1 = bad[1] + nbo; \
      SUB(f1, f0, va, vb0, vb1, 0, if (MODE == 0) { WAITL(0); BAR(); } else asm volatile("ds_write_b32 %0, %1" ::"v"(myFL), "v"(1u) : "memory"); SBAR()); \
    } else { asm volatile("ds_write_b32 %1, %2\n ds_read_b32 %0, %3\n s_waitcnt lgkmcnt(2)" : "=&v"(fl0) : "v"(myFL), "v"(2u), "v"(adFL) : "memory"); SBAR(); \
      MF(0, 2, f1); MF(1, 2, f1); MF(2, 2, f1); MF(3, 2, f1); MF(0, 3, f1); MF(1, 3, f1); MF(2, 3, f1); MF(3, 3, f1); \
      MF(0, 4, f1); MF(1, 4, f1); MF(2, 4, f1); MF(3, 4, f1); MF(0, 5, f1); MF(1, 5, f1); MF(2, 5, f1); MF(3, 5, f1); \
      MF(0, 0, f1); MF(1, 0, f1); MF(2, 0, f1); MF(3, 0, f1); MF(0, 1, f1); MF(1, 1, f1); MF(2, 1, f1); MF(3, 1, f1); } \
    buf = nbuf; } while (0)
  u2 xv[8];
#pragma unroll
  for (int t = 0; t < 8; ++t) asm volatile("" : "=v"(xv[t]));
  const int sx = (nb - rot) & 15;
  const unsigned xad = ldsb + (w * 32 + (l & 31)) * 128 + ((r15 >> 1) & 7) * 16 + (l >> 5) * 8;
  STMP(1);
  WAITL(0);
  BAR();
  STMP(2);
  WAITL(0);
  FQ f0, f1;
  { unsigned va = aad[0], vb0 = bad[0], vb1 = bad[1];
    RA(f0, 0, va); RB(f0, 0, vb0, vb1, 0, 0); RB(f0, 0, vb0, vb1, 0, 1); RA(f0, 1, va); RA(f0, 2, va); RA(f0, 3, va);
    RB(f0, 1, vb0, vb1, 0, 0); RB(f0, 1, vb0, vb1, 0, 1); RB(f0, 2, vb0, vb1, 0, 0); RB(f0, 2, vb0, vb1, 0, 1); RB(f0, 3, vb0, vb1, 0, 0); RB(f0, 3, vb0, vb1, 0, 1);
    RB(f0, 4, vb0, vb1, 0, 0); RB(f0, 4, vb0, vb1, 0, 1); RB(f0, 5, vb0, vb1, 0, 0); RB(f0, 5, vb0, vb1, 0, 1);
    WAITL(0); SBAR(); }
  int buf = 0; unsigned fl0;
#pragma unroll 1
  for (int kt = 0; kt < 14; ++kt) KSTEP(0);
  STMP(14); SBAR();
  { const int kt = 14; KSTEP(1); }
  STMP(7); SBAR();
  { const int kt = 15; KSTEP(2); }
  asm volatile("s_waitcnt lgkmcnt(0)" : "+v"(fl0) : : "memory");
  if (__builtin_expect(((unsigned)__ballot(fl0 < 1u) & 0xC0u) != 0u, 0)) lwait(adFL, 0xC0u, 1);
  STMP(3);
  __builtin_amdgcn_s_setprio(2);
  const LAS float* Lm = (const LAS float*)(lds + L_M) + wr * 64;
  const LAS float* Lr = (const LAS float*)(lds + L_RS) + wr * 64;
  const LAS float* Lc = (const LAS float*)(lds + L_CS) + wc * 96;
  f4 kv[4], zt[2];
  {
    float cs[4];
#pragma unroll
    for (int n = 0; n < 4; ++n) cs[n] = Lc[32 + 16 * n + r15];
    const u4 ones = (u4)(0x3F803F80u);
#pragma unroll
    for (int t = 0; t < 2; ++t) {
      u4 kf[2], vf[2];
#pragma unroll
      for (int mm = 0; mm < 2; ++mm) {
        const int m = 2 * t + mm;
        const f4 mv = *(const LAS f4*)(Lm + m * 16 + 4 * g4), rv = *(const LAS f4*)(Lr + m * 16 + 4 * g4);
#pragma unroll
        for (int a = 0; a < 2; ++a) {
          const f4 uk = acc[m][2 + a] - mv * cs[a];
          const f4 uv = (acc[m][4 + a] - mv * cs[2 + a]) * rv;
          kf[a][2 * mm] = phi2((f2){uk[0], uk[1]}, (f2){rv[0], rv[1]}); kf[a][2 * mm + 1] = phi2((f2){uk[2], uk[3]}, (f2){rv[2], rv[3]});
          vf[a][2 * mm] = pk2(uv[0], uv[1]); vf[a][2 * mm + 1] = pk2(uv[2], uv[3]);
        }
      }
#pragma unroll
      for (int tt = 0; tt < 4; ++tt) kv[tt] = MFMA16(S8(kf[tt & 1]), S8(vf[tt >> 1]), t ? kv[tt] : (f4)(0.f));
#pragma unroll
      for (int a = 0; a < 2; ++a) zt[a] = MFMA16(S8(kf[a]), S8(ones), t ? zt[a] : (f4)(0.f));
    }
  }
  const int hd = hp * 2 + wc;
  {
    LAS f4* ks = (LAS f4*)(lds + L_KVX + w * 3072) + l;
    const LAS f4* kr = (const LAS f4*)(lds + L_KVX + (w ^ 2) * 3072) + l;
    ks[0] = wr ? kv[0] : kv[2]; ks[64] = wr ? kv[1] : kv[3]; ks[128] = wr ? zt[0] : zt[1];
    lsig(myFL, 3);
    const f4 m0 = wr ? kv[2] : kv[0], m1 = wr ? kv[3] : kv[1], mz = wr ? zt[1] : zt[0];
    lwait(adFL, 1u << (w ^ 2), 3);
    const f4 s0 = m0 + kr[0], s1 = m1 + kr[64], sz = mz + kr[128];
    u4 o; o[0] = min(pk2(s0[0], s0[1]), POIS - 1u); o[1] = pk2(s0[2], s0[3]); o[2] = pk2(s1[0], s1[1]); o[3] = pk2(s1[2], s1[3]);
    ST128(o, WS_KVP + (hd * 16 + mb) * 2048 + wr * 1024 + l * 16);
    u4 zo = __builtin_bit_cast(u4, sz); zo[0] = min(zo[0], POIS - 1u);
    if (r15 == 0) ST128(zo, WS_ZP + (hd * 16 + mb) * 128 + g4 * 32 + wr * 16);
  }
  STMP(4);
  {
    const f4 c0 = *(const LAS f4*)(Lc + 4 * g4), c1 = *(const LAS f4*)(Lc + 16 + 4 * g4);
#pragma unroll
    for (int m = 0; m < 4; ++m) {
      const float mv = Lm[m * 16 + r15], rv = Lr[m * 16 + r15];
      const f4 u0 = acc[m][0] - c0 * mv, u1 = acc[m][1] - c1 * mv;
      u4 pf;
      pf[0] = phi2((f2){u0[0], u0[1]}, (f2)(rv)); pf[1] = phi2((f2){u0[2], u0[3]}, (f2)(rv));
      pf[2] = phi2((f2){u1[0], u1[1]}, (f2)(rv)); pf[3] = phi2((f2){u1[2], u1[3]}, (f2)(rv));
      *(LAS u4*)(lds + L_PQ + wr * 8192 + wc * 4096 + m * 1024 + l * 16) = pf;
    }
  }
  lsig(myFL, 4);
  __builtin_amdgcn_s_setprio(0);
  STMP(5);
  const int trow = mb * 128 + w * 32 + (l & 31);
  u2 oh;
  const unsigned flo = PAIRAT(r15) * 128;
  const char* fb = uptr(wsx + WS_OH + mb * 2048);
  int sel = -1;
  {
    const u4 s0 = *(const LAS u4*)(lds + L_SEL), s1 = *(const LAS u4*)(lds + L_SEL + 16);
#pragma unroll
    for (int i = 0; i < 8; ++i) { const int v = (int)(i < 4 ? s0[i & 3] : s1[i & 3]); sel = v == trow ? i : sel; }
  }
  const bool anysel = __ballot(sel >= 0) != 0ull;
  f16v oc[2][2];
  {
    const float m = *(const LAS float*)(lds + L_M + (w * 32 + (l & 31)) * 4), rs = *(const LAS float*)(lds + L_RS + (w * 32 + (l & 31)) * 4);
#pragma unroll
    for (int t = 0; t < 8; ++t) {
      const f4 xf = (f4){lo(xv[t][0]), hi(xv[t][0]), lo(xv[t][1]), hi(xv[t][1])};
      const f4 y = (xf - m) * rs + xf;
#pragma unroll
      for (int i = 0; i < 4; ++i) oc[0][t >> 2][(t & 3) * 4 + i] = y[i];
    }
    asm volatile("" : "+v"(oc[0][0]), "+v"(oc[0][1]));
  }
  unsigned wa[2];
#pragma unroll
  for (int t = 0; t < 2; ++t) wa[t] = ldsb + L_WO + wc * 4096 + (4 * (g4 >> 1) + q) * 128 + (((4 * t + 2 * (g4 & 1) + (p >> 1)) ^ ((q >> 1) << 2)) * 16) + (p & 1) * 8;
  u4 a[RD][4];
#pragma unroll
  for (int k = 0; k < RD; ++k) asm volatile("" : "=v"(a[k][0]), "=v"(a[k][1]), "=v"(a[k][2]), "=v"(a[k][3]));
  const char* ob = uptr(wsx + WS_OF + mb * 262144 + wc * 8192 + wr * 4096);
  const unsigned ov0 = l * 16 + wc * 2048, ov1 = ov0 ^ 2048;
  STMP(6);
  {
    unsigned vfw[4];
#pragma unroll
    for (int i = 0; i < 4; ++i) { vfw[i] = VFW(i); asm volatile("" : "+v"(vfw[i])); }
    lwait<2>(adFL, 0x3Fu, (l & 4) ? 3u : 4u);
    asm volatile("global_load_dwordx2 %0, %1, %2 sc1" : "=&v"(oh) : "v"(flo), "s"(fb) : "memory");
#pragma unroll
    for (int i = WE / 2; i < 16; i += 2) {
      const int pp = i + wr;
      const char* wp = wob - 3072 + (2 * PAIRAT(pp) + wc) * 65536; const unsigned wm = ldsb + (2 * pp + wc) * 4096;
      GL4(vfw, wp, wm, L_WO);
    }
    WAITV(32 - WE);
    asm volatile("" : "+v"(oh));
  }
  unsigned seen = (unsigned)__ballot(oh[0] == tag), go = 0;
  auto need = [&](unsigned mask) {
    if (__builtin_expect((go & mask) != mask, 0)) {
      RETRY2();
      _Pragma("unroll 1") for (unsigned it = 0; it < SPIN; ++it) {
        if ((seen & mask) != mask) {
          asm volatile("global_load_dwordx2 %0, %1, %2 sc1\n s_waitcnt vmcnt(0)" : "+v"(oh) : "v"(flo), "s"(fb) : "memory");
          seen = (unsigned)__ballot(oh[0] == tag);
        }
        go = seen & (unsigned)__ballot(oh[1] - NOW() - 1u >= 1000u);
        if ((go & mask) == mask) break;
        SLEEP(1);
      }
    }
  };
#define OX 0
#define OL1(pos, i) do { if (OX == 2 && (pos) >= RD) break; \
    asm volatile("global_load_dwordx4 %0, %1, %2 offset:%3 sc1" : "+v"(a[(pos) % RD][i]) : "v"((i) & 1 ? ov1 : ov0), "s"(ob + (OX == 1 ? ((pos) & 1) : PAIRAT(pos)) * 16384), "i"(((i) >> 1) * 1024) : "memory"); } while (0)
#define OLD(pos) do { OL1(pos, 0); OL1(pos, 1); OL1(pos, 2); OL1(pos, 3); } while (0)
  auto omax = [&](u4(&v)[4]) {
    asm volatile("" : "+v"(v[0]), "+v"(v[1]), "+v"(v[2]), "+v"(v[3]));
    return max(umax3(v[0][0], v[1][0], v[2][0]), v[3][0]);
  };
#define OFIX(pos, m) do { \
    if (__builtin_expect(__ballot((m) == POIS) != 0ull, 0)) { RETRY(); _Pragma("unroll 1") for (unsigned it = 0; it < SPIN; ++it) { SLEEP(4); OLD(pos); WAITV(0); if (__ballot(omax(a[(pos) % RD]) == POIS) == 0ull) break; } } } while (0)
#define OS1(i)
#define OS2(i)
  lwait<1>(adFL, 0x30u, 4);
  __builtin_amdgcn_s_setprio(2);
  go = seen & (unsigned)__ballot(oh[1] - NOW() - 1u >= 1000u);
  need(1u);
  STMP(15);
  u2 oh2 = oh;
  if (__builtin_expect(seen != ~0u, 0)) asm volatile("global_load_dwordx2 %0, %1, %2 sc1" : "+v"(oh2) : "v"(flo), "s"(fb) : "memory");
  OS1(0); OLD(0);
#pragma unroll
  for (int i = 1; i < RD; ++i) { need(1u << i); OS1(i); OLD(i); }
  STMP(10);
  s4 b[8];
#define ORD(pos, f) do { if ((f) < 2 && (pos) == 8) { wa[(f) & 1] += 65536; asm volatile("" : "+v"(wa[(f) & 1])); } \
    RDTR(b[2 * (f)], wa[(f) & 1], ((pos) & 7) * 8192 + ((f) >> 1) * 1024); RDTR(b[2 * (f) + 1], wa[(f) & 1], ((pos) & 7) * 8192 + ((f) >> 1) * 1024 + 2048); } while (0)
#define OMF(pos, f, r) do { if ((f) < 2 && (pos) == 0 && (r) == 1) asm volatile("v_mfma_f32_32x32x16_bf16 %0, %1, %2, 0" : "=&v"(oc[r][(f) & 1]) \
      : "v"(CAT(b[2 * (f)], b[2 * (f) + 1])), "v"(a[(pos) % RD][((f) & 2) + (r)])); \
    else asm volatile("v_mfma_f32_32x32x16_bf16 %0, %1, %2, %0" : "+v"(oc[r][(f) & 1]) \
      : "v"(CAT(b[2 * (f)], b[2 * (f) + 1])), "v"(a[(pos) % RD][((f) & 2) + (r)])); \
    SBAR(); if ((r) == 1 && (pos) < 15) ORD(((pos) + 1) & 15, f); SBAR(); } while (0)
#define ONX(pos) ((pos) > 0 && (pos) + RD - 1 < 16)
#define ONL(pos, i) do { if (ONX(pos)) OL1(((pos) + RD - 1) & 15, i); SBAR(); } while (0)
#define OWL(pos, f) do { WAITL((pos) < 15 ? 6 : 6 - 2 * (f)); SBAR(); } while (0)
#define OHEAD(pos) do { unsigned m_ = 0; \
    if ((pos) == WE / 2 - 1 && WE < 32) { asm volatile("" : "+v"(fl3)); if (__builtin_expect(((unsigned)__ballot(fl3 < 5u) >> (w ^ 2)) & 1u, 0)) lwait(adFL, 1u << (w ^ 2), 5); SBAR(); } \
    OWL(pos, 0); OMF(pos, 0, 0); if (ONX(pos)) { need(1u << (((pos) + RD - 1) & 15)); OS1(((pos) + RD - 1) & 15); } SBAR(); \
    OMF(pos, 0, 1); ONL(pos, 0); \
    OWL(pos, 1); OMF(pos, 1, 0); ONL(pos, 1); \
    OMF(pos, 1, 1); ONL(pos, 2); \
    OWL(pos, 2); OMF(pos, 2, 0); ONL(pos, 3); \
    if ((pos) == 0 || (pos) == 4) { asm volatile("ds_write_b32 %0, %1" ::"v"(myFL), "v"((pos) ? 6u : 5u) : "memory"); SBAR(); } \
    if ((pos) == WE / 2 - 3) { asm volatile("ds_read_b32 %0, %1" : "=v"(fl3) : "v"(adFL) : "memory"); SBAR(); } \
    if ((pos) == 13) { asm volatile("ds_read_b32 %0, %1" : "=v"(fl3) : "v"(adFL) : "memory"); SBAR(); } \
    OMF(pos, 2, 1); \
    OWL(pos, 3); OMF(pos, 3, 0); \
    if ((pos) < 15) { WAITV(4 * ((pos) + RD - 1 < 16 ? RD - 2 : 14 - (pos))); m_ = omax(a[((pos) + 1) % RD]); asm volatile("" : "+v"(m_)); SBAR(); } \
    OMF(pos, 3, 1); \
    if ((pos) < 15) { OFIX(((pos) + 1) & 15, m_); OS2(((pos) + 1) & 15); } SBAR(); } while (0)
  unsigned fl3;
  ORD(0, 0); ORD(0, 1); ORD(0, 2); ORD(0, 3);
  { WAITV(4 * (RD - 1)); const unsigned m_ = omax(a[0]); OFIX(0, m_); OS2(0);
    asm volatile("" : "+v"(oh2));
    if (oh2[0] == tag) oh = oh2;
    seen = (unsigned)__ballot(oh[0] == tag);
    go = seen & (unsigned)__ballot(oh[1] - NOW() - max(r15 - (RD - 1), 0) * ADV - 1u >= 1000u);
    SBAR(); }
  OHEAD(0); OHEAD(1); STMP(11); OHEAD(2); OHEAD(3); OHEAD(4); OHEAD(5); OHEAD(6); OHEAD(7); STMP(12);
  OHEAD(8); OHEAD(9); STMP(13); OHEAD(10); OHEAD(11); OHEAD(12); OHEAD(13); OHEAD(14);
  f4 xi[8];
  {
    asm volatile("" : "+v"(fl3));
    if (__builtin_expect(((unsigned)__ballot(fl3 < 6u) >> (w ^ 2)) & 1u, 0)) lwait(adFL, 1u << (w ^ 2), 6);
    if (__builtin_expect(((unsigned)__ballot(fl3 < 4u) & 0x30u) != 0u, 0)) lwait(adFL, 0x30u, 4);
    const unsigned xw = ldsb + L_WO + (4 * wr + wc) * 4096 + l * 16, xr = xw ^ 4096;
#define XW(t) asm volatile("ds_write_b128 %0, %1 offset:%2" ::"v"(xw), "v"(__builtin_shufflevector(oc[1][(t) >> 2], oc[1][(t) >> 2], ((t) & 3) * 4, ((t) & 3) * 4 + 1, ((t) & 3) * 4 + 2, ((t) & 3) * 4 + 3)), \
      "i"(((t) >> 2) * 8192 + ((t) & 3) * 1024) : "memory")
    SBAR();
    OWL(15, 0); OMF(15, 0, 1); OWL(15, 1); OMF(15, 1, 1); OWL(15, 2); OMF(15, 2, 1); OWL(15, 3); OMF(15, 3, 1);
    OMF(15, 0, 0); OMF(15, 1, 0); XW(0); XW(1); XW(2); XW(3); SBAR();
    OMF(15, 2, 0); XW(4); XW(5); XW(6); XW(7); SBAR();
    OMF(15, 3, 0);
    asm volatile("ds_write_b32 %0, %1" ::"v"(myFL), "v"(7u) : "memory");
    _Pragma("unroll 1") for (unsigned it = 0; it < SPIN; ++it) {
      unsigned v;
      asm volatile("ds_read_b32 %0, %1" : "=v"(v) : "v"(adFL) : "memory");
#pragma unroll
      for (int t = 0; t < 8; ++t) RD128(xi[t], xr, (t >> 2) * 8192 + (t & 3) * 1024);
      WAITL(0);
      asm volatile("" : "+v"(v), "+v"(xi[0]), "+v"(xi[1]), "+v"(xi[2]), "+v"(xi[3]), "+v"(xi[4]), "+v"(xi[5]), "+v"(xi[6]), "+v"(xi[7]));
      if ((((unsigned)__ballot(v < 7u) >> (w ^ 1)) & 1u) == 0u) break;
    }
    asm volatile("" : "+v"(oc[0][0]), "+v"(oc[0][1]));
#pragma unroll
    for (int t = 0; t < 8; ++t)
#pragma unroll
      for (int i = 0; i < 4; ++i) xi[t][i] += oc[0][t >> 2][(t & 3) * 4 + i];
  }
  STMP(8);
  if (anysel) {
    lwait(adFL, 0xC0u, 3);
    if (sel >= 0) {
#pragma unroll
      for (int t = 0; t < 8; ++t) xi[t] += *(const LAS f4*)(lds + L_KVX + sel * 256 + (8 * t + 4 * (l >> 5)) * 4);
    }
  }
  const __amdgpu_buffer_rsrc_t outr = __builtin_amdgcn_make_buffer_rsrc(out, 0, 2048 * D * 2, 0x00020000);
  if (FULL) {
    LAS char* tb = (LAS char*)lds + L_PQ + w * 4096;
#pragma unroll
    for (int t = 0; t < 8; ++t) *(LAS u2*)(tb + (l & 31) * 128 + ((t ^ (l & 7)) * 16) + (l >> 5) * 8) = (u2){pk2(xi[t][0], xi[t][1]), pk2(xi[t][2], xi[t][3])};
#pragma unroll
    for (int i = 0; i < 4; ++i) {
      const u4 v = *(const LAS u4*)(tb + i * 1024 + (l >> 3) * 128 + (((l ^ (l >> 3)) & 7) * 16));
      __builtin_amdgcn_raw_buffer_store_b128(v, outr, (mb * 128 + w * 32 + i * 8 + (l >> 3)) * 2048 + nb * 128 + (l & 7) * 16, 0, 16);
    }
  } else {
#pragma unroll
  for (int hh = 0; hh < 4; ++hh) {
    const f4 ta = xi[2 * hh], tb = xi[2 * hh + 1];
    auto r0 = __builtin_amdgcn_permlane32_swap(pk2(ta[0], ta[1]), pk2(tb[0], tb[1]), false, false);
    auto r1 = __builtin_amdgcn_permlane32_swap(pk2(ta[2], ta[3]), pk2(tb[2], tb[3]), false, false);
    __builtin_amdgcn_raw_buffer_store_b128(((u4){r0[0], r1[0], r0[1], r1[1]}), outr, (trow * D + nb * 64 + 16 * hh + 8 * (l >> 5)) * 2, 0, 16);
  }
  }
  if (g == 0 && w == 0 && l == 0) __builtin_amdgcn_raw_buffer_store_b32(tag, wse, WS_E, 0, 16);
  STMP(9);
  STFLUSH();
  asm volatile(".Lend_c:");
  return;
  }
  {
    const int lw = w - 4;
    unsigned ev;
    asm volatile("global_load_dword %0, %1, %2 sc1" : "=v"(ev) : "v"(0u), "s"(ws) : "memory");
    unsigned vf[20], vfw[4];
#pragma unroll
    for (int i = 0; i < 8; ++i) { const int row = 8 * (lw * 8 + i) + (l >> 3); vf[i] = row * 2048 + (((l & 7) ^ ((row >> 1) & 7)) * 16) + (3 - (i & 3)) * 1024; }
#pragma unroll
    for (int i = 0; i < 12; ++i) {
      const int pc = lw * 12 + i, row = (pc & 7) * 8 + (l >> 3);
      vf[8 + i] = row * 6144 + (pc >> 3) * 2048 + (((l & 7) ^ ((((row >> 1) & 1) << 2) | (((row >> 3) & 1) << 1))) * 16) + (3 - (i & 3)) * 1024;
    }
#pragma unroll
    for (int i = 0; i < 4; ++i) vfw[i] = VFW(i);
    const char* Ab = uptr((const char*)x + (size_t)mb * 128 * 2048) - 3072;
    const char* Bb = uptr((const char*)wqkv + hp * 128) - 3072;
    const unsigned mba = ldsb + lw * 8192, mbb = ldsb + 16384 + lw * 12288;
    auto issue = [&](int s, int u0, int u1) {
      const int kt = (s + rot) & 15;
      const char* sa = Ab + kt * 128; const char* sb = Bb + kt * (64 * 6144);
      const unsigned so = (s & 3) * STG, ma = mba + so, mq = mbb + so;
#pragma unroll
      for (int u = u0; u < u1; ++u) { if (u < 2) GL4(vf + 4 * u, sa, ma, u * 4096); else GL4(vf + 4 * u, sb, mq, (u - 2) * 4096); }
    };
    int tk = 0;
#define LBAR() do { BST(tk + 1); BAR(); BST(tk + 21); ++tk; } while (0)
    issue(0, 0, 5); issue(1, 0, 5); issue(2, 0, U0);
    BST(60);
    WAITV(20 + 4 * U0); BST(0); BAR(); BST(20);
    asm volatile("" : "+v"(ev));
    const unsigned tag = __builtin_amdgcn_readfirstlane(ev) + 1u;
    MKWS(tag);
    issue(2, U0, 5);
    const unsigned ck0 = (unsigned)__builtin_amdgcn_s_memtime(), rt0 = NOW();
    int s0 = 3;
    if (U3 < 5) { issue(3, 0, U3); BST(tk + 40); WAITV(20 + 4 * U3); LBAR(); issue(3, U3, 5); s0 = 4; }
#pragma unroll 1
    for (; s0 < (sp ? 16 : 6 + ph); ++s0) { issue(s0, 0, 5); BST(tk + 40); WAITV(40); LBAR(); }
#pragma unroll 1
    for (; s0 < 16; s0 += 5) {
      WAITV(20); LBAR();
      WAITV(0); LBAR();
      issue(s0, 0, 5); issue(s0 + 1, 0, U1); BST(tk + 40); WAITV(4 * U1); LBAR();
      issue(s0 + 1, U1, 5); issue(s0 + 2, 0, 5);
      if (s0 + 3 >= 16) break;
      issue(s0 + 3, 0, U2); BST(tk + 40); WAITV(20 + 4 * U2); LBAR();
      issue(s0 + 3, U2, 5);
      if (s0 + 4 >= 16) break;
      issue(s0 + 4, 0, 5); BST(tk + 40); WAITV(40); LBAR();
    }
    int wq = 0;
    auto wo_upto = [&](int lim) {
      _Pragma("unroll 1") for (; wq < lim; ++wq) { const char* wp = wob - 3072 + HEADAT(wq) * 65536; const unsigned wm = ldsb + wq * 4096; GL4(vfw, wp, wm, L_WO); }
    };
    asm volatile("ds_write_b32 %0, %1" ::"v"(adFL), "v"(0u) : "memory");
    unsigned kd = sp ? KS : KD, od = OD;
    if (CF) {
      const unsigned dc = (unsigned)__builtin_amdgcn_s_memtime() - ck0, dr = NOW() - rt0;
      const float sc = fminf((float)dr * (float)CF * __builtin_amdgcn_rcpf((float)dc), 2.f);
      kd = KL + (unsigned)((float)(kd - KL) * sc); od = OL + (unsigned)((float)(OD - OL) * sc);
      asm volatile("" : "+v"(kd), "+v"(od));
    }
    WAITV(0); WAITL(0); LBAR();
    const int hd = hp * 2 + lw;
    u4 dg;
    if (lw) {
      const u4 pz = (u4)(POIS);
      ST64(((u2){tag, NOW() + kd}), l == 0 ? WS_HK + hp * 512 + (mb >> 2) * 128 + (mb & 3) * 8 : WS_SZ);
#pragma unroll
      for (int i = 0; i < 16; ++i) ST128(pz, WS_OF + (mb * 32 + hp * 2) * 8192 + i * 1024 + l * 16);
      BST(61);
      const char* hb = uptr(wsx + WS_HK + hp * 512);
      lwait<4>(adFL, 0xCFu, (l & 4) ? 1u : 2u);
      if (HT) SLEEP(HT);
      STMP(1);
      unsigned t0, rq[4];
      _Pragma("unroll 1") for (unsigned it = 0; it < 64; ++it) {
        u8v h[4];
        asm volatile("s_load_dwordx8 %0, %4, 0 glc\n s_load_dwordx8 %1, %4, 128 glc\n s_load_dwordx8 %2, %4, 256 glc\n s_load_dwordx8 %3, %4, 384 glc\n s_waitcnt lgkmcnt(0)"
                     : "=&s"(h[0]), "=&s"(h[1]), "=&s"(h[2]), "=&s"(h[3]) : "s"(hb) : "memory");
        t0 = NOW() - 512u;
        BST(16);
        unsigned bad = 0;
#pragma unroll
        for (int i = 0; i < 16; ++i) {
          unsigned e = h[i >> 2][(i & 3) * 2] ^ tag;
          asm("" : "+s"(e));
          bad |= e;
          const unsigned d = h[i >> 2][(i & 3) * 2 + 1] - t0, r = d < 1536u ? d : 0u; rq[i >> 2] = (i & 3) ? max(rq[i >> 2], r) : r;
        }
        if (bad == 0u) break;
        RETRY2();
        SLEEP(HKS);
      }
      const bool up = max(rq[0], rq[1]) > max(rq[2], rq[3]);
      const unsigned a0 = up ? rq[2] : rq[0], a1 = up ? rq[3] : rq[1], a2 = up ? rq[0] : rq[2], a3 = up ? rq[1] : rq[3];
      const bool od2 = a2 > a3;
      dg = (u4){((t0 + (od2 ? a1 : a0) + 3u) & ~3u) | (up ? 2u : 0u) | (od2 ? 1u : 0u), t0 + (od2 ? a0 : a1), t0 + (od2 ? a3 : a2), t0 + (od2 ? a2 : a3)};
      *(LAS u4*)(lds + L_FL + 32) = dg;
      lsig(myFL, 1);
      BST(15);
      WAITV(0);
      ST64(((u2){tag, t0 + max(max(a2, a3), 512u + OM) + od}), l == 0 ? WS_OH + (mb * 16 + hp) * 128 : WS_SZ);
    } else {
      if (WS0) SLEEP(WS0);
      wo_upto(WE < 12 ? WE : 12);
      BST(15);
      if (WE > 12) { lwait<LW1>(adFL, 0x0Fu, 1); wo_upto(WE < 22 ? WE : 22); }
      BST(16);
      if (WE > 22) { lwait<LW1>(adFL, 0x0Fu, 2); wo_upto(WE); }
      STMP(1);
      u4 v;
      _Pragma("unroll 1") for (unsigned it = 0; it < SPIN; ++it) {
        unsigned f;
        asm volatile("ds_read_b32 %0, %2 offset:20\n ds_read_b128 %1, %2 offset:32\n s_waitcnt lgkmcnt(0)" : "=&v"(f), "=&v"(v) : "v"(ldsb + L_FL) : "memory");
        if (__builtin_amdgcn_readfirstlane(f)) break;
        SLEEP(2);
      }
#pragma unroll
      for (int e = 0; e < 4; ++e) dg[e] = __builtin_amdgcn_readfirstlane(v[e]);
    }
    const int gx = dg[0] & 3u;
    __builtin_amdgcn_s_setprio(3);
    u4 sel[2];
#pragma unroll
    for (int e = 0; e < 2; ++e) {
      const unsigned v = (r15 >> 2) == g4 ? (((r15 & 3) == 2 * e ? 0x3F80u : 0u) | ((r15 & 3) == 2 * e + 1 ? 0x3F800000u : 0u)) : 0u;
      sel[0][e] = v; sel[0][2 + e] = 0u; sel[1][e] = 0u; sel[1][2 + e] = v;
    }
    u4 pv[16][2], zv0, zv1, pq[8];
    const char* kvb = uptr(wsx + WS_KVP + (size_t)hd * 32768);
    const char* zb = uptr(wsx + WS_ZP + (size_t)hd * 2048);
    const unsigned lv16 = l * 16, zoff = r15 * 128 + g4 * 32;
    const unsigned pa = ldsb + L_PQ + lw * 4096 + l * 16, omask = lw ? 0x0Au : 0x25u;
    STMP(2);
#define GLD(gq) do { _Pragma("unroll") for (int i = 0; i < 4; ++i) { const char* sb = kvb + (((gq) ^ gx) * 4 + (i & 2)) * 2048; \
      asm volatile("global_load_dwordx4 %0, %1, %2 offset:%3 sc1" : "=v"(pv[(gq) * 4 + i][0]) : "v"(lv16), "s"(sb), "i"((i & 1) * 2048) : "memory"); \
      asm volatile("global_load_dwordx4 %0, %1, %2 offset:%3 sc1" : "=v"(pv[(gq) * 4 + i][1]) : "v"(lv16), "s"(sb), "i"((i & 1) * 2048 + 1024) : "memory"); } } while (0)
#define ZLD() do { asm volatile("global_load_dwordx4 %0, %1, %2 sc1" : "=v"(zv0) : "v"(zoff), "s"(zb) : "memory"); \
      asm volatile("global_load_dwordx4 %0, %1, %2 offset:16 sc1" : "=v"(zv1) : "v"(zoff), "s"(zb) : "memory"); } while (0)
#pragma unroll
    for (int gq = 0; gq < 4; ++gq) {
      _Pragma("unroll 1") for (unsigned it = 0; it < SPIN; ++it) if ((int)(NOW() - dg[gq]) >= 0) break;
      if (gq == 3) ZLD();
      GLD(gq);
    }
    lsig(myFL, 3);
    STMP(5);
    lwait(adFL, omask, (l & 4) ? 3u : 4u);
#pragma unroll
    for (int i = 0; i < 8; ++i) RD128(pq[i], pa, (i >> 2) * 8192 + (i & 3) * 1024);
#pragma unroll
    for (int gq = 0; gq < 4; ++gq) {
      SBAR(); WAITV(gq == 0 ? 26 : gq == 1 ? 18 : gq == 2 ? 10 : 0);
      _Pragma("unroll 1") for (unsigned it = 0; it < SPIN; ++it) {
#pragma unroll
        for (int i = 0; i < 4; ++i) asm volatile("" : "+v"(pv[gq * 4 + i][0]), "+v"(pv[gq * 4 + i][1]));
        unsigned m = umax3(pv[gq * 4][0][0], pv[gq * 4][1][0], pv[gq * 4 + 1][0][0]);
        m = umax3(m, pv[gq * 4 + 1][1][0], pv[gq * 4 + 2][0][0]);
        m = umax3(m, pv[gq * 4 + 2][1][0], pv[gq * 4 + 3][0][0]);
        m = max(m, pv[gq * 4 + 3][1][0]);
        if (gq == 3) { asm volatile("" : "+v"(zv0), "+v"(zv1)); m = umax3(m, zv0[0], zv1[0]); }
        if (__ballot(m == POIS) == 0ull) break;
        RETRY();
        SLEEP(2);
        if (gq == 3) ZLD();
        GLD(gq);
        WAITV(0);
      }
      f4 ga[4];
#pragma unroll
      for (int i = 0; i < 4; ++i)
#pragma unroll
        for (int t = 0; t < 4; ++t) ga[t] = MFMA16(S8(sel[t & 1]), S8(pv[gq * 4 + i][t >> 1]), i ? ga[t] : (f4)(0.f));
#pragma unroll
      for (int t = 0; t < 4; ++t) pv[gq * 4 + (t >> 1)][t & 1] = __builtin_bit_cast(u4, ga[t]);
    }
    STMP(3);
    WAITL(0);
#pragma unroll
    for (int i = 0; i < 8; i += 2) asm volatile("" : "+v"(pq[i]), "+v"(pq[i + 1]));
    u4 kA[2], zA;
    {
      float z[8];
#pragma unroll
      for (int e = 0; e < 4; ++e) { z[e] = __uint_as_float(zv0[e]); z[4 + e] = __uint_as_float(zv1[e]); }
#define ZR(n) "v_add_f32_dpp %0, %0, %0 row_ror:" #n " row_mask:0xf bank_mask:0xf\n v_add_f32_dpp %1, %1, %1 row_ror:" #n " row_mask:0xf bank_mask:0xf\n" \
              "v_add_f32_dpp %2, %2, %2 row_ror:" #n " row_mask:0xf bank_mask:0xf\n v_add_f32_dpp %3, %3, %3 row_ror:" #n " row_mask:0xf bank_mask:0xf\n" \
              "v_add_f32_dpp %4, %4, %4 row_ror:" #n " row_mask:0xf bank_mask:0xf\n v_add_f32_dpp %5, %5, %5 row_ror:" #n " row_mask:0xf bank_mask:0xf\n" \
              "v_add_f32_dpp %6, %6, %6 row_ror:" #n " row_mask:0xf bank_mask:0xf\n v_add_f32_dpp %7, %7, %7 row_ror:" #n " row_mask:0xf bank_mask:0xf\n"
      asm volatile("s_nop 1\n" ZR(8) ZR(4) ZR(2) ZR(1) : "+v"(z[0]), "+v"(z[1]), "+v"(z[2]), "+v"(z[3]), "+v"(z[4]), "+v"(z[5]), "+v"(z[6]), "+v"(z[7]));
#pragma unroll
      for (int e = 0; e < 4; ++e) zA[e] = pk2(z[2 * e], z[2 * e + 1]);
    }
    float inv[8];
#pragma unroll
    for (int i = 0; i < 8; ++i) { f4 dn = MFMA16(S8(zA), S8(pq[i]), (f4)(0.f)); inv[i] = dn[0]; }
#pragma unroll
    for (int t = 0; t < 4; ++t) {
#define PF(gq) __builtin_bit_cast(f4, pv[(gq) * 4 + (t >> 1)][t & 1])
      f4 s = (PF(0) + PF(1)) + (PF(2) + PF(3));
      kA[t >> 1][(t & 1) * 2] = pk2(s[0], s[1]); kA[t >> 1][(t & 1) * 2 + 1] = pk2(s[2], s[3]);
    }
    f4 o0[8], o1[8];
#pragma unroll
    for (int i = 0; i < 8; ++i) {
      o0[i] = MFMA16(S8(kA[0]), S8(pq[i]), (f4)(0.f)); o1[i] = MFMA16(S8(kA[1]), S8(pq[i]), (f4)(0.f));
      inv[i] = __builtin_amdgcn_rcpf(inv[i] + 1e-6f);
    }
#pragma unroll
    for (int i = 0; i < 8; ++i) {
      o0[i] *= inv[i]; o1[i] *= inv[i];
      u4 o; o[0] = min(pk2(o0[i][0], o0[i][1]), POIS - 1u); o[1] = pk2(o0[i][2], o0[i][3]); o[2] = pk2(o1[i][0], o1[i][1]); o[3] = pk2(o1[i][2], o1[i][3]);
      ST128(o, WS_OF + (mb * 32 + hd) * 8192 + (i >> 1) * 2048 + (i & 1) * 256 + g4 * 512 + r15 * 16);
    }
    lsig(myFL, 4);
    STMP(4);
    STFLUSH();
    asm volatile(".Lend_l:");
  }

}

extern "C" void kernel_launch(void* const* d_in, const int* in_sizes, int n_in, void* d_out, int out_size,
                              void* d_ws, size_t ws_size, hipStream_t stream) {
  (void)hipFuncSetAttribute((const void*)fused, hipFuncAttributeMaxDynamicSharedMemorySize, LDS_SZ);
  fused<<<256, 512, LDS_SZ, stream>>>((const u16*)d_in[0], (const u16*)d_in[1], (const u16*)d_in[2], (const u16*)d_in[4],
                                      (const u16*)d_in[5], (const u16*)d_in[6], (const u16*)d_in[7], (char*)d_ws, (u16*)d_out);
}
```

```cpp
#include <hip/hip_runtime.h>
#include <stdint.h>

typedef unsigned short u16;
typedef short s4 __attribute__((ext_vector_type(4)));
typedef short s8 __attribute__((ext_vector_type(8)));
typedef float f4 __attribute__((ext_vector_type(4)));
typedef float f16v __attribute__((ext_vector_type(16)));
typedef float f2 __attribute__((ext_vector_type(2)));
typedef unsigned u4 __attribute__((ext_vector_type(4)));
typedef unsigned u2 __attribute__((ext_vector_type(2)));
typedef unsigned u8v __attribute__((ext_vector_type(8)));
typedef __bf16 bf2 __attribute__((ext_vector_type(2)));
#define LAS __attribute__((address_space(3)))
#define DEV __device__ __forceinline__

constexpr int D = 1024, F = 3584;
#define NOMOE 0
#define KD 275
#define KS 268
#define OD 195
#define OHP 1
#define FS 128
#define GSL 8
#define YSL 16
#define TP1 0
#define TP2 3
#define TLBW 0
#define FULL 1
#define HPRIO 1
#define OWN 1
#define NOPOIS 0
#define CAP 7
#define RD 4
#define DYN 1
#define JX 0
#define NOSKIP 0
#define GRP 8
#define ADV 15
#define HB 1
#define RT 1
#define WD 0
#define WU1 12
#define WU2 22
#define PL 0
#define PO 0
#define WG 0
#define DS 0
#define D1 16
#define D2 2
#define HKS 2
#define LW1 1
#define HW1 2
#define HS0 0
#define GPV 2
#define XO 1
#define TP0 3
#define G1 0x31
#define G1S 4
#define G2S 8
#define TD 1
#define YD 170
#define WU0 4
#define WE 12
#define HT 14
#define CF 19
#define KL 145
#define OL 115
#define CL 0
#define WS0 0
#define OM 0
#define HG 128
#define U0 0
#define U3 5
#define TL 0
#define MH 0
#define HWT 3
#define YG 0
#define GG 1
#define PV 1
#define U1 4
#define U2 2
#define ROT 0

constexpr unsigned WS_E = 0;
constexpr unsigned WS_YF = 524288;
constexpr unsigned WS_CAND = 12288;
constexpr unsigned WS_RG = 49152;
constexpr unsigned WS_ST = 65536;
constexpr unsigned WS_HK = 589824;
constexpr unsigned WS_ZP = 131072;
constexpr unsigned WS_CS = 196608;
constexpr unsigned WS_OH = 458752;
constexpr unsigned WS_KVP = 1u << 20;
constexpr unsigned WS_GP = 2u << 20;
constexpr unsigned WS_YP = 3u << 20;
constexpr unsigned WS_OF = 5u << 20;
constexpr unsigned WS_SZ = 9u << 20;
#define NR 4
#define RGO(tag) ((64u << 20) + ((tag) & (NR - 1u)) * (10u << 20))
#define MKWS(tag) char* const wsx = ws + RGO(tag); const __amdgpu_buffer_rsrc_t wsr = __builtin_amdgcn_make_buffer_rsrc(wsx, 0, WS_SZ, 0x00020000)

constexpr int STG = 40960;
constexpr int L_KVX = 0;
constexpr int L_PQ = 12288;
constexpr int L_M = 28672, L_RS = L_M + 512, L_CS = L_RS + 512, L_SEL = L_CS + 768, L_FL = L_SEL + 64;
constexpr int L_HX = 30720, L_WO = 32768;
constexpr int LDS_SZ = 163840;
constexpr unsigned POIS = 0xFFFFFFFFu;

DEV unsigned pk2(float a, float b) { bf2 v; v[0] = (__bf16)a; v[1] = (__bf16)b; return __builtin_bit_cast(unsigned, v); }
DEV float lo(unsigned u) { return __uint_as_float(u << 16); }
DEV float hi(unsigned u) { return __uint_as_float(u & 0xFFFF0000u); }
DEV float rbf(float a) { return lo(pk2(a, a)); }
DEV float dot2(unsigned a, unsigned b, float c) { return __builtin_amdgcn_fdot2_f32_bf16(__builtin_bit_cast(bf2, a), __builtin_bit_cast(bf2, b), c, false); }
#define RD128(dst, addr, OFF) asm volatile("ds_read_b128 %0, %1 offset:%2" : "=v"(dst) : "v"(addr), "i"(OFF) : "memory")
#define RDTR(dst, addr, OFF) asm volatile("ds_read_b64_tr_b16 %0, %1 offset:%2" : "=v"(dst) : "v"(addr), "i"(OFF) : "memory")
#define SBAR() __builtin_amdgcn_sched_barrier(0)
#define CAT(x, y) __builtin_shufflevector(x, y, 0, 1, 2, 3, 4, 5, 6, 7)
#define WAITV(n) asm volatile("s_waitcnt vmcnt(%0)" ::"i"(n) : "memory")
#define WAITL(n) asm volatile("s_waitcnt lgkmcnt(%0)" ::"i"(n) : "memory")
#define MEMB() asm volatile("" ::: "memory")
#define SLEEP(n) asm volatile("s_sleep %0" ::"i"(n) : "memory")
#define BAR() asm volatile("s_barrier" ::: "memory")
#define GL0(vo, sb, m0v) asm volatile("s_mov_b32 m0, %2\n s_nop 0\n global_load_lds_dwordx4 %0, %1" ::"v"(vo), "s"(sb), "s"(m0v) : "memory")
#define GLI(vo, sb, OFF) asm volatile("global_load_lds_dwordx4 %0, %1 offset:%2" ::"v"(vo), "s"(sb), "i"(OFF) : "memory")
#define GL4(v, sb, mm, MO) do { asm volatile("s_add_i32 m0, %2, %3\n s_nop 0\n global_load_lds_dwordx4 %0, %1" ::"v"((v)[0]), "s"(sb), "s"(mm), "i"(MO) : "memory", "scc"); \
      GLI((v)[1], sb, 1024); GLI((v)[2], sb, 2048); GLI((v)[3], sb, 3072); } while (0)
#define VFW(i) ((8 * (i) + (l >> 3)) * 2048 + (((l & 7) ^ (((l >> 4) & 1) << 2)) * 16) + (3 - (i)) * 1024)
#define MFMA16(a, b, c) __builtin_amdgcn_mfma_f32_16x16x32_bf16(a, b, c, 0, 0, 0)
#define S8(x) __builtin_bit_cast(s8, x)
#define LD128(off) __builtin_amdgcn_raw_buffer_load_b128(wsr, (off), 0, 16)
#define LD64(off) __builtin_amdgcn_raw_buffer_load_b64(wsr, (off), 0, 16)
#define ST128(v, off) __builtin_amdgcn_raw_buffer_store_b128((v), wsr, (off), 0, 16)
#define ST64(v, off) __builtin_amdgcn_raw_buffer_store_b64((v), wsr, (off), 0, 16)
#define OFA(i) (WS_OF + ((mb * 32 + hd) * 8 + (i)) * 1024 + l * 16)
#define POISON(i) ST128(pz, (i) < 2 ? WS_KVP + (hd * 16 + mb) * 2048 + (i) * 1024 + l * 16 : (i) == 2 ? WS_ZP + (hd * 16 + mb) * 128 + (l & 7) * 16 : OFA((i) - 3))
#define NOW() ((unsigned)__builtin_amdgcn_s_memrealtime())
constexpr unsigned SPIN = 1u << 21;
DEV const char* uptr(const char* p) {
  unsigned long long v = (unsigned long long)p;
  unsigned lo_ = __builtin_amdgcn_readfirstlane((unsigned)v), hi_ = __builtin_amdgcn_readfirstlane((unsigned)(v >> 32));
  return (const char*)(((unsigned long long)hi_ << 32) | lo_);
}
DEV void lsig(unsigned addr, unsigned val) { asm volatile("s_waitcnt lgkmcnt(0)\n ds_write_b32 %0, %1" ::"v"(addr), "v"(val) : "memory"); }
template <int SL = 0> DEV void lwait(unsigned addr, unsigned mask, unsigned target) {
  _Pragma("unroll 1") for (unsigned it = 0; it < SPIN; ++it) {
    unsigned v;
    asm volatile("ds_read_b32 %0, %1\n s_waitcnt lgkmcnt(0)" : "=v"(v) : "v"(addr) : "memory");
    if (((unsigned)__ballot(v < target) & mask) == 0u) break;
    if (SL) SLEEP(SL);
  }
}
DEV unsigned nopois(unsigned a) { return min(a, POIS - 1u); }
DEV u4 tagf(f4 v, unsigned tag) {
  u4 r;
#pragma unroll
  for (int e = 0; e < 4; ++e) r[e] = __builtin_amdgcn_perm(__float_as_uint(v[e]), tag, 0x07060500u + e);
  return r;
}
DEV void untag(u4& v, unsigned tag, unsigned& bad) {
#pragma unroll
  for (int e = 0; e < 4; ++e) v[e] ^= (tag >> (8 * e)) & 255u;
  bad |= v[0] | v[1]; bad |= v[2] | v[3];
}
DEV unsigned umax3(unsigned a, unsigned b, unsigned c) { return max(max(a, b), c); }
template <int C> DEV float dppf(float v) { return __int_as_float(__builtin_amdgcn_update_dpp(0, __float_as_int(v), C, 0xf, 0xf, false)); }
DEV unsigned wave_max(unsigned v) {
  v = max(v, (unsigned)__builtin_amdgcn_update_dpp(0, (int)v, 0x111, 0xf, 0xf, false));
  v = max(v, (unsigned)__builtin_amdgcn_update_dpp(0, (int)v, 0x112, 0xf, 0xf, false));
  v = max(v, (unsigned)__builtin_amdgcn_update_dpp(0, (int)v, 0x114, 0xf, 0xf, false));
  v = max(v, (unsigned)__builtin_amdgcn_update_dpp(0, (int)v, 0x118, 0xf, 0xf, false));
  v = max(v, (unsigned)__builtin_amdgcn_update_dpp(0, (int)v, 0x142, 0xa, 0xf, false));
  v = max(v, (unsigned)__builtin_amdgcn_update_dpp(0, (int)v, 0x143, 0xc, 0xf, false));
  return (unsigned)__builtin_amdgcn_readlane((int)v, 63);
}
DEV unsigned phi2(f2 u, f2 rs) {
  f2 t = u * rs;
  f2 a = t * 1.44269504f, p = t + 1.f, r;
  r[0] = __builtin_amdgcn_fmed3f(__builtin_amdgcn_exp2f(a[0]), p[0], 1.f);
  r[1] = __builtin_amdgcn_fmed3f(__builtin_amdgcn_exp2f(a[1]), p[1], 1.f);
  r += 1e-6f;
  return pk2(r[0], r[1]);
}

#define STDECL
#undef HB
#define HB 0
#define BSTV cw
#define STMP(i)
#define BST(i)
#define STFLUSH()
#define RETRY()
#define RETRY2()

DEV float half_sum(float v) {
  asm volatile("s_nop 1\n v_add_f32_dpp %0, %0, %0 row_ror:8 row_mask:0xf bank_mask:0xf\n s_nop 1\n v_add_f32_dpp %0, %0, %0 row_ror:4 row_mask:0xf bank_mask:0xf\n"
               "s_nop 1\n v_add_f32_dpp %0, %0, %0 row_ror:2 row_mask:0xf bank_mask:0xf\n s_nop 1\n v_add_f32_dpp %0, %0, %0 row_ror:1 row_mask:0xf bank_mask:0xf\n s_nop 1" : "+v"(v));
  auto r = __builtin_amdgcn_permlane16_swap(__float_as_uint(v), __float_as_uint(v), false, false);
  return __uint_as_float(r[0]) + __uint_as_float(r[1]);
}

struct FQ { s8 a[4]; s4 b[12]; };
template <bool B> struct BC { static constexpr bool v = B; };
template <int I> struct IC { static constexpr int v = I; };

__global__ __launch_bounds__(512) void fused(const u16* __restrict__ x, const u16* __restrict__ wqkv, const u16* __restrict__ wrt,
                                             char* __restrict__ ws_, const u16* __restrict__ wo_, const u16* __restrict__ wg,
                                             const u16* __restrict__ wu, const u16* __restrict__ wd, u16* __restrict__ out) {
  extern __shared__ __attribute__((aligned(16))) char lds[];
  const char *kx, *kq, *kr, *wo; char* ws;
  asm volatile("s_load_dwordx2 %0, %5, 0\n s_load_dwordx2 %1, %5, 8\n s_load_dwordx2 %2, %5, 16\n s_load_dwordx2 %3, %5, 24\n s_load_dwordx2 %4, %5, 32"
               : "=&s"(kx), "=&s"(kq), "=&s"(kr), "=&s"(ws), "=&s"(wo) : "s"(__builtin_amdgcn_kernarg_segment_ptr()));
  SBAR();
  const int w = __builtin_amdgcn_readfirstlane(threadIdx.x >> 6), l = threadIdx.x & 63;
  STDECL;
  STMP(0);
  const int g = blockIdx.x, xcd = g & 7, j = g >> 3, ii = j >> 2;
  const int mb = (xcd & 1) * 8 + ii, hp = (xcd >> 1) * 4 + (j & 3);
  unsigned cw, cwo = (l * 32 + j) * 64; unsigned long long cwb;
  asm volatile("s_getpc_b64 %2\n.Lcode_here:\n v_min_u32 %1, max(.Lend_c - .Lcode_here, .Lend_l - .Lcode_here, .Lend_h - .Lcode_here, .Lend_s - .Lcode_here) - 4, %1\n"
               "s_cmp_lg_u32 %3, 7\n s_cbranch_scc1 .Lskip_w\n global_load_dword %0, %1, %2\n.Lskip_w:" : "=&v"(cw), "+v"(cwo), "=&s"(cwb) : "s"(w) : "memory", "scc");
  SBAR();
  const int lw = w & 1, rot = 2 * xcd + 4 * ((ii + JX * j) & 3);
  const unsigned ldsb = (unsigned)(size_t)(LAS char*)lds;
  unsigned va[4];
#pragma unroll
  for (int i = 0; i < 4; ++i) va[i] = (8 * i + (l >> 3)) * 2048 + (((l & 7) ^ ((i & 1) * 4 + (l >> 4))) * 16) + (3 - i) * 1024;
  const int ao = (mb * 128 + lw * 64) * 2048 - 3072;
  long long ao0 = ao + (rot & 15) * 128;
  unsigned mba = ldsb + lw * 8192;
  const int srow = mb * 128 + hp * 8 + 2 * w + (l >> 5);
  unsigned xo = srow * 2048 + (l & 31) * 16, ro = (l & 31) * 16;
  asm volatile("" : "+v"(va[0]), "+v"(va[1]), "+v"(va[2]), "+v"(va[3]), "+v"(xo), "+v"(ro));
  asm volatile("s_waitcnt lgkmcnt(0)" : "+s"(kx), "+s"(kq), "+s"(kr), "+s"(ws), "+s"(wo), "+s"(ao0), "+s"(mba));
  SBAR();
  asm volatile("s_sub_u32 m0, %6, 4\n s_cmp_gt_u32 m0, 1\n s_cbranch_scc1 .Lskip_l\n s_mov_b32 m0, %5\n s_nop 0\n global_load_lds_dwordx4 %0, %4\n global_load_lds_dwordx4 %1, %4 offset:1024\n"
               "global_load_lds_dwordx4 %2, %4 offset:2048\n global_load_lds_dwordx4 %3, %4 offset:3072\n.Lskip_l:"
               :: "v"(va[0]), "v"(va[1]), "v"(va[2]), "v"(va[3]), "s"(kx + ao0), "s"(mba), "s"(w) : "memory", "scc");
  u4 xa[4], rw[4]; unsigned ep;
  asm volatile("s_cmp_gt_u32 %14, 3\n s_cbranch_scc1 .Lskip_x\n"
               "global_load_dwordx4 %0, %9, %11\n global_load_dwordx4 %1, %9, %11 offset:512\n global_load_dwordx4 %2, %9, %11 offset:1024\n global_load_dwordx4 %3, %9, %11 offset:1536\n"
               "global_load_dwordx4 %4, %10, %12\n global_load_dwordx4 %5, %10, %12 offset:512\n global_load_dwordx4 %6, %10, %12 offset:1024\n global_load_dwordx4 %7, %10, %12 offset:1536\n"
               ".Lskip_x:\n s_load_dword %8, %13, 0"
               : "=&v"(xa[0]), "=&v"(xa[1]), "=&v"(xa[2]), "=&v"(xa[3]), "=&v"(rw[0]), "=&v"(rw[1]), "=&v"(rw[2]), "=&v"(rw[3]), "=&s"(ep)
               : "v"(xo), "v"(ro), "s"(kx), "s"(kr), "s"(ws), "s"(w) : "memory", "scc");
#define GETTAG(t) unsigned t; asm volatile("s_waitcnt lgkmcnt(0)\n s_add_u32 %0, %1, 1" : "=s"(t) : "s"(ep) : "memory", "scc")
  SBAR();
#define PTAB 0xA98FEDC1045B7623ull
#define PHT 0x6624924999924924ull
#define CX 4
  const bool sp = (hp & 7) == 3;
  const int nb = hp;
#define PAIRAT(p) ((int)(PTAB >> (4 * (p))) & 15)
#define HEADAT(s) (2 * PAIRAT((s) >> 1) + ((s) & 1))
  const int ph = (int)(PHT >> (2 * j)) & 3;
#define HQT 0xDCBA098765320410ull
#define CXA 1
#define CHUNK(b) ((b) < 14 ? 4 * (b) + 3 : ((b) - 14) + ((((b) - 14) * 43) >> 7))
  const unsigned adFL = ldsb + L_FL + 4 * (l & 7), myFL = ldsb + L_FL + 4 * w;
  const __amdgpu_buffer_rsrc_t wse = __builtin_amdgcn_make_buffer_rsrc(ws, 0, 64, 0x00020000);
  const int r15 = l & 15, g4 = l >> 4, q = (l & 15) >> 2, p = l & 3;

  if (w != 4 && w != 5) {
  if (w >= 6) {
    const int mw = w - 6;
    GETTAG(tag);
    const int bq = ((int)(HQT >> (4 * hp)) & 15) * 4 + (ii & 3), ksp = (xcd & 1) * 2 + (ii >> 2);
    const int chunk = CHUNK(bq), bg = (CXA || bq < 14) ? bq ^ (w & 1) : bq, chg = CHUNK(bg);
    auto helper = [&](auto SP) {
    constexpr bool spare = decltype(SP)::v, moe = !spare && !NOMOE;
    int nbar = 0;
#define TICK() do { if (HB) { BST(nbar); asm volatile("" : "+v"(BSTV)); } SBAR(); BAR(); SBAR(); ++nbar; } while (0)
#define DTICK(lim) do { if (nbar < (lim)) TICK(); else SLEEP(2); } while (0)
    const int hd = hp * 2 + mw;
    const u4 pz = (u4)(POIS);
    MKWS(tag);
    const int np = l & 7, kset = (l >> 3) & 1;
    u4 gw[4][8], dw[2][8];
    const __amdgpu_buffer_rsrc_t wgr = __builtin_amdgcn_make_buffer_rsrc((void*)(mw ? wu : wg), 0, D * F * 2, 0x00020000);
    const __amdgpu_buffer_rsrc_t wdr = __builtin_amdgcn_make_buffer_rsrc((void*)wd, 0, D * F * 2, 0x00020000);
    const __amdgpu_buffer_rsrc_t wor = __builtin_amdgcn_make_buffer_rsrc((void*)wo, 0, D * D * 2, 0x00020000);
    const unsigned gvo = ((256 * ksp + 32 * kset + 8 * g4) * F + 64 * chg + 8 * np) * 2;
    const unsigned dvo = ((64 * chunk + 8 * g4) * D + 256 * ksp + 128 * mw + 8 * r15) * 2;
    auto burst = [&](int k, int i0 = 0, int i1 = 16) {
      if (moe) {
#pragma unroll
        for (int i = i0; i < i1; ++i) {
          if (k < 2) gw[(2 * k + (i >> 3)) & 3][i & 7] = __builtin_amdgcn_raw_buffer_load_b128(wgr, gvo, (128 * k + 64 * (i >> 3) + (i & 7)) * F * 2, 2);
          else dw[i >> 3][i & 7] = __builtin_amdgcn_raw_buffer_load_b128(wdr, dvo, (32 * (i >> 3) + (i & 7)) * D * 2, 2);
        }
      }
    };
    auto transp = [&](u4(&v)[8]) {
      if (moe) {
        u4 f[8];
#pragma unroll
        for (int c = 0; c < 8; ++c)
#pragma unroll
          for (int e = 0; e < 4; ++e) {
            const unsigned ev = v[2 * e][c >> 1], od = v[2 * e + 1][c >> 1];
            f[c][e] = (c & 1) ? __builtin_amdgcn_perm(od, ev, 0x07060302u) : __builtin_amdgcn_perm(od, ev, 0x05040100u);
          }
#pragma unroll
        for (int c = 0; c < 8; ++c) { v[c] = f[c]; asm volatile("" : "+v"(v[c])); }
      }
    };
    auto pois = [&]() {
      ST128(pz, WS_KVP + (hd * 16 + mb) * 2048 + l * 16); ST128(pz, WS_KVP + (hd * 16 + mb) * 2048 + 1024 + l * 16); ST128(pz, WS_ZP + (hd * 16 + mb) * 128 + (l & 7) * 16);
    };
    unsigned wt0, wt1, wt2 = 0, wt3 = 0;
    auto touch = [&]() {
      const unsigned to = (j * 32 + mw * 16 + (l >> 3)) * 2048 + (xcd >> 1) * 512 + (l & 7) * 64;
      wt0 = __builtin_amdgcn_raw_buffer_load_b32(wor, to, 0, 0); wt1 = __builtin_amdgcn_raw_buffer_load_b32(wor, to, 16384, 0);
      if (TLBW) {
        const __amdgpu_buffer_rsrc_t outr = __builtin_amdgcn_make_buffer_rsrc(out, 0, 2048 * D * 2, 0x00020000);
        wt2 = __builtin_amdgcn_raw_buffer_load_b32(outr, (mb * 128 + mw * 64 + (l & 3) * 16) * 2048 + nb * 128, 0, 0);
        wt3 = __builtin_amdgcn_raw_buffer_load_b32(wsr, (l & 1) ? WS_GP + g * 4096 + mw * 2048 : WS_YP + (g >> 2) * 32768 + (l & 2) * 8192, 0, 0);
      }
    };
    s4 sb[24];
    auto colrd = [&](int h) {
      const unsigned bo = ldsb + (1 + h) * STG + 16384 + (8 * g4 + q) * 128 + (p & 1) * 8;
      const int sw = ((q >> 1) << 2) | ((g4 & 1) << 1);
      const unsigned b0 = bo + (((mw * 4 + (p >> 1)) ^ sw) * 16), b1 = bo + (((mw * 4 + 2 + (p >> 1)) ^ sw) * 16);
#pragma unroll
      for (int n = 0; n < 12; ++n) { RDTR(sb[2 * n], (n & 1) ? b1 : b0, ((n % 6) >> 1) * 8192 + (n / 6) * 4096); RDTR(sb[2 * n + 1], (n & 1) ? b1 : b0, ((n % 6) >> 1) * 8192 + (n / 6) * 4096 + 512); }
      WAITL(0);
      SBAR();
    };
    auto colsum = [&]() {
      const u4 ones = (u4)(0x3F803F80u);
      f4 c[6];
#pragma unroll
      for (int n = 0; n < 12; ++n) c[n % 6] = MFMA16(S8(ones), CAT(sb[2 * n], sb[2 * n + 1]), n >= 6 ? c[n % 6] : (f4)(0.f));
      const u4 csg = (u4){__float_as_uint(g4 ? c[3][0] : c[0][0]), __float_as_uint(g4 ? c[4][0] : c[1][0]), __float_as_uint(g4 ? c[5][0] : c[2][0]), tag};
      WAITV(0);
      asm volatile("" : "+v"(cw));
      ST128(csg, l < 32 ? WS_CS + (hd * 16 + mb) * 512 + l * 16 : WS_SZ);
    };
    u4 stv, cv[8], cd; float cs[3];
    auto gload = [&]() {
      stv = LD128(WS_ST + (mb * 128 + mw * 64 + l) * 16);
#pragma unroll
      for (int i = 0; i < 8; ++i) cv[i] = LD128(WS_CS + hd * 8192 + i * 1024 + l * 16);
    };
    auto gbad = [&]() {
      unsigned bad = (stv[1] ^ tag) | (stv[3] ^ tag);
#pragma unroll
      for (int i = 0; i < 8; ++i) bad |= cv[i][3] ^ tag;
      return __ballot(bad != 0u) != 0ull;
    };
    int idx;
    auto gfin = [&]() {
      if (gbad()) { RETRY(); _Pragma("unroll 1") for (unsigned it = 0; it < SPIN; ++it) { SLEEP(2); gload(); if (!gbad()) break; } }
#pragma unroll
      for (int e = 0; e < 3; ++e) {
        float a = __uint_as_float(cv[0][e]);
#pragma unroll
        for (int i = 1; i < 8; ++i) a += __uint_as_float(cv[i][e]);
        auto r = __builtin_amdgcn_permlane32_swap(__float_as_uint(a), __float_as_uint(a), false, false);
        cs[e] = __uint_as_float(r[0]) + __uint_as_float(r[1]);
      }
      asm volatile("" : "+v"(cs[0]), "+v"(cs[1]), "+v"(cs[2]) : "v"(wt0), "v"(wt1), "v"(wt2), "v"(wt3));
    };
    auto cload = [&]() { cd = LD128(WS_CAND + (((hp >> 2) == 2) * 16 + mb) * 128 + (l & 7) * 16); };
    auto cfin = [&]() {
      if (__builtin_expect(__ballot(cd[1] != tag || cd[3] != tag) != 0ull, 0)) {
        RETRY();
        _Pragma("unroll 1") for (unsigned it = 0; it < SPIN; ++it) { SLEEP(4); cload(); if (__ballot(cd[1] != tag || cd[3] != tag) == 0ull) break; }
      }
      cd[0] = max(cd[0], cd[2]);
      idx = (int)(cd[0] & 2047u);
    };
    auto lpub = [&]() {
      *(LAS float*)(lds + L_M + (mw * 64 + l) * 4) = __uint_as_float(stv[0]);
      *(LAS float*)(lds + L_RS + (mw * 64 + l) * 4) = __uint_as_float(stv[2]);
      if (l < 32) {
#pragma unroll
        for (int e = 0; e < 3; ++e) *(LAS float*)(lds + L_CS + mw * 384 + ((3 * g4 + e) * 16 + r15) * 4) = cs[e];
      }
      if (mw == 0 && l < 8) *(LAS int*)(lds + L_SEL + l * 4) = idx;
      lsig(myFL, 1u);
    };
    u4 xs[4]; float xrs;
    asm volatile("" ::"s"(wg), "s"(wu), "s"(wd), "v"(cw));
    if (HPRIO) __builtin_amdgcn_s_setprio(HPRIO);
    TICK();
    if (spare) {
      TICK();
      pois();
      if (ii >> 2) TICK();
      colrd(ii >> 2);
      if (!(ii >> 2)) TICK();
      u4 kk[8];
      auto kload = [&]() {
#pragma unroll
        for (int i = 0; i < 8; ++i) kk[i] = LD128(WS_RG + mw * 8192 + i * 1024 + l * 16);
      };
      kload();
      TICK();
      _Pragma("unroll 1") for (unsigned it = 0; it < SPIN; ++it) {
        unsigned bad = 0;
#pragma unroll
        for (int i = 0; i < 8; ++i) { bad |= (kk[i][1] ^ tag) | (kk[i][3] ^ tag); asm volatile("" : "+v"(bad)); }
        if (__ballot(bad != 0u) == 0ull) break;
        RETRY();
        kload();
        DTICK(7);
      }
      __builtin_amdgcn_s_setprio(3);
      unsigned m1 = max(kk[0][0], kk[0][2]), m2 = min(kk[0][0], kk[0][2]), m3 = 0;
#pragma unroll
      for (int i = 2; i < 16; ++i) {
        const unsigned a = kk[i >> 1][(i & 1) * 2];
        asm("v_med3_u32 %0, %1, %2, %0" : "+v"(m3) : "v"(m2), "v"(a));
        asm("v_med3_u32 %0, %1, %2, %0" : "+v"(m2) : "v"(m1), "v"(a));
        m1 = max(m1, a);
      }
      unsigned mine8 = 0;
#pragma unroll
      for (int it = 0; it < 8; ++it) {
        const unsigned km = wave_max(m1);
        mine8 = l == it ? km : mine8;
        const bool win = m1 == km;
        if (it < 7) { m1 = win ? m2 : m1; m2 = win ? m3 : m2; m3 = win ? 0u : m3; }
        if (it == 3 && nbar < 7) TICK();
      }
      if (__builtin_expect(__ballot(m1 == 0u) != 0ull, 0)) {
        unsigned lim = 0;
#pragma unroll 1
        for (int it = 0; it < 8; ++it) {
          unsigned m = 0;
#pragma unroll
          for (int i = 0; i < 8; ++i) m = umax3(m, kk[i][0] - lim, kk[i][2] - lim);
          lim += wave_max(m);
          mine8 = l == it ? lim : mine8;
        }
      }
      ST64(((u2){mine8, tag}), l < 8 ? WS_CAND + ((hp >> 3) * 16 + mb) * 128 + (mw ? 7 - l : l) * 16 + mw * 8 : WS_SZ);
      __builtin_amdgcn_s_setprio(HPRIO);
      STMP(2);
      TICK();
      colsum();
      DTICK(9);
      touch();
      while (nbar < 10) TICK();
      gload(); cload();
      if (RT && mw) asm volatile("global_load_dword %0, %1, %2 sc1" : "+v"(cw) : "v"(cwo), "s"(cwb) : "memory");
      TICK(); TICK();
      WAITV(0);
      asm volatile("" : "+v"(cw));
      gfin(); cfin();
      STMP(3);
      TICK(); TICK(); TICK();
      lpub();
    } else {
      if (PV == 0) pois();
      TICK();
      if (PV == 1) pois();
      if (ii >> 2) TICK();
      colrd(ii >> 2);
      TICK();
      if (CL && !(ii >> 2)) TICK();
      colsum();
      if (!CL && !(ii >> 2)) TICK();
      u4 sv; f4 acc[8];
      auto xload = [&]() {
        const u16* xp = x + (size_t)idx * D + 256 * ksp + 32 * kset + 8 * g4;
        sv = LD128(WS_ST + idx * 16);
#pragma unroll
        for (int it = 0; it < 4; ++it) xs[it] = *(const u4*)(xp + 64 * it);
        SBAR();
      };
      auto xnorm = [&]() {
        if (__builtin_expect(__ballot(sv[1] != tag || sv[3] != tag) != 0ull, 0)) {
          _Pragma("unroll 1") for (unsigned it = 0; it < SPIN; ++it) { SLEEP(1); sv = LD128(WS_ST + idx * 16); if (__ballot(sv[1] != tag || sv[3] != tag) == 0ull) break; }
        }
        const float nm = -__uint_as_float(sv[0]);
        xrs = __uint_as_float(sv[2]);
#pragma unroll
        for (int it = 0; it < 4; ++it) {
          float t0, t1, t2, t3; unsigned x0 = xs[it][0], x1 = xs[it][1], x2 = xs[it][2], x3 = xs[it][3];
          asm volatile("v_dot2_f32_bf16 %4, %0, %8, %10\n v_dot2_f32_bf16 %0, %0, %9, %10\n v_dot2_f32_bf16 %5, %1, %8, %10\n v_dot2_f32_bf16 %1, %1, %9, %10\n"
                       "v_dot2_f32_bf16 %6, %2, %8, %10\n v_dot2_f32_bf16 %2, %2, %9, %10\n v_dot2_f32_bf16 %7, %3, %8, %10\n v_dot2_f32_bf16 %3, %3, %9, %10\n"
                       "v_cvt_pk_bf16_f32 %0, %4, %0\n v_cvt_pk_bf16_f32 %1, %5, %1\n v_cvt_pk_bf16_f32 %2, %6, %2\n s_nop 0\n v_cvt_pk_bf16_f32 %3, %7, %3"
                       : "+v"(x0), "+v"(x1), "+v"(x2), "+v"(x3), "=&v"(t0), "=&v"(t1), "=&v"(t2), "=&v"(t3) : "s"(0x00003F80u), "s"(0x3F800000u), "v"(nm));
          xs[it] = (u4){x0, x1, x2, x3};
        }
        asm volatile("s_nop 1" : "+v"(xs[0]), "+v"(xs[1]), "+v"(xs[2]), "+v"(xs[3]), "+v"(xrs));
        SBAR();
      };
      auto gmul = [&](int i0, int i1) {
#pragma unroll
        for (int it = i0; it < i1; ++it)
#pragma unroll
          for (int c = 0; c < 8; ++c) acc[c] = MFMA16(S8(gw[it][c]), S8(xs[it]), it ? acc[c] : (f4)(0.f));
#pragma unroll
        for (int c = 0; c < 8; ++c) asm volatile("" : "+v"(acc[c]));
        SBAR();
      };
      auto gout = [&]() {
        unsigned go = r15 < 8 ? WS_GP + ((chg * 4 + ksp) * 2 + mw) * 2048 + (g4 >> 1) * 1024 + (g4 & 1) * 512 + r15 * 16 : WS_SZ;
        asm volatile("" : "+v"(go));
#pragma unroll
        for (int i = 0; i < 4; ++i) {
          unsigned a[4], b[4];
#pragma unroll
          for (int c = 0; c < 4; ++c) {
            auto r = __builtin_amdgcn_permlane32_swap(__float_as_uint(acc[c][i]), __float_as_uint(acc[c + 4][i]), false, false);
            a[c] = r[0]; b[c] = r[1];
          }
          unsigned s0, s1, s2, s3;
#define AR(d, x, y) "v_add_f32_dpp %" #d ", %" #x ", %" #y " row_ror:8 row_mask:0xf bank_mask:0xf\n"
          asm volatile("s_nop 1\n" AR(0, 8, 4) AR(1, 9, 5) AR(2, 10, 6) AR(3, 11, 7) : "=&v"(s0), "=&v"(s1), "=&v"(s2), "=&v"(s3)
                       : "v"(a[0]), "v"(a[1]), "v"(a[2]), "v"(a[3]), "v"(b[0]), "v"(b[1]), "v"(b[2]), "v"(b[3]));
          ST128(tagf(__builtin_bit_cast(f4, ((u4){s0, s1, s2, s3})), tag), go + i * 128);
        }
        SBAR();
      };
      for (int i = 0; i < ph; ++i) TICK();
      burst(0); touch();
      TICK();
      TICK();
      if (TL) { transp(gw[0]); transp(gw[1]); }
      TICK();
      if (!TL) transp(gw[0]);
      TICK();
      if (!TL) transp(gw[1]);
      TICK();
      if (ph) cload();
      if (RT && mw) asm volatile("global_load_dword %0, %1, %2 sc1" : "+v"(cw) : "v"(cwo), "s"(cwb) : "memory");
      SBAR();
      burst(1);
      TICK();
      TICK(); TICK();
      transp(gw[2]);
      WAITV(0);
      asm volatile("" : "+v"(cw));
      if (ph < 2) TICK();
      transp(gw[3]);
      if (ph < 1) { SBAR(); cload(); TICK(); }
      SBAR();
      if (XO == 0) { cfin(); xload(); gload(); } else { gload(); SBAR(); cfin(); xload(); }
      TICK();
      gfin();
      lpub();
      SBAR();
      __builtin_amdgcn_s_setprio(0);
      burst(2, 0, D1);
      STMP(3);
      SBAR();
      if (HS0) SLEEP(HS0);
      if (G1 & 1) { lwait<HW1>(adFL, 0x0Fu, G1 >> 4); if (G1S) SLEEP(G1S); __builtin_amdgcn_s_setprio(TP0); }
      xnorm();
      if (G1 & 2) { lwait<2>(adFL, 0x0Fu, G1 >> 4); if (G1S) SLEEP(G1S); __builtin_amdgcn_s_setprio(TP0); }
      STMP(5);
      gmul(0, 4);
      gout();
      STMP(6);
      SBAR();
    }
    __builtin_amdgcn_s_setprio(TP1);
    STMP(4);
    if (moe) {
      if (D2 == 0) { burst(2, D1, 16); SBAR(); }
      if (D1 >= 8 || D2 == 0) transp(dw[0]); if (D1 == 16 || D2 == 0) transp(dw[1]);
      SBAR();
      lwait<2>(adFL, 0x30u, 3);
      if (G2S) SLEEP(G2S);
      STMP(7);
      if (D2 == 1) { burst(2, D1, 16); SBAR(); }
      f4 acc[8];
      u4 ha[2];
      {
        u4 pg[4][2];
        auto pload = [&]() {
#pragma unroll
          for (int ks = 0; ks < 4; ++ks)
#pragma unroll
            for (int t = 0; t < 2; ++t) pg[ks][t] = LD128(WS_GP + ((chunk * 4 + ks) * 4 + 2 * t + mw) * 1024 + (4 * kset + g4) * 128 + np * 16);
        };
        auto pbad = [&]() {
          unsigned bad = 0;
#pragma unroll
          for (int i = 0; i < 8; ++i) untag(pg[i >> 1][i & 1], tag, bad);
          return __ballot((bad & 255u) != 0u) != 0ull;
        };
        pload();
        SBAR();
        if (D2 == 2) { burst(2, D1, 16); SBAR(); }
        __builtin_amdgcn_s_setprio(TP2);
        if (D1 < 8 && D2 == 1) transp(dw[0]);
        if (D1 < 16 && D2 == 1) transp(dw[1]);
        SBAR();
        if (__builtin_expect(pbad(), 0)) { _Pragma("unroll 1") for (unsigned it = 0; it < SPIN; ++it) { RETRY2(); SLEEP(4); pload(); if (!pbad()) break; } }
        STMP(10);
        f4 gq[2];
#pragma unroll
        for (int t = 0; t < 2; ++t)
          gq[t] = ((__builtin_bit_cast(f4, pg[0][t]) + __builtin_bit_cast(f4, pg[1][t])) + (__builtin_bit_cast(f4, pg[2][t]) + __builtin_bit_cast(f4, pg[3][t]))) * xrs;
        float hv[4];
#pragma unroll
        for (int e = 0; e < 4; ++e) {
          const float gg = rbf(gq[0][e]), uu = rbf(gq[1][e]);
          hv[e] = rbf(gg / (1.f + __expf(-gg))) * uu;
        }
        *(LAS u2*)(lds + L_HX + l * 16 + mw * 8) = (u2){pk2(hv[0], hv[1]), pk2(hv[2], hv[3])};
        lsig(myFL, 2u);
        if (D1 < 16 && D2 == 2) { SBAR(); if (D1 < 8) transp(dw[0]); transp(dw[1]); SBAR(); }
        lwait(adFL, 0xC0u, 2);
        ha[0] = *(const LAS u4*)(lds + L_HX + l * 16); ha[1] = *(const LAS u4*)(lds + L_HX + (l ^ 8) * 16);
      }
#pragma unroll
      for (int it = 0; it < 2; ++it)
#pragma unroll
        for (int c = 0; c < 8; ++c) acc[c] = MFMA16(S8(ha[it]), S8(dw[it][c]), it ? acc[c] : (f4)(0.f));
      if (g4 < 2) {
#pragma unroll
        for (int i = 0; i < 4; ++i) {
          const unsigned yo = WS_YP + ((chunk * 8 + 4 * g4 + i) * D + 256 * ksp + 128 * mw + 4 * r15) * 4;
          ST128(tagf((f4){acc[0][i], acc[1][i], acc[2][i], acc[3][i]}, tag), yo);
          ST128(tagf((f4){acc[4][i], acc[5][i], acc[6][i], acc[7][i]}, tag), yo + 256);
        }
      }
      STMP(11);
    }
    float sg;
    {
      const unsigned mono = cd[0] >> 16, rb = (mono & 0x8000u) ? (mono & 0x7FFFu) : (~mono & 0xFFFFu);
      sg = rbf(1.f / (1.f + __expf(-lo(rb))));
    }
    unsigned mine = (unsigned)__ballot(!NOMOE && l < 8 && (idx >> 7) == mb && (l & 1) == mw);
    if (mine) {
      if (spare) lwait<4>(adFL, 0x30u, 3);
      {
        const u2 f = LD64(WS_OH + (mb * 16 + r15) * 128);
        const unsigned t0 = NOW();
        unsigned rel = f[1] + YD - t0;
        rel = wave_max(f[0] == tag && rel < 600u ? rel : 0u);
        _Pragma("unroll 1") for (unsigned it = 0; it < SPIN; ++it) { if (NOW() - t0 >= rel) break; SLEEP(2); }
      }
      STMP(8);
      lwait<1>(adFL, 0x0Fu, 4);
      __builtin_amdgcn_s_setprio(TP2);
      while (mine) {
        const int t = __builtin_ctz(mine); mine &= mine - 1;
        u4 yv[14];
        _Pragma("unroll 1") for (unsigned it = 0; it < SPIN; ++it) {
          unsigned bad = 0;
#pragma unroll
          for (int i = 0; i < 14; ++i) yv[i] = LD128(WS_YP + (((i * 4 + g4) * 8 + t) * D + (nb >> 1) * 128 + (r15 >> 3) * 64 + (nb & 1) * 32 + (r15 & 7) * 4) * 4);
          SBAR();
#pragma unroll
          for (int i = 0; i < 14; ++i) untag(yv[i], tag, bad);
          if (__ballot((bad & 255u) != 0u) == 0ull) break;
          RETRY();
          SLEEP(YSL);
        }
        f4 y = __builtin_bit_cast(f4, yv[0]);
#pragma unroll
        for (int i = 1; i < 14; ++i) y += __builtin_bit_cast(f4, yv[i]);
        const float gt = __int_as_float(__builtin_amdgcn_readlane(__float_as_int(sg), t));
#pragma unroll
        for (int e = 0; e < 4; ++e) {
          auto r = __builtin_amdgcn_permlane32_swap(__float_as_uint(y[e]), __float_as_uint(y[e]), false, false);
          const float a = __uint_as_float(r[0]) + __uint_as_float(r[1]);
          auto r2 = __builtin_amdgcn_permlane16_swap(__float_as_uint(a), __float_as_uint(a), false, false);
          y[e] = rbf(rbf(__uint_as_float(r2[0]) + __uint_as_float(r2[1])) * gt);
        }
        if (l < 16) *(LAS f4*)(lds + L_KVX + t * 256 + (l & 7) * 32 + (l >> 3) * 16) = y;
      }
    }
    lsig(myFL, 3u);
    STMP(9);
    STFLUSH();
    if (spare) asm volatile(".Lend_s:"); else asm volatile(".Lend_h:");
    };
    if (sp) helper(BC<true>()); else helper(BC<false>());
    return;
  }

  const int wr = w >> 1, wc = w & 1;
  u2 kg; u4 sg4; unsigned tag;
  {
    const int row = srow;
    SBAR(); BST(0); SBAR();
    asm volatile("s_waitcnt vmcnt(4)" : "+v"(xa[0]), "+v"(xa[1]), "+v"(xa[2]), "+v"(xa[3]));
    SBAR(); BST(1); SBAR();
    float s0 = 0, s1 = 0;
#pragma unroll
    for (int c = 0; c < 4; ++c)
#pragma unroll
      for (int e = 0; e < 4; e += 2) { unsigned x0 = xa[c][e], x1 = xa[c][e + 1]; s0 = dot2(x0, 0x3F803F80u, s0); s1 = dot2(x1, 0x3F803F80u, s1); }
    const float m = half_sum(s0 + s1) * (1.f / D);
    f2 v[16], q2 = (f2)(0.f), q3 = (f2)(0.f);
#pragma unroll
    for (int c = 0; c < 16; ++c) v[c] = (f2){lo(xa[c >> 2][c & 3]), hi(xa[c >> 2][c & 3])} - m;
#pragma unroll
    for (int c = 0; c < 16; c += 2) { q2 += v[c] * v[c]; q3 += v[c + 1] * v[c + 1]; }
    q2 += q3;
    const float mr = rbf(m);
    const float rs = 1.f / rbf(sqrtf(rbf(rbf(half_sum(q2[0] + q2[1]) * (1.f / D)) + 1e-5f)));
    float d0 = 0, d1 = 0;
    asm volatile("s_waitcnt vmcnt(0)" : "+v"(rw[0]), "+v"(rw[1]), "+v"(rw[2]), "+v"(rw[3]), "+v"(d0) : "v"(rs));
#pragma unroll
    for (int c = 0; c < 16; c += 2) {
      const f2 e0 = (f2){lo(xa[c >> 2][c & 3]), hi(xa[c >> 2][c & 3])} - mr, e1 = (f2){lo(xa[c >> 2][(c & 3) + 1]), hi(xa[c >> 2][(c & 3) + 1])} - mr;
      const unsigned p0 = pk2(e0[0], e0[1]), p1 = pk2(e1[0], e1[1]);
      const f2 n0 = (f2){lo(p0), hi(p0)} * rs, n1 = (f2){lo(p1), hi(p1)} * rs;
      unsigned c0 = rw[c >> 2][c & 3], c1 = rw[c >> 2][(c & 3) + 1];
      d0 = dot2(pk2(n0[0], n0[1]), c0, d0); d1 = dot2(pk2(n1[0], n1[1]), c1, d1);
    }
    const float d = half_sum(d0 + d1);
    const unsigned rb = pk2(d, d) & 0xFFFFu;
    const unsigned mono = (rb & 0x8000u) ? (~rb & 0xFFFFu) : (rb | 0x8000u);
    SBAR();
    GETTAG(tg); tag = tg;
    kg = (u2){(mono << 16) | (unsigned)row, tag};
    sg4 = (u4){__float_as_uint(mr), tag, __float_as_uint(rs), tag};
  }
  MKWS(tag);
  SBAR(); BST(2); SBAR();
  if ((l & 31) == 0) { ST64(kg, WS_RG + srow * 8); ST128(sg4, WS_ST + srow * 16); }
  unsigned aad[2], bad[2];
#pragma unroll
  for (int s = 0; s < 2; ++s) aad[s] = ldsb + (wr * 64 + r15) * 128 + (((4 * s + g4) ^ ((r15 >> 1) & 7)) * 16);
#pragma unroll
  for (int nh = 0; nh < 2; ++nh) bad[nh] = ldsb + 16384 + (8 * g4 + q) * 128 + (((wc * 4 + 2 * nh + (p >> 1)) ^ (((q >> 1) << 2) | ((g4 & 1) << 1))) * 16) + (p & 1) * 8;
  f4 acc[4][6];
#pragma unroll
  for (int m = 0; m < 4; ++m)
#pragma unroll
    for (int n = 0; n < 6; ++n) { acc[m][n] = (f4)(0.f); asm volatile("" : "+v"(acc[m][n])); }
#define MF(m, n, Fq) do { if (n < 2) acc[m][n] = MFMA16(CAT(Fq.b[2 * n], Fq.b[2 * n + 1]), Fq.a[m], acc[m][n]); \
     else acc[m][n] = MFMA16(Fq.a[m], CAT(Fq.b[2 * n], Fq.b[2 * n + 1]), acc[m][n]); SBAR(); } while (0)
#define RB(G, n, vb0, vb1, SS, hf) RDTR(G.b[2 * (n) + (hf)], (((n) & 1) ? vb1 : vb0), ((n) >> 1) * 8192 + (SS) * 4096 + (hf) * 512)
#define RA(G, m, va) RD128(G.a[m], va, (m) * 2048)
#define SUB(Fq, G, va, vb0, vb1, SS, PRE) do { \
    WAITL(13); SBAR(); \
    MF(0, 0, Fq); PRE; RA(G, 0, va); SBAR(); \
    WAITL(11); SBAR(); \
    MF(1, 0, Fq); RB(G, 0, vb0, vb1, SS, 0); SBAR(); MF(2, 0, Fq); RB(G, 0, vb0, vb1, SS, 1); SBAR(); MF(3, 0, Fq); RA(G, 1, va); SBAR(); \
    WAITL(12); SBAR(); \
    MF(0, 1, Fq); RA(G, 2, va); SBAR(); MF(1, 1, Fq); RA(G, 3, va); SBAR(); MF(2, 1, Fq); RB(G, 1, vb0, vb1, SS, 0); SBAR(); MF(3, 1, Fq); RB(G, 1, vb0, vb1, SS, 1); SBAR(); \
    WAITL(8); SBAR(); \
    MF(0, 2, Fq); RB(G, 2, vb0, vb1, SS, 0); SBAR(); MF(1, 2, Fq); RB(G, 2, vb0, vb1, SS, 1); SBAR(); MF(2, 2, Fq); RB(G, 3, vb0, vb1, SS, 0); SBAR(); MF(3, 2, Fq); RB(G, 3, vb0, vb1, SS, 1); SBAR(); \
    MF(0, 3, Fq); RB(G, 4, vb0, vb1, SS, 0); SBAR(); MF(1, 3, Fq); RB(G, 4, vb0, vb1, SS, 1); SBAR(); MF(2, 3, Fq); RB(G, 5, vb0, vb1, SS, 0); SBAR(); MF(3, 3, Fq); RB(G, 5, vb0, vb1, SS, 1); SBAR(); \
    MF(0, 4, Fq); MF(1, 4, Fq); MF(2, 4, Fq); MF(3, 4, Fq); MF(0, 5, Fq); MF(1, 5, Fq); MF(2, 5, Fq); MF(3, 5, Fq); } while (0)
#define KSTEP(MODE) do { \
    if (kt == sx) { const unsigned xo = xad + buf * STG; \
      _Pragma("unroll") for (int t = 0; t < 8; ++t) { const unsigned a_ = xo ^ (t * 16); \
        asm volatile("ds_read_b64 %0, %1" : "+v"(xv[t]) : "v"(a_) : "memory"); } \
      WAITL(0); SBAR(); } \
    const int nbuf = (buf + 1) & 3; \
    const unsigned bo = buf * STG, nbo = nbuf * STG; \
    { unsigned va = aad[1] + bo, vb0 = bad[0] + bo, vb1 = bad[1] + bo; SUB(f0, f1, va, vb0, vb1, 1, ); } \
    if (MODE < 2) { \
      unsigned va = aad[0] + nbo, vb0 = bad[0] + nbo, vb1 = bad[1] + nbo; \
      SUB(f1, f0, va, vb0, vb1, 0, if (MODE == 0) { WAITL(0); BAR(); } else asm volatile("ds_write_b32 %0, %1" ::"v"(myFL), "v"(1u) : "memory"); SBAR()); \
    } else { asm volatile("ds_write_b32 %1, %2\n ds_read_b32 %0, %3\n s_waitcnt lgkmcnt(2)" : "=&v"(fl0) : "v"(myFL), "v"(2u), "v"(adFL) : "memory"); SBAR(); \
      MF(0, 2, f1); MF(1, 2, f1); MF(2, 2, f1); MF(3, 2, f1); MF(0, 3, f1); MF(1, 3, f1); MF(2, 3, f1); MF(3, 3, f1); \
      MF(0, 4, f1); MF(1, 4, f1); MF(2, 4, f1); MF(3, 4, f1); MF(0, 5, f1); MF(1, 5, f1); MF(2, 5, f1); MF(3, 5, f1); \
      MF(0, 0, f1); MF(1, 0, f1); MF(2, 0, f1); MF(3, 0, f1); MF(0, 1, f1); MF(1, 1, f1); MF(2, 1, f1); MF(3, 1, f1); } \
    buf = nbuf; } while (0)
  u2 xv[8];
#pragma unroll
  for (int t = 0; t < 8; ++t) asm volatile("" : "=v"(xv[t]));
  const int sx = (nb - rot) & 15;
  const unsigned xad = ldsb + (w * 32 + (l & 31)) * 128 + ((r15 >> 1) & 7) * 16 + (l >> 5) * 8;
  STMP(1);
  WAITL(0);
  BAR();
  STMP(2);
  WAITL(0);
  FQ f0, f1;
  { unsigned va = aad[0], vb0 = bad[0], vb1 = bad[1];
    RA(f0, 0, va); RB(f0, 0, vb0, vb1, 0, 0); RB(f0, 0, vb0, vb1, 0, 1); RA(f0, 1, va); RA(f0, 2, va); RA(f0, 3, va);
    RB(f0, 1, vb0, vb1, 0, 0); RB(f0, 1, vb0, vb1, 0, 1); RB(f0, 2, vb0, vb1, 0, 0); RB(f0, 2, vb0, vb1, 0, 1); RB(f0, 3, vb0, vb1, 0, 0); RB(f0, 3, vb0, vb1, 0, 1);
    RB(f0, 4, vb0, vb1, 0, 0); RB(f0, 4, vb0, vb1, 0, 1); RB(f0, 5, vb0, vb1, 0, 0); RB(f0, 5, vb0, vb1, 0, 1);
    WAITL(0); SBAR(); }
  int buf = 0; unsigned fl0;
#pragma unroll 1
  for (int kt = 0; kt < 14; ++kt) KSTEP(0);
  STMP(14); SBAR();
  { const int kt = 14; KSTEP(1); }
  STMP(7); SBAR();
  { const int kt = 15; KSTEP(2); }
  asm volatile("s_waitcnt lgkmcnt(0)" : "+v"(fl0) : : "memory");
  if (__builtin_expect(((unsigned)__ballot(fl0 < 1u) & 0xC0u) != 0u, 0)) lwait(adFL, 0xC0u, 1);
  STMP(3);
  __builtin_amdgcn_s_setprio(2);
  const LAS float* Lm = (const LAS float*)(lds + L_M) + wr * 64;
  const LAS float* Lr = (const LAS float*)(lds + L_RS) + wr * 64;
  const LAS float* Lc = (const LAS float*)(lds + L_CS) + wc * 96;
  f4 kv[4], zt[2];
  {
    float cs[4];
#pragma unroll
    for (int n = 0; n < 4; ++n) cs[n] = Lc[32 + 16 * n + r15];
    const u4 ones = (u4)(0x3F803F80u);
#pragma unroll
    for (int t = 0; t < 2; ++t) {
      u4 kf[2], vf[2];
#pragma unroll
      for (int mm = 0; mm < 2; ++mm) {
        const int m = 2 * t + mm;
        const f4 mv = *(const LAS f4*)(Lm + m * 16 + 4 * g4), rv = *(const LAS f4*)(Lr + m * 16 + 4 * g4);
#pragma unroll
        for (int a = 0; a < 2; ++a) {
          const f4 uk = acc[m][2 + a] - mv * cs[a];
          const f4 uv = (acc[m][4 + a] - mv * cs[2 + a]) * rv;
          kf[a][2 * mm] = phi2((f2){uk[0], uk[1]}, (f2){rv[0], rv[1]}); kf[a][2 * mm + 1] = phi2((f2){uk[2], uk[3]}, (f2){rv[2], rv[3]});
          vf[a][2 * mm] = pk2(uv[0], uv[1]); vf[a][2 * mm + 1] = pk2(uv[2], uv[3]);
        }
      }
#pragma unroll
      for (int tt = 0; tt < 4; ++tt) kv[tt] = MFMA16(S8(kf[tt & 1]), S8(vf[tt >> 1]), t ? kv[tt] : (f4)(0.f));
#pragma unroll
      for (int a = 0; a < 2; ++a) zt[a] = MFMA16(S8(kf[a]), S8(ones), t ? zt[a] : (f4)(0.f));
    }
  }
  const int hd = hp * 2 + wc;
  {
    LAS f4* ks = (LAS f4*)(lds + L_KVX + w * 3072) + l;
    const LAS f4* kr = (const LAS f4*)(lds + L_KVX + (w ^ 2) * 3072) + l;
    ks[0] = wr ? kv[0] : kv[2]; ks[64] = wr ? kv[1] : kv[3]; ks[128] = wr ? zt[0] : zt[1];
    lsig(myFL, 3);
    const f4 m0 = wr ? kv[2] : kv[0], m1 = wr ? kv[3] : kv[1], mz = wr ? zt[1] : zt[0];
    lwait(adFL, 1u << (w ^ 2), 3);
    const f4 s0 = m0 + kr[0], s1 = m1 + kr[64], sz = mz + kr[128];
    u4 o; o[0] = min(pk2(s0[0], s0[1]), POIS - 1u); o[1] = pk2(s0[2], s0[3]); o[2] = pk2(s1[0], s1[1]); o[3] = pk2(s1[2], s1[3]);
    ST128(o, WS_KVP + (hd * 16 + mb) * 2048 + wr * 1024 + l * 16);
    u4 zo = __builtin_bit_cast(u4, sz); zo[0] = min(zo[0], POIS - 1u);
    if (r15 == 0) ST128(zo, WS_ZP + (hd * 16 + mb) * 128 + g4 * 32 + wr * 16);
  }
  STMP(4);
  {
    const f4 c0 = *(const LAS f4*)(Lc + 4 * g4), c1 = *(const LAS f4*)(Lc + 16 + 4 * g4);
#pragma unroll
    for (int m = 0; m < 4; ++m) {
      const float mv = Lm[m * 16 + r15], rv = Lr[m * 16 + r15];
      const f4 u0 = acc[m][0] - c0 * mv, u1 = acc[m][1] - c1 * mv;
      u4 pf;
      pf[0] = phi2((f2){u0[0], u0[1]}, (f2)(rv)); pf[1] = phi2((f2){u0[2], u0[3]}, (f2)(rv));
      pf[2] = phi2((f2){u1[0], u1[1]}, (f2)(rv)); pf[3] = phi2((f2){u1[2], u1[3]}, (f2)(rv));
      *(LAS u4*)(lds + L_PQ + wr * 8192 + wc * 4096 + m * 1024 + l * 16) = pf;
    }
  }
  lsig(myFL, 4);
  __builtin_amdgcn_s_setprio(0);
  STMP(5);
  {
  int l;
  asm volatile("v_mbcnt_lo_u32_b32 %0, -1, 0\n v_mbcnt_hi_u32_b32 %0, -1, %0" : "=v"(l));
  __builtin_assume(l >= 0 && l < 64);
  const int r15 = l & 15, g4 = l >> 4, q = (l & 15) >> 2, p = l & 3;
  const int trow = mb * 128 + w * 32 + (l & 31);
  u2 oh;
  const unsigned flo = PAIRAT(r15) * 128;
  const char* fb = uptr(wsx + WS_OH + mb * 2048);
  int sel = -1;
  {
    const u4 s0 = *(const LAS u4*)(lds + L_SEL), s1 = *(const LAS u4*)(lds + L_SEL + 16);
#pragma unroll
    for (int i = 0; i < 8; ++i) { const int v = (int)(i < 4 ? s0[i & 3] : s1[i & 3]); sel = v == trow ? i : sel; }
  }
  const bool anysel = __ballot(sel >= 0) != 0ull;
  f16v oc[2][2];
  {
    const float m = *(const LAS float*)(lds + L_M + (w * 32 + (l & 31)) * 4), rs = *(const LAS float*)(lds + L_RS + (w * 32 + (l & 31)) * 4);
#pragma unroll
    for (int t = 0; t < 8; ++t) {
      const f4 xf = (f4){lo(xv[t][0]), hi(xv[t][0]), lo(xv[t][1]), hi(xv[t][1])};
      const f4 y = (xf - m) * rs + xf;
#pragma unroll
      for (int i = 0; i < 4; ++i) oc[0][t >> 2][(t & 3) * 4 + i] = y[i];
    }
    asm volatile("" : "+v"(oc[0][0]), "+v"(oc[0][1]));
  }
  unsigned wa[2];
#pragma unroll
  for (int t = 0; t < 2; ++t) wa[t] = ldsb + L_WO + wc * 4096 + (4 * (g4 >> 1) + q) * 128 + (((4 * t + 2 * (g4 & 1) + (p >> 1)) ^ ((q >> 1) << 2)) * 16) + (p & 1) * 8;
  u4 a[RD][4];
#pragma unroll
  for (int k = 0; k < RD; ++k) asm volatile("" : "=v"(a[k][0]), "=v"(a[k][1]), "=v"(a[k][2]), "=v"(a[k][3]));
  const char* ob = uptr(wsx + WS_OF + mb * 262144 + wc * 8192 + wr * 4096);
  const unsigned ov0 = l * 16 + wc * 2048, ov1 = ov0 ^ 2048;
  STMP(6);
  {
    unsigned vfw[4];
#pragma unroll
    for (int i = 0; i < 4; ++i) { vfw[i] = VFW(i); asm volatile("" : "+v"(vfw[i])); }
    lwait<2>(adFL, 0x3Fu, (l & 4) ? 3u : 4u);
    asm volatile("global_load_dwordx2 %0, %1, %2 sc1" : "=&v"(oh) : "v"(flo), "s"(fb) : "memory");
#pragma unroll
    for (int i = WE / 2; i < 16; i += 2) {
      const int pp = i + wr;
      const char* wp = (const char*)wo + (nb * 128 - 3072) + (2 * PAIRAT(pp) + wc) * 65536; const unsigned wm = ldsb + (2 * pp + wc) * 4096;
      GL4(vfw, wp, wm, L_WO);
    }
    WAITV(32 - WE);
    asm volatile("" : "+v"(oh));
  }
  unsigned seen = (unsigned)__ballot(oh[0] == tag), go = 0;
  auto need = [&](unsigned mask) {
    if (__builtin_expect((go & mask) != mask, 0)) {
      RETRY2();
      _Pragma("unroll 1") for (unsigned it = 0; it < SPIN; ++it) {
        if ((seen & mask) != mask) {
          asm volatile("global_load_dwordx2 %0, %1, %2 sc1\n s_waitcnt vmcnt(0)" : "+v"(oh) : "v"(flo), "s"(fb) : "memory");
          seen = (unsigned)__ballot(oh[0] == tag);
        }
        go = seen & (unsigned)__ballot(oh[1] - NOW() - 1u >= 1000u);
        if ((go & mask) == mask) break;
        SLEEP(1);
      }
    }
  };
#define OX 0
#define OL1(pos, i) do { if (OX == 2 && (pos) >= RD) break; \
    asm volatile("global_load_dwordx4 %0, %1, %2 offset:%3 sc1" : "+v"(a[(pos) % RD][i]) : "v"((i) & 1 ? ov1 : ov0), "s"(ob + (OX == 1 ? ((pos) & 1) : PAIRAT(pos)) * 16384), "i"(((i) >> 1) * 1024) : "memory"); } while (0)
#define OLD(pos) do { OL1(pos, 0); OL1(pos, 1); OL1(pos, 2); OL1(pos, 3); } while (0)
  auto omax = [&](u4(&v)[4]) {
    asm volatile("" : "+v"(v[0]), "+v"(v[1]), "+v"(v[2]), "+v"(v[3]));
    return max(umax3(v[0][0], v[1][0], v[2][0]), v[3][0]);
  };
#define OFIX(pos, m) do { \
    if (__builtin_expect(__ballot((m) == POIS) != 0ull, 0)) { RETRY(); _Pragma("unroll 1") for (unsigned it = 0; it < SPIN; ++it) { SLEEP(4); OLD(pos); WAITV(0); if (__ballot(omax(a[(pos) % RD]) == POIS) == 0ull) break; } } } while (0)
#define OS1(i)
#define OS2(i)
  lwait<1>(adFL, 0x30u, 4);
  __builtin_amdgcn_s_setprio(2);
  go = seen & (unsigned)__ballot(oh[1] - NOW() - 1u >= 1000u);
  need(1u);
  STMP(15);
  u2 oh2 = oh;
  if (__builtin_expect(seen != ~0u, 0)) asm volatile("global_load_dwordx2 %0, %1, %2 sc1" : "+v"(oh2) : "v"(flo), "s"(fb) : "memory");
  OS1(0); OLD(0);
#pragma unroll
  for (int i = 1; i < RD; ++i) { need(1u << i); OS1(i); OLD(i); }
  STMP(10);
  s4 b[8];
#define ORD(pos, f) do { if ((f) < 2 && (pos) == 8) { wa[(f) & 1] += 65536; asm volatile("" : "+v"(wa[(f) & 1])); } \
    RDTR(b[2 * (f)], wa[(f) & 1], ((pos) & 7) * 8192 + ((f) >> 1) * 1024); RDTR(b[2 * (f) + 1], wa[(f) & 1], ((pos) & 7) * 8192 + ((f) >> 1) * 1024 + 2048); } while (0)
#define OMF(pos, f, r) do { if ((f) < 2 && (pos) == 0 && (r) == 1) asm volatile("v_mfma_f32_32x32x16_bf16 %0, %1, %2, 0" : "=&v"(oc[r][(f) & 1]) \
      : "v"(CAT(b[2 * (f)], b[2 * (f) + 1])), "v"(a[(pos) % RD][((f) & 2) + (r)])); \
    else asm volatile("v_mfma_f32_32x32x16_bf16 %0, %1, %2, %0" : "+v"(oc[r][(f) & 1]) \
      : "v"(CAT(b[2 * (f)], b[2 * (f) + 1])), "v"(a[(pos) % RD][((f) & 2) + (r)])); \
    SBAR(); if ((r) == 1 && (pos) < 15) ORD(((pos) + 1) & 15, f); SBAR(); } while (0)
#define ONX(pos) ((pos) > 0 && (pos) + RD - 1 < 16)
#define ONL(pos, i) do { if (ONX(pos)) OL1(((pos) + RD - 1) & 15, i); SBAR(); } while (0)
#define OWL(pos, f) do { WAITL((pos) < 15 ? 6 : 6 - 2 * (f)); SBAR(); } while (0)
#define OHEAD(pos) do { unsigned m_ = 0; \
    if ((pos) == WE / 2 - 1 && WE < 32) { asm volatile("" : "+v"(fl3)); if (__builtin_expect(((unsigned)__ballot(fl3 < 5u) >> (w ^ 2)) & 1u, 0)) lwait(adFL, 1u << (w ^ 2), 5); SBAR(); } \
    OWL(pos, 0); OMF(pos, 0, 0); if (ONX(pos)) { need(1u << (((pos) + RD - 1) & 15)); OS1(((pos) + RD - 1) & 15); } SBAR(); \
    OMF(pos, 0, 1); ONL(pos, 0); \
    OWL(pos, 1); OMF(pos, 1, 0); ONL(pos, 1); \
    OMF(pos, 1, 1); ONL(pos, 2); \
    OWL(pos, 2); OMF(pos, 2, 0); ONL(pos, 3); \
    if ((pos) == 0 || (pos) == 4) { asm volatile("ds_write_b32 %0, %1" ::"v"(myFL), "v"((pos) ? 6u : 5u) : "memory"); SBAR(); } \
    if ((pos) == WE / 2 - 3) { asm volatile("ds_read_b32 %0, %1" : "=v"(fl3) : "v"(adFL) : "memory"); SBAR(); } \
    if ((pos) == 13) { asm volatile("ds_read_b32 %0, %1" : "=v"(fl3) : "v"(adFL) : "memory"); SBAR(); } \
    OMF(pos, 2, 1); \
    OWL(pos, 3); OMF(pos, 3, 0); \
    if ((pos) < 15) { WAITV(4 * ((pos) + RD - 1 < 16 ? RD - 2 : 14 - (pos))); m_ = omax(a[((pos) + 1) % RD]); asm volatile("" : "+v"(m_)); SBAR(); } \
    OMF(pos, 3, 1); \
    if ((pos) < 15) { OFIX(((pos) + 1) & 15, m_); OS2(((pos) + 1) & 15); } SBAR(); } while (0)
  unsigned fl3;
  ORD(0, 0); ORD(0, 1); ORD(0, 2); ORD(0, 3);
  { WAITV(4 * (RD - 1)); const unsigned m_ = omax(a[0]); OFIX(0, m_); OS2(0);
    asm volatile("" : "+v"(oh2));
    if (oh2[0] == tag) oh = oh2;
    seen = (unsigned)__ballot(oh[0] == tag);
    go = seen & (unsigned)__ballot(oh[1] - NOW() - max(r15 - (RD - 1), 0) * ADV - 1u >= 1000u);
    SBAR(); }
  OHEAD(0); OHEAD(1); STMP(11); OHEAD(2); OHEAD(3); OHEAD(4); OHEAD(5); OHEAD(6); OHEAD(7); STMP(12);
  OHEAD(8); OHEAD(9); STMP(13); OHEAD(10); OHEAD(11); OHEAD(12); OHEAD(13); OHEAD(14);
  f4 xi[8];
  {
    asm volatile("" : "+v"(fl3));
    if (__builtin_expect(((unsigned)__ballot(fl3 < 6u) >> (w ^ 2)) & 1u, 0)) lwait(adFL, 1u << (w ^ 2), 6);
    if (__builtin_expect(((unsigned)__ballot(fl3 < 4u) & 0x30u) != 0u, 0)) lwait(adFL, 0x30u, 4);
    const unsigned xw = ldsb + L_WO + (4 * wr + wc) * 4096 + l * 16, xr = xw ^ 4096;
#define XW(t) asm volatile("ds_write_b128 %0, %1 offset:%2" ::"v"(xw), "v"(__builtin_shufflevector(oc[1][(t) >> 2], oc[1][(t) >> 2], ((t) & 3) * 4, ((t) & 3) * 4 + 1, ((t) & 3) * 4 + 2, ((t) & 3) * 4 + 3)), \
      "i"(((t) >> 2) * 8192 + ((t) & 3) * 1024) : "memory")
    SBAR();
    OWL(15, 0); OMF(15, 0, 1); OWL(15, 1); OMF(15, 1, 1); OWL(15, 2); OMF(15, 2, 1); OWL(15, 3); OMF(15, 3, 1);
    OMF(15, 0, 0); OMF(15, 1, 0); XW(0); XW(1); XW(2); XW(3); SBAR();
    OMF(15, 2, 0); XW(4); XW(5); XW(6); XW(7); SBAR();
    OMF(15, 3, 0);
    asm volatile("ds_write_b32 %0, %1" ::"v"(myFL), "v"(7u) : "memory");
    _Pragma("unroll 1") for (unsigned it = 0; it < SPIN; ++it) {
      unsigned v;
      asm volatile("ds_read_b32 %0, %1" : "=v"(v) : "v"(adFL) : "memory");
#pragma unroll
      for (int t = 0; t < 8; ++t) RD128(xi[t], xr, (t >> 2) * 8192 + (t & 3) * 1024);
      WAITL(0);
      asm volatile("" : "+v"(v), "+v"(xi[0]), "+v"(xi[1]), "+v"(xi[2]), "+v"(xi[3]), "+v"(xi[4]), "+v"(xi[5]), "+v"(xi[6]), "+v"(xi[7]));
      if ((((unsigned)__ballot(v < 7u) >> (w ^ 1)) & 1u) == 0u) break;
    }
    asm volatile("" : "+v"(oc[0][0]), "+v"(oc[0][1]));
#pragma unroll
    for (int t = 0; t < 8; ++t)
#pragma unroll
      for (int i = 0; i < 4; ++i) xi[t][i] += oc[0][t >> 2][(t & 3) * 4 + i];
  }
  STMP(8);
  if (anysel) {
    lwait(adFL, 0xC0u, 3);
    if (sel >= 0) {
#pragma unroll
      for (int t = 0; t < 8; ++t) xi[t] += *(const LAS f4*)(lds + L_KVX + sel * 256 + (8 * t + 4 * (l >> 5)) * 4);
    }
  }
  const __amdgpu_buffer_rsrc_t outr = __builtin_amdgcn_make_buffer_rsrc(out, 0, 2048 * D * 2, 0x00020000);
  if (FULL) {
    LAS char* tb = (LAS char*)lds + L_PQ + w * 4096;
#pragma unroll
    for (int t = 0; t < 8; ++t) *(LAS u2*)(tb + (l & 31) * 128 + ((t ^ (l & 7)) * 16) + (l >> 5) * 8) = (u2){pk2(xi[t][0], xi[t][1]), pk2(xi[t][2], xi[t][3])};
#pragma unroll
    for (int i = 0; i < 4; ++i) {
      const u4 v = *(const LAS u4*)(tb + i * 1024 + (l >> 3) * 128 + (((l ^ (l >> 3)) & 7) * 16));
      __builtin_amdgcn_raw_buffer_store_b128(v, outr, (mb * 128 + w * 32 + i * 8 + (l >> 3)) * 2048 + nb * 128 + (l & 7) * 16, 0, 16);
    }
  } else {
#pragma unroll
  for (int hh = 0; hh < 4; ++hh) {
    const f4 ta = xi[2 * hh], tb = xi[2 * hh + 1];
    auto r0 = __builtin_amdgcn_permlane32_swap(pk2(ta[0], ta[1]), pk2(tb[0], tb[1]), false, false);
    auto r1 = __builtin_amdgcn_permlane32_swap(pk2(ta[2], ta[3]), pk2(tb[2], tb[3]), false, false);
    __builtin_amdgcn_raw_buffer_store_b128(((u4){r0[0], r1[0], r0[1], r1[1]}), outr, (trow * D + nb * 64 + 16 * hh + 8 * (l >> 5)) * 2, 0, 16);
  }
  }
  if (g == 0 && w == 0 && l == 0) __builtin_amdgcn_raw_buffer_store_b32(tag, wse, WS_E, 0, 16);
  STMP(9);
  STFLUSH();
  asm volatile(".Lend_c:");
  }
  return;
  }
  {
    unsigned vb[4], vfw[4];
    STMP(6); SBAR();
#pragma unroll
    for (int i = 0; i < 4; ++i) vb[i] = (8 * i + (l >> 3)) * 6144 + (((l & 7) ^ ((((l >> 4) & 1) << 2) | ((i & 1) << 1))) * 16) + (3 - i) * 1024;
    const unsigned lb0 = lw * 196608, lb1 = 196608 - lw * 194560, mbb = ldsb + 16384 + lw * 12288;
    const char* Ab = kx + ao;
    const char* Bb = kq + (hp * 128 + lw * 2048 - 3072);
    auto issue = [&](int s, int u0, int u1) {
      const int kt = (s + rot) & 15;
      const char* sa = Ab + kt * 128; const char* sb = Bb + kt * (64 * 6144);
      const unsigned so = (s & 3) * STG, ma = mba + so, mq = mbb + so;
#pragma unroll
      for (int u = u0; u < u1; ++u) { if (u < 2) GL4(va, sa + u * 65536, ma, u * 4096); else GL4(vb, sb + (u == 2 ? lb0 : u == 3 ? lb1 : lb0 + 2048), mq, (u - 2) * 4096); }
    };
    int tk = 0;
#define LBAR() do { BST(tk + 1); BAR(); BST(tk + 21); ++tk; } while (0)
    issue(0, 2, 3); issue(0, 1, 2); issue(0, 3, 5); SBAR(); STMP(8); SBAR(); issue(1, 0, 5); issue(2, 0, U0);
    BST(60);
    WAITV(20 + 4 * U0); BST(0); BAR(); BST(20);
    issue(2, U0, 5);
    GETTAG(tag);
    MKWS(tag);
    const unsigned ck0 = (unsigned)__builtin_amdgcn_s_memtime(), rt0 = NOW();
    int s0 = 3;
    if (U3 < 5) { issue(3, 0, U3); BST(tk + 40); WAITV(20 + 4 * U3); LBAR(); issue(3, U3, 5); s0 = 4; }
#pragma unroll 1
    for (; s0 < (sp ? 16 : 6 + ph); ++s0) { issue(s0, 0, 5); BST(tk + 40); WAITV(40); LBAR(); }
#pragma unroll 1
    for (; s0 < 16; s0 += 5) {
      WAITV(20); LBAR();
      WAITV(0); LBAR();
      issue(s0, 0, 5); issue(s0 + 1, 0, U1); BST(tk + 40); WAITV(4 * U1); LBAR();
      issue(s0 + 1, U1, 5); issue(s0 + 2, 0, 5);
      if (s0 + 3 >= 16) break;
      issue(s0 + 3, 0, U2); BST(tk + 40); WAITV(20 + 4 * U2); LBAR();
      issue(s0 + 3, U2, 5);
      if (s0 + 4 >= 16) break;
      issue(s0 + 4, 0, 5); BST(tk + 40); WAITV(40); LBAR();
    }
    int wq = 0;
#pragma unroll
    for (int i = 0; i < 4; ++i) vfw[i] = VFW(i);
    auto wo_upto = [&](int lim) {
      _Pragma("unroll 1") for (; wq < lim; ++wq) { const char* wp = (const char*)wo + (nb * 128 - 3072) + HEADAT(wq) * 65536; const unsigned wm = ldsb + wq * 4096; GL4(vfw, wp, wm, L_WO); }
    };
    asm volatile("ds_write_b32 %0, %1" ::"v"(adFL), "v"(0u) : "memory");
    unsigned kd = sp ? KS : KD, od = OD;
    if (CF) {
      const unsigned dc = (unsigned)__builtin_amdgcn_s_memtime() - ck0, dr = NOW() - rt0;
      const float sc = fminf((float)dr * (float)CF * __builtin_amdgcn_rcpf((float)dc), 2.f);
      kd = KL + (unsigned)((float)(kd - KL) * sc); od = OL + (unsigned)((float)(OD - OL) * sc);
      asm volatile("" : "+v"(kd), "+v"(od));
    }
    WAITV(0); WAITL(0); LBAR();
    const int hd = hp * 2 + lw;
    u4 dg;
    if (lw) {
      const u4 pz = (u4)(POIS);
      ST64(((u2){tag, NOW() + kd}), l == 0 ? WS_HK + hp * 512 + (mb >> 2) * 128 + (mb & 3) * 8 : WS_SZ);
#pragma unroll
      for (int i = 0; i < 16; ++i) ST128(pz, WS_OF + (mb * 32 + hp * 2) * 8192 + i * 1024 + l * 16);
      BST(61);
      const char* hb = uptr(wsx + WS_HK + hp * 512);
      lwait<4>(adFL, 0xCFu, (l & 4) ? 1u : 2u);
      if (HT) SLEEP(HT);
      STMP(1);
      unsigned t0, rq[4];
      _Pragma("unroll 1") for (unsigned it = 0; it < 64; ++it) {
        u8v h[4];
        asm volatile("s_load_dwordx8 %0, %4, 0 glc\n s_load_dwordx8 %1, %4, 128 glc\n s_load_dwordx8 %2, %4, 256 glc\n s_load_dwordx8 %3, %4, 384 glc\n s_waitcnt lgkmcnt(0)"
                     : "=&s"(h[0]), "=&s"(h[1]), "=&s"(h[2]), "=&s"(h[3]) : "s"(hb) : "memory");
        t0 = NOW() - 512u;
        BST(16);
        unsigned bad = 0;
#pragma unroll
        for (int i = 0; i < 16; ++i) {
          unsigned e = h[i >> 2][(i & 3) * 2] ^ tag;
          asm("" : "+s"(e));
          bad |= e;
          const unsigned d = h[i >> 2][(i & 3) * 2 + 1] - t0, r = d < 1536u ? d : 0u; rq[i >> 2] = (i & 3) ? max(rq[i >> 2], r) : r;
        }
        if (bad == 0u) break;
        RETRY2();
        SLEEP(HKS);
      }
      const bool up = max(rq[0], rq[1]) > max(rq[2], rq[3]);
      const unsigned a0 = up ? rq[2] : rq[0], a1 = up ? rq[3] : rq[1], a2 = up ? rq[0] : rq[2], a3 = up ? rq[1] : rq[3];
      const bool od2 = a2 > a3;
      dg = (u4){((t0 + (od2 ? a1 : a0) + 3u) & ~3u) | (up ? 2u : 0u) | (od2 ? 1u : 0u), t0 + (od2 ? a0 : a1), t0 + (od2 ? a3 : a2), t0 + (od2 ? a2 : a3)};
      *(LAS u4*)(lds + L_FL + 32) = dg;
      lsig(myFL, 1);
      BST(15);
      WAITV(0);
      ST64(((u2){tag, t0 + max(max(a2, a3), 512u + OM) + od}), l == 0 ? WS_OH + (mb * 16 + hp) * 128 : WS_SZ);
    } else {
      if (WS0) SLEEP(WS0);
      wo_upto(WE < 12 ? WE : 12);
      BST(15);
      if (WE > 12) { lwait<LW1>(adFL, 0x0Fu, 1); wo_upto(WE < 22 ? WE : 22); }
      BST(16);
      if (WE > 22) { lwait<LW1>(adFL, 0x0Fu, 2); wo_upto(WE); }
      STMP(1);
      u4 v;
      _Pragma("unroll 1") for (unsigned it = 0; it < SPIN; ++it) {
        unsigned f;
        asm volatile("ds_read_b32 %0, %2 offset:20\n ds_read_b128 %1, %2 offset:32\n s_waitcnt lgkmcnt(0)" : "=&v"(f), "=&v"(v) : "v"(ldsb + L_FL) : "memory");
        if (__builtin_amdgcn_readfirstlane(f)) break;
        SLEEP(2);
      }
#pragma unroll
      for (int e = 0; e < 4; ++e) dg[e] = __builtin_amdgcn_readfirstlane(v[e]);
    }
    const int gx = dg[0] & 3u;
    __builtin_amdgcn_s_setprio(3);
    u4 sel[2];
#pragma unroll
    for (int e = 0; e < 2; ++e) {
      const unsigned v = (r15 >> 2) == g4 ? (((r15 & 3) == 2 * e ? 0x3F80u : 0u) | ((r15 & 3) == 2 * e + 1 ? 0x3F800000u : 0u)) : 0u;
      sel[0][e] = v; sel[0][2 + e] = 0u; sel[1][e] = 0u; sel[1][2 + e] = v;
    }
    u4 pv[16][2], zv0, zv1, pq[8];
    const char* kvb = uptr(wsx + WS_KVP + (size_t)hd * 32768);
    const char* zb = uptr(wsx + WS_ZP + (size_t)hd * 2048);
    const unsigned lv16 = l * 16, zoff = r15 * 128 + g4 * 32;
    const unsigned pa = ldsb + L_PQ + lw * 4096 + l * 16, omask = lw ? 0x0Au : 0x25u;
    STMP(2);
#define GLD(gq) do { _Pragma("unroll") for (int i = 0; i < 4; ++i) { const char* sb = kvb + (((gq) ^ gx) * 4 + (i & 2)) * 2048; \
      asm volatile("global_load_dwordx4 %0, %1, %2 offset:%3 sc1" : "=v"(pv[(gq) * 4 + i][0]) : "v"(lv16), "s"(sb), "i"((i & 1) * 2048) : "memory"); \
      asm volatile("global_load_dwordx4 %0, %1, %2 offset:%3 sc1" : "=v"(pv[(gq) * 4 + i][1]) : "v"(lv16), "s"(sb), "i"((i & 1) * 2048 + 1024) : "memory"); } } while (0)
#define ZLD() do { asm volatile("global_load_dwordx4 %0, %1, %2 sc1" : "=v"(zv0) : "v"(zoff), "s"(zb) : "memory"); \
      asm volatile("global_load_dwordx4 %0, %1, %2 offset:16 sc1" : "=v"(zv1) : "v"(zoff), "s"(zb) : "memory"); } while (0)
#pragma unroll
    for (int gq = 0; gq < 4; ++gq) {
      _Pragma("unroll 1") for (unsigned it = 0; it < SPIN; ++it) if ((int)(NOW() - dg[gq]) >= 0) break;
      if (gq == 3) ZLD();
      GLD(gq);
    }
    lsig(myFL, 3);
    STMP(5);
    lwait(adFL, omask, (l & 4) ? 3u : 4u);
#pragma unroll
    for (int i = 0; i < 8; ++i) RD128(pq[i], pa, (i >> 2) * 8192 + (i & 3) * 1024);
#pragma unroll
    for (int gq = 0; gq < 4; ++gq) {
      SBAR(); WAITV(gq == 0 ? 26 : gq == 1 ? 18 : gq == 2 ? 10 : 0);
      _Pragma("unroll 1") for (unsigned it = 0; it < SPIN; ++it) {
#pragma unroll
        for (int i = 0; i < 4; ++i) asm volatile("" : "+v"(pv[gq * 4 + i][0]), "+v"(pv[gq * 4 + i][1]));
        unsigned m = umax3(pv[gq * 4][0][0], pv[gq * 4][1][0], pv[gq * 4 + 1][0][0]);
        m = umax3(m, pv[gq * 4 + 1][1][0], pv[gq * 4 + 2][0][0]);
        m = umax3(m, pv[gq * 4 + 2][1][0], pv[gq * 4 + 3][0][0]);
        m = max(m, pv[gq * 4 + 3][1][0]);
        if (gq == 3) { asm volatile("" : "+v"(zv0), "+v"(zv1)); m = umax3(m, zv0[0], zv1[0]); }
        if (__ballot(m == POIS) == 0ull) break;
        RETRY();
        SLEEP(2);
        if (gq == 3) ZLD();
        GLD(gq);
        WAITV(0);
      }
      f4 ga[4];
#pragma unroll
      for (int i = 0; i < 4; ++i)
#pragma unroll
        for (int t = 0; t < 4; ++t) ga[t] = MFMA16(S8(sel[t & 1]), S8(pv[gq * 4 + i][t >> 1]), i ? ga[t] : (f4)(0.f));
#pragma unroll
      for (int t = 0; t < 4; ++t) pv[gq * 4 + (t >> 1)][t & 1] = __builtin_bit_cast(u4, ga[t]);
    }
    STMP(3);
    WAITL(0);
#pragma unroll
    for (int i = 0; i < 8; i += 2) asm volatile("" : "+v"(pq[i]), "+v"(pq[i + 1]));
    u4 kA[2], zA;
    {
      float z[8];
#pragma unroll
      for (int e = 0; e < 4; ++e) { z[e] = __uint_as_float(zv0[e]); z[4 + e] = __uint_as_float(zv1[e]); }
#define ZR(n) "v_add_f32_dpp %0, %0, %0 row_ror:" #n " row_mask:0xf bank_mask:0xf\n v_add_f32_dpp %1, %1, %1 row_ror:" #n " row_mask:0xf bank_mask:0xf\n" \
              "v_add_f32_dpp %2, %2, %2 row_ror:" #n " row_mask:0xf bank_mask:0xf\n v_add_f32_dpp %3, %3, %3 row_ror:" #n " row_mask:0xf bank_mask:0xf\n" \
              "v_add_f32_dpp %4, %4, %4 row_ror:" #n " row_mask:0xf bank_mask:0xf\n v_add_f32_dpp %5, %5, %5 row_ror:" #n " row_mask:0xf bank_mask:0xf\n" \
              "v_add_f32_dpp %6, %6, %6 row_ror:" #n " row_mask:0xf bank_mask:0xf\n v_add_f32_dpp %7, %7, %7 row_ror:" #n " row_mask:0xf bank_mask:0xf\n"
      asm volatile("s_nop 1\n" ZR(8) ZR(4) ZR(2) ZR(1) : "+v"(z[0]), "+v"(z[1]), "+v"(z[2]), "+v"(z[3]), "+v"(z[4]), "+v"(z[5]), "+v"(z[6]), "+v"(z[7]));
#pragma unroll
      for (int e = 0; e < 4; ++e) zA[e] = pk2(z[2 * e], z[2 * e + 1]);
    }
    float inv[8];
#pragma unroll
    for (int i = 0; i < 8; ++i) { f4 dn = MFMA16(S8(zA), S8(pq[i]), (f4)(0.f)); inv[i] = dn[0]; }
#pragma unroll
    for (int t = 0; t < 4; ++t) {
#define PF(gq) __builtin_bit_cast(f4, pv[(gq) * 4 + (t >> 1)][t & 1])
      f4 s = (PF(0) + PF(1)) + (PF(2) + PF(3));
      kA[t >> 1][(t & 1) * 2] = pk2(s[0], s[1]); kA[t >> 1][(t & 1) * 2 + 1] = pk2(s[2], s[3]);
    }
    f4 o0[8], o1[8];
#pragma unroll
    for (int i = 0; i < 8; ++i) {
      o0[i] = MFMA16(S8(kA[0]), S8(pq[i]), (f4)(0.f)); o1[i] = MFMA16(S8(kA[1]), S8(pq[i]), (f4)(0.f));
      inv[i] = __builtin_amdgcn_rcpf(inv[i] + 1e-6f);
    }
#pragma unroll
    for (int i = 0; i < 8; ++i) {
      o0[i] *= inv[i]; o1[i] *= inv[i];
      u4 o; o[0] = min(pk2(o0[i][0], o0[i][1]), POIS - 1u); o[1] = pk2(o0[i][2], o0[i][3]); o[2] = pk2(o1[i][0], o1[i][1]); o[3] = pk2(o1[i][2], o1[i][3]);
      ST128(o, WS_OF + (mb * 32 + hd) * 8192 + (i >> 1) * 2048 + (i & 1) * 256 + g4 * 512 + r15 * 16);
    }
    lsig(myFL, 4);
    STMP(4);
    STFLUSH();
    asm volatile(".Lend_l:");
  }

}

extern "C" void kernel_launch(void* const* d_in, const int* in_sizes, int n_in, void* d_out, int out_size,
                              void* d_ws, size_t ws_size, hipStream_t stream) {
  (void)hipFuncSetAttribute((const void*)fused, hipFuncAttributeMaxDynamicSharedMemorySize, LDS_SZ);
  fused<<<256, 512, LDS_SZ, stream>>>((const u16*)d_in[0], (const u16*)d_in[1], (const u16*)d_in[4], (char*)d_ws, (const u16*)d_in[2],
                                      (const u16*)d_in[5], (const u16*)d_in[6], (const u16*)d_in[7], (u16*)d_out);
}
```

```cpp
#include <hip/hip_runtime.h>
#include <stdint.h>

typedef unsigned short u16;
typedef short s4 __attribute__((ext_vector_type(4)));
typedef short s8 __attribute__((ext_vector_type(8)));
typedef float f4 __attribute__((ext_vector_type(4)));
typedef float f16v __attribute__((ext_vector_type(16)));
typedef float f2 __attribute__((ext_vector_type(2)));
typedef unsigned u4 __attribute__((ext_vector_type(4)));
typedef unsigned u2 __attribute__((ext_vector_type(2)));
typedef unsigned u8v __attribute__((ext_vector_type(8)));
typedef __bf16 bf2 __attribute__((ext_vector_type(2)));
#define LAS __attribute__((address_space(3)))
#define DEV __device__ __forceinline__

constexpr int D = 1024, F = 3584;
#define NOMOE 0
#define KD 225
#define KS 219
#define OD 185
#define OHP 1
#define FS 128
#define GSL 8
#define YSL 16
#define TP1 0
#define TP2 3
#define TLBW 0
#define FULL 1
#define HPRIO 1
#define OWN 1
#define NOPOIS 0
#define CAP 7
#define RD 4
#define DYN 1
#define JX 0
#define NOSKIP 0
#define GRP 8
#define ADV 15
#define HB 1
#define RT 1
#define WD 0
#define WU1 12
#define WU2 22
#define PL 0
#define PO 0
#define WG 0
#define DS 0
#define D1 16
#define D2 2
#define HKS 2
#define LW1 1
#define HW1 2
#define HS0 0
#define GPV 2
#define XO 1
#define TP0 3
#define G1 0x31
#define G1S 4
#define G2S 8
#define TD 1
#define YD 170
#define WU0 4
#define WE 12
#define HT 115
#define CF 19
#define KL 145
#define OL 115
#define CL 0
#define WS0 4
#define OM 0
#define HG 128
#define U0 0
#define U3 5
#define TL 0
#define MH 0
#define HWT 3
#define YG 0
#define GG 1
#define PV 1
#define U1 4
#define U2 2
#define QD 1
#define LP0 3
#define LP1 3
#define WA 8
#define PW 3
#define WB 24
#define ROT 0

constexpr unsigned WS_E = 0;
constexpr unsigned WS_YF = 524288;
constexpr unsigned WS_CAND = 12288;
constexpr unsigned WS_RG = 49152;
constexpr unsigned WS_ST = 65536;
constexpr unsigned WS_HK = 589824;
constexpr unsigned WS_ZP = 131072;
constexpr unsigned WS_CS = 196608;
constexpr unsigned WS_OH = 458752;
constexpr unsigned WS_KVP = 1u << 20;
constexpr unsigned WS_GP = 2u << 20;
constexpr unsigned WS_YP = 3u << 20;
constexpr unsigned WS_OF = 5u << 20;
constexpr unsigned WS_SZ = 9u << 20;
#define NR 4
#define RGO(tag) ((64u << 20) + ((tag) & (NR - 1u)) * (10u << 20))
#define MKWS(tag) char* const wsx = ws + RGO(tag); const __amdgpu_buffer_rsrc_t wsr = __builtin_amdgcn_make_buffer_rsrc(wsx, 0, WS_SZ, 0x00020000)

constexpr int STG = 40960;
constexpr int L_KVX = 0;
constexpr int L_PQ = 12288;
constexpr int L_M = 28672, L_RS = L_M + 512, L_CS = L_RS + 512, L_SEL = L_CS + 768, L_FL = L_SEL + 64;
constexpr int L_HX = 30720, L_RL = 31744, L_WO = 32768;
constexpr int LDS_SZ = 163840;
constexpr unsigned POIS = 0xFFFFFFFFu;

DEV unsigned pk2(float a, float b) { bf2 v; v[0] = (__bf16)a; v[1] = (__bf16)b; return __builtin_bit_cast(unsigned, v); }
DEV float lo(unsigned u) { return __uint_as_float(u << 16); }
DEV float hi(unsigned u) { return __uint_as_float(u & 0xFFFF0000u); }
DEV float rbf(float a) { return lo(pk2(a, a)); }
DEV float dot2(unsigned a, unsigned b, float c) { return __builtin_amdgcn_fdot2_f32_bf16(__builtin_bit_cast(bf2, a), __builtin_bit_cast(bf2, b), c, false); }
#define RD128(dst, addr, OFF) asm volatile("ds_read_b128 %0, %1 offset:%2" : "=v"(dst) : "v"(addr), "i"(OFF) : "memory")
#define RDTR(dst, addr, OFF) asm volatile("ds_read_b64_tr_b16 %0, %1 offset:%2" : "=v"(dst) : "v"(addr), "i"(OFF) : "memory")
#define SBAR() __builtin_amdgcn_sched_barrier(0)
#define CAT(x, y) __builtin_shufflevector(x, y, 0, 1, 2, 3, 4, 5, 6, 7)
#define WAITV(n) asm volatile("s_waitcnt vmcnt(%0)" ::"i"(n) : "memory")
#define WAITL(n) asm volatile("s_waitcnt lgkmcnt(%0)" ::"i"(n) : "memory")
#define MEMB() asm volatile("" ::: "memory")
#define SLEEP(n) asm volatile("s_sleep %0" ::"i"(n) : "memory")
#define BAR() asm volatile("s_barrier" ::: "memory")
#define GL0(vo, sb, m0v) asm volatile("s_mov_b32 m0, %2\n s_nop 0\n global_load_lds_dwordx4 %0, %1" ::"v"(vo), "s"(sb), "s"(m0v) : "memory")
#define GLI(vo, sb, OFF) asm volatile("global_load_lds_dwordx4 %0, %1 offset:%2" ::"v"(vo), "s"(sb), "i"(OFF) : "memory")
#define GL4(v, sb, mm, MO) do { asm volatile("s_add_i32 m0, %2, %3\n s_nop 0\n global_load_lds_dwordx4 %0, %1" ::"v"((v)[0]), "s"(sb), "s"(mm), "i"(MO) : "memory", "scc"); \
      GLI((v)[1], sb, 1024); GLI((v)[2], sb, 2048); GLI((v)[3], sb, 3072); } while (0)
#define VFW(i) ((8 * (i) + (l >> 3)) * 2048 + (((l & 7) ^ (((l >> 4) & 1) << 2)) * 16) + (3 - (i)) * 1024)
#define MFMA16(a, b, c) __builtin_amdgcn_mfma_f32_16x16x32_bf16(a, b, c, 0, 0, 0)
#define S8(x) __builtin_bit_cast(s8, x)
#define LD128(off) __builtin_amdgcn_raw_buffer_load_b128(wsr, (off), 0, 16)
#define LD64(off) __builtin_amdgcn_raw_buffer_load_b64(wsr, (off), 0, 16)
#define ST128(v, off) __builtin_amdgcn_raw_buffer_store_b128((v), wsr, (off), 0, 16)
#define ST64(v, off) __builtin_amdgcn_raw_buffer_store_b64((v), wsr, (off), 0, 16)
#define OFA(i) (WS_OF + ((mb * 32 + hd) * 8 + (i)) * 1024 + l * 16)
#define POISON(i) ST128(pz, (i) < 2 ? WS_KVP + (hd * 16 + mb) * 2048 + (i) * 1024 + l * 16 : (i) == 2 ? WS_ZP + (hd * 16 + mb) * 128 + (l & 7) * 16 : OFA((i) - 3))
#define NOW() ((unsigned)__builtin_amdgcn_s_memrealtime())
constexpr unsigned SPIN = 1u << 21;
DEV const char* uptr(const char* p) {
  unsigned long long v = (unsigned long long)p;
  unsigned lo_ = __builtin_amdgcn_readfirstlane((unsigned)v), hi_ = __builtin_amdgcn_readfirstlane((unsigned)(v >> 32));
  return (const char*)(((unsigned long long)hi_ << 32) | lo_);
}
DEV const char* padd(const char* p, unsigned o) {
  unsigned lo_, hi_;
  asm("s_add_u32 %0, %2, %4\n s_addc_u32 %1, %3, 0" : "=&s"(lo_), "=s"(hi_) : "s"((unsigned)(unsigned long long)p), "s"((unsigned)((unsigned long long)p >> 32)), "s"(o) : "scc");
  return (const char*)(((unsigned long long)hi_ << 32) | lo_);
}
DEV void lsig(unsigned addr, unsigned val) { asm volatile("s_waitcnt lgkmcnt(0)\n ds_write_b32 %0, %1" ::"v"(addr), "v"(val) : "memory"); }
template <int SL = 0> DEV void lwait(unsigned addr, unsigned mask, unsigned target) {
  _Pragma("unroll 1") for (unsigned it = 0; it < SPIN; ++it) {
    unsigned v;
    asm volatile("ds_read_b32 %0, %1\n s_waitcnt lgkmcnt(0)" : "=v"(v) : "v"(addr) : "memory");
    if (((unsigned)__ballot(v < target) & mask) == 0u) break;
    if (SL) SLEEP(SL);
  }
}
DEV unsigned nopois(unsigned a) { return min(a, POIS - 1u); }
DEV u4 tagf(f4 v, unsigned tag) {
  u4 r;
#pragma unroll
  for (int e = 0; e < 4; ++e) r[e] = __builtin_amdgcn_perm(__float_as_uint(v[e]), tag, 0x07060500u + e);
  return r;
}
DEV void untag(u4& v, unsigned tag, unsigned& bad) {
#pragma unroll
  for (int e = 0; e < 4; ++e) v[e] ^= (tag >> (8 * e)) & 255u;
  bad |= v[0] | v[1]; bad |= v[2] | v[3];
}
DEV unsigned umax3(unsigned a, unsigned b, unsigned c) { return max(max(a, b), c); }
template <int C> DEV float dppf(float v) { return __int_as_float(__builtin_amdgcn_update_dpp(0, __float_as_int(v), C, 0xf, 0xf, false)); }
DEV unsigned wave_max(unsigned v) {
  v = max(v, (unsigned)__builtin_amdgcn_update_dpp(0, (int)v, 0x111, 0xf, 0xf, false));
  v = max(v, (unsigned)__builtin_amdgcn_update_dpp(0, (int)v, 0x112, 0xf, 0xf, false));
  v = max(v, (unsigned)__builtin_amdgcn_update_dpp(0, (int)v, 0x114, 0xf, 0xf, false));
  v = max(v, (unsigned)__builtin_amdgcn_update_dpp(0, (int)v, 0x118, 0xf, 0xf, false));
  v = max(v, (unsigned)__builtin_amdgcn_update_dpp(0, (int)v, 0x142, 0xa, 0xf, false));
  v = max(v, (unsigned)__builtin_amdgcn_update_dpp(0, (int)v, 0x143, 0xc, 0xf, false));
  return (unsigned)__builtin_amdgcn_readlane((int)v, 63);
}
DEV unsigned phi2(f2 u, f2 rs, f2 rl) {
  f2 a = u * rl, p = u * rs + 1.f, r;
  r[0] = __builtin_amdgcn_fmed3f(__builtin_amdgcn_exp2f(a[0]), p[0], 1.f);
  r[1] = __builtin_amdgcn_fmed3f(__builtin_amdgcn_exp2f(a[1]), p[1], 1.f);
  r += 1e-6f;
  return pk2(r[0], r[1]);
}

#define STDECL
#undef HB
#define HB 0
#define BSTV cw
#define STMP(i)
#define BST(i)
#define STFLUSH()
#define RETRY()
#define RETRY2()

DEV float half_sum(float v) {
  asm volatile("s_nop 1\n v_add_f32_dpp %0, %0, %0 row_ror:8 row_mask:0xf bank_mask:0xf\n s_nop 1\n v_add_f32_dpp %0, %0, %0 row_ror:4 row_mask:0xf bank_mask:0xf\n"
               "s_nop 1\n v_add_f32_dpp %0, %0, %0 row_ror:2 row_mask:0xf bank_mask:0xf\n s_nop 1\n v_add_f32_dpp %0, %0, %0 row_ror:1 row_mask:0xf bank_mask:0xf\n s_nop 1" : "+v"(v));
  auto r = __builtin_amdgcn_permlane16_swap(__float_as_uint(v), __float_as_uint(v), false, false);
  return __uint_as_float(r[0]) + __uint_as_float(r[1]);
}

struct FQ { s8 a[4]; s4 b[12]; };
template <bool B> struct BC { static constexpr bool v = B; };
template <int I> struct IC { static constexpr int v = I; };

__global__ __launch_bounds__(512) void fused(const u16* __restrict__ x, const u16* __restrict__ wqkv, const u16* __restrict__ wrt,
                                             char* __restrict__ ws_, const u16* __restrict__ wo_, const u16* __restrict__ wg,
                                             const u16* __restrict__ wu, const u16* __restrict__ wd, u16* __restrict__ out) {
  extern __shared__ __attribute__((aligned(16))) char lds[];
  const char *kx, *kq, *kr, *wo; char* ws;
  asm volatile("s_load_dwordx2 %0, %5, 0\n s_load_dwordx2 %1, %5, 8\n s_load_dwordx2 %2, %5, 16\n s_load_dwordx2 %3, %5, 24\n s_load_dwordx2 %4, %5, 32"
               : "=&s"(kx), "=&s"(kq), "=&s"(kr), "=&s"(ws), "=&s"(wo) : "s"(__builtin_amdgcn_kernarg_segment_ptr()));
  SBAR();
  const int w = __builtin_amdgcn_readfirstlane(threadIdx.x >> 6), l = threadIdx.x & 63;
  STDECL;
  STMP(0);
  const int g = blockIdx.x, xcd = g & 7, j = g >> 3, ii = j >> 2;
  const int mb = (xcd & 1) * 8 + ii, hp = (xcd >> 1) * 4 + (j & 3);
  unsigned cw, cwo = (l * 32 + j) * 64; unsigned long long cwb;
  asm volatile("s_getpc_b64 %2\n.Lcode_here:\n v_min_u32 %1, max(.Lend_c - .Lcode_here, .Lend_l - .Lcode_here, .Lend_h - .Lcode_here, .Lend_s - .Lcode_here) - 4, %1\n"
               "s_cmp_lg_u32 %3, 7\n s_cbranch_scc1 .Lskip_w\n global_load_dword %0, %1, %2\n.Lskip_w:" : "=&v"(cw), "+v"(cwo), "=&s"(cwb) : "s"(w) : "memory", "scc");
  SBAR();
  const int lw = w & 1, rot = 2 * xcd + 4 * ((ii + JX * j) & 3);
  const unsigned ldsb = (unsigned)(size_t)(LAS char*)lds;
  unsigned va[4];
#pragma unroll
  for (int i = 0; i < 4; ++i) va[i] = (8 * i + (l >> 3)) * 2048 + (((l & 7) ^ ((i & 1) * 4 + (l >> 4))) * 16) + (3 - i) * 1024;
  const int ao = (mb * 128 + lw * 64) * 2048 - 3072;
  long long ao0 = ao + (rot & 15) * 128;
  unsigned mba = ldsb + lw * 8192;
  const int srow = mb * 128 + hp * 8 + 2 * w + (l >> 5);
  unsigned xo = srow * 2048 + (l & 31) * 16, ro = (l & 31) * 16;
  asm volatile("" : "+v"(va[0]), "+v"(va[1]), "+v"(va[2]), "+v"(va[3]), "+v"(xo), "+v"(ro));
  asm volatile("s_waitcnt lgkmcnt(0)" : "+s"(kx), "+s"(kq), "+s"(kr), "+s"(ws), "+s"(wo), "+s"(ao0), "+s"(mba));
  SBAR();
  asm volatile("s_sub_u32 m0, %6, 4\n s_cmp_gt_u32 m0, 1\n s_cbranch_scc1 .Lskip_l\n s_mov_b32 m0, %5\n s_nop 0\n global_load_lds_dwordx4 %0, %4\n global_load_lds_dwordx4 %1, %4 offset:1024\n"
               "global_load_lds_dwordx4 %2, %4 offset:2048\n global_load_lds_dwordx4 %3, %4 offset:3072\n.Lskip_l:"
               :: "v"(va[0]), "v"(va[1]), "v"(va[2]), "v"(va[3]), "s"(kx + ao0), "s"(mba), "s"(w) : "memory", "scc");
  u4 xa[4], rw[4]; unsigned ep;
  asm volatile("s_cmp_gt_u32 %14, 3\n s_cbranch_scc1 .Lskip_x\n"
               "global_load_dwordx4 %0, %9, %11\n global_load_dwordx4 %1, %9, %11 offset:512\n global_load_dwordx4 %2, %9, %11 offset:1024\n global_load_dwordx4 %3, %9, %11 offset:1536\n"
               "global_load_dwordx4 %4, %10, %12\n global_load_dwordx4 %5, %10, %12 offset:512\n global_load_dwordx4 %6, %10, %12 offset:1024\n global_load_dwordx4 %7, %10, %12 offset:1536\n"
               ".Lskip_x:\n s_load_dword %8, %13, 0"
               : "=&v"(xa[0]), "=&v"(xa[1]), "=&v"(xa[2]), "=&v"(xa[3]), "=&v"(rw[0]), "=&v"(rw[1]), "=&v"(rw[2]), "=&v"(rw[3]), "=&s"(ep)
               : "v"(xo), "v"(ro), "s"(kx), "s"(kr), "s"(ws), "s"(w) : "memory", "scc");
#define GETTAG(t) unsigned t; asm volatile("s_waitcnt lgkmcnt(0)\n s_add_u32 %0, %1, 1" : "=s"(t) : "s"(ep) : "memory", "scc")
  SBAR();
#define PTAB 0xA98FEDC1045B7623ull
#define PHT 0x6624924999924924ull
#define CX 4
  const bool sp = (hp & 7) == 3;
  const int nb = hp;
#define PAIRAT(p) ((int)(PTAB >> (4 * (p))) & 15)
#define HEADAT(s) (2 * PAIRAT((s) >> 1) + ((s) & 1))
  const int ph = (int)(PHT >> (2 * j)) & 3;
#define HQT 0xDCBA098765320410ull
#define CXA 1
#define CHUNK(b) ((b) < 14 ? 4 * (b) + 3 : ((b) - 14) + ((((b) - 14) * 43) >> 7))
  const unsigned adFL = ldsb + L_FL + 4 * (l & 7), myFL = ldsb + L_FL + 4 * w;
  const __amdgpu_buffer_rsrc_t wse = __builtin_amdgcn_make_buffer_rsrc(ws, 0, 64, 0x00020000);
  const int r15 = l & 15, g4 = l >> 4, q = (l & 15) >> 2, p = l & 3;

  if (w != 4 && w != 5) {
  if (w >= 6) {
    const int mw = w - 6;
    GETTAG(tag);
    const int bq = ((int)(HQT >> (4 * hp)) & 15) * 4 + (ii & 3), ksp = (xcd & 1) * 2 + (ii >> 2);
    const int chunk = CHUNK(bq), bg = (CXA || bq < 14) ? bq ^ (w & 1) : bq, chg = CHUNK(bg);
    auto helper = [&](auto SP) {
    constexpr bool spare = decltype(SP)::v, moe = !spare && !NOMOE;
    int nbar = 0;
#define TICK() do { if (HB) { BST(nbar); asm volatile("" : "+v"(BSTV)); } SBAR(); BAR(); SBAR(); ++nbar; } while (0)
#define DTICK(lim) do { if (nbar < (lim)) TICK(); else SLEEP(2); } while (0)
    const int hd = hp * 2 + mw;
    const u4 pz = (u4)(POIS);
    MKWS(tag);
    const int np = l & 7, kset = (l >> 3) & 1;
    u4 gw[4][8], dw[2][8];
    const __amdgpu_buffer_rsrc_t wgr = __builtin_amdgcn_make_buffer_rsrc((void*)(mw ? wu : wg), 0, D * F * 2, 0x00020000);
    const __amdgpu_buffer_rsrc_t wdr = __builtin_amdgcn_make_buffer_rsrc((void*)wd, 0, D * F * 2, 0x00020000);
    const __amdgpu_buffer_rsrc_t wor = __builtin_amdgcn_make_buffer_rsrc((void*)wo, 0, D * D * 2, 0x00020000);
    const unsigned gvo = ((256 * ksp + 32 * kset + 8 * g4) * F + 64 * chg + 8 * np) * 2;
    const unsigned dvo = ((64 * chunk + 8 * g4) * D + 256 * ksp + 128 * mw + 8 * r15) * 2;
    auto burst = [&](int k, int i0 = 0, int i1 = 16) {
      if (moe) {
#pragma unroll
        for (int i = i0; i < i1; ++i) {
          if (k < 2) gw[(2 * k + (i >> 3)) & 3][i & 7] = __builtin_amdgcn_raw_buffer_load_b128(wgr, gvo, (128 * k + 64 * (i >> 3) + (i & 7)) * F * 2, 2);
          else dw[i >> 3][i & 7] = __builtin_amdgcn_raw_buffer_load_b128(wdr, dvo, (32 * (i >> 3) + (i & 7)) * D * 2, 2);
        }
      }
    };
    auto transp = [&](u4(&v)[8]) {
      if (moe) {
        u4 f[8];
#pragma unroll
        for (int c = 0; c < 8; ++c)
#pragma unroll
          for (int e = 0; e < 4; ++e) {
            const unsigned ev = v[2 * e][c >> 1], od = v[2 * e + 1][c >> 1];
            f[c][e] = (c & 1) ? __builtin_amdgcn_perm(od, ev, 0x07060302u) : __builtin_amdgcn_perm(od, ev, 0x05040100u);
          }
#pragma unroll
        for (int c = 0; c < 8; ++c) { v[c] = f[c]; asm volatile("" : "+v"(v[c])); }
      }
    };
    auto pois = [&]() {
      ST128(pz, WS_KVP + (hd * 16 + mb) * 2048 + l * 16); ST128(pz, WS_KVP + (hd * 16 + mb) * 2048 + 1024 + l * 16); ST128(pz, WS_ZP + (hd * 16 + mb) * 128 + (l & 7) * 16);
    };
    unsigned wt0, wt1, wt2 = 0, wt3 = 0;
    auto touch = [&]() {
      const unsigned to = (j * 32 + mw * 16 + (l >> 3)) * 2048 + (xcd >> 1) * 512 + (l & 7) * 64;
      wt0 = __builtin_amdgcn_raw_buffer_load_b32(wor, to, 0, 0); wt1 = __builtin_amdgcn_raw_buffer_load_b32(wor, to, 16384, 0);
      if (TLBW) {
        const __amdgpu_buffer_rsrc_t outr = __builtin_amdgcn_make_buffer_rsrc(out, 0, 2048 * D * 2, 0x00020000);
        wt2 = __builtin_amdgcn_raw_buffer_load_b32(outr, (mb * 128 + mw * 64 + (l & 3) * 16) * 2048 + nb * 128, 0, 0);
        wt3 = __builtin_amdgcn_raw_buffer_load_b32(wsr, (l & 1) ? WS_GP + g * 4096 + mw * 2048 : WS_YP + (g >> 2) * 32768 + (l & 2) * 8192, 0, 0);
      }
    };
    s4 sb[24];
    auto colrd = [&](int h) {
      const unsigned bo = ldsb + (1 + h) * STG + 16384 + (8 * g4 + q) * 128 + (p & 1) * 8;
      const int sw = ((q >> 1) << 2) | ((g4 & 1) << 1);
      const unsigned b0 = bo + (((mw * 4 + (p >> 1)) ^ sw) * 16), b1 = bo + (((mw * 4 + 2 + (p >> 1)) ^ sw) * 16);
#pragma unroll
      for (int n = 0; n < 12; ++n) { RDTR(sb[2 * n], (n & 1) ? b1 : b0, ((n % 6) >> 1) * 8192 + (n / 6) * 4096); RDTR(sb[2 * n + 1], (n & 1) ? b1 : b0, ((n % 6) >> 1) * 8192 + (n / 6) * 4096 + 512); }
      WAITL(0);
      SBAR();
    };
    auto colsum = [&]() {
      const u4 ones = (u4)(0x3F803F80u);
      f4 c[6];
#pragma unroll
      for (int n = 0; n < 12; ++n) c[n % 6] = MFMA16(S8(ones), CAT(sb[2 * n], sb[2 * n + 1]), n >= 6 ? c[n % 6] : (f4)(0.f));
      const u4 csg = (u4){__float_as_uint(g4 ? c[3][0] : c[0][0]), __float_as_uint(g4 ? c[4][0] : c[1][0]), __float_as_uint(g4 ? c[5][0] : c[2][0]), tag};
      WAITV(0);
      asm volatile("" : "+v"(cw));
      ST128(csg, l < 32 ? WS_CS + (hd * 16 + mb) * 512 + l * 16 : WS_SZ);
    };
    u4 stv, cv[8], cd; float cs[3];
    auto gload = [&]() {
      stv = LD128(WS_ST + (mb * 128 + mw * 64 + l) * 16);
#pragma unroll
      for (int i = 0; i < 8; ++i) cv[i] = LD128(WS_CS + hd * 8192 + i * 1024 + l * 16);
    };
    auto gbad = [&]() {
      unsigned bad = (stv[1] ^ tag) | (stv[3] ^ tag);
#pragma unroll
      for (int i = 0; i < 8; ++i) bad |= cv[i][3] ^ tag;
      return __ballot(bad != 0u) != 0ull;
    };
    int idx;
    auto gfin = [&]() {
      if (gbad()) { RETRY(); _Pragma("unroll 1") for (unsigned it = 0; it < SPIN; ++it) { SLEEP(2); gload(); if (!gbad()) break; } }
#pragma unroll
      for (int e = 0; e < 3; ++e) {
        float a = __uint_as_float(cv[0][e]);
#pragma unroll
        for (int i = 1; i < 8; ++i) a += __uint_as_float(cv[i][e]);
        auto r = __builtin_amdgcn_permlane32_swap(__float_as_uint(a), __float_as_uint(a), false, false);
        cs[e] = __uint_as_float(r[0]) + __uint_as_float(r[1]);
      }
      asm volatile("" : "+v"(cs[0]), "+v"(cs[1]), "+v"(cs[2]) : "v"(wt0), "v"(wt1), "v"(wt2), "v"(wt3));
    };
    auto cload = [&]() { cd = LD128(WS_CAND + (((hp >> 2) == 2) * 16 + mb) * 128 + (l & 7) * 16); };
    auto cfin = [&]() {
      if (__builtin_expect(__ballot(cd[1] != tag || cd[3] != tag) != 0ull, 0)) {
        RETRY();
        _Pragma("unroll 1") for (unsigned it = 0; it < SPIN; ++it) { SLEEP(4); cload(); if (__ballot(cd[1] != tag || cd[3] != tag) == 0ull) break; }
      }
      cd[0] = max(cd[0], cd[2]);
      idx = (int)(cd[0] & 2047u);
    };
    auto lpub = [&]() {
      *(LAS float*)(lds + L_M + (mw * 64 + l) * 4) = __uint_as_float(stv[0]);
      *(LAS float*)(lds + L_RS + (mw * 64 + l) * 4) = __uint_as_float(stv[2]);
      *(LAS float*)(lds + L_RL + (mw * 64 + l) * 4) = __uint_as_float(stv[2]) * 1.44269504f;
      if (l < 32) {
#pragma unroll
        for (int e = 0; e < 3; ++e) *(LAS float*)(lds + L_CS + mw * 384 + ((3 * g4 + e) * 16 + r15) * 4) = -cs[e];
      }
      if (mw == 0 && l < 8) *(LAS int*)(lds + L_SEL + l * 4) = idx;
      lsig(myFL, 1u);
    };
    u4 xs[4]; float xrs;
    asm volatile("" ::"s"(wg), "s"(wu), "s"(wd), "v"(cw));
    if (HPRIO) __builtin_amdgcn_s_setprio(HPRIO);
    TICK();
    if (spare) {
      TICK();
      pois();
      if (ii >> 2) TICK();
      colrd(ii >> 2);
      if (!(ii >> 2)) TICK();
      u4 kk[8];
      auto kload = [&]() {
#pragma unroll
        for (int i = 0; i < 8; ++i) kk[i] = LD128(WS_RG + mw * 8192 + i * 1024 + l * 16);
      };
      kload();
      TICK();
      _Pragma("unroll 1") for (unsigned it = 0; it < SPIN; ++it) {
        unsigned bad = 0;
#pragma unroll
        for (int i = 0; i < 8; ++i) { bad |= (kk[i][1] ^ tag) | (kk[i][3] ^ tag); asm volatile("" : "+v"(bad)); }
        if (__ballot(bad != 0u) == 0ull) break;
        RETRY();
        kload();
        DTICK(7);
      }
      __builtin_amdgcn_s_setprio(3);
      unsigned m1 = max(kk[0][0], kk[0][2]), m2 = min(kk[0][0], kk[0][2]), m3 = 0;
#pragma unroll
      for (int i = 2; i < 16; ++i) {
        const unsigned a = kk[i >> 1][(i & 1) * 2];
        asm("v_med3_u32 %0, %1, %2, %0" : "+v"(m3) : "v"(m2), "v"(a));
        asm("v_med3_u32 %0, %1, %2, %0" : "+v"(m2) : "v"(m1), "v"(a));
        m1 = max(m1, a);
      }
      unsigned mine8 = 0;
#pragma unroll
      for (int it = 0; it < 8; ++it) {
        const unsigned km = wave_max(m1);
        mine8 = l == it ? km : mine8;
        const bool win = m1 == km;
        if (it < 7) { m1 = win ? m2 : m1; m2 = win ? m3 : m2; m3 = win ? 0u : m3; }
        if (it == 3 && nbar < 7) TICK();
      }
      if (__builtin_expect(__ballot(m1 == 0u) != 0ull, 0)) {
        unsigned lim = 0;
#pragma unroll 1
        for (int it = 0; it < 8; ++it) {
          unsigned m = 0;
#pragma unroll
          for (int i = 0; i < 8; ++i) m = umax3(m, kk[i][0] - lim, kk[i][2] - lim);
          lim += wave_max(m);
          mine8 = l == it ? lim : mine8;
        }
      }
      ST64(((u2){mine8, tag}), l < 8 ? WS_CAND + ((hp >> 3) * 16 + mb) * 128 + (mw ? 7 - l : l) * 16 + mw * 8 : WS_SZ);
      __builtin_amdgcn_s_setprio(HPRIO);
      STMP(2);
      TICK();
      colsum();
      DTICK(9);
      touch();
      while (nbar < 10) TICK();
      gload(); cload();
      if (RT && mw) asm volatile("global_load_dword %0, %1, %2 sc1" : "+v"(cw) : "v"(cwo), "s"(cwb) : "memory");
      TICK(); TICK();
      WAITV(0);
      asm volatile("" : "+v"(cw));
      gfin(); cfin();
      STMP(3);
      TICK(); TICK(); TICK();
      lpub();
    } else {
      if (PV == 0) pois();
      TICK();
      if (PV == 1) pois();
      if (ii >> 2) TICK();
      colrd(ii >> 2);
      TICK();
      if (CL && !(ii >> 2)) TICK();
      colsum();
      if (!CL && !(ii >> 2)) TICK();
      u4 sv; f4 acc[8];
      auto xload = [&]() {
        const u16* xp = x + (size_t)idx * D + 256 * ksp + 32 * kset + 8 * g4;
        sv = LD128(WS_ST + idx * 16);
#pragma unroll
        for (int it = 0; it < 4; ++it) xs[it] = *(const u4*)(xp + 64 * it);
        SBAR();
      };
      auto xnorm = [&]() {
        if (__builtin_expect(__ballot(sv[1] != tag || sv[3] != tag) != 0ull, 0)) {
          _Pragma("unroll 1") for (unsigned it = 0; it < SPIN; ++it) { SLEEP(1); sv = LD128(WS_ST + idx * 16); if (__ballot(sv[1] != tag || sv[3] != tag) == 0ull) break; }
        }
        const float nm = -__uint_as_float(sv[0]);
        xrs = __uint_as_float(sv[2]);
#pragma unroll
        for (int it = 0; it < 4; ++it) {
          float t0, t1, t2, t3; unsigned x0 = xs[it][0], x1 = xs[it][1], x2 = xs[it][2], x3 = xs[it][3];
          asm volatile("v_dot2_f32_bf16 %4, %0, %8, %10\n v_dot2_f32_bf16 %0, %0, %9, %10\n v_dot2_f32_bf16 %5, %1, %8, %10\n v_dot2_f32_bf16 %1, %1, %9, %10\n"
                       "v_dot2_f32_bf16 %6, %2, %8, %10\n v_dot2_f32_bf16 %2, %2, %9, %10\n v_dot2_f32_bf16 %7, %3, %8, %10\n v_dot2_f32_bf16 %3, %3, %9, %10\n"
                       "v_cvt_pk_bf16_f32 %0, %4, %0\n v_cvt_pk_bf16_f32 %1, %5, %1\n v_cvt_pk_bf16_f32 %2, %6, %2\n s_nop 0\n v_cvt_pk_bf16_f32 %3, %7, %3"
                       : "+v"(x0), "+v"(x1), "+v"(x2), "+v"(x3), "=&v"(t0), "=&v"(t1), "=&v"(t2), "=&v"(t3) : "s"(0x00003F80u), "s"(0x3F800000u), "v"(nm));
          xs[it] = (u4){x0, x1, x2, x3};
        }
        asm volatile("s_nop 1" : "+v"(xs[0]), "+v"(xs[1]), "+v"(xs[2]), "+v"(xs[3]), "+v"(xrs));
        SBAR();
      };
      auto gmul = [&](int i0, int i1) {
#pragma unroll
        for (int it = i0; it < i1; ++it)
#pragma unroll
          for (int c = 0; c < 8; ++c) acc[c] = MFMA16(S8(gw[it][c]), S8(xs[it]), it ? acc[c] : (f4)(0.f));
#pragma unroll
        for (int c = 0; c < 8; ++c) asm volatile("" : "+v"(acc[c]));
        SBAR();
      };
      auto gout = [&]() {
        unsigned go = r15 < 8 ? WS_GP + ((chg * 4 + ksp) * 2 + mw) * 2048 + (g4 >> 1) * 1024 + (g4 & 1) * 512 + r15 * 16 : WS_SZ;
        asm volatile("" : "+v"(go));
#pragma unroll
        for (int i = 0; i < 4; ++i) {
          unsigned a[4], b[4];
#pragma unroll
          for (int c = 0; c < 4; ++c) {
            auto r = __builtin_amdgcn_permlane32_swap(__float_as_uint(acc[c][i]), __float_as_uint(acc[c + 4][i]), false, false);
            a[c] = r[0]; b[c] = r[1];
          }
          unsigned s0, s1, s2, s3;
#define AR(d, x, y) "v_add_f32_dpp %" #d ", %" #x ", %" #y " row_ror:8 row_mask:0xf bank_mask:0xf\n"
          asm volatile("s_nop 1\n" AR(0, 8, 4) AR(1, 9, 5) AR(2, 10, 6) AR(3, 11, 7) : "=&v"(s0), "=&v"(s1), "=&v"(s2), "=&v"(s3)
                       : "v"(a[0]), "v"(a[1]), "v"(a[2]), "v"(a[3]), "v"(b[0]), "v"(b[1]), "v"(b[2]), "v"(b[3]));
          ST128(tagf(__builtin_bit_cast(f4, ((u4){s0, s1, s2, s3})), tag), go + i * 128);
        }
        SBAR();
      };
      for (int i = 0; i < ph; ++i) TICK();
      burst(0); touch();
      TICK();
      TICK();
      if (TL) { transp(gw[0]); transp(gw[1]); }
      TICK();
      if (!TL) transp(gw[0]);
      TICK();
      if (!TL) transp(gw[1]);
      TICK();
      if (ph) cload();
      if (RT && mw) asm volatile("global_load_dword %0, %1, %2 sc1" : "+v"(cw) : "v"(cwo), "s"(cwb) : "memory");
      SBAR();
      burst(1);
      TICK();
      TICK(); TICK();
      transp(gw[2]);
      WAITV(0);
      asm volatile("" : "+v"(cw));
      if (ph < 2) TICK();
      transp(gw[3]);
      if (ph < 1) { SBAR(); cload(); TICK(); }
      SBAR();
      if (XO == 0) { cfin(); xload(); gload(); } else { gload(); SBAR(); cfin(); xload(); }
      TICK();
      gfin();
      lpub();
      STMP(12);
      SBAR();
      __builtin_amdgcn_s_setprio(0);
      burst(2, 0, D1);
      STMP(3);
      SBAR();
      if (HS0) SLEEP(HS0);
      if (G1 & 1) { lwait<HW1>(adFL, 0x0Fu, G1 >> 4); if (G1S) SLEEP(G1S); __builtin_amdgcn_s_setprio(TP0); }
      xnorm();
      if (G1 & 2) { lwait<2>(adFL, 0x0Fu, G1 >> 4); if (G1S) SLEEP(G1S); __builtin_amdgcn_s_setprio(TP0); }
      STMP(5);
      gmul(0, 4);
      gout();
      STMP(6);
      SBAR();
    }
    __builtin_amdgcn_s_setprio(TP1);
    STMP(4);
    if (moe) {
      if (D2 == 0) { burst(2, D1, 16); SBAR(); }
      if (D1 >= 8 || D2 == 0) transp(dw[0]); if (D1 == 16 || D2 == 0) transp(dw[1]);
      SBAR();
      lwait<2>(adFL, 0x30u, 3);
      if (G2S) SLEEP(G2S);
      STMP(7);
      if (D2 == 1) { burst(2, D1, 16); SBAR(); }
      f4 acc[8];
      u4 ha[2];
      {
        u4 pg[4][2];
        auto pload = [&]() {
#pragma unroll
          for (int ks = 0; ks < 4; ++ks)
#pragma unroll
            for (int t = 0; t < 2; ++t) pg[ks][t] = LD128(WS_GP + ((chunk * 4 + ks) * 4 + 2 * t + mw) * 1024 + (4 * kset + g4) * 128 + np * 16);
        };
        auto pbad = [&]() {
          unsigned bad = 0;
#pragma unroll
          for (int i = 0; i < 8; ++i) untag(pg[i >> 1][i & 1], tag, bad);
          return __ballot((bad & 255u) != 0u) != 0ull;
        };
        pload();
        SBAR();
        if (D2 == 2) { burst(2, D1, 16); SBAR(); }
        __builtin_amdgcn_s_setprio(TP2);
        if (D1 < 8 && D2 == 1) transp(dw[0]);
        if (D1 < 16 && D2 == 1) transp(dw[1]);
        SBAR();
        if (__builtin_expect(pbad(), 0)) { _Pragma("unroll 1") for (unsigned it = 0; it < SPIN; ++it) { RETRY2(); SLEEP(4); pload(); if (!pbad()) break; } }
        STMP(10);
        f4 gq[2];
#pragma unroll
        for (int t = 0; t < 2; ++t)
          gq[t] = ((__builtin_bit_cast(f4, pg[0][t]) + __builtin_bit_cast(f4, pg[1][t])) + (__builtin_bit_cast(f4, pg[2][t]) + __builtin_bit_cast(f4, pg[3][t]))) * xrs;
        float hv[4];
#pragma unroll
        for (int e = 0; e < 4; ++e) {
          const float gg = rbf(gq[0][e]), uu = rbf(gq[1][e]);
          hv[e] = rbf(gg / (1.f + __expf(-gg))) * uu;
        }
        *(LAS u2*)(lds + L_HX + l * 16 + mw * 8) = (u2){pk2(hv[0], hv[1]), pk2(hv[2], hv[3])};
        lsig(myFL, 2u);
        if (D1 < 16 && D2 == 2) { SBAR(); if (D1 < 8) transp(dw[0]); transp(dw[1]); SBAR(); }
        lwait(adFL, 0xC0u, 2);
        ha[0] = *(const LAS u4*)(lds + L_HX + l * 16); ha[1] = *(const LAS u4*)(lds + L_HX + (l ^ 8) * 16);
      }
#pragma unroll
      for (int it = 0; it < 2; ++it)
#pragma unroll
        for (int c = 0; c < 8; ++c) acc[c] = MFMA16(S8(ha[it]), S8(dw[it][c]), it ? acc[c] : (f4)(0.f));
      if (g4 < 2) {
#pragma unroll
        for (int i = 0; i < 4; ++i) {
          const unsigned yo = WS_YP + ((chunk * 8 + 4 * g4 + i) * D + 256 * ksp + 128 * mw + 4 * r15) * 4;
          ST128(tagf((f4){acc[0][i], acc[1][i], acc[2][i], acc[3][i]}, tag), yo);
          ST128(tagf((f4){acc[4][i], acc[5][i], acc[6][i], acc[7][i]}, tag), yo + 256);
        }
      }
      STMP(11);
    }
    float sg;
    {
      const unsigned mono = cd[0] >> 16, rb = (mono & 0x8000u) ? (mono & 0x7FFFu) : (~mono & 0xFFFFu);
      sg = rbf(1.f / (1.f + __expf(-lo(rb))));
    }
    unsigned mine = (unsigned)__ballot(!NOMOE && l < 8 && (idx >> 7) == mb && (l & 1) == mw);
    if (mine) {
      if (spare) lwait<4>(adFL, 0x30u, 3);
      {
        const u2 f = LD64(WS_OH + (mb * 16 + r15) * 128);
        const unsigned t0 = NOW();
        unsigned rel = f[1] + YD - t0;
        rel = wave_max(f[0] == tag && rel < 600u ? rel : 0u);
        _Pragma("unroll 1") for (unsigned it = 0; it < SPIN; ++it) { if (NOW() - t0 >= rel) break; SLEEP(2); }
      }
      STMP(8);
      lwait<1>(adFL, 0x0Fu, 4);
      __builtin_amdgcn_s_setprio(TP2);
      while (mine) {
        const int t = __builtin_ctz(mine); mine &= mine - 1;
        u4 yv[14];
        _Pragma("unroll 1") for (unsigned it = 0; it < SPIN; ++it) {
          unsigned bad = 0;
#pragma unroll
          for (int i = 0; i < 14; ++i) yv[i] = LD128(WS_YP + (((i * 4 + g4) * 8 + t) * D + (nb >> 1) * 128 + (r15 >> 3) * 64 + (nb & 1) * 32 + (r15 & 7) * 4) * 4);
          SBAR();
#pragma unroll
          for (int i = 0; i < 14; ++i) untag(yv[i], tag, bad);
          if (__ballot((bad & 255u) != 0u) == 0ull) break;
          RETRY();
          SLEEP(YSL);
        }
        f4 y = __builtin_bit_cast(f4, yv[0]);
#pragma unroll
        for (int i = 1; i < 14; ++i) y += __builtin_bit_cast(f4, yv[i]);
        const float gt = __int_as_float(__builtin_amdgcn_readlane(__float_as_int(sg), t));
#pragma unroll
        for (int e = 0; e < 4; ++e) {
          auto r = __builtin_amdgcn_permlane32_swap(__float_as_uint(y[e]), __float_as_uint(y[e]), false, false);
          const float a = __uint_as_float(r[0]) + __uint_as_float(r[1]);
          auto r2 = __builtin_amdgcn_permlane16_swap(__float_as_uint(a), __float_as_uint(a), false, false);
          y[e] = rbf(rbf(__uint_as_float(r2[0]) + __uint_as_float(r2[1])) * gt);
        }
        if (l < 16) *(LAS f4*)(lds + L_KVX + t * 256 + (l & 7) * 32 + (l >> 3) * 16) = y;
      }
    }
    lsig(myFL, 3u);
    STMP(9);
    STFLUSH();
    if (spare) asm volatile(".Lend_s:"); else asm volatile(".Lend_h:");
    };
    if (sp) helper(BC<true>()); else helper(BC<false>());
    return;
  }

  const int wr = w >> 1, wc = w & 1;
  u2 kg; u4 sg4; unsigned tag;
  {
    const int row = srow;
    SBAR(); BST(0); SBAR();
    asm volatile("s_waitcnt vmcnt(4)" : "+v"(xa[0]), "+v"(xa[1]), "+v"(xa[2]), "+v"(xa[3]));
    SBAR(); BST(1); SBAR();
    float s0 = 0, s1 = 0;
#pragma unroll
    for (int c = 0; c < 4; ++c)
#pragma unroll
      for (int e = 0; e < 4; e += 2) { unsigned x0 = xa[c][e], x1 = xa[c][e + 1]; s0 = dot2(x0, 0x3F803F80u, s0); s1 = dot2(x1, 0x3F803F80u, s1); }
    const float m = half_sum(s0 + s1) * (1.f / D);
    f2 v[16], q2 = (f2)(0.f), q3 = (f2)(0.f);
#pragma unroll
    for (int c = 0; c < 16; ++c) v[c] = (f2){lo(xa[c >> 2][c & 3]), hi(xa[c >> 2][c & 3])} - m;
#pragma unroll
    for (int c = 0; c < 16; c += 2) { q2 += v[c] * v[c]; q3 += v[c + 1] * v[c + 1]; }
    q2 += q3;
    const float mr = rbf(m);
    const float rs = 1.f / rbf(sqrtf(rbf(rbf(half_sum(q2[0] + q2[1]) * (1.f / D)) + 1e-5f)));
    float d0 = 0, d1 = 0;
    asm volatile("s_waitcnt vmcnt(0)" : "+v"(rw[0]), "+v"(rw[1]), "+v"(rw[2]), "+v"(rw[3]), "+v"(d0) : "v"(rs));
#pragma unroll
    for (int c = 0; c < 16; c += 2) {
      const f2 e0 = (f2){lo(xa[c >> 2][c & 3]), hi(xa[c >> 2][c & 3])} - mr, e1 = (f2){lo(xa[c >> 2][(c & 3) + 1]), hi(xa[c >> 2][(c & 3) + 1])} - mr;
      const unsigned p0 = pk2(e0[0], e0[1]), p1 = pk2(e1[0], e1[1]);
      const f2 n0 = (f2){lo(p0), hi(p0)} * rs, n1 = (f2){lo(p1), hi(p1)} * rs;
      unsigned c0 = rw[c >> 2][c & 3], c1 = rw[c >> 2][(c & 3) + 1];
      d0 = dot2(pk2(n0[0], n0[1]), c0, d0); d1 = dot2(pk2(n1[0], n1[1]), c1, d1);
    }
    const float d = half_sum(d0 + d1);
    const unsigned rb = pk2(d, d) & 0xFFFFu;
    const unsigned mono = (rb & 0x8000u) ? (~rb & 0xFFFFu) : (rb | 0x8000u);
    SBAR();
    GETTAG(tg); tag = tg;
    kg = (u2){(mono << 16) | (unsigned)row, tag};
    sg4 = (u4){__float_as_uint(mr), tag, __float_as_uint(rs), tag};
  }
  MKWS(tag);
  SBAR(); BST(2); SBAR();
  if ((l & 31) == 0) { ST64(kg, WS_RG + srow * 8); ST128(sg4, WS_ST + srow * 16); }
  unsigned aad[2], bad[2];
#pragma unroll
  for (int s = 0; s < 2; ++s) aad[s] = ldsb + (wr * 64 + r15) * 128 + (((4 * s + g4) ^ ((r15 >> 1) & 7)) * 16);
#pragma unroll
  for (int nh = 0; nh < 2; ++nh) bad[nh] = ldsb + 16384 + (8 * g4 + q) * 128 + (((wc * 4 + 2 * (nh ^ wr) + (p >> 1)) ^ (((q >> 1) << 2) | ((g4 & 1) << 1))) * 16) + (p & 1) * 8;
  f4 acc[4][6];
#pragma unroll
  for (int m = 0; m < 4; ++m)
#pragma unroll
    for (int n = 0; n < 6; ++n) { acc[m][n] = (f4)(0.f); asm volatile("" : "+v"(acc[m][n])); }
#define MF(m, n, Fq) do { if (n < 2) acc[m][n] = MFMA16(CAT(Fq.b[2 * n], Fq.b[2 * n + 1]), Fq.a[m], acc[m][n]); \
     else acc[m][n] = MFMA16(Fq.a[m], CAT(Fq.b[2 * n], Fq.b[2 * n + 1]), acc[m][n]); SBAR(); } while (0)
#define RB(G, n, vb0, vb1, SS, hf) RDTR(G.b[2 * (n) + (hf)], (((n) & 1) ? vb1 : vb0), ((n) >> 1) * 8192 + (SS) * 4096 + (hf) * 512)
#define RA(G, m, va) RD128(G.a[m], va, (m) * 2048)
#define SUB(Fq, G, va, vb0, vb1, SS, PRE) do { \
    WAITL(13); SBAR(); \
    MF(0, 0, Fq); PRE; RA(G, 0, va); SBAR(); \
    WAITL(11); SBAR(); \
    MF(1, 0, Fq); RB(G, 0, vb0, vb1, SS, 0); SBAR(); MF(2, 0, Fq); RB(G, 0, vb0, vb1, SS, 1); SBAR(); MF(3, 0, Fq); RA(G, 1, va); SBAR(); \
    WAITL(12); SBAR(); \
    MF(0, 1, Fq); RA(G, 2, va); SBAR(); MF(1, 1, Fq); RA(G, 3, va); SBAR(); MF(2, 1, Fq); RB(G, 1, vb0, vb1, SS, 0); SBAR(); MF(3, 1, Fq); RB(G, 1, vb0, vb1, SS, 1); SBAR(); \
    WAITL(8); SBAR(); \
    MF(0, 2, Fq); RB(G, 2, vb0, vb1, SS, 0); SBAR(); MF(1, 2, Fq); RB(G, 2, vb0, vb1, SS, 1); SBAR(); MF(2, 2, Fq); RB(G, 3, vb0, vb1, SS, 0); SBAR(); MF(3, 2, Fq); RB(G, 3, vb0, vb1, SS, 1); SBAR(); \
    MF(0, 3, Fq); RB(G, 4, vb0, vb1, SS, 0); SBAR(); MF(1, 3, Fq); RB(G, 4, vb0, vb1, SS, 1); SBAR(); MF(2, 3, Fq); RB(G, 5, vb0, vb1, SS, 0); SBAR(); MF(3, 3, Fq); RB(G, 5, vb0, vb1, SS, 1); SBAR(); \
    MF(0, 4, Fq); MF(1, 4, Fq); MF(2, 4, Fq); MF(3, 4, Fq); MF(0, 5, Fq); MF(1, 5, Fq); MF(2, 5, Fq); MF(3, 5, Fq); } while (0)
#define KSTEP(MODE) do { \
    if (kt == sx) { const unsigned xo = xad + buf * STG; \
      _Pragma("unroll") for (int t = 0; t < 8; ++t) { const unsigned a_ = xo ^ (t * 16); \
        asm volatile("ds_read_b64 %0, %1" : "+v"(xv[t]) : "v"(a_) : "memory"); } \
      WAITL(0); SBAR(); } \
    const int nbuf = (buf + 1) & 3; \
    const unsigned bo = buf * STG, nbo = nbuf * STG; \
    { unsigned va = aad[1] + bo, vb0 = bad[0] + bo, vb1 = bad[1] + bo; SUB(f0, f1, va, vb0, vb1, 1, ); } \
    if (MODE < 2) { \
      unsigned va = aad[0] + nbo, vb0 = bad[0] + nbo, vb1 = bad[1] + nbo; \
      SUB(f1, f0, va, vb0, vb1, 0, if (MODE == 0) { WAITL(0); BAR(); } else asm volatile("ds_write_b32 %0, %1" ::"v"(myFL), "v"(1u) : "memory"); SBAR()); \
    } else { asm volatile("ds_write_b32 %1, %2\n ds_read_b32 %0, %3\n s_waitcnt lgkmcnt(2)" : "=&v"(fl0) : "v"(myFL), "v"(2u), "v"(adFL) : "memory"); SBAR(); \
      MF(0, 2, f1); MF(1, 2, f1); MF(2, 2, f1); MF(3, 2, f1); MF(0, 3, f1); MF(1, 3, f1); MF(2, 3, f1); MF(3, 3, f1); \
      MF(0, 4, f1); MF(1, 4, f1); MF(2, 4, f1); MF(3, 4, f1); MF(0, 5, f1); MF(1, 5, f1); MF(2, 5, f1); MF(3, 5, f1); \
      MF(0, 0, f1); MF(1, 0, f1); MF(2, 0, f1); MF(3, 0, f1); MF(0, 1, f1); MF(1, 1, f1); MF(2, 1, f1); MF(3, 1, f1); } \
    buf = nbuf; } while (0)
#define SUBK(Fq, G, va, vb0, vb1, SS, W0, PRE) do { \
    WAITL(W0); SBAR(); \
    MF(0, 2, Fq); PRE; RA(G, 0, va); SBAR(); \
    WAITL(7); SBAR(); \
    MF(1, 2, Fq); RB(G, 2, vb0, vb1, SS, 0); SBAR(); MF(2, 2, Fq); RB(G, 2, vb0, vb1, SS, 1); SBAR(); MF(3, 2, Fq); RA(G, 1, va); SBAR(); \
    WAITL(8); SBAR(); \
    MF(0, 3, Fq); RA(G, 2, va); SBAR(); MF(1, 3, Fq); RA(G, 3, va); SBAR(); MF(2, 3, Fq); RB(G, 3, vb0, vb1, SS, 0); SBAR(); MF(3, 3, Fq); RB(G, 3, vb0, vb1, SS, 1); SBAR(); \
    WAITL(10); SBAR(); \
    MF(0, 4, Fq); RB(G, 4, vb0, vb1, SS, 0); SBAR(); MF(1, 4, Fq); RB(G, 4, vb0, vb1, SS, 1); SBAR(); MF(2, 4, Fq); RB(G, 5, vb0, vb1, SS, 0); SBAR(); MF(3, 4, Fq); RB(G, 5, vb0, vb1, SS, 1); SBAR(); \
    WAITL(12); SBAR(); \
    MF(0, 5, Fq); MF(1, 5, Fq); MF(2, 5, Fq); MF(3, 5, Fq); } while (0)
#define RQ(G, va, vb0, vb1, SS) do { RA(G, 0, va); RB(G, 0, vb0, vb1, SS, 0); RB(G, 0, vb0, vb1, SS, 1); RA(G, 1, va); RA(G, 2, va); RA(G, 3, va); \
    RB(G, 1, vb0, vb1, SS, 0); RB(G, 1, vb0, vb1, SS, 1); SBAR(); } while (0)
#define MQ(Fq) do { MF(0, 0, Fq); MF(1, 0, Fq); MF(2, 0, Fq); MF(3, 0, Fq); MF(0, 1, Fq); MF(1, 1, Fq); MF(2, 1, Fq); MF(3, 1, Fq); } while (0)
  u2 xv[8];
#pragma unroll
  for (int t = 0; t < 8; ++t) asm volatile("" : "=v"(xv[t]));
  const int sx = (nb - rot) & 15;
  const unsigned xad = ldsb + (w * 32 + (l & 31)) * 128 + ((r15 >> 1) & 7) * 16 + (l >> 5) * 8;
  STMP(1);
  WAITL(0);
  BAR();
  STMP(2);
  WAITL(0);
  FQ f0, f1;
  { unsigned va = aad[0], vb0 = bad[0], vb1 = bad[1];
    RA(f0, 0, va); RB(f0, 0, vb0, vb1, 0, 0); RB(f0, 0, vb0, vb1, 0, 1); RA(f0, 1, va); RA(f0, 2, va); RA(f0, 3, va);
    RB(f0, 1, vb0, vb1, 0, 0); RB(f0, 1, vb0, vb1, 0, 1); RB(f0, 2, vb0, vb1, 0, 0); RB(f0, 2, vb0, vb1, 0, 1); RB(f0, 3, vb0, vb1, 0, 0); RB(f0, 3, vb0, vb1, 0, 1);
    RB(f0, 4, vb0, vb1, 0, 0); RB(f0, 4, vb0, vb1, 0, 1); RB(f0, 5, vb0, vb1, 0, 0); RB(f0, 5, vb0, vb1, 0, 1); SBAR(); }
  unsigned sad = ldsb + L_M + wr * 256 + g4 * 16, cad = ldsb + L_CS + wc * 384 + r15 * 4 + wr * 64;
  const int hd = hp * 2 + wc;
  unsigned kvo = WS_KVP + (hd * 16 + mb) * 2048 + wr * 1024 + l * 16, zo = r15 == 0 ? WS_ZP + (hd * 16 + mb) * 128 + g4 * 32 + wr * 16 : WS_SZ;
  unsigned xw = ldsb + L_KVX + w * 3072 + l * 16, xr = ldsb + L_KVX + (w ^ 2) * 3072 + l * 16;
  asm volatile("s_waitcnt lgkmcnt(0)" : "+v"(sad), "+v"(cad), "+v"(kvo), "+v"(zo), "+v"(xw), "+v"(xr) : : "memory"); SBAR();
  int buf = 0; unsigned fl0;
#pragma unroll 1
  for (int kt = 0; kt < 14; ++kt) KSTEP(0);
  STMP(14); SBAR();
  f4 sm[4], sr[4], sl[4]; f2 ck, cv;
  unsigned c2 = 2u, c3 = 3u, vfl = myFL, b30, b31;
  u4 ones = (u4)(0x3F803F80u);
#define SC0() asm volatile("ds_read_b32 %0, %2 offset:128\n ds_read_b32 %1, %2 offset:256" : "=&v"(ck[0]), "=&v"(cv[0]) : "v"(cad) : "memory")
#define SC1() asm volatile("ds_read_b32 %0, %2 offset:128\n ds_read_b32 %1, %2 offset:256" : "=&v"(ck[1]), "=&v"(cv[1]) : "v"(cad ^ 64) : "memory")
  const unsigned b20 = bad[0] + 2 * STG, b21 = bad[1] + 2 * STG;
#define SM(m) do { RD128(sm[m], sad, (m) * 64); RD128(sl[m], sad, L_RL - L_M + (m) * 64); RD128(sr[m], sad, L_RS - L_M + (m) * 64); } while (0)
  {
    { const unsigned va = aad[1] + 2 * STG; SUBK(f0, f1, va, b20, b21, 1, 6, asm volatile("" ::"v"(f0.b[0]), "v"(f0.b[1]), "v"(f0.b[2]), "v"(f0.b[3]))); }
    { const unsigned va = aad[0] + 3 * STG; b30 = bad[0] + 3 * STG; b31 = bad[1] + 3 * STG; SUBK(f1, f0, va, b30, b31, 0, 9, asm volatile("" : "+v"(c2), "+v"(c3), "+v"(vfl))); }
    STMP(7); SBAR();
    { const unsigned va = aad[1] + 3 * STG; SUBK(f0, f1, va, b30, b31, 1, 9, asm volatile("" : "+v"(ones))); }
    asm volatile("ds_write_b32 %1, %2\n ds_read_b32 %0, %3\n s_waitcnt lgkmcnt(8)" : "=&v"(fl0) : "v"(vfl), "v"(c2), "v"(adFL) : "memory"); SBAR();
    MF(0, 2, f1); SC0(); SBAR(); MF(1, 2, f1); SC1(); SBAR(); MF(2, 2, f1); MF(3, 2, f1); WAITL(10); SBAR();
    MF(0, 3, f1); SM(0); SBAR(); MF(1, 3, f1); MF(2, 3, f1); SM(1); SBAR(); MF(3, 3, f1);
    WAITL(12); SBAR();
    MF(0, 4, f1); SM(2); SBAR(); MF(1, 4, f1); MF(2, 4, f1); SM(3); SBAR(); MF(3, 4, f1); MF(0, 5, f1); MF(1, 5, f1); MF(2, 5, f1); MF(3, 5, f1);
  }
  asm volatile("s_waitcnt lgkmcnt(0)" : "+v"(fl0) : : "memory");
  if (__builtin_expect(((unsigned)__ballot(fl0 < 1u) & 0xC0u) != 0u, 0)) {
    lwait(adFL, 0xC0u, 1);
    SC0(); SC1(); SM(0); SM(1); SM(2); SM(3); WAITL(0);
  }
  asm volatile("" : "+v"(ck), "+v"(cv), "+v"(sm[0]), "+v"(sm[1]), "+v"(sm[2]), "+v"(sm[3]), "+v"(sr[0]), "+v"(sr[1]), "+v"(sr[2]), "+v"(sr[3]));
  asm volatile("" : "+v"(sl[0]), "+v"(sl[1]), "+v"(sl[2]), "+v"(sl[3]));
  STMP(3);
  __builtin_amdgcn_s_setprio(2);
  {
    f4 kv[4], zt[2];
    u4 kf[2][2], vf[2][2];
#define MKV(t, i) do { if ((i) == 2 || (i) == 5) zt[(i) == 2] = MFMA16(S8(kf[t][(i) == 2]), S8(ones), (t) ? zt[(i) == 2] : (f4)(0.f)); \
      else kv[((i) + 2) % 5] = MFMA16(S8(kf[t][(((i) + 2) % 5) & 1]), S8(vf[t][(((i) + 2) % 5) >> 1]), (t) ? kv[((i) + 2) % 5] : (f4)(0.f)); SBAR(); } while (0)
#pragma unroll
    for (int m = 0; m < 4; ++m) {
      const int t = m >> 1, mm = m & 1;
      f4 ea[2], pp[2];
#pragma unroll
      for (int a = 0; a < 2; ++a) {
        const f4 uv = (acc[m][4 + a] + sm[m] * cv[a]) * sr[m];
        vf[t][a][2 * mm] = pk2(uv[0], uv[1]); vf[t][a][2 * mm + 1] = pk2(uv[2], uv[3]);
        const f4 uk = acc[m][2 + a] + sm[m] * ck[a];
        ea[a] = uk * sl[m]; pp[a] = uk * sr[m] + 1.f;
      }
      SBAR();
#pragma unroll
      for (int e = 0; e < 8; ++e) { if (t && (e == 0 || e == 3 || e == 6)) MKV(0, 3 * mm + e / 3); ea[e >> 2][e & 3] = __builtin_amdgcn_exp2f(ea[e >> 2][e & 3]); SBAR(); }
#pragma unroll
      for (int e = 0; e < 8; ++e) pp[e >> 2][e & 3] = __builtin_amdgcn_fmed3f(ea[e >> 2][e & 3], pp[e >> 2][e & 3], 1.f);
      SBAR();
#pragma unroll
      for (int a = 0; a < 2; ++a) { pp[a] += 1e-6f; kf[t][a][2 * mm] = pk2(pp[a][0], pp[a][1]); kf[t][a][2 * mm + 1] = pk2(pp[a][2], pp[a][3]); }
      SBAR();
    }
    MKV(1, 0); MKV(1, 1); MKV(1, 2); MKV(1, 3);
    asm volatile("ds_write_b128 %0, %1" ::"v"(xw), "v"(kv[2]) : "memory"); SBAR();
    MKV(1, 4);
    asm volatile("ds_write_b128 %0, %1 offset:1024" ::"v"(xw), "v"(kv[3]) : "memory"); SBAR();
    MKV(1, 5);
    asm volatile("ds_write_b128 %0, %1 offset:2048\n ds_write_b32 %2, %3" ::"v"(xw), "v"(zt[1]), "v"(vfl), "v"(c3) : "memory");
    f4 r0, r1, rz;
    _Pragma("unroll 1") for (unsigned it = 0; it < SPIN; ++it) {
      unsigned v;
      asm volatile("ds_read_b32 %0, %4\n ds_read_b128 %1, %5 offset:1024\n ds_read_b128 %2, %5\n ds_read_b128 %3, %5 offset:2048\n s_waitcnt lgkmcnt(0)"
                   : "=&v"(v), "=&v"(r0), "=&v"(r1), "=&v"(rz) : "v"(adFL), "v"(xr) : "memory");
      if ((((unsigned)__ballot(v < 3u) >> (w ^ 2)) & 1u) == 0u) break;
    }
    const f4 s0 = kv[0] + r0, s1 = kv[1] + r1, sz = zt[0] + rz;
    u4 o; o[0] = min(pk2(s0[0], s0[1]), POIS - 1u); o[1] = pk2(s0[2], s0[3]); o[2] = pk2(s1[0], s1[1]); o[3] = pk2(s1[2], s1[3]);
    ST128(o, kvo);
    u4 zq = __builtin_bit_cast(u4, sz); zq[0] = min(zq[0], POIS - 1u);
    ST128(zq, zo);
  }
  STMP(4);
  {
    SBAR();
    const LAS float* Lm = (const LAS float*)(lds + L_M) + wr * 64 + r15;
    const LAS float* Lc = (const LAS float*)(lds + L_CS) + wc * 96 + 4 * g4;
    { const unsigned va = aad[0] + 2 * STG; RQ(f0, va, b20, b21, 0); }
    { const unsigned va = aad[1] + 2 * STG; RQ(f1, va, b20, b21, 1); }
    WAITL(8); SBAR(); MQ(f0);
    { const unsigned va = aad[0] + 3 * STG; RQ(f0, va, b30, b31, 0); }
    WAITL(8); SBAR(); MQ(f1);
    { const unsigned va = aad[1] + 3 * STG; RQ(f1, va, b30, b31, 1); }
    WAITL(8); SBAR();
    const f4 c0 = *(const LAS f4*)(Lc + 16 * wr), c1 = *(const LAS f4*)(Lc + 16 - 16 * wr);
    float mv[4], rv[4], rl[4];
#pragma unroll
    for (int m = 0; m < 4; ++m) { mv[m] = Lm[m * 16]; rv[m] = Lm[m * 16 + (L_RS - L_M) / 4]; rl[m] = Lm[m * 16 + (L_RL - L_M) / 4]; }
    SBAR(); MQ(f0);
    if (sx >= 14) {
      const unsigned xo = xad + (sx - 12) * STG;
#pragma unroll
      for (int t = 0; t < 8; ++t) { const unsigned a_ = xo ^ (t * 16); asm volatile("ds_read_b64 %0, %1" : "+v"(xv[t]) : "v"(a_) : "memory"); }
    }
    WAITL(0); SBAR(); MQ(f1);
    LAS char* pw = (LAS char*)lds + L_PQ + wr * 8192 + wc * 4096 + l * 16;
#pragma unroll
    for (int m = 0; m < 4; ++m) {
      const f4 u0 = acc[m][0] + c0 * mv[m], u1 = acc[m][1] + c1 * mv[m];
      *(LAS u2*)(pw + wr * 8 + m * 1024) = (u2){phi2((f2){u0[0], u0[1]}, (f2)(rv[m]), (f2)(rl[m])), phi2((f2){u0[2], u0[3]}, (f2)(rv[m]), (f2)(rl[m]))};
      *(LAS u2*)(pw + 8 - wr * 8 + m * 1024) = (u2){phi2((f2){u1[0], u1[1]}, (f2)(rv[m]), (f2)(rl[m])), phi2((f2){u1[2], u1[3]}, (f2)(rv[m]), (f2)(rl[m]))};
    }
  }
  lsig(myFL, 4);
  __builtin_amdgcn_s_setprio(0);
  STMP(5);
  {
  int l;
  asm volatile("v_mbcnt_lo_u32_b32 %0, -1, 0\n v_mbcnt_hi_u32_b32 %0, -1, %0" : "=v"(l));
  __builtin_assume(l >= 0 && l < 64);
  const int r15 = l & 15, g4 = l >> 4, q = (l & 15) >> 2, p = l & 3;
  const int trow = mb * 128 + w * 32 + (l & 31);
  u2 oh;
  const unsigned flo = PAIRAT(r15) * 128;
  const char* fb = uptr(wsx + WS_OH + mb * 2048);
  int sel = -1;
  {
    const u4 s0 = *(const LAS u4*)(lds + L_SEL), s1 = *(const LAS u4*)(lds + L_SEL + 16);
#pragma unroll
    for (int i = 0; i < 8; ++i) { const int v = (int)(i < 4 ? s0[i & 3] : s1[i & 3]); sel = v == trow ? i : sel; }
  }
  const bool anysel = __ballot(sel >= 0) != 0ull;
  f16v oc[2][2];
  {
    const float m = *(const LAS float*)(lds + L_M + (w * 32 + (l & 31)) * 4), rs = *(const LAS float*)(lds + L_RS + (w * 32 + (l & 31)) * 4);
#pragma unroll
    for (int t = 0; t < 8; ++t) {
      const f4 xf = (f4){lo(xv[t][0]), hi(xv[t][0]), lo(xv[t][1]), hi(xv[t][1])};
      const f4 y = (xf - m) * rs + xf;
#pragma unroll
      for (int i = 0; i < 4; ++i) oc[0][t >> 2][(t & 3) * 4 + i] = y[i];
    }
    asm volatile("" : "+v"(oc[0][0]), "+v"(oc[0][1]));
  }
  unsigned wa[2];
#pragma unroll
  for (int t = 0; t < 2; ++t) wa[t] = ldsb + L_WO + wc * 4096 + (4 * (g4 >> 1) + q) * 128 + (((4 * t + 2 * (g4 & 1) + (p >> 1)) ^ ((q >> 1) << 2)) * 16) + (p & 1) * 8;
  u4 a[RD][4];
#pragma unroll
  for (int k = 0; k < RD; ++k) asm volatile("" : "=v"(a[k][0]), "=v"(a[k][1]), "=v"(a[k][2]), "=v"(a[k][3]));
  const char* ob = uptr(wsx + WS_OF + mb * 262144 + wc * 8192 + wr * 4096);
  const unsigned ov0 = l * 16 + wc * 2048, ov1 = ov0 ^ 2048;
  STMP(6);
  unsigned vfw[4];
#pragma unroll
  for (int i = 0; i < 4; ++i) { vfw[i] = VFW(i); asm volatile("" : "+v"(vfw[i])); }
  auto wo_part = [&](int s0, int s1) {
#pragma unroll
    for (int i = s0 / 2; i < s1 / 2; i += 2) {
      const int pp = i + wr;
      const char* wp = (const char*)wo + (nb * 128 - 3072) + (2 * PAIRAT(pp) + wc) * 65536; const unsigned wm = ldsb + (2 * pp + wc) * 4096;
      GL4(vfw, wp, wm, L_WO);
    }
  };
  lwait<2>(adFL, 0x3Fu, (l & 4) ? 3u : 4u);
  asm volatile("global_load_dwordx2 %0, %1, %2 sc1" : "=&v"(oh) : "v"(flo), "s"(fb) : "memory");
  wo_part(WE, WB);
  WAITV(WB - WE);
  asm volatile("" : "+v"(oh));
  unsigned seen = (unsigned)__ballot(oh[0] == tag), go = 0;
  auto need = [&](unsigned mask) {
    if (__builtin_expect((go & mask) != mask, 0)) {
      RETRY2();
      _Pragma("unroll 1") for (unsigned it = 0; it < SPIN; ++it) {
        if ((seen & mask) != mask) {
          asm volatile("global_load_dwordx2 %0, %1, %2 sc1\n s_waitcnt vmcnt(0)" : "+v"(oh) : "v"(flo), "s"(fb) : "memory");
          seen = (unsigned)__ballot(oh[0] == tag);
        }
        go = seen & (unsigned)__ballot(oh[1] - NOW() - 1u >= 1000u);
        if ((go & mask) == mask) break;
        SLEEP(1);
      }
    }
  };
#define OX 0
#define OSC "sc1"
#define OL1(pos, i) do { if (OX == 2 && (pos) >= RD) break; \
    asm volatile("global_load_dwordx4 %0, %1, %2 offset:%3 " OSC : "+v"(a[(pos) % RD][i]) : "v"((i) & 1 ? ov1 : ov0), "s"(ob + (OX == 1 ? ((pos) & 1) : PAIRAT(pos)) * 16384), "i"(((i) >> 1) * 1024) : "memory"); } while (0)
#define OLR(pos) do { _Pragma("unroll") for (int i_ = 0; i_ < 4; ++i_) asm volatile("global_load_dwordx4 %0, %1, %2 sc1" : "+v"(a[(pos) % RD][i_]) : "v"((i_ & 1 ? ov1 : ov0) + (i_ >> 1) * 1024), "s"(ob + PAIRAT(pos) * 16384) : "memory"); } while (0)
#define OLD(pos) do { OL1(pos, 0); OL1(pos, 1); OL1(pos, 2); OL1(pos, 3); } while (0)
  auto omax = [&](u4(&v)[4]) {
    asm volatile("" : "+v"(v[0]), "+v"(v[1]), "+v"(v[2]), "+v"(v[3]));
    return max(umax3(v[0][0], v[1][0], v[2][0]), v[3][0]);
  };
#define OFIX(pos, m) do { \
    if (__builtin_expect(__ballot((m) == POIS) != 0ull, 0)) { RETRY(); _Pragma("unroll 1") for (unsigned it = 0; it < SPIN; ++it) { SLEEP(4); OLR(pos); WAITV(0); if (__ballot(omax(a[(pos) % RD]) == POIS) == 0ull) break; } } } while (0)
#define OS1(i)
#define OS2(i)
  lwait<1>(adFL, 0x30u, 4);
  wo_part(WB, 32);
  __builtin_amdgcn_s_setprio(2);
  go = seen & (unsigned)__ballot(oh[1] - NOW() - 1u >= 1000u);
  need(1u);
  STMP(15);
  u2 oh2 = oh;
  if (__builtin_expect(seen != ~0u, 0)) asm volatile("global_load_dwordx2 %0, %1, %2 sc1" : "+v"(oh2) : "v"(flo), "s"(fb) : "memory");
  OS1(0); OLD(0);
#pragma unroll
  for (int i = 1; i < RD; ++i) { need(1u << i); OS1(i); OLD(i); }
  STMP(10);
  s4 b[8];
#define ORD(pos, f) do { if ((f) < 2 && (pos) == 8) { wa[(f) & 1] += 65536; asm volatile("" : "+v"(wa[(f) & 1])); } \
    RDTR(b[2 * (f)], wa[(f) & 1], ((pos) & 7) * 8192 + ((f) >> 1) * 1024); RDTR(b[2 * (f) + 1], wa[(f) & 1], ((pos) & 7) * 8192 + ((f) >> 1) * 1024 + 2048); } while (0)
#define OMF(pos, f, r) do { if ((f) < 2 && (pos) == 0 && (r) == 1) asm volatile("v_mfma_f32_32x32x16_bf16 %0, %1, %2, 0" : "=&v"(oc[r][(f) & 1]) \
      : "v"(CAT(b[2 * (f)], b[2 * (f) + 1])), "v"(a[(pos) % RD][((f) & 2) + (r)])); \
    else asm volatile("v_mfma_f32_32x32x16_bf16 %0, %1, %2, %0" : "+v"(oc[r][(f) & 1]) \
      : "v"(CAT(b[2 * (f)], b[2 * (f) + 1])), "v"(a[(pos) % RD][((f) & 2) + (r)])); \
    SBAR(); if ((r) == 1 && (pos) < 15) ORD(((pos) + 1) & 15, f); SBAR(); } while (0)
#define ONX(pos) ((pos) > 0 && (pos) + RD - 1 < 16)
#define ONL(pos, i) do { if (ONX(pos)) OL1(((pos) + RD - 1) & 15, i); SBAR(); } while (0)
#define OWL(pos, f) do { WAITL((pos) < 15 ? 6 : 6 - 2 * (f)); SBAR(); } while (0)
#define OHEAD(pos) do { unsigned m_ = 0; \
    if ((pos) == WE / 2 - 1 && WE < 32) { asm volatile("" : "+v"(fl3)); if (__builtin_expect(((unsigned)__ballot(fl3 < 5u) >> (w ^ 2)) & 1u, 0)) lwait(adFL, 1u << (w ^ 2), 5); SBAR(); } \
    OWL(pos, 0); OMF(pos, 0, 0); if (ONX(pos)) { need(1u << (((pos) + RD - 1) & 15)); OS1(((pos) + RD - 1) & 15); } SBAR(); \
    OMF(pos, 0, 1); ONL(pos, 0); \
    OWL(pos, 1); OMF(pos, 1, 0); ONL(pos, 1); \
    OMF(pos, 1, 1); ONL(pos, 2); \
    OWL(pos, 2); OMF(pos, 2, 0); ONL(pos, 3); \
    if ((pos) == 0 || (pos) == 4) { asm volatile("ds_write_b32 %0, %1" ::"v"(myFL), "v"((pos) ? 6u : 5u) : "memory"); SBAR(); } \
    if ((pos) == WE / 2 - 3) { asm volatile("ds_read_b32 %0, %1" : "=v"(fl3) : "v"(adFL) : "memory"); SBAR(); } \
    if ((pos) == 13) { asm volatile("ds_read_b32 %0, %1" : "=v"(fl3) : "v"(adFL) : "memory"); SBAR(); } \
    OMF(pos, 2, 1); \
    OWL(pos, 3); OMF(pos, 3, 0); \
    if ((pos) < 15) { WAITV(4 * ((pos) + RD - 1 < 16 ? RD - 2 : 14 - (pos))); m_ = omax(a[((pos) + 1) % RD]); asm volatile("" : "+v"(m_)); SBAR(); } \
    OMF(pos, 3, 1); \
    if ((pos) < 15) { OFIX(((pos) + 1) & 15, m_); OS2(((pos) + 1) & 15); } SBAR(); } while (0)
  unsigned fl3;
  ORD(0, 0); ORD(0, 1); ORD(0, 2); ORD(0, 3);
  { WAITV(4 * (RD - 1)); const unsigned m_ = omax(a[0]); OFIX(0, m_); OS2(0);
    asm volatile("" : "+v"(oh2));
    if (oh2[0] == tag) oh = oh2;
    seen = (unsigned)__ballot(oh[0] == tag);
    go = seen & (unsigned)__ballot(oh[1] - NOW() - max(r15 - (RD - 1), 0) * ADV - 1u >= 1000u);
    SBAR(); }
  OHEAD(0); OHEAD(1); STMP(11); OHEAD(2); OHEAD(3); OHEAD(4); OHEAD(5); OHEAD(6); OHEAD(7); STMP(12);
  OHEAD(8); OHEAD(9); STMP(13); OHEAD(10); OHEAD(11); OHEAD(12); OHEAD(13); OHEAD(14);
  f4 xi[8];
  {
    asm volatile("" : "+v"(fl3));
    if (__builtin_expect(((unsigned)__ballot(fl3 < 6u) >> (w ^ 2)) & 1u, 0)) lwait(adFL, 1u << (w ^ 2), 6);
    if (__builtin_expect(((unsigned)__ballot(fl3 < 4u) & 0x30u) != 0u, 0)) lwait(adFL, 0x30u, 4);
    const unsigned xw = ldsb + L_WO + (4 * wr + wc) * 4096 + l * 16, xr = xw ^ 4096;
#define XW(t) asm volatile("ds_write_b128 %0, %1 offset:%2" ::"v"(xw), "v"(__builtin_shufflevector(oc[1][(t) >> 2], oc[1][(t) >> 2], ((t) & 3) * 4, ((t) & 3) * 4 + 1, ((t) & 3) * 4 + 2, ((t) & 3) * 4 + 3)), \
      "i"(((t) >> 2) * 8192 + ((t) & 3) * 1024) : "memory")
    SBAR();
    OWL(15, 0); OMF(15, 0, 1); OWL(15, 1); OMF(15, 1, 1); OWL(15, 2); OMF(15, 2, 1); OWL(15, 3); OMF(15, 3, 1);
    OMF(15, 0, 0); OMF(15, 1, 0); XW(0); XW(1); XW(2); XW(3); SBAR();
    OMF(15, 2, 0); XW(4); XW(5); XW(6); XW(7); SBAR();
    OMF(15, 3, 0);
    asm volatile("ds_write_b32 %0, %1" ::"v"(myFL), "v"(7u) : "memory");
    _Pragma("unroll 1") for (unsigned it = 0; it < SPIN; ++it) {
      unsigned v;
      asm volatile("ds_read_b32 %0, %1" : "=v"(v) : "v"(adFL) : "memory");
#pragma unroll
      for (int t = 0; t < 8; ++t) RD128(xi[t], xr, (t >> 2) * 8192 + (t & 3) * 1024);
      WAITL(0);
      asm volatile("" : "+v"(v), "+v"(xi[0]), "+v"(xi[1]), "+v"(xi[2]), "+v"(xi[3]), "+v"(xi[4]), "+v"(xi[5]), "+v"(xi[6]), "+v"(xi[7]));
      if ((((unsigned)__ballot(v < 7u) >> (w ^ 1)) & 1u) == 0u) break;
    }
    asm volatile("" : "+v"(oc[0][0]), "+v"(oc[0][1]));
#pragma unroll
    for (int t = 0; t < 8; ++t)
#pragma unroll
      for (int i = 0; i < 4; ++i) xi[t][i] += oc[0][t >> 2][(t & 3) * 4 + i];
  }
  STMP(8);
  if (anysel) {
    lwait(adFL, 0xC0u, 3);
    if (sel >= 0) {
#pragma unroll
      for (int t = 0; t < 8; ++t) xi[t] += *(const LAS f4*)(lds + L_KVX + sel * 256 + (8 * t + 4 * (l >> 5)) * 4);
    }
  }
  const __amdgpu_buffer_rsrc_t outr = __builtin_amdgcn_make_buffer_rsrc(out, 0, 2048 * D * 2, 0x00020000);
  if (FULL) {
    LAS char* tb = (LAS char*)lds + L_PQ + w * 4096;
#pragma unroll
    for (int t = 0; t < 8; ++t) *(LAS u2*)(tb + (l & 31) * 128 + ((t ^ (l & 7)) * 16) + (l >> 5) * 8) = (u2){pk2(xi[t][0], xi[t][1]), pk2(xi[t][2], xi[t][3])};
#pragma unroll
    for (int i = 0; i < 4; ++i) {
      const u4 v = *(const LAS u4*)(tb + i * 1024 + (l >> 3) * 128 + (((l ^ (l >> 3)) & 7) * 16));
      __builtin_amdgcn_raw_buffer_store_b128(v, outr, (mb * 128 + w * 32 + i * 8 + (l >> 3)) * 2048 + nb * 128 + (l & 7) * 16, 0, 16);
    }
  } else {
#pragma unroll
  for (int hh = 0; hh < 4; ++hh) {
    const f4 ta = xi[2 * hh], tb = xi[2 * hh + 1];
    auto r0 = __builtin_amdgcn_permlane32_swap(pk2(ta[0], ta[1]), pk2(tb[0], tb[1]), false, false);
    auto r1 = __builtin_amdgcn_permlane32_swap(pk2(ta[2], ta[3]), pk2(tb[2], tb[3]), false, false);
    __builtin_amdgcn_raw_buffer_store_b128(((u4){r0[0], r1[0], r0[1], r1[1]}), outr, (trow * D + nb * 64 + 16 * hh + 8 * (l >> 5)) * 2, 0, 16);
  }
  }
  if (g == 0 && w == 0 && l == 0) __builtin_amdgcn_raw_buffer_store_b32(tag, wse, WS_E, 0, 16);
  STMP(9);
  STFLUSH();
  asm volatile(".Lend_c:");
  }
  return;
  }
  {
    unsigned vb[4], vfw[4];
    STMP(6); SBAR();
#pragma unroll
    for (int i = 0; i < 4; ++i) vb[i] = (8 * i + (l >> 3)) * 6144 + (((l & 7) ^ ((((l >> 4) & 1) << 2) | ((i & 1) << 1))) * 16) + (3 - i) * 1024;
    const unsigned lb0 = lw * 196608, lb1 = 196608 - lw * 194560, mbb = ldsb + 16384 + lw * 12288;
    const char* Ab = kx + ao;
    const char* Bb = kq + (hp * 128 + lw * 2048 - 3072);
    auto issue = [&](int s, int u0, int u1) {
      const int kt = (s + rot) & 15;
      const char* sa = Ab + kt * 128; const char* sb = Bb + kt * (64 * 6144);
      const unsigned so = (s & 3) * STG, ma = mba + so, mq = mbb + so;
#pragma unroll
      for (int u = u0; u < u1; ++u) { if (u < 2) GL4(va, sa + u * 65536, ma, u * 4096); else GL4(vb, sb + (u == 2 ? lb0 : u == 3 ? lb1 : lb0 + 2048), mq, (u - 2) * 4096); }
    };
    int tk = 0;
#define LBAR() do { BST(tk + 1); BAR(); BST(tk + 21); ++tk; } while (0)
    issue(0, 2, 3); issue(0, 1, 2); issue(0, 3, 5); SBAR(); STMP(8); SBAR(); issue(1, 0, 5); issue(2, 0, U0);
    BST(60);
    WAITV(20 + 4 * U0); BST(0); BAR(); BST(20);
    issue(2, U0, 5);
    GETTAG(tag);
    MKWS(tag);
    const unsigned ck0 = (unsigned)__builtin_amdgcn_s_memtime(), rt0 = NOW();
    int s0 = 3;
    if (U3 < 5) { issue(3, 0, U3); BST(tk + 40); WAITV(20 + 4 * U3); LBAR(); issue(3, U3, 5); s0 = 4; }
#pragma unroll 1
    for (; s0 < (sp ? 16 : 6 + ph); ++s0) { issue(s0, 0, 5); BST(tk + 40); WAITV(40); LBAR(); }
#pragma unroll 1
    for (; s0 < 16; s0 += 5) {
      WAITV(20); LBAR();
      WAITV(0); LBAR();
      issue(s0, 0, 5); issue(s0 + 1, 0, U1); BST(tk + 40); WAITV(4 * U1); LBAR();
      issue(s0 + 1, U1, 5); issue(s0 + 2, 0, 5);
      if (s0 + 3 >= 16) break;
      issue(s0 + 3, 0, U2); BST(tk + 40); WAITV(20 + 4 * U2); LBAR();
      issue(s0 + 3, U2, 5);
      if (s0 + 4 >= 16) break;
      issue(s0 + 4, 0, 5); BST(tk + 40); WAITV(40); LBAR();
    }
    int wq = 0;
#pragma unroll
    for (int i = 0; i < 4; ++i) vfw[i] = VFW(i);
    auto wo_upto = [&](int lim) {
      _Pragma("unroll 1") for (; wq < lim; ++wq) { const char* wp = (const char*)wo + (nb * 128 - 3072) + HEADAT(wq) * 65536; const unsigned wm = ldsb + wq * 4096; GL4(vfw, wp, wm, L_WO); }
    };
    asm volatile("ds_write_b32 %0, %1" ::"v"(adFL), "v"(0u) : "memory");
    unsigned kd = sp ? KS : KD, od = OD;
    if (CF) {
      const unsigned dc = (unsigned)__builtin_amdgcn_s_memtime() - ck0, dr = NOW() - rt0;
      const float sc = fminf((float)dr * (float)CF * __builtin_amdgcn_rcpf((float)dc), 2.f);
      kd = KL + (unsigned)((float)(kd - KL) * sc); od = OL + (unsigned)((float)(OD - OL) * sc);
      asm volatile("" : "+v"(kd), "+v"(od));
    }
    const int hd = hp * 2 + lw;
    u4 sel[2];
#pragma unroll
    for (int e = 0; e < 2; ++e) {
      unsigned v = (r15 >> 2) == g4 ? (((r15 & 3) == 2 * e ? 0x3F80u : 0u) | ((r15 & 3) == 2 * e + 1 ? 0x3F800000u : 0u)) : 0u;
      asm volatile("" : "+v"(v));
      sel[0][e] = v; sel[0][2 + e] = 0u; sel[1][e] = 0u; sel[1][2 + e] = v;
    }
    const char* kvb = uptr(wsx + WS_KVP + (size_t)hd * 32768);
    const char* zb = uptr(wsx + WS_ZP + (size_t)hd * 2048);
    unsigned zoff = r15 * 128 + g4 * 32, oho = l == 0 ? WS_OH + (mb * 16 + hp) * 128 : WS_SZ, fla = ldsb + L_FL;
    asm volatile("" : "+v"(zoff), "+v"(oho), "+v"(fla), "+s"(kvb), "+s"(zb));
    WAITV(0); WAITL(0); LBAR();
    u4 dg;
    unsigned now;
    if (lw) {
      const u4 pz = (u4)(POIS);
      const unsigned tb = NOW();
      __builtin_amdgcn_raw_buffer_store_b32(tb + kd, wsr, l == 0 ? WS_HK + hp * 512 + (mb >> 2) * 128 + (mb & 3) * 4 : WS_SZ, 0, 16);
#pragma unroll
      for (int i = 0; i < 16; ++i) ST128(pz, WS_OF + (mb * 32 + hp * 2) * 8192 + i * 1024 + l * 16);
      BST(61);
      const char* hb = uptr(wsx + WS_HK + hp * 512);
      __builtin_amdgcn_s_setprio(LP1);
      lwait<4>(adFL, 0xC0u, 1u);
      while (NOW() - tb < HT) SLEEP(1);
      STMP(1);
      unsigned t0, rq[4], m0, m1, nt;
#define HG4(d, a, b, c, e) "s_sub_u32 s" #a ", s" #a ", %0\n s_sub_u32 s" #b ", s" #b ", %0\n s_sub_u32 s" #c ", s" #c ", %0\n s_sub_u32 s" #e ", s" #e ", %0\n" \
      "s_max_u32 s" #a ", s" #a ", s" #b "\n s_max_u32 s" #c ", s" #c ", s" #e "\n s_max_u32 %" #d ", s" #a ", s" #c "\n"
      asm volatile("s_mov_b32 %7, 15\n.Lhint:\n s_load_dwordx4 s[72:75], %8, 0 glc\n s_load_dwordx4 s[76:79], %8, 128 glc\n s_load_dwordx4 s[80:83], %8, 256 glc\n"
                   "s_load_dwordx4 s[84:87], %8, 384 glc\n s_memrealtime s[88:89]\n s_waitcnt lgkmcnt(0)\n s_sub_u32 %0, s88, 0x800\n"
                   HG4(1, 72, 73, 74, 75) HG4(2, 76, 77, 78, 79) HG4(3, 80, 81, 82, 83) HG4(4, 84, 85, 86, 87)
                   "s_max_u32 %5, %1, %2\n s_max_u32 %6, %3, %4\n s_max_u32 s72, %5, %6\n s_cmpk_lt_u32 s72, 0xc00\n s_cbranch_scc1 .Lhdone\n"
                   "s_sleep %9\n s_sub_u32 %7, %7, 1\n s_cbranch_scc0 .Lhint\n"
                   "s_mov_b32 %1, 0\n s_mov_b32 %2, 0\n s_mov_b32 %3, 0\n s_mov_b32 %4, 0\n s_mov_b32 %5, 0\n s_mov_b32 %6, 0\n.Lhdone:"
                   : "=&s"(t0), "=&s"(rq[0]), "=&s"(rq[1]), "=&s"(rq[2]), "=&s"(rq[3]), "=&s"(m0), "=&s"(m1), "=&s"(nt) : "s"(hb), "i"(HKS)
                   : "memory", "scc", "s72", "s73", "s74", "s75", "s76", "s77", "s78", "s79", "s80", "s81", "s82", "s83", "s84", "s85", "s86", "s87", "s88", "s89");
      BST(16);
      const bool up = m0 > m1;
      const unsigned a0 = up ? rq[2] : rq[0], a1 = up ? rq[3] : rq[1], a2 = up ? rq[0] : rq[2], a3 = up ? rq[1] : rq[3], gu = up ? 2u : 0u;
      unsigned e0, e1, go;
      asm("s_cmp_gt_u32 %3, %4\n s_cselect_b32 %0, %6, %5\n s_cselect_b32 %1, %5, %6\n s_addc_u32 %2, %7, 0" : "=&s"(e0), "=&s"(e1), "=&s"(go) : "s"(a2), "s"(a3), "s"(a0), "s"(a1), "s"(gu) : "scc");
      dg = (u4){((t0 + 3u + e0) & ~3u) | go, t0 + e1, t0 + min(a2, a3), t0 + max(m0, m1)};
      asm volatile("ds_write_b128 %0, %1 offset:32\n ds_write_b32 %0, %2 offset:20" ::"v"(fla), "v"(dg), "v"(1u) : "memory");
      BST(15);
      now = NOW();
      WAITV(0);
      ST64(((u2){tag, ((int)(dg[3] - now) > 0 ? dg[3] : now) + od}), oho);
    } else {
      wo_upto(WA);
      BST(15);
      if (WA < WE) { lwait<2>(adFL, 0x0Fu, 3); if (WS0) SLEEP(WS0); wo_upto(WE); }
      STMP(1);
      __builtin_amdgcn_s_setprio(LP0);
      u4 v;
      _Pragma("unroll 1") for (unsigned it = 0; it < SPIN; ++it) {
        unsigned f;
        asm volatile("ds_read_b32 %0, %2 offset:20\n ds_read_b128 %1, %2 offset:32\n s_waitcnt lgkmcnt(0)" : "=&v"(f), "=&v"(v) : "v"(fla) : "memory");
        if (__builtin_amdgcn_readfirstlane(f)) break;
        SLEEP(2);
      }
#pragma unroll
      for (int e = 0; e < 4; ++e) dg[e] = __builtin_amdgcn_readfirstlane(v[e]);
      now = NOW();
    }
    unsigned gx = (dg[0] & 3u) * 8192u;
    asm volatile("" : "+s"(gx));
    __builtin_amdgcn_s_setprio(3);
    u4 pv[16][2], zv0, zv1, pq[8];
    const unsigned lv16 = l * 16;
    const unsigned pa = ldsb + L_PQ + lw * 4096 + l * 16, omask = lw ? 0x0Au : 0x25u;
    STMP(2);
#define GLD(gq) do { _Pragma("unroll") for (int i = 0; i < 4; ++i) { const char* sb = padd(kvb, (((gq) * 8192u + (i & 2) * 2048u) ^ gx)); \
      asm volatile("global_load_dwordx4 %0, %1, %2 offset:%3 sc1" : "=v"(pv[(gq) * 4 + i][0]) : "v"(lv16), "s"(sb), "i"((i & 1) * 2048) : "memory"); \
      asm volatile("global_load_dwordx4 %0, %1, %2 offset:%3 sc1" : "=v"(pv[(gq) * 4 + i][1]) : "v"(lv16), "s"(sb), "i"((i & 1) * 2048 + 1024) : "memory"); } } while (0)
#define ZLD() do { asm volatile("global_load_dwordx4 %0, %1, %2 sc1" : "=v"(zv0) : "v"(zoff), "s"(zb) : "memory"); \
      asm volatile("global_load_dwordx4 %0, %1, %2 offset:16 sc1" : "=v"(zv1) : "v"(zoff), "s"(zb) : "memory"); } while (0)
#pragma unroll
    for (int gq = 0; gq < 4; ++gq) {
      while ((int)(now - dg[gq]) < 0) now = NOW();
      if (gq == 3) ZLD();
      GLD(gq);
    }
    lsig(myFL, 3);
    STMP(5);
    unsigned ofo = WS_OF + (mb * 32 + hd) * 8192 + g4 * 512 + r15 * 16;
    unsigned ofp = ofo + 4096;
    f4 eps = (f4)(1e-6f);
    asm volatile("" : "+v"(ofo), "+v"(ofp), "+v"(eps), "+v"(sel[0]), "+v"(sel[1]));
    f4 gs[4][4]; float z[8];
#pragma unroll
    for (int gq = 0; gq < 4; ++gq) {
      if (gq == PW) {
        SBAR();
        lwait(adFL, omask, (l & 4) ? 3u : 4u);
#pragma unroll
        for (int i = 0; i < 8; ++i) RD128(pq[i], pa, (i >> 2) * 8192 + (i & 3) * 1024);
      }
      SBAR(); WAITV(gq == 0 ? 26 : gq == 1 ? 18 : gq == 2 ? 10 : 0);
      if (gq == 3) BST(17);
      auto gbad = [&]() {
#pragma unroll
        for (int i = 0; i < 4; ++i) asm volatile("" : "+v"(pv[gq * 4 + i][0]), "+v"(pv[gq * 4 + i][1]));
        unsigned m = umax3(pv[gq * 4][0][0], pv[gq * 4][1][0], pv[gq * 4 + 1][0][0]);
        m = umax3(m, pv[gq * 4 + 1][1][0], pv[gq * 4 + 2][0][0]);
        m = umax3(m, pv[gq * 4 + 2][1][0], pv[gq * 4 + 3][0][0]);
        m = max(m, pv[gq * 4 + 3][1][0]);
        if (gq == 3) { asm volatile("" : "+v"(zv0), "+v"(zv1)); m = umax3(m, zv0[0], zv1[0]); }
        return __ballot(m == POIS) != 0ull;
      };
      if (__builtin_expect(gbad(), 0)) {
        _Pragma("unroll 1") for (unsigned it = 0; it < SPIN; ++it) {
          RETRY();
          SLEEP(2);
          if (gq == 3) ZLD();
          GLD(gq);
          WAITV(0);
          if (!gbad()) break;
        }
      }
      if (gq == 3) {
#pragma unroll
        for (int e = 0; e < 4; ++e) { z[e] = __uint_as_float(zv0[e]); z[4 + e] = __uint_as_float(zv1[e]); }
        SBAR();
      }
#pragma unroll
      for (int i = 0; i < 4; ++i)
#pragma unroll
        for (int t = 0; t < 4; ++t) {
          gs[gq][t] = MFMA16(S8(sel[(t ^ (t >> 1)) & 1]), S8(pv[gq * 4 + i][t >> 1]), i ? gs[gq][t] : (f4)(0.f));
          if (gq == 3) {
            SBAR();
            if (i + t == 0) asm volatile("s_nop 1");
            asm volatile("v_add_f32_dpp %0, %0, %0 row_ror:%2 row_mask:0xf bank_mask:0xf\n v_add_f32_dpp %1, %1, %1 row_ror:%2 row_mask:0xf bank_mask:0xf" : "+v"(z[2 * t]), "+v"(z[2 * t + 1]) : "i"(8 >> i));
            SBAR();
          }
        }
      if (gq == 2) {
#pragma unroll
        for (int t = 0; t < 4; ++t) { gs[0][t] += gs[1][t]; asm volatile("" : "+v"(gs[0][t])); }
      }
    }
    STMP(3);
    WAITL(0);
#pragma unroll
    for (int i = 0; i < 8; i += 2) asm volatile("" : "+v"(pq[i]), "+v"(pq[i + 1]));
    u4 kA[2], zA;
#pragma unroll
    for (int e = 0; e < 4; ++e) zA[e] = pk2(z[2 * e], z[2 * e + 1]);
    SBAR();
#pragma unroll
    for (int t = 0; t < 4; ++t) gs[0][t] += gs[2][t] + gs[3][t];
    SBAR();
    f4 dn[8], o0[8], o1[8]; float inv[8];
#pragma unroll
    for (int i = 0; i < 8; ++i) {
      dn[i] = MFMA16(S8(zA), S8(pq[i]), eps);
      SBAR();
      kA[i >> 2][i & 3] = pk2(gs[0][i >> 1][(i & 1) * 2], gs[0][i >> 1][(i & 1) * 2 + 1]);
      SBAR();
    }
#pragma unroll
    for (int i = 0; i < 8; ++i) {
      o0[i] = MFMA16(S8(kA[0]), S8(pq[i]), (f4)(0.f)); SBAR();
      o1[i] = MFMA16(S8(kA[1]), S8(pq[i]), (f4)(0.f)); SBAR();
      inv[i] = __builtin_amdgcn_rcpf(dn[i][0]); asm volatile("" ::"v"(dn[i])); SBAR();
    }
    BST(18);
    u4 op = sel[0];
#pragma unroll
    for (int i = 0; i < 8; ++i) {
      o0[i] *= inv[i]; o1[i] *= inv[i];
      u4 o; o[0] = min(pk2(o0[i][0], o0[i][1]), POIS - 1u); o[1] = pk2(o0[i][2], o0[i][3]); o[2] = pk2(o1[i][0], o1[i][1]); o[3] = pk2(o1[i][2], o1[i][3]);
      asm volatile("" : "+v"(o) : "v"(op));
      ST128(o, (i & 4 ? ofp : ofo) + (i & 1) * 256 + (i & 2) * 1024);
      op = o;
      SBAR();
    }
    lsig(myFL, 4);
    STMP(4);
    STFLUSH();
    asm volatile(".Lend_l:");
  }

}

extern "C" void kernel_launch(void* const* d_in, const int* in_sizes, int n_in, void* d_out, int out_size,
                              void* d_ws, size_t ws_size, hipStream_t stream) {
  (void)hipFuncSetAttribute((const void*)fused, hipFuncAttributeMaxDynamicSharedMemorySize, LDS_SZ);
  fused<<<256, 512, LDS_SZ, stream>>>((const u16*)d_in[0], (const u16*)d_in[1], (const u16*)d_in[4], (char*)d_ws, (const u16*)d_in[2],
                                      (const u16*)d_in[5], (const u16*)d_in[6], (const u16*)d_in[7], (u16*)d_out);
}
```

```cpp
#define EVW w
#define EVG w==0
#define EVX 0
#include <hip/hip_runtime.h>
#include <stdint.h>

typedef unsigned short u16;
typedef short s4 __attribute__((ext_vector_type(4)));
typedef short s8 __attribute__((ext_vector_type(8)));
typedef float f4 __attribute__((ext_vector_type(4)));
typedef float f16v __attribute__((ext_vector_type(16)));
typedef float f2 __attribute__((ext_vector_type(2)));
typedef unsigned u4 __attribute__((ext_vector_type(4)));
typedef unsigned u2 __attribute__((ext_vector_type(2)));
typedef unsigned u8v __attribute__((ext_vector_type(8)));
typedef __bf16 bf2 __attribute__((ext_vector_type(2)));
#define LAS __attribute__((address_space(3)))
#define DEV __device__ __forceinline__

constexpr int D = 1024, F = 3584;
#define NOMOE 0
#define KD 215
#define KS 209
#define OD 175
#define OHP 1
#define FS 128
#define GSL 8
#define YSL 16
#define TP1 0
#define TP2 3
#define TLBW 0
#define FULL 1
#define HPRIO 1
#define OWN 1
#define NOPOIS 0
#define CAP 7
#define RD 4
#define DYN 1
#define JX 0
#define NOSKIP 0
#define GRP 8
#define ADV 15
#define HB 1
#define RT 1
#define WD 0
#define WU1 12
#define WU2 22
#define PL 0
#define PO 0
#define WG 0
#define DS 0
#define D1 16
#define D2 2
#define HKS 2
#define LW1 1
#define HW1 2
#define HS0 0
#define GPV 2
#define XO 1
#define TP0 3
#define G1 0x31
#define G1S 4
#define G2S 8
#define TD 1
#define YD 170
#define WU0 4
#define WE 12
#define HT 115
#define CF 19
#define KL 145
#define OL 115
#define CL 0
#define WS0 4
#define OM 0
#define HG 128
#define U0 0
#define U3 5
#define TL 0
#define MH 0
#define HWT 3
#define YG 0
#define GG 1
#define PV 1
#define U1 4
#define U2 2
#define QD 1
#define LP0 3
#define LP1 3
#define WA 8
#define PW 3
#define WB 24
#define ROT 0

constexpr unsigned WS_E = 0;
constexpr unsigned WS_YF = 524288;
constexpr unsigned WS_CAND = 12288;
constexpr unsigned WS_RG = 49152;
constexpr unsigned WS_ST = 65536;
constexpr unsigned WS_HK = 589824;
constexpr unsigned WS_ZP = 131072;
constexpr unsigned WS_CS = 196608;
constexpr unsigned WS_OH = 458752;
constexpr unsigned WS_KVP = 1u << 20;
constexpr unsigned WS_GP = 2u << 20;
constexpr unsigned WS_YP = 3u << 20;
constexpr unsigned WS_OF = 5u << 20;
constexpr unsigned WS_SZ = 9u << 20;
#define NR 4
#define RGO(tag) ((64u << 20) + ((tag) & (NR - 1u)) * (10u << 20))
#define MKWS(tag) char* const wsx = ws + RGO(tag); const __amdgpu_buffer_rsrc_t wsr = __builtin_amdgcn_make_buffer_rsrc(wsx, 0, WS_SZ, 0x00020000)

constexpr int STG = 40960;
constexpr int L_KVX = 0;
constexpr int L_PQ = 12288;
constexpr int L_M = 28672, L_RS = L_M + 512, L_CS = L_RS + 512, L_SEL = L_CS + 768, L_FL = L_SEL + 64;
constexpr int L_HX = 30720, L_RL = 31744, L_WO = 32768;
constexpr int LDS_SZ = 163840;
constexpr unsigned POIS = 0xFFFFFFFFu;

DEV unsigned pk2(float a, float b) { bf2 v; v[0] = (__bf16)a; v[1] = (__bf16)b; return __builtin_bit_cast(unsigned, v); }
DEV float lo(unsigned u) { return __uint_as_float(u << 16); }
DEV float hi(unsigned u) { return __uint_as_float(u & 0xFFFF0000u); }
DEV float rbf(float a) { return lo(pk2(a, a)); }
DEV float dot2(unsigned a, unsigned b, float c) { return __builtin_amdgcn_fdot2_f32_bf16(__builtin_bit_cast(bf2, a), __builtin_bit_cast(bf2, b), c, false); }
#define RD128(dst, addr, OFF) asm volatile("ds_read_b128 %0, %1 offset:%2" : "=v"(dst) : "v"(addr), "i"(OFF) : "memory")
#define RDTR(dst, addr, OFF) asm volatile("ds_read_b64_tr_b16 %0, %1 offset:%2" : "=v"(dst) : "v"(addr), "i"(OFF) : "memory")
#define SBAR() __builtin_amdgcn_sched_barrier(0)
#define CAT(x, y) __builtin_shufflevector(x, y, 0, 1, 2, 3, 4, 5, 6, 7)
#define WAITV(n) asm volatile("s_waitcnt vmcnt(%0)" ::"i"(n) : "memory")
#define WAITL(n) asm volatile("s_waitcnt lgkmcnt(%0)" ::"i"(n) : "memory")
#define MEMB() asm volatile("" ::: "memory")
#define SLEEP(n) asm volatile("s_sleep %0" ::"i"(n) : "memory")
#define BAR() asm volatile("s_barrier" ::: "memory")
#define GL0(vo, sb, m0v) asm volatile("s_mov_b32 m0, %2\n s_nop 0\n global_load_lds_dwordx4 %0, %1" ::"v"(vo), "s"(sb), "s"(m0v) : "memory")
#define GLI(vo, sb, OFF) asm volatile("global_load_lds_dwordx4 %0, %1 offset:%2" ::"v"(vo), "s"(sb), "i"(OFF) : "memory")
#define GL4(v, sb, mm, MO) do { asm volatile("s_add_i32 m0, %2, %3\n s_nop 0\n global_load_lds_dwordx4 %0, %1" ::"v"((v)[0]), "s"(sb), "s"(mm), "i"(MO) : "memory", "scc"); \
      GLI((v)[1], sb, 1024); GLI((v)[2], sb, 2048); GLI((v)[3], sb, 3072); } while (0)
#define VFW(i) ((8 * (i) + (l >> 3)) * 2048 + (((l & 7) ^ (((l >> 4) & 1) << 2)) * 16) + (3 - (i)) * 1024)
#define MFMA16(a, b, c) __builtin_amdgcn_mfma_f32_16x16x32_bf16(a, b, c, 0, 0, 0)
#define S8(x) __builtin_bit_cast(s8, x)
#define LD128(off) __builtin_amdgcn_raw_buffer_load_b128(wsr, (off), 0, 16)
#define LD64(off) __builtin_amdgcn_raw_buffer_load_b64(wsr, (off), 0, 16)
#define ST128(v, off) __builtin_amdgcn_raw_buffer_store_b128((v), wsr, (off), 0, 16)
#define ST64(v, off) __builtin_amdgcn_raw_buffer_store_b64((v), wsr, (off), 0, 16)
#define OFA(i) (WS_OF + ((mb * 32 + hd) * 8 + (i)) * 1024 + l * 16)
#define POISON(i) ST128(pz, (i) < 2 ? WS_KVP + (hd * 16 + mb) * 2048 + (i) * 1024 + l * 16 : (i) == 2 ? WS_ZP + (hd * 16 + mb) * 128 + (l & 7) * 16 : OFA((i) - 3))
#define NOW() ((unsigned)__builtin_amdgcn_s_memrealtime())
constexpr unsigned SPIN = 1u << 21;
DEV const char* uptr(const char* p) {
  unsigned long long v = (unsigned long long)p;
  unsigned lo_ = __builtin_amdgcn_readfirstlane((unsigned)v), hi_ = __builtin_amdgcn_readfirstlane((unsigned)(v >> 32));
  return (const char*)(((unsigned long long)hi_ << 32) | lo_);
}
DEV const char* padd(const char* p, unsigned o) {
  unsigned lo_, hi_;
  asm("s_add_u32 %0, %2, %4\n s_addc_u32 %1, %3, 0" : "=&s"(lo_), "=s"(hi_) : "s"((unsigned)(unsigned long long)p), "s"((unsigned)((unsigned long long)p >> 32)), "s"(o) : "scc");
  return (const char*)(((unsigned long long)hi_ << 32) | lo_);
}
DEV void lsig(unsigned addr, unsigned val) { asm volatile("s_waitcnt lgkmcnt(0)\n ds_write_b32 %0, %1" ::"v"(addr), "v"(val) : "memory"); }
template <int SL = 0> DEV void lwait(unsigned addr, unsigned mask, unsigned target) {
  _Pragma("unroll 1") for (unsigned it = 0; it < SPIN; ++it) {
    unsigned v;
    asm volatile("ds_read_b32 %0, %1\n s_waitcnt lgkmcnt(0)" : "=v"(v) : "v"(addr) : "memory");
    if (((unsigned)__ballot(v < target) & mask) == 0u) break;
    if (SL) SLEEP(SL);
  }
}
DEV unsigned nopois(unsigned a) { return min(a, POIS - 1u); }
DEV u4 tagf(f4 v, unsigned tag) {
  u4 r;
#pragma unroll
  for (int e = 0; e < 4; ++e) r[e] = __builtin_amdgcn_perm(__float_as_uint(v[e]), tag, 0x07060500u + e);
  return r;
}
DEV void untag(u4& v, unsigned tag, unsigned& bad) {
#pragma unroll
  for (int e = 0; e < 4; ++e) v[e] ^= (tag >> (8 * e)) & 255u;
  bad |= v[0] | v[1]; bad |= v[2] | v[3];
}
DEV unsigned umax3(unsigned a, unsigned b, unsigned c) { return max(max(a, b), c); }
template <int C> DEV float dppf(float v) { return __int_as_float(__builtin_amdgcn_update_dpp(0, __float_as_int(v), C, 0xf, 0xf, false)); }
DEV unsigned wave_max(unsigned v) {
  v = max(v, (unsigned)__builtin_amdgcn_update_dpp(0, (int)v, 0x111, 0xf, 0xf, false));
  v = max(v, (unsigned)__builtin_amdgcn_update_dpp(0, (int)v, 0x112, 0xf, 0xf, false));
  v = max(v, (unsigned)__builtin_amdgcn_update_dpp(0, (int)v, 0x114, 0xf, 0xf, false));
  v = max(v, (unsigned)__builtin_amdgcn_update_dpp(0, (int)v, 0x118, 0xf, 0xf, false));
  v = max(v, (unsigned)__builtin_amdgcn_update_dpp(0, (int)v, 0x142, 0xa, 0xf, false));
  v = max(v, (unsigned)__builtin_amdgcn_update_dpp(0, (int)v, 0x143, 0xc, 0xf, false));
  return (unsigned)__builtin_amdgcn_readlane((int)v, 63);
}
DEV unsigned phi2(f2 u, f2 rs, f2 rl) {
  f2 a = u * rl, p = u * rs + 1.f, r;
  r[0] = __builtin_amdgcn_fmed3f(__builtin_amdgcn_exp2f(a[0]), p[0], 1.f);
  r[1] = __builtin_amdgcn_fmed3f(__builtin_amdgcn_exp2f(a[1]), p[1], 1.f);
  r += 1e-6f;
  return pk2(r[0], r[1]);
}

#define STDECL unsigned rt_ = 0, rt2_ = 0, st_[16] = {0, 0, 0, 0, 0, 0, 0, 0, 0, 0, 0, 0, 0, 0, 0, 0}
#define STMP(i) st_[i] = (unsigned)__builtin_amdgcn_s_memrealtime()
#define BST(i)
#undef HB
#define HB 0
#define BSTV cw
#define RETRY() (++rt_)
#define RETRY2() (++rt2_)
#define STFLUSH() do { const int b_ = blockIdx.x; if (w == EVW && (EVG)) { const unsigned v_ = min((unsigned)(EVX), 300000u); _Pragma("unroll 1") for (unsigned it = 0; it < SPIN; ++it) { if (NOW() - st_[0] >= 4500u + v_) break; SLEEP(1); } } } while (0)

DEV float half_sum(float v) {
  asm volatile("s_nop 1\n v_add_f32_dpp %0, %0, %0 row_ror:8 row_mask:0xf bank_mask:0xf\n s_nop 1\n v_add_f32_dpp %0, %0, %0 row_ror:4 row_mask:0xf bank_mask:0xf\n"
               "s_nop 1\n v_add_f32_dpp %0, %0, %0 row_ror:2 row_mask:0xf bank_mask:0xf\n s_nop 1\n v_add_f32_dpp %0, %0, %0 row_ror:1 row_mask:0xf bank_mask:0xf\n s_nop 1" : "+v"(v));
  auto r = __builtin_amdgcn_permlane16_swap(__float_as_uint(v), __float_as_uint(v), false, false);
  return __uint_as_float(r[0]) + __uint_as_float(r[1]);
}

struct FQ { s8 a[4]; s4 b[12]; };
template <bool B> struct BC { static constexpr bool v = B; };
template <int I> struct IC { static constexpr int v = I; };

__global__ __launch_bounds__(512) void fused(const u16* __restrict__ x, const u16* __restrict__ wqkv, const u16* __restrict__ wrt,
                                             char* __restrict__ ws_, const u16* __restrict__ wo_, const u16* __restrict__ wg,
                                             const u16* __restrict__ wu, const u16* __restrict__ wd, u16* __restrict__ out) {
  extern __shared__ __attribute__((aligned(16))) char lds[];
  const char *kx, *kq, *kr, *wo; char* ws;
  asm volatile("s_load_dwordx2 %0, %5, 0\n s_load_dwordx2 %1, %5, 8\n s_load_dwordx2 %2, %5, 16\n s_load_dwordx2 %3, %5, 24\n s_load_dwordx2 %4, %5, 32"
               : "=&s"(kx), "=&s"(kq), "=&s"(kr), "=&s"(ws), "=&s"(wo) : "s"(__builtin_amdgcn_kernarg_segment_ptr()));
  SBAR();
  const int w = __builtin_amdgcn_readfirstlane(threadIdx.x >> 6), l = threadIdx.x & 63;
  STDECL;
  STMP(0);
  const int g = blockIdx.x, xcd = g & 7, j = g >> 3, ii = j >> 2;
  const int mb = (xcd & 1) * 8 + ii, hp = (xcd >> 1) * 4 + (j & 3);
  unsigned cw, cwo = (l * 32 + j) * 64; unsigned long long cwb;
  asm volatile("s_getpc_b64 %2\n.Lcode_here:\n v_min_u32 %1, max(.Lend_c - .Lcode_here, .Lend_l - .Lcode_here, .Lend_h - .Lcode_here, .Lend_s - .Lcode_here) - 4, %1\n"
               "s_cmp_lg_u32 %3, 7\n s_cbranch_scc1 .Lskip_w\n global_load_dword %0, %1, %2\n.Lskip_w:" : "=&v"(cw), "+v"(cwo), "=&s"(cwb) : "s"(w) : "memory", "scc");
  SBAR();
  const int lw = w & 1, rot = 2 * xcd + 4 * ((ii + JX * j) & 3);
  const unsigned ldsb = (unsigned)(size_t)(LAS char*)lds;
  unsigned va[4];
#pragma unroll
  for (int i = 0; i < 4; ++i) va[i] = (8 * i + (l >> 3)) * 2048 + (((l & 7) ^ ((i & 1) * 4 + (l >> 4))) * 16) + (3 - i) * 1024;
  const int ao = (mb * 128 + lw * 64) * 2048 - 3072;
  long long ao0 = ao + (rot & 15) * 128;
  unsigned mba = ldsb + lw * 8192;
  const int srow = mb * 128 + hp * 8 + 2 * w + (l >> 5);
  unsigned xo = srow * 2048 + (l & 31) * 16, ro = (l & 31) * 16;
  asm volatile("" : "+v"(va[0]), "+v"(va[1]), "+v"(va[2]), "+v"(va[3]), "+v"(xo), "+v"(ro));
  asm volatile("s_waitcnt lgkmcnt(0)" : "+s"(kx), "+s"(kq), "+s"(kr), "+s"(ws), "+s"(wo), "+s"(ao0), "+s"(mba));
  SBAR();
  asm volatile("s_sub_u32 m0, %6, 4\n s_cmp_gt_u32 m0, 1\n s_cbranch_scc1 .Lskip_l\n s_mov_b32 m0, %5\n s_nop 0\n global_load_lds_dwordx4 %0, %4\n global_load_lds_dwordx4 %1, %4 offset:1024\n"
               "global_load_lds_dwordx4 %2, %4 offset:2048\n global_load_lds_dwordx4 %3, %4 offset:3072\n.Lskip_l:"
               :: "v"(va[0]), "v"(va[1]), "v"(va[2]), "v"(va[3]), "s"(kx + ao0), "s"(mba), "s"(w) : "memory", "scc");
  u4 xa[4], rw[4]; unsigned ep;
  asm volatile("s_cmp_gt_u32 %14, 3\n s_cbranch_scc1 .Lskip_x\n"
               "global_load_dwordx4 %0, %9, %11\n global_load_dwordx4 %1, %9, %11 offset:512\n global_load_dwordx4 %2, %9, %11 offset:1024\n global_load_dwordx4 %3, %9, %11 offset:1536\n"
               "global_load_dwordx4 %4, %10, %12\n global_load_dwordx4 %5, %10, %12 offset:512\n global_load_dwordx4 %6, %10, %12 offset:1024\n global_load_dwordx4 %7, %10, %12 offset:1536\n"
               ".Lskip_x:\n s_load_dword %8, %13, 0"
               : "=&v"(xa[0]), "=&v"(xa[1]), "=&v"(xa[2]), "=&v"(xa[3]), "=&v"(rw[0]), "=&v"(rw[1]), "=&v"(rw[2]), "=&v"(rw[3]), "=&s"(ep)
               : "v"(xo), "v"(ro), "s"(kx), "s"(kr), "s"(ws), "s"(w) : "memory", "scc");
#define GETTAG(t) unsigned t; asm volatile("s_waitcnt lgkmcnt(0)\n s_add_u32 %0, %1, 1" : "=s"(t) : "s"(ep) : "memory", "scc")
  SBAR();
#define PTAB 0xA98FEDC1045B7623ull
#define PHT 0x6624924999924924ull
#define CX 4
  const bool sp = (hp & 7) == 3;
  const int nb = hp;
#define PAIRAT(p) ((int)(PTAB >> (4 * (p))) & 15)
#define HEADAT(s) (2 * PAIRAT((s) >> 1) + ((s) & 1))
  const int ph = (int)(PHT >> (2 * j)) & 3;
#define HQT 0xDCBA098765320410ull
#define CXA 1
#define CHUNK(b) ((b) < 14 ? 4 * (b) + 3 : ((b) - 14) + ((((b) - 14) * 43) >> 7))
  const unsigned adFL = ldsb + L_FL + 4 * (l & 7), myFL = ldsb + L_FL + 4 * w;
  const __amdgpu_buffer_rsrc_t wse = __builtin_amdgcn_make_buffer_rsrc(ws, 0, 64, 0x00020000);
  const int r15 = l & 15, g4 = l >> 4, q = (l & 15) >> 2, p = l & 3;

  if (w != 4 && w != 5) {
  if (w >= 6) {
    const int mw = w - 6;
    GETTAG(tag);
    const int bq = ((int)(HQT >> (4 * hp)) & 15) * 4 + (ii & 3), ksp = (xcd & 1) * 2 + (ii >> 2);
    const int chunk = CHUNK(bq), bg = (CXA || bq < 14) ? bq ^ (w & 1) : bq, chg = CHUNK(bg);
    auto helper = [&](auto SP) {
    constexpr bool spare = decltype(SP)::v, moe = !spare && !NOMOE;
    int nbar = 0;
#define TICK() do { if (HB) { BST(nbar); asm volatile("" : "+v"(BSTV)); } SBAR(); BAR(); SBAR(); ++nbar; } while (0)
#define DTICK(lim) do { if (nbar < (lim)) TICK(); else SLEEP(2); } while (0)
    const int hd = hp * 2 + mw;
    const u4 pz = (u4)(POIS);
    MKWS(tag);
    const int np = l & 7, kset = (l >> 3) & 1;
    u4 gw[4][8], dw[2][8];
    const __amdgpu_buffer_rsrc_t wgr = __builtin_amdgcn_make_buffer_rsrc((void*)(mw ? wu : wg), 0, D * F * 2, 0x00020000);
    const __amdgpu_buffer_rsrc_t wdr = __builtin_amdgcn_make_buffer_rsrc((void*)wd, 0, D * F * 2, 0x00020000);
    const __amdgpu_buffer_rsrc_t wor = __builtin_amdgcn_make_buffer_rsrc((void*)wo, 0, D * D * 2, 0x00020000);
    const unsigned gvo = ((256 * ksp + 32 * kset + 8 * g4) * F + 64 * chg + 8 * np) * 2;
    const unsigned dvo = ((64 * chunk + 8 * g4) * D + 256 * ksp + 128 * mw + 8 * r15) * 2;
    auto burst = [&](int k, int i0 = 0, int i1 = 16) {
      if (moe) {
#pragma unroll
        for (int i = i0; i < i1; ++i) {
          if (k < 2) gw[(2 * k + (i >> 3)) & 3][i & 7] = __builtin_amdgcn_raw_buffer_load_b128(wgr, gvo, (128 * k + 64 * (i >> 3) + (i & 7)) * F * 2, 2);
          else dw[i >> 3][i & 7] = __builtin_amdgcn_raw_buffer_load_b128(wdr, dvo, (32 * (i >> 3) + (i & 7)) * D * 2, 2);
        }
      }
    };
    auto transp = [&](u4(&v)[8]) {
      if (moe) {
        u4 f[8];
#pragma unroll
        for (int c = 0; c < 8; ++c)
#pragma unroll
          for (int e = 0; e < 4; ++e) {
            const unsigned ev = v[2 * e][c >> 1], od = v[2 * e + 1][c >> 1];
            f[c][e] = (c & 1) ? __builtin_amdgcn_perm(od, ev, 0x07060302u) : __builtin_amdgcn_perm(od, ev, 0x05040100u);
          }
#pragma unroll
        for (int c = 0; c < 8; ++c) { v[c] = f[c]; asm volatile("" : "+v"(v[c])); }
      }
    };
    auto pois = [&]() {
      ST128(pz, WS_KVP + (hd * 16 + mb) * 2048 + l * 16); ST128(pz, WS_KVP + (hd * 16 + mb) * 2048 + 1024 + l * 16); ST128(pz, WS_ZP + (hd * 16 + mb) * 128 + (l & 7) * 16);
    };
    unsigned wt0, wt1, wt2 = 0, wt3 = 0;
    auto touch = [&]() {
      const unsigned to = (j * 32 + mw * 16 + (l >> 3)) * 2048 + (xcd >> 1) * 512 + (l & 7) * 64;
      wt0 = __builtin_amdgcn_raw_buffer_load_b32(wor, to, 0, 0); wt1 = __builtin_amdgcn_raw_buffer_load_b32(wor, to, 16384, 0);
      if (TLBW) {
        const __amdgpu_buffer_rsrc_t outr = __builtin_amdgcn_make_buffer_rsrc(out, 0, 2048 * D * 2, 0x00020000);
        wt2 = __builtin_amdgcn_raw_buffer_load_b32(outr, (mb * 128 + mw * 64 + (l & 3) * 16) * 2048 + nb * 128, 0, 0);
        wt3 = __builtin_amdgcn_raw_buffer_load_b32(wsr, (l & 1) ? WS_GP + g * 4096 + mw * 2048 : WS_YP + (g >> 2) * 32768 + (l & 2) * 8192, 0, 0);
      }
    };
    s4 sb[24];
    auto colrd = [&](int h) {
      const unsigned bo = ldsb + (1 + h) * STG + 16384 + (8 * g4 + q) * 128 + (p & 1) * 8;
      const int sw = ((q >> 1) << 2) | ((g4 & 1) << 1);
      const unsigned b0 = bo + (((mw * 4 + (p >> 1)) ^ sw) * 16), b1 = bo + (((mw * 4 + 2 + (p >> 1)) ^ sw) * 16);
#pragma unroll
      for (int n = 0; n < 12; ++n) { RDTR(sb[2 * n], (n & 1) ? b1 : b0, ((n % 6) >> 1) * 8192 + (n / 6) * 4096); RDTR(sb[2 * n + 1], (n & 1) ? b1 : b0, ((n % 6) >> 1) * 8192 + (n / 6) * 4096 + 512); }
      WAITL(0);
      SBAR();
    };
    auto colsum = [&]() {
      const u4 ones = (u4)(0x3F803F80u);
      f4 c[6];
#pragma unroll
      for (int n = 0; n < 12; ++n) c[n % 6] = MFMA16(S8(ones), CAT(sb[2 * n], sb[2 * n + 1]), n >= 6 ? c[n % 6] : (f4)(0.f));
      const u4 csg = (u4){__float_as_uint(g4 ? c[3][0] : c[0][0]), __float_as_uint(g4 ? c[4][0] : c[1][0]), __float_as_uint(g4 ? c[5][0] : c[2][0]), tag};
      WAITV(0);
      asm volatile("" : "+v"(cw));
      ST128(csg, l < 32 ? WS_CS + (hd * 16 + mb) * 512 + l * 16 : WS_SZ);
    };
    u4 stv, cv[8], cd; float cs[3];
    auto gload = [&]() {
      stv = LD128(WS_ST + (mb * 128 + mw * 64 + l) * 16);
#pragma unroll
      for (int i = 0; i < 8; ++i) cv[i] = LD128(WS_CS + hd * 8192 + i * 1024 + l * 16);
    };
    auto gbad = [&]() {
      unsigned bad = (stv[1] ^ tag) | (stv[3] ^ tag);
#pragma unroll
      for (int i = 0; i < 8; ++i) bad |= cv[i][3] ^ tag;
      return __ballot(bad != 0u) != 0ull;
    };
    int idx;
    auto gfin = [&]() {
      if (gbad()) { RETRY(); _Pragma("unroll 1") for (unsigned it = 0; it < SPIN; ++it) { SLEEP(2); gload(); if (!gbad()) break; } }
#pragma unroll
      for (int e = 0; e < 3; ++e) {
        float a = __uint_as_float(cv[0][e]);
#pragma unroll
        for (int i = 1; i < 8; ++i) a += __uint_as_float(cv[i][e]);
        auto r = __builtin_amdgcn_permlane32_swap(__float_as_uint(a), __float_as_uint(a), false, false);
        cs[e] = __uint_as_float(r[0]) + __uint_as_float(r[1]);
      }
      asm volatile("" : "+v"(cs[0]), "+v"(cs[1]), "+v"(cs[2]) : "v"(wt0), "v"(wt1), "v"(wt2), "v"(wt3));
    };
    auto cload = [&]() { cd = LD128(WS_CAND + (((hp >> 2) == 2) * 16 + mb) * 128 + (l & 7) * 16); };
    auto cfin = [&]() {
      if (__builtin_expect(__ballot(cd[1] != tag || cd[3] != tag) != 0ull, 0)) {
        RETRY();
        _Pragma("unroll 1") for (unsigned it = 0; it < SPIN; ++it) { SLEEP(4); cload(); if (__ballot(cd[1] != tag || cd[3] != tag) == 0ull) break; }
      }
      cd[0] = max(cd[0], cd[2]);
      idx = (int)(cd[0] & 2047u);
    };
    auto lpub = [&]() {
      *(LAS float*)(lds + L_M + (mw * 64 + l) * 4) = __uint_as_float(stv[0]);
      *(LAS float*)(lds + L_RS + (mw * 64 + l) * 4) = __uint_as_float(stv[2]);
      *(LAS float*)(lds + L_RL + (mw * 64 + l) * 4) = __uint_as_float(stv[2]) * 1.44269504f;
      if (l < 32) {
#pragma unroll
        for (int e = 0; e < 3; ++e) *(LAS float*)(lds + L_CS + mw * 384 + ((3 * g4 + e) * 16 + r15) * 4) = -cs[e];
      }
      if (mw == 0 && l < 8) *(LAS int*)(lds + L_SEL + l * 4) = idx;
      lsig(myFL, 1u);
    };
    u4 xs[4]; float xrs;
    asm volatile("" ::"s"(wg), "s"(wu), "s"(wd), "v"(cw));
    if (HPRIO) __builtin_amdgcn_s_setprio(HPRIO);
    TICK();
    if (spare) {
      TICK();
      pois();
      if (ii >> 2) TICK();
      colrd(ii >> 2);
      if (!(ii >> 2)) TICK();
      u4 kk[8];
      auto kload = [&]() {
#pragma unroll
        for (int i = 0; i < 8; ++i) kk[i] = LD128(WS_RG + mw * 8192 + i * 1024 + l * 16);
      };
      kload();
      TICK();
      _Pragma("unroll 1") for (unsigned it = 0; it < SPIN; ++it) {
        unsigned bad = 0;
#pragma unroll
        for (int i = 0; i < 8; ++i) { bad |= (kk[i][1] ^ tag) | (kk[i][3] ^ tag); asm volatile("" : "+v"(bad)); }
        if (__ballot(bad != 0u) == 0ull) break;
        RETRY();
        kload();
        DTICK(7);
      }
      __builtin_amdgcn_s_setprio(3);
      unsigned m1 = max(kk[0][0], kk[0][2]), m2 = min(kk[0][0], kk[0][2]), m3 = 0;
#pragma unroll
      for (int i = 2; i < 16; ++i) {
        const unsigned a = kk[i >> 1][(i & 1) * 2];
        asm("v_med3_u32 %0, %1, %2, %0" : "+v"(m3) : "v"(m2), "v"(a));
        asm("v_med3_u32 %0, %1, %2, %0" : "+v"(m2) : "v"(m1), "v"(a));
        m1 = max(m1, a);
      }
      unsigned mine8 = 0;
#pragma unroll
      for (int it = 0; it < 8; ++it) {
        const unsigned km = wave_max(m1);
        mine8 = l == it ? km : mine8;
        const bool win = m1 == km;
        if (it < 7) { m1 = win ? m2 : m1; m2 = win ? m3 : m2; m3 = win ? 0u : m3; }
        if (it == 3 && nbar < 7) TICK();
      }
      if (__builtin_expect(__ballot(m1 == 0u) != 0ull, 0)) {
        unsigned lim = 0;
#pragma unroll 1
        for (int it = 0; it < 8; ++it) {
          unsigned m = 0;
#pragma unroll
          for (int i = 0; i < 8; ++i) m = umax3(m, kk[i][0] - lim, kk[i][2] - lim);
          lim += wave_max(m);
          mine8 = l == it ? lim : mine8;
        }
      }
      ST64(((u2){mine8, tag}), l < 8 ? WS_CAND + ((hp >> 3) * 16 + mb) * 128 + (mw ? 7 - l : l) * 16 + mw * 8 : WS_SZ);
      __builtin_amdgcn_s_setprio(HPRIO);
      STMP(2);
      TICK();
      colsum();
      DTICK(9);
      touch();
      while (nbar < 10) TICK();
      gload(); cload();
      if (RT && mw) asm volatile("global_load_dword %0, %1, %2 sc1" : "+v"(cw) : "v"(cwo), "s"(cwb) : "memory");
      TICK(); TICK();
      WAITV(0);
      asm volatile("" : "+v"(cw));
      gfin(); cfin();
      STMP(3);
      TICK(); TICK(); TICK();
      lpub();
    } else {
      if (PV == 0) pois();
      TICK();
      if (PV == 1) pois();
      if (ii >> 2) TICK();
      colrd(ii >> 2);
      TICK();
      if (CL && !(ii >> 2)) TICK();
      colsum();
      if (!CL && !(ii >> 2)) TICK();
      u4 sv; f4 acc[8];
      auto xload = [&]() {
        const u16* xp = x + (size_t)idx * D + 256 * ksp + 32 * kset + 8 * g4;
        sv = LD128(WS_ST + idx * 16);
#pragma unroll
        for (int it = 0; it < 4; ++it) xs[it] = *(const u4*)(xp + 64 * it);
        SBAR();
      };
      auto xnorm = [&]() {
        if (__builtin_expect(__ballot(sv[1] != tag || sv[3] != tag) != 0ull, 0)) {
          _Pragma("unroll 1") for (unsigned it = 0; it < SPIN; ++it) { SLEEP(1); sv = LD128(WS_ST + idx * 16); if (__ballot(sv[1] != tag || sv[3] != tag) == 0ull) break; }
        }
        const float nm = -__uint_as_float(sv[0]);
        xrs = __uint_as_float(sv[2]);
#pragma unroll
        for (int it = 0; it < 4; ++it) {
          float t0, t1, t2, t3; unsigned x0 = xs[it][0], x1 = xs[it][1], x2 = xs[it][2], x3 = xs[it][3];
          asm volatile("v_dot2_f32_bf16 %4, %0, %8, %10\n v_dot2_f32_bf16 %0, %0, %9, %10\n v_dot2_f32_bf16 %5, %1, %8, %10\n v_dot2_f32_bf16 %1, %1, %9, %10\n"
                       "v_dot2_f32_bf16 %6, %2, %8, %10\n v_dot2_f32_bf16 %2, %2, %9, %10\n v_dot2_f32_bf16 %7, %3, %8, %10\n v_dot2_f32_bf16 %3, %3, %9, %10\n"
                       "v_cvt_pk_bf16_f32 %0, %4, %0\n v_cvt_pk_bf16_f32 %1, %5, %1\n v_cvt_pk_bf16_f32 %2, %6, %2\n s_nop 0\n v_cvt_pk_bf16_f32 %3, %7, %3"
                       : "+v"(x0), "+v"(x1), "+v"(x2), "+v"(x3), "=&v"(t0), "=&v"(t1), "=&v"(t2), "=&v"(t3) : "s"(0x00003F80u), "s"(0x3F800000u), "v"(nm));
          xs[it] = (u4){x0, x1, x2, x3};
        }
        asm volatile("s_nop 1" : "+v"(xs[0]), "+v"(xs[1]), "+v"(xs[2]), "+v"(xs[3]), "+v"(xrs));
        SBAR();
      };
      auto gmul = [&](int i0, int i1) {
#pragma unroll
        for (int it = i0; it < i1; ++it)
#pragma unroll
          for (int c = 0; c < 8; ++c) acc[c] = MFMA16(S8(gw[it][c]), S8(xs[it]), it ? acc[c] : (f4)(0.f));
#pragma unroll
        for (int c = 0; c < 8; ++c) asm volatile("" : "+v"(acc[c]));
        SBAR();
      };
      auto gout = [&]() {
        unsigned go = r15 < 8 ? WS_GP + ((chg * 4 + ksp) * 2 + mw) * 2048 + (g4 >> 1) * 1024 + (g4 & 1) * 512 + r15 * 16 : WS_SZ;
        asm volatile("" : "+v"(go));
#pragma unroll
        for (int i = 0; i < 4; ++i) {
          unsigned a[4], b[4];
#pragma unroll
          for (int c = 0; c < 4; ++c) {
            auto r = __builtin_amdgcn_permlane32_swap(__float_as_uint(acc[c][i]), __float_as_uint(acc[c + 4][i]), false, false);
            a[c] = r[0]; b[c] = r[1];
          }
          unsigned s0, s1, s2, s3;
#define AR(d, x, y) "v_add_f32_dpp %" #d ", %" #x ", %" #y " row_ror:8 row_mask:0xf bank_mask:0xf\n"
          asm volatile("s_nop 1\n" AR(0, 8, 4) AR(1, 9, 5) AR(2, 10, 6) AR(3, 11, 7) : "=&v"(s0), "=&v"(s1), "=&v"(s2), "=&v"(s3)
                       : "v"(a[0]), "v"(a[1]), "v"(a[2]), "v"(a[3]), "v"(b[0]), "v"(b[1]), "v"(b[2]), "v"(b[3]));
          ST128(tagf(__builtin_bit_cast(f4, ((u4){s0, s1, s2, s3})), tag), go + i * 128);
        }
        SBAR();
      };
      for (int i = 0; i < ph; ++i) TICK();
      burst(0); touch();
      TICK();
      TICK();
      if (TL) { transp(gw[0]); transp(gw[1]); }
      TICK();
      if (!TL) transp(gw[0]);
      TICK();
      if (!TL) transp(gw[1]);
      TICK();
      if (ph) cload();
      if (RT && mw) asm volatile("global_load_dword %0, %1, %2 sc1" : "+v"(cw) : "v"(cwo), "s"(cwb) : "memory");
      SBAR();
      burst(1);
      TICK();
      TICK(); TICK();
      transp(gw[2]);
      WAITV(0);
      asm volatile("" : "+v"(cw));
      if (ph < 2) TICK();
      transp(gw[3]);
      if (ph < 1) { SBAR(); cload(); TICK(); }
      SBAR();
      if (XO == 0) { cfin(); xload(); gload(); } else { gload(); SBAR(); cfin(); xload(); }
      TICK();
      gfin();
      lpub();
      STMP(12);
      SBAR();
      __builtin_amdgcn_s_setprio(0);
      burst(2, 0, D1);
      STMP(3);
      SBAR();
      if (HS0) SLEEP(HS0);
      if (G1 & 1) { lwait<HW1>(adFL, 0x0Fu, G1 >> 4); if (G1S) SLEEP(G1S); __builtin_amdgcn_s_setprio(TP0); }
      xnorm();
      if (G1 & 2) { lwait<2>(adFL, 0x0Fu, G1 >> 4); if (G1S) SLEEP(G1S); __builtin_amdgcn_s_setprio(TP0); }
      STMP(5);
      gmul(0, 4);
      gout();
      STMP(6);
      SBAR();
    }
    __builtin_amdgcn_s_setprio(TP1);
    STMP(4);
    if (moe) {
      if (D2 == 0) { burst(2, D1, 16); SBAR(); }
      if (D1 >= 8 || D2 == 0) transp(dw[0]); if (D1 == 16 || D2 == 0) transp(dw[1]);
      SBAR();
      lwait<2>(adFL, 0x30u, 3);
      if (G2S) SLEEP(G2S);
      STMP(7);
      if (D2 == 1) { burst(2, D1, 16); SBAR(); }
      f4 acc[8];
      u4 ha[2];
      {
        u4 pg[4][2];
        auto pload = [&]() {
#pragma unroll
          for (int ks = 0; ks < 4; ++ks)
#pragma unroll
            for (int t = 0; t < 2; ++t) pg[ks][t] = LD128(WS_GP + ((chunk * 4 + ks) * 4 + 2 * t + mw) * 1024 + (4 * kset + g4) * 128 + np * 16);
        };
        auto pbad = [&]() {
          unsigned bad = 0;
#pragma unroll
          for (int i = 0; i < 8; ++i) untag(pg[i >> 1][i & 1], tag, bad);
          return __ballot((bad & 255u) != 0u) != 0ull;
        };
        pload();
        SBAR();
        if (D2 == 2) { burst(2, D1, 16); SBAR(); }
        __builtin_amdgcn_s_setprio(TP2);
        if (D1 < 8 && D2 == 1) transp(dw[0]);
        if (D1 < 16 && D2 == 1) transp(dw[1]);
        SBAR();
        if (__builtin_expect(pbad(), 0)) { _Pragma("unroll 1") for (unsigned it = 0; it < SPIN; ++it) { RETRY2(); SLEEP(4); pload(); if (!pbad()) break; } }
        STMP(10);
        f4 gq[2];
#pragma unroll
        for (int t = 0; t < 2; ++t)
          gq[t] = ((__builtin_bit_cast(f4, pg[0][t]) + __builtin_bit_cast(f4, pg[1][t])) + (__builtin_bit_cast(f4, pg[2][t]) + __builtin_bit_cast(f4, pg[3][t]))) * xrs;
        float hv[4];
#pragma unroll
        for (int e = 0; e < 4; ++e) {
          const float gg = rbf(gq[0][e]), uu = rbf(gq[1][e]);
          hv[e] = rbf(gg / (1.f + __expf(-gg))) * uu;
        }
        *(LAS u2*)(lds + L_HX + l * 16 + mw * 8) = (u2){pk2(hv[0], hv[1]), pk2(hv[2], hv[3])};
        lsig(myFL, 2u);
        if (D1 < 16 && D2 == 2) { SBAR(); if (D1 < 8) transp(dw[0]); transp(dw[1]); SBAR(); }
        lwait(adFL, 0xC0u, 2);
        ha[0] = *(const LAS u4*)(lds + L_HX + l * 16); ha[1] = *(const LAS u4*)(lds + L_HX + (l ^ 8) * 16);
      }
#pragma unroll
      for (int it = 0; it < 2; ++it)
#pragma unroll
        for (int c = 0; c < 8; ++c) acc[c] = MFMA16(S8(ha[it]), S8(dw[it][c]), it ? acc[c] : (f4)(0.f));
      if (g4 < 2) {
#pragma unroll
        for (int i = 0; i < 4; ++i) {
          const unsigned yo = WS_YP + ((chunk * 8 + 4 * g4 + i) * D + 256 * ksp + 128 * mw + 4 * r15) * 4;
          ST128(tagf((f4){acc[0][i], acc[1][i], acc[2][i], acc[3][i]}, tag), yo);
          ST128(tagf((f4){acc[4][i], acc[5][i], acc[6][i], acc[7][i]}, tag), yo + 256);
        }
      }
      STMP(11);
    }
    float sg;
    {
      const unsigned mono = cd[0] >> 16, rb = (mono & 0x8000u) ? (mono & 0x7FFFu) : (~mono & 0xFFFFu);
      sg = rbf(1.f / (1.f + __expf(-lo(rb))));
    }
    unsigned mine = (unsigned)__ballot(!NOMOE && l < 8 && (idx >> 7) == mb && (l & 1) == mw);
    if (mine) {
      if (spare) lwait<4>(adFL, 0x30u, 3);
      {
        const u2 f = LD64(WS_OH + (mb * 16 + r15) * 128);
        const unsigned t0 = NOW();
        unsigned rel = f[1] + YD - t0;
        rel = wave_max(f[0] == tag && rel < 600u ? rel : 0u);
        _Pragma("unroll 1") for (unsigned it = 0; it < SPIN; ++it) { if (NOW() - t0 >= rel) break; SLEEP(2); }
      }
      STMP(8);
      lwait<1>(adFL, 0x0Fu, 4);
      __builtin_amdgcn_s_setprio(TP2);
      while (mine) {
        const int t = __builtin_ctz(mine); mine &= mine - 1;
        u4 yv[14];
        _Pragma("unroll 1") for (unsigned it = 0; it < SPIN; ++it) {
          unsigned bad = 0;
#pragma unroll
          for (int i = 0; i < 14; ++i) yv[i] = LD128(WS_YP + (((i * 4 + g4) * 8 + t) * D + (nb >> 1) * 128 + (r15 >> 3) * 64 + (nb & 1) * 32 + (r15 & 7) * 4) * 4);
          SBAR();
#pragma unroll
          for (int i = 0; i < 14; ++i) untag(yv[i], tag, bad);
          if (__ballot((bad & 255u) != 0u) == 0ull) break;
          RETRY();
          SLEEP(YSL);
        }
        f4 y = __builtin_bit_cast(f4, yv[0]);
#pragma unroll
        for (int i = 1; i < 14; ++i) y += __builtin_bit_cast(f4, yv[i]);
        const float gt = __int_as_float(__builtin_amdgcn_readlane(__float_as_int(sg), t));
#pragma unroll
        for (int e = 0; e < 4; ++e) {
          auto r = __builtin_amdgcn_permlane32_swap(__float_as_uint(y[e]), __float_as_uint(y[e]), false, false);
          const float a = __uint_as_float(r[0]) + __uint_as_float(r[1]);
          auto r2 = __builtin_amdgcn_permlane16_swap(__float_as_uint(a), __float_as_uint(a), false, false);
          y[e] = rbf(rbf(__uint_as_float(r2[0]) + __uint_as_float(r2[1])) * gt);
        }
        if (l < 16) *(LAS f4*)(lds + L_KVX + t * 256 + (l & 7) * 32 + (l >> 3) * 16) = y;
      }
    }
    lsig(myFL, 3u);
    STMP(9);
    STFLUSH();
    if (spare) asm volatile(".Lend_s:"); else asm volatile(".Lend_h:");
    };
    if (sp) helper(BC<true>()); else helper(BC<false>());
    return;
  }

  const int wr = w >> 1, wc = w & 1;
  u2 kg; u4 sg4; unsigned tag;
  {
    const int row = srow;
    SBAR(); BST(0); SBAR();
    asm volatile("s_waitcnt vmcnt(4)" : "+v"(xa[0]), "+v"(xa[1]), "+v"(xa[2]), "+v"(xa[3]));
    SBAR(); BST(1); SBAR();
    float s0 = 0, s1 = 0;
#pragma unroll
    for (int c = 0; c < 4; ++c)
#pragma unroll
      for (int e = 0; e < 4; e += 2) { unsigned x0 = xa[c][e], x1 = xa[c][e + 1]; s0 = dot2(x0, 0x3F803F80u, s0); s1 = dot2(x1, 0x3F803F80u, s1); }
    const float m = half_sum(s0 + s1) * (1.f / D);
    f2 v[16], q2 = (f2)(0.f), q3 = (f2)(0.f);
#pragma unroll
    for (int c = 0; c < 16; ++c) v[c] = (f2){lo(xa[c >> 2][c & 3]), hi(xa[c >> 2][c & 3])} - m;
#pragma unroll
    for (int c = 0; c < 16; c += 2) { q2 += v[c] * v[c]; q3 += v[c + 1] * v[c + 1]; }
    q2 += q3;
    const float mr = rbf(m);
    const float rs = 1.f / rbf(sqrtf(rbf(rbf(half_sum(q2[0] + q2[1]) * (1.f / D)) + 1e-5f)));
    float d0 = 0, d1 = 0;
    asm volatile("s_waitcnt vmcnt(0)" : "+v"(rw[0]), "+v"(rw[1]), "+v"(rw[2]), "+v"(rw[3]), "+v"(d0) : "v"(rs));
#pragma unroll
    for (int c = 0; c < 16; c += 2) {
      const f2 e0 = (f2){lo(xa[c >> 2][c & 3]), hi(xa[c >> 2][c & 3])} - mr, e1 = (f2){lo(xa[c >> 2][(c & 3) + 1]), hi(xa[c >> 2][(c & 3) + 1])} - mr;
      const unsigned p0 = pk2(e0[0], e0[1]), p1 = pk2(e1[0], e1[1]);
      const f2 n0 = (f2){lo(p0), hi(p0)} * rs, n1 = (f2){lo(p1), hi(p1)} * rs;
      unsigned c0 = rw[c >> 2][c & 3], c1 = rw[c >> 2][(c & 3) + 1];
      d0 = dot2(pk2(n0[0], n0[1]), c0, d0); d1 = dot2(pk2(n1[0], n1[1]), c1, d1);
    }
    const float d = half_sum(d0 + d1);
    const unsigned rb = pk2(d, d) & 0xFFFFu;
    const unsigned mono = (rb & 0x8000u) ? (~rb & 0xFFFFu) : (rb | 0x8000u);
    SBAR();
    GETTAG(tg); tag = tg;
    kg = (u2){(mono << 16) | (unsigned)row, tag};
    sg4 = (u4){__float_as_uint(mr), tag, __float_as_uint(rs), tag};
  }
  MKWS(tag);
  SBAR(); BST(2); SBAR();
  if ((l & 31) == 0) { ST64(kg, WS_RG + srow * 8); ST128(sg4, WS_ST + srow * 16); }
  unsigned aad[2], bad[2];
#pragma unroll
  for (int s = 0; s < 2; ++s) aad[s] = ldsb + (wr * 64 + r15) * 128 + (((4 * s + g4) ^ ((r15 >> 1) & 7)) * 16);
#pragma unroll
  for (int nh = 0; nh < 2; ++nh) bad[nh] = ldsb + 16384 + (8 * g4 + q) * 128 + (((wc * 4 + 2 * (nh ^ wr) + (p >> 1)) ^ (((q >> 1) << 2) | ((g4 & 1) << 1))) * 16) + (p & 1) * 8;
  f4 acc[4][6];
#pragma unroll
  for (int m = 0; m < 4; ++m)
#pragma unroll
    for (int n = 0; n < 6; ++n) { acc[m][n] = (f4)(0.f); asm volatile("" : "+v"(acc[m][n])); }
#define MF(m, n, Fq) do { if (n < 2) acc[m][n] = MFMA16(CAT(Fq.b[2 * n], Fq.b[2 * n + 1]), Fq.a[m], acc[m][n]); \
     else acc[m][n] = MFMA16(Fq.a[m], CAT(Fq.b[2 * n], Fq.b[2 * n + 1]), acc[m][n]); SBAR(); } while (0)
#define RB(G, n, vb0, vb1, SS, hf) RDTR(G.b[2 * (n) + (hf)], (((n) & 1) ? vb1 : vb0), ((n) >> 1) * 8192 + (SS) * 4096 + (hf) * 512)
#define RA(G, m, va) RD128(G.a[m], va, (m) * 2048)
#define SUB(Fq, G, va, vb0, vb1, SS, PRE) do { \
    WAITL(13); SBAR(); \
    MF(0, 0, Fq); PRE; RA(G, 0, va); SBAR(); \
    WAITL(11); SBAR(); \
    MF(1, 0, Fq); RB(G, 0, vb0, vb1, SS, 0); SBAR(); MF(2, 0, Fq); RB(G, 0, vb0, vb1, SS, 1); SBAR(); MF(3, 0, Fq); RA(G, 1, va); SBAR(); \
    WAITL(12); SBAR(); \
    MF(0, 1, Fq); RA(G, 2, va); SBAR(); MF(1, 1, Fq); RA(G, 3, va); SBAR(); MF(2, 1, Fq); RB(G, 1, vb0, vb1, SS, 0); SBAR(); MF(3, 1, Fq); RB(G, 1, vb0, vb1, SS, 1); SBAR(); \
    WAITL(8); SBAR(); \
    MF(0, 2, Fq); RB(G, 2, vb0, vb1, SS, 0); SBAR(); MF(1, 2, Fq); RB(G, 2, vb0, vb1, SS, 1); SBAR(); MF(2, 2, Fq); RB(G, 3, vb0, vb1, SS, 0); SBAR(); MF(3, 2, Fq); RB(G, 3, vb0, vb1, SS, 1); SBAR(); \
    MF(0, 3, Fq); RB(G, 4, vb0, vb1, SS, 0); SBAR(); MF(1, 3, Fq); RB(G, 4, vb0, vb1, SS, 1); SBAR(); MF(2, 3, Fq); RB(G, 5, vb0, vb1, SS, 0); SBAR(); MF(3, 3, Fq); RB(G, 5, vb0, vb1, SS, 1); SBAR(); \
    MF(0, 4, Fq); MF(1, 4, Fq); MF(2, 4, Fq); MF(3, 4, Fq); MF(0, 5, Fq); MF(1, 5, Fq); MF(2, 5, Fq); MF(3, 5, Fq); } while (0)
#define KSTEP(MODE) do { \
    if (kt == sx) { const unsigned xo = xad + buf * STG; \
      _Pragma("unroll") for (int t = 0; t < 8; ++t) { const unsigned a_ = xo ^ (t * 16); \
        asm volatile("ds_read_b64 %0, %1" : "+v"(xv[t]) : "v"(a_) : "memory"); } \
      WAITL(0); SBAR(); } \
    const int nbuf = (buf + 1) & 3; \
    const unsigned bo = buf * STG, nbo = nbuf * STG; \
    { unsigned va = aad[1] + bo, vb0 = bad[0] + bo, vb1 = bad[1] + bo; SUB(f0, f1, va, vb0, vb1, 1, ); } \
    if (MODE < 2) { \
      unsigned va = aad[0] + nbo, vb0 = bad[0] + nbo, vb1 = bad[1] + nbo; \
      SUB(f1, f0, va, vb0, vb1, 0, if (MODE == 0) { WAITL(0); BAR(); } else asm volatile("ds_write_b32 %0, %1" ::"v"(myFL), "v"(1u) : "memory"); SBAR()); \
    } else { asm volatile("ds_write_b32 %1, %2\n ds_read_b32 %0, %3\n s_waitcnt lgkmcnt(2)" : "=&v"(fl0) : "v"(myFL), "v"(2u), "v"(adFL) : "memory"); SBAR(); \
      MF(0, 2, f1); MF(1, 2, f1); MF(2, 2, f1); MF(3, 2, f1); MF(0, 3, f1); MF(1, 3, f1); MF(2, 3, f1); MF(3, 3, f1); \
      MF(0, 4, f1); MF(1, 4, f1); MF(2, 4, f1); MF(3, 4, f1); MF(0, 5, f1); MF(1, 5, f1); MF(2, 5, f1); MF(3, 5, f1); \
      MF(0, 0, f1); MF(1, 0, f1); MF(2, 0, f1); MF(3, 0, f1); MF(0, 1, f1); MF(1, 1, f1); MF(2, 1, f1); MF(3, 1, f1); } \
    buf = nbuf; } while (0)
#define SUBK(Fq, G, va, vb0, vb1, SS, W0, PRE) do { \
    WAITL(W0); SBAR(); \
    MF(0, 2, Fq); PRE; RA(G, 0, va); SBAR(); \
    WAITL(7); SBAR(); \
    MF(1, 2, Fq); RB(G, 2, vb0, vb1, SS, 0); SBAR(); MF(2, 2, Fq); RB(G, 2, vb0, vb1, SS, 1); SBAR(); MF(3, 2, Fq); RA(G, 1, va); SBAR(); \
    WAITL(8); SBAR(); \
    MF(0, 3, Fq); RA(G, 2, va); SBAR(); MF(1, 3, Fq); RA(G, 3, va); SBAR(); MF(2, 3, Fq); RB(G, 3, vb0, vb1, SS, 0); SBAR(); MF(3, 3, Fq); RB(G, 3, vb0, vb1, SS, 1); SBAR(); \
    WAITL(10); SBAR(); \
    MF(0, 4, Fq); RB(G, 4, vb0, vb1, SS, 0); SBAR(); MF(1, 4, Fq); RB(G, 4, vb0, vb1, SS, 1); SBAR(); MF(2, 4, Fq); RB(G, 5, vb0, vb1, SS, 0); SBAR(); MF(3, 4, Fq); RB(G, 5, vb0, vb1, SS, 1); SBAR(); \
    WAITL(12); SBAR(); \
    MF(0, 5, Fq); MF(1, 5, Fq); MF(2, 5, Fq); MF(3, 5, Fq); } while (0)
#define RQ(G, va, vb0, vb1, SS) do { RA(G, 0, va); RB(G, 0, vb0, vb1, SS, 0); RB(G, 0, vb0, vb1, SS, 1); RA(G, 1, va); RA(G, 2, va); RA(G, 3, va); \
    RB(G, 1, vb0, vb1, SS, 0); RB(G, 1, vb0, vb1, SS, 1); SBAR(); } while (0)
#define MQ(Fq) do { MF(0, 0, Fq); MF(1, 0, Fq); MF(2, 0, Fq); MF(3, 0, Fq); MF(0, 1, Fq); MF(1, 1, Fq); MF(2, 1, Fq); MF(3, 1, Fq); } while (0)
  u2 xv[8];
#pragma unroll
  for (int t = 0; t < 8; ++t) asm volatile("" : "=v"(xv[t]));
  const int sx = (nb - rot) & 15;
  const unsigned xad = ldsb + (w * 32 + (l & 31)) * 128 + ((r15 >> 1) & 7) * 16 + (l >> 5) * 8;
  STMP(1);
  WAITL(0);
  BAR();
  STMP(2);
  WAITL(0);
  FQ f0, f1;
  { unsigned va = aad[0], vb0 = bad[0], vb1 = bad[1];
    RA(f0, 0, va); RB(f0, 0, vb0, vb1, 0, 0); RB(f0, 0, vb0, vb1, 0, 1); RA(f0, 1, va); RA(f0, 2, va); RA(f0, 3, va);
    RB(f0, 1, vb0, vb1, 0, 0); RB(f0, 1, vb0, vb1, 0, 1); RB(f0, 2, vb0, vb1, 0, 0); RB(f0, 2, vb0, vb1, 0, 1); RB(f0, 3, vb0, vb1, 0, 0); RB(f0, 3, vb0, vb1, 0, 1);
    RB(f0, 4, vb0, vb1, 0, 0); RB(f0, 4, vb0, vb1, 0, 1); RB(f0, 5, vb0, vb1, 0, 0); RB(f0, 5, vb0, vb1, 0, 1); SBAR(); }
  unsigned sad = ldsb + L_M + wr * 256 + g4 * 16, cad = ldsb + L_CS + wc * 384 + r15 * 4 + wr * 64;
  const int hd = hp * 2 + wc;
  unsigned kvo = WS_KVP + (hd * 16 + mb) * 2048 + wr * 1024 + l * 16, zo = r15 == 0 ? WS_ZP + (hd * 16 + mb) * 128 + g4 * 32 + wr * 16 : WS_SZ;
  unsigned xw = ldsb + L_KVX + w * 3072 + l * 16, xr = ldsb + L_KVX + (w ^ 2) * 3072 + l * 16;
  asm volatile("s_waitcnt lgkmcnt(0)" : "+v"(sad), "+v"(cad), "+v"(kvo), "+v"(zo), "+v"(xw), "+v"(xr) : : "memory"); SBAR();
  int buf = 0; unsigned fl0;
#pragma unroll 1
  for (int kt = 0; kt < 14; ++kt) KSTEP(0);
  STMP(14); SBAR();
  f4 sm[4], sr[4], sl[4]; f2 ck, cv;
  unsigned c2 = 2u, c3 = 3u, vfl = myFL, b30, b31;
  u4 ones = (u4)(0x3F803F80u);
#define SC0() asm volatile("ds_read_b32 %0, %2 offset:128\n ds_read_b32 %1, %2 offset:256" : "=&v"(ck[0]), "=&v"(cv[0]) : "v"(cad) : "memory")
#define SC1() asm volatile("ds_read_b32 %0, %2 offset:128\n ds_read_b32 %1, %2 offset:256" : "=&v"(ck[1]), "=&v"(cv[1]) : "v"(cad ^ 64) : "memory")
  const unsigned b20 = bad[0] + 2 * STG, b21 = bad[1] + 2 * STG;
#define SM(m) do { RD128(sm[m], sad, (m) * 64); RD128(sl[m], sad, L_RL - L_M + (m) * 64); RD128(sr[m], sad, L_RS - L_M + (m) * 64); } while (0)
  {
    { const unsigned va = aad[1] + 2 * STG; SUBK(f0, f1, va, b20, b21, 1, 6, asm volatile("" ::"v"(f0.b[0]), "v"(f0.b[1]), "v"(f0.b[2]), "v"(f0.b[3]))); }
    { const unsigned va = aad[0] + 3 * STG; b30 = bad[0] + 3 * STG; b31 = bad[1] + 3 * STG; SUBK(f1, f0, va, b30, b31, 0, 9, asm volatile("" : "+v"(c2), "+v"(c3), "+v"(vfl))); }
    STMP(7); SBAR();
    { const unsigned va = aad[1] + 3 * STG; SUBK(f0, f1, va, b30, b31, 1, 9, asm volatile("" : "+v"(ones))); }
    asm volatile("ds_write_b32 %1, %2\n ds_read_b32 %0, %3\n s_waitcnt lgkmcnt(8)" : "=&v"(fl0) : "v"(vfl), "v"(c2), "v"(adFL) : "memory"); SBAR();
    MF(0, 2, f1); SC0(); SBAR(); MF(1, 2, f1); SC1(); SBAR(); MF(2, 2, f1); MF(3, 2, f1); WAITL(10); SBAR();
    MF(0, 3, f1); SM(0); SBAR(); MF(1, 3, f1); MF(2, 3, f1); SM(1); SBAR(); MF(3, 3, f1);
    WAITL(12); SBAR();
    MF(0, 4, f1); SM(2); SBAR(); MF(1, 4, f1); MF(2, 4, f1); SM(3); SBAR(); MF(3, 4, f1); MF(0, 5, f1); MF(1, 5, f1); MF(2, 5, f1); MF(3, 5, f1);
  }
  asm volatile("s_waitcnt lgkmcnt(0)" : "+v"(fl0) : : "memory");
  if (__builtin_expect(((unsigned)__ballot(fl0 < 1u) & 0xC0u) != 0u, 0)) {
    lwait(adFL, 0xC0u, 1);
    SC0(); SC1(); SM(0); SM(1); SM(2); SM(3); WAITL(0);
  }
  asm volatile("" : "+v"(ck), "+v"(cv), "+v"(sm[0]), "+v"(sm[1]), "+v"(sm[2]), "+v"(sm[3]), "+v"(sr[0]), "+v"(sr[1]), "+v"(sr[2]), "+v"(sr[3]));
  asm volatile("" : "+v"(sl[0]), "+v"(sl[1]), "+v"(sl[2]), "+v"(sl[3]));
  STMP(3);
  __builtin_amdgcn_s_setprio(2);
  {
    f4 kv[4], zt[2];
    u4 kf[2][2], vf[2][2];
#define MKV(t, i) do { if ((i) == 2 || (i) == 5) zt[(i) == 2] = MFMA16(S8(kf[t][(i) == 2]), S8(ones), (t) ? zt[(i) == 2] : (f4)(0.f)); \
      else kv[((i) + 2) % 5] = MFMA16(S8(kf[t][(((i) + 2) % 5) & 1]), S8(vf[t][(((i) + 2) % 5) >> 1]), (t) ? kv[((i) + 2) % 5] : (f4)(0.f)); SBAR(); } while (0)
#pragma unroll
    for (int m = 0; m < 4; ++m) {
      const int t = m >> 1, mm = m & 1;
      f4 ea[2], pp[2];
#pragma unroll
      for (int a = 0; a < 2; ++a) {
        const f4 uv = (acc[m][4 + a] + sm[m] * cv[a]) * sr[m];
        vf[t][a][2 * mm] = pk2(uv[0], uv[1]); vf[t][a][2 * mm + 1] = pk2(uv[2], uv[3]);
        const f4 uk = acc[m][2 + a] + sm[m] * ck[a];
        ea[a] = uk * sl[m]; pp[a] = uk * sr[m] + 1.f;
      }
      SBAR();
#pragma unroll
      for (int e = 0; e < 8; ++e) { if (t && (e == 0 || e == 3 || e == 6)) MKV(0, 3 * mm + e / 3); ea[e >> 2][e & 3] = __builtin_amdgcn_exp2f(ea[e >> 2][e & 3]); SBAR(); }
#pragma unroll
      for (int e = 0; e < 8; ++e) pp[e >> 2][e & 3] = __builtin_amdgcn_fmed3f(ea[e >> 2][e & 3], pp[e >> 2][e & 3], 1.f);
      SBAR();
#pragma unroll
      for (int a = 0; a < 2; ++a) { pp[a] += 1e-6f; kf[t][a][2 * mm] = pk2(pp[a][0], pp[a][1]); kf[t][a][2 * mm + 1] = pk2(pp[a][2], pp[a][3]); }
      SBAR();
    }
    MKV(1, 0); MKV(1, 1); MKV(1, 2); MKV(1, 3);
    asm volatile("ds_write_b128 %0, %1" ::"v"(xw), "v"(kv[2]) : "memory"); SBAR();
    MKV(1, 4);
    asm volatile("ds_write_b128 %0, %1 offset:1024" ::"v"(xw), "v"(kv[3]) : "memory"); SBAR();
    MKV(1, 5);
    asm volatile("ds_write_b128 %0, %1 offset:2048\n ds_write_b32 %2, %3" ::"v"(xw), "v"(zt[1]), "v"(vfl), "v"(c3) : "memory");
    f4 r0, r1, rz;
    _Pragma("unroll 1") for (unsigned it = 0; it < SPIN; ++it) {
      unsigned v;
      asm volatile("ds_read_b32 %0, %4\n ds_read_b128 %1, %5 offset:1024\n ds_read_b128 %2, %5\n ds_read_b128 %3, %5 offset:2048\n s_waitcnt lgkmcnt(0)"
                   : "=&v"(v), "=&v"(r0), "=&v"(r1), "=&v"(rz) : "v"(adFL), "v"(xr) : "memory");
      if ((((unsigned)__ballot(v < 3u) >> (w ^ 2)) & 1u) == 0u) break;
    }
    const f4 s0 = kv[0] + r0, s1 = kv[1] + r1, sz = zt[0] + rz;
    u4 o; o[0] = min(pk2(s0[0], s0[1]), POIS - 1u); o[1] = pk2(s0[2], s0[3]); o[2] = pk2(s1[0], s1[1]); o[3] = pk2(s1[2], s1[3]);
    ST128(o, kvo);
    u4 zq = __builtin_bit_cast(u4, sz); zq[0] = min(zq[0], POIS - 1u);
    ST128(zq, zo);
  }
  STMP(4);
  {
    SBAR();
    const LAS float* Lm = (const LAS float*)(lds + L_M) + wr * 64 + r15;
    const LAS float* Lc = (const LAS float*)(lds + L_CS) + wc * 96 + 4 * g4;
    { const unsigned va = aad[0] + 2 * STG; RQ(f0, va, b20, b21, 0); }
    { const unsigned va = aad[1] + 2 * STG; RQ(f1, va, b20, b21, 1); }
    WAITL(8); SBAR(); MQ(f0);
    { const unsigned va = aad[0] + 3 * STG; RQ(f0, va, b30, b31, 0); }
    WAITL(8); SBAR(); MQ(f1);
    { const unsigned va = aad[1] + 3 * STG; RQ(f1, va, b30, b31, 1); }
    WAITL(8); SBAR();
    const f4 c0 = *(const LAS f4*)(Lc + 16 * wr), c1 = *(const LAS f4*)(Lc + 16 - 16 * wr);
    float mv[4], rv[4], rl[4];
#pragma unroll
    for (int m = 0; m < 4; ++m) { mv[m] = Lm[m * 16]; rv[m] = Lm[m * 16 + (L_RS - L_M) / 4]; rl[m] = Lm[m * 16 + (L_RL - L_M) / 4]; }
    SBAR(); MQ(f0);
    if (sx >= 14) {
      const unsigned xo = xad + (sx - 12) * STG;
#pragma unroll
      for (int t = 0; t < 8; ++t) { const unsigned a_ = xo ^ (t * 16); asm volatile("ds_read_b64 %0, %1" : "+v"(xv[t]) : "v"(a_) : "memory"); }
    }
    WAITL(0); SBAR(); MQ(f1);
    LAS char* pw = (LAS char*)lds + L_PQ + wr * 8192 + wc * 4096 + l * 16;
#pragma unroll
    for (int m = 0; m < 4; ++m) {
      const f4 u0 = acc[m][0] + c0 * mv[m], u1 = acc[m][1] + c1 * mv[m];
      *(LAS u2*)(pw + wr * 8 + m * 1024) = (u2){phi2((f2){u0[0], u0[1]}, (f2)(rv[m]), (f2)(rl[m])), phi2((f2){u0[2], u0[3]}, (f2)(rv[m]), (f2)(rl[m]))};
      *(LAS u2*)(pw + 8 - wr * 8 + m * 1024) = (u2){phi2((f2){u1[0], u1[1]}, (f2)(rv[m]), (f2)(rl[m])), phi2((f2){u1[2], u1[3]}, (f2)(rv[m]), (f2)(rl[m]))};
    }
  }
  lsig(myFL, 4);
  __builtin_amdgcn_s_setprio(0);
  STMP(5);
  {
  int l;
  asm volatile("v_mbcnt_lo_u32_b32 %0, -1, 0\n v_mbcnt_hi_u32_b32 %0, -1, %0" : "=v"(l));
  __builtin_assume(l >= 0 && l < 64);
  const int r15 = l & 15, g4 = l >> 4, q = (l & 15) >> 2, p = l & 3;
  const int trow = mb * 128 + w * 32 + (l & 31);
  u2 oh;
  const unsigned flo = PAIRAT(r15) * 128;
  const char* fb = uptr(wsx + WS_OH + mb * 2048);
  int sel = -1;
  {
    const u4 s0 = *(const LAS u4*)(lds + L_SEL), s1 = *(const LAS u4*)(lds + L_SEL + 16);
#pragma unroll
    for (int i = 0; i < 8; ++i) { const int v = (int)(i < 4 ? s0[i & 3] : s1[i & 3]); sel = v == trow ? i : sel; }
  }
  const bool anysel = __ballot(sel >= 0) != 0ull;
  f16v oc[2][2];
  {
    const float m = *(const LAS float*)(lds + L_M + (w * 32 + (l & 31)) * 4), rs = *(const LAS float*)(lds + L_RS + (w * 32 + (l & 31)) * 4);
#pragma unroll
    for (int t = 0; t < 8; ++t) {
      const f4 xf = (f4){lo(xv[t][0]), hi(xv[t][0]), lo(xv[t][1]), hi(xv[t][1])};
      const f4 y = (xf - m) * rs + xf;
#pragma unroll
      for (int i = 0; i < 4; ++i) oc[0][t >> 2][(t & 3) * 4 + i] = y[i];
    }
    asm volatile("" : "+v"(oc[0][0]), "+v"(oc[0][1]));
  }
  unsigned wa[2];
#pragma unroll
  for (int t = 0; t < 2; ++t) wa[t] = ldsb + L_WO + wc * 4096 + (4 * (g4 >> 1) + q) * 128 + (((4 * t + 2 * (g4 & 1) + (p >> 1)) ^ ((q >> 1) << 2)) * 16) + (p & 1) * 8;
  u4 a[RD][4];
#pragma unroll
  for (int k = 0; k < RD; ++k) asm volatile("" : "=v"(a[k][0]), "=v"(a[k][1]), "=v"(a[k][2]), "=v"(a[k][3]));
  const char* ob = uptr(wsx + WS_OF + mb * 262144 + wc * 8192 + wr * 4096);
  const unsigned ov0 = l * 16 + wc * 2048, ov1 = ov0 ^ 2048;
  STMP(6);
  unsigned vfw[4];
#pragma unroll
  for (int i = 0; i < 4; ++i) { vfw[i] = VFW(i); asm volatile("" : "+v"(vfw[i])); }
  auto wo_part = [&](int s0, int s1) {
#pragma unroll
    for (int i = s0 / 2; i < s1 / 2; i += 2) {
      const int pp = i + wr;
      const char* wp = (const char*)wo + (nb * 128 - 3072) + (2 * PAIRAT(pp) + wc) * 65536; const unsigned wm = ldsb + (2 * pp + wc) * 4096;
      GL4(vfw, wp, wm, L_WO);
    }
  };
  lwait<2>(adFL, 0x3Fu, (l & 4) ? 3u : 4u);
  asm volatile("global_load_dwordx2 %0, %1, %2 sc1" : "=&v"(oh) : "v"(flo), "s"(fb) : "memory");
  wo_part(WE, WB);
  WAITV(WB - WE);
  asm volatile("" : "+v"(oh));
  unsigned seen = (unsigned)__ballot(oh[0] == tag), go = 0;
  auto need = [&](unsigned mask) {
    if (__builtin_expect((go & mask) != mask, 0)) {
      RETRY2();
      _Pragma("unroll 1") for (unsigned it = 0; it < SPIN; ++it) {
        if ((seen & mask) != mask) {
          asm volatile("global_load_dwordx2 %0, %1, %2 sc1\n s_waitcnt vmcnt(0)" : "+v"(oh) : "v"(flo), "s"(fb) : "memory");
          seen = (unsigned)__ballot(oh[0] == tag);
        }
        go = seen & (unsigned)__ballot(oh[1] - NOW() - 1u >= 1000u);
        if ((go & mask) == mask) break;
        SLEEP(1);
      }
    }
  };
#define OX 0
#define OSC "sc1"
#define OL1(pos, i) do { if (OX == 2 && (pos) >= RD) break; \
    asm volatile("global_load_dwordx4 %0, %1, %2 offset:%3 " OSC : "+v"(a[(pos) % RD][i]) : "v"((i) & 1 ? ov1 : ov0), "s"(ob + (OX == 1 ? ((pos) & 1) : PAIRAT(pos)) * 16384), "i"(((i) >> 1) * 1024) : "memory"); } while (0)
#define OLR(pos) do { _Pragma("unroll") for (int i_ = 0; i_ < 4; ++i_) asm volatile("global_load_dwordx4 %0, %1, %2 sc1" : "+v"(a[(pos) % RD][i_]) : "v"((i_ & 1 ? ov1 : ov0) + (i_ >> 1) * 1024), "s"(ob + PAIRAT(pos) * 16384) : "memory"); } while (0)
#define OLD(pos) do { OL1(pos, 0); OL1(pos, 1); OL1(pos, 2); OL1(pos, 3); } while (0)
  auto omax = [&](u4(&v)[4]) {
    asm volatile("" : "+v"(v[0]), "+v"(v[1]), "+v"(v[2]), "+v"(v[3]));
    return max(umax3(v[0][0], v[1][0], v[2][0]), v[3][0]);
  };
#define OFIX(pos, m) do { \
    if (__builtin_expect(__ballot((m) == POIS) != 0ull, 0)) { RETRY(); _Pragma("unroll 1") for (unsigned it = 0; it < SPIN; ++it) { SLEEP(4); OLR(pos); WAITV(0); if (__ballot(omax(a[(pos) % RD]) == POIS) == 0ull) break; } } } while (0)
#define OS1(i)
#define OS2(i)
  lwait<1>(adFL, 0x30u, 4);
  wo_part(WB, 32);
  __builtin_amdgcn_s_setprio(2);
  go = seen & (unsigned)__ballot(oh[1] - NOW() - 1u >= 1000u);
  need(1u);
  STMP(15);
  u2 oh2 = oh;
  if (__builtin_expect(seen != ~0u, 0)) asm volatile("global_load_dwordx2 %0, %1, %2 sc1" : "+v"(oh2) : "v"(flo), "s"(fb) : "memory");
  OS1(0); OLD(0);
#pragma unroll
  for (int i = 1; i < RD; ++i) { need(1u << i); OS1(i); OLD(i); }
  STMP(10);
  s4 b[8];
#define ORD(pos, f) do { if ((f) < 2 && (pos) == 8) { wa[(f) & 1] += 65536; asm volatile("" : "+v"(wa[(f) & 1])); } \
    RDTR(b[2 * (f)], wa[(f) & 1], ((pos) & 7) * 8192 + ((f) >> 1) * 1024); RDTR(b[2 * (f) + 1], wa[(f) & 1], ((pos) & 7) * 8192 + ((f) >> 1) * 1024 + 2048); } while (0)
#define OMF(pos, f, r) do { if ((f) < 2 && (pos) == 0 && (r) == 1) asm volatile("v_mfma_f32_32x32x16_bf16 %0, %1, %2, 0" : "=&v"(oc[r][(f) & 1]) \
      : "v"(CAT(b[2 * (f)], b[2 * (f) + 1])), "v"(a[(pos) % RD][((f) & 2) + (r)])); \
    else asm volatile("v_mfma_f32_32x32x16_bf16 %0, %1, %2, %0" : "+v"(oc[r][(f) & 1]) \
      : "v"(CAT(b[2 * (f)], b[2 * (f) + 1])), "v"(a[(pos) % RD][((f) & 2) + (r)])); \
    SBAR(); if ((r) == 1 && (pos) < 15) ORD(((pos) + 1) & 15, f); SBAR(); } while (0)
#define ONX(pos) ((pos) > 0 && (pos) + RD - 1 < 16)
#define ONL(pos, i) do { if (ONX(pos)) OL1(((pos) + RD - 1) & 15, i); SBAR(); } while (0)
#define OWL(pos, f) do { WAITL((pos) < 15 ? 6 : 6 - 2 * (f)); SBAR(); } while (0)
#define OHEAD(pos) do { unsigned m_ = 0; \
    if ((pos) == WE / 2 - 1 && WE < 32) { asm volatile("" : "+v"(fl3)); if (__builtin_expect(((unsigned)__ballot(fl3 < 5u) >> (w ^ 2)) & 1u, 0)) lwait(adFL, 1u << (w ^ 2), 5); SBAR(); } \
    OWL(pos, 0); OMF(pos, 0, 0); if (ONX(pos)) { need(1u << (((pos) + RD - 1) & 15)); OS1(((pos) + RD - 1) & 15); } SBAR(); \
    OMF(pos, 0, 1); ONL(pos, 0); \
    OWL(pos, 1); OMF(pos, 1, 0); ONL(pos, 1); \
    OMF(pos, 1, 1); ONL(pos, 2); \
    OWL(pos, 2); OMF(pos, 2, 0); ONL(pos, 3); \
    if ((pos) == 0 || (pos) == 4) { asm volatile("ds_write_b32 %0, %1" ::"v"(myFL), "v"((pos) ? 6u : 5u) : "memory"); SBAR(); } \
    if ((pos) == WE / 2 - 3) { asm volatile("ds_read_b32 %0, %1" : "=v"(fl3) : "v"(adFL) : "memory"); SBAR(); } \
    if ((pos) == 13) { asm volatile("ds_read_b32 %0, %1" : "=v"(fl3) : "v"(adFL) : "memory"); SBAR(); } \
    OMF(pos, 2, 1); \
    OWL(pos, 3); OMF(pos, 3, 0); \
    if ((pos) < 15) { WAITV(4 * ((pos) + RD - 1 < 16 ? RD - 2 : 14 - (pos))); m_ = omax(a[((pos) + 1) % RD]); asm volatile("" : "+v"(m_)); SBAR(); } \
    OMF(pos, 3, 1); \
    if ((pos) < 15) { OFIX(((pos) + 1) & 15, m_); OS2(((pos) + 1) & 15); } SBAR(); } while (0)
  unsigned fl3;
  ORD(0, 0); ORD(0, 1); ORD(0, 2); ORD(0, 3);
  { WAITV(4 * (RD - 1)); const unsigned m_ = omax(a[0]); OFIX(0, m_); OS2(0);
    asm volatile("" : "+v"(oh2));
    if (oh2[0] == tag) oh = oh2;
    seen = (unsigned)__ballot(oh[0] == tag);
    go = seen & (unsigned)__ballot(oh[1] - NOW() - max(r15 - (RD - 1), 0) * ADV - 1u >= 1000u);
    SBAR(); }
  OHEAD(0); OHEAD(1); STMP(11); OHEAD(2); OHEAD(3); OHEAD(4); OHEAD(5); OHEAD(6); OHEAD(7); STMP(12);
  OHEAD(8); OHEAD(9); STMP(13); OHEAD(10); OHEAD(11); OHEAD(12); OHEAD(13); OHEAD(14);
  f4 xi[8];
  {
    asm volatile("" : "+v"(fl3));
    if (__builtin_expect(((unsigned)__ballot(fl3 < 6u) >> (w ^ 2)) & 1u, 0)) lwait(adFL, 1u << (w ^ 2), 6);
    if (__builtin_expect(((unsigned)__ballot(fl3 < 4u) & 0x30u) != 0u, 0)) lwait(adFL, 0x30u, 4);
    const unsigned xw = ldsb + L_WO + (4 * wr + wc) * 4096 + l * 16, xr = xw ^ 4096;
#define XW(t) asm volatile("ds_write_b128 %0, %1 offset:%2" ::"v"(xw), "v"(__builtin_shufflevector(oc[1][(t) >> 2], oc[1][(t) >> 2], ((t) & 3) * 4, ((t) & 3) * 4 + 1, ((t) & 3) * 4 + 2, ((t) & 3) * 4 + 3)), \
      "i"(((t) >> 2) * 8192 + ((t) & 3) * 1024) : "memory")
    SBAR();
    OWL(15, 0); OMF(15, 0, 1); OWL(15, 1); OMF(15, 1, 1); OWL(15, 2); OMF(15, 2, 1); OWL(15, 3); OMF(15, 3, 1);
    OMF(15, 0, 0); OMF(15, 1, 0); XW(0); XW(1); XW(2); XW(3); SBAR();
    OMF(15, 2, 0); XW(4); XW(5); XW(6); XW(7); SBAR();
    OMF(15, 3, 0);
    asm volatile("ds_write_b32 %0, %1" ::"v"(myFL), "v"(7u) : "memory");
    _Pragma("unroll 1") for (unsigned it = 0; it < SPIN; ++it) {
      unsigned v;
      asm volatile("ds_read_b32 %0, %1" : "=v"(v) : "v"(adFL) : "memory");
#pragma unroll
      for (int t = 0; t < 8; ++t) RD128(xi[t], xr, (t >> 2) * 8192 + (t & 3) * 1024);
      WAITL(0);
      asm volatile("" : "+v"(v), "+v"(xi[0]), "+v"(xi[1]), "+v"(xi[2]), "+v"(xi[3]), "+v"(xi[4]), "+v"(xi[5]), "+v"(xi[6]), "+v"(xi[7]));
      if ((((unsigned)__ballot(v < 7u) >> (w ^ 1)) & 1u) == 0u) break;
    }
    asm volatile("" : "+v"(oc[0][0]), "+v"(oc[0][1]));
#pragma unroll
    for (int t = 0; t < 8; ++t)
#pragma unroll
      for (int i = 0; i < 4; ++i) xi[t][i] += oc[0][t >> 2][(t & 3) * 4 + i];
  }
  STMP(8);
  if (anysel) {
    lwait(adFL, 0xC0u, 3);
    if (sel >= 0) {
#pragma unroll
      for (int t = 0; t < 8; ++t) xi[t] += *(const LAS f4*)(lds + L_KVX + sel * 256 + (8 * t + 4 * (l >> 5)) * 4);
    }
  }
  const __amdgpu_buffer_rsrc_t outr = __builtin_amdgcn_make_buffer_rsrc(out, 0, 2048 * D * 2, 0x00020000);
  if (FULL) {
    LAS char* tb = (LAS char*)lds + L_PQ + w * 4096;
#pragma unroll
    for (int t = 0; t < 8; ++t) *(LAS u2*)(tb + (l & 31) * 128 + ((t ^ (l & 7)) * 16) + (l >> 5) * 8) = (u2){pk2(xi[t][0], xi[t][1]), pk2(xi[t][2], xi[t][3])};
#pragma unroll
    for (int i = 0; i < 4; ++i) {
      const u4 v = *(const LAS u4*)(tb + i * 1024 + (l >> 3) * 128 + (((l ^ (l >> 3)) & 7) * 16));
      __builtin_amdgcn_raw_buffer_store_b128(v, outr, (mb * 128 + w * 32 + i * 8 + (l >> 3)) * 2048 + nb * 128 + (l & 7) * 16, 0, 16);
    }
  } else {
#pragma unroll
  for (int hh = 0; hh < 4; ++hh) {
    const f4 ta = xi[2 * hh], tb = xi[2 * hh + 1];
    auto r0 = __builtin_amdgcn_permlane32_swap(pk2(ta[0], ta[1]), pk2(tb[0], tb[1]), false, false);
    auto r1 = __builtin_amdgcn_permlane32_swap(pk2(ta[2], ta[3]), pk2(tb[2], tb[3]), false, false);
    __builtin_amdgcn_raw_buffer_store_b128(((u4){r0[0], r1[0], r0[1], r1[1]}), outr, (trow * D + nb * 64 + 16 * hh + 8 * (l >> 5)) * 2, 0, 16);
  }
  }
  if (g == 0 && w == 0 && l == 0) __builtin_amdgcn_raw_buffer_store_b32(tag, wse, WS_E, 0, 16);
  STMP(9);
  STFLUSH();
  asm volatile(".Lend_c:");
  }
  return;
  }
  {
    unsigned vb[4], vfw[4];
    STMP(6); SBAR();
#pragma unroll
    for (int i = 0; i < 4; ++i) vb[i] = (8 * i + (l >> 3)) * 6144 + (((l & 7) ^ ((((l >> 4) & 1) << 2) | ((i & 1) << 1))) * 16) + (3 - i) * 1024;
    const unsigned lb0 = lw * 196608, lb1 = 196608 - lw * 194560, mbb = ldsb + 16384 + lw * 12288;
    const char* Ab = kx + ao;
    const char* Bb = kq + (hp * 128 + lw * 2048 - 3072);
    auto issue = [&](int s, int u0, int u1) {
      const int kt = (s + rot) & 15;
      const char* sa = Ab + kt * 128; const char* sb = Bb + kt * (64 * 6144);
      const unsigned so = (s & 3) * STG, ma = mba + so, mq = mbb + so;
#pragma unroll
      for (int u = u0; u < u1; ++u) { if (u < 2) GL4(va, sa + u * 65536, ma, u * 4096); else GL4(vb, sb + (u == 2 ? lb0 : u == 3 ? lb1 : lb0 + 2048), mq, (u - 2) * 4096); }
    };
    int tk = 0;
#define LBAR() do { BST(tk + 1); BAR(); BST(tk + 21); ++tk; } while (0)
    issue(0, 2, 3); issue(0, 1, 2); issue(0, 3, 5); SBAR(); STMP(8); SBAR(); issue(1, 0, 5); issue(2, 0, U0);
    BST(60);
    WAITV(20 + 4 * U0); BST(0); BAR(); BST(20);
    issue(2, U0, 5);
    GETTAG(tag);
    MKWS(tag);
    const unsigned ck0 = (unsigned)__builtin_amdgcn_s_memtime(), rt0 = NOW();
    int s0 = 3;
    if (U3 < 5) { issue(3, 0, U3); BST(tk + 40); WAITV(20 + 4 * U3); LBAR(); issue(3, U3, 5); s0 = 4; }
#pragma unroll 1
    for (; s0 < (sp ? 16 : 6 + ph); ++s0) { issue(s0, 0, 5); BST(tk + 40); WAITV(40); LBAR(); }
#pragma unroll 1
    for (; s0 < 16; s0 += 5) {
      WAITV(20); LBAR();
      WAITV(0); LBAR();
      issue(s0, 0, 5); issue(s0 + 1, 0, U1); BST(tk + 40); WAITV(4 * U1); LBAR();
      issue(s0 + 1, U1, 5); issue(s0 + 2, 0, 5);
      if (s0 + 3 >= 16) break;
      issue(s0 + 3, 0, U2); BST(tk + 40); WAITV(20 + 4 * U2); LBAR();
      issue(s0 + 3, U2, 5);
      if (s0 + 4 >= 16) break;
      issue(s0 + 4, 0, 5); BST(tk + 40); WAITV(40); LBAR();
    }
    int wq = 0;
#pragma unroll
    for (int i = 0; i < 4; ++i) vfw[i] = VFW(i);
    auto wo_upto = [&](int lim) {
      _Pragma("unroll 1") for (; wq < lim; ++wq) { const char* wp = (const char*)wo + (nb * 128 - 3072) + HEADAT(wq) * 65536; const unsigned wm = ldsb + wq * 4096; GL4(vfw, wp, wm, L_WO); }
    };
    asm volatile("ds_write_b32 %0, %1" ::"v"(adFL), "v"(0u) : "memory");
    unsigned kd = sp ? KS : KD, od = OD;
    if (CF) {
      const unsigned dc = (unsigned)__builtin_amdgcn_s_memtime() - ck0, dr = NOW() - rt0;
      const float sc = fminf((float)dr * (float)CF * __builtin_amdgcn_rcpf((float)dc), 2.f);
      kd = KL + (unsigned)((float)(kd - KL) * sc); od = OL + (unsigned)((float)(OD - OL) * sc);
      asm volatile("" : "+v"(kd), "+v"(od));
    }
    const int hd = hp * 2 + lw;
    u4 sel[2];
#pragma unroll
    for (int e = 0; e < 2; ++e) {
      unsigned v = (r15 >> 2) == g4 ? (((r15 & 3) == 2 * e ? 0x3F80u : 0u) | ((r15 & 3) == 2 * e + 1 ? 0x3F800000u : 0u)) : 0u;
      asm volatile("" : "+v"(v));
      sel[0][e] = v; sel[0][2 + e] = 0u; sel[1][e] = 0u; sel[1][2 + e] = v;
    }
    const char* kvb = uptr(wsx + WS_KVP + (size_t)hd * 32768);
    const char* zb = uptr(wsx + WS_ZP + (size_t)hd * 2048);
    unsigned zoff = r15 * 128 + g4 * 32, oho = l == 0 ? WS_OH + (mb * 16 + hp) * 128 : WS_SZ, fla = ldsb + L_FL;
    asm volatile("" : "+v"(zoff), "+v"(oho), "+v"(fla), "+s"(kvb), "+s"(zb));
    WAITV(0); WAITL(0); LBAR();
    u4 dg;
    unsigned now;
    if (lw) {
      const u4 pz = (u4)(POIS);
      const unsigned tb = NOW();
      __builtin_amdgcn_raw_buffer_store_b32(tb + kd, wsr, l == 0 ? WS_HK + hp * 512 + (mb >> 2) * 128 + (mb & 3) * 4 : WS_SZ, 0, 16);
#pragma unroll
      for (int i = 0; i < 16; ++i) ST128(pz, WS_OF + (mb * 32 + hp * 2) * 8192 + i * 1024 + l * 16);
      BST(61);
      const char* hb = uptr(wsx + WS_HK + hp * 512);
      __builtin_amdgcn_s_setprio(LP1);
      lwait<4>(adFL, 0xC0u, 1u);
      while (NOW() - tb < HT) SLEEP(1);
      STMP(1);
      unsigned t0, rq[4], m0, m1, nt;
#define HG4(d, a, b, c, e) "s_sub_u32 s" #a ", s" #a ", %0\n s_sub_u32 s" #b ", s" #b ", %0\n s_sub_u32 s" #c ", s" #c ", %0\n s_sub_u32 s" #e ", s" #e ", %0\n" \
      "s_max_u32 s" #a ", s" #a ", s" #b "\n s_max_u32 s" #c ", s" #c ", s" #e "\n s_max_u32 %" #d ", s" #a ", s" #c "\n"
      asm volatile("s_mov_b32 %7, 15\n.Lhint:\n s_load_dwordx4 s[72:75], %8, 0 glc\n s_load_dwordx4 s[76:79], %8, 128 glc\n s_load_dwordx4 s[80:83], %8, 256 glc\n"
                   "s_load_dwordx4 s[84:87], %8, 384 glc\n s_memrealtime s[88:89]\n s_waitcnt lgkmcnt(0)\n s_sub_u32 %0, s88, 0x800\n"
                   HG4(1, 72, 73, 74, 75) HG4(2, 76, 77, 78, 79) HG4(3, 80, 81, 82, 83) HG4(4, 84, 85, 86, 87)
                   "s_max_u32 %5, %1, %2\n s_max_u32 %6, %3, %4\n s_max_u32 s72, %5, %6\n s_cmpk_lt_u32 s72, 0xc00\n s_cbranch_scc1 .Lhdone\n"
                   "s_sleep %9\n s_sub_u32 %7, %7, 1\n s_cbranch_scc0 .Lhint\n"
                   "s_mov_b32 %1, 0\n s_mov_b32 %2, 0\n s_mov_b32 %3, 0\n s_mov_b32 %4, 0\n s_mov_b32 %5, 0\n s_mov_b32 %6, 0\n.Lhdone:"
                   : "=&s"(t0), "=&s"(rq[0]), "=&s"(rq[1]), "=&s"(rq[2]), "=&s"(rq[3]), "=&s"(m0), "=&s"(m1), "=&s"(nt) : "s"(hb), "i"(HKS)
                   : "memory", "scc", "s72", "s73", "s74", "s75", "s76", "s77", "s78", "s79", "s80", "s81", "s82", "s83", "s84", "s85", "s86", "s87", "s88", "s89");
      BST(16);
      const bool up = m0 > m1;
      const unsigned a0 = up ? rq[2] : rq[0], a1 = up ? rq[3] : rq[1], a2 = up ? rq[0] : rq[2], a3 = up ? rq[1] : rq[3], gu = up ? 2u : 0u;
      unsigned e0, e1, go;
      asm("s_cmp_gt_u32 %3, %4\n s_cselect_b32 %0, %6, %5\n s_cselect_b32 %1, %5, %6\n s_addc_u32 %2, %7, 0" : "=&s"(e0), "=&s"(e1), "=&s"(go) : "s"(a2), "s"(a3), "s"(a0), "s"(a1), "s"(gu) : "scc");
      dg = (u4){((t0 + 3u + e0) & ~3u) | go, t0 + e1, t0 + min(a2, a3), t0 + max(m0, m1)};
      asm volatile("ds_write_b128 %0, %1 offset:32\n ds_write_b32 %0, %2 offset:20" ::"v"(fla), "v"(dg), "v"(1u) : "memory");
      BST(15);
      now = NOW();
      WAITV(0);
      ST64(((u2){tag, ((int)(dg[3] - now) > 0 ? dg[3] : now) + od}), oho);
    } else {
      wo_upto(WA);
      BST(15);
      if (WA < WE) { lwait<2>(adFL, 0x0Fu, 3); if (WS0) SLEEP(WS0); wo_upto(WE); }
      STMP(1);
      __builtin_amdgcn_s_setprio(LP0);
      u4 v;
      _Pragma("unroll 1") for (unsigned it = 0; it < SPIN; ++it) {
        unsigned f;
        asm volatile("ds_read_b32 %0, %2 offset:20\n ds_read_b128 %1, %2 offset:32\n s_waitcnt lgkmcnt(0)" : "=&v"(f), "=&v"(v) : "v"(fla) : "memory");
        if (__builtin_amdgcn_readfirstlane(f)) break;
        SLEEP(2);
      }
#pragma unroll
      for (int e = 0; e < 4; ++e) dg[e] = __builtin_amdgcn_readfirstlane(v[e]);
      now = NOW();
    }
    unsigned gx = (dg[0] & 3u) * 8192u;
    asm volatile("" : "+s"(gx));
    __builtin_amdgcn_s_setprio(3);
    u4 pv[16][2], zv0, zv1, pq[8];
    const unsigned lv16 = l * 16;
    const unsigned pa = ldsb + L_PQ + lw * 4096 + l * 16, omask = lw ? 0x0Au : 0x25u;
    STMP(2);
#define GLD(gq) do { _Pragma("unroll") for (int i = 0; i < 4; ++i) { const char* sb = padd(kvb, (((gq) * 8192u + (i & 2) * 2048u) ^ gx)); \
      asm volatile("global_load_dwordx4 %0, %1, %2 offset:%3 sc1" : "=v"(pv[(gq) * 4 + i][0]) : "v"(lv16), "s"(sb), "i"((i & 1) * 2048) : "memory"); \
      asm volatile("global_load_dwordx4 %0, %1, %2 offset:%3 sc1" : "=v"(pv[(gq) * 4 + i][1]) : "v"(lv16), "s"(sb), "i"((i & 1) * 2048 + 1024) : "memory"); } } while (0)
#define ZLD() do { asm volatile("global_load_dwordx4 %0, %1, %2 sc1" : "=v"(zv0) : "v"(zoff), "s"(zb) : "memory"); \
      asm volatile("global_load_dwordx4 %0, %1, %2 offset:16 sc1" : "=v"(zv1) : "v"(zoff), "s"(zb) : "memory"); } while (0)
#pragma unroll
    for (int gq = 0; gq < 4; ++gq) {
      while ((int)(now - dg[gq]) < 0) now = NOW();
      if (gq == 3) ZLD();
      GLD(gq);
    }
    lsig(myFL, 3);
    STMP(5);
    unsigned ofo = WS_OF + (mb * 32 + hd) * 8192 + g4 * 512 + r15 * 16;
    unsigned ofp = ofo + 4096;
    f4 eps = (f4)(1e-6f);
    asm volatile("" : "+v"(ofo), "+v"(ofp), "+v"(eps), "+v"(sel[0]), "+v"(sel[1]));
    f4 gs[4][4]; float z[8];
#pragma unroll
    for (int gq = 0; gq < 4; ++gq) {
      if (gq == PW) {
        SBAR();
        lwait(adFL, omask, (l & 4) ? 3u : 4u);
#pragma unroll
        for (int i = 0; i < 8; ++i) RD128(pq[i], pa, (i >> 2) * 8192 + (i & 3) * 1024);
      }
      SBAR(); WAITV(gq == 0 ? 26 : gq == 1 ? 18 : gq == 2 ? 10 : 0);
      if (gq == 3) BST(17);
      auto gbad = [&]() {
#pragma unroll
        for (int i = 0; i < 4; ++i) asm volatile("" : "+v"(pv[gq * 4 + i][0]), "+v"(pv[gq * 4 + i][1]));
        unsigned m = umax3(pv[gq * 4][0][0], pv[gq * 4][1][0], pv[gq * 4 + 1][0][0]);
        m = umax3(m, pv[gq * 4 + 1][1][0], pv[gq * 4 + 2][0][0]);
        m = umax3(m, pv[gq * 4 + 2][1][0], pv[gq * 4 + 3][0][0]);
        m = max(m, pv[gq * 4 + 3][1][0]);
        if (gq == 3) { asm volatile("" : "+v"(zv0), "+v"(zv1)); m = umax3(m, zv0[0], zv1[0]); }
        return __ballot(m == POIS) != 0ull;
      };
      if (__builtin_expect(gbad(), 0)) {
        _Pragma("unroll 1") for (unsigned it = 0; it < SPIN; ++it) {
          RETRY();
          SLEEP(2);
          if (gq == 3) ZLD();
          GLD(gq);
          WAITV(0);
          if (!gbad()) break;
        }
      }
      if (gq == 3) {
#pragma unroll
        for (int e = 0; e < 4; ++e) { z[e] = __uint_as_float(zv0[e]); z[4 + e] = __uint_as_float(zv1[e]); }
        SBAR();
      }
#pragma unroll
      for (int i = 0; i < 4; ++i)
#pragma unroll
        for (int t = 0; t < 4; ++t) {
          gs[gq][t] = MFMA16(S8(sel[(t ^ (t >> 1)) & 1]), S8(pv[gq * 4 + i][t >> 1]), i ? gs[gq][t] : (f4)(0.f));
          if (gq == 3) {
            SBAR();
            if (i + t == 0) asm volatile("s_nop 1");
            asm volatile("v_add_f32_dpp %0, %0, %0 row_ror:%2 row_mask:0xf bank_mask:0xf\n v_add_f32_dpp %1, %1, %1 row_ror:%2 row_mask:0xf bank_mask:0xf" : "+v"(z[2 * t]), "+v"(z[2 * t + 1]) : "i"(8 >> i));
            SBAR();
          }
        }
      if (gq == 2) {
#pragma unroll
        for (int t = 0; t < 4; ++t) { gs[0][t] += gs[1][t]; asm volatile("" : "+v"(gs[0][t])); }
      }
    }
    STMP(3);
    WAITL(0);
#pragma unroll
    for (int i = 0; i < 8; i += 2) asm volatile("" : "+v"(pq[i]), "+v"(pq[i + 1]));
    u4 kA[2], zA;
#pragma unroll
    for (int e = 0; e < 4; ++e) zA[e] = pk2(z[2 * e], z[2 * e + 1]);
    SBAR();
#pragma unroll
    for (int t = 0; t < 4; ++t) gs[0][t] += gs[2][t] + gs[3][t];
    SBAR();
    f4 dn[8], o0[8], o1[8]; float inv[8];
#pragma unroll
    for (int i = 0; i < 8; ++i) {
      dn[i] = MFMA16(S8(zA), S8(pq[i]), eps);
      SBAR();
      kA[i >> 2][i & 3] = pk2(gs[0][i >> 1][(i & 1) * 2], gs[0][i >> 1][(i & 1) * 2 + 1]);
      SBAR();
    }
#pragma unroll
    for (int i = 0; i < 8; ++i) {
      o0[i] = MFMA16(S8(kA[0]), S8(pq[i]), (f4)(0.f)); SBAR();
      o1[i] = MFMA16(S8(kA[1]), S8(pq[i]), (f4)(0.f)); SBAR();
      inv[i] = __builtin_amdgcn_rcpf(dn[i][0]); asm volatile("" ::"v"(dn[i])); SBAR();
    }
    BST(18);
    u4 op = sel[0];
#pragma unroll
    for (int i = 0; i < 8; ++i) {
      o0[i] *= inv[i]; o1[i] *= inv[i];
      u4 o; o[0] = min(pk2(o0[i][0], o0[i][1]), POIS - 1u); o[1] = pk2(o0[i][2], o0[i][3]); o[2] = pk2(o1[i][0], o1[i][1]); o[3] = pk2(o1[i][2], o1[i][3]);
      asm volatile("" : "+v"(o) : "v"(op));
      ST128(o, (i & 4 ? ofp : ofo) + (i & 1) * 256 + (i & 2) * 1024);
      op = o;
      SBAR();
    }
    lsig(myFL, 4);
    STMP(4);
    STFLUSH();
    asm volatile(".Lend_l:");
  }

}

extern "C" void kernel_launch(void* const* d_in, const int* in_sizes, int n_in, void* d_out, int out_size,
                              void* d_ws, size_t ws_size, hipStream_t stream) {
  (void)hipFuncSetAttribute((const void*)fused, hipFuncAttributeMaxDynamicSharedMemorySize, LDS_SZ);
  fused<<<256, 512, LDS_SZ, stream>>>((const u16*)d_in[0], (const u16*)d_in[1], (const u16*)d_in[4], (char*)d_ws, (const u16*)d_in[2],
                                      (const u16*)d_in[5], (const u16*)d_in[6], (const u16*)d_in[7], (u16*)d_out);
}
```

```cpp
#include <hip/hip_runtime.h>
#include <stdint.h>

typedef unsigned short u16;
typedef short s4 __attribute__((ext_vector_type(4)));
typedef short s8 __attribute__((ext_vector_type(8)));
typedef float f4 __attribute__((ext_vector_type(4)));
typedef float f16v __attribute__((ext_vector_type(16)));
typedef float f2 __attribute__((ext_vector_type(2)));
typedef unsigned u4 __attribute__((ext_vector_type(4)));
typedef unsigned u2 __attribute__((ext_vector_type(2)));
typedef unsigned u8v __attribute__((ext_vector_type(8)));
typedef __bf16 bf2 __attribute__((ext_vector_type(2)));
#define LAS __attribute__((address_space(3)))
#define DEV __device__ __forceinline__

constexpr int D = 1024, F = 3584;
#define NOMOE 0
#define KD 220
#define KS 214
#define OD 180
#define OHP 1
#define FS 128
#define GSL 8
#define YSL 16
#define TP1 0
#define TP2 3
#define TLBW 0
#define FULL 1
#define HPRIO 1
#define OWN 1
#define NOPOIS 0
#define CAP 7
#define RD 4
#define DYN 1
#define JX 0
#define NOSKIP 0
#define GRP 8
#define ADV 15
#define HB 1
#define RT 1
#define WD 0
#define WU1 12
#define WU2 22
#define PL 0
#define PO 0
#define WG 0
#define DS 0
#define D1 16
#define D2 2
#define HKS 2
#define LW1 1
#define HW1 2
#define HS0 0
#define GPV 2
#define XO 1
#define TP0 3
#define G1 0x31
#define G1S 4
#define G2S 8
#define TD 1
#define YD 170
#define WU0 4
#define WE 12
#define HT 115
#define CF 19
#define KL 145
#define OL 115
#define CL 0
#define WS0 4
#define OM 0
#define HG 128
#define U0 0
#define U3 5
#define TL 0
#define MH 0
#define HWT 3
#define YG 0
#define GG 1
#define PV 1
#define U1 4
#define U2 2
#define QD 1
#define LP0 3
#define LP1 3
#define WA 8
#define PW 3
#define WB 24
#define PF 1
#define PFS 6
#define PA 0
#define PAS 0
#define ROT 0

constexpr unsigned WS_E = 0;
constexpr unsigned WS_YF = 524288;
constexpr unsigned WS_CAND = 12288;
constexpr unsigned WS_RG = 49152;
constexpr unsigned WS_ST = 65536;
constexpr unsigned WS_HK = 589824;
constexpr unsigned WS_ZP = 131072;
constexpr unsigned WS_CS = 196608;
constexpr unsigned WS_OH = 458752;
constexpr unsigned WS_KVP = 1u << 20;
constexpr unsigned WS_GP = 2u << 20;
constexpr unsigned WS_YP = 3u << 20;
constexpr unsigned WS_OF = 5u << 20;
constexpr unsigned WS_SZ = 9u << 20;
#define NR 4
#define RGO(tag) ((64u << 20) + ((tag) & (NR - 1u)) * (10u << 20))
#define MKWS(tag) char* const wsx = ws + RGO(tag); const __amdgpu_buffer_rsrc_t wsr = __builtin_amdgcn_make_buffer_rsrc(wsx, 0, WS_SZ, 0x00020000)

constexpr int STG = 40960;
constexpr int L_KVX = 0;
constexpr int L_PQ = 12288;
constexpr int L_M = 28672, L_RS = L_M + 512, L_CS = L_RS + 512, L_SEL = L_CS + 768, L_FL = L_SEL + 64;
constexpr int L_HX = 30720, L_RL = 31744, L_WO = 32768;
constexpr int LDS_SZ = 163840;
constexpr unsigned POIS = 0xFFFFFFFFu;

DEV unsigned pk2(float a, float b) { bf2 v; v[0] = (__bf16)a; v[1] = (__bf16)b; return __builtin_bit_cast(unsigned, v); }
DEV float lo(unsigned u) { return __uint_as_float(u << 16); }
DEV float hi(unsigned u) { return __uint_as_float(u & 0xFFFF0000u); }
DEV float rbf(float a) { return lo(pk2(a, a)); }
DEV float dot2(unsigned a, unsigned b, float c) { return __builtin_amdgcn_fdot2_f32_bf16(__builtin_bit_cast(bf2, a), __builtin_bit_cast(bf2, b), c, false); }
#define RD128(dst, addr, OFF) asm volatile("ds_read_b128 %0, %1 offset:%2" : "=v"(dst) : "v"(addr), "i"(OFF) : "memory")
#define RDTR(dst, addr, OFF) asm volatile("ds_read_b64_tr_b16 %0, %1 offset:%2" : "=v"(dst) : "v"(addr), "i"(OFF) : "memory")
#define SBAR() __builtin_amdgcn_sched_barrier(0)
#define CAT(x, y) __builtin_shufflevector(x, y, 0, 1, 2, 3, 4, 5, 6, 7)
#define WAITV(n) asm volatile("s_waitcnt vmcnt(%0)" ::"i"(n) : "memory")
#define WAITL(n) asm volatile("s_waitcnt lgkmcnt(%0)" ::"i"(n) : "memory")
#define MEMB() asm volatile("" ::: "memory")
#define SLEEP(n) asm volatile("s_sleep %0" ::"i"(n) : "memory")
#define BAR() asm volatile("s_barrier" ::: "memory")
#define GL0(vo, sb, m0v) asm volatile("s_mov_b32 m0, %2\n s_nop 0\n global_load_lds_dwordx4 %0, %1" ::"v"(vo), "s"(sb), "s"(m0v) : "memory")
#define GLI(vo, sb, OFF) asm volatile("global_load_lds_dwordx4 %0, %1 offset:%2" ::"v"(vo), "s"(sb), "i"(OFF) : "memory")
#define GL4(v, sb, mm, MO) do { asm volatile("s_add_i32 m0, %2, %3\n s_nop 0\n global_load_lds_dwordx4 %0, %1" ::"v"((v)[0]), "s"(sb), "s"(mm), "i"(MO) : "memory", "scc"); \
      GLI((v)[1], sb, 1024); GLI((v)[2], sb, 2048); GLI((v)[3], sb, 3072); } while (0)
#define VFW(i) ((8 * (i) + (l >> 3)) * 2048 + (((l & 7) ^ (((l >> 4) & 1) << 2)) * 16) + (3 - (i)) * 1024)
#define MFMA16(a, b, c) __builtin_amdgcn_mfma_f32_16x16x32_bf16(a, b, c, 0, 0, 0)
#define S8(x) __builtin_bit_cast(s8, x)
#define LD128(off) __builtin_amdgcn_raw_buffer_load_b128(wsr, (off), 0, 16)
#define LD64(off) __builtin_amdgcn_raw_buffer_load_b64(wsr, (off), 0, 16)
#define ST128(v, off) __builtin_amdgcn_raw_buffer_store_b128((v), wsr, (off), 0, 16)
#define ST64(v, off) __builtin_amdgcn_raw_buffer_store_b64((v), wsr, (off), 0, 16)
#define OFA(i) (WS_OF + ((mb * 32 + hd) * 8 + (i)) * 1024 + l * 16)
#define POISON(i) ST128(pz, (i) < 2 ? WS_KVP + (hd * 16 + mb) * 2048 + (i) * 1024 + l * 16 : (i) == 2 ? WS_ZP + (hd * 16 + mb) * 128 + (l & 7) * 16 : OFA((i) - 3))
#define NOW() ((unsigned)__builtin_amdgcn_s_memrealtime())
constexpr unsigned SPIN = 1u << 21;
DEV const char* uptr(const char* p) {
  unsigned long long v = (unsigned long long)p;
  unsigned lo_ = __builtin_amdgcn_readfirstlane((unsigned)v), hi_ = __builtin_amdgcn_readfirstlane((unsigned)(v >> 32));
  return (const char*)(((unsigned long long)hi_ << 32) | lo_);
}
DEV const char* padd(const char* p, unsigned o) {
  unsigned lo_, hi_;
  asm("s_add_u32 %0, %2, %4\n s_addc_u32 %1, %3, 0" : "=&s"(lo_), "=s"(hi_) : "s"((unsigned)(unsigned long long)p), "s"((unsigned)((unsigned long long)p >> 32)), "s"(o) : "scc");
  return (const char*)(((unsigned long long)hi_ << 32) | lo_);
}
DEV void lsig(unsigned addr, unsigned val) { asm volatile("s_waitcnt lgkmcnt(0)\n ds_write_b32 %0, %1" ::"v"(addr), "v"(val) : "memory"); }
template <int SL = 0> DEV void lwait(unsigned addr, unsigned mask, unsigned target) {
  _Pragma("unroll 1") for (unsigned it = 0; it < SPIN; ++it) {
    unsigned v;
    asm volatile("ds_read_b32 %0, %1\n s_waitcnt lgkmcnt(0)" : "=v"(v) : "v"(addr) : "memory");
    if (((unsigned)__ballot(v < target) & mask) == 0u) break;
    if (SL) SLEEP(SL);
  }
}
DEV unsigned nopois(unsigned a) { return min(a, POIS - 1u); }
DEV u4 tagf(f4 v, unsigned tag) {
  u4 r;
#pragma unroll
  for (int e = 0; e < 4; ++e) r[e] = __builtin_amdgcn_perm(__float_as_uint(v[e]), tag, 0x07060500u + e);
  return r;
}
DEV void untag(u4& v, unsigned tag, unsigned& bad) {
#pragma unroll
  for (int e = 0; e < 4; ++e) v[e] ^= (tag >> (8 * e)) & 255u;
  bad |= v[0] | v[1]; bad |= v[2] | v[3];
}
DEV unsigned umax3(unsigned a, unsigned b, unsigned c) { return max(max(a, b), c); }
template <int C> DEV float dppf(float v) { return __int_as_float(__builtin_amdgcn_update_dpp(0, __float_as_int(v), C, 0xf, 0xf, false)); }
DEV unsigned wave_max(unsigned v) {
  v = max(v, (unsigned)__builtin_amdgcn_update_dpp(0, (int)v, 0x111, 0xf, 0xf, false));
  v = max(v, (unsigned)__builtin_amdgcn_update_dpp(0, (int)v, 0x112, 0xf, 0xf, false));
  v = max(v, (unsigned)__builtin_amdgcn_update_dpp(0, (int)v, 0x114, 0xf, 0xf, false));
  v = max(v, (unsigned)__builtin_amdgcn_update_dpp(0, (int)v, 0x118, 0xf, 0xf, false));
  v = max(v, (unsigned)__builtin_amdgcn_update_dpp(0, (int)v, 0x142, 0xa, 0xf, false));
  v = max(v, (unsigned)__builtin_amdgcn_update_dpp(0, (int)v, 0x143, 0xc, 0xf, false));
  return (unsigned)__builtin_amdgcn_readlane((int)v, 63);
}
DEV unsigned phi2(f2 u, f2 rs, f2 rl) {
  f2 a = u * rl, p = u * rs + 1.f, r;
  r[0] = __builtin_amdgcn_fmed3f(__builtin_amdgcn_exp2f(a[0]), p[0], 1.f);
  r[1] = __builtin_amdgcn_fmed3f(__builtin_amdgcn_exp2f(a[1]), p[1], 1.f);
  r += 1e-6f;
  return pk2(r[0], r[1]);
}

#define STDECL
#undef HB
#define HB 0
#define BSTV cw
#define STMP(i)
#define BST(i)
#define STFLUSH()
#define RETRY()
#define RETRY2()

DEV float half_sum(float v) {
  asm volatile("s_nop 1\n v_add_f32_dpp %0, %0, %0 row_ror:8 row_mask:0xf bank_mask:0xf\n s_nop 1\n v_add_f32_dpp %0, %0, %0 row_ror:4 row_mask:0xf bank_mask:0xf\n"
               "s_nop 1\n v_add_f32_dpp %0, %0, %0 row_ror:2 row_mask:0xf bank_mask:0xf\n s_nop 1\n v_add_f32_dpp %0, %0, %0 row_ror:1 row_mask:0xf bank_mask:0xf\n s_nop 1" : "+v"(v));
  auto r = __builtin_amdgcn_permlane16_swap(__float_as_uint(v), __float_as_uint(v), false, false);
  return __uint_as_float(r[0]) + __uint_as_float(r[1]);
}

struct FQ { s8 a[4]; s4 b[12]; };
template <bool B> struct BC { static constexpr bool v = B; };
template <int I> struct IC { static constexpr int v = I; };

__global__ __launch_bounds__(512) void fused(const u16* __restrict__ x, const u16* __restrict__ wqkv, const u16* __restrict__ wrt,
                                             char* __restrict__ ws_, const u16* __restrict__ wo_, const u16* __restrict__ wg,
                                             const u16* __restrict__ wu, const u16* __restrict__ wd, u16* __restrict__ out) {
  extern __shared__ __attribute__((aligned(16))) char lds[];
  const char *kx, *kq, *kr, *wo; char* ws;
  asm volatile("s_load_dwordx2 %0, %5, 0\n s_load_dwordx2 %1, %5, 8\n s_load_dwordx2 %2, %5, 16\n s_load_dwordx2 %3, %5, 24\n s_load_dwordx2 %4, %5, 32"
               : "=&s"(kx), "=&s"(kq), "=&s"(kr), "=&s"(ws), "=&s"(wo) : "s"(__builtin_amdgcn_kernarg_segment_ptr()));
  SBAR();
  const int w = __builtin_amdgcn_readfirstlane(threadIdx.x >> 6), l = threadIdx.x & 63;
  STDECL;
  STMP(0);
  const int g = blockIdx.x, xcd = g & 7, j = g >> 3, ii = j >> 2;
  const int mb = (xcd & 1) * 8 + ii, hp = (xcd >> 1) * 4 + (j & 3);
  unsigned cw, cwo = (l * 32 + j) * 64; unsigned long long cwb;
  asm volatile("s_getpc_b64 %2\n.Lcode_here:\n v_min_u32 %1, max(.Lend_c - .Lcode_here, .Lend_l - .Lcode_here, .Lend_h - .Lcode_here, .Lend_s - .Lcode_here) - 4, %1\n"
               "s_cmp_lg_u32 %3, 7\n s_cbranch_scc1 .Lskip_w\n global_load_dword %0, %1, %2\n.Lskip_w:" : "=&v"(cw), "+v"(cwo), "=&s"(cwb) : "s"(w) : "memory", "scc");
  SBAR();
  const int lw = w & 1, rot = 2 * xcd + 4 * ((ii + JX * j) & 3);
  const unsigned ldsb = (unsigned)(size_t)(LAS char*)lds;
  unsigned va[4];
#pragma unroll
  for (int i = 0; i < 4; ++i) va[i] = (8 * i + (l >> 3)) * 2048 + (((l & 7) ^ ((i & 1) * 4 + (l >> 4))) * 16) + (3 - i) * 1024;
  const int ao = (mb * 128 + lw * 64) * 2048 - 3072;
  long long ao0 = ao + (rot & 15) * 128;
  unsigned mba = ldsb + lw * 8192;
  const int srow = mb * 128 + hp * 8 + 2 * w + (l >> 5);
  unsigned xo = srow * 2048 + (l & 31) * 16, ro = (l & 31) * 16;
  asm volatile("" : "+v"(va[0]), "+v"(va[1]), "+v"(va[2]), "+v"(va[3]), "+v"(xo), "+v"(ro));
  asm volatile("s_waitcnt lgkmcnt(0)" : "+s"(kx), "+s"(kq), "+s"(kr), "+s"(ws), "+s"(wo), "+s"(ao0), "+s"(mba));
  SBAR();
  asm volatile("s_sub_u32 m0, %6, 4\n s_cmp_gt_u32 m0, 1\n s_cbranch_scc1 .Lskip_l\n s_mov_b32 m0, %5\n s_nop 0\n global_load_lds_dwordx4 %0, %4\n global_load_lds_dwordx4 %1, %4 offset:1024\n"
               "global_load_lds_dwordx4 %2, %4 offset:2048\n global_load_lds_dwordx4 %3, %4 offset:3072\n.Lskip_l:"
               :: "v"(va[0]), "v"(va[1]), "v"(va[2]), "v"(va[3]), "s"(kx + ao0), "s"(mba), "s"(w) : "memory", "scc");
  u4 xa[4], rw[4]; unsigned ep;
  asm volatile("s_cmp_gt_u32 %14, 3\n s_cbranch_scc1 .Lskip_x\n"
               "global_load_dwordx4 %0, %9, %11\n global_load_dwordx4 %1, %9, %11 offset:512\n global_load_dwordx4 %2, %9, %11 offset:1024\n global_load_dwordx4 %3, %9, %11 offset:1536\n"
               "global_load_dwordx4 %4, %10, %12\n global_load_dwordx4 %5, %10, %12 offset:512\n global_load_dwordx4 %6, %10, %12 offset:1024\n global_load_dwordx4 %7, %10, %12 offset:1536\n"
               ".Lskip_x:\n s_load_dword %8, %13, 0"
               : "=&v"(xa[0]), "=&v"(xa[1]), "=&v"(xa[2]), "=&v"(xa[3]), "=&v"(rw[0]), "=&v"(rw[1]), "=&v"(rw[2]), "=&v"(rw[3]), "=&s"(ep)
               : "v"(xo), "v"(ro), "s"(kx), "s"(kr), "s"(ws), "s"(w) : "memory", "scc");
#define GETTAG(t) unsigned t; asm volatile("s_waitcnt lgkmcnt(0)\n s_add_u32 %0, %1, 1" : "=s"(t) : "s"(ep) : "memory", "scc")
  SBAR();
#define PTAB 0xA98FEDC1045B7623ull
#define PHT 0x6624924999924924ull
#define CX 4
  const bool sp = (hp & 7) == 3;
  const int nb = hp;
#define PAIRAT(p) ((int)(PTAB >> (4 * (p))) & 15)
#define HEADAT(s) (2 * PAIRAT((s) >> 1) + ((s) & 1))
  const int ph = (int)(PHT >> (2 * j)) & 3;
#define HQT 0xDCBA098765320410ull
#define CXA 1
#define CHUNK(b) ((b) < 14 ? 4 * (b) + 3 : ((b) - 14) + ((((b) - 14) * 43) >> 7))
  const unsigned adFL = ldsb + L_FL + 4 * (l & 7), myFL = ldsb + L_FL + 4 * w;
  const __amdgpu_buffer_rsrc_t wse = __builtin_amdgcn_make_buffer_rsrc(ws, 0, 64, 0x00020000);
  const int r15 = l & 15, g4 = l >> 4, q = (l & 15) >> 2, p = l & 3;

  if (w != 4 && w != 5) {
  if (w >= 6) {
    const int mw = w - 6;
    unsigned pf = 0;
    if (PF && !(xcd & 2) && !sp && (mw == 0 || PF == 3 || (PF == 2 && (ii >> 2)))) {
      const unsigned po = (((rot + 2 - 2 * mw + (ii >> 2)) & 15) * 64 + l) * 6144 + (j & 3) * 2048 + (hp | 3) * 128;
      if (PFS) SLEEP(PFS);
      asm volatile("global_load_dword %0, %1, %2" : "+v"(pf) : "v"(po), "s"(kq) : "memory");
    }
    if (PA && mw == 1) {
      const unsigned po = (mb * 128 + (j & 3) * 32 + (l & 31)) * 2048 + (l >> 5) * 1024 + 384;
      if (PAS) SLEEP(PAS);
      asm volatile("global_load_dword %0, %1, %2" : "+v"(pf) : "v"(po), "s"(kx) : "memory");
    }
    GETTAG(tag);
    const int bq = ((int)(HQT >> (4 * hp)) & 15) * 4 + (ii & 3), ksp = (xcd & 1) * 2 + (ii >> 2);
    const int chunk = CHUNK(bq), bg = (CXA || bq < 14) ? bq ^ (w & 1) : bq, chg = CHUNK(bg);
    auto helper = [&](auto SP) {
    constexpr bool spare = decltype(SP)::v, moe = !spare && !NOMOE;
    int nbar = 0;
#define TICK() do { if (HB) { BST(nbar); asm volatile("" : "+v"(BSTV)); } SBAR(); BAR(); SBAR(); ++nbar; } while (0)
#define DTICK(lim) do { if (nbar < (lim)) TICK(); else SLEEP(2); } while (0)
    const int hd = hp * 2 + mw;
    const u4 pz = (u4)(POIS);
    MKWS(tag);
    const int np = l & 7, kset = (l >> 3) & 1;
    u4 gw[4][8], dw[2][8];
    const __amdgpu_buffer_rsrc_t wgr = __builtin_amdgcn_make_buffer_rsrc((void*)(mw ? wu : wg), 0, D * F * 2, 0x00020000);
    const __amdgpu_buffer_rsrc_t wdr = __builtin_amdgcn_make_buffer_rsrc((void*)wd, 0, D * F * 2, 0x00020000);
    const __amdgpu_buffer_rsrc_t wor = __builtin_amdgcn_make_buffer_rsrc((void*)wo, 0, D * D * 2, 0x00020000);
    const unsigned gvo = ((256 * ksp + 32 * kset + 8 * g4) * F + 64 * chg + 8 * np) * 2;
    const unsigned dvo = ((64 * chunk + 8 * g4) * D + 256 * ksp + 128 * mw + 8 * r15) * 2;
    auto burst = [&](int k, int i0 = 0, int i1 = 16) {
      if (moe) {
#pragma unroll
        for (int i = i0; i < i1; ++i) {
          if (k < 2) gw[(2 * k + (i >> 3)) & 3][i & 7] = __builtin_amdgcn_raw_buffer_load_b128(wgr, gvo, (128 * k + 64 * (i >> 3) + (i & 7)) * F * 2, 2);
          else dw[i >> 3][i & 7] = __builtin_amdgcn_raw_buffer_load_b128(wdr, dvo, (32 * (i >> 3) + (i & 7)) * D * 2, 2);
        }
      }
    };
    auto transp = [&](u4(&v)[8]) {
      if (moe) {
        u4 f[8];
#pragma unroll
        for (int c = 0; c < 8; ++c)
#pragma unroll
          for (int e = 0; e < 4; ++e) {
            const unsigned ev = v[2 * e][c >> 1], od = v[2 * e + 1][c >> 1];
            f[c][e] = (c & 1) ? __builtin_amdgcn_perm(od, ev, 0x07060302u) : __builtin_amdgcn_perm(od, ev, 0x05040100u);
          }
#pragma unroll
        for (int c = 0; c < 8; ++c) { v[c] = f[c]; asm volatile("" : "+v"(v[c])); }
      }
    };
    auto pois = [&]() {
      ST128(pz, WS_KVP + (hd * 16 + mb) * 2048 + l * 16); ST128(pz, WS_KVP + (hd * 16 + mb) * 2048 + 1024 + l * 16); ST128(pz, WS_ZP + (hd * 16 + mb) * 128 + (l & 7) * 16);
    };
    unsigned wt0, wt1, wt2 = 0, wt3 = 0;
    auto touch = [&]() {
      const unsigned to = (j * 32 + mw * 16 + (l >> 3)) * 2048 + (xcd >> 1) * 512 + (l & 7) * 64;
      wt0 = __builtin_amdgcn_raw_buffer_load_b32(wor, to, 0, 0); wt1 = __builtin_amdgcn_raw_buffer_load_b32(wor, to, 16384, 0);
      if (TLBW) {
        const __amdgpu_buffer_rsrc_t outr = __builtin_amdgcn_make_buffer_rsrc(out, 0, 2048 * D * 2, 0x00020000);
        wt2 = __builtin_amdgcn_raw_buffer_load_b32(outr, (mb * 128 + mw * 64 + (l & 3) * 16) * 2048 + nb * 128, 0, 0);
        wt3 = __builtin_amdgcn_raw_buffer_load_b32(wsr, (l & 1) ? WS_GP + g * 4096 + mw * 2048 : WS_YP + (g >> 2) * 32768 + (l & 2) * 8192, 0, 0);
      }
    };
    s4 sb[24];
    auto colrd = [&](int h) {
      const unsigned bo = ldsb + (1 + h) * STG + 16384 + (8 * g4 + q) * 128 + (p & 1) * 8;
      const int sw = ((q >> 1) << 2) | ((g4 & 1) << 1);
      const unsigned b0 = bo + (((mw * 4 + (p >> 1)) ^ sw) * 16), b1 = bo + (((mw * 4 + 2 + (p >> 1)) ^ sw) * 16);
#pragma unroll
      for (int n = 0; n < 12; ++n) { RDTR(sb[2 * n], (n & 1) ? b1 : b0, ((n % 6) >> 1) * 8192 + (n / 6) * 4096); RDTR(sb[2 * n + 1], (n & 1) ? b1 : b0, ((n % 6) >> 1) * 8192 + (n / 6) * 4096 + 512); }
      WAITL(0);
      SBAR();
    };
    auto colsum = [&]() {
      const u4 ones = (u4)(0x3F803F80u);
      f4 c[6];
#pragma unroll
      for (int n = 0; n < 12; ++n) c[n % 6] = MFMA16(S8(ones), CAT(sb[2 * n], sb[2 * n + 1]), n >= 6 ? c[n % 6] : (f4)(0.f));
      const u4 csg = (u4){__float_as_uint(g4 ? c[3][0] : c[0][0]), __float_as_uint(g4 ? c[4][0] : c[1][0]), __float_as_uint(g4 ? c[5][0] : c[2][0]), tag};
      WAITV(0);
      asm volatile("" : "+v"(cw) : "v"(pf));
      ST128(csg, l < 32 ? WS_CS + (hd * 16 + mb) * 512 + l * 16 : WS_SZ);
    };
    u4 stv, cv[8], cd; float cs[3];
    auto gload = [&]() {
      stv = LD128(WS_ST + (mb * 128 + mw * 64 + l) * 16);
#pragma unroll
      for (int i = 0; i < 8; ++i) cv[i] = LD128(WS_CS + hd * 8192 + i * 1024 + l * 16);
    };
    auto gbad = [&]() {
      unsigned bad = (stv[1] ^ tag) | (stv[3] ^ tag);
#pragma unroll
      for (int i = 0; i < 8; ++i) bad |= cv[i][3] ^ tag;
      return __ballot(bad != 0u) != 0ull;
    };
    int idx;
    auto gfin = [&]() {
      if (gbad()) { RETRY(); _Pragma("unroll 1") for (unsigned it = 0; it < SPIN; ++it) { SLEEP(2); gload(); if (!gbad()) break; } }
#pragma unroll
      for (int e = 0; e < 3; ++e) {
        float a = __uint_as_float(cv[0][e]);
#pragma unroll
        for (int i = 1; i < 8; ++i) a += __uint_as_float(cv[i][e]);
        auto r = __builtin_amdgcn_permlane32_swap(__float_as_uint(a), __float_as_uint(a), false, false);
        cs[e] = __uint_as_float(r[0]) + __uint_as_float(r[1]);
      }
      asm volatile("" : "+v"(cs[0]), "+v"(cs[1]), "+v"(cs[2]) : "v"(wt0), "v"(wt1), "v"(wt2), "v"(wt3));
    };
    auto cload = [&]() { cd = LD128(WS_CAND + (((hp >> 2) == 2) * 16 + mb) * 128 + (l & 7) * 16); };
    auto cfin = [&]() {
      if (__builtin_expect(__ballot(cd[1] != tag || cd[3] != tag) != 0ull, 0)) {
        RETRY();
        _Pragma("unroll 1") for (unsigned it = 0; it < SPIN; ++it) { SLEEP(4); cload(); if (__ballot(cd[1] != tag || cd[3] != tag) == 0ull) break; }
      }
      cd[0] = max(cd[0], cd[2]);
      idx = (int)(cd[0] & 2047u);
    };
    auto lpub = [&]() {
      *(LAS float*)(lds + L_M + (mw * 64 + l) * 4) = __uint_as_float(stv[0]);
      *(LAS float*)(lds + L_RS + (mw * 64 + l) * 4) = __uint_as_float(stv[2]);
      *(LAS float*)(lds + L_RL + (mw * 64 + l) * 4) = __uint_as_float(stv[2]) * 1.44269504f;
      if (l < 32) {
#pragma unroll
        for (int e = 0; e < 3; ++e) *(LAS float*)(lds + L_CS + mw * 384 + ((3 * g4 + e) * 16 + r15) * 4) = -cs[e];
      }
      if (mw == 0 && l < 8) *(LAS int*)(lds + L_SEL + l * 4) = idx;
      lsig(myFL, 1u);
    };
    u4 xs[4]; float xrs;
    asm volatile("" ::"s"(wg), "s"(wu), "s"(wd), "v"(cw));
    if (HPRIO) __builtin_amdgcn_s_setprio(HPRIO);
    TICK();
    if (spare) {
      TICK();
      pois();
      if (ii >> 2) TICK();
      colrd(ii >> 2);
      if (!(ii >> 2)) TICK();
      u4 kk[8];
      auto kload = [&]() {
#pragma unroll
        for (int i = 0; i < 8; ++i) kk[i] = LD128(WS_RG + mw * 8192 + i * 1024 + l * 16);
      };
      kload();
      TICK();
      _Pragma("unroll 1") for (unsigned it = 0; it < SPIN; ++it) {
        unsigned bad = 0;
#pragma unroll
        for (int i = 0; i < 8; ++i) { bad |= (kk[i][1] ^ tag) | (kk[i][3] ^ tag); asm volatile("" : "+v"(bad)); }
        if (__ballot(bad != 0u) == 0ull) break;
        RETRY();
        kload();
        DTICK(7);
      }
      __builtin_amdgcn_s_setprio(3);
      unsigned m1 = max(kk[0][0], kk[0][2]), m2 = min(kk[0][0], kk[0][2]), m3 = 0;
#pragma unroll
      for (int i = 2; i < 16; ++i) {
        const unsigned a = kk[i >> 1][(i & 1) * 2];
        asm("v_med3_u32 %0, %1, %2, %0" : "+v"(m3) : "v"(m2), "v"(a));
        asm("v_med3_u32 %0, %1, %2, %0" : "+v"(m2) : "v"(m1), "v"(a));
        m1 = max(m1, a);
      }
      unsigned mine8 = 0;
#pragma unroll
      for (int it = 0; it < 8; ++it) {
        const unsigned km = wave_max(m1);
        mine8 = l == it ? km : mine8;
        const bool win = m1 == km;
        if (it < 7) { m1 = win ? m2 : m1; m2 = win ? m3 : m2; m3 = win ? 0u : m3; }
        if (it == 3 && nbar < 7) TICK();
      }
      if (__builtin_expect(__ballot(m1 == 0u) != 0ull, 0)) {
        unsigned lim = 0;
#pragma unroll 1
        for (int it = 0; it < 8; ++it) {
          unsigned m = 0;
#pragma unroll
          for (int i = 0; i < 8; ++i) m = umax3(m, kk[i][0] - lim, kk[i][2] - lim);
          lim += wave_max(m);
          mine8 = l == it ? lim : mine8;
        }
      }
      ST64(((u2){mine8, tag}), l < 8 ? WS_CAND + ((hp >> 3) * 16 + mb) * 128 + (mw ? 7 - l : l) * 16 + mw * 8 : WS_SZ);
      __builtin_amdgcn_s_setprio(HPRIO);
      STMP(2);
      TICK();
      colsum();
      DTICK(9);
      touch();
      while (nbar < 10) TICK();
      gload(); cload();
      if (RT && mw) asm volatile("global_load_dword %0, %1, %2 sc1" : "+v"(cw) : "v"(cwo), "s"(cwb) : "memory");
      TICK(); TICK();
      WAITV(0);
      asm volatile("" : "+v"(cw));
      gfin(); cfin();
      STMP(3);
      TICK(); TICK(); TICK();
      lpub();
    } else {
      if (PV == 0) pois();
      TICK();
      if (PV == 1) pois();
      if (ii >> 2) TICK();
      colrd(ii >> 2);
      TICK();
      if (CL && !(ii >> 2)) TICK();
      colsum();
      if (!CL && !(ii >> 2)) TICK();
      u4 sv; f4 acc[8];
      auto xload = [&]() {
        const u16* xp = x + (size_t)idx * D + 256 * ksp + 32 * kset + 8 * g4;
        sv = LD128(WS_ST + idx * 16);
#pragma unroll
        for (int it = 0; it < 4; ++it) xs[it] = *(const u4*)(xp + 64 * it);
        SBAR();
      };
      auto xnorm = [&]() {
        if (__builtin_expect(__ballot(sv[1] != tag || sv[3] != tag) != 0ull, 0)) {
          _Pragma("unroll 1") for (unsigned it = 0; it < SPIN; ++it) { SLEEP(1); sv = LD128(WS_ST + idx * 16); if (__ballot(sv[1] != tag || sv[3] != tag) == 0ull) break; }
        }
        const float nm = -__uint_as_float(sv[0]);
        xrs = __uint_as_float(sv[2]);
#pragma unroll
        for (int it = 0; it < 4; ++it) {
          float t0, t1, t2, t3; unsigned x0 = xs[it][0], x1 = xs[it][1], x2 = xs[it][2], x3 = xs[it][3];
          asm volatile("v_dot2_f32_bf16 %4, %0, %8, %10\n v_dot2_f32_bf16 %0, %0, %9, %10\n v_dot2_f32_bf16 %5, %1, %8, %10\n v_dot2_f32_bf16 %1, %1, %9, %10\n"
                       "v_dot2_f32_bf16 %6, %2, %8, %10\n v_dot2_f32_bf16 %2, %2, %9, %10\n v_dot2_f32_bf16 %7, %3, %8, %10\n v_dot2_f32_bf16 %3, %3, %9, %10\n"
                       "v_cvt_pk_bf16_f32 %0, %4, %0\n v_cvt_pk_bf16_f32 %1, %5, %1\n v_cvt_pk_bf16_f32 %2, %6, %2\n s_nop 0\n v_cvt_pk_bf16_f32 %3, %7, %3"
                       : "+v"(x0), "+v"(x1), "+v"(x2), "+v"(x3), "=&v"(t0), "=&v"(t1), "=&v"(t2), "=&v"(t3) : "s"(0x00003F80u), "s"(0x3F800000u), "v"(nm));
          xs[it] = (u4){x0, x1, x2, x3};
        }
        asm volatile("s_nop 1" : "+v"(xs[0]), "+v"(xs[1]), "+v"(xs[2]), "+v"(xs[3]), "+v"(xrs));
        SBAR();
      };
      auto gmul = [&](int i0, int i1) {
#pragma unroll
        for (int it = i0; it < i1; ++it)
#pragma unroll
          for (int c = 0; c < 8; ++c) acc[c] = MFMA16(S8(gw[it][c]), S8(xs[it]), it ? acc[c] : (f4)(0.f));
#pragma unroll
        for (int c = 0; c < 8; ++c) asm volatile("" : "+v"(acc[c]));
        SBAR();
      };
      auto gout = [&]() {
        unsigned go = r15 < 8 ? WS_GP + ((chg * 4 + ksp) * 2 + mw) * 2048 + (g4 >> 1) * 1024 + (g4 & 1) * 512 + r15 * 16 : WS_SZ;
        asm volatile("" : "+v"(go));
#pragma unroll
        for (int i = 0; i < 4; ++i) {
          unsigned a[4], b[4];
#pragma unroll
          for (int c = 0; c < 4; ++c) {
            auto r = __builtin_amdgcn_permlane32_swap(__float_as_uint(acc[c][i]), __float_as_uint(acc[c + 4][i]), false, false);
            a[c] = r[0]; b[c] = r[1];
          }
          unsigned s0, s1, s2, s3;
#define AR(d, x, y) "v_add_f32_dpp %" #d ", %" #x ", %" #y " row_ror:8 row_mask:0xf bank_mask:0xf\n"
          asm volatile("s_nop 1\n" AR(0, 8, 4) AR(1, 9, 5) AR(2, 10, 6) AR(3, 11, 7) : "=&v"(s0), "=&v"(s1), "=&v"(s2), "=&v"(s3)
                       : "v"(a[0]), "v"(a[1]), "v"(a[2]), "v"(a[3]), "v"(b[0]), "v"(b[1]), "v"(b[2]), "v"(b[3]));
          ST128(tagf(__builtin_bit_cast(f4, ((u4){s0, s1, s2, s3})), tag), go + i * 128);
        }
        SBAR();
      };
      for (int i = 0; i < ph; ++i) TICK();
      burst(0); touch();
      TICK();
      TICK();
      if (TL) { transp(gw[0]); transp(gw[1]); }
      TICK();
      if (!TL) transp(gw[0]);
      TICK();
      if (!TL) transp(gw[1]);
      TICK();
      if (ph) cload();
      if (RT && mw) asm volatile("global_load_dword %0, %1, %2 sc1" : "+v"(cw) : "v"(cwo), "s"(cwb) : "memory");
      SBAR();
      burst(1);
      TICK();
      TICK(); TICK();
      transp(gw[2]);
      WAITV(0);
      asm volatile("" : "+v"(cw));
      if (ph < 2) TICK();
      transp(gw[3]);
      if (ph < 1) { SBAR(); cload(); TICK(); }
      SBAR();
      if (XO == 0) { cfin(); xload(); gload(); } else { gload(); SBAR(); cfin(); xload(); }
      TICK();
      gfin();
      lpub();
      STMP(12);
      SBAR();
      __builtin_amdgcn_s_setprio(0);
      burst(2, 0, D1);
      STMP(3);
      SBAR();
      if (HS0) SLEEP(HS0);
      if (G1 & 1) { lwait<HW1>(adFL, 0x0Fu, G1 >> 4); if (G1S) SLEEP(G1S); __builtin_amdgcn_s_setprio(TP0); }
      xnorm();
      if (G1 & 2) { lwait<2>(adFL, 0x0Fu, G1 >> 4); if (G1S) SLEEP(G1S); __builtin_amdgcn_s_setprio(TP0); }
      STMP(5);
      gmul(0, 4);
      gout();
      STMP(6);
      SBAR();
    }
    __builtin_amdgcn_s_setprio(TP1);
    STMP(4);
    if (moe) {
      if (D2 == 0) { burst(2, D1, 16); SBAR(); }
      if (D1 >= 8 || D2 == 0) transp(dw[0]); if (D1 == 16 || D2 == 0) transp(dw[1]);
      SBAR();
      lwait<2>(adFL, 0x30u, 3);
      if (G2S) SLEEP(G2S);
      STMP(7);
      if (D2 == 1) { burst(2, D1, 16); SBAR(); }
      f4 acc[8];
      u4 ha[2];
      {
        u4 pg[4][2];
        auto pload = [&]() {
#pragma unroll
          for (int ks = 0; ks < 4; ++ks)
#pragma unroll
            for (int t = 0; t < 2; ++t) pg[ks][t] = LD128(WS_GP + ((chunk * 4 + ks) * 4 + 2 * t + mw) * 1024 + (4 * kset + g4) * 128 + np * 16);
        };
        auto pbad = [&]() {
          unsigned bad = 0;
#pragma unroll
          for (int i = 0; i < 8; ++i) untag(pg[i >> 1][i & 1], tag, bad);
          return __ballot((bad & 255u) != 0u) != 0ull;
        };
        pload();
        SBAR();
        if (D2 == 2) { burst(2, D1, 16); SBAR(); }
        __builtin_amdgcn_s_setprio(TP2);
        if (D1 < 8 && D2 == 1) transp(dw[0]);
        if (D1 < 16 && D2 == 1) transp(dw[1]);
        SBAR();
        if (__builtin_expect(pbad(), 0)) { _Pragma("unroll 1") for (unsigned it = 0; it < SPIN; ++it) { RETRY2(); SLEEP(4); pload(); if (!pbad()) break; } }
        STMP(10);
        f4 gq[2];
#pragma unroll
        for (int t = 0; t < 2; ++t)
          gq[t] = ((__builtin_bit_cast(f4, pg[0][t]) + __builtin_bit_cast(f4, pg[1][t])) + (__builtin_bit_cast(f4, pg[2][t]) + __builtin_bit_cast(f4, pg[3][t]))) * xrs;
        float hv[4];
#pragma unroll
        for (int e = 0; e < 4; ++e) {
          const float gg = rbf(gq[0][e]), uu = rbf(gq[1][e]);
          hv[e] = rbf(gg / (1.f + __expf(-gg))) * uu;
        }
        *(LAS u2*)(lds + L_HX + l * 16 + mw * 8) = (u2){pk2(hv[0], hv[1]), pk2(hv[2], hv[3])};
        lsig(myFL, 2u);
        if (D1 < 16 && D2 == 2) { SBAR(); if (D1 < 8) transp(dw[0]); transp(dw[1]); SBAR(); }
        lwait(adFL, 0xC0u, 2);
        ha[0] = *(const LAS u4*)(lds + L_HX + l * 16); ha[1] = *(const LAS u4*)(lds + L_HX + (l ^ 8) * 16);
      }
#pragma unroll
      for (int it = 0; it < 2; ++it)
#pragma unroll
        for (int c = 0; c < 8; ++c) acc[c] = MFMA16(S8(ha[it]), S8(dw[it][c]), it ? acc[c] : (f4)(0.f));
      if (g4 < 2) {
#pragma unroll
        for (int i = 0; i < 4; ++i) {
          const unsigned yo = WS_YP + ((chunk * 8 + 4 * g4 + i) * D + 256 * ksp + 128 * mw + 4 * r15) * 4;
          ST128(tagf((f4){acc[0][i], acc[1][i], acc[2][i], acc[3][i]}, tag), yo);
          ST128(tagf((f4){acc[4][i], acc[5][i], acc[6][i], acc[7][i]}, tag), yo + 256);
        }
      }
      STMP(11);
    }
    float sg;
    {
      const unsigned mono = cd[0] >> 16, rb = (mono & 0x8000u) ? (mono & 0x7FFFu) : (~mono & 0xFFFFu);
      sg = rbf(1.f / (1.f + __expf(-lo(rb))));
    }
    unsigned mine = (unsigned)__ballot(!NOMOE && l < 8 && (idx >> 7) == mb && (l & 1) == mw);
    if (mine) {
      if (spare) lwait<4>(adFL, 0x30u, 3);
      {
        const u2 f = LD64(WS_OH + (mb * 16 + r15) * 128);
        const unsigned t0 = NOW();
        unsigned rel = f[1] + YD - t0;
        rel = wave_max(f[0] == tag && rel < 600u ? rel : 0u);
        _Pragma("unroll 1") for (unsigned it = 0; it < SPIN; ++it) { if (NOW() - t0 >= rel) break; SLEEP(2); }
      }
      STMP(8);
      lwait<1>(adFL, 0x0Fu, 4);
      __builtin_amdgcn_s_setprio(TP2);
      while (mine) {
        const int t = __builtin_ctz(mine); mine &= mine - 1;
        u4 yv[14];
        _Pragma("unroll 1") for (unsigned it = 0; it < SPIN; ++it) {
          unsigned bad = 0;
#pragma unroll
          for (int i = 0; i < 14; ++i) yv[i] = LD128(WS_YP + (((i * 4 + g4) * 8 + t) * D + (nb >> 1) * 128 + (r15 >> 3) * 64 + (nb & 1) * 32 + (r15 & 7) * 4) * 4);
          SBAR();
#pragma unroll
          for (int i = 0; i < 14; ++i) untag(yv[i], tag, bad);
          if (__ballot((bad & 255u) != 0u) == 0ull) break;
          RETRY();
          SLEEP(YSL);
        }
        f4 y = __builtin_bit_cast(f4, yv[0]);
#pragma unroll
        for (int i = 1; i < 14; ++i) y += __builtin_bit_cast(f4, yv[i]);
        const float gt = __int_as_float(__builtin_amdgcn_readlane(__float_as_int(sg), t));
#pragma unroll
        for (int e = 0; e < 4; ++e) {
          auto r = __builtin_amdgcn_permlane32_swap(__float_as_uint(y[e]), __float_as_uint(y[e]), false, false);
          const float a = __uint_as_float(r[0]) + __uint_as_float(r[1]);
          auto r2 = __builtin_amdgcn_permlane16_swap(__float_as_uint(a), __float_as_uint(a), false, false);
          y[e] = rbf(rbf(__uint_as_float(r2[0]) + __uint_as_float(r2[1])) * gt);
        }
        if (l < 16) *(LAS f4*)(lds + L_KVX + t * 256 + (l & 7) * 32 + (l >> 3) * 16) = y;
      }
    }
    lsig(myFL, 3u);
    STMP(9);
    STFLUSH();
    if (spare) asm volatile(".Lend_s:"); else asm volatile(".Lend_h:");
    };
    if (sp) helper(BC<true>()); else helper(BC<false>());
    return;
  }

  const int wr = w >> 1, wc = w & 1;
  u2 kg; u4 sg4; unsigned tag;
  {
    const int row = srow;
    SBAR(); BST(0); SBAR();
    asm volatile("s_waitcnt vmcnt(4)" : "+v"(xa[0]), "+v"(xa[1]), "+v"(xa[2]), "+v"(xa[3]));
    SBAR(); BST(1); SBAR();
    float s0 = 0, s1 = 0;
#pragma unroll
    for (int c = 0; c < 4; ++c)
#pragma unroll
      for (int e = 0; e < 4; e += 2) { unsigned x0 = xa[c][e], x1 = xa[c][e + 1]; s0 = dot2(x0, 0x3F803F80u, s0); s1 = dot2(x1, 0x3F803F80u, s1); }
    const float m = half_sum(s0 + s1) * (1.f / D);
    f2 v[16], q2 = (f2)(0.f), q3 = (f2)(0.f);
#pragma unroll
    for (int c = 0; c < 16; ++c) v[c] = (f2){lo(xa[c >> 2][c & 3]), hi(xa[c >> 2][c & 3])} - m;
#pragma unroll
    for (int c = 0; c < 16; c += 2) { q2 += v[c] * v[c]; q3 += v[c + 1] * v[c + 1]; }
    q2 += q3;
    const float mr = rbf(m);
    const float rs = 1.f / rbf(sqrtf(rbf(rbf(half_sum(q2[0] + q2[1]) * (1.f / D)) + 1e-5f)));
    float d0 = 0, d1 = 0;
    asm volatile("s_waitcnt vmcnt(0)" : "+v"(rw[0]), "+v"(rw[1]), "+v"(rw[2]), "+v"(rw[3]), "+v"(d0) : "v"(rs));
#pragma unroll
    for (int c = 0; c < 16; c += 2) {
      const f2 e0 = (f2){lo(xa[c >> 2][c & 3]), hi(xa[c >> 2][c & 3])} - mr, e1 = (f2){lo(xa[c >> 2][(c & 3) + 1]), hi(xa[c >> 2][(c & 3) + 1])} - mr;
      const unsigned p0 = pk2(e0[0], e0[1]), p1 = pk2(e1[0], e1[1]);
      const f2 n0 = (f2){lo(p0), hi(p0)} * rs, n1 = (f2){lo(p1), hi(p1)} * rs;
      unsigned c0 = rw[c >> 2][c & 3], c1 = rw[c >> 2][(c & 3) + 1];
      d0 = dot2(pk2(n0[0], n0[1]), c0, d0); d1 = dot2(pk2(n1[0], n1[1]), c1, d1);
    }
    const float d = half_sum(d0 + d1);
    const unsigned rb = pk2(d, d) & 0xFFFFu;
    const unsigned mono = (rb & 0x8000u) ? (~rb & 0xFFFFu) : (rb | 0x8000u);
    SBAR();
    GETTAG(tg); tag = tg;
    kg = (u2){(mono << 16) | (unsigned)row, tag};
    sg4 = (u4){__float_as_uint(mr), tag, __float_as_uint(rs), tag};
  }
  MKWS(tag);
  SBAR(); BST(2); SBAR();
  if ((l & 31) == 0) { ST64(kg, WS_RG + srow * 8); ST128(sg4, WS_ST + srow * 16); }
  unsigned aad[2], bad[2];
#pragma unroll
  for (int s = 0; s < 2; ++s) aad[s] = ldsb + (wr * 64 + r15) * 128 + (((4 * s + g4) ^ ((r15 >> 1) & 7)) * 16);
#pragma unroll
  for (int nh = 0; nh < 2; ++nh) bad[nh] = ldsb + 16384 + (8 * g4 + q) * 128 + (((wc * 4 + 2 * (nh ^ wr) + (p >> 1)) ^ (((q >> 1) << 2) | ((g4 & 1) << 1))) * 16) + (p & 1) * 8;
  f4 acc[4][6];
#pragma unroll
  for (int m = 0; m < 4; ++m)
#pragma unroll
    for (int n = 0; n < 6; ++n) { acc[m][n] = (f4)(0.f); asm volatile("" : "+v"(acc[m][n])); }
#define MF(m, n, Fq) do { if (n < 2) acc[m][n] = MFMA16(CAT(Fq.b[2 * n], Fq.b[2 * n + 1]), Fq.a[m], acc[m][n]); \
     else acc[m][n] = MFMA16(Fq.a[m], CAT(Fq.b[2 * n], Fq.b[2 * n + 1]), acc[m][n]); SBAR(); } while (0)
#define RB(G, n, vb0, vb1, SS, hf) RDTR(G.b[2 * (n) + (hf)], (((n) & 1) ? vb1 : vb0), ((n) >> 1) * 8192 + (SS) * 4096 + (hf) * 512)
#define RA(G, m, va) RD128(G.a[m], va, (m) * 2048)
#define SUB(Fq, G, va, vb0, vb1, SS, PRE) do { \
    WAITL(13); SBAR(); \
    MF(0, 0, Fq); PRE; RA(G, 0, va); SBAR(); \
    WAITL(11); SBAR(); \
    MF(1, 0, Fq); RB(G, 0, vb0, vb1, SS, 0); SBAR(); MF(2, 0, Fq); RB(G, 0, vb0, vb1, SS, 1); SBAR(); MF(3, 0, Fq); RA(G, 1, va); SBAR(); \
    WAITL(12); SBAR(); \
    MF(0, 1, Fq); RA(G, 2, va); SBAR(); MF(1, 1, Fq); RA(G, 3, va); SBAR(); MF(2, 1, Fq); RB(G, 1, vb0, vb1, SS, 0); SBAR(); MF(3, 1, Fq); RB(G, 1, vb0, vb1, SS, 1); SBAR(); \
    WAITL(8); SBAR(); \
    MF(0, 2, Fq); RB(G, 2, vb0, vb1, SS, 0); SBAR(); MF(1, 2, Fq); RB(G, 2, vb0, vb1, SS, 1); SBAR(); MF(2, 2, Fq); RB(G, 3, vb0, vb1, SS, 0); SBAR(); MF(3, 2, Fq); RB(G, 3, vb0, vb1, SS, 1); SBAR(); \
    MF(0, 3, Fq); RB(G, 4, vb0, vb1, SS, 0); SBAR(); MF(1, 3, Fq); RB(G, 4, vb0, vb1, SS, 1); SBAR(); MF(2, 3, Fq); RB(G, 5, vb0, vb1, SS, 0); SBAR(); MF(3, 3, Fq); RB(G, 5, vb0, vb1, SS, 1); SBAR(); \
    MF(0, 4, Fq); MF(1, 4, Fq); MF(2, 4, Fq); MF(3, 4, Fq); MF(0, 5, Fq); MF(1, 5, Fq); MF(2, 5, Fq); MF(3, 5, Fq); } while (0)
#define KSTEP(MODE) do { \
    if (kt == sx) { const unsigned xo = xad + buf * STG; \
      _Pragma("unroll") for (int t = 0; t < 8; ++t) { const unsigned a_ = xo ^ (t * 16); \
        asm volatile("ds_read_b64 %0, %1" : "+v"(xv[t]) : "v"(a_) : "memory"); } \
      WAITL(0); SBAR(); } \
    const int nbuf = (buf + 1) & 3; \
    const unsigned bo = buf * STG, nbo = nbuf * STG; \
    { unsigned va = aad[1] + bo, vb0 = bad[0] + bo, vb1 = bad[1] + bo; SUB(f0, f1, va, vb0, vb1, 1, ); } \
    if (MODE < 2) { \
      unsigned va = aad[0] + nbo, vb0 = bad[0] + nbo, vb1 = bad[1] + nbo; \
      SUB(f1, f0, va, vb0, vb1, 0, if (MODE == 0) { WAITL(0); BAR(); } else asm volatile("ds_write_b32 %0, %1" ::"v"(myFL), "v"(1u) : "memory"); SBAR()); \
    } else { asm volatile("ds_write_b32 %1, %2\n ds_read_b32 %0, %3\n s_waitcnt lgkmcnt(2)" : "=&v"(fl0) : "v"(myFL), "v"(2u), "v"(adFL) : "memory"); SBAR(); \
      MF(0, 2, f1); MF(1, 2, f1); MF(2, 2, f1); MF(3, 2, f1); MF(0, 3, f1); MF(1, 3, f1); MF(2, 3, f1); MF(3, 3, f1); \
      MF(0, 4, f1); MF(1, 4, f1); MF(2, 4, f1); MF(3, 4, f1); MF(0, 5, f1); MF(1, 5, f1); MF(2, 5, f1); MF(3, 5, f1); \
      MF(0, 0, f1); MF(1, 0, f1); MF(2, 0, f1); MF(3, 0, f1); MF(0, 1, f1); MF(1, 1, f1); MF(2, 1, f1); MF(3, 1, f1); } \
    buf = nbuf; } while (0)
#define SUBK(Fq, G, va, vb0, vb1, SS, W0, PRE) do { \
    WAITL(W0); SBAR(); \
    MF(0, 2, Fq); PRE; RA(G, 0, va); SBAR(); \
    WAITL(7); SBAR(); \
    MF(1, 2, Fq); RB(G, 2, vb0, vb1, SS, 0); SBAR(); MF(2, 2, Fq); RB(G, 2, vb0, vb1, SS, 1); SBAR(); MF(3, 2, Fq); RA(G, 1, va); SBAR(); \
    WAITL(8); SBAR(); \
    MF(0, 3, Fq); RA(G, 2, va); SBAR(); MF(1, 3, Fq); RA(G, 3, va); SBAR(); MF(2, 3, Fq); RB(G, 3, vb0, vb1, SS, 0); SBAR(); MF(3, 3, Fq); RB(G, 3, vb0, vb1, SS, 1); SBAR(); \
    WAITL(10); SBAR(); \
    MF(0, 4, Fq); RB(G, 4, vb0, vb1, SS, 0); SBAR(); MF(1, 4, Fq); RB(G, 4, vb0, vb1, SS, 1); SBAR(); MF(2, 4, Fq); RB(G, 5, vb0, vb1, SS, 0); SBAR(); MF(3, 4, Fq); RB(G, 5, vb0, vb1, SS, 1); SBAR(); \
    WAITL(12); SBAR(); \
    MF(0, 5, Fq); MF(1, 5, Fq); MF(2, 5, Fq); MF(3, 5, Fq); } while (0)
#define RQ(G, va, vb0, vb1, SS) do { RA(G, 0, va); RB(G, 0, vb0, vb1, SS, 0); RB(G, 0, vb0, vb1, SS, 1); RA(G, 1, va); RA(G, 2, va); RA(G, 3, va); \
    RB(G, 1, vb0, vb1, SS, 0); RB(G, 1, vb0, vb1, SS, 1); SBAR(); } while (0)
#define MQ(Fq) do { MF(0, 0, Fq); MF(1, 0, Fq); MF(2, 0, Fq); MF(3, 0, Fq); MF(0, 1, Fq); MF(1, 1, Fq); MF(2, 1, Fq); MF(3, 1, Fq); } while (0)
  u2 xv[8];
#pragma unroll
  for (int t = 0; t < 8; ++t) asm volatile("" : "=v"(xv[t]));
  const int sx = (nb - rot) & 15;
  const unsigned xad = ldsb + (w * 32 + (l & 31)) * 128 + ((r15 >> 1) & 7) * 16 + (l >> 5) * 8;
  STMP(1);
  WAITL(0);
  BAR();
  STMP(2);
  WAITL(0);
  FQ f0, f1;
  { unsigned va = aad[0], vb0 = bad[0], vb1 = bad[1];
    RA(f0, 0, va); RB(f0, 0, vb0, vb1, 0, 0); RB(f0, 0, vb0, vb1, 0, 1); RA(f0, 1, va); RA(f0, 2, va); RA(f0, 3, va);
    RB(f0, 1, vb0, vb1, 0, 0); RB(f0, 1, vb0, vb1, 0, 1); RB(f0, 2, vb0, vb1, 0, 0); RB(f0, 2, vb0, vb1, 0, 1); RB(f0, 3, vb0, vb1, 0, 0); RB(f0, 3, vb0, vb1, 0, 1);
    RB(f0, 4, vb0, vb1, 0, 0); RB(f0, 4, vb0, vb1, 0, 1); RB(f0, 5, vb0, vb1, 0, 0); RB(f0, 5, vb0, vb1, 0, 1); SBAR(); }
  unsigned sad = ldsb + L_M + wr * 256 + g4 * 16, cad = ldsb + L_CS + wc * 384 + r15 * 4 + wr * 64;
  const int hd = hp * 2 + wc;
  unsigned kvo = WS_KVP + (hd * 16 + mb) * 2048 + wr * 1024 + l * 16, zo = r15 == 0 ? WS_ZP + (hd * 16 + mb) * 128 + g4 * 32 + wr * 16 : WS_SZ;
  unsigned xw = ldsb + L_KVX + w * 3072 + l * 16, xr = ldsb + L_KVX + (w ^ 2) * 3072 + l * 16;
  asm volatile("s_waitcnt lgkmcnt(0)" : "+v"(sad), "+v"(cad), "+v"(kvo), "+v"(zo), "+v"(xw), "+v"(xr) : : "memory"); SBAR();
  int buf = 0; unsigned fl0;
#pragma unroll 1
  for (int kt = 0; kt < 14; ++kt) KSTEP(0);
  STMP(14); SBAR();
  f4 sm[4], sr[4], sl[4]; f2 ck, cv;
  unsigned c2 = 2u, c3 = 3u, vfl = myFL, b30, b31;
  u4 ones = (u4)(0x3F803F80u);
#define SC0() asm volatile("ds_read_b32 %0, %2 offset:128\n ds_read_b32 %1, %2 offset:256" : "=&v"(ck[0]), "=&v"(cv[0]) : "v"(cad) : "memory")
#define SC1() asm volatile("ds_read_b32 %0, %2 offset:128\n ds_read_b32 %1, %2 offset:256" : "=&v"(ck[1]), "=&v"(cv[1]) : "v"(cad ^ 64) : "memory")
  const unsigned b20 = bad[0] + 2 * STG, b21 = bad[1] + 2 * STG;
#define SM(m) do { RD128(sm[m], sad, (m) * 64); RD128(sl[m], sad, L_RL - L_M + (m) * 64); RD128(sr[m], sad, L_RS - L_M + (m) * 64); } while (0)
  {
    { const unsigned va = aad[1] + 2 * STG; SUBK(f0, f1, va, b20, b21, 1, 6, asm volatile("" ::"v"(f0.b[0]), "v"(f0.b[1]), "v"(f0.b[2]), "v"(f0.b[3]))); }
    { const unsigned va = aad[0] + 3 * STG; b30 = bad[0] + 3 * STG; b31 = bad[1] + 3 * STG; SUBK(f1, f0, va, b30, b31, 0, 9, asm volatile("" : "+v"(c2), "+v"(c3), "+v"(vfl))); }
    STMP(7); SBAR();
    { const unsigned va = aad[1] + 3 * STG; SUBK(f0, f1, va, b30, b31, 1, 9, asm volatile("" : "+v"(ones))); }
    asm volatile("ds_write_b32 %1, %2\n ds_read_b32 %0, %3\n s_waitcnt lgkmcnt(8)" : "=&v"(fl0) : "v"(vfl), "v"(c2), "v"(adFL) : "memory"); SBAR();
    MF(0, 2, f1); SC0(); SBAR(); MF(1, 2, f1); SC1(); SBAR(); MF(2, 2, f1); MF(3, 2, f1); WAITL(10); SBAR();
    MF(0, 3, f1); SM(0); SBAR(); MF(1, 3, f1); MF(2, 3, f1); SM(1); SBAR(); MF(3, 3, f1);
    WAITL(12); SBAR();
    MF(0, 4, f1); SM(2); SBAR(); MF(1, 4, f1); MF(2, 4, f1); SM(3); SBAR(); MF(3, 4, f1); MF(0, 5, f1); MF(1, 5, f1); MF(2, 5, f1); MF(3, 5, f1);
  }
  asm volatile("s_waitcnt lgkmcnt(0)" : "+v"(fl0) : : "memory");
  if (__builtin_expect(((unsigned)__ballot(fl0 < 1u) & 0xC0u) != 0u, 0)) {
    lwait(adFL, 0xC0u, 1);
    SC0(); SC1(); SM(0); SM(1); SM(2); SM(3); WAITL(0);
  }
  asm volatile("" : "+v"(ck), "+v"(cv), "+v"(sm[0]), "+v"(sm[1]), "+v"(sm[2]), "+v"(sm[3]), "+v"(sr[0]), "+v"(sr[1]), "+v"(sr[2]), "+v"(sr[3]));
  asm volatile("" : "+v"(sl[0]), "+v"(sl[1]), "+v"(sl[2]), "+v"(sl[3]));
  STMP(3);
  __builtin_amdgcn_s_setprio(2);
  {
    f4 kv[4], zt[2];
    u4 kf[2][2], vf[2][2];
#define MKV(t, i) do { if ((i) == 2 || (i) == 5) zt[(i) == 2] = MFMA16(S8(kf[t][(i) == 2]), S8(ones), (t) ? zt[(i) == 2] : (f4)(0.f)); \
      else kv[((i) + 2) % 5] = MFMA16(S8(kf[t][(((i) + 2) % 5) & 1]), S8(vf[t][(((i) + 2) % 5) >> 1]), (t) ? kv[((i) + 2) % 5] : (f4)(0.f)); SBAR(); } while (0)
#pragma unroll
    for (int m = 0; m < 4; ++m) {
      const int t = m >> 1, mm = m & 1;
      f4 ea[2], pp[2];
#pragma unroll
      for (int a = 0; a < 2; ++a) {
        const f4 uv = (acc[m][4 + a] + sm[m] * cv[a]) * sr[m];
        vf[t][a][2 * mm] = pk2(uv[0], uv[1]); vf[t][a][2 * mm + 1] = pk2(uv[2], uv[3]);
        const f4 uk = acc[m][2 + a] + sm[m] * ck[a];
        ea[a] = uk * sl[m]; pp[a] = uk * sr[m] + 1.f;
      }
      SBAR();
#pragma unroll
      for (int e = 0; e < 8; ++e) { if (t && (e == 0 || e == 3 || e == 6)) MKV(0, 3 * mm + e / 3); ea[e >> 2][e & 3] = __builtin_amdgcn_exp2f(ea[e >> 2][e & 3]); SBAR(); }
#pragma unroll
      for (int e = 0; e < 8; ++e) pp[e >> 2][e & 3] = __builtin_amdgcn_fmed3f(ea[e >> 2][e & 3], pp[e >> 2][e & 3], 1.f);
      SBAR();
#pragma unroll
      for (int a = 0; a < 2; ++a) { pp[a] += 1e-6f; kf[t][a][2 * mm] = pk2(pp[a][0], pp[a][1]); kf[t][a][2 * mm + 1] = pk2(pp[a][2], pp[a][3]); }
      SBAR();
    }
    MKV(1, 0); MKV(1, 1); MKV(1, 2); MKV(1, 3);
    asm volatile("ds_write_b128 %0, %1" ::"v"(xw), "v"(kv[2]) : "memory"); SBAR();
    MKV(1, 4);
    asm volatile("ds_write_b128 %0, %1 offset:1024" ::"v"(xw), "v"(kv[3]) : "memory"); SBAR();
    MKV(1, 5);
    asm volatile("ds_write_b128 %0, %1 offset:2048\n ds_write_b32 %2, %3" ::"v"(xw), "v"(zt[1]), "v"(vfl), "v"(c3) : "memory");
    f4 r0, r1, rz;
    _Pragma("unroll 1") for (unsigned it = 0; it < SPIN; ++it) {
      unsigned v;
      asm volatile("ds_read_b32 %0, %4\n ds_read_b128 %1, %5 offset:1024\n ds_read_b128 %2, %5\n ds_read_b128 %3, %5 offset:2048\n s_waitcnt lgkmcnt(0)"
                   : "=&v"(v), "=&v"(r0), "=&v"(r1), "=&v"(rz) : "v"(adFL), "v"(xr) : "memory");
      if ((((unsigned)__ballot(v < 3u) >> (w ^ 2)) & 1u) == 0u) break;
    }
    const f4 s0 = kv[0] + r0, s1 = kv[1] + r1, sz = zt[0] + rz;
    u4 o; o[0] = min(pk2(s0[0], s0[1]), POIS - 1u); o[1] = pk2(s0[2], s0[3]); o[2] = pk2(s1[0], s1[1]); o[3] = pk2(s1[2], s1[3]);
    ST128(o, kvo);
    u4 zq = __builtin_bit_cast(u4, sz); zq[0] = min(zq[0], POIS - 1u);
    ST128(zq, zo);
  }
  STMP(4);
  {
    SBAR();
    const LAS float* Lm = (const LAS float*)(lds + L_M) + wr * 64 + r15;
    const LAS float* Lc = (const LAS float*)(lds + L_CS) + wc * 96 + 4 * g4;
    { const unsigned va = aad[0] + 2 * STG; RQ(f0, va, b20, b21, 0); }
    { const unsigned va = aad[1] + 2 * STG; RQ(f1, va, b20, b21, 1); }
    WAITL(8); SBAR(); MQ(f0);
    { const unsigned va = aad[0] + 3 * STG; RQ(f0, va, b30, b31, 0); }
    WAITL(8); SBAR(); MQ(f1);
    { const unsigned va = aad[1] + 3 * STG; RQ(f1, va, b30, b31, 1); }
    WAITL(8); SBAR();
    const f4 c0 = *(const LAS f4*)(Lc + 16 * wr), c1 = *(const LAS f4*)(Lc + 16 - 16 * wr);
    float mv[4], rv[4], rl[4];
#pragma unroll
    for (int m = 0; m < 4; ++m) { mv[m] = Lm[m * 16]; rv[m] = Lm[m * 16 + (L_RS - L_M) / 4]; rl[m] = Lm[m * 16 + (L_RL - L_M) / 4]; }
    SBAR(); MQ(f0);
    if (sx >= 14) {
      const unsigned xo = xad + (sx - 12) * STG;
#pragma unroll
      for (int t = 0; t < 8; ++t) { const unsigned a_ = xo ^ (t * 16); asm volatile("ds_read_b64 %0, %1" : "+v"(xv[t]) : "v"(a_) : "memory"); }
    }
    WAITL(0); SBAR(); MQ(f1);
    LAS char* pw = (LAS char*)lds + L_PQ + wr * 8192 + wc * 4096 + l * 16;
#pragma unroll
    for (int m = 0; m < 4; ++m) {
      const f4 u0 = acc[m][0] + c0 * mv[m], u1 = acc[m][1] + c1 * mv[m];
      *(LAS u2*)(pw + wr * 8 + m * 1024) = (u2){phi2((f2){u0[0], u0[1]}, (f2)(rv[m]), (f2)(rl[m])), phi2((f2){u0[2], u0[3]}, (f2)(rv[m]), (f2)(rl[m]))};
      *(LAS u2*)(pw + 8 - wr * 8 + m * 1024) = (u2){phi2((f2){u1[0], u1[1]}, (f2)(rv[m]), (f2)(rl[m])), phi2((f2){u1[2], u1[3]}, (f2)(rv[m]), (f2)(rl[m]))};
    }
  }
  lsig(myFL, 4);
  __builtin_amdgcn_s_setprio(0);
  STMP(5);
  {
  int l;
  asm volatile("v_mbcnt_lo_u32_b32 %0, -1, 0\n v_mbcnt_hi_u32_b32 %0, -1, %0" : "=v"(l));
  __builtin_assume(l >= 0 && l < 64);
  const int r15 = l & 15, g4 = l >> 4, q = (l & 15) >> 2, p = l & 3;
  const int trow = mb * 128 + w * 32 + (l & 31);
  u2 oh;
  const unsigned flo = PAIRAT(r15) * 128;
  const char* fb = uptr(wsx + WS_OH + mb * 2048);
  int sel = -1;
  {
    const u4 s0 = *(const LAS u4*)(lds + L_SEL), s1 = *(const LAS u4*)(lds + L_SEL + 16);
#pragma unroll
    for (int i = 0; i < 8; ++i) { const int v = (int)(i < 4 ? s0[i & 3] : s1[i & 3]); sel = v == trow ? i : sel; }
  }
  const bool anysel = __ballot(sel >= 0) != 0ull;
  f16v oc[2][2];
  {
    const float m = *(const LAS float*)(lds + L_M + (w * 32 + (l & 31)) * 4), rs = *(const LAS float*)(lds + L_RS + (w * 32 + (l & 31)) * 4);
#pragma unroll
    for (int t = 0; t < 8; ++t) {
      const f4 xf = (f4){lo(xv[t][0]), hi(xv[t][0]), lo(xv[t][1]), hi(xv[t][1])};
      const f4 y = (xf - m) * rs + xf;
#pragma unroll
      for (int i = 0; i < 4; ++i) oc[0][t >> 2][(t & 3) * 4 + i] = y[i];
    }
    asm volatile("" : "+v"(oc[0][0]), "+v"(oc[0][1]));
  }
  unsigned wa[2];
#pragma unroll
  for (int t = 0; t < 2; ++t) wa[t] = ldsb + L_WO + wc * 4096 + (4 * (g4 >> 1) + q) * 128 + (((4 * t + 2 * (g4 & 1) + (p >> 1)) ^ ((q >> 1) << 2)) * 16) + (p & 1) * 8;
  u4 a[RD][4];
#pragma unroll
  for (int k = 0; k < RD; ++k) asm volatile("" : "=v"(a[k][0]), "=v"(a[k][1]), "=v"(a[k][2]), "=v"(a[k][3]));
  const char* ob = uptr(wsx + WS_OF + mb * 262144 + wc * 8192 + wr * 4096);
  const unsigned ov0 = l * 16 + wc * 2048, ov1 = ov0 ^ 2048;
  STMP(6);
  unsigned vfw[4];
#pragma unroll
  for (int i = 0; i < 4; ++i) { vfw[i] = VFW(i); asm volatile("" : "+v"(vfw[i])); }
  auto wo_part = [&](int s0, int s1) {
#pragma unroll
    for (int i = s0 / 2; i < s1 / 2; i += 2) {
      const int pp = i + wr;
      const char* wp = (const char*)wo + (nb * 128 - 3072) + (2 * PAIRAT(pp) + wc) * 65536; const unsigned wm = ldsb + (2 * pp + wc) * 4096;
      GL4(vfw, wp, wm, L_WO);
    }
  };
  lwait<2>(adFL, 0x3Fu, (l & 4) ? 3u : 4u);
  asm volatile("global_load_dwordx2 %0, %1, %2 sc1" : "=&v"(oh) : "v"(flo), "s"(fb) : "memory");
  wo_part(WE, WB);
  WAITV(WB - WE);
  asm volatile("" : "+v"(oh));
  unsigned seen = (unsigned)__ballot(oh[0] == tag), go = 0;
  auto need = [&](unsigned mask) {
    if (__builtin_expect((go & mask) != mask, 0)) {
      RETRY2();
      _Pragma("unroll 1") for (unsigned it = 0; it < SPIN; ++it) {
        if ((seen & mask) != mask) {
          asm volatile("global_load_dwordx2 %0, %1, %2 sc1\n s_waitcnt vmcnt(0)" : "+v"(oh) : "v"(flo), "s"(fb) : "memory");
          seen = (unsigned)__ballot(oh[0] == tag);
        }
        go = seen & (unsigned)__ballot(oh[1] - NOW() - 1u >= 1000u);
        if ((go & mask) == mask) break;
        SLEEP(1);
      }
    }
  };
#define OX 0
#define OSC "sc1"
#define OL1(pos, i) do { if (OX == 2 && (pos) >= RD) break; \
    asm volatile("global_load_dwordx4 %0, %1, %2 offset:%3 " OSC : "+v"(a[(pos) % RD][i]) : "v"((i) & 1 ? ov1 : ov0), "s"(ob + (OX == 1 ? ((pos) & 1) : PAIRAT(pos)) * 16384), "i"(((i) >> 1) * 1024) : "memory"); } while (0)
#define OLR(pos) do { _Pragma("unroll") for (int i_ = 0; i_ < 4; ++i_) asm volatile("global_load_dwordx4 %0, %1, %2 sc1" : "+v"(a[(pos) % RD][i_]) : "v"((i_ & 1 ? ov1 : ov0) + (i_ >> 1) * 1024), "s"(ob + PAIRAT(pos) * 16384) : "memory"); } while (0)
#define OLD(pos) do { OL1(pos, 0); OL1(pos, 1); OL1(pos, 2); OL1(pos, 3); } while (0)
  auto omax = [&](u4(&v)[4]) {
    asm volatile("" : "+v"(v[0]), "+v"(v[1]), "+v"(v[2]), "+v"(v[3]));
    return max(umax3(v[0][0], v[1][0], v[2][0]), v[3][0]);
  };
#define OFIX(pos, m) do { \
    if (__builtin_expect(__ballot((m) == POIS) != 0ull, 0)) { RETRY(); _Pragma("unroll 1") for (unsigned it = 0; it < SPIN; ++it) { SLEEP(4); OLR(pos); WAITV(0); if (__ballot(omax(a[(pos) % RD]) == POIS) == 0ull) break; } } } while (0)
#define OS1(i)
#define OS2(i)
  lwait<1>(adFL, 0x30u, 4);
  wo_part(WB, 32);
  __builtin_amdgcn_s_setprio(2);
  go = seen & (unsigned)__ballot(oh[1] - NOW() - 1u >= 1000u);
  need(1u);
  STMP(15);
  u2 oh2 = oh;
  if (__builtin_expect(seen != ~0u, 0)) asm volatile("global_load_dwordx2 %0, %1, %2 sc1" : "+v"(oh2) : "v"(flo), "s"(fb) : "memory");
  OS1(0); OLD(0);
#pragma unroll
  for (int i = 1; i < RD; ++i) { need(1u << i); OS1(i); OLD(i); }
  STMP(10);
  s4 b[8];
#define ORD(pos, f) do { if ((f) < 2 && (pos) == 8) { wa[(f) & 1] += 65536; asm volatile("" : "+v"(wa[(f) & 1])); } \
    RDTR(b[2 * (f)], wa[(f) & 1], ((pos) & 7) * 8192 + ((f) >> 1) * 1024); RDTR(b[2 * (f) + 1], wa[(f) & 1], ((pos) & 7) * 8192 + ((f) >> 1) * 1024 + 2048); } while (0)
#define OMF(pos, f, r) do { if ((f) < 2 && (pos) == 0 && (r) == 1) asm volatile("v_mfma_f32_32x32x16_bf16 %0, %1, %2, 0" : "=&v"(oc[r][(f) & 1]) \
      : "v"(CAT(b[2 * (f)], b[2 * (f) + 1])), "v"(a[(pos) % RD][((f) & 2) + (r)])); \
    else asm volatile("v_mfma_f32_32x32x16_bf16 %0, %1, %2, %0" : "+v"(oc[r][(f) & 1]) \
      : "v"(CAT(b[2 * (f)], b[2 * (f) + 1])), "v"(a[(pos) % RD][((f) & 2) + (r)])); \
    SBAR(); if ((r) == 1 && (pos) < 15) ORD(((pos) + 1) & 15, f); SBAR(); } while (0)
#define ONX(pos) ((pos) > 0 && (pos) + RD - 1 < 16)
#define ONL(pos, i) do { if (ONX(pos)) OL1(((pos) + RD - 1) & 15, i); SBAR(); } while (0)
#define OWL(pos, f) do { WAITL((pos) < 15 ? 6 : 6 - 2 * (f)); SBAR(); } while (0)
#define OHEAD(pos) do { unsigned m_ = 0; \
    if ((pos) == WE / 2 - 1 && WE < 32) { asm volatile("" : "+v"(fl3)); if (__builtin_expect(((unsigned)__ballot(fl3 < 5u) >> (w ^ 2)) & 1u, 0)) lwait(adFL, 1u << (w ^ 2), 5); SBAR(); } \
    OWL(pos, 0); OMF(pos, 0, 0); if (ONX(pos)) { need(1u << (((pos) + RD - 1) & 15)); OS1(((pos) + RD - 1) & 15); } SBAR(); \
    OMF(pos, 0, 1); ONL(pos, 0); \
    OWL(pos, 1); OMF(pos, 1, 0); ONL(pos, 1); \
    OMF(pos, 1, 1); ONL(pos, 2); \
    OWL(pos, 2); OMF(pos, 2, 0); ONL(pos, 3); \
    if ((pos) == 0 || (pos) == 4) { asm volatile("ds_write_b32 %0, %1" ::"v"(myFL), "v"((pos) ? 6u : 5u) : "memory"); SBAR(); } \
    if ((pos) == WE / 2 - 3) { asm volatile("ds_read_b32 %0, %1" : "=v"(fl3) : "v"(adFL) : "memory"); SBAR(); } \
    if ((pos) == 13) { asm volatile("ds_read_b32 %0, %1" : "=v"(fl3) : "v"(adFL) : "memory"); SBAR(); } \
    OMF(pos, 2, 1); \
    OWL(pos, 3); OMF(pos, 3, 0); \
    if ((pos) < 15) { WAITV(4 * ((pos) + RD - 1 < 16 ? RD - 2 : 14 - (pos))); m_ = omax(a[((pos) + 1) % RD]); asm volatile("" : "+v"(m_)); SBAR(); } \
    OMF(pos, 3, 1); \
    if ((pos) < 15) { OFIX(((pos) + 1) & 15, m_); OS2(((pos) + 1) & 15); } SBAR(); } while (0)
  unsigned fl3;
  ORD(0, 0); ORD(0, 1); ORD(0, 2); ORD(0, 3);
  { WAITV(4 * (RD - 1)); const unsigned m_ = omax(a[0]); OFIX(0, m_); OS2(0);
    asm volatile("" : "+v"(oh2));
    if (oh2[0] == tag) oh = oh2;
    seen = (unsigned)__ballot(oh[0] == tag);
    go = seen & (unsigned)__ballot(oh[1] - NOW() - max(r15 - (RD - 1), 0) * ADV - 1u >= 1000u);
    SBAR(); }
  OHEAD(0); OHEAD(1); STMP(11); OHEAD(2); OHEAD(3); OHEAD(4); OHEAD(5); OHEAD(6); OHEAD(7); STMP(12);
  OHEAD(8); OHEAD(9); STMP(13); OHEAD(10); OHEAD(11); OHEAD(12); OHEAD(13); OHEAD(14);
  f4 xi[8];
  {
    asm volatile("" : "+v"(fl3));
    if (__builtin_expect(((unsigned)__ballot(fl3 < 6u) >> (w ^ 2)) & 1u, 0)) lwait(adFL, 1u << (w ^ 2), 6);
    if (__builtin_expect(((unsigned)__ballot(fl3 < 4u) & 0x30u) != 0u, 0)) lwait(adFL, 0x30u, 4);
    const unsigned xw = ldsb + L_WO + (4 * wr + wc) * 4096 + l * 16, xr = xw ^ 4096;
#define XW(t) asm volatile("ds_write_b128 %0, %1 offset:%2" ::"v"(xw), "v"(__builtin_shufflevector(oc[1][(t) >> 2], oc[1][(t) >> 2], ((t) & 3) * 4, ((t) & 3) * 4 + 1, ((t) & 3) * 4 + 2, ((t) & 3) * 4 + 3)), \
      "i"(((t) >> 2) * 8192 + ((t) & 3) * 1024) : "memory")
    SBAR();
    OWL(15, 0); OMF(15, 0, 1); OWL(15, 1); OMF(15, 1, 1); OWL(15, 2); OMF(15, 2, 1); OWL(15, 3); OMF(15, 3, 1);
    OMF(15, 0, 0); OMF(15, 1, 0); XW(0); XW(1); XW(2); XW(3); SBAR();
    OMF(15, 2, 0); XW(4); XW(5); XW(6); XW(7); SBAR();
    OMF(15, 3, 0);
    asm volatile("ds_write_b32 %0, %1" ::"v"(myFL), "v"(7u) : "memory");
    _Pragma("unroll 1") for (unsigned it = 0; it < SPIN; ++it) {
      unsigned v;
      asm volatile("ds_read_b32 %0, %1" : "=v"(v) : "v"(adFL) : "memory");
#pragma unroll
      for (int t = 0; t < 8; ++t) RD128(xi[t], xr, (t >> 2) * 8192 + (t & 3) * 1024);
      WAITL(0);
      asm volatile("" : "+v"(v), "+v"(xi[0]), "+v"(xi[1]), "+v"(xi[2]), "+v"(xi[3]), "+v"(xi[4]), "+v"(xi[5]), "+v"(xi[6]), "+v"(xi[7]));
      if ((((unsigned)__ballot(v < 7u) >> (w ^ 1)) & 1u) == 0u) break;
    }
    asm volatile("" : "+v"(oc[0][0]), "+v"(oc[0][1]));
#pragma unroll
    for (int t = 0; t < 8; ++t)
#pragma unroll
      for (int i = 0; i < 4; ++i) xi[t][i] += oc[0][t >> 2][(t & 3) * 4 + i];
  }
  STMP(8);
  if (anysel) {
    lwait(adFL, 0xC0u, 3);
    if (sel >= 0) {
#pragma unroll
      for (int t = 0; t < 8; ++t) xi[t] += *(const LAS f4*)(lds + L_KVX + sel * 256 + (8 * t + 4 * (l >> 5)) * 4);
    }
  }
  const __amdgpu_buffer_rsrc_t outr = __builtin_amdgcn_make_buffer_rsrc(out, 0, 2048 * D * 2, 0x00020000);
  if (FULL) {
    LAS char* tb = (LAS char*)lds + L_PQ + w * 4096;
#pragma unroll
    for (int t = 0; t < 8; ++t) *(LAS u2*)(tb + (l & 31) * 128 + ((t ^ (l & 7)) * 16) + (l >> 5) * 8) = (u2){pk2(xi[t][0], xi[t][1]), pk2(xi[t][2], xi[t][3])};
#pragma unroll
    for (int i = 0; i < 4; ++i) {
      const u4 v = *(const LAS u4*)(tb + i * 1024 + (l >> 3) * 128 + (((l ^ (l >> 3)) & 7) * 16));
      __builtin_amdgcn_raw_buffer_store_b128(v, outr, (mb * 128 + w * 32 + i * 8 + (l >> 3)) * 2048 + nb * 128 + (l & 7) * 16, 0, 16);
    }
  } else {
#pragma unroll
  for (int hh = 0; hh < 4; ++hh) {
    const f4 ta = xi[2 * hh], tb = xi[2 * hh + 1];
    auto r0 = __builtin_amdgcn_permlane32_swap(pk2(ta[0], ta[1]), pk2(tb[0], tb[1]), false, false);
    auto r1 = __builtin_amdgcn_permlane32_swap(pk2(ta[2], ta[3]), pk2(tb[2], tb[3]), false, false);
    __builtin_amdgcn_raw_buffer_store_b128(((u4){r0[0], r1[0], r0[1], r1[1]}), outr, (trow * D + nb * 64 + 16 * hh + 8 * (l >> 5)) * 2, 0, 16);
  }
  }
  if (g == 0 && w == 0 && l == 0) __builtin_amdgcn_raw_buffer_store_b32(tag, wse, WS_E, 0, 16);
  STMP(9);
  STFLUSH();
  asm volatile(".Lend_c:");
  }
  return;
  }
  {
    unsigned vb[4], vfw[4];
    STMP(6); SBAR();
#pragma unroll
    for (int i = 0; i < 4; ++i) vb[i] = (8 * i + (l >> 3)) * 6144 + (((l & 7) ^ ((((l >> 4) & 1) << 2) | ((i & 1) << 1))) * 16) + (3 - i) * 1024;
    const unsigned lb0 = lw * 196608, lb1 = 196608 - lw * 194560, mbb = ldsb + 16384 + lw * 12288;
    const char* Ab = kx + ao;
    const char* Bb = kq + (hp * 128 + lw * 2048 - 3072);
    auto issue = [&](int s, int u0, int u1) {
      const int kt = (s + rot) & 15;
      const char* sa = Ab + kt * 128; const char* sb = Bb + kt * (64 * 6144);
      const unsigned so = (s & 3) * STG, ma = mba + so, mq = mbb + so;
#pragma unroll
      for (int u = u0; u < u1; ++u) { if (u < 2) GL4(va, sa + u * 65536, ma, u * 4096); else GL4(vb, sb + (u == 2 ? lb0 : u == 3 ? lb1 : lb0 + 2048), mq, (u - 2) * 4096); }
    };
    int tk = 0;
#define LBAR() do { BST(tk + 1); BAR(); BST(tk + 21); ++tk; } while (0)
    issue(0, 2, 3); issue(0, 1, 2); issue(0, 3, 5); SBAR(); STMP(8); SBAR(); issue(1, 0, 5); issue(2, 0, U0);
    BST(60);
    WAITV(20 + 4 * U0); BST(0); BAR(); BST(20);
    issue(2, U0, 5);
    GETTAG(tag);
    MKWS(tag);
    const unsigned ck0 = (unsigned)__builtin_amdgcn_s_memtime(), rt0 = NOW();
    int s0 = 3;
    if (U3 < 5) { issue(3, 0, U3); BST(tk + 40); WAITV(20 + 4 * U3); LBAR(); issue(3, U3, 5); s0 = 4; }
#pragma unroll 1
    for (; s0 < (sp ? 16 : 6 + ph); ++s0) { issue(s0, 0, 5); BST(tk + 40); WAITV(40); LBAR(); }
#pragma unroll 1
    for (; s0 < 16; s0 += 5) {
      WAITV(20); LBAR();
      WAITV(0); LBAR();
      issue(s0, 0, 5); issue(s0 + 1, 0, U1); BST(tk + 40); WAITV(4 * U1); LBAR();
      issue(s0 + 1, U1, 5); issue(s0 + 2, 0, 5);
      if (s0 + 3 >= 16) break;
      issue(s0 + 3, 0, U2); BST(tk + 40); WAITV(20 + 4 * U2); LBAR();
      issue(s0 + 3, U2, 5);
      if (s0 + 4 >= 16) break;
      issue(s0 + 4, 0, 5); BST(tk + 40); WAITV(40); LBAR();
    }
    int wq = 0;
#pragma unroll
    for (int i = 0; i < 4; ++i) vfw[i] = VFW(i);
    auto wo_upto = [&](int lim) {
      _Pragma("unroll 1") for (; wq < lim; ++wq) { const char* wp = (const char*)wo + (nb * 128 - 3072) + HEADAT(wq) * 65536; const unsigned wm = ldsb + wq * 4096; GL4(vfw, wp, wm, L_WO); }
    };
    asm volatile("ds_write_b32 %0, %1" ::"v"(adFL), "v"(0u) : "memory");
    unsigned kd = sp ? KS : KD, od = OD;
    if (CF) {
      const unsigned dc = (unsigned)__builtin_amdgcn_s_memtime() - ck0, dr = NOW() - rt0;
      const float sc = fminf((float)dr * (float)CF * __builtin_amdgcn_rcpf((float)dc), 2.f);
      kd = KL + (unsigned)((float)(kd - KL) * sc); od = OL + (unsigned)((float)(OD - OL) * sc);
      asm volatile("" : "+v"(kd), "+v"(od));
    }
    const int hd = hp * 2 + lw;
    u4 sel[2];
#pragma unroll
    for (int e = 0; e < 2; ++e) {
      unsigned v = (r15 >> 2) == g4 ? (((r15 & 3) == 2 * e ? 0x3F80u : 0u) | ((r15 & 3) == 2 * e + 1 ? 0x3F800000u : 0u)) : 0u;
      asm volatile("" : "+v"(v));
      sel[0][e] = v; sel[0][2 + e] = 0u; sel[1][e] = 0u; sel[1][2 + e] = v;
    }
    const char* kvb = uptr(wsx + WS_KVP + (size_t)hd * 32768);
    const char* zb = uptr(wsx + WS_ZP + (size_t)hd * 2048);
    unsigned zoff = r15 * 128 + g4 * 32, oho = l == 0 ? WS_OH + (mb * 16 + hp) * 128 : WS_SZ, fla = ldsb + L_FL;
    asm volatile("" : "+v"(zoff), "+v"(oho), "+v"(fla), "+s"(kvb), "+s"(zb));
    WAITV(0); WAITL(0); LBAR();
    u4 dg;
    unsigned now;
    if (lw) {
      const u4 pz = (u4)(POIS);
      const unsigned tb = NOW();
      __builtin_amdgcn_raw_buffer_store_b32(tb + kd, wsr, l == 0 ? WS_HK + hp * 512 + (mb >> 2) * 128 + (mb & 3) * 4 : WS_SZ, 0, 16);
#pragma unroll
      for (int i = 0; i < 16; ++i) ST128(pz, WS_OF + (mb * 32 + hp * 2) * 8192 + i * 1024 + l * 16);
      BST(61);
      const char* hb = uptr(wsx + WS_HK + hp * 512);
      __builtin_amdgcn_s_setprio(LP1);
      lwait<4>(adFL, 0xC0u, 1u);
      while (NOW() - tb < HT) SLEEP(1);
      STMP(1);
      unsigned t0, rq[4], m0, m1, nt;
#define HG4(d, a, b, c, e) "s_sub_u32 s" #a ", s" #a ", %0\n s_sub_u32 s" #b ", s" #b ", %0\n s_sub_u32 s" #c ", s" #c ", %0\n s_sub_u32 s" #e ", s" #e ", %0\n" \
      "s_max_u32 s" #a ", s" #a ", s" #b "\n s_max_u32 s" #c ", s" #c ", s" #e "\n s_max_u32 %" #d ", s" #a ", s" #c "\n"
      asm volatile("s_mov_b32 %7, 15\n.Lhint:\n s_load_dwordx4 s[72:75], %8, 0 glc\n s_load_dwordx4 s[76:79], %8, 128 glc\n s_load_dwordx4 s[80:83], %8, 256 glc\n"
                   "s_load_dwordx4 s[84:87], %8, 384 glc\n s_memrealtime s[88:89]\n s_waitcnt lgkmcnt(0)\n s_sub_u32 %0, s88, 0x800\n"
                   HG4(1, 72, 73, 74, 75) HG4(2, 76, 77, 78, 79) HG4(3, 80, 81, 82, 83) HG4(4, 84, 85, 86, 87)
                   "s_max_u32 %5, %1, %2\n s_max_u32 %6, %3, %4\n s_max_u32 s72, %5, %6\n s_cmpk_lt_u32 s72, 0xc00\n s_cbranch_scc1 .Lhdone\n"
                   "s_sleep %9\n s_sub_u32 %7, %7, 1\n s_cbranch_scc0 .Lhint\n"
                   "s_mov_b32 %1, 0\n s_mov_b32 %2, 0\n s_mov_b32 %3, 0\n s_mov_b32 %4, 0\n s_mov_b32 %5, 0\n s_mov_b32 %6, 0\n.Lhdone:"
                   : "=&s"(t0), "=&s"(rq[0]), "=&s"(rq[1]), "=&s"(rq[2]), "=&s"(rq[3]), "=&s"(m0), "=&s"(m1), "=&s"(nt) : "s"(hb), "i"(HKS)
                   : "memory", "scc", "s72", "s73", "s74", "s75", "s76", "s77", "s78", "s79", "s80", "s81", "s82", "s83", "s84", "s85", "s86", "s87", "s88", "s89");
      BST(16);
      const bool up = m0 > m1;
      const unsigned a0 = up ? rq[2] : rq[0], a1 = up ? rq[3] : rq[1], a2 = up ? rq[0] : rq[2], a3 = up ? rq[1] : rq[3], gu = up ? 2u : 0u;
      unsigned e0, e1, go;
      asm("s_cmp_gt_u32 %3, %4\n s_cselect_b32 %0, %6, %5\n s_cselect_b32 %1, %5, %6\n s_addc_u32 %2, %7, 0" : "=&s"(e0), "=&s"(e1), "=&s"(go) : "s"(a2), "s"(a3), "s"(a0), "s"(a1), "s"(gu) : "scc");
      dg = (u4){((t0 + 3u + e0) & ~3u) | go, t0 + e1, t0 + min(a2, a3), t0 + max(m0, m1)};
      asm volatile("ds_write_b128 %0, %1 offset:32\n ds_write_b32 %0, %2 offset:20" ::"v"(fla), "v"(dg), "v"(1u) : "memory");
      BST(15);
      now = NOW();
      WAITV(0);
      ST64(((u2){tag, ((int)(dg[3] - now) > 0 ? dg[3] : now) + od}), oho);
    } else {
      wo_upto(WA);
      BST(15);
      if (WA < WE) { lwait<2>(adFL, 0x0Fu, 3); if (WS0) SLEEP(WS0); wo_upto(WE); }
      STMP(1);
      __builtin_amdgcn_s_setprio(LP0);
      u4 v;
      _Pragma("unroll 1") for (unsigned it = 0; it < SPIN; ++it) {
        unsigned f;
        asm volatile("ds_read_b32 %0, %2 offset:20\n ds_read_b128 %1, %2 offset:32\n s_waitcnt lgkmcnt(0)" : "=&v"(f), "=&v"(v) : "v"(fla) : "memory");
        if (__builtin_amdgcn_readfirstlane(f)) break;
        SLEEP(2);
      }
#pragma unroll
      for (int e = 0; e < 4; ++e) dg[e] = __builtin_amdgcn_readfirstlane(v[e]);
      now = NOW();
    }
    unsigned gx = (dg[0] & 3u) * 8192u;
    asm volatile("" : "+s"(gx));
    __builtin_amdgcn_s_setprio(3);
    u4 pv[16][2], zv0, zv1, pq[8];
    const unsigned lv16 = l * 16;
    const unsigned pa = ldsb + L_PQ + lw * 4096 + l * 16, omask = lw ? 0x0Au : 0x25u;
    STMP(2);
#define GLD(gq) do { _Pragma("unroll") for (int i = 0; i < 4; ++i) { const char* sb = padd(kvb, (((gq) * 8192u + (i & 2) * 2048u) ^ gx)); \
      asm volatile("global_load_dwordx4 %0, %1, %2 offset:%3 sc1" : "=v"(pv[(gq) * 4 + i][0]) : "v"(lv16), "s"(sb), "i"((i & 1) * 2048) : "memory"); \
      asm volatile("global_load_dwordx4 %0, %1, %2 offset:%3 sc1" : "=v"(pv[(gq) * 4 + i][1]) : "v"(lv16), "s"(sb), "i"((i & 1) * 2048 + 1024) : "memory"); } } while (0)
#define ZLD() do { asm volatile("global_load_dwordx4 %0, %1, %2 sc1" : "=v"(zv0) : "v"(zoff), "s"(zb) : "memory"); \
      asm volatile("global_load_dwordx4 %0, %1, %2 offset:16 sc1" : "=v"(zv1) : "v"(zoff), "s"(zb) : "memory"); } while (0)
#pragma unroll
    for (int gq = 0; gq < 4; ++gq) {
      while ((int)(now - dg[gq]) < 0) now = NOW();
      if (gq == 3) ZLD();
      GLD(gq);
    }
    lsig(myFL, 3);
    STMP(5);
    unsigned ofo = WS_OF + (mb * 32 + hd) * 8192 + g4 * 512 + r15 * 16;
    unsigned ofp = ofo + 4096;
    f4 eps = (f4)(1e-6f);
    asm volatile("" : "+v"(ofo), "+v"(ofp), "+v"(eps), "+v"(sel[0]), "+v"(sel[1]));
    f4 gs[4][4]; float z[8];
#pragma unroll
    for (int gq = 0; gq < 4; ++gq) {
      if (gq == PW) {
        SBAR();
        lwait(adFL, omask, (l & 4) ? 3u : 4u);
#pragma unroll
        for (int i = 0; i < 8; ++i) RD128(pq[i], pa, (i >> 2) * 8192 + (i & 3) * 1024);
      }
      SBAR(); WAITV(gq == 0 ? 26 : gq == 1 ? 18 : gq == 2 ? 10 : 0);
      if (gq == 3) BST(17);
      auto gbad = [&]() {
#pragma unroll
        for (int i = 0; i < 4; ++i) asm volatile("" : "+v"(pv[gq * 4 + i][0]), "+v"(pv[gq * 4 + i][1]));
        unsigned m = umax3(pv[gq * 4][0][0], pv[gq * 4][1][0], pv[gq * 4 + 1][0][0]);
        m = umax3(m, pv[gq * 4 + 1][1][0], pv[gq * 4 + 2][0][0]);
        m = umax3(m, pv[gq * 4 + 2][1][0], pv[gq * 4 + 3][0][0]);
        m = max(m, pv[gq * 4 + 3][1][0]);
        if (gq == 3) { asm volatile("" : "+v"(zv0), "+v"(zv1)); m = umax3(m, zv0[0], zv1[0]); }
        return __ballot(m == POIS) != 0ull;
      };
      if (__builtin_expect(gbad(), 0)) {
        _Pragma("unroll 1") for (unsigned it = 0; it < SPIN; ++it) {
          RETRY();
          SLEEP(2);
          if (gq == 3) ZLD();
          GLD(gq);
          WAITV(0);
          if (!gbad()) break;
        }
      }
      if (gq == 3) {
#pragma unroll
        for (int e = 0; e < 4; ++e) { z[e] = __uint_as_float(zv0[e]); z[4 + e] = __uint_as_float(zv1[e]); }
        SBAR();
      }
#pragma unroll
      for (int i = 0; i < 4; ++i)
#pragma unroll
        for (int t = 0; t < 4; ++t) {
          gs[gq][t] = MFMA16(S8(sel[(t ^ (t >> 1)) & 1]), S8(pv[gq * 4 + i][t >> 1]), i ? gs[gq][t] : (f4)(0.f));
          if (gq == 3) {
            SBAR();
            if (i + t == 0) asm volatile("s_nop 1");
            asm volatile("v_add_f32_dpp %0, %0, %0 row_ror:%2 row_mask:0xf bank_mask:0xf\n v_add_f32_dpp %1, %1, %1 row_ror:%2 row_mask:0xf bank_mask:0xf" : "+v"(z[2 * t]), "+v"(z[2 * t + 1]) : "i"(8 >> i));
            SBAR();
          }
        }
      if (gq == 2) {
#pragma unroll
        for (int t = 0; t < 4; ++t) { gs[0][t] += gs[1][t]; asm volatile("" : "+v"(gs[0][t])); }
      }
    }
    STMP(3);
    WAITL(0);
#pragma unroll
    for (int i = 0; i < 8; i += 2) asm volatile("" : "+v"(pq[i]), "+v"(pq[i + 1]));
    u4 kA[2], zA;
#pragma unroll
    for (int e = 0; e < 4; ++e) zA[e] = pk2(z[2 * e], z[2 * e + 1]);
    SBAR();
#pragma unroll
    for (int t = 0; t < 4; ++t) gs[0][t] += gs[2][t] + gs[3][t];
    SBAR();
    f4 dn[8], o0[8], o1[8]; float inv[8];
#pragma unroll
    for (int i = 0; i < 8; ++i) {
      dn[i] = MFMA16(S8(zA), S8(pq[i]), eps);
      SBAR();
      kA[i >> 2][i & 3] = pk2(gs[0][i >> 1][(i & 1) * 2], gs[0][i >> 1][(i & 1) * 2 + 1]);
      SBAR();
    }
#pragma unroll
    for (int i = 0; i < 8; ++i) {
      o0[i] = MFMA16(S8(kA[0]), S8(pq[i]), (f4)(0.f)); SBAR();
      o1[i] = MFMA16(S8(kA[1]), S8(pq[i]), (f4)(0.f)); SBAR();
      inv[i] = __builtin_amdgcn_rcpf(dn[i][0]); asm volatile("" ::"v"(dn[i])); SBAR();
    }
    BST(18);
    u4 op = sel[0];
#pragma unroll
    for (int i = 0; i < 8; ++i) {
      o0[i] *= inv[i]; o1[i] *= inv[i];
      u4 o; o[0] = min(pk2(o0[i][0], o0[i][1]), POIS - 1u); o[1] = pk2(o0[i][2], o0[i][3]); o[2] = pk2(o1[i][0], o1[i][1]); o[3] = pk2(o1[i][2], o1[i][3]);
      asm volatile("" : "+v"(o) : "v"(op));
      ST128(o, (i & 4 ? ofp : ofo) + (i & 1) * 256 + (i & 2) * 1024);
      op = o;
      SBAR();
    }
    lsig(myFL, 4);
    STMP(4);
    STFLUSH();
    asm volatile(".Lend_l:");
  }

}

extern "C" void kernel_launch(void* const* d_in, const int* in_sizes, int n_in, void* d_out, int out_size,
                              void* d_ws, size_t ws_size, hipStream_t stream) {
  (void)hipFuncSetAttribute((const void*)fused, hipFuncAttributeMaxDynamicSharedMemorySize, LDS_SZ);
  fused<<<256, 512, LDS_SZ, stream>>>((const u16*)d_in[0], (const u16*)d_in[1], (const u16*)d_in[4], (char*)d_ws, (const u16*)d_in[2],
                                      (const u16*)d_in[5], (const u16*)d_in[6], (const u16*)d_in[7], (u16*)d_out);
}
```

```cpp
#define EVW 0
#define EVG (b_&7)==0
#define EVX 0
#include <hip/hip_runtime.h>
#include <stdint.h>

typedef unsigned short u16;
typedef short s4 __attribute__((ext_vector_type(4)));
typedef short s8 __attribute__((ext_vector_type(8)));
typedef float f4 __attribute__((ext_vector_type(4)));
typedef float f16v __attribute__((ext_vector_type(16)));
typedef float f2 __attribute__((ext_vector_type(2)));
typedef unsigned u4 __attribute__((ext_vector_type(4)));
typedef unsigned u2 __attribute__((ext_vector_type(2)));
typedef unsigned u8v __attribute__((ext_vector_type(8)));
typedef __bf16 bf2 __attribute__((ext_vector_type(2)));
#define LAS __attribute__((address_space(3)))
#define DEV __device__ __forceinline__

constexpr int D = 1024, F = 3584;
#define NOMOE 0
#define KD 220
#define KS 214
#define OD 180
#define OHP 1
#define FS 128
#define GSL 8
#define YSL 16
#define TP1 0
#define TP2 3
#define TLBW 0
#define FULL 1
#define HPRIO 1
#define OWN 1
#define NOPOIS 0
#define CAP 7
#define RD 4
#define DYN 1
#define JX 0
#define NOSKIP 0
#define GRP 8
#define ADV 15
#define HB 1
#define RT 1
#define WD 0
#define WU1 12
#define WU2 22
#define PL 0
#define PO 0
#define WG 0
#define DS 0
#define D1 16
#define D2 2
#define HKS 2
#define LW1 1
#define HW1 2
#define HS0 0
#define GPV 2
#define XO 1
#define TP0 3
#define G1 0x31
#define G1S 4
#define G2S 8
#define TD 1
#define YD 170
#define WU0 4
#define WE 12
#define HT 115
#define CF 19
#define KL 145
#define OL 115
#define CL 0
#define WS0 4
#define OM 0
#define HG 128
#define U0 0
#define U3 5
#define TL 0
#define MH 0
#define HWT 3
#define YG 0
#define GG 1
#define PV 1
#define U1 4
#define U2 2
#define QD 1
#define LP0 3
#define LP1 3
#define WA 8
#define PW 3
#define WB 24
#define PF 1
#define PFS 6
#define PA 0
#define PAS 0
#define ROT 0

constexpr unsigned WS_E = 0;
constexpr unsigned WS_YF = 524288;
constexpr unsigned WS_CAND = 12288;
constexpr unsigned WS_RG = 49152;
constexpr unsigned WS_ST = 65536;
constexpr unsigned WS_HK = 589824;
constexpr unsigned WS_ZP = 131072;
constexpr unsigned WS_CS = 196608;
constexpr unsigned WS_OH = 458752;
constexpr unsigned WS_KVP = 1u << 20;
constexpr unsigned WS_GP = 2u << 20;
constexpr unsigned WS_YP = 3u << 20;
constexpr unsigned WS_OF = 5u << 20;
constexpr unsigned WS_SZ = 9u << 20;
#define NR 4
#define RGO(tag) ((64u << 20) + ((tag) & (NR - 1u)) * (10u << 20))
#define MKWS(tag) char* const wsx = ws + RGO(tag); const __amdgpu_buffer_rsrc_t wsr = __builtin_amdgcn_make_buffer_rsrc(wsx, 0, WS_SZ, 0x00020000)

constexpr int STG = 40960;
constexpr int L_KVX = 0;
constexpr int L_PQ = 12288;
constexpr int L_M = 28672, L_RS = L_M + 512, L_CS = L_RS + 512, L_SEL = L_CS + 768, L_FL = L_SEL + 64;
constexpr int L_HX = 30720, L_RL = 31744, L_WO = 32768;
constexpr int LDS_SZ = 163840;
constexpr unsigned POIS = 0xFFFFFFFFu;

DEV unsigned pk2(float a, float b) { bf2 v; v[0] = (__bf16)a; v[1] = (__bf16)b; return __builtin_bit_cast(unsigned, v); }
DEV float lo(unsigned u) { return __uint_as_float(u << 16); }
DEV float hi(unsigned u) { return __uint_as_float(u & 0xFFFF0000u); }
DEV float rbf(float a) { return lo(pk2(a, a)); }
DEV float dot2(unsigned a, unsigned b, float c) { return __builtin_amdgcn_fdot2_f32_bf16(__builtin_bit_cast(bf2, a), __builtin_bit_cast(bf2, b), c, false); }
#define RD128(dst, addr, OFF) asm volatile("ds_read_b128 %0, %1 offset:%2" : "=v"(dst) : "v"(addr), "i"(OFF) : "memory")
#define RDTR(dst, addr, OFF) asm volatile("ds_read_b64_tr_b16 %0, %1 offset:%2" : "=v"(dst) : "v"(addr), "i"(OFF) : "memory")
#define SBAR() __builtin_amdgcn_sched_barrier(0)
#define CAT(x, y) __builtin_shufflevector(x, y, 0, 1, 2, 3, 4, 5, 6, 7)
#define WAITV(n) asm volatile("s_waitcnt vmcnt(%0)" ::"i"(n) : "memory")
#define WAITL(n) asm volatile("s_waitcnt lgkmcnt(%0)" ::"i"(n) : "memory")
#define MEMB() asm volatile("" ::: "memory")
#define SLEEP(n) asm volatile("s_sleep %0" ::"i"(n) : "memory")
#define BAR() asm volatile("s_barrier" ::: "memory")
#define GL0(vo, sb, m0v) asm volatile("s_mov_b32 m0, %2\n s_nop 0\n global_load_lds_dwordx4 %0, %1" ::"v"(vo), "s"(sb), "s"(m0v) : "memory")
#define GLI(vo, sb, OFF) asm volatile("global_load_lds_dwordx4 %0, %1 offset:%2" ::"v"(vo), "s"(sb), "i"(OFF) : "memory")
#define GL4(v, sb, mm, MO) do { asm volatile("s_add_i32 m0, %2, %3\n s_nop 0\n global_load_lds_dwordx4 %0, %1" ::"v"((v)[0]), "s"(sb), "s"(mm), "i"(MO) : "memory", "scc"); \
      GLI((v)[1], sb, 1024); GLI((v)[2], sb, 2048); GLI((v)[3], sb, 3072); } while (0)
#define VFW(i) ((8 * (i) + (l >> 3)) * 2048 + (((l & 7) ^ (((l >> 4) & 1) << 2)) * 16) + (3 - (i)) * 1024)
#define MFMA16(a, b, c) __builtin_amdgcn_mfma_f32_16x16x32_bf16(a, b, c, 0, 0, 0)
#define S8(x) __builtin_bit_cast(s8, x)
#define LD128(off) __builtin_amdgcn_raw_buffer_load_b128(wsr, (off), 0, 16)
#define LD64(off) __builtin_amdgcn_raw_buffer_load_b64(wsr, (off), 0, 16)
#define ST128(v, off) __builtin_amdgcn_raw_buffer_store_b128((v), wsr, (off), 0, 16)
#define ST64(v, off) __builtin_amdgcn_raw_buffer_store_b64((v), wsr, (off), 0, 16)
#define OFA(i) (WS_OF + ((mb * 32 + hd) * 8 + (i)) * 1024 + l * 16)
#define POISON(i) ST128(pz, (i) < 2 ? WS_KVP + (hd * 16 + mb) * 2048 + (i) * 1024 + l * 16 : (i) == 2 ? WS_ZP + (hd * 16 + mb) * 128 + (l & 7) * 16 : OFA((i) - 3))
#define NOW() ((unsigned)__builtin_amdgcn_s_memrealtime())
constexpr unsigned SPIN = 1u << 21;
DEV const char* uptr(const char* p) {
  unsigned long long v = (unsigned long long)p;
  unsigned lo_ = __builtin_amdgcn_readfirstlane((unsigned)v), hi_ = __builtin_amdgcn_readfirstlane((unsigned)(v >> 32));
  return (const char*)(((unsigned long long)hi_ << 32) | lo_);
}
DEV const char* padd(const char* p, unsigned o) {
  unsigned lo_, hi_;
  asm("s_add_u32 %0, %2, %4\n s_addc_u32 %1, %3, 0" : "=&s"(lo_), "=s"(hi_) : "s"((unsigned)(unsigned long long)p), "s"((unsigned)((unsigned long long)p >> 32)), "s"(o) : "scc");
  return (const char*)(((unsigned long long)hi_ << 32) | lo_);
}
DEV void lsig(unsigned addr, unsigned val) { asm volatile("s_waitcnt lgkmcnt(0)\n ds_write_b32 %0, %1" ::"v"(addr), "v"(val) : "memory"); }
template <int SL = 0> DEV void lwait(unsigned addr, unsigned mask, unsigned target) {
  _Pragma("unroll 1") for (unsigned it = 0; it < SPIN; ++it) {
    unsigned v;
    asm volatile("ds_read_b32 %0, %1\n s_waitcnt lgkmcnt(0)" : "=v"(v) : "v"(addr) : "memory");
    if (((unsigned)__ballot(v < target) & mask) == 0u) break;
    if (SL) SLEEP(SL);
  }
}
DEV unsigned nopois(unsigned a) { return min(a, POIS - 1u); }
DEV u4 tagf(f4 v, unsigned tag) {
  u4 r;
#pragma unroll
  for (int e = 0; e < 4; ++e) r[e] = __builtin_amdgcn_perm(__float_as_uint(v[e]), tag, 0x07060500u + e);
  return r;
}
DEV void untag(u4& v, unsigned tag, unsigned& bad) {
#pragma unroll
  for (int e = 0; e < 4; ++e) v[e] ^= (tag >> (8 * e)) & 255u;
  bad |= v[0] | v[1]; bad |= v[2] | v[3];
}
DEV unsigned umax3(unsigned a, unsigned b, unsigned c) { return max(max(a, b), c); }
template <int C> DEV float dppf(float v) { return __int_as_float(__builtin_amdgcn_update_dpp(0, __float_as_int(v), C, 0xf, 0xf, false)); }
DEV unsigned wave_max(unsigned v) {
  v = max(v, (unsigned)__builtin_amdgcn_update_dpp(0, (int)v, 0x111, 0xf, 0xf, false));
  v = max(v, (unsigned)__builtin_amdgcn_update_dpp(0, (int)v, 0x112, 0xf, 0xf, false));
  v = max(v, (unsigned)__builtin_amdgcn_update_dpp(0, (int)v, 0x114, 0xf, 0xf, false));
  v = max(v, (unsigned)__builtin_amdgcn_update_dpp(0, (int)v, 0x118, 0xf, 0xf, false));
  v = max(v, (unsigned)__builtin_amdgcn_update_dpp(0, (int)v, 0x142, 0xa, 0xf, false));
  v = max(v, (unsigned)__builtin_amdgcn_update_dpp(0, (int)v, 0x143, 0xc, 0xf, false));
  return (unsigned)__builtin_amdgcn_readlane((int)v, 63);
}
DEV unsigned phi2(f2 u, f2 rs, f2 rl) {
  f2 a = u * rl, p = u * rs + 1.f, r;
  r[0] = __builtin_amdgcn_fmed3f(__builtin_amdgcn_exp2f(a[0]), p[0], 1.f);
  r[1] = __builtin_amdgcn_fmed3f(__builtin_amdgcn_exp2f(a[1]), p[1], 1.f);
  r += 1e-6f;
  return pk2(r[0], r[1]);
}

#define STDECL unsigned rt_ = 0, rt2_ = 0, st_[16] = {0, 0, 0, 0, 0, 0, 0, 0, 0, 0, 0, 0, 0, 0, 0, 0}
#define STMP(i) st_[i] = (unsigned)__builtin_amdgcn_s_memrealtime()
#define BST(i) do { if ((i) == 21) st_[9] = NOW(); if ((i) == 24) st_[10] = NOW(); if ((i) == 28) st_[11] = NOW(); if ((i) == 31) st_[12] = NOW(); if ((i) == 34) st_[13] = NOW(); if ((i) == 60) st_[14] = NOW(); } while (0)
#undef HB
#define HB 0
#define BSTV cw
#define RETRY() (++rt_)
#define RETRY2() (++rt2_)
#define STFLUSH() do { const int b_ = blockIdx.x; if (w == EVW && (EVG)) { const unsigned v_ = min((unsigned)(EVX), 300000u); _Pragma("unroll 1") for (unsigned it = 0; it < SPIN; ++it) { if (NOW() - st_[0] >= 4500u + v_) break; SLEEP(1); } } } while (0)

DEV float half_sum(float v) {
  asm volatile("s_nop 1\n v_add_f32_dpp %0, %0, %0 row_ror:8 row_mask:0xf bank_mask:0xf\n s_nop 1\n v_add_f32_dpp %0, %0, %0 row_ror:4 row_mask:0xf bank_mask:0xf\n"
               "s_nop 1\n v_add_f32_dpp %0, %0, %0 row_ror:2 row_mask:0xf bank_mask:0xf\n s_nop 1\n v_add_f32_dpp %0, %0, %0 row_ror:1 row_mask:0xf bank_mask:0xf\n s_nop 1" : "+v"(v));
  auto r = __builtin_amdgcn_permlane16_swap(__float_as_uint(v), __float_as_uint(v), false, false);
  return __uint_as_float(r[0]) + __uint_as_float(r[1]);
}

struct FQ { s8 a[4]; s4 b[12]; };
template <bool B> struct BC { static constexpr bool v = B; };
template <int I> struct IC { static constexpr int v = I; };

__global__ __launch_bounds__(512) void fused(const u16* __restrict__ x, const u16* __restrict__ wqkv, const u16* __restrict__ wrt,
                                             char* __restrict__ ws_, const u16* __restrict__ wo_, const u16* __restrict__ wg,
                                             const u16* __restrict__ wu, const u16* __restrict__ wd, u16* __restrict__ out) {
  extern __shared__ __attribute__((aligned(16))) char lds[];
  const char *kx, *kq, *kr, *wo; char* ws;
  asm volatile("s_load_dwordx2 %0, %5, 0\n s_load_dwordx2 %1, %5, 8\n s_load_dwordx2 %2, %5, 16\n s_load_dwordx2 %3, %5, 24\n s_load_dwordx2 %4, %5, 32"
               : "=&s"(kx), "=&s"(kq), "=&s"(kr), "=&s"(ws), "=&s"(wo) : "s"(__builtin_amdgcn_kernarg_segment_ptr()));
  SBAR();
  const int w = __builtin_amdgcn_readfirstlane(threadIdx.x >> 6), l = threadIdx.x & 63;
  STDECL;
  STMP(0);
  const int g = blockIdx.x, xcd = g & 7, j = g >> 3, ii = j >> 2;
  const int mb = (xcd & 1) * 8 + ii, hp = (xcd >> 1) * 4 + (j & 3);
  unsigned cw, cwo = (l * 32 + j) * 64; unsigned long long cwb;
  asm volatile("s_getpc_b64 %2\n.Lcode_here:\n v_min_u32 %1, max(.Lend_c - .Lcode_here, .Lend_l - .Lcode_here, .Lend_h - .Lcode_here, .Lend_s - .Lcode_here) - 4, %1\n"
               "s_cmp_lg_u32 %3, 7\n s_cbranch_scc1 .Lskip_w\n global_load_dword %0, %1, %2\n.Lskip_w:" : "=&v"(cw), "+v"(cwo), "=&s"(cwb) : "s"(w) : "memory", "scc");
  SBAR();
  const int lw = w & 1, rot = 2 * xcd + 4 * ((ii + JX * j) & 3);
  const unsigned ldsb = (unsigned)(size_t)(LAS char*)lds;
  unsigned va[4];
#pragma unroll
  for (int i = 0; i < 4; ++i) va[i] = (8 * i + (l >> 3)) * 2048 + (((l & 7) ^ ((i & 1) * 4 + (l >> 4))) * 16) + (3 - i) * 1024;
  const int ao = (mb * 128 + lw * 64) * 2048 - 3072;
  long long ao0 = ao + (rot & 15) * 128;
  unsigned mba = ldsb + lw * 8192;
  const int srow = mb * 128 + hp * 8 + 2 * w + (l >> 5);
  unsigned xo = srow * 2048 + (l & 31) * 16, ro = (l & 31) * 16;
  asm volatile("" : "+v"(va[0]), "+v"(va[1]), "+v"(va[2]), "+v"(va[3]), "+v"(xo), "+v"(ro));
  asm volatile("s_waitcnt lgkmcnt(0)" : "+s"(kx), "+s"(kq), "+s"(kr), "+s"(ws), "+s"(wo), "+s"(ao0), "+s"(mba));
  SBAR();
  asm volatile("s_sub_u32 m0, %6, 4\n s_cmp_gt_u32 m0, 1\n s_cbranch_scc1 .Lskip_l\n s_mov_b32 m0, %5\n s_nop 0\n global_load_lds_dwordx4 %0, %4\n global_load_lds_dwordx4 %1, %4 offset:1024\n"
               "global_load_lds_dwordx4 %2, %4 offset:2048\n global_load_lds_dwordx4 %3, %4 offset:3072\n.Lskip_l:"
               :: "v"(va[0]), "v"(va[1]), "v"(va[2]), "v"(va[3]), "s"(kx + ao0), "s"(mba), "s"(w) : "memory", "scc");
  u4 xa[4], rw[4]; unsigned ep;
  asm volatile("s_cmp_gt_u32 %14, 3\n s_cbranch_scc1 .Lskip_x\n"
               "global_load_dwordx4 %0, %9, %11\n global_load_dwordx4 %1, %9, %11 offset:512\n global_load_dwordx4 %2, %9, %11 offset:1024\n global_load_dwordx4 %3, %9, %11 offset:1536\n"
               "global_load_dwordx4 %4, %10, %12\n global_load_dwordx4 %5, %10, %12 offset:512\n global_load_dwordx4 %6, %10, %12 offset:1024\n global_load_dwordx4 %7, %10, %12 offset:1536\n"
               ".Lskip_x:\n s_load_dword %8, %13, 0"
               : "=&v"(xa[0]), "=&v"(xa[1]), "=&v"(xa[2]), "=&v"(xa[3]), "=&v"(rw[0]), "=&v"(rw[1]), "=&v"(rw[2]), "=&v"(rw[3]), "=&s"(ep)
               : "v"(xo), "v"(ro), "s"(kx), "s"(kr), "s"(ws), "s"(w) : "memory", "scc");
#define GETTAG(t) unsigned t; asm volatile("s_waitcnt lgkmcnt(0)\n s_add_u32 %0, %1, 1" : "=s"(t) : "s"(ep) : "memory", "scc")
  SBAR();
#define PTAB 0xA98FEDC1045B7623ull
#define PHT 0x6624924999924924ull
#define CX 4
  const bool sp = (hp & 7) == 3;
  const int nb = hp;
#define PAIRAT(p) ((int)(PTAB >> (4 * (p))) & 15)
#define HEADAT(s) (2 * PAIRAT((s) >> 1) + ((s) & 1))
  const int ph = (int)(PHT >> (2 * j)) & 3;
#define HQT 0xDCBA098765320410ull
#define CXA 1
#define CHUNK(b) ((b) < 14 ? 4 * (b) + 3 : ((b) - 14) + ((((b) - 14) * 43) >> 7))
  const unsigned adFL = ldsb + L_FL + 4 * (l & 7), myFL = ldsb + L_FL + 4 * w;
  const __amdgpu_buffer_rsrc_t wse = __builtin_amdgcn_make_buffer_rsrc(ws, 0, 64, 0x00020000);
  const int r15 = l & 15, g4 = l >> 4, q = (l & 15) >> 2, p = l & 3;

  if (w != 4 && w != 5) {
  if (w >= 6) {
    const int mw = w - 6;
    unsigned pf = 0;
    if (PF && !(xcd & 2) && !sp && (mw == 0 || PF == 3 || (PF == 2 && (ii >> 2)))) {
      const unsigned po = (((rot + 2 - 2 * mw + (ii >> 2)) & 15) * 64 + l) * 6144 + (j & 3) * 2048 + (hp | 3) * 128;
      if (PFS) SLEEP(PFS);
      asm volatile("global_load_dword %0, %1, %2" : "+v"(pf) : "v"(po), "s"(kq) : "memory");
    }
    if (PA && mw == 1) {
      const unsigned po = (mb * 128 + (j & 3) * 32 + (l & 31)) * 2048 + (l >> 5) * 1024 + 384;
      if (PAS) SLEEP(PAS);
      asm volatile("global_load_dword %0, %1, %2" : "+v"(pf) : "v"(po), "s"(kx) : "memory");
    }
    GETTAG(tag);
    const int bq = ((int)(HQT >> (4 * hp)) & 15) * 4 + (ii & 3), ksp = (xcd & 1) * 2 + (ii >> 2);
    const int chunk = CHUNK(bq), bg = (CXA || bq < 14) ? bq ^ (w & 1) : bq, chg = CHUNK(bg);
    auto helper = [&](auto SP) {
    constexpr bool spare = decltype(SP)::v, moe = !spare && !NOMOE;
    int nbar = 0;
#define TICK() do { if (HB) { BST(nbar); asm volatile("" : "+v"(BSTV)); } SBAR(); BAR(); SBAR(); ++nbar; } while (0)
#define DTICK(lim) do { if (nbar < (lim)) TICK(); else SLEEP(2); } while (0)
    const int hd = hp * 2 + mw;
    const u4 pz = (u4)(POIS);
    MKWS(tag);
    const int np = l & 7, kset = (l >> 3) & 1;
    u4 gw[4][8], dw[2][8];
    const __amdgpu_buffer_rsrc_t wgr = __builtin_amdgcn_make_buffer_rsrc((void*)(mw ? wu : wg), 0, D * F * 2, 0x00020000);
    const __amdgpu_buffer_rsrc_t wdr = __builtin_amdgcn_make_buffer_rsrc((void*)wd, 0, D * F * 2, 0x00020000);
    const __amdgpu_buffer_rsrc_t wor = __builtin_amdgcn_make_buffer_rsrc((void*)wo, 0, D * D * 2, 0x00020000);
    const unsigned gvo = ((256 * ksp + 32 * kset + 8 * g4) * F + 64 * chg + 8 * np) * 2;
    const unsigned dvo = ((64 * chunk + 8 * g4) * D + 256 * ksp + 128 * mw + 8 * r15) * 2;
    auto burst = [&](int k, int i0 = 0, int i1 = 16) {
      if (moe) {
#pragma unroll
        for (int i = i0; i < i1; ++i) {
          if (k < 2) gw[(2 * k + (i >> 3)) & 3][i & 7] = __builtin_amdgcn_raw_buffer_load_b128(wgr, gvo, (128 * k + 64 * (i >> 3) + (i & 7)) * F * 2, 2);
          else dw[i >> 3][i & 7] = __builtin_amdgcn_raw_buffer_load_b128(wdr, dvo, (32 * (i >> 3) + (i & 7)) * D * 2, 2);
        }
      }
    };
    auto transp = [&](u4(&v)[8]) {
      if (moe) {
        u4 f[8];
#pragma unroll
        for (int c = 0; c < 8; ++c)
#pragma unroll
          for (int e = 0; e < 4; ++e) {
            const unsigned ev = v[2 * e][c >> 1], od = v[2 * e + 1][c >> 1];
            f[c][e] = (c & 1) ? __builtin_amdgcn_perm(od, ev, 0x07060302u) : __builtin_amdgcn_perm(od, ev, 0x05040100u);
          }
#pragma unroll
        for (int c = 0; c < 8; ++c) { v[c] = f[c]; asm volatile("" : "+v"(v[c])); }
      }
    };
    auto pois = [&]() {
      ST128(pz, WS_KVP + (hd * 16 + mb) * 2048 + l * 16); ST128(pz, WS_KVP + (hd * 16 + mb) * 2048 + 1024 + l * 16); ST128(pz, WS_ZP + (hd * 16 + mb) * 128 + (l & 7) * 16);
    };
    unsigned wt0, wt1, wt2 = 0, wt3 = 0;
    auto touch = [&]() {
      const unsigned to = (j * 32 + mw * 16 + (l >> 3)) * 2048 + (xcd >> 1) * 512 + (l & 7) * 64;
      wt0 = __builtin_amdgcn_raw_buffer_load_b32(wor, to, 0, 0); wt1 = __builtin_amdgcn_raw_buffer_load_b32(wor, to, 16384, 0);
      if (TLBW) {
        const __amdgpu_buffer_rsrc_t outr = __builtin_amdgcn_make_buffer_rsrc(out, 0, 2048 * D * 2, 0x00020000);
        wt2 = __builtin_amdgcn_raw_buffer_load_b32(outr, (mb * 128 + mw * 64 + (l & 3) * 16) * 2048 + nb * 128, 0, 0);
        wt3 = __builtin_amdgcn_raw_buffer_load_b32(wsr, (l & 1) ? WS_GP + g * 4096 + mw * 2048 : WS_YP + (g >> 2) * 32768 + (l & 2) * 8192, 0, 0);
      }
    };
    s4 sb[24];
    auto colrd = [&](int h) {
      const unsigned bo = ldsb + (1 + h) * STG + 16384 + (8 * g4 + q) * 128 + (p & 1) * 8;
      const int sw = ((q >> 1) << 2) | ((g4 & 1) << 1);
      const unsigned b0 = bo + (((mw * 4 + (p >> 1)) ^ sw) * 16), b1 = bo + (((mw * 4 + 2 + (p >> 1)) ^ sw) * 16);
#pragma unroll
      for (int n = 0; n < 12; ++n) { RDTR(sb[2 * n], (n & 1) ? b1 : b0, ((n % 6) >> 1) * 8192 + (n / 6) * 4096); RDTR(sb[2 * n + 1], (n & 1) ? b1 : b0, ((n % 6) >> 1) * 8192 + (n / 6) * 4096 + 512); }
      WAITL(0);
      SBAR();
    };
    auto colsum = [&]() {
      const u4 ones = (u4)(0x3F803F80u);
      f4 c[6];
#pragma unroll
      for (int n = 0; n < 12; ++n) c[n % 6] = MFMA16(S8(ones), CAT(sb[2 * n], sb[2 * n + 1]), n >= 6 ? c[n % 6] : (f4)(0.f));
      const u4 csg = (u4){__float_as_uint(g4 ? c[3][0] : c[0][0]), __float_as_uint(g4 ? c[4][0] : c[1][0]), __float_as_uint(g4 ? c[5][0] : c[2][0]), tag};
      WAITV(0);
      asm volatile("" : "+v"(cw) : "v"(pf));
      ST128(csg, l < 32 ? WS_CS + (hd * 16 + mb) * 512 + l * 16 : WS_SZ);
    };
    u4 stv, cv[8], cd; float cs[3];
    auto gload = [&]() {
      stv = LD128(WS_ST + (mb * 128 + mw * 64 + l) * 16);
#pragma unroll
      for (int i = 0; i < 8; ++i) cv[i] = LD128(WS_CS + hd * 8192 + i * 1024 + l * 16);
    };
    auto gbad = [&]() {
      unsigned bad = (stv[1] ^ tag) | (stv[3] ^ tag);
#pragma unroll
      for (int i = 0; i < 8; ++i) bad |= cv[i][3] ^ tag;
      return __ballot(bad != 0u) != 0ull;
    };
    int idx;
    auto gfin = [&]() {
      if (gbad()) { RETRY(); _Pragma("unroll 1") for (unsigned it = 0; it < SPIN; ++it) { SLEEP(2); gload(); if (!gbad()) break; } }
#pragma unroll
      for (int e = 0; e < 3; ++e) {
        float a = __uint_as_float(cv[0][e]);
#pragma unroll
        for (int i = 1; i < 8; ++i) a += __uint_as_float(cv[i][e]);
        auto r = __builtin_amdgcn_permlane32_swap(__float_as_uint(a), __float_as_uint(a), false, false);
        cs[e] = __uint_as_float(r[0]) + __uint_as_float(r[1]);
      }
      asm volatile("" : "+v"(cs[0]), "+v"(cs[1]), "+v"(cs[2]) : "v"(wt0), "v"(wt1), "v"(wt2), "v"(wt3));
    };
    auto cload = [&]() { cd = LD128(WS_CAND + (((hp >> 2) == 2) * 16 + mb) * 128 + (l & 7) * 16); };
    auto cfin = [&]() {
      if (__builtin_expect(__ballot(cd[1] != tag || cd[3] != tag) != 0ull, 0)) {
        RETRY();
        _Pragma("unroll 1") for (unsigned it = 0; it < SPIN; ++it) { SLEEP(4); cload(); if (__ballot(cd[1] != tag || cd[3] != tag) == 0ull) break; }
      }
      cd[0] = max(cd[0], cd[2]);
      idx = (int)(cd[0] & 2047u);
    };
    auto lpub = [&]() {
      *(LAS float*)(lds + L_M + (mw * 64 + l) * 4) = __uint_as_float(stv[0]);
      *(LAS float*)(lds + L_RS + (mw * 64 + l) * 4) = __uint_as_float(stv[2]);
      *(LAS float*)(lds + L_RL + (mw * 64 + l) * 4) = __uint_as_float(stv[2]) * 1.44269504f;
      if (l < 32) {
#pragma unroll
        for (int e = 0; e < 3; ++e) *(LAS float*)(lds + L_CS + mw * 384 + ((3 * g4 + e) * 16 + r15) * 4) = -cs[e];
      }
      if (mw == 0 && l < 8) *(LAS int*)(lds + L_SEL + l * 4) = idx;
      lsig(myFL, 1u);
    };
    u4 xs[4]; float xrs;
    asm volatile("" ::"s"(wg), "s"(wu), "s"(wd), "v"(cw));
    if (HPRIO) __builtin_amdgcn_s_setprio(HPRIO);
    TICK();
    if (spare) {
      TICK();
      pois();
      if (ii >> 2) TICK();
      colrd(ii >> 2);
      if (!(ii >> 2)) TICK();
      u4 kk[8];
      auto kload = [&]() {
#pragma unroll
        for (int i = 0; i < 8; ++i) kk[i] = LD128(WS_RG + mw * 8192 + i * 1024 + l * 16);
      };
      kload();
      TICK();
      _Pragma("unroll 1") for (unsigned it = 0; it < SPIN; ++it) {
        unsigned bad = 0;
#pragma unroll
        for (int i = 0; i < 8; ++i) { bad |= (kk[i][1] ^ tag) | (kk[i][3] ^ tag); asm volatile("" : "+v"(bad)); }
        if (__ballot(bad != 0u) == 0ull) break;
        RETRY();
        kload();
        DTICK(7);
      }
      __builtin_amdgcn_s_setprio(3);
      unsigned m1 = max(kk[0][0], kk[0][2]), m2 = min(kk[0][0], kk[0][2]), m3 = 0;
#pragma unroll
      for (int i = 2; i < 16; ++i) {
        const unsigned a = kk[i >> 1][(i & 1) * 2];
        asm("v_med3_u32 %0, %1, %2, %0" : "+v"(m3) : "v"(m2), "v"(a));
        asm("v_med3_u32 %0, %1, %2, %0" : "+v"(m2) : "v"(m1), "v"(a));
        m1 = max(m1, a);
      }
      unsigned mine8 = 0;
#pragma unroll
      for (int it = 0; it < 8; ++it) {
        const unsigned km = wave_max(m1);
        mine8 = l == it ? km : mine8;
        const bool win = m1 == km;
        if (it < 7) { m1 = win ? m2 : m1; m2 = win ? m3 : m2; m3 = win ? 0u : m3; }
        if (it == 3 && nbar < 7) TICK();
      }
      if (__builtin_expect(__ballot(m1 == 0u) != 0ull, 0)) {
        unsigned lim = 0;
#pragma unroll 1
        for (int it = 0; it < 8; ++it) {
          unsigned m = 0;
#pragma unroll
          for (int i = 0; i < 8; ++i) m = umax3(m, kk[i][0] - lim, kk[i][2] - lim);
          lim += wave_max(m);
          mine8 = l == it ? lim : mine8;
        }
      }
      ST64(((u2){mine8, tag}), l < 8 ? WS_CAND + ((hp >> 3) * 16 + mb) * 128 + (mw ? 7 - l : l) * 16 + mw * 8 : WS_SZ);
      __builtin_amdgcn_s_setprio(HPRIO);
      STMP(2);
      TICK();
      colsum();
      DTICK(9);
      touch();
      while (nbar < 10) TICK();
      gload(); cload();
      if (RT && mw) asm volatile("global_load_dword %0, %1, %2 sc1" : "+v"(cw) : "v"(cwo), "s"(cwb) : "memory");
      TICK(); TICK();
      WAITV(0);
      asm volatile("" : "+v"(cw));
      gfin(); cfin();
      STMP(3);
      TICK(); TICK(); TICK();
      lpub();
    } else {
      if (PV == 0) pois();
      TICK();
      if (PV == 1) pois();
      if (ii >> 2) TICK();
      colrd(ii >> 2);
      TICK();
      if (CL && !(ii >> 2)) TICK();
      colsum();
      if (!CL && !(ii >> 2)) TICK();
      u4 sv; f4 acc[8];
      auto xload = [&]() {
        const u16* xp = x + (size_t)idx * D + 256 * ksp + 32 * kset + 8 * g4;
        sv = LD128(WS_ST + idx * 16);
#pragma unroll
        for (int it = 0; it < 4; ++it) xs[it] = *(const u4*)(xp + 64 * it);
        SBAR();
      };
      auto xnorm = [&]() {
        if (__builtin_expect(__ballot(sv[1] != tag || sv[3] != tag) != 0ull, 0)) {
          _Pragma("unroll 1") for (unsigned it = 0; it < SPIN; ++it) { SLEEP(1); sv = LD128(WS_ST + idx * 16); if (__ballot(sv[1] != tag || sv[3] != tag) == 0ull) break; }
        }
        const float nm = -__uint_as_float(sv[0]);
        xrs = __uint_as_float(sv[2]);
#pragma unroll
        for (int it = 0; it < 4; ++it) {
          float t0, t1, t2, t3; unsigned x0 = xs[it][0], x1 = xs[it][1], x2 = xs[it][2], x3 = xs[it][3];
          asm volatile("v_dot2_f32_bf16 %4, %0, %8, %10\n v_dot2_f32_bf16 %0, %0, %9, %10\n v_dot2_f32_bf16 %5, %1, %8, %10\n v_dot2_f32_bf16 %1, %1, %9, %10\n"
                       "v_dot2_f32_bf16 %6, %2, %8, %10\n v_dot2_f32_bf16 %2, %2, %9, %10\n v_dot2_f32_bf16 %7, %3, %8, %10\n v_dot2_f32_bf16 %3, %3, %9, %10\n"
                       "v_cvt_pk_bf16_f32 %0, %4, %0\n v_cvt_pk_bf16_f32 %1, %5, %1\n v_cvt_pk_bf16_f32 %2, %6, %2\n s_nop 0\n v_cvt_pk_bf16_f32 %3, %7, %3"
                       : "+v"(x0), "+v"(x1), "+v"(x2), "+v"(x3), "=&v"(t0), "=&v"(t1), "=&v"(t2), "=&v"(t3) : "s"(0x00003F80u), "s"(0x3F800000u), "v"(nm));
          xs[it] = (u4){x0, x1, x2, x3};
        }
        asm volatile("s_nop 1" : "+v"(xs[0]), "+v"(xs[1]), "+v"(xs[2]), "+v"(xs[3]), "+v"(xrs));
        SBAR();
      };
      auto gmul = [&](int i0, int i1) {
#pragma unroll
        for (int it = i0; it < i1; ++it)
#pragma unroll
          for (int c = 0; c < 8; ++c) acc[c] = MFMA16(S8(gw[it][c]), S8(xs[it]), it ? acc[c] : (f4)(0.f));
#pragma unroll
        for (int c = 0; c < 8; ++c) asm volatile("" : "+v"(acc[c]));
        SBAR();
      };
      auto gout = [&]() {
        unsigned go = r15 < 8 ? WS_GP + ((chg * 4 + ksp) * 2 + mw) * 2048 + (g4 >> 1) * 1024 + (g4 & 1) * 512 + r15 * 16 : WS_SZ;
        asm volatile("" : "+v"(go));
#pragma unroll
        for (int i = 0; i < 4; ++i) {
          unsigned a[4], b[4];
#pragma unroll
          for (int c = 0; c < 4; ++c) {
            auto r = __builtin_amdgcn_permlane32_swap(__float_as_uint(acc[c][i]), __float_as_uint(acc[c + 4][i]), false, false);
            a[c] = r[0]; b[c] = r[1];
          }
          unsigned s0, s1, s2, s3;
#define AR(d, x, y) "v_add_f32_dpp %" #d ", %" #x ", %" #y " row_ror:8 row_mask:0xf bank_mask:0xf\n"
          asm volatile("s_nop 1\n" AR(0, 8, 4) AR(1, 9, 5) AR(2, 10, 6) AR(3, 11, 7) : "=&v"(s0), "=&v"(s1), "=&v"(s2), "=&v"(s3)
                       : "v"(a[0]), "v"(a[1]), "v"(a[2]), "v"(a[3]), "v"(b[0]), "v"(b[1]), "v"(b[2]), "v"(b[3]));
          ST128(tagf(__builtin_bit_cast(f4, ((u4){s0, s1, s2, s3})), tag), go + i * 128);
        }
        SBAR();
      };
      for (int i = 0; i < ph; ++i) TICK();
      burst(0); touch();
      TICK();
      TICK();
      if (TL) { transp(gw[0]); transp(gw[1]); }
      TICK();
      if (!TL) transp(gw[0]);
      TICK();
      if (!TL) transp(gw[1]);
      TICK();
      if (ph) cload();
      if (RT && mw) asm volatile("global_load_dword %0, %1, %2 sc1" : "+v"(cw) : "v"(cwo), "s"(cwb) : "memory");
      SBAR();
      burst(1);
      TICK();
      TICK(); TICK();
      transp(gw[2]);
      WAITV(0);
      asm volatile("" : "+v"(cw));
      if (ph < 2) TICK();
      transp(gw[3]);
      if (ph < 1) { SBAR(); cload(); TICK(); }
      SBAR();
      if (XO == 0) { cfin(); xload(); gload(); } else { gload(); SBAR(); cfin(); xload(); }
      TICK();
      gfin();
      lpub();
      STMP(12);
      SBAR();
      __builtin_amdgcn_s_setprio(0);
      burst(2, 0, D1);
      STMP(3);
      SBAR();
      if (HS0) SLEEP(HS0);
      if (G1 & 1) { lwait<HW1>(adFL, 0x0Fu, G1 >> 4); if (G1S) SLEEP(G1S); __builtin_amdgcn_s_setprio(TP0); }
      xnorm();
      if (G1 & 2) { lwait<2>(adFL, 0x0Fu, G1 >> 4); if (G1S) SLEEP(G1S); __builtin_amdgcn_s_setprio(TP0); }
      STMP(5);
      gmul(0, 4);
      gout();
      STMP(6);
      SBAR();
    }
    __builtin_amdgcn_s_setprio(TP1);
    STMP(4);
    if (moe) {
      if (D2 == 0) { burst(2, D1, 16); SBAR(); }
      if (D1 >= 8 || D2 == 0) transp(dw[0]); if (D1 == 16 || D2 == 0) transp(dw[1]);
      SBAR();
      lwait<2>(adFL, 0x30u, 3);
      if (G2S) SLEEP(G2S);
      STMP(7);
      if (D2 == 1) { burst(2, D1, 16); SBAR(); }
      f4 acc[8];
      u4 ha[2];
      {
        u4 pg[4][2];
        auto pload = [&]() {
#pragma unroll
          for (int ks = 0; ks < 4; ++ks)
#pragma unroll
            for (int t = 0; t < 2; ++t) pg[ks][t] = LD128(WS_GP + ((chunk * 4 + ks) * 4 + 2 * t + mw) * 1024 + (4 * kset + g4) * 128 + np * 16);
        };
        auto pbad = [&]() {
          unsigned bad = 0;
#pragma unroll
          for (int i = 0; i < 8; ++i) untag(pg[i >> 1][i & 1], tag, bad);
          return __ballot((bad & 255u) != 0u) != 0ull;
        };
        pload();
        SBAR();
        if (D2 == 2) { burst(2, D1, 16); SBAR(); }
        __builtin_amdgcn_s_setprio(TP2);
        if (D1 < 8 && D2 == 1) transp(dw[0]);
        if (D1 < 16 && D2 == 1) transp(dw[1]);
        SBAR();
        if (__builtin_expect(pbad(), 0)) { _Pragma("unroll 1") for (unsigned it = 0; it < SPIN; ++it) { RETRY2(); SLEEP(4); pload(); if (!pbad()) break; } }
        STMP(10);
        f4 gq[2];
#pragma unroll
        for (int t = 0; t < 2; ++t)
          gq[t] = ((__builtin_bit_cast(f4, pg[0][t]) + __builtin_bit_cast(f4, pg[1][t])) + (__builtin_bit_cast(f4, pg[2][t]) + __builtin_bit_cast(f4, pg[3][t]))) * xrs;
        float hv[4];
#pragma unroll
        for (int e = 0; e < 4; ++e) {
          const float gg = rbf(gq[0][e]), uu = rbf(gq[1][e]);
          hv[e] = rbf(gg / (1.f + __expf(-gg))) * uu;
        }
        *(LAS u2*)(lds + L_HX + l * 16 + mw * 8) = (u2){pk2(hv[0], hv[1]), pk2(hv[2], hv[3])};
        lsig(myFL, 2u);
        if (D1 < 16 && D2 == 2) { SBAR(); if (D1 < 8) transp(dw[0]); transp(dw[1]); SBAR(); }
        lwait(adFL, 0xC0u, 2);
        ha[0] = *(const LAS u4*)(lds + L_HX + l * 16); ha[1] = *(const LAS u4*)(lds + L_HX + (l ^ 8) * 16);
      }
#pragma unroll
      for (int it = 0; it < 2; ++it)
#pragma unroll
        for (int c = 0; c < 8; ++c) acc[c] = MFMA16(S8(ha[it]), S8(dw[it][c]), it ? acc[c] : (f4)(0.f));
      if (g4 < 2) {
#pragma unroll
        for (int i = 0; i < 4; ++i) {
          const unsigned yo = WS_YP + ((chunk * 8 + 4 * g4 + i) * D + 256 * ksp + 128 * mw + 4 * r15) * 4;
          ST128(tagf((f4){acc[0][i], acc[1][i], acc[2][i], acc[3][i]}, tag), yo);
          ST128(tagf((f4){acc[4][i], acc[5][i], acc[6][i], acc[7][i]}, tag), yo + 256);
        }
      }
      STMP(11);
    }
    float sg;
    {
      const unsigned mono = cd[0] >> 16, rb = (mono & 0x8000u) ? (mono & 0x7FFFu) : (~mono & 0xFFFFu);
      sg = rbf(1.f / (1.f + __expf(-lo(rb))));
    }
    unsigned mine = (unsigned)__ballot(!NOMOE && l < 8 && (idx >> 7) == mb && (l & 1) == mw);
    if (mine) {
      if (spare) lwait<4>(adFL, 0x30u, 3);
      {
        const u2 f = LD64(WS_OH + (mb * 16 + r15) * 128);
        const unsigned t0 = NOW();
        unsigned rel = f[1] + YD - t0;
        rel = wave_max(f[0] == tag && rel < 600u ? rel : 0u);
        _Pragma("unroll 1") for (unsigned it = 0; it < SPIN; ++it) { if (NOW() - t0 >= rel) break; SLEEP(2); }
      }
      STMP(8);
      lwait<1>(adFL, 0x0Fu, 4);
      __builtin_amdgcn_s_setprio(TP2);
      while (mine) {
        const int t = __builtin_ctz(mine); mine &= mine - 1;
        u4 yv[14];
        _Pragma("unroll 1") for (unsigned it = 0; it < SPIN; ++it) {
          unsigned bad = 0;
#pragma unroll
          for (int i = 0; i < 14; ++i) yv[i] = LD128(WS_YP + (((i * 4 + g4) * 8 + t) * D + (nb >> 1) * 128 + (r15 >> 3) * 64 + (nb & 1) * 32 + (r15 & 7) * 4) * 4);
          SBAR();
#pragma unroll
          for (int i = 0; i < 14; ++i) untag(yv[i], tag, bad);
          if (__ballot((bad & 255u) != 0u) == 0ull) break;
          RETRY();
          SLEEP(YSL);
        }
        f4 y = __builtin_bit_cast(f4, yv[0]);
#pragma unroll
        for (int i = 1; i < 14; ++i) y += __builtin_bit_cast(f4, yv[i]);
        const float gt = __int_as_float(__builtin_amdgcn_readlane(__float_as_int(sg), t));
#pragma unroll
        for (int e = 0; e < 4; ++e) {
          auto r = __builtin_amdgcn_permlane32_swap(__float_as_uint(y[e]), __float_as_uint(y[e]), false, false);
          const float a = __uint_as_float(r[0]) + __uint_as_float(r[1]);
          auto r2 = __builtin_amdgcn_permlane16_swap(__float_as_uint(a), __float_as_uint(a), false, false);
          y[e] = rbf(rbf(__uint_as_float(r2[0]) + __uint_as_float(r2[1])) * gt);
        }
        if (l < 16) *(LAS f4*)(lds + L_KVX + t * 256 + (l & 7) * 32 + (l >> 3) * 16) = y;
      }
    }
    lsig(myFL, 3u);
    STMP(9);
    STFLUSH();
    if (spare) asm volatile(".Lend_s:"); else asm volatile(".Lend_h:");
    };
    if (sp) helper(BC<true>()); else helper(BC<false>());
    return;
  }

  const int wr = w >> 1, wc = w & 1;
  u2 kg; u4 sg4; unsigned tag;
  {
    const int row = srow;
    SBAR(); BST(0); SBAR();
    asm volatile("s_waitcnt vmcnt(4)" : "+v"(xa[0]), "+v"(xa[1]), "+v"(xa[2]), "+v"(xa[3]));
    SBAR(); BST(1); SBAR();
    float s0 = 0, s1 = 0;
#pragma unroll
    for (int c = 0; c < 4; ++c)
#pragma unroll
      for (int e = 0; e < 4; e += 2) { unsigned x0 = xa[c][e], x1 = xa[c][e + 1]; s0 = dot2(x0, 0x3F803F80u, s0); s1 = dot2(x1, 0x3F803F80u, s1); }
    const float m = half_sum(s0 + s1) * (1.f / D);
    f2 v[16], q2 = (f2)(0.f), q3 = (f2)(0.f);
#pragma unroll
    for (int c = 0; c < 16; ++c) v[c] = (f2){lo(xa[c >> 2][c & 3]), hi(xa[c >> 2][c & 3])} - m;
#pragma unroll
    for (int c = 0; c < 16; c += 2) { q2 += v[c] * v[c]; q3 += v[c + 1] * v[c + 1]; }
    q2 += q3;
    const float mr = rbf(m);
    const float rs = 1.f / rbf(sqrtf(rbf(rbf(half_sum(q2[0] + q2[1]) * (1.f / D)) + 1e-5f)));
    float d0 = 0, d1 = 0;
    asm volatile("s_waitcnt vmcnt(0)" : "+v"(rw[0]), "+v"(rw[1]), "+v"(rw[2]), "+v"(rw[3]), "+v"(d0) : "v"(rs));
#pragma unroll
    for (int c = 0; c < 16; c += 2) {
      const f2 e0 = (f2){lo(xa[c >> 2][c & 3]), hi(xa[c >> 2][c & 3])} - mr, e1 = (f2){lo(xa[c >> 2][(c & 3) + 1]), hi(xa[c >> 2][(c & 3) + 1])} - mr;
      const unsigned p0 = pk2(e0[0], e0[1]), p1 = pk2(e1[0], e1[1]);
      const f2 n0 = (f2){lo(p0), hi(p0)} * rs, n1 = (f2){lo(p1), hi(p1)} * rs;
      unsigned c0 = rw[c >> 2][c & 3], c1 = rw[c >> 2][(c & 3) + 1];
      d0 = dot2(pk2(n0[0], n0[1]), c0, d0); d1 = dot2(pk2(n1[0], n1[1]), c1, d1);
    }
    const float d = half_sum(d0 + d1);
    const unsigned rb = pk2(d, d) & 0xFFFFu;
    const unsigned mono = (rb & 0x8000u) ? (~rb & 0xFFFFu) : (rb | 0x8000u);
    SBAR();
    GETTAG(tg); tag = tg;
    kg = (u2){(mono << 16) | (unsigned)row, tag};
    sg4 = (u4){__float_as_uint(mr), tag, __float_as_uint(rs), tag};
  }
  MKWS(tag);
  SBAR(); BST(2); SBAR();
  if ((l & 31) == 0) { ST64(kg, WS_RG + srow * 8); ST128(sg4, WS_ST + srow * 16); }
  unsigned aad[2], bad[2];
#pragma unroll
  for (int s = 0; s < 2; ++s) aad[s] = ldsb + (wr * 64 + r15) * 128 + (((4 * s + g4) ^ ((r15 >> 1) & 7)) * 16);
#pragma unroll
  for (int nh = 0; nh < 2; ++nh) bad[nh] = ldsb + 16384 + (8 * g4 + q) * 128 + (((wc * 4 + 2 * (nh ^ wr) + (p >> 1)) ^ (((q >> 1) << 2) | ((g4 & 1) << 1))) * 16) + (p & 1) * 8;
  f4 acc[4][6];
#pragma unroll
  for (int m = 0; m < 4; ++m)
#pragma unroll
    for (int n = 0; n < 6; ++n) { acc[m][n] = (f4)(0.f); asm volatile("" : "+v"(acc[m][n])); }
#define MF(m, n, Fq) do { if (n < 2) acc[m][n] = MFMA16(CAT(Fq.b[2 * n], Fq.b[2 * n + 1]), Fq.a[m], acc[m][n]); \
     else acc[m][n] = MFMA16(Fq.a[m], CAT(Fq.b[2 * n], Fq.b[2 * n + 1]), acc[m][n]); SBAR(); } while (0)
#define RB(G, n, vb0, vb1, SS, hf) RDTR(G.b[2 * (n) + (hf)], (((n) & 1) ? vb1 : vb0), ((n) >> 1) * 8192 + (SS) * 4096 + (hf) * 512)
#define RA(G, m, va) RD128(G.a[m], va, (m) * 2048)
#define SUB(Fq, G, va, vb0, vb1, SS, PRE) do { \
    WAITL(13); SBAR(); \
    MF(0, 0, Fq); PRE; RA(G, 0, va); SBAR(); \
    WAITL(11); SBAR(); \
    MF(1, 0, Fq); RB(G, 0, vb0, vb1, SS, 0); SBAR(); MF(2, 0, Fq); RB(G, 0, vb0, vb1, SS, 1); SBAR(); MF(3, 0, Fq); RA(G, 1, va); SBAR(); \
    WAITL(12); SBAR(); \
    MF(0, 1, Fq); RA(G, 2, va); SBAR(); MF(1, 1, Fq); RA(G, 3, va); SBAR(); MF(2, 1, Fq); RB(G, 1, vb0, vb1, SS, 0); SBAR(); MF(3, 1, Fq); RB(G, 1, vb0, vb1, SS, 1); SBAR(); \
    WAITL(8); SBAR(); \
    MF(0, 2, Fq); RB(G, 2, vb0, vb1, SS, 0); SBAR(); MF(1, 2, Fq); RB(G, 2, vb0, vb1, SS, 1); SBAR(); MF(2, 2, Fq); RB(G, 3, vb0, vb1, SS, 0); SBAR(); MF(3, 2, Fq); RB(G, 3, vb0, vb1, SS, 1); SBAR(); \
    MF(0, 3, Fq); RB(G, 4, vb0, vb1, SS, 0); SBAR(); MF(1, 3, Fq); RB(G, 4, vb0, vb1, SS, 1); SBAR(); MF(2, 3, Fq); RB(G, 5, vb0, vb1, SS, 0); SBAR(); MF(3, 3, Fq); RB(G, 5, vb0, vb1, SS, 1); SBAR(); \
    MF(0, 4, Fq); MF(1, 4, Fq); MF(2, 4, Fq); MF(3, 4, Fq); MF(0, 5, Fq); MF(1, 5, Fq); MF(2, 5, Fq); MF(3, 5, Fq); } while (0)
#define KSTEP(MODE) do { \
    if (kt == sx) { const unsigned xo = xad + buf * STG; \
      _Pragma("unroll") for (int t = 0; t < 8; ++t) { const unsigned a_ = xo ^ (t * 16); \
        asm volatile("ds_read_b64 %0, %1" : "+v"(xv[t]) : "v"(a_) : "memory"); } \
      WAITL(0); SBAR(); } \
    const int nbuf = (buf + 1) & 3; \
    const unsigned bo = buf * STG, nbo = nbuf * STG; \
    { unsigned va = aad[1] + bo, vb0 = bad[0] + bo, vb1 = bad[1] + bo; SUB(f0, f1, va, vb0, vb1, 1, ); } \
    if (MODE < 2) { \
      unsigned va = aad[0] + nbo, vb0 = bad[0] + nbo, vb1 = bad[1] + nbo; \
      SUB(f1, f0, va, vb0, vb1, 0, if (MODE == 0) { WAITL(0); BAR(); } else asm volatile("ds_write_b32 %0, %1" ::"v"(myFL), "v"(1u) : "memory"); SBAR()); \
    } else { asm volatile("ds_write_b32 %1, %2\n ds_read_b32 %0, %3\n s_waitcnt lgkmcnt(2)" : "=&v"(fl0) : "v"(myFL), "v"(2u), "v"(adFL) : "memory"); SBAR(); \
      MF(0, 2, f1); MF(1, 2, f1); MF(2, 2, f1); MF(3, 2, f1); MF(0, 3, f1); MF(1, 3, f1); MF(2, 3, f1); MF(3, 3, f1); \
      MF(0, 4, f1); MF(1, 4, f1); MF(2, 4, f1); MF(3, 4, f1); MF(0, 5, f1); MF(1, 5, f1); MF(2, 5, f1); MF(3, 5, f1); \
      MF(0, 0, f1); MF(1, 0, f1); MF(2, 0, f1); MF(3, 0, f1); MF(0, 1, f1); MF(1, 1, f1); MF(2, 1, f1); MF(3, 1, f1); } \
    buf = nbuf; } while (0)
#define SUBK(Fq, G, va, vb0, vb1, SS, W0, PRE) do { \
    WAITL(W0); SBAR(); \
    MF(0, 2, Fq); PRE; RA(G, 0, va); SBAR(); \
    WAITL(7); SBAR(); \
    MF(1, 2, Fq); RB(G, 2, vb0, vb1, SS, 0); SBAR(); MF(2, 2, Fq); RB(G, 2, vb0, vb1, SS, 1); SBAR(); MF(3, 2, Fq); RA(G, 1, va); SBAR(); \
    WAITL(8); SBAR(); \
    MF(0, 3, Fq); RA(G, 2, va); SBAR(); MF(1, 3, Fq); RA(G, 3, va); SBAR(); MF(2, 3, Fq); RB(G, 3, vb0, vb1, SS, 0); SBAR(); MF(3, 3, Fq); RB(G, 3, vb0, vb1, SS, 1); SBAR(); \
    WAITL(10); SBAR(); \
    MF(0, 4, Fq); RB(G, 4, vb0, vb1, SS, 0); SBAR(); MF(1, 4, Fq); RB(G, 4, vb0, vb1, SS, 1); SBAR(); MF(2, 4, Fq); RB(G, 5, vb0, vb1, SS, 0); SBAR(); MF(3, 4, Fq); RB(G, 5, vb0, vb1, SS, 1); SBAR(); \
    WAITL(12); SBAR(); \
    MF(0, 5, Fq); MF(1, 5, Fq); MF(2, 5, Fq); MF(3, 5, Fq); } while (0)
#define RQ(G, va, vb0, vb1, SS) do { RA(G, 0, va); RB(G, 0, vb0, vb1, SS, 0); RB(G, 0, vb0, vb1, SS, 1); RA(G, 1, va); RA(G, 2, va); RA(G, 3, va); \
    RB(G, 1, vb0, vb1, SS, 0); RB(G, 1, vb0, vb1, SS, 1); SBAR(); } while (0)
#define MQ(Fq) do { MF(0, 0, Fq); MF(1, 0, Fq); MF(2, 0, Fq); MF(3, 0, Fq); MF(0, 1, Fq); MF(1, 1, Fq); MF(2, 1, Fq); MF(3, 1, Fq); } while (0)
  u2 xv[8];
#pragma unroll
  for (int t = 0; t < 8; ++t) asm volatile("" : "=v"(xv[t]));
  const int sx = (nb - rot) & 15;
  const unsigned xad = ldsb + (w * 32 + (l & 31)) * 128 + ((r15 >> 1) & 7) * 16 + (l >> 5) * 8;
  STMP(1);
  WAITL(0);
  BAR();
  STMP(2);
  WAITL(0);
  FQ f0, f1;
  { unsigned va = aad[0], vb0 = bad[0], vb1 = bad[1];
    RA(f0, 0, va); RB(f0, 0, vb0, vb1, 0, 0); RB(f0, 0, vb0, vb1, 0, 1); RA(f0, 1, va); RA(f0, 2, va); RA(f0, 3, va);
    RB(f0, 1, vb0, vb1, 0, 0); RB(f0, 1, vb0, vb1, 0, 1); RB(f0, 2, vb0, vb1, 0, 0); RB(f0, 2, vb0, vb1, 0, 1); RB(f0, 3, vb0, vb1, 0, 0); RB(f0, 3, vb0, vb1, 0, 1);
    RB(f0, 4, vb0, vb1, 0, 0); RB(f0, 4, vb0, vb1, 0, 1); RB(f0, 5, vb0, vb1, 0, 0); RB(f0, 5, vb0, vb1, 0, 1); SBAR(); }
  unsigned sad = ldsb + L_M + wr * 256 + g4 * 16, cad = ldsb + L_CS + wc * 384 + r15 * 4 + wr * 64;
  const int hd = hp * 2 + wc;
  unsigned kvo = WS_KVP + (hd * 16 + mb) * 2048 + wr * 1024 + l * 16, zo = r15 == 0 ? WS_ZP + (hd * 16 + mb) * 128 + g4 * 32 + wr * 16 : WS_SZ;
  unsigned xw = ldsb + L_KVX + w * 3072 + l * 16, xr = ldsb + L_KVX + (w ^ 2) * 3072 + l * 16;
  asm volatile("s_waitcnt lgkmcnt(0)" : "+v"(sad), "+v"(cad), "+v"(kvo), "+v"(zo), "+v"(xw), "+v"(xr) : : "memory"); SBAR();
  int buf = 0; unsigned fl0;
#pragma unroll 1
  for (int kt = 0; kt < 14; ++kt) KSTEP(0);
  STMP(14); SBAR();
  f4 sm[4], sr[4], sl[4]; f2 ck, cv;
  unsigned c2 = 2u, c3 = 3u, vfl = myFL, b30, b31;
  u4 ones = (u4)(0x3F803F80u);
#define SC0() asm volatile("ds_read_b32 %0, %2 offset:128\n ds_read_b32 %1, %2 offset:256" : "=&v"(ck[0]), "=&v"(cv[0]) : "v"(cad) : "memory")
#define SC1() asm volatile("ds_read_b32 %0, %2 offset:128\n ds_read_b32 %1, %2 offset:256" : "=&v"(ck[1]), "=&v"(cv[1]) : "v"(cad ^ 64) : "memory")
  const unsigned b20 = bad[0] + 2 * STG, b21 = bad[1] + 2 * STG;
#define SM(m) do { RD128(sm[m], sad, (m) * 64); RD128(sl[m], sad, L_RL - L_M + (m) * 64); RD128(sr[m], sad, L_RS - L_M + (m) * 64); } while (0)
  {
    { const unsigned va = aad[1] + 2 * STG; SUBK(f0, f1, va, b20, b21, 1, 6, asm volatile("" ::"v"(f0.b[0]), "v"(f0.b[1]), "v"(f0.b[2]), "v"(f0.b[3]))); }
    { const unsigned va = aad[0] + 3 * STG; b30 = bad[0] + 3 * STG; b31 = bad[1] + 3 * STG; SUBK(f1, f0, va, b30, b31, 0, 9, asm volatile("" : "+v"(c2), "+v"(c3), "+v"(vfl))); }
    STMP(7); SBAR();
    { const unsigned va = aad[1] + 3 * STG; SUBK(f0, f1, va, b30, b31, 1, 9, asm volatile("" : "+v"(ones))); }
    asm volatile("ds_write_b32 %1, %2\n ds_read_b32 %0, %3\n s_waitcnt lgkmcnt(8)" : "=&v"(fl0) : "v"(vfl), "v"(c2), "v"(adFL) : "memory"); SBAR();
    MF(0, 2, f1); SC0(); SBAR(); MF(1, 2, f1); SC1(); SBAR(); MF(2, 2, f1); MF(3, 2, f1); WAITL(10); SBAR();
    MF(0, 3, f1); SM(0); SBAR(); MF(1, 3, f1); MF(2, 3, f1); SM(1); SBAR(); MF(3, 3, f1);
    WAITL(12); SBAR();
    MF(0, 4, f1); SM(2); SBAR(); MF(1, 4, f1); MF(2, 4, f1); SM(3); SBAR(); MF(3, 4, f1); MF(0, 5, f1); MF(1, 5, f1); MF(2, 5, f1); MF(3, 5, f1);
  }
  asm volatile("s_waitcnt lgkmcnt(0)" : "+v"(fl0) : : "memory");
  if (__builtin_expect(((unsigned)__ballot(fl0 < 1u) & 0xC0u) != 0u, 0)) {
    lwait(adFL, 0xC0u, 1);
    SC0(); SC1(); SM(0); SM(1); SM(2); SM(3); WAITL(0);
  }
  asm volatile("" : "+v"(ck), "+v"(cv), "+v"(sm[0]), "+v"(sm[1]), "+v"(sm[2]), "+v"(sm[3]), "+v"(sr[0]), "+v"(sr[1]), "+v"(sr[2]), "+v"(sr[3]));
  asm volatile("" : "+v"(sl[0]), "+v"(sl[1]), "+v"(sl[2]), "+v"(sl[3]));
  STMP(3);
  __builtin_amdgcn_s_setprio(2);
  {
    f4 kv[4], zt[2];
    u4 kf[2][2], vf[2][2];
#define MKV(t, i) do { if ((i) == 2 || (i) == 5) zt[(i) == 2] = MFMA16(S8(kf[t][(i) == 2]), S8(ones), (t) ? zt[(i) == 2] : (f4)(0.f)); \
      else kv[((i) + 2) % 5] = MFMA16(S8(kf[t][(((i) + 2) % 5) & 1]), S8(vf[t][(((i) + 2) % 5) >> 1]), (t) ? kv[((i) + 2) % 5] : (f4)(0.f)); SBAR(); } while (0)
#pragma unroll
    for (int m = 0; m < 4; ++m) {
      const int t = m >> 1, mm = m & 1;
      f4 ea[2], pp[2];
#pragma unroll
      for (int a = 0; a < 2; ++a) {
        const f4 uv = (acc[m][4 + a] + sm[m] * cv[a]) * sr[m];
        vf[t][a][2 * mm] = pk2(uv[0], uv[1]); vf[t][a][2 * mm + 1] = pk2(uv[2], uv[3]);
        const f4 uk = acc[m][2 + a] + sm[m] * ck[a];
        ea[a] = uk * sl[m]; pp[a] = uk * sr[m] + 1.f;
      }
      SBAR();
#pragma unroll
      for (int e = 0; e < 8; ++e) { if (t && (e == 0 || e == 3 || e == 6)) MKV(0, 3 * mm + e / 3); ea[e >> 2][e & 3] = __builtin_amdgcn_exp2f(ea[e >> 2][e & 3]); SBAR(); }
#pragma unroll
      for (int e = 0; e < 8; ++e) pp[e >> 2][e & 3] = __builtin_amdgcn_fmed3f(ea[e >> 2][e & 3], pp[e >> 2][e & 3], 1.f);
      SBAR();
#pragma unroll
      for (int a = 0; a < 2; ++a) { pp[a] += 1e-6f; kf[t][a][2 * mm] = pk2(pp[a][0], pp[a][1]); kf[t][a][2 * mm + 1] = pk2(pp[a][2], pp[a][3]); }
      SBAR();
    }
    MKV(1, 0); MKV(1, 1); MKV(1, 2); MKV(1, 3);
    asm volatile("ds_write_b128 %0, %1" ::"v"(xw), "v"(kv[2]) : "memory"); SBAR();
    MKV(1, 4);
    asm volatile("ds_write_b128 %0, %1 offset:1024" ::"v"(xw), "v"(kv[3]) : "memory"); SBAR();
    MKV(1, 5);
    asm volatile("ds_write_b128 %0, %1 offset:2048\n ds_write_b32 %2, %3" ::"v"(xw), "v"(zt[1]), "v"(vfl), "v"(c3) : "memory");
    f4 r0, r1, rz;
    _Pragma("unroll 1") for (unsigned it = 0; it < SPIN; ++it) {
      unsigned v;
      asm volatile("ds_read_b32 %0, %4\n ds_read_b128 %1, %5 offset:1024\n ds_read_b128 %2, %5\n ds_read_b128 %3, %5 offset:2048\n s_waitcnt lgkmcnt(0)"
                   : "=&v"(v), "=&v"(r0), "=&v"(r1), "=&v"(rz) : "v"(adFL), "v"(xr) : "memory");
      if ((((unsigned)__ballot(v < 3u) >> (w ^ 2)) & 1u) == 0u) break;
    }
    const f4 s0 = kv[0] + r0, s1 = kv[1] + r1, sz = zt[0] + rz;
    u4 o; o[0] = min(pk2(s0[0], s0[1]), POIS - 1u); o[1] = pk2(s0[2], s0[3]); o[2] = pk2(s1[0], s1[1]); o[3] = pk2(s1[2], s1[3]);
    ST128(o, kvo);
    u4 zq = __builtin_bit_cast(u4, sz); zq[0] = min(zq[0], POIS - 1u);
    ST128(zq, zo);
  }
  STMP(4);
  {
    SBAR();
    const LAS float* Lm = (const LAS float*)(lds + L_M) + wr * 64 + r15;
    const LAS float* Lc = (const LAS float*)(lds + L_CS) + wc * 96 + 4 * g4;
    { const unsigned va = aad[0] + 2 * STG; RQ(f0, va, b20, b21, 0); }
    { const unsigned va = aad[1] + 2 * STG; RQ(f1, va, b20, b21, 1); }
    WAITL(8); SBAR(); MQ(f0);
    { const unsigned va = aad[0] + 3 * STG; RQ(f0, va, b30, b31, 0); }
    WAITL(8); SBAR(); MQ(f1);
    { const unsigned va = aad[1] + 3 * STG; RQ(f1, va, b30, b31, 1); }
    WAITL(8); SBAR();
    const f4 c0 = *(const LAS f4*)(Lc + 16 * wr), c1 = *(const LAS f4*)(Lc + 16 - 16 * wr);
    float mv[4], rv[4], rl[4];
#pragma unroll
    for (int m = 0; m < 4; ++m) { mv[m] = Lm[m * 16]; rv[m] = Lm[m * 16 + (L_RS - L_M) / 4]; rl[m] = Lm[m * 16 + (L_RL - L_M) / 4]; }
    SBAR(); MQ(f0);
    if (sx >= 14) {
      const unsigned xo = xad + (sx - 12) * STG;
#pragma unroll
      for (int t = 0; t < 8; ++t) { const unsigned a_ = xo ^ (t * 16); asm volatile("ds_read_b64 %0, %1" : "+v"(xv[t]) : "v"(a_) : "memory"); }
    }
    WAITL(0); SBAR(); MQ(f1);
    LAS char* pw = (LAS char*)lds + L_PQ + wr * 8192 + wc * 4096 + l * 16;
#pragma unroll
    for (int m = 0; m < 4; ++m) {
      const f4 u0 = acc[m][0] + c0 * mv[m], u1 = acc[m][1] + c1 * mv[m];
      *(LAS u2*)(pw + wr * 8 + m * 1024) = (u2){phi2((f2){u0[0], u0[1]}, (f2)(rv[m]), (f2)(rl[m])), phi2((f2){u0[2], u0[3]}, (f2)(rv[m]), (f2)(rl[m]))};
      *(LAS u2*)(pw + 8 - wr * 8 + m * 1024) = (u2){phi2((f2){u1[0], u1[1]}, (f2)(rv[m]), (f2)(rl[m])), phi2((f2){u1[2], u1[3]}, (f2)(rv[m]), (f2)(rl[m]))};
    }
  }
  lsig(myFL, 4);
  __builtin_amdgcn_s_setprio(0);
  STMP(5);
  {
  int l;
  asm volatile("v_mbcnt_lo_u32_b32 %0, -1, 0\n v_mbcnt_hi_u32_b32 %0, -1, %0" : "=v"(l));
  __builtin_assume(l >= 0 && l < 64);
  const int r15 = l & 15, g4 = l >> 4, q = (l & 15) >> 2, p = l & 3;
  const int trow = mb * 128 + w * 32 + (l & 31);
  u2 oh;
  const unsigned flo = PAIRAT(r15) * 128;
  const char* fb = uptr(wsx + WS_OH + mb * 2048);
  int sel = -1;
  {
    const u4 s0 = *(const LAS u4*)(lds + L_SEL), s1 = *(const LAS u4*)(lds + L_SEL + 16);
#pragma unroll
    for (int i = 0; i < 8; ++i) { const int v = (int)(i < 4 ? s0[i & 3] : s1[i & 3]); sel = v == trow ? i : sel; }
  }
  const bool anysel = __ballot(sel >= 0) != 0ull;
  f16v oc[2][2];
  {
    const float m = *(const LAS float*)(lds + L_M + (w * 32 + (l & 31)) * 4), rs = *(const LAS float*)(lds + L_RS + (w * 32 + (l & 31)) * 4);
#pragma unroll
    for (int t = 0; t < 8; ++t) {
      const f4 xf = (f4){lo(xv[t][0]), hi(xv[t][0]), lo(xv[t][1]), hi(xv[t][1])};
      const f4 y = (xf - m) * rs + xf;
#pragma unroll
      for (int i = 0; i < 4; ++i) oc[0][t >> 2][(t & 3) * 4 + i] = y[i];
    }
    asm volatile("" : "+v"(oc[0][0]), "+v"(oc[0][1]));
  }
  unsigned wa[2];
#pragma unroll
  for (int t = 0; t < 2; ++t) wa[t] = ldsb + L_WO + wc * 4096 + (4 * (g4 >> 1) + q) * 128 + (((4 * t + 2 * (g4 & 1) + (p >> 1)) ^ ((q >> 1) << 2)) * 16) + (p & 1) * 8;
  u4 a[RD][4];
#pragma unroll
  for (int k = 0; k < RD; ++k) asm volatile("" : "=v"(a[k][0]), "=v"(a[k][1]), "=v"(a[k][2]), "=v"(a[k][3]));
  const char* ob = uptr(wsx + WS_OF + mb * 262144 + wc * 8192 + wr * 4096);
  const unsigned ov0 = l * 16 + wc * 2048, ov1 = ov0 ^ 2048;
  STMP(6);
  unsigned vfw[4];
#pragma unroll
  for (int i = 0; i < 4; ++i) { vfw[i] = VFW(i); asm volatile("" : "+v"(vfw[i])); }
  auto wo_part = [&](int s0, int s1) {
#pragma unroll
    for (int i = s0 / 2; i < s1 / 2; i += 2) {
      const int pp = i + wr;
      const char* wp = (const char*)wo + (nb * 128 - 3072) + (2 * PAIRAT(pp) + wc) * 65536; const unsigned wm = ldsb + (2 * pp + wc) * 4096;
      GL4(vfw, wp, wm, L_WO);
    }
  };
  lwait<2>(adFL, 0x3Fu, (l & 4) ? 3u : 4u);
  asm volatile("global_load_dwordx2 %0, %1, %2 sc1" : "=&v"(oh) : "v"(flo), "s"(fb) : "memory");
  wo_part(WE, WB);
  WAITV(WB - WE);
  asm volatile("" : "+v"(oh));
  unsigned seen = (unsigned)__ballot(oh[0] == tag), go = 0;
  auto need = [&](unsigned mask) {
    if (__builtin_expect((go & mask) != mask, 0)) {
      RETRY2();
      _Pragma("unroll 1") for (unsigned it = 0; it < SPIN; ++it) {
        if ((seen & mask) != mask) {
          asm volatile("global_load_dwordx2 %0, %1, %2 sc1\n s_waitcnt vmcnt(0)" : "+v"(oh) : "v"(flo), "s"(fb) : "memory");
          seen = (unsigned)__ballot(oh[0] == tag);
        }
        go = seen & (unsigned)__ballot(oh[1] - NOW() - 1u >= 1000u);
        if ((go & mask) == mask) break;
        SLEEP(1);
      }
    }
  };
#define OX 0
#define OSC "sc1"
#define OL1(pos, i) do { if (OX == 2 && (pos) >= RD) break; \
    asm volatile("global_load_dwordx4 %0, %1, %2 offset:%3 " OSC : "+v"(a[(pos) % RD][i]) : "v"((i) & 1 ? ov1 : ov0), "s"(ob + (OX == 1 ? ((pos) & 1) : PAIRAT(pos)) * 16384), "i"(((i) >> 1) * 1024) : "memory"); } while (0)
#define OLR(pos) do { _Pragma("unroll") for (int i_ = 0; i_ < 4; ++i_) asm volatile("global_load_dwordx4 %0, %1, %2 sc1" : "+v"(a[(pos) % RD][i_]) : "v"((i_ & 1 ? ov1 : ov0) + (i_ >> 1) * 1024), "s"(ob + PAIRAT(pos) * 16384) : "memory"); } while (0)
#define OLD(pos) do { OL1(pos, 0); OL1(pos, 1); OL1(pos, 2); OL1(pos, 3); } while (0)
  auto omax = [&](u4(&v)[4]) {
    asm volatile("" : "+v"(v[0]), "+v"(v[1]), "+v"(v[2]), "+v"(v[3]));
    return max(umax3(v[0][0], v[1][0], v[2][0]), v[3][0]);
  };
#define OFIX(pos, m) do { \
    if (__builtin_expect(__ballot((m) == POIS) != 0ull, 0)) { RETRY(); _Pragma("unroll 1") for (unsigned it = 0; it < SPIN; ++it) { SLEEP(4); OLR(pos); WAITV(0); if (__ballot(omax(a[(pos) % RD]) == POIS) == 0ull) break; } } } while (0)
#define OS1(i)
#define OS2(i)
  lwait<1>(adFL, 0x30u, 4);
  wo_part(WB, 32);
  __builtin_amdgcn_s_setprio(2);
  go = seen & (unsigned)__ballot(oh[1] - NOW() - 1u >= 1000u);
  need(1u);
  STMP(15);
  u2 oh2 = oh;
  if (__builtin_expect(seen != ~0u, 0)) asm volatile("global_load_dwordx2 %0, %1, %2 sc1" : "+v"(oh2) : "v"(flo), "s"(fb) : "memory");
  OS1(0); OLD(0);
#pragma unroll
  for (int i = 1; i < RD; ++i) { need(1u << i); OS1(i); OLD(i); }
  STMP(10);
  s4 b[8];
#define ORD(pos, f) do { if ((f) < 2 && (pos) == 8) { wa[(f) & 1] += 65536; asm volatile("" : "+v"(wa[(f) & 1])); } \
    RDTR(b[2 * (f)], wa[(f) & 1], ((pos) & 7) * 8192 + ((f) >> 1) * 1024); RDTR(b[2 * (f) + 1], wa[(f) & 1], ((pos) & 7) * 8192 + ((f) >> 1) * 1024 + 2048); } while (0)
#define OMF(pos, f, r) do { if ((f) < 2 && (pos) == 0 && (r) == 1) asm volatile("v_mfma_f32_32x32x16_bf16 %0, %1, %2, 0" : "=&v"(oc[r][(f) & 1]) \
      : "v"(CAT(b[2 * (f)], b[2 * (f) + 1])), "v"(a[(pos) % RD][((f) & 2) + (r)])); \
    else asm volatile("v_mfma_f32_32x32x16_bf16 %0, %1, %2, %0" : "+v"(oc[r][(f) & 1]) \
      : "v"(CAT(b[2 * (f)], b[2 * (f) + 1])), "v"(a[(pos) % RD][((f) & 2) + (r)])); \
    SBAR(); if ((r) == 1 && (pos) < 15) ORD(((pos) + 1) & 15, f); SBAR(); } while (0)
#define ONX(pos) ((pos) > 0 && (pos) + RD - 1 < 16)
#define ONL(pos, i) do { if (ONX(pos)) OL1(((pos) + RD - 1) & 15, i); SBAR(); } while (0)
#define OWL(pos, f) do { WAITL((pos) < 15 ? 6 : 6 - 2 * (f)); SBAR(); } while (0)
#define OHEAD(pos) do { unsigned m_ = 0; \
    if ((pos) == WE / 2 - 1 && WE < 32) { asm volatile("" : "+v"(fl3)); if (__builtin_expect(((unsigned)__ballot(fl3 < 5u) >> (w ^ 2)) & 1u, 0)) lwait(adFL, 1u << (w ^ 2), 5); SBAR(); } \
    OWL(pos, 0); OMF(pos, 0, 0); if (ONX(pos)) { need(1u << (((pos) + RD - 1) & 15)); OS1(((pos) + RD - 1) & 15); } SBAR(); \
    OMF(pos, 0, 1); ONL(pos, 0); \
    OWL(pos, 1); OMF(pos, 1, 0); ONL(pos, 1); \
    OMF(pos, 1, 1); ONL(pos, 2); \
    OWL(pos, 2); OMF(pos, 2, 0); ONL(pos, 3); \
    if ((pos) == 0 || (pos) == 4) { asm volatile("ds_write_b32 %0, %1" ::"v"(myFL), "v"((pos) ? 6u : 5u) : "memory"); SBAR(); } \
    if ((pos) == WE / 2 - 3) { asm volatile("ds_read_b32 %0, %1" : "=v"(fl3) : "v"(adFL) : "memory"); SBAR(); } \
    if ((pos) == 13) { asm volatile("ds_read_b32 %0, %1" : "=v"(fl3) : "v"(adFL) : "memory"); SBAR(); } \
    OMF(pos, 2, 1); \
    OWL(pos, 3); OMF(pos, 3, 0); \
    if ((pos) < 15) { WAITV(4 * ((pos) + RD - 1 < 16 ? RD - 2 : 14 - (pos))); m_ = omax(a[((pos) + 1) % RD]); asm volatile("" : "+v"(m_)); SBAR(); } \
    OMF(pos, 3, 1); \
    if ((pos) < 15) { OFIX(((pos) + 1) & 15, m_); OS2(((pos) + 1) & 15); } SBAR(); } while (0)
  unsigned fl3;
  ORD(0, 0); ORD(0, 1); ORD(0, 2); ORD(0, 3);
  { WAITV(4 * (RD - 1)); const unsigned m_ = omax(a[0]); OFIX(0, m_); OS2(0);
    asm volatile("" : "+v"(oh2));
    if (oh2[0] == tag) oh = oh2;
    seen = (unsigned)__ballot(oh[0] == tag);
    go = seen & (unsigned)__ballot(oh[1] - NOW() - max(r15 - (RD - 1), 0) * ADV - 1u >= 1000u);
    SBAR(); }
  OHEAD(0); OHEAD(1); STMP(11); OHEAD(2); OHEAD(3); OHEAD(4); OHEAD(5); OHEAD(6); OHEAD(7); STMP(12);
  OHEAD(8); OHEAD(9); STMP(13); OHEAD(10); OHEAD(11); OHEAD(12); OHEAD(13); OHEAD(14);
  f4 xi[8];
  {
    asm volatile("" : "+v"(fl3));
    if (__builtin_expect(((unsigned)__ballot(fl3 < 6u) >> (w ^ 2)) & 1u, 0)) lwait(adFL, 1u << (w ^ 2), 6);
    if (__builtin_expect(((unsigned)__ballot(fl3 < 4u) & 0x30u) != 0u, 0)) lwait(adFL, 0x30u, 4);
    const unsigned xw = ldsb + L_WO + (4 * wr + wc) * 4096 + l * 16, xr = xw ^ 4096;
#define XW(t) asm volatile("ds_write_b128 %0, %1 offset:%2" ::"v"(xw), "v"(__builtin_shufflevector(oc[1][(t) >> 2], oc[1][(t) >> 2], ((t) & 3) * 4, ((t) & 3) * 4 + 1, ((t) & 3) * 4 + 2, ((t) & 3) * 4 + 3)), \
      "i"(((t) >> 2) * 8192 + ((t) & 3) * 1024) : "memory")
    SBAR();
    OWL(15, 0); OMF(15, 0, 1); OWL(15, 1); OMF(15, 1, 1); OWL(15, 2); OMF(15, 2, 1); OWL(15, 3); OMF(15, 3, 1);
    OMF(15, 0, 0); OMF(15, 1, 0); XW(0); XW(1); XW(2); XW(3); SBAR();
    OMF(15, 2, 0); XW(4); XW(5); XW(6); XW(7); SBAR();
    OMF(15, 3, 0);
    asm volatile("ds_write_b32 %0, %1" ::"v"(myFL), "v"(7u) : "memory");
    _Pragma("unroll 1") for (unsigned it = 0; it < SPIN; ++it) {
      unsigned v;
      asm volatile("ds_read_b32 %0, %1" : "=v"(v) : "v"(adFL) : "memory");
#pragma unroll
      for (int t = 0; t < 8; ++t) RD128(xi[t], xr, (t >> 2) * 8192 + (t & 3) * 1024);
      WAITL(0);
      asm volatile("" : "+v"(v), "+v"(xi[0]), "+v"(xi[1]), "+v"(xi[2]), "+v"(xi[3]), "+v"(xi[4]), "+v"(xi[5]), "+v"(xi[6]), "+v"(xi[7]));
      if ((((unsigned)__ballot(v < 7u) >> (w ^ 1)) & 1u) == 0u) break;
    }
    asm volatile("" : "+v"(oc[0][0]), "+v"(oc[0][1]));
#pragma unroll
    for (int t = 0; t < 8; ++t)
#pragma unroll
      for (int i = 0; i < 4; ++i) xi[t][i] += oc[0][t >> 2][(t & 3) * 4 + i];
  }
  STMP(8);
  if (anysel) {
    lwait(adFL, 0xC0u, 3);
    if (sel >= 0) {
#pragma unroll
      for (int t = 0; t < 8; ++t) xi[t] += *(const LAS f4*)(lds + L_KVX + sel * 256 + (8 * t + 4 * (l >> 5)) * 4);
    }
  }
  const __amdgpu_buffer_rsrc_t outr = __builtin_amdgcn_make_buffer_rsrc(out, 0, 2048 * D * 2, 0x00020000);
  if (FULL) {
    LAS char* tb = (LAS char*)lds + L_PQ + w * 4096;
#pragma unroll
    for (int t = 0; t < 8; ++t) *(LAS u2*)(tb + (l & 31) * 128 + ((t ^ (l & 7)) * 16) + (l >> 5) * 8) = (u2){pk2(xi[t][0], xi[t][1]), pk2(xi[t][2], xi[t][3])};
#pragma unroll
    for (int i = 0; i < 4; ++i) {
      const u4 v = *(const LAS u4*)(tb + i * 1024 + (l >> 3) * 128 + (((l ^ (l >> 3)) & 7) * 16));
      __builtin_amdgcn_raw_buffer_store_b128(v, outr, (mb * 128 + w * 32 + i * 8 + (l >> 3)) * 2048 + nb * 128 + (l & 7) * 16, 0, 16);
    }
  } else {
#pragma unroll
  for (int hh = 0; hh < 4; ++hh) {
    const f4 ta = xi[2 * hh], tb = xi[2 * hh + 1];
    auto r0 = __builtin_amdgcn_permlane32_swap(pk2(ta[0], ta[1]), pk2(tb[0], tb[1]), false, false);
    auto r1 = __builtin_amdgcn_permlane32_swap(pk2(ta[2], ta[3]), pk2(tb[2], tb[3]), false, false);
    __builtin_amdgcn_raw_buffer_store_b128(((u4){r0[0], r1[0], r0[1], r1[1]}), outr, (trow * D + nb * 64 + 16 * hh + 8 * (l >> 5)) * 2, 0, 16);
  }
  }
  if (g == 0 && w == 0 && l == 0) __builtin_amdgcn_raw_buffer_store_b32(tag, wse, WS_E, 0, 16);
  STMP(9);
  STFLUSH();
  asm volatile(".Lend_c:");
  }
  return;
  }
  {
    unsigned vb[4], vfw[4];
    STMP(6); SBAR();
#pragma unroll
    for (int i = 0; i < 4; ++i) vb[i] = (8 * i + (l >> 3)) * 6144 + (((l & 7) ^ ((((l >> 4) & 1) << 2) | ((i & 1) << 1))) * 16) + (3 - i) * 1024;
    const unsigned lb0 = lw * 196608, lb1 = 196608 - lw * 194560, mbb = ldsb + 16384 + lw * 12288;
    const char* Ab = kx + ao;
    const char* Bb = kq + (hp * 128 + lw * 2048 - 3072);
    auto issue = [&](int s, int u0, int u1) {
      const int kt = (s + rot) & 15;
      const char* sa = Ab + kt * 128; const char* sb = Bb + kt * (64 * 6144);
      const unsigned so = (s & 3) * STG, ma = mba + so, mq = mbb + so;
#pragma unroll
      for (int u = u0; u < u1; ++u) { if (u < 2) GL4(va, sa + u * 65536, ma, u * 4096); else GL4(vb, sb + (u == 2 ? lb0 : u == 3 ? lb1 : lb0 + 2048), mq, (u - 2) * 4096); }
    };
    int tk = 0;
#define LBAR() do { BST(tk + 1); BAR(); BST(tk + 21); ++tk; } while (0)
    issue(0, 2, 3); issue(0, 1, 2); issue(0, 3, 5); SBAR(); STMP(8); SBAR(); issue(1, 0, 5); issue(2, 0, U0);
    BST(60);
    WAITV(20 + 4 * U0); BST(0); BAR(); BST(20);
    issue(2, U0, 5);
    GETTAG(tag);
    MKWS(tag);
    const unsigned ck0 = (unsigned)__builtin_amdgcn_s_memtime(), rt0 = NOW();
    int s0 = 3;
    if (U3 < 5) { issue(3, 0, U3); BST(tk + 40); WAITV(20 + 4 * U3); LBAR(); issue(3, U3, 5); s0 = 4; }
#pragma unroll 1
    for (; s0 < (sp ? 16 : 6 + ph); ++s0) { issue(s0, 0, 5); BST(tk + 40); WAITV(40); LBAR(); }
#pragma unroll 1
    for (; s0 < 16; s0 += 5) {
      WAITV(20); LBAR();
      WAITV(0); LBAR();
      issue(s0, 0, 5); issue(s0 + 1, 0, U1); BST(tk + 40); WAITV(4 * U1); LBAR();
      issue(s0 + 1, U1, 5); issue(s0 + 2, 0, 5);
      if (s0 + 3 >= 16) break;
      issue(s0 + 3, 0, U2); BST(tk + 40); WAITV(20 + 4 * U2); LBAR();
      issue(s0 + 3, U2, 5);
      if (s0 + 4 >= 16) break;
      issue(s0 + 4, 0, 5); BST(tk + 40); WAITV(40); LBAR();
    }
    int wq = 0;
#pragma unroll
    for (int i = 0; i < 4; ++i) vfw[i] = VFW(i);
    auto wo_upto = [&](int lim) {
      _Pragma("unroll 1") for (; wq < lim; ++wq) { const char* wp = (const char*)wo + (nb * 128 - 3072) + HEADAT(wq) * 65536; const unsigned wm = ldsb + wq * 4096; GL4(vfw, wp, wm, L_WO); }
    };
    asm volatile("ds_write_b32 %0, %1" ::"v"(adFL), "v"(0u) : "memory");
    unsigned kd = sp ? KS : KD, od = OD;
    if (CF) {
      const unsigned dc = (unsigned)__builtin_amdgcn_s_memtime() - ck0, dr = NOW() - rt0;
      const float sc = fminf((float)dr * (float)CF * __builtin_amdgcn_rcpf((float)dc), 2.f);
      kd = KL + (unsigned)((float)(kd - KL) * sc); od = OL + (unsigned)((float)(OD - OL) * sc);
      asm volatile("" : "+v"(kd), "+v"(od));
    }
    const int hd = hp * 2 + lw;
    u4 sel[2];
#pragma unroll
    for (int e = 0; e < 2; ++e) {
      unsigned v = (r15 >> 2) == g4 ? (((r15 & 3) == 2 * e ? 0x3F80u : 0u) | ((r15 & 3) == 2 * e + 1 ? 0x3F800000u : 0u)) : 0u;
      asm volatile("" : "+v"(v));
      sel[0][e] = v; sel[0][2 + e] = 0u; sel[1][e] = 0u; sel[1][2 + e] = v;
    }
    const char* kvb = uptr(wsx + WS_KVP + (size_t)hd * 32768);
    const char* zb = uptr(wsx + WS_ZP + (size_t)hd * 2048);
    unsigned zoff = r15 * 128 + g4 * 32, oho = l == 0 ? WS_OH + (mb * 16 + hp) * 128 : WS_SZ, fla = ldsb + L_FL;
    asm volatile("" : "+v"(zoff), "+v"(oho), "+v"(fla), "+s"(kvb), "+s"(zb));
    WAITV(0); WAITL(0); LBAR();
    u4 dg;
    unsigned now;
    if (lw) {
      const u4 pz = (u4)(POIS);
      const unsigned tb = NOW();
      __builtin_amdgcn_raw_buffer_store_b32(tb + kd, wsr, l == 0 ? WS_HK + hp * 512 + (mb >> 2) * 128 + (mb & 3) * 4 : WS_SZ, 0, 16);
#pragma unroll
      for (int i = 0; i < 16; ++i) ST128(pz, WS_OF + (mb * 32 + hp * 2) * 8192 + i * 1024 + l * 16);
      BST(61);
      const char* hb = uptr(wsx + WS_HK + hp * 512);
      __builtin_amdgcn_s_setprio(LP1);
      lwait<4>(adFL, 0xC0u, 1u);
      while (NOW() - tb < HT) SLEEP(1);
      STMP(1);
      unsigned t0, rq[4], m0, m1, nt;
#define HG4(d, a, b, c, e) "s_sub_u32 s" #a ", s" #a ", %0\n s_sub_u32 s" #b ", s" #b ", %0\n s_sub_u32 s" #c ", s" #c ", %0\n s_sub_u32 s" #e ", s" #e ", %0\n" \
      "s_max_u32 s" #a ", s" #a ", s" #b "\n s_max_u32 s" #c ", s" #c ", s" #e "\n s_max_u32 %" #d ", s" #a ", s" #c "\n"
      asm volatile("s_mov_b32 %7, 15\n.Lhint:\n s_load_dwordx4 s[72:75], %8, 0 glc\n s_load_dwordx4 s[76:79], %8, 128 glc\n s_load_dwordx4 s[80:83], %8, 256 glc\n"
                   "s_load_dwordx4 s[84:87], %8, 384 glc\n s_memrealtime s[88:89]\n s_waitcnt lgkmcnt(0)\n s_sub_u32 %0, s88, 0x800\n"
                   HG4(1, 72, 73, 74, 75) HG4(2, 76, 77, 78, 79) HG4(3, 80, 81, 82, 83) HG4(4, 84, 85, 86, 87)
                   "s_max_u32 %5, %1, %2\n s_max_u32 %6, %3, %4\n s_max_u32 s72, %5, %6\n s_cmpk_lt_u32 s72, 0xc00\n s_cbranch_scc1 .Lhdone\n"
                   "s_sleep %9\n s_sub_u32 %7, %7, 1\n s_cbranch_scc0 .Lhint\n"
                   "s_mov_b32 %1, 0\n s_mov_b32 %2, 0\n s_mov_b32 %3, 0\n s_mov_b32 %4, 0\n s_mov_b32 %5, 0\n s_mov_b32 %6, 0\n.Lhdone:"
                   : "=&s"(t0), "=&s"(rq[0]), "=&s"(rq[1]), "=&s"(rq[2]), "=&s"(rq[3]), "=&s"(m0), "=&s"(m1), "=&s"(nt) : "s"(hb), "i"(HKS)
                   : "memory", "scc", "s72", "s73", "s74", "s75", "s76", "s77", "s78", "s79", "s80", "s81", "s82", "s83", "s84", "s85", "s86", "s87", "s88", "s89");
      BST(16);
      const bool up = m0 > m1;
      const unsigned a0 = up ? rq[2] : rq[0], a1 = up ? rq[3] : rq[1], a2 = up ? rq[0] : rq[2], a3 = up ? rq[1] : rq[3], gu = up ? 2u : 0u;
      unsigned e0, e1, go;
      asm("s_cmp_gt_u32 %3, %4\n s_cselect_b32 %0, %6, %5\n s_cselect_b32 %1, %5, %6\n s_addc_u32 %2, %7, 0" : "=&s"(e0), "=&s"(e1), "=&s"(go) : "s"(a2), "s"(a3), "s"(a0), "s"(a1), "s"(gu) : "scc");
      dg = (u4){((t0 + 3u + e0) & ~3u) | go, t0 + e1, t0 + min(a2, a3), t0 + max(m0, m1)};
      asm volatile("ds_write_b128 %0, %1 offset:32\n ds_write_b32 %0, %2 offset:20" ::"v"(fla), "v"(dg), "v"(1u) : "memory");
      BST(15);
      now = NOW();
      WAITV(0);
      ST64(((u2){tag, ((int)(dg[3] - now) > 0 ? dg[3] : now) + od}), oho);
    } else {
      wo_upto(WA);
      BST(15);
      if (WA < WE) { lwait<2>(adFL, 0x0Fu, 3); if (WS0) SLEEP(WS0); wo_upto(WE); }
      STMP(1);
      __builtin_amdgcn_s_setprio(LP0);
      u4 v;
      _Pragma("unroll 1") for (unsigned it = 0; it < SPIN; ++it) {
        unsigned f;
        asm volatile("ds_read_b32 %0, %2 offset:20\n ds_read_b128 %1, %2 offset:32\n s_waitcnt lgkmcnt(0)" : "=&v"(f), "=&v"(v) : "v"(fla) : "memory");
        if (__builtin_amdgcn_readfirstlane(f)) break;
        SLEEP(2);
      }
#pragma unroll
      for (int e = 0; e < 4; ++e) dg[e] = __builtin_amdgcn_readfirstlane(v[e]);
      now = NOW();
    }
    unsigned gx = (dg[0] & 3u) * 8192u;
    asm volatile("" : "+s"(gx));
    __builtin_amdgcn_s_setprio(3);
    u4 pv[16][2], zv0, zv1, pq[8];
    const unsigned lv16 = l * 16;
    const unsigned pa = ldsb + L_PQ + lw * 4096 + l * 16, omask = lw ? 0x0Au : 0x25u;
    STMP(2);
#define GLD(gq) do { _Pragma("unroll") for (int i = 0; i < 4; ++i) { const char* sb = padd(kvb, (((gq) * 8192u + (i & 2) * 2048u) ^ gx)); \
      asm volatile("global_load_dwordx4 %0, %1, %2 offset:%3 sc1" : "=v"(pv[(gq) * 4 + i][0]) : "v"(lv16), "s"(sb), "i"((i & 1) * 2048) : "memory"); \
      asm volatile("global_load_dwordx4 %0, %1, %2 offset:%3 sc1" : "=v"(pv[(gq) * 4 + i][1]) : "v"(lv16), "s"(sb), "i"((i & 1) * 2048 + 1024) : "memory"); } } while (0)
#define ZLD() do { asm volatile("global_load_dwordx4 %0, %1, %2 sc1" : "=v"(zv0) : "v"(zoff), "s"(zb) : "memory"); \
      asm volatile("global_load_dwordx4 %0, %1, %2 offset:16 sc1" : "=v"(zv1) : "v"(zoff), "s"(zb) : "memory"); } while (0)
#pragma unroll
    for (int gq = 0; gq < 4; ++gq) {
      while ((int)(now - dg[gq]) < 0) now = NOW();
      if (gq == 3) ZLD();
      GLD(gq);
    }
    lsig(myFL, 3);
    STMP(5);
    unsigned ofo = WS_OF + (mb * 32 + hd) * 8192 + g4 * 512 + r15 * 16;
    unsigned ofp = ofo + 4096;
    f4 eps = (f4)(1e-6f);
    asm volatile("" : "+v"(ofo), "+v"(ofp), "+v"(eps), "+v"(sel[0]), "+v"(sel[1]));
    f4 gs[4][4]; float z[8];
#pragma unroll
    for (int gq = 0; gq < 4; ++gq) {
      if (gq == PW) {
        SBAR();
        lwait(adFL, omask, (l & 4) ? 3u : 4u);
#pragma unroll
        for (int i = 0; i < 8; ++i) RD128(pq[i], pa, (i >> 2) * 8192 + (i & 3) * 1024);
      }
      SBAR(); WAITV(gq == 0 ? 26 : gq == 1 ? 18 : gq == 2 ? 10 : 0);
      if (gq == 3) BST(17);
      auto gbad = [&]() {
#pragma unroll
        for (int i = 0; i < 4; ++i) asm volatile("" : "+v"(pv[gq * 4 + i][0]), "+v"(pv[gq * 4 + i][1]));
        unsigned m = umax3(pv[gq * 4][0][0], pv[gq * 4][1][0], pv[gq * 4 + 1][0][0]);
        m = umax3(m, pv[gq * 4 + 1][1][0], pv[gq * 4 + 2][0][0]);
        m = umax3(m, pv[gq * 4 + 2][1][0], pv[gq * 4 + 3][0][0]);
        m = max(m, pv[gq * 4 + 3][1][0]);
        if (gq == 3) { asm volatile("" : "+v"(zv0), "+v"(zv1)); m = umax3(m, zv0[0], zv1[0]); }
        return __ballot(m == POIS) != 0ull;
      };
      if (__builtin_expect(gbad(), 0)) {
        _Pragma("unroll 1") for (unsigned it = 0; it < SPIN; ++it) {
          RETRY();
          SLEEP(2);
          if (gq == 3) ZLD();
          GLD(gq);
          WAITV(0);
          if (!gbad()) break;
        }
      }
      if (gq == 3) {
#pragma unroll
        for (int e = 0; e < 4; ++e) { z[e] = __uint_as_float(zv0[e]); z[4 + e] = __uint_as_float(zv1[e]); }
        SBAR();
      }
#pragma unroll
      for (int i = 0; i < 4; ++i)
#pragma unroll
        for (int t = 0; t < 4; ++t) {
          gs[gq][t] = MFMA16(S8(sel[(t ^ (t >> 1)) & 1]), S8(pv[gq * 4 + i][t >> 1]), i ? gs[gq][t] : (f4)(0.f));
          if (gq == 3) {
            SBAR();
            if (i + t == 0) asm volatile("s_nop 1");
            asm volatile("v_add_f32_dpp %0, %0, %0 row_ror:%2 row_mask:0xf bank_mask:0xf\n v_add_f32_dpp %1, %1, %1 row_ror:%2 row_mask:0xf bank_mask:0xf" : "+v"(z[2 * t]), "+v"(z[2 * t + 1]) : "i"(8 >> i));
            SBAR();
          }
        }
      if (gq == 2) {
#pragma unroll
        for (int t = 0; t < 4; ++t) { gs[0][t] += gs[1][t]; asm volatile("" : "+v"(gs[0][t])); }
      }
    }
    STMP(3);
    WAITL(0);
#pragma unroll
    for (int i = 0; i < 8; i += 2) asm volatile("" : "+v"(pq[i]), "+v"(pq[i + 1]));
    u4 kA[2], zA;
#pragma unroll
    for (int e = 0; e < 4; ++e) zA[e] = pk2(z[2 * e], z[2 * e + 1]);
    SBAR();
#pragma unroll
    for (int t = 0; t < 4; ++t) gs[0][t] += gs[2][t] + gs[3][t];
    SBAR();
    f4 dn[8], o0[8], o1[8]; float inv[8];
#pragma unroll
    for (int i = 0; i < 8; ++i) {
      dn[i] = MFMA16(S8(zA), S8(pq[i]), eps);
      SBAR();
      kA[i >> 2][i & 3] = pk2(gs[0][i >> 1][(i & 1) * 2], gs[0][i >> 1][(i & 1) * 2 + 1]);
      SBAR();
    }
#pragma unroll
    for (int i = 0; i < 8; ++i) {
      o0[i] = MFMA16(S8(kA[0]), S8(pq[i]), (f4)(0.f)); SBAR();
      o1[i] = MFMA16(S8(kA[1]), S8(pq[i]), (f4)(0.f)); SBAR();
      inv[i] = __builtin_amdgcn_rcpf(dn[i][0]); asm volatile("" ::"v"(dn[i])); SBAR();
    }
    BST(18);
    u4 op = sel[0];
#pragma unroll
    for (int i = 0; i < 8; ++i) {
      o0[i] *= inv[i]; o1[i] *= inv[i];
      u4 o; o[0] = min(pk2(o0[i][0], o0[i][1]), POIS - 1u); o[1] = pk2(o0[i][2], o0[i][3]); o[2] = pk2(o1[i][0], o1[i][1]); o[3] = pk2(o1[i][2], o1[i][3]);
      asm volatile("" : "+v"(o) : "v"(op));
      ST128(o, (i & 4 ? ofp : ofo) + (i & 1) * 256 + (i & 2) * 1024);
      op = o;
      SBAR();
    }
    lsig(myFL, 4);
    STMP(4);
    STFLUSH();
    asm volatile(".Lend_l:");
  }

}

extern "C" void kernel_launch(void* const* d_in, const int* in_sizes, int n_in, void* d_out, int out_size,
                              void* d_ws, size_t ws_size, hipStream_t stream) {
  (void)hipFuncSetAttribute((const void*)fused, hipFuncAttributeMaxDynamicSharedMemorySize, LDS_SZ);
  fused<<<256, 512, LDS_SZ, stream>>>((const u16*)d_in[0], (const u16*)d_in[1], (const u16*)d_in[4], (char*)d_ws, (const u16*)d_in[2],
                                      (const u16*)d_in[5], (const u16*)d_in[6], (const u16*)d_in[7], (u16*)d_out);
}
```

```cpp
#define EVS 0
#define EVK 1
#define EVO 0u
#include <hip/hip_runtime.h>
#include <stdint.h>

typedef unsigned short u16;
typedef short s4 __attribute__((ext_vector_type(4)));
typedef short s8 __attribute__((ext_vector_type(8)));
typedef float f4 __attribute__((ext_vector_type(4)));
typedef float f16v __attribute__((ext_vector_type(16)));
typedef float f2 __attribute__((ext_vector_type(2)));
typedef unsigned u4 __attribute__((ext_vector_type(4)));
typedef unsigned u2 __attribute__((ext_vector_type(2)));
typedef unsigned u8v __attribute__((ext_vector_type(8)));
typedef __bf16 bf2 __attribute__((ext_vector_type(2)));
#define LAS __attribute__((address_space(3)))
#define DEV __device__ __forceinline__

constexpr int D = 1024, F = 3584;
#define NOMOE 0
#define KD 220
#define KS 214
#define OD 180
#define OHP 1
#define FS 128
#define GSL 8
#define YSL 16
#define TP1 0
#define TP2 3
#define TLBW 0
#define FULL 1
#define HPRIO 1
#define OWN 1
#define NOPOIS 0
#define CAP 7
#define RD 4
#define DYN 1
#define JX 0
#define NOSKIP 0
#define GRP 8
#define ADV 15
#define HB 1
#define RT 1
#define WD 0
#define WU1 12
#define WU2 22
#define PL 0
#define PO 0
#define WG 0
#define DS 0
#define D1 16
#define D2 2
#define HKS 2
#define LW1 1
#define HW1 2
#define HS0 0
#define GPV 2
#define XO 1
#define TP0 3
#define G1 0x31
#define G1S 4
#define G2S 8
#define TD 1
#define YD 170
#define WU0 4
#define WE 12
#define HT 115
#define CF 19
#define KL 145
#define OL 115
#define CL 0
#define WS0 4
#define OM 0
#define HG 128
#define U0 0
#define U3 5
#define TL 0
#define MH 0
#define HWT 3
#define YG 0
#define GG 1
#define PV 1
#define U1 4
#define U2 2
#define QD 1
#define LP0 3
#define LP1 3
#define WA 8
#define PW 3
#define WB 24
#define PF 1
#define PFS 6
#define PA 0
#define PAS 0
#define ROT 0

constexpr unsigned WS_E = 0;
constexpr unsigned WS_YF = 524288;
constexpr unsigned WS_CAND = 12288;
constexpr unsigned WS_RG = 49152;
constexpr unsigned WS_ST = 65536;
constexpr unsigned WS_HK = 589824;
constexpr unsigned WS_ZP = 131072;
constexpr unsigned WS_CS = 196608;
constexpr unsigned WS_OH = 458752;
constexpr unsigned WS_KVP = 1u << 20;
constexpr unsigned WS_GP = 2u << 20;
constexpr unsigned WS_YP = 3u << 20;
constexpr unsigned WS_OF = 5u << 20;
constexpr unsigned WS_SZ = 9u << 20;
#define NR 4
#define RGO(tag) ((64u << 20) + ((tag) & (NR - 1u)) * (10u << 20))
#define MKWS(tag) char* const wsx = ws + RGO(tag); const __amdgpu_buffer_rsrc_t wsr = __builtin_amdgcn_make_buffer_rsrc(wsx, 0, WS_SZ, 0x00020000)

constexpr int STG = 40960;
constexpr int L_KVX = 0;
constexpr int L_PQ = 12288;
constexpr int L_M = 28672, L_RS = L_M + 512, L_CS = L_RS + 512, L_SEL = L_CS + 768, L_FL = L_SEL + 64;
constexpr int L_HX = 30720, L_RL = 31744, L_WO = 32768;
constexpr int LDS_SZ = 163840;
constexpr unsigned POIS = 0xFFFFFFFFu;

DEV unsigned pk2(float a, float b) { bf2 v; v[0] = (__bf16)a; v[1] = (__bf16)b; return __builtin_bit_cast(unsigned, v); }
DEV float lo(unsigned u) { return __uint_as_float(u << 16); }
DEV float hi(unsigned u) { return __uint_as_float(u & 0xFFFF0000u); }
DEV float rbf(float a) { return lo(pk2(a, a)); }
DEV float dot2(unsigned a, unsigned b, float c) { return __builtin_amdgcn_fdot2_f32_bf16(__builtin_bit_cast(bf2, a), __builtin_bit_cast(bf2, b), c, false); }
#define RD128(dst, addr, OFF) asm volatile("ds_read_b128 %0, %1 offset:%2" : "=v"(dst) : "v"(addr), "i"(OFF) : "memory")
#define RDTR(dst, addr, OFF) asm volatile("ds_read_b64_tr_b16 %0, %1 offset:%2" : "=v"(dst) : "v"(addr), "i"(OFF) : "memory")
#define SBAR() __builtin_amdgcn_sched_barrier(0)
#define CAT(x, y) __builtin_shufflevector(x, y, 0, 1, 2, 3, 4, 5, 6, 7)
#define WAITV(n) asm volatile("s_waitcnt vmcnt(%0)" ::"i"(n) : "memory")
#define WAITL(n) asm volatile("s_waitcnt lgkmcnt(%0)" ::"i"(n) : "memory")
#define MEMB() asm volatile("" ::: "memory")
#define SLEEP(n) asm volatile("s_sleep %0" ::"i"(n) : "memory")
#define BAR() asm volatile("s_barrier" ::: "memory")
#define GL0(vo, sb, m0v) asm volatile("s_mov_b32 m0, %2\n s_nop 0\n global_load_lds_dwordx4 %0, %1" ::"v"(vo), "s"(sb), "s"(m0v) : "memory")
#define GLI(vo, sb, OFF) asm volatile("global_load_lds_dwordx4 %0, %1 offset:%2" ::"v"(vo), "s"(sb), "i"(OFF) : "memory")
#define GL4(v, sb, mm, MO) do { asm volatile("s_add_i32 m0, %2, %3\n s_nop 0\n global_load_lds_dwordx4 %0, %1" ::"v"((v)[0]), "s"(sb), "s"(mm), "i"(MO) : "memory", "scc"); \
      GLI((v)[1], sb, 1024); GLI((v)[2], sb, 2048); GLI((v)[3], sb, 3072); } while (0)
#define VFW(i) ((8 * (i) + (l >> 3)) * 2048 + (((l & 7) ^ (((l >> 4) & 1) << 2)) * 16) + (3 - (i)) * 1024)
#define MFMA16(a, b, c) __builtin_amdgcn_mfma_f32_16x16x32_bf16(a, b, c, 0, 0, 0)
#define S8(x) __builtin_bit_cast(s8, x)
#define LD128(off) __builtin_amdgcn_raw_buffer_load_b128(wsr, (off), 0, 16)
#define LD64(off) __builtin_amdgcn_raw_buffer_load_b64(wsr, (off), 0, 16)
#define ST128(v, off) __builtin_amdgcn_raw_buffer_store_b128((v), wsr, (off), 0, 16)
#define ST64(v, off) __builtin_amdgcn_raw_buffer_store_b64((v), wsr, (off), 0, 16)
#define OFA(i) (WS_OF + ((mb * 32 + hd) * 8 + (i)) * 1024 + l * 16)
#define POISON(i) ST128(pz, (i) < 2 ? WS_KVP + (hd * 16 + mb) * 2048 + (i) * 1024 + l * 16 : (i) == 2 ? WS_ZP + (hd * 16 + mb) * 128 + (l & 7) * 16 : OFA((i) - 3))
#define NOW() ((unsigned)__builtin_amdgcn_s_memrealtime())
constexpr unsigned SPIN = 1u << 21;
DEV const char* uptr(const char* p) {
  unsigned long long v = (unsigned long long)p;
  unsigned lo_ = __builtin_amdgcn_readfirstlane((unsigned)v), hi_ = __builtin_amdgcn_readfirstlane((unsigned)(v >> 32));
  return (const char*)(((unsigned long long)hi_ << 32) | lo_);
}
DEV const char* padd(const char* p, unsigned o) {
  unsigned lo_, hi_;
  asm("s_add_u32 %0, %2, %4\n s_addc_u32 %1, %3, 0" : "=&s"(lo_), "=s"(hi_) : "s"((unsigned)(unsigned long long)p), "s"((unsigned)((unsigned long long)p >> 32)), "s"(o) : "scc");
  return (const char*)(((unsigned long long)hi_ << 32) | lo_);
}
DEV void lsig(unsigned addr, unsigned val) { asm volatile("s_waitcnt lgkmcnt(0)\n ds_write_b32 %0, %1" ::"v"(addr), "v"(val) : "memory"); }
template <int SL = 0> DEV void lwait(unsigned addr, unsigned mask, unsigned target) {
  _Pragma("unroll 1") for (unsigned it = 0; it < SPIN; ++it) {
    unsigned v;
    asm volatile("ds_read_b32 %0, %1\n s_waitcnt lgkmcnt(0)" : "=v"(v) : "v"(addr) : "memory");
    if (((unsigned)__ballot(v < target) & mask) == 0u) break;
    if (SL) SLEEP(SL);
  }
}
DEV unsigned nopois(unsigned a) { return min(a, POIS - 1u); }
DEV u4 tagf(f4 v, unsigned tag) {
  u4 r;
#pragma unroll
  for (int e = 0; e < 4; ++e) r[e] = __builtin_amdgcn_perm(__float_as_uint(v[e]), tag, 0x07060500u + e);
  return r;
}
DEV void untag(u4& v, unsigned tag, unsigned& bad) {
#pragma unroll
  for (int e = 0; e < 4; ++e) v[e] ^= (tag >> (8 * e)) & 255u;
  bad |= v[0] | v[1]; bad |= v[2] | v[3];
}
DEV unsigned umax3(unsigned a, unsigned b, unsigned c) { return max(max(a, b), c); }
template <int C> DEV float dppf(float v) { return __int_as_float(__builtin_amdgcn_update_dpp(0, __float_as_int(v), C, 0xf, 0xf, false)); }
DEV unsigned wave_max(unsigned v) {
  v = max(v, (unsigned)__builtin_amdgcn_update_dpp(0, (int)v, 0x111, 0xf, 0xf, false));
  v = max(v, (unsigned)__builtin_amdgcn_update_dpp(0, (int)v, 0x112, 0xf, 0xf, false));
  v = max(v, (unsigned)__builtin_amdgcn_update_dpp(0, (int)v, 0x114, 0xf, 0xf, false));
  v = max(v, (unsigned)__builtin_amdgcn_update_dpp(0, (int)v, 0x118, 0xf, 0xf, false));
  v = max(v, (unsigned)__builtin_amdgcn_update_dpp(0, (int)v, 0x142, 0xa, 0xf, false));
  v = max(v, (unsigned)__builtin_amdgcn_update_dpp(0, (int)v, 0x143, 0xc, 0xf, false));
  return (unsigned)__builtin_amdgcn_readlane((int)v, 63);
}
DEV unsigned phi2(f2 u, f2 rs, f2 rl) {
  f2 a = u * rl, p = u * rs + 1.f, r;
  r[0] = __builtin_amdgcn_fmed3f(__builtin_amdgcn_exp2f(a[0]), p[0], 1.f);
  r[1] = __builtin_amdgcn_fmed3f(__builtin_amdgcn_exp2f(a[1]), p[1], 1.f);
  r += 1e-6f;
  return pk2(r[0], r[1]);
}

#define STDECL unsigned st_[16] = {0, 0, 0, 0, 0, 0, 0, 0, 0, 0, 0, 0, 0, 0, 0, 0}
#define STMP(i) st_[i] = (unsigned)__builtin_amdgcn_s_memrealtime()
#define BST(i)
#undef HB
#define HB 0
#define BSTV cw
#define RETRY()
#define RETRY2()
#define STFLUSH() do { if (w == 0) { const __amdgpu_buffer_rsrc_t dr_ = __builtin_amdgcn_make_buffer_rsrc(ws + (40u << 20), 0, 1u << 20, 0x00020000); \
  if (l == 0) __builtin_amdgcn_raw_buffer_store_b64(((u2){st_[0], st_[EVS]}), dr_, blockIdx.x * 128, 0, 16); \
  if (blockIdx.x == 0) { while (NOW() - st_[0] < 3500u) SLEEP(1); \
    const u2 a_ = __builtin_amdgcn_raw_buffer_load_b64(dr_, ((l & 31) * 8 + EVK) * 128, 0, 16); \
    float d_ = half_sum((float)(int)(a_[1] - st_[0] + 200u - EVO)) * (10.f / 32.f); \
    const unsigned v_ = min((unsigned)__builtin_amdgcn_readfirstlane((int)fmaxf(d_, 0.f)), 300000u); \
    _Pragma("unroll 1") for (unsigned it = 0; it < SPIN; ++it) { if (NOW() - st_[0] >= 4500u + v_) break; SLEEP(1); } } } } while (0)

DEV float half_sum(float v) {
  asm volatile("s_nop 1\n v_add_f32_dpp %0, %0, %0 row_ror:8 row_mask:0xf bank_mask:0xf\n s_nop 1\n v_add_f32_dpp %0, %0, %0 row_ror:4 row_mask:0xf bank_mask:0xf\n"
               "s_nop 1\n v_add_f32_dpp %0, %0, %0 row_ror:2 row_mask:0xf bank_mask:0xf\n s_nop 1\n v_add_f32_dpp %0, %0, %0 row_ror:1 row_mask:0xf bank_mask:0xf\n s_nop 1" : "+v"(v));
  auto r = __builtin_amdgcn_permlane16_swap(__float_as_uint(v), __float_as_uint(v), false, false);
  return __uint_as_float(r[0]) + __uint_as_float(r[1]);
}

struct FQ { s8 a[4]; s4 b[12]; };
template <bool B> struct BC { static constexpr bool v = B; };
template <int I> struct IC { static constexpr int v = I; };

__global__ __launch_bounds__(512) void fused(const u16* __restrict__ x, const u16* __restrict__ wqkv, const u16* __restrict__ wrt,
                                             char* __restrict__ ws_, const u16* __restrict__ wo_, const u16* __restrict__ wg,
                                             const u16* __restrict__ wu, const u16* __restrict__ wd, u16* __restrict__ out) {
  extern __shared__ __attribute__((aligned(16))) char lds[];
  const char *kx, *kq, *kr, *wo; char* ws;
  asm volatile("s_load_dwordx2 %0, %5, 0\n s_load_dwordx2 %1, %5, 8\n s_load_dwordx2 %2, %5, 16\n s_load_dwordx2 %3, %5, 24\n s_load_dwordx2 %4, %5, 32"
               : "=&s"(kx), "=&s"(kq), "=&s"(kr), "=&s"(ws), "=&s"(wo) : "s"(__builtin_amdgcn_kernarg_segment_ptr()));
  SBAR();
  const int w = __builtin_amdgcn_readfirstlane(threadIdx.x >> 6), l = threadIdx.x & 63;
  STDECL;
  STMP(0);
  const int g = blockIdx.x, xcd = g & 7, j = g >> 3, ii = j >> 2;
  const int mb = (xcd & 1) * 8 + ii, hp = (xcd >> 1) * 4 + (j & 3);
  unsigned cw, cwo = (l * 32 + j) * 64; unsigned long long cwb;
  asm volatile("s_getpc_b64 %2\n.Lcode_here:\n v_min_u32 %1, max(.Lend_c - .Lcode_here, .Lend_l - .Lcode_here, .Lend_h - .Lcode_here, .Lend_s - .Lcode_here) - 4, %1\n"
               "s_cmp_lg_u32 %3, 7\n s_cbranch_scc1 .Lskip_w\n global_load_dword %0, %1, %2\n.Lskip_w:" : "=&v"(cw), "+v"(cwo), "=&s"(cwb) : "s"(w) : "memory", "scc");
  SBAR();
  const int lw = w & 1, rot = 2 * xcd + 4 * ((ii + JX * j) & 3);
  const unsigned ldsb = (unsigned)(size_t)(LAS char*)lds;
  unsigned va[4];
#pragma unroll
  for (int i = 0; i < 4; ++i) va[i] = (8 * i + (l >> 3)) * 2048 + (((l & 7) ^ ((i & 1) * 4 + (l >> 4))) * 16) + (3 - i) * 1024;
  const int ao = (mb * 128 + lw * 64) * 2048 - 3072;
  long long ao0 = ao + (rot & 15) * 128;
  unsigned mba = ldsb + lw * 8192;
  const int srow = mb * 128 + hp * 8 + 2 * w + (l >> 5);
  unsigned xo = srow * 2048 + (l & 31) * 16, ro = (l & 31) * 16;
  asm volatile("" : "+v"(va[0]), "+v"(va[1]), "+v"(va[2]), "+v"(va[3]), "+v"(xo), "+v"(ro));
  asm volatile("s_waitcnt lgkmcnt(0)" : "+s"(kx), "+s"(kq), "+s"(kr), "+s"(ws), "+s"(wo), "+s"(ao0), "+s"(mba));
  SBAR();
  asm volatile("s_sub_u32 m0, %6, 4\n s_cmp_gt_u32 m0, 1\n s_cbranch_scc1 .Lskip_l\n s_mov_b32 m0, %5\n s_nop 0\n global_load_lds_dwordx4 %0, %4\n global_load_lds_dwordx4 %1, %4 offset:1024\n"
               "global_load_lds_dwordx4 %2, %4 offset:2048\n global_load_lds_dwordx4 %3, %4 offset:3072\n.Lskip_l:"
               :: "v"(va[0]), "v"(va[1]), "v"(va[2]), "v"(va[3]), "s"(kx + ao0), "s"(mba), "s"(w) : "memory", "scc");
  u4 xa[4], rw[4]; unsigned ep;
  asm volatile("s_cmp_gt_u32 %14, 3\n s_cbranch_scc1 .Lskip_x\n"
               "global_load_dwordx4 %0, %9, %11\n global_load_dwordx4 %1, %9, %11 offset:512\n global_load_dwordx4 %2, %9, %11 offset:1024\n global_load_dwordx4 %3, %9, %11 offset:1536\n"
               "global_load_dwordx4 %4, %10, %12\n global_load_dwordx4 %5, %10, %12 offset:512\n global_load_dwordx4 %6, %10, %12 offset:1024\n global_load_dwordx4 %7, %10, %12 offset:1536\n"
               ".Lskip_x:\n s_load_dword %8, %13, 0"
               : "=&v"(xa[0]), "=&v"(xa[1]), "=&v"(xa[2]), "=&v"(xa[3]), "=&v"(rw[0]), "=&v"(rw[1]), "=&v"(rw[2]), "=&v"(rw[3]), "=&s"(ep)
               : "v"(xo), "v"(ro), "s"(kx), "s"(kr), "s"(ws), "s"(w) : "memory", "scc");
#define GETTAG(t) unsigned t; asm volatile("s_waitcnt lgkmcnt(0)\n s_add_u32 %0, %1, 1" : "=s"(t) : "s"(ep) : "memory", "scc")
  SBAR();
#define PTAB 0xA98FEDC1045B7623ull
#define PHT 0x6624924999924924ull
#define CX 4
  const bool sp = (hp & 7) == 3;
  const int nb = hp;
#define PAIRAT(p) ((int)(PTAB >> (4 * (p))) & 15)
#define HEADAT(s) (2 * PAIRAT((s) >> 1) + ((s) & 1))
  const int ph = (int)(PHT >> (2 * j)) & 3;
#define HQT 0xDCBA098765320410ull
#define CXA 1
#define CHUNK(b) ((b) < 14 ? 4 * (b) + 3 : ((b) - 14) + ((((b) - 14) * 43) >> 7))
  const unsigned adFL = ldsb + L_FL + 4 * (l & 7), myFL = ldsb + L_FL + 4 * w;
  const __amdgpu_buffer_rsrc_t wse = __builtin_amdgcn_make_buffer_rsrc(ws, 0, 64, 0x00020000);
  const int r15 = l & 15, g4 = l >> 4, q = (l & 15) >> 2, p = l & 3;

  if (w != 4 && w != 5) {
  if (w >= 6) {
    const int mw = w - 6;
    unsigned pf = 0;
    if (PF && !(xcd & 2) && !sp && (mw == 0 || PF == 3 || (PF == 2 && (ii >> 2)))) {
      const unsigned po = (((rot + 2 - 2 * mw + (ii >> 2)) & 15) * 64 + l) * 6144 + (j & 3) * 2048 + (hp | 3) * 128;
      if (PFS) SLEEP(PFS);
      asm volatile("global_load_dword %0, %1, %2" : "+v"(pf) : "v"(po), "s"(kq) : "memory");
    }
    if (PA && mw == 1) {
      const unsigned po = (mb * 128 + (j & 3) * 32 + (l & 31)) * 2048 + (l >> 5) * 1024 + 384;
      if (PAS) SLEEP(PAS);
      asm volatile("global_load_dword %0, %1, %2" : "+v"(pf) : "v"(po), "s"(kx) : "memory");
    }
    GETTAG(tag);
    const int bq = ((int)(HQT >> (4 * hp)) & 15) * 4 + (ii & 3), ksp = (xcd & 1) * 2 + (ii >> 2);
    const int chunk = CHUNK(bq), bg = (CXA || bq < 14) ? bq ^ (w & 1) : bq, chg = CHUNK(bg);
    auto helper = [&](auto SP) {
    constexpr bool spare = decltype(SP)::v, moe = !spare && !NOMOE;
    int nbar = 0;
#define TICK() do { if (HB) { BST(nbar); asm volatile("" : "+v"(BSTV)); } SBAR(); BAR(); SBAR(); ++nbar; } while (0)
#define DTICK(lim) do { if (nbar < (lim)) TICK(); else SLEEP(2); } while (0)
    const int hd = hp * 2 + mw;
    const u4 pz = (u4)(POIS);
    MKWS(tag);
    const int np = l & 7, kset = (l >> 3) & 1;
    u4 gw[4][8], dw[2][8];
    const __amdgpu_buffer_rsrc_t wgr = __builtin_amdgcn_make_buffer_rsrc((void*)(mw ? wu : wg), 0, D * F * 2, 0x00020000);
    const __amdgpu_buffer_rsrc_t wdr = __builtin_amdgcn_make_buffer_rsrc((void*)wd, 0, D * F * 2, 0x00020000);
    const __amdgpu_buffer_rsrc_t wor = __builtin_amdgcn_make_buffer_rsrc((void*)wo, 0, D * D * 2, 0x00020000);
    const unsigned gvo = ((256 * ksp + 32 * kset + 8 * g4) * F + 64 * chg + 8 * np) * 2;
    const unsigned dvo = ((64 * chunk + 8 * g4) * D + 256 * ksp + 128 * mw + 8 * r15) * 2;
    auto burst = [&](int k, int i0 = 0, int i1 = 16) {
      if (moe) {
#pragma unroll
        for (int i = i0; i < i1; ++i) {
          if (k < 2) gw[(2 * k + (i >> 3)) & 3][i & 7] = __builtin_amdgcn_raw_buffer_load_b128(wgr, gvo, (128 * k + 64 * (i >> 3) + (i & 7)) * F * 2, 2);
          else dw[i >> 3][i & 7] = __builtin_amdgcn_raw_buffer_load_b128(wdr, dvo, (32 * (i >> 3) + (i & 7)) * D * 2, 2);
        }
      }
    };
    auto transp = [&](u4(&v)[8]) {
      if (moe) {
        u4 f[8];
#pragma unroll
        for (int c = 0; c < 8; ++c)
#pragma unroll
          for (int e = 0; e < 4; ++e) {
            const unsigned ev = v[2 * e][c >> 1], od = v[2 * e + 1][c >> 1];
            f[c][e] = (c & 1) ? __builtin_amdgcn_perm(od, ev, 0x07060302u) : __builtin_amdgcn_perm(od, ev, 0x05040100u);
          }
#pragma unroll
        for (int c = 0; c < 8; ++c) { v[c] = f[c]; asm volatile("" : "+v"(v[c])); }
      }
    };
    auto pois = [&]() {
      ST128(pz, WS_KVP + (hd * 16 + mb) * 2048 + l * 16); ST128(pz, WS_KVP + (hd * 16 + mb) * 2048 + 1024 + l * 16); ST128(pz, WS_ZP + (hd * 16 + mb) * 128 + (l & 7) * 16);
    };
    unsigned wt0, wt1, wt2 = 0, wt3 = 0;
    auto touch = [&]() {
      const unsigned to = (j * 32 + mw * 16 + (l >> 3)) * 2048 + (xcd >> 1) * 512 + (l & 7) * 64;
      wt0 = __builtin_amdgcn_raw_buffer_load_b32(wor, to, 0, 0); wt1 = __builtin_amdgcn_raw_buffer_load_b32(wor, to, 16384, 0);
      if (TLBW) {
        const __amdgpu_buffer_rsrc_t outr = __builtin_amdgcn_make_buffer_rsrc(out, 0, 2048 * D * 2, 0x00020000);
        wt2 = __builtin_amdgcn_raw_buffer_load_b32(outr, (mb * 128 + mw * 64 + (l & 3) * 16) * 2048 + nb * 128, 0, 0);
        wt3 = __builtin_amdgcn_raw_buffer_load_b32(wsr, (l & 1) ? WS_GP + g * 4096 + mw * 2048 : WS_YP + (g >> 2) * 32768 + (l & 2) * 8192, 0, 0);
      }
    };
    s4 sb[24];
    auto colrd = [&](int h) {
      const unsigned bo = ldsb + (1 + h) * STG + 16384 + (8 * g4 + q) * 128 + (p & 1) * 8;
      const int sw = ((q >> 1) << 2) | ((g4 & 1) << 1);
      const unsigned b0 = bo + (((mw * 4 + (p >> 1)) ^ sw) * 16), b1 = bo + (((mw * 4 + 2 + (p >> 1)) ^ sw) * 16);
#pragma unroll
      for (int n = 0; n < 12; ++n) { RDTR(sb[2 * n], (n & 1) ? b1 : b0, ((n % 6) >> 1) * 8192 + (n / 6) * 4096); RDTR(sb[2 * n + 1], (n & 1) ? b1 : b0, ((n % 6) >> 1) * 8192 + (n / 6) * 4096 + 512); }
      WAITL(0);
      SBAR();
    };
    auto colsum = [&]() {
      const u4 ones = (u4)(0x3F803F80u);
      f4 c[6];
#pragma unroll
      for (int n = 0; n < 12; ++n) c[n % 6] = MFMA16(S8(ones), CAT(sb[2 * n], sb[2 * n + 1]), n >= 6 ? c[n % 6] : (f4)(0.f));
      const u4 csg = (u4){__float_as_uint(g4 ? c[3][0] : c[0][0]), __float_as_uint(g4 ? c[4][0] : c[1][0]), __float_as_uint(g4 ? c[5][0] : c[2][0]), tag};
      WAITV(0);
      asm volatile("" : "+v"(cw) : "v"(pf));
      ST128(csg, l < 32 ? WS_CS + (hd * 16 + mb) * 512 + l * 16 : WS_SZ);
    };
    u4 stv, cv[8], cd; float cs[3];
    auto gload = [&]() {
      stv = LD128(WS_ST + (mb * 128 + mw * 64 + l) * 16);
#pragma unroll
      for (int i = 0; i < 8; ++i) cv[i] = LD128(WS_CS + hd * 8192 + i * 1024 + l * 16);
    };
    auto gbad = [&]() {
      unsigned bad = (stv[1] ^ tag) | (stv[3] ^ tag);
#pragma unroll
      for (int i = 0; i < 8; ++i) bad |= cv[i][3] ^ tag;
      return __ballot(bad != 0u) != 0ull;
    };
    int idx;
    auto gfin = [&]() {
      if (gbad()) { RETRY(); _Pragma("unroll 1") for (unsigned it = 0; it < SPIN; ++it) { SLEEP(2); gload(); if (!gbad()) break; } }
#pragma unroll
      for (int e = 0; e < 3; ++e) {
        float a = __uint_as_float(cv[0][e]);
#pragma unroll
        for (int i = 1; i < 8; ++i) a += __uint_as_float(cv[i][e]);
        auto r = __builtin_amdgcn_permlane32_swap(__float_as_uint(a), __float_as_uint(a), false, false);
        cs[e] = __uint_as_float(r[0]) + __uint_as_float(r[1]);
      }
      asm volatile("" : "+v"(cs[0]), "+v"(cs[1]), "+v"(cs[2]) : "v"(wt0), "v"(wt1), "v"(wt2), "v"(wt3));
    };
    auto cload = [&]() { cd = LD128(WS_CAND + (((hp >> 2) == 2) * 16 + mb) * 128 + (l & 7) * 16); };
    auto cfin = [&]() {
      if (__builtin_expect(__ballot(cd[1] != tag || cd[3] != tag) != 0ull, 0)) {
        RETRY();
        _Pragma("unroll 1") for (unsigned it = 0; it < SPIN; ++it) { SLEEP(4); cload(); if (__ballot(cd[1] != tag || cd[3] != tag) == 0ull) break; }
      }
      cd[0] = max(cd[0], cd[2]);
      idx = (int)(cd[0] & 2047u);
    };
    auto lpub = [&]() {
      *(LAS float*)(lds + L_M + (mw * 64 + l) * 4) = __uint_as_float(stv[0]);
      *(LAS float*)(lds + L_RS + (mw * 64 + l) * 4) = __uint_as_float(stv[2]);
      *(LAS float*)(lds + L_RL + (mw * 64 + l) * 4) = __uint_as_float(stv[2]) * 1.44269504f;
      if (l < 32) {
#pragma unroll
        for (int e = 0; e < 3; ++e) *(LAS float*)(lds + L_CS + mw * 384 + ((3 * g4 + e) * 16 + r15) * 4) = -cs[e];
      }
      if (mw == 0 && l < 8) *(LAS int*)(lds + L_SEL + l * 4) = idx;
      lsig(myFL, 1u);
    };
    u4 xs[4]; float xrs;
    asm volatile("" ::"s"(wg), "s"(wu), "s"(wd), "v"(cw));
    if (HPRIO) __builtin_amdgcn_s_setprio(HPRIO);
    TICK();
    if (spare) {
      TICK();
      pois();
      if (ii >> 2) TICK();
      colrd(ii >> 2);
      if (!(ii >> 2)) TICK();
      u4 kk[8];
      auto kload = [&]() {
#pragma unroll
        for (int i = 0; i < 8; ++i) kk[i] = LD128(WS_RG + mw * 8192 + i * 1024 + l * 16);
      };
      kload();
      TICK();
      _Pragma("unroll 1") for (unsigned it = 0; it < SPIN; ++it) {
        unsigned bad = 0;
#pragma unroll
        for (int i = 0; i < 8; ++i) { bad |= (kk[i][1] ^ tag) | (kk[i][3] ^ tag); asm volatile("" : "+v"(bad)); }
        if (__ballot(bad != 0u) == 0ull) break;
        RETRY();
        kload();
        DTICK(7);
      }
      __builtin_amdgcn_s_setprio(3);
      unsigned m1 = max(kk[0][0], kk[0][2]), m2 = min(kk[0][0], kk[0][2]), m3 = 0;
#pragma unroll
      for (int i = 2; i < 16; ++i) {
        const unsigned a = kk[i >> 1][(i & 1) * 2];
        asm("v_med3_u32 %0, %1, %2, %0" : "+v"(m3) : "v"(m2), "v"(a));
        asm("v_med3_u32 %0, %1, %2, %0" : "+v"(m2) : "v"(m1), "v"(a));
        m1 = max(m1, a);
      }
      unsigned mine8 = 0;
#pragma unroll
      for (int it = 0; it < 8; ++it) {
        const unsigned km = wave_max(m1);
        mine8 = l == it ? km : mine8;
        const bool win = m1 == km;
        if (it < 7) { m1 = win ? m2 : m1; m2 = win ? m3 : m2; m3 = win ? 0u : m3; }
        if (it == 3 && nbar < 7) TICK();
      }
      if (__builtin_expect(__ballot(m1 == 0u) != 0ull, 0)) {
        unsigned lim = 0;
#pragma unroll 1
        for (int it = 0; it < 8; ++it) {
          unsigned m = 0;
#pragma unroll
          for (int i = 0; i < 8; ++i) m = umax3(m, kk[i][0] - lim, kk[i][2] - lim);
          lim += wave_max(m);
          mine8 = l == it ? lim : mine8;
        }
      }
      ST64(((u2){mine8, tag}), l < 8 ? WS_CAND + ((hp >> 3) * 16 + mb) * 128 + (mw ? 7 - l : l) * 16 + mw * 8 : WS_SZ);
      __builtin_amdgcn_s_setprio(HPRIO);
      STMP(2);
      TICK();
      colsum();
      DTICK(9);
      touch();
      while (nbar < 10) TICK();
      gload(); cload();
      if (RT && mw) asm volatile("global_load_dword %0, %1, %2 sc1" : "+v"(cw) : "v"(cwo), "s"(cwb) : "memory");
      TICK(); TICK();
      WAITV(0);
      asm volatile("" : "+v"(cw));
      gfin(); cfin();
      STMP(3);
      TICK(); TICK(); TICK();
      lpub();
    } else {
      if (PV == 0) pois();
      TICK();
      if (PV == 1) pois();
      if (ii >> 2) TICK();
      colrd(ii >> 2);
      TICK();
      if (CL && !(ii >> 2)) TICK();
      colsum();
      if (!CL && !(ii >> 2)) TICK();
      u4 sv; f4 acc[8];
      auto xload = [&]() {
        const u16* xp = x + (size_t)idx * D + 256 * ksp + 32 * kset + 8 * g4;
        sv = LD128(WS_ST + idx * 16);
#pragma unroll
        for (int it = 0; it < 4; ++it) xs[it] = *(const u4*)(xp + 64 * it);
        SBAR();
      };
      auto xnorm = [&]() {
        if (__builtin_expect(__ballot(sv[1] != tag || sv[3] != tag) != 0ull, 0)) {
          _Pragma("unroll 1") for (unsigned it = 0; it < SPIN; ++it) { SLEEP(1); sv = LD128(WS_ST + idx * 16); if (__ballot(sv[1] != tag || sv[3] != tag) == 0ull) break; }
        }
        const float nm = -__uint_as_float(sv[0]);
        xrs = __uint_as_float(sv[2]);
#pragma unroll
        for (int it = 0; it < 4; ++it) {
          float t0, t1, t2, t3; unsigned x0 = xs[it][0], x1 = xs[it][1], x2 = xs[it][2], x3 = xs[it][3];
          asm volatile("v_dot2_f32_bf16 %4, %0, %8, %10\n v_dot2_f32_bf16 %0, %0, %9, %10\n v_dot2_f32_bf16 %5, %1, %8, %10\n v_dot2_f32_bf16 %1, %1, %9, %10\n"
                       "v_dot2_f32_bf16 %6, %2, %8, %10\n v_dot2_f32_bf16 %2, %2, %9, %10\n v_dot2_f32_bf16 %7, %3, %8, %10\n v_dot2_f32_bf16 %3, %3, %9, %10\n"
                       "v_cvt_pk_bf16_f32 %0, %4, %0\n v_cvt_pk_bf16_f32 %1, %5, %1\n v_cvt_pk_bf16_f32 %2, %6, %2\n s_nop 0\n v_cvt_pk_bf16_f32 %3, %7, %3"
                       : "+v"(x0), "+v"(x1), "+v"(x2), "+v"(x3), "=&v"(t0), "=&v"(t1), "=&v"(t2), "=&v"(t3) : "s"(0x00003F80u), "s"(0x3F800000u), "v"(nm));
          xs[it] = (u4){x0, x1, x2, x3};
        }
        asm volatile("s_nop 1" : "+v"(xs[0]), "+v"(xs[1]), "+v"(xs[2]), "+v"(xs[3]), "+v"(xrs));
        SBAR();
      };
      auto gmul = [&](int i0, int i1) {
#pragma unroll
        for (int it = i0; it < i1; ++it)
#pragma unroll
          for (int c = 0; c < 8; ++c) acc[c] = MFMA16(S8(gw[it][c]), S8(xs[it]), it ? acc[c] : (f4)(0.f));
#pragma unroll
        for (int c = 0; c < 8; ++c) asm volatile("" : "+v"(acc[c]));
        SBAR();
      };
      auto gout = [&]() {
        unsigned go = r15 < 8 ? WS_GP + ((chg * 4 + ksp) * 2 + mw) * 2048 + (g4 >> 1) * 1024 + (g4 & 1) * 512 + r15 * 16 : WS_SZ;
        asm volatile("" : "+v"(go));
#pragma unroll
        for (int i = 0; i < 4; ++i) {
          unsigned a[4], b[4];
#pragma unroll
          for (int c = 0; c < 4; ++c) {
            auto r = __builtin_amdgcn_permlane32_swap(__float_as_uint(acc[c][i]), __float_as_uint(acc[c + 4][i]), false, false);
            a[c] = r[0]; b[c] = r[1];
          }
          unsigned s0, s1, s2, s3;
#define AR(d, x, y) "v_add_f32_dpp %" #d ", %" #x ", %" #y " row_ror:8 row_mask:0xf bank_mask:0xf\n"
          asm volatile("s_nop 1\n" AR(0, 8, 4) AR(1, 9, 5) AR(2, 10, 6) AR(3, 11, 7) : "=&v"(s0), "=&v"(s1), "=&v"(s2), "=&v"(s3)
                       : "v"(a[0]), "v"(a[1]), "v"(a[2]), "v"(a[3]), "v"(b[0]), "v"(b[1]), "v"(b[2]), "v"(b[3]));
          ST128(tagf(__builtin_bit_cast(f4, ((u4){s0, s1, s2, s3})), tag), go + i * 128);
        }
        SBAR();
      };
      for (int i = 0; i < ph; ++i) TICK();
      burst(0); touch();
      TICK();
      TICK();
      if (TL) { transp(gw[0]); transp(gw[1]); }
      TICK();
      if (!TL) transp(gw[0]);
      TICK();
      if (!TL) transp(gw[1]);
      TICK();
      if (ph) cload();
      if (RT && mw) asm volatile("global_load_dword %0, %1, %2 sc1" : "+v"(cw) : "v"(cwo), "s"(cwb) : "memory");
      SBAR();
      burst(1);
      TICK();
      TICK(); TICK();
      transp(gw[2]);
      WAITV(0);
      asm volatile("" : "+v"(cw));
      if (ph < 2) TICK();
      transp(gw[3]);
      if (ph < 1) { SBAR(); cload(); TICK(); }
      SBAR();
      if (XO == 0) { cfin(); xload(); gload(); } else { gload(); SBAR(); cfin(); xload(); }
      TICK();
      gfin();
      lpub();
      STMP(12);
      SBAR();
      __builtin_amdgcn_s_setprio(0);
      burst(2, 0, D1);
      STMP(3);
      SBAR();
      if (HS0) SLEEP(HS0);
      if (G1 & 1) { lwait<HW1>(adFL, 0x0Fu, G1 >> 4); if (G1S) SLEEP(G1S); __builtin_amdgcn_s_setprio(TP0); }
      xnorm();
      if (G1 & 2) { lwait<2>(adFL, 0x0Fu, G1 >> 4); if (G1S) SLEEP(G1S); __builtin_amdgcn_s_setprio(TP0); }
      STMP(5);
      gmul(0, 4);
      gout();
      STMP(6);
      SBAR();
    }
    __builtin_amdgcn_s_setprio(TP1);
    STMP(4);
    if (moe) {
      if (D2 == 0) { burst(2, D1, 16); SBAR(); }
      if (D1 >= 8 || D2 == 0) transp(dw[0]); if (D1 == 16 || D2 == 0) transp(dw[1]);
      SBAR();
      lwait<2>(adFL, 0x30u, 3);
      if (G2S) SLEEP(G2S);
      STMP(7);
      if (D2 == 1) { burst(2, D1, 16); SBAR(); }
      f4 acc[8];
      u4 ha[2];
      {
        u4 pg[4][2];
        auto pload = [&]() {
#pragma unroll
          for (int ks = 0; ks < 4; ++ks)
#pragma unroll
            for (int t = 0; t < 2; ++t) pg[ks][t] = LD128(WS_GP + ((chunk * 4 + ks) * 4 + 2 * t + mw) * 1024 + (4 * kset + g4) * 128 + np * 16);
        };
        auto pbad = [&]() {
          unsigned bad = 0;
#pragma unroll
          for (int i = 0; i < 8; ++i) untag(pg[i >> 1][i & 1], tag, bad);
          return __ballot((bad & 255u) != 0u) != 0ull;
        };
        pload();
        SBAR();
        if (D2 == 2) { burst(2, D1, 16); SBAR(); }
        __builtin_amdgcn_s_setprio(TP2);
        if (D1 < 8 && D2 == 1) transp(dw[0]);
        if (D1 < 16 && D2 == 1) transp(dw[1]);
        SBAR();
        if (__builtin_expect(pbad(), 0)) { _Pragma("unroll 1") for (unsigned it = 0; it < SPIN; ++it) { RETRY2(); SLEEP(4); pload(); if (!pbad()) break; } }
        STMP(10);
        f4 gq[2];
#pragma unroll
        for (int t = 0; t < 2; ++t)
          gq[t] = ((__builtin_bit_cast(f4, pg[0][t]) + __builtin_bit_cast(f4, pg[1][t])) + (__builtin_bit_cast(f4, pg[2][t]) + __builtin_bit_cast(f4, pg[3][t]))) * xrs;
        float hv[4];
#pragma unroll
        for (int e = 0; e < 4; ++e) {
          const float gg = rbf(gq[0][e]), uu = rbf(gq[1][e]);
          hv[e] = rbf(gg / (1.f + __expf(-gg))) * uu;
        }
        *(LAS u2*)(lds + L_HX + l * 16 + mw * 8) = (u2){pk2(hv[0], hv[1]), pk2(hv[2], hv[3])};
        lsig(myFL, 2u);
        if (D1 < 16 && D2 == 2) { SBAR(); if (D1 < 8) transp(dw[0]); transp(dw[1]); SBAR(); }
        lwait(adFL, 0xC0u, 2);
        ha[0] = *(const LAS u4*)(lds + L_HX + l * 16); ha[1] = *(const LAS u4*)(lds + L_HX + (l ^ 8) * 16);
      }
#pragma unroll
      for (int it = 0; it < 2; ++it)
#pragma unroll
        for (int c = 0; c < 8; ++c) acc[c] = MFMA16(S8(ha[it]), S8(dw[it][c]), it ? acc[c] : (f4)(0.f));
      if (g4 < 2) {
#pragma unroll
        for (int i = 0; i < 4; ++i) {
          const unsigned yo = WS_YP + ((chunk * 8 + 4 * g4 + i) * D + 256 * ksp + 128 * mw + 4 * r15) * 4;
          ST128(tagf((f4){acc[0][i], acc[1][i], acc[2][i], acc[3][i]}, tag), yo);
          ST128(tagf((f4){acc[4][i], acc[5][i], acc[6][i], acc[7][i]}, tag), yo + 256);
        }
      }
      STMP(11);
    }
    float sg;
    {
      const unsigned mono = cd[0] >> 16, rb = (mono & 0x8000u) ? (mono & 0x7FFFu) : (~mono & 0xFFFFu);
      sg = rbf(1.f / (1.f + __expf(-lo(rb))));
    }
    unsigned mine = (unsigned)__ballot(!NOMOE && l < 8 && (idx >> 7) == mb && (l & 1) == mw);
    if (mine) {
      if (spare) lwait<4>(adFL, 0x30u, 3);
      {
        const u2 f = LD64(WS_OH + (mb * 16 + r15) * 128);
        const unsigned t0 = NOW();
        unsigned rel = f[1] + YD - t0;
        rel = wave_max(f[0] == tag && rel < 600u ? rel : 0u);
        _Pragma("unroll 1") for (unsigned it = 0; it < SPIN; ++it) { if (NOW() - t0 >= rel) break; SLEEP(2); }
      }
      STMP(8);
      lwait<1>(adFL, 0x0Fu, 4);
      __builtin_amdgcn_s_setprio(TP2);
      while (mine) {
        const int t = __builtin_ctz(mine); mine &= mine - 1;
        u4 yv[14];
        _Pragma("unroll 1") for (unsigned it = 0; it < SPIN; ++it) {
          unsigned bad = 0;
#pragma unroll
          for (int i = 0; i < 14; ++i) yv[i] = LD128(WS_YP + (((i * 4 + g4) * 8 + t) * D + (nb >> 1) * 128 + (r15 >> 3) * 64 + (nb & 1) * 32 + (r15 & 7) * 4) * 4);
          SBAR();
#pragma unroll
          for (int i = 0; i < 14; ++i) untag(yv[i], tag, bad);
          if (__ballot((bad & 255u) != 0u) == 0ull) break;
          RETRY();
          SLEEP(YSL);
        }
        f4 y = __builtin_bit_cast(f4, yv[0]);
#pragma unroll
        for (int i = 1; i < 14; ++i) y += __builtin_bit_cast(f4, yv[i]);
        const float gt = __int_as_float(__builtin_amdgcn_readlane(__float_as_int(sg), t));
#pragma unroll
        for (int e = 0; e < 4; ++e) {
          auto r = __builtin_amdgcn_permlane32_swap(__float_as_uint(y[e]), __float_as_uint(y[e]), false, false);
          const float a = __uint_as_float(r[0]) + __uint_as_float(r[1]);
          auto r2 = __builtin_amdgcn_permlane16_swap(__float_as_uint(a), __float_as_uint(a), false, false);
          y[e] = rbf(rbf(__uint_as_float(r2[0]) + __uint_as_float(r2[1])) * gt);
        }
        if (l < 16) *(LAS f4*)(lds + L_KVX + t * 256 + (l & 7) * 32 + (l >> 3) * 16) = y;
      }
    }
    lsig(myFL, 3u);
    STMP(9);
    STFLUSH();
    if (spare) asm volatile(".Lend_s:"); else asm volatile(".Lend_h:");
    };
    if (sp) helper(BC<true>()); else helper(BC<false>());
    return;
  }

  const int wr = w >> 1, wc = w & 1;
  u2 kg; u4 sg4; unsigned tag;
  {
    const int row = srow;
    SBAR(); BST(0); SBAR();
    asm volatile("s_waitcnt vmcnt(4)" : "+v"(xa[0]), "+v"(xa[1]), "+v"(xa[2]), "+v"(xa[3]));
    SBAR(); BST(1); SBAR();
    float s0 = 0, s1 = 0;
#pragma unroll
    for (int c = 0; c < 4; ++c)
#pragma unroll
      for (int e = 0; e < 4; e += 2) { unsigned x0 = xa[c][e], x1 = xa[c][e + 1]; s0 = dot2(x0, 0x3F803F80u, s0); s1 = dot2(x1, 0x3F803F80u, s1); }
    const float m = half_sum(s0 + s1) * (1.f / D);
    f2 v[16], q2 = (f2)(0.f), q3 = (f2)(0.f);
#pragma unroll
    for (int c = 0; c < 16; ++c) v[c] = (f2){lo(xa[c >> 2][c & 3]), hi(xa[c >> 2][c & 3])} - m;
#pragma unroll
    for (int c = 0; c < 16; c += 2) { q2 += v[c] * v[c]; q3 += v[c + 1] * v[c + 1]; }
    q2 += q3;
    const float mr = rbf(m);
    const float rs = 1.f / rbf(sqrtf(rbf(rbf(half_sum(q2[0] + q2[1]) * (1.f / D)) + 1e-5f)));
    float d0 = 0, d1 = 0;
    asm volatile("s_waitcnt vmcnt(0)" : "+v"(rw[0]), "+v"(rw[1]), "+v"(rw[2]), "+v"(rw[3]), "+v"(d0) : "v"(rs));
#pragma unroll
    for (int c = 0; c < 16; c += 2) {
      const f2 e0 = (f2){lo(xa[c >> 2][c & 3]), hi(xa[c >> 2][c & 3])} - mr, e1 = (f2){lo(xa[c >> 2][(c & 3) + 1]), hi(xa[c >> 2][(c & 3) + 1])} - mr;
      const unsigned p0 = pk2(e0[0], e0[1]), p1 = pk2(e1[0], e1[1]);
      const f2 n0 = (f2){lo(p0), hi(p0)} * rs, n1 = (f2){lo(p1), hi(p1)} * rs;
      unsigned c0 = rw[c >> 2][c & 3], c1 = rw[c >> 2][(c & 3) + 1];
      d0 = dot2(pk2(n0[0], n0[1]), c0, d0); d1 = dot2(pk2(n1[0], n1[1]), c1, d1);
    }
    const float d = half_sum(d0 + d1);
    const unsigned rb = pk2(d, d) & 0xFFFFu;
    const unsigned mono = (rb & 0x8000u) ? (~rb & 0xFFFFu) : (rb | 0x8000u);
    SBAR();
    GETTAG(tg); tag = tg;
    kg = (u2){(mono << 16) | (unsigned)row, tag};
    sg4 = (u4){__float_as_uint(mr), tag, __float_as_uint(rs), tag};
  }
  MKWS(tag);
  SBAR(); BST(2); SBAR();
  if ((l & 31) == 0) { ST64(kg, WS_RG + srow * 8); ST128(sg4, WS_ST + srow * 16); }
  unsigned aad[2], bad[2];
#pragma unroll
  for (int s = 0; s < 2; ++s) aad[s] = ldsb + (wr * 64 + r15) * 128 + (((4 * s + g4) ^ ((r15 >> 1) & 7)) * 16);
#pragma unroll
  for (int nh = 0; nh < 2; ++nh) bad[nh] = ldsb + 16384 + (8 * g4 + q) * 128 + (((wc * 4 + 2 * (nh ^ wr) + (p >> 1)) ^ (((q >> 1) << 2) | ((g4 & 1) << 1))) * 16) + (p & 1) * 8;
  f4 acc[4][6];
#pragma unroll
  for (int m = 0; m < 4; ++m)
#pragma unroll
    for (int n = 0; n < 6; ++n) { acc[m][n] = (f4)(0.f); asm volatile("" : "+v"(acc[m][n])); }
#define MF(m, n, Fq) do { if (n < 2) acc[m][n] = MFMA16(CAT(Fq.b[2 * n], Fq.b[2 * n + 1]), Fq.a[m], acc[m][n]); \
     else acc[m][n] = MFMA16(Fq.a[m], CAT(Fq.b[2 * n], Fq.b[2 * n + 1]), acc[m][n]); SBAR(); } while (0)
#define RB(G, n, vb0, vb1, SS, hf) RDTR(G.b[2 * (n) + (hf)], (((n) & 1) ? vb1 : vb0), ((n) >> 1) * 8192 + (SS) * 4096 + (hf) * 512)
#define RA(G, m, va) RD128(G.a[m], va, (m) * 2048)
#define SUB(Fq, G, va, vb0, vb1, SS, PRE) do { \
    WAITL(13); SBAR(); \
    MF(0, 0, Fq); PRE; RA(G, 0, va); SBAR(); \
    WAITL(11); SBAR(); \
    MF(1, 0, Fq); RB(G, 0, vb0, vb1, SS, 0); SBAR(); MF(2, 0, Fq); RB(G, 0, vb0, vb1, SS, 1); SBAR(); MF(3, 0, Fq); RA(G, 1, va); SBAR(); \
    WAITL(12); SBAR(); \
    MF(0, 1, Fq); RA(G, 2, va); SBAR(); MF(1, 1, Fq); RA(G, 3, va); SBAR(); MF(2, 1, Fq); RB(G, 1, vb0, vb1, SS, 0); SBAR(); MF(3, 1, Fq); RB(G, 1, vb0, vb1, SS, 1); SBAR(); \
    WAITL(8); SBAR(); \
    MF(0, 2, Fq); RB(G, 2, vb0, vb1, SS, 0); SBAR(); MF(1, 2, Fq); RB(G, 2, vb0, vb1, SS, 1); SBAR(); MF(2, 2, Fq); RB(G, 3, vb0, vb1, SS, 0); SBAR(); MF(3, 2, Fq); RB(G, 3, vb0, vb1, SS, 1); SBAR(); \
    MF(0, 3, Fq); RB(G, 4, vb0, vb1, SS, 0); SBAR(); MF(1, 3, Fq); RB(G, 4, vb0, vb1, SS, 1); SBAR(); MF(2, 3, Fq); RB(G, 5, vb0, vb1, SS, 0); SBAR(); MF(3, 3, Fq); RB(G, 5, vb0, vb1, SS, 1); SBAR(); \
    MF(0, 4, Fq); MF(1, 4, Fq); MF(2, 4, Fq); MF(3, 4, Fq); MF(0, 5, Fq); MF(1, 5, Fq); MF(2, 5, Fq); MF(3, 5, Fq); } while (0)
#define KSTEP(MODE) do { \
    if (kt == sx) { const unsigned xo = xad + buf * STG; \
      _Pragma("unroll") for (int t = 0; t < 8; ++t) { const unsigned a_ = xo ^ (t * 16); \
        asm volatile("ds_read_b64 %0, %1" : "+v"(xv[t]) : "v"(a_) : "memory"); } \
      WAITL(0); SBAR(); } \
    const int nbuf = (buf + 1) & 3; \
    const unsigned bo = buf * STG, nbo = nbuf * STG; \
    { unsigned va = aad[1] + bo, vb0 = bad[0] + bo, vb1 = bad[1] + bo; SUB(f0, f1, va, vb0, vb1, 1, ); } \
    if (MODE < 2) { \
      unsigned va = aad[0] + nbo, vb0 = bad[0] + nbo, vb1 = bad[1] + nbo; \
      SUB(f1, f0, va, vb0, vb1, 0, if (MODE == 0) { WAITL(0); BAR(); } else asm volatile("ds_write_b32 %0, %1" ::"v"(myFL), "v"(1u) : "memory"); SBAR()); \
    } else { asm volatile("ds_write_b32 %1, %2\n ds_read_b32 %0, %3\n s_waitcnt lgkmcnt(2)" : "=&v"(fl0) : "v"(myFL), "v"(2u), "v"(adFL) : "memory"); SBAR(); \
      MF(0, 2, f1); MF(1, 2, f1); MF(2, 2, f1); MF(3, 2, f1); MF(0, 3, f1); MF(1, 3, f1); MF(2, 3, f1); MF(3, 3, f1); \
      MF(0, 4, f1); MF(1, 4, f1); MF(2, 4, f1); MF(3, 4, f1); MF(0, 5, f1); MF(1, 5, f1); MF(2, 5, f1); MF(3, 5, f1); \
      MF(0, 0, f1); MF(1, 0, f1); MF(2, 0, f1); MF(3, 0, f1); MF(0, 1, f1); MF(1, 1, f1); MF(2, 1, f1); MF(3, 1, f1); } \
    buf = nbuf; } while (0)
#define SUBK(Fq, G, va, vb0, vb1, SS, W0, PRE) do { \
    WAITL(W0); SBAR(); \
    MF(0, 2, Fq); PRE; RA(G, 0, va); SBAR(); \
    WAITL(7); SBAR(); \
    MF(1, 2, Fq); RB(G, 2, vb0, vb1, SS, 0); SBAR(); MF(2, 2, Fq); RB(G, 2, vb0, vb1, SS, 1); SBAR(); MF(3, 2, Fq); RA(G, 1, va); SBAR(); \
    WAITL(8); SBAR(); \
    MF(0, 3, Fq); RA(G, 2, va); SBAR(); MF(1, 3, Fq); RA(G, 3, va); SBAR(); MF(2, 3, Fq); RB(G, 3, vb0, vb1, SS, 0); SBAR(); MF(3, 3, Fq); RB(G, 3, vb0, vb1, SS, 1); SBAR(); \
    WAITL(10); SBAR(); \
    MF(0, 4, Fq); RB(G, 4, vb0, vb1, SS, 0); SBAR(); MF(1, 4, Fq); RB(G, 4, vb0, vb1, SS, 1); SBAR(); MF(2, 4, Fq); RB(G, 5, vb0, vb1, SS, 0); SBAR(); MF(3, 4, Fq); RB(G, 5, vb0, vb1, SS, 1); SBAR(); \
    WAITL(12); SBAR(); \
    MF(0, 5, Fq); MF(1, 5, Fq); MF(2, 5, Fq); MF(3, 5, Fq); } while (0)
#define RQ(G, va, vb0, vb1, SS) do { RA(G, 0, va); RB(G, 0, vb0, vb1, SS, 0); RB(G, 0, vb0, vb1, SS, 1); RA(G, 1, va); RA(G, 2, va); RA(G, 3, va); \
    RB(G, 1, vb0, vb1, SS, 0); RB(G, 1, vb0, vb1, SS, 1); SBAR(); } while (0)
#define MQ(Fq) do { MF(0, 0, Fq); MF(1, 0, Fq); MF(2, 0, Fq); MF(3, 0, Fq); MF(0, 1, Fq); MF(1, 1, Fq); MF(2, 1, Fq); MF(3, 1, Fq); } while (0)
  u2 xv[8];
#pragma unroll
  for (int t = 0; t < 8; ++t) asm volatile("" : "=v"(xv[t]));
  const int sx = (nb - rot) & 15;
  const unsigned xad = ldsb + (w * 32 + (l & 31)) * 128 + ((r15 >> 1) & 7) * 16 + (l >> 5) * 8;
  STMP(1);
  WAITL(0);
  BAR();
  STMP(2);
  WAITL(0);
  FQ f0, f1;
  { unsigned va = aad[0], vb0 = bad[0], vb1 = bad[1];
    RA(f0, 0, va); RB(f0, 0, vb0, vb1, 0, 0); RB(f0, 0, vb0, vb1, 0, 1); RA(f0, 1, va); RA(f0, 2, va); RA(f0, 3, va);
    RB(f0, 1, vb0, vb1, 0, 0); RB(f0, 1, vb0, vb1, 0, 1); RB(f0, 2, vb0, vb1, 0, 0); RB(f0, 2, vb0, vb1, 0, 1); RB(f0, 3, vb0, vb1, 0, 0); RB(f0, 3, vb0, vb1, 0, 1);
    RB(f0, 4, vb0, vb1, 0, 0); RB(f0, 4, vb0, vb1, 0, 1); RB(f0, 5, vb0, vb1, 0, 0); RB(f0, 5, vb0, vb1, 0, 1); SBAR(); }
  unsigned sad = ldsb + L_M + wr * 256 + g4 * 16, cad = ldsb + L_CS + wc * 384 + r15 * 4 + wr * 64;
  const int hd = hp * 2 + wc;
  unsigned kvo = WS_KVP + (hd * 16 + mb) * 2048 + wr * 1024 + l * 16, zo = r15 == 0 ? WS_ZP + (hd * 16 + mb) * 128 + g4 * 32 + wr * 16 : WS_SZ;
  unsigned xw = ldsb + L_KVX + w * 3072 + l * 16, xr = ldsb + L_KVX + (w ^ 2) * 3072 + l * 16;
  asm volatile("s_waitcnt lgkmcnt(0)" : "+v"(sad), "+v"(cad), "+v"(kvo), "+v"(zo), "+v"(xw), "+v"(xr) : : "memory"); SBAR();
  int buf = 0; unsigned fl0;
#pragma unroll 1
  for (int kt = 0; kt < 14; ++kt) KSTEP(0);
  STMP(14); SBAR();
  f4 sm[4], sr[4], sl[4]; f2 ck, cv;
  unsigned c2 = 2u, c3 = 3u, vfl = myFL, b30, b31;
  u4 ones = (u4)(0x3F803F80u);
#define SC0() asm volatile("ds_read_b32 %0, %2 offset:128\n ds_read_b32 %1, %2 offset:256" : "=&v"(ck[0]), "=&v"(cv[0]) : "v"(cad) : "memory")
#define SC1() asm volatile("ds_read_b32 %0, %2 offset:128\n ds_read_b32 %1, %2 offset:256" : "=&v"(ck[1]), "=&v"(cv[1]) : "v"(cad ^ 64) : "memory")
  const unsigned b20 = bad[0] + 2 * STG, b21 = bad[1] + 2 * STG;
#define SM(m) do { RD128(sm[m], sad, (m) * 64); RD128(sl[m], sad, L_RL - L_M + (m) * 64); RD128(sr[m], sad, L_RS - L_M + (m) * 64); } while (0)
  {
    { const unsigned va = aad[1] + 2 * STG; SUBK(f0, f1, va, b20, b21, 1, 6, asm volatile("" ::"v"(f0.b[0]), "v"(f0.b[1]), "v"(f0.b[2]), "v"(f0.b[3]))); }
    { const unsigned va = aad[0] + 3 * STG; b30 = bad[0] + 3 * STG; b31 = bad[1] + 3 * STG; SUBK(f1, f0, va, b30, b31, 0, 9, asm volatile("" : "+v"(c2), "+v"(c3), "+v"(vfl))); }
    STMP(7); SBAR();
    { const unsigned va = aad[1] + 3 * STG; SUBK(f0, f1, va, b30, b31, 1, 9, asm volatile("" : "+v"(ones))); }
    asm volatile("ds_write_b32 %1, %2\n ds_read_b32 %0, %3\n s_waitcnt lgkmcnt(8)" : "=&v"(fl0) : "v"(vfl), "v"(c2), "v"(adFL) : "memory"); SBAR();
    MF(0, 2, f1); SC0(); SBAR(); MF(1, 2, f1); SC1(); SBAR(); MF(2, 2, f1); MF(3, 2, f1); WAITL(10); SBAR();
    MF(0, 3, f1); SM(0); SBAR(); MF(1, 3, f1); MF(2, 3, f1); SM(1); SBAR(); MF(3, 3, f1);
    WAITL(12); SBAR();
    MF(0, 4, f1); SM(2); SBAR(); MF(1, 4, f1); MF(2, 4, f1); SM(3); SBAR(); MF(3, 4, f1); MF(0, 5, f1); MF(1, 5, f1); MF(2, 5, f1); MF(3, 5, f1);
  }
  asm volatile("s_waitcnt lgkmcnt(0)" : "+v"(fl0) : : "memory");
  if (__builtin_expect(((unsigned)__ballot(fl0 < 1u) & 0xC0u) != 0u, 0)) {
    lwait(adFL, 0xC0u, 1);
    SC0(); SC1(); SM(0); SM(1); SM(2); SM(3); WAITL(0);
  }
  asm volatile("" : "+v"(ck), "+v"(cv), "+v"(sm[0]), "+v"(sm[1]), "+v"(sm[2]), "+v"(sm[3]), "+v"(sr[0]), "+v"(sr[1]), "+v"(sr[2]), "+v"(sr[3]));
  asm volatile("" : "+v"(sl[0]), "+v"(sl[1]), "+v"(sl[2]), "+v"(sl[3]));
  STMP(3);
  __builtin_amdgcn_s_setprio(2);
  {
    f4 kv[4], zt[2];
    u4 kf[2][2], vf[2][2];
#define MKV(t, i) do { if ((i) == 2 || (i) == 5) zt[(i) == 2] = MFMA16(S8(kf[t][(i) == 2]), S8(ones), (t) ? zt[(i) == 2] : (f4)(0.f)); \
      else kv[((i) + 2) % 5] = MFMA16(S8(kf[t][(((i) + 2) % 5) & 1]), S8(vf[t][(((i) + 2) % 5) >> 1]), (t) ? kv[((i) + 2) % 5] : (f4)(0.f)); SBAR(); } while (0)
#pragma unroll
    for (int m = 0; m < 4; ++m) {
      const int t = m >> 1, mm = m & 1;
      f4 ea[2], pp[2];
#pragma unroll
      for (int a = 0; a < 2; ++a) {
        const f4 uv = (acc[m][4 + a] + sm[m] * cv[a]) * sr[m];
        vf[t][a][2 * mm] = pk2(uv[0], uv[1]); vf[t][a][2 * mm + 1] = pk2(uv[2], uv[3]);
        const f4 uk = acc[m][2 + a] + sm[m] * ck[a];
        ea[a] = uk * sl[m]; pp[a] = uk * sr[m] + 1.f;
      }
      SBAR();
#pragma unroll
      for (int e = 0; e < 8; ++e) { if (t && (e == 0 || e == 3 || e == 6)) MKV(0, 3 * mm + e / 3); ea[e >> 2][e & 3] = __builtin_amdgcn_exp2f(ea[e >> 2][e & 3]); SBAR(); }
#pragma unroll
      for (int e = 0; e < 8; ++e) pp[e >> 2][e & 3] = __builtin_amdgcn_fmed3f(ea[e >> 2][e & 3], pp[e >> 2][e & 3], 1.f);
      SBAR();
#pragma unroll
      for (int a = 0; a < 2; ++a) { pp[a] += 1e-6f; kf[t][a][2 * mm] = pk2(pp[a][0], pp[a][1]); kf[t][a][2 * mm + 1] = pk2(pp[a][2], pp[a][3]); }
      SBAR();
    }
    MKV(1, 0); MKV(1, 1); MKV(1, 2); MKV(1, 3);
    asm volatile("ds_write_b128 %0, %1" ::"v"(xw), "v"(kv[2]) : "memory"); SBAR();
    MKV(1, 4);
    asm volatile("ds_write_b128 %0, %1 offset:1024" ::"v"(xw), "v"(kv[3]) : "memory"); SBAR();
    MKV(1, 5);
    asm volatile("ds_write_b128 %0, %1 offset:2048\n ds_write_b32 %2, %3" ::"v"(xw), "v"(zt[1]), "v"(vfl), "v"(c3) : "memory");
    f4 r0, r1, rz;
    _Pragma("unroll 1") for (unsigned it = 0; it < SPIN; ++it) {
      unsigned v;
      asm volatile("ds_read_b32 %0, %4\n ds_read_b128 %1, %5 offset:1024\n ds_read_b128 %2, %5\n ds_read_b128 %3, %5 offset:2048\n s_waitcnt lgkmcnt(0)"
                   : "=&v"(v), "=&v"(r0), "=&v"(r1), "=&v"(rz) : "v"(adFL), "v"(xr) : "memory");
      if ((((unsigned)__ballot(v < 3u) >> (w ^ 2)) & 1u) == 0u) break;
    }
    const f4 s0 = kv[0] + r0, s1 = kv[1] + r1, sz = zt[0] + rz;
    u4 o; o[0] = min(pk2(s0[0], s0[1]), POIS - 1u); o[1] = pk2(s0[2], s0[3]); o[2] = pk2(s1[0], s1[1]); o[3] = pk2(s1[2], s1[3]);
    ST128(o, kvo);
    u4 zq = __builtin_bit_cast(u4, sz); zq[0] = min(zq[0], POIS - 1u);
    ST128(zq, zo);
  }
  STMP(4);
  {
    SBAR();
    const LAS float* Lm = (const LAS float*)(lds + L_M) + wr * 64 + r15;
    const LAS float* Lc = (const LAS float*)(lds + L_CS) + wc * 96 + 4 * g4;
    { const unsigned va = aad[0] + 2 * STG; RQ(f0, va, b20, b21, 0); }
    { const unsigned va = aad[1] + 2 * STG; RQ(f1, va, b20, b21, 1); }
    WAITL(8); SBAR(); MQ(f0);
    { const unsigned va = aad[0] + 3 * STG; RQ(f0, va, b30, b31, 0); }
    WAITL(8); SBAR(); MQ(f1);
    { const unsigned va = aad[1] + 3 * STG; RQ(f1, va, b30, b31, 1); }
    WAITL(8); SBAR();
    const f4 c0 = *(const LAS f4*)(Lc + 16 * wr), c1 = *(const LAS f4*)(Lc + 16 - 16 * wr);
    float mv[4], rv[4], rl[4];
#pragma unroll
    for (int m = 0; m < 4; ++m) { mv[m] = Lm[m * 16]; rv[m] = Lm[m * 16 + (L_RS - L_M) / 4]; rl[m] = Lm[m * 16 + (L_RL - L_M) / 4]; }
    SBAR(); MQ(f0);
    if (sx >= 14) {
      const unsigned xo = xad + (sx - 12) * STG;
#pragma unroll
      for (int t = 0; t < 8; ++t) { const unsigned a_ = xo ^ (t * 16); asm volatile("ds_read_b64 %0, %1" : "+v"(xv[t]) : "v"(a_) : "memory"); }
    }
    WAITL(0); SBAR(); MQ(f1);
    LAS char* pw = (LAS char*)lds + L_PQ + wr * 8192 + wc * 4096 + l * 16;
#pragma unroll
    for (int m = 0; m < 4; ++m) {
      const f4 u0 = acc[m][0] + c0 * mv[m], u1 = acc[m][1] + c1 * mv[m];
      *(LAS u2*)(pw + wr * 8 + m * 1024) = (u2){phi2((f2){u0[0], u0[1]}, (f2)(rv[m]), (f2)(rl[m])), phi2((f2){u0[2], u0[3]}, (f2)(rv[m]), (f2)(rl[m]))};
      *(LAS u2*)(pw + 8 - wr * 8 + m * 1024) = (u2){phi2((f2){u1[0], u1[1]}, (f2)(rv[m]), (f2)(rl[m])), phi2((f2){u1[2], u1[3]}, (f2)(rv[m]), (f2)(rl[m]))};
    }
  }
  lsig(myFL, 4);
  __builtin_amdgcn_s_setprio(0);
  STMP(5);
  {
  int l;
  asm volatile("v_mbcnt_lo_u32_b32 %0, -1, 0\n v_mbcnt_hi_u32_b32 %0, -1, %0" : "=v"(l));
  __builtin_assume(l >= 0 && l < 64);
  const int r15 = l & 15, g4 = l >> 4, q = (l & 15) >> 2, p = l & 3;
  const int trow = mb * 128 + w * 32 + (l & 31);
  u2 oh;
  const unsigned flo = PAIRAT(r15) * 128;
  const char* fb = uptr(wsx + WS_OH + mb * 2048);
  int sel = -1;
  {
    const u4 s0 = *(const LAS u4*)(lds + L_SEL), s1 = *(const LAS u4*)(lds + L_SEL + 16);
#pragma unroll
    for (int i = 0; i < 8; ++i) { const int v = (int)(i < 4 ? s0[i & 3] : s1[i & 3]); sel = v == trow ? i : sel; }
  }
  const bool anysel = __ballot(sel >= 0) != 0ull;
  f16v oc[2][2];
  {
    const float m = *(const LAS float*)(lds + L_M + (w * 32 + (l & 31)) * 4), rs = *(const LAS float*)(lds + L_RS + (w * 32 + (l & 31)) * 4);
#pragma unroll
    for (int t = 0; t < 8; ++t) {
      const f4 xf = (f4){lo(xv[t][0]), hi(xv[t][0]), lo(xv[t][1]), hi(xv[t][1])};
      const f4 y = (xf - m) * rs + xf;
#pragma unroll
      for (int i = 0; i < 4; ++i) oc[0][t >> 2][(t & 3) * 4 + i] = y[i];
    }
    asm volatile("" : "+v"(oc[0][0]), "+v"(oc[0][1]));
  }
  unsigned wa[2];
#pragma unroll
  for (int t = 0; t < 2; ++t) wa[t] = ldsb + L_WO + wc * 4096 + (4 * (g4 >> 1) + q) * 128 + (((4 * t + 2 * (g4 & 1) + (p >> 1)) ^ ((q >> 1) << 2)) * 16) + (p & 1) * 8;
  u4 a[RD][4];
#pragma unroll
  for (int k = 0; k < RD; ++k) asm volatile("" : "=v"(a[k][0]), "=v"(a[k][1]), "=v"(a[k][2]), "=v"(a[k][3]));
  const char* ob = uptr(wsx + WS_OF + mb * 262144 + wc * 8192 + wr * 4096);
  const unsigned ov0 = l * 16 + wc * 2048, ov1 = ov0 ^ 2048;
  STMP(6);
  unsigned vfw[4];
#pragma unroll
  for (int i = 0; i < 4; ++i) { vfw[i] = VFW(i); asm volatile("" : "+v"(vfw[i])); }
  auto wo_part = [&](int s0, int s1) {
#pragma unroll
    for (int i = s0 / 2; i < s1 / 2; i += 2) {
      const int pp = i + wr;
      const char* wp = (const char*)wo + (nb * 128 - 3072) + (2 * PAIRAT(pp) + wc) * 65536; const unsigned wm = ldsb + (2 * pp + wc) * 4096;
      GL4(vfw, wp, wm, L_WO);
    }
  };
  lwait<2>(adFL, 0x3Fu, (l & 4) ? 3u : 4u);
  asm volatile("global_load_dwordx2 %0, %1, %2 sc1" : "=&v"(oh) : "v"(flo), "s"(fb) : "memory");
  wo_part(WE, WB);
  WAITV(WB - WE);
  asm volatile("" : "+v"(oh));
  unsigned seen = (unsigned)__ballot(oh[0] == tag), go = 0;
  auto need = [&](unsigned mask) {
    if (__builtin_expect((go & mask) != mask, 0)) {
      RETRY2();
      _Pragma("unroll 1") for (unsigned it = 0; it < SPIN; ++it) {
        if ((seen & mask) != mask) {
          asm volatile("global_load_dwordx2 %0, %1, %2 sc1\n s_waitcnt vmcnt(0)" : "+v"(oh) : "v"(flo), "s"(fb) : "memory");
          seen = (unsigned)__ballot(oh[0] == tag);
        }
        go = seen & (unsigned)__ballot(oh[1] - NOW() - 1u >= 1000u);
        if ((go & mask) == mask) break;
        SLEEP(1);
      }
    }
  };
#define OX 0
#define OSC "sc1"
#define OL1(pos, i) do { if (OX == 2 && (pos) >= RD) break; \
    asm volatile("global_load_dwordx4 %0, %1, %2 offset:%3 " OSC : "+v"(a[(pos) % RD][i]) : "v"((i) & 1 ? ov1 : ov0), "s"(ob + (OX == 1 ? ((pos) & 1) : PAIRAT(pos)) * 16384), "i"(((i) >> 1) * 1024) : "memory"); } while (0)
#define OLR(pos) do { _Pragma("unroll") for (int i_ = 0; i_ < 4; ++i_) asm volatile("global_load_dwordx4 %0, %1, %2 sc1" : "+v"(a[(pos) % RD][i_]) : "v"((i_ & 1 ? ov1 : ov0) + (i_ >> 1) * 1024), "s"(ob + PAIRAT(pos) * 16384) : "memory"); } while (0)
#define OLD(pos) do { OL1(pos, 0); OL1(pos, 1); OL1(pos, 2); OL1(pos, 3); } while (0)
  auto omax = [&](u4(&v)[4]) {
    asm volatile("" : "+v"(v[0]), "+v"(v[1]), "+v"(v[2]), "+v"(v[3]));
    return max(umax3(v[0][0], v[1][0], v[2][0]), v[3][0]);
  };
#define OFIX(pos, m) do { \
    if (__builtin_expect(__ballot((m) == POIS) != 0ull, 0)) { RETRY(); _Pragma("unroll 1") for (unsigned it = 0; it < SPIN; ++it) { SLEEP(4); OLR(pos); WAITV(0); if (__ballot(omax(a[(pos) % RD]) == POIS) == 0ull) break; } } } while (0)
#define OS1(i)
#define OS2(i)
  lwait<1>(adFL, 0x30u, 4);
  wo_part(WB, 32);
  __builtin_amdgcn_s_setprio(2);
  go = seen & (unsigned)__ballot(oh[1] - NOW() - 1u >= 1000u);
  need(1u);
  STMP(15);
  u2 oh2 = oh;
  if (__builtin_expect(seen != ~0u, 0)) asm volatile("global_load_dwordx2 %0, %1, %2 sc1" : "+v"(oh2) : "v"(flo), "s"(fb) : "memory");
  OS1(0); OLD(0);
#pragma unroll
  for (int i = 1; i < RD; ++i) { need(1u << i); OS1(i); OLD(i); }
  STMP(10);
  s4 b[8];
#define ORD(pos, f) do { if ((f) < 2 && (pos) == 8) { wa[(f) & 1] += 65536; asm volatile("" : "+v"(wa[(f) & 1])); } \
    RDTR(b[2 * (f)], wa[(f) & 1], ((pos) & 7) * 8192 + ((f) >> 1) * 1024); RDTR(b[2 * (f) + 1], wa[(f) & 1], ((pos) & 7) * 8192 + ((f) >> 1) * 1024 + 2048); } while (0)
#define OMF(pos, f, r) do { if ((f) < 2 && (pos) == 0 && (r) == 1) asm volatile("v_mfma_f32_32x32x16_bf16 %0, %1, %2, 0" : "=&v"(oc[r][(f) & 1]) \
      : "v"(CAT(b[2 * (f)], b[2 * (f) + 1])), "v"(a[(pos) % RD][((f) & 2) + (r)])); \
    else asm volatile("v_mfma_f32_32x32x16_bf16 %0, %1, %2, %0" : "+v"(oc[r][(f) & 1]) \
      : "v"(CAT(b[2 * (f)], b[2 * (f) + 1])), "v"(a[(pos) % RD][((f) & 2) + (r)])); \
    SBAR(); if ((r) == 1 && (pos) < 15) ORD(((pos) + 1) & 15, f); SBAR(); } while (0)
#define ONX(pos) ((pos) > 0 && (pos) + RD - 1 < 16)
#define ONL(pos, i) do { if (ONX(pos)) OL1(((pos) + RD - 1) & 15, i); SBAR(); } while (0)
#define OWL(pos, f) do { WAITL((pos) < 15 ? 6 : 6 - 2 * (f)); SBAR(); } while (0)
#define OHEAD(pos) do { unsigned m_ = 0; \
    if ((pos) == WE / 2 - 1 && WE < 32) { asm volatile("" : "+v"(fl3)); if (__builtin_expect(((unsigned)__ballot(fl3 < 5u) >> (w ^ 2)) & 1u, 0)) lwait(adFL, 1u << (w ^ 2), 5); SBAR(); } \
    OWL(pos, 0); OMF(pos, 0, 0); if (ONX(pos)) { need(1u << (((pos) + RD - 1) & 15)); OS1(((pos) + RD - 1) & 15); } SBAR(); \
    OMF(pos, 0, 1); ONL(pos, 0); \
    OWL(pos, 1); OMF(pos, 1, 0); ONL(pos, 1); \
    OMF(pos, 1, 1); ONL(pos, 2); \
    OWL(pos, 2); OMF(pos, 2, 0); ONL(pos, 3); \
    if ((pos) == 0 || (pos) == 4) { asm volatile("ds_write_b32 %0, %1" ::"v"(myFL), "v"((pos) ? 6u : 5u) : "memory"); SBAR(); } \
    if ((pos) == WE / 2 - 3) { asm volatile("ds_read_b32 %0, %1" : "=v"(fl3) : "v"(adFL) : "memory"); SBAR(); } \
    if ((pos) == 13) { asm volatile("ds_read_b32 %0, %1" : "=v"(fl3) : "v"(adFL) : "memory"); SBAR(); } \
    OMF(pos, 2, 1); \
    OWL(pos, 3); OMF(pos, 3, 0); \
    if ((pos) < 15) { WAITV(4 * ((pos) + RD - 1 < 16 ? RD - 2 : 14 - (pos))); m_ = omax(a[((pos) + 1) % RD]); asm volatile("" : "+v"(m_)); SBAR(); } \
    OMF(pos, 3, 1); \
    if ((pos) < 15) { OFIX(((pos) + 1) & 15, m_); OS2(((pos) + 1) & 15); } SBAR(); } while (0)
  unsigned fl3;
  ORD(0, 0); ORD(0, 1); ORD(0, 2); ORD(0, 3);
  { WAITV(4 * (RD - 1)); const unsigned m_ = omax(a[0]); OFIX(0, m_); OS2(0);
    asm volatile("" : "+v"(oh2));
    if (oh2[0] == tag) oh = oh2;
    seen = (unsigned)__ballot(oh[0] == tag);
    go = seen & (unsigned)__ballot(oh[1] - NOW() - max(r15 - (RD - 1), 0) * ADV - 1u >= 1000u);
    SBAR(); }
  OHEAD(0); OHEAD(1); STMP(11); OHEAD(2); OHEAD(3); OHEAD(4); OHEAD(5); OHEAD(6); OHEAD(7); STMP(12);
  OHEAD(8); OHEAD(9); STMP(13); OHEAD(10); OHEAD(11); OHEAD(12); OHEAD(13); OHEAD(14);
  f4 xi[8];
  {
    asm volatile("" : "+v"(fl3));
    if (__builtin_expect(((unsigned)__ballot(fl3 < 6u) >> (w ^ 2)) & 1u, 0)) lwait(adFL, 1u << (w ^ 2), 6);
    if (__builtin_expect(((unsigned)__ballot(fl3 < 4u) & 0x30u) != 0u, 0)) lwait(adFL, 0x30u, 4);
    const unsigned xw = ldsb + L_WO + (4 * wr + wc) * 4096 + l * 16, xr = xw ^ 4096;
#define XW(t) asm volatile("ds_write_b128 %0, %1 offset:%2" ::"v"(xw), "v"(__builtin_shufflevector(oc[1][(t) >> 2], oc[1][(t) >> 2], ((t) & 3) * 4, ((t) & 3) * 4 + 1, ((t) & 3) * 4 + 2, ((t) & 3) * 4 + 3)), \
      "i"(((t) >> 2) * 8192 + ((t) & 3) * 1024) : "memory")
    SBAR();
    OWL(15, 0); OMF(15, 0, 1); OWL(15, 1); OMF(15, 1, 1); OWL(15, 2); OMF(15, 2, 1); OWL(15, 3); OMF(15, 3, 1);
    OMF(15, 0, 0); OMF(15, 1, 0); XW(0); XW(1); XW(2); XW(3); SBAR();
    OMF(15, 2, 0); XW(4); XW(5); XW(6); XW(7); SBAR();
    OMF(15, 3, 0);
    asm volatile("ds_write_b32 %0, %1" ::"v"(myFL), "v"(7u) : "memory");
    _Pragma("unroll 1") for (unsigned it = 0; it < SPIN; ++it) {
      unsigned v;
      asm volatile("ds_read_b32 %0, %1" : "=v"(v) : "v"(adFL) : "memory");
#pragma unroll
      for (int t = 0; t < 8; ++t) RD128(xi[t], xr, (t >> 2) * 8192 + (t & 3) * 1024);
      WAITL(0);
      asm volatile("" : "+v"(v), "+v"(xi[0]), "+v"(xi[1]), "+v"(xi[2]), "+v"(xi[3]), "+v"(xi[4]), "+v"(xi[5]), "+v"(xi[6]), "+v"(xi[7]));
      if ((((unsigned)__ballot(v < 7u) >> (w ^ 1)) & 1u) == 0u) break;
    }
    asm volatile("" : "+v"(oc[0][0]), "+v"(oc[0][1]));
#pragma unroll
    for (int t = 0; t < 8; ++t)
#pragma unroll
      for (int i = 0; i < 4; ++i) xi[t][i] += oc[0][t >> 2][(t & 3) * 4 + i];
  }
  STMP(8);
  if (anysel) {
    lwait(adFL, 0xC0u, 3);
    if (sel >= 0) {
#pragma unroll
      for (int t = 0; t < 8; ++t) xi[t] += *(const LAS f4*)(lds + L_KVX + sel * 256 + (8 * t + 4 * (l >> 5)) * 4);
    }
  }
  const __amdgpu_buffer_rsrc_t outr = __builtin_amdgcn_make_buffer_rsrc(out, 0, 2048 * D * 2, 0x00020000);
  if (FULL) {
    LAS char* tb = (LAS char*)lds + L_PQ + w * 4096;
#pragma unroll
    for (int t = 0; t < 8; ++t) *(LAS u2*)(tb + (l & 31) * 128 + ((t ^ (l & 7)) * 16) + (l >> 5) * 8) = (u2){pk2(xi[t][0], xi[t][1]), pk2(xi[t][2], xi[t][3])};
#pragma unroll
    for (int i = 0; i < 4; ++i) {
      const u4 v = *(const LAS u4*)(tb + i * 1024 + (l >> 3) * 128 + (((l ^ (l >> 3)) & 7) * 16));
      __builtin_amdgcn_raw_buffer_store_b128(v, outr, (mb * 128 + w * 32 + i * 8 + (l >> 3)) * 2048 + nb * 128 + (l & 7) * 16, 0, 16);
    }
  } else {
#pragma unroll
  for (int hh = 0; hh < 4; ++hh) {
    const f4 ta = xi[2 * hh], tb = xi[2 * hh + 1];
    auto r0 = __builtin_amdgcn_permlane32_swap(pk2(ta[0], ta[1]), pk2(tb[0], tb[1]), false, false);
    auto r1 = __builtin_amdgcn_permlane32_swap(pk2(ta[2], ta[3]), pk2(tb[2], tb[3]), false, false);
    __builtin_amdgcn_raw_buffer_store_b128(((u4){r0[0], r1[0], r0[1], r1[1]}), outr, (trow * D + nb * 64 + 16 * hh + 8 * (l >> 5)) * 2, 0, 16);
  }
  }
  if (g == 0 && w == 0 && l == 0) __builtin_amdgcn_raw_buffer_store_b32(tag, wse, WS_E, 0, 16);
  STMP(9);
  STFLUSH();
  asm volatile(".Lend_c:");
  }
  return;
  }
  {
    unsigned vb[4], vfw[4];
    STMP(6); SBAR();
#pragma unroll
    for (int i = 0; i < 4; ++i) vb[i] = (8 * i + (l >> 3)) * 6144 + (((l & 7) ^ ((((l >> 4) & 1) << 2) | ((i & 1) << 1))) * 16) + (3 - i) * 1024;
    const unsigned lb0 = lw * 196608, lb1 = 196608 - lw * 194560, mbb = ldsb + 16384 + lw * 12288;
    const char* Ab = kx + ao;
    const char* Bb = kq + (hp * 128 + lw * 2048 - 3072);
    auto issue = [&](int s, int u0, int u1) {
      const int kt = (s + rot) & 15;
      const char* sa = Ab + kt * 128; const char* sb = Bb + kt * (64 * 6144);
      const unsigned so = (s & 3) * STG, ma = mba + so, mq = mbb + so;
#pragma unroll
      for (int u = u0; u < u1; ++u) { if (u < 2) GL4(va, sa + u * 65536, ma, u * 4096); else GL4(vb, sb + (u == 2 ? lb0 : u == 3 ? lb1 : lb0 + 2048), mq, (u - 2) * 4096); }
    };
    int tk = 0;
#define LBAR() do { BST(tk + 1); BAR(); BST(tk + 21); ++tk; } while (0)
    issue(0, 2, 3); issue(0, 1, 2); issue(0, 3, 5); SBAR(); STMP(8); SBAR(); issue(1, 0, 5); issue(2, 0, U0);
    BST(60);
    WAITV(20 + 4 * U0); BST(0); BAR(); BST(20);
    issue(2, U0, 5);
    GETTAG(tag);
    MKWS(tag);
    const unsigned ck0 = (unsigned)__builtin_amdgcn_s_memtime(), rt0 = NOW();
    int s0 = 3;
    if (U3 < 5) { issue(3, 0, U3); BST(tk + 40); WAITV(20 + 4 * U3); LBAR(); issue(3, U3, 5); s0 = 4; }
#pragma unroll 1
    for (; s0 < (sp ? 16 : 6 + ph); ++s0) { issue(s0, 0, 5); BST(tk + 40); WAITV(40); LBAR(); }
#pragma unroll 1
    for (; s0 < 16; s0 += 5) {
      WAITV(20); LBAR();
      WAITV(0); LBAR();
      issue(s0, 0, 5); issue(s0 + 1, 0, U1); BST(tk + 40); WAITV(4 * U1); LBAR();
      issue(s0 + 1, U1, 5); issue(s0 + 2, 0, 5);
      if (s0 + 3 >= 16) break;
      issue(s0 + 3, 0, U2); BST(tk + 40); WAITV(20 + 4 * U2); LBAR();
      issue(s0 + 3, U2, 5);
      if (s0 + 4 >= 16) break;
      issue(s0 + 4, 0, 5); BST(tk + 40); WAITV(40); LBAR();
    }
    int wq = 0;
#pragma unroll
    for (int i = 0; i < 4; ++i) vfw[i] = VFW(i);
    auto wo_upto = [&](int lim) {
      _Pragma("unroll 1") for (; wq < lim; ++wq) { const char* wp = (const char*)wo + (nb * 128 - 3072) + HEADAT(wq) * 65536; const unsigned wm = ldsb + wq * 4096; GL4(vfw, wp, wm, L_WO); }
    };
    asm volatile("ds_write_b32 %0, %1" ::"v"(adFL), "v"(0u) : "memory");
    unsigned kd = sp ? KS : KD, od = OD;
    if (CF) {
      const unsigned dc = (unsigned)__builtin_amdgcn_s_memtime() - ck0, dr = NOW() - rt0;
      const float sc = fminf((float)dr * (float)CF * __builtin_amdgcn_rcpf((float)dc), 2.f);
      kd = KL + (unsigned)((float)(kd - KL) * sc); od = OL + (unsigned)((float)(OD - OL) * sc);
      asm volatile("" : "+v"(kd), "+v"(od));
    }
    const int hd = hp * 2 + lw;
    u4 sel[2];
#pragma unroll
    for (int e = 0; e < 2; ++e) {
      unsigned v = (r15 >> 2) == g4 ? (((r15 & 3) == 2 * e ? 0x3F80u : 0u) | ((r15 & 3) == 2 * e + 1 ? 0x3F800000u : 0u)) : 0u;
      asm volatile("" : "+v"(v));
      sel[0][e] = v; sel[0][2 + e] = 0u; sel[1][e] = 0u; sel[1][2 + e] = v;
    }
    const char* kvb = uptr(wsx + WS_KVP + (size_t)hd * 32768);
    const char* zb = uptr(wsx + WS_ZP + (size_t)hd * 2048);
    unsigned zoff = r15 * 128 + g4 * 32, oho = l == 0 ? WS_OH + (mb * 16 + hp) * 128 : WS_SZ, fla = ldsb + L_FL;
    asm volatile("" : "+v"(zoff), "+v"(oho), "+v"(fla), "+s"(kvb), "+s"(zb));
    WAITV(0); WAITL(0); LBAR();
    u4 dg;
    unsigned now;
    if (lw) {
      const u4 pz = (u4)(POIS);
      const unsigned tb = NOW();
      __builtin_amdgcn_raw_buffer_store_b32(tb + kd, wsr, l == 0 ? WS_HK + hp * 512 + (mb >> 2) * 128 + (mb & 3) * 4 : WS_SZ, 0, 16);
#pragma unroll
      for (int i = 0; i < 16; ++i) ST128(pz, WS_OF + (mb * 32 + hp * 2) * 8192 + i * 1024 + l * 16);
      BST(61);
      const char* hb = uptr(wsx + WS_HK + hp * 512);
      __builtin_amdgcn_s_setprio(LP1);
      lwait<4>(adFL, 0xC0u, 1u);
      while (NOW() - tb < HT) SLEEP(1);
      STMP(1);
      unsigned t0, rq[4], m0, m1, nt;
#define HG4(d, a, b, c, e) "s_sub_u32 s" #a ", s" #a ", %0\n s_sub_u32 s" #b ", s" #b ", %0\n s_sub_u32 s" #c ", s" #c ", %0\n s_sub_u32 s" #e ", s" #e ", %0\n" \
      "s_max_u32 s" #a ", s" #a ", s" #b "\n s_max_u32 s" #c ", s" #c ", s" #e "\n s_max_u32 %" #d ", s" #a ", s" #c "\n"
      asm volatile("s_mov_b32 %7, 15\n.Lhint:\n s_load_dwordx4 s[72:75], %8, 0 glc\n s_load_dwordx4 s[76:79], %8, 128 glc\n s_load_dwordx4 s[80:83], %8, 256 glc\n"
                   "s_load_dwordx4 s[84:87], %8, 384 glc\n s_memrealtime s[88:89]\n s_waitcnt lgkmcnt(0)\n s_sub_u32 %0, s88, 0x800\n"
                   HG4(1, 72, 73, 74, 75) HG4(2, 76, 77, 78, 79) HG4(3, 80, 81, 82, 83) HG4(4, 84, 85, 86, 87)
                   "s_max_u32 %5, %1, %2\n s_max_u32 %6, %3, %4\n s_max_u32 s72, %5, %6\n s_cmpk_lt_u32 s72, 0xc00\n s_cbranch_scc1 .Lhdone\n"
                   "s_sleep %9\n s_sub_u32 %7, %7, 1\n s_cbranch_scc0 .Lhint\n"
                   "s_mov_b32 %1, 0\n s_mov_b32 %2, 0\n s_mov_b32 %3, 0\n s_mov_b32 %4, 0\n s_mov_b32 %5, 0\n s_mov_b32 %6, 0\n.Lhdone:"
                   : "=&s"(t0), "=&s"(rq[0]), "=&s"(rq[1]), "=&s"(rq[2]), "=&s"(rq[3]), "=&s"(m0), "=&s"(m1), "=&s"(nt) : "s"(hb), "i"(HKS)
                   : "memory", "scc", "s72", "s73", "s74", "s75", "s76", "s77", "s78", "s79", "s80", "s81", "s82", "s83", "s84", "s85", "s86", "s87", "s88", "s89");
      BST(16);
      const bool up = m0 > m1;
      const unsigned a0 = up ? rq[2] : rq[0], a1 = up ? rq[3] : rq[1], a2 = up ? rq[0] : rq[2], a3 = up ? rq[1] : rq[3], gu = up ? 2u : 0u;
      unsigned e0, e1, go;
      asm("s_cmp_gt_u32 %3, %4\n s_cselect_b32 %0, %6, %5\n s_cselect_b32 %1, %5, %6\n s_addc_u32 %2, %7, 0" : "=&s"(e0), "=&s"(e1), "=&s"(go) : "s"(a2), "s"(a3), "s"(a0), "s"(a1), "s"(gu) : "scc");
      dg = (u4){((t0 + 3u + e0) & ~3u) | go, t0 + e1, t0 + min(a2, a3), t0 + max(m0, m1)};
      asm volatile("ds_write_b128 %0, %1 offset:32\n ds_write_b32 %0, %2 offset:20" ::"v"(fla), "v"(dg), "v"(1u) : "memory");
      BST(15);
      now = NOW();
      WAITV(0);
      ST64(((u2){tag, ((int)(dg[3] - now) > 0 ? dg[3] : now) + od}), oho);
    } else {
      wo_upto(WA);
      BST(15);
      if (WA < WE) { lwait<2>(adFL, 0x0Fu, 3); if (WS0) SLEEP(WS0); wo_upto(WE); }
      STMP(1);
      __builtin_amdgcn_s_setprio(LP0);
      u4 v;
      _Pragma("unroll 1") for (unsigned it = 0; it < SPIN; ++it) {
        unsigned f;
        asm volatile("ds_read_b32 %0, %2 offset:20\n ds_read_b128 %1, %2 offset:32\n s_waitcnt lgkmcnt(0)" : "=&v"(f), "=&v"(v) : "v"(fla) : "memory");
        if (__builtin_amdgcn_readfirstlane(f)) break;
        SLEEP(2);
      }
#pragma unroll
      for (int e = 0; e < 4; ++e) dg[e] = __builtin_amdgcn_readfirstlane(v[e]);
      now = NOW();
    }
    unsigned gx = (dg[0] & 3u) * 8192u;
    asm volatile("" : "+s"(gx));
    __builtin_amdgcn_s_setprio(3);
    u4 pv[16][2], zv0, zv1, pq[8];
    const unsigned lv16 = l * 16;
    const unsigned pa = ldsb + L_PQ + lw * 4096 + l * 16, omask = lw ? 0x0Au : 0x25u;
    STMP(2);
#define GLD(gq) do { _Pragma("unroll") for (int i = 0; i < 4; ++i) { const char* sb = padd(kvb, (((gq) * 8192u + (i & 2) * 2048u) ^ gx)); \
      asm volatile("global_load_dwordx4 %0, %1, %2 offset:%3 sc1" : "=v"(pv[(gq) * 4 + i][0]) : "v"(lv16), "s"(sb), "i"((i & 1) * 2048) : "memory"); \
      asm volatile("global_load_dwordx4 %0, %1, %2 offset:%3 sc1" : "=v"(pv[(gq) * 4 + i][1]) : "v"(lv16), "s"(sb), "i"((i & 1) * 2048 + 1024) : "memory"); } } while (0)
#define ZLD() do { asm volatile("global_load_dwordx4 %0, %1, %2 sc1" : "=v"(zv0) : "v"(zoff), "s"(zb) : "memory"); \
      asm volatile("global_load_dwordx4 %0, %1, %2 offset:16 sc1" : "=v"(zv1) : "v"(zoff), "s"(zb) : "memory"); } while (0)
#pragma unroll
    for (int gq = 0; gq < 4; ++gq) {
      while ((int)(now - dg[gq]) < 0) now = NOW();
      if (gq == 3) ZLD();
      GLD(gq);
    }
    lsig(myFL, 3);
    STMP(5);
    unsigned ofo = WS_OF + (mb * 32 + hd) * 8192 + g4 * 512 + r15 * 16;
    unsigned ofp = ofo + 4096;
    f4 eps = (f4)(1e-6f);
    asm volatile("" : "+v"(ofo), "+v"(ofp), "+v"(eps), "+v"(sel[0]), "+v"(sel[1]));
    f4 gs[4][4]; float z[8];
#pragma unroll
    for (int gq = 0; gq < 4; ++gq) {
      if (gq == PW) {
        SBAR();
        lwait(adFL, omask, (l & 4) ? 3u : 4u);
#pragma unroll
        for (int i = 0; i < 8; ++i) RD128(pq[i], pa, (i >> 2) * 8192 + (i & 3) * 1024);
      }
      SBAR(); WAITV(gq == 0 ? 26 : gq == 1 ? 18 : gq == 2 ? 10 : 0);
      if (gq == 3) BST(17);
      auto gbad = [&]() {
#pragma unroll
        for (int i = 0; i < 4; ++i) asm volatile("" : "+v"(pv[gq * 4 + i][0]), "+v"(pv[gq * 4 + i][1]));
        unsigned m = umax3(pv[gq * 4][0][0], pv[gq * 4][1][0], pv[gq * 4 + 1][0][0]);
        m = umax3(m, pv[gq * 4 + 1][1][0], pv[gq * 4 + 2][0][0]);
        m = umax3(m, pv[gq * 4 + 2][1][0], pv[gq * 4 + 3][0][0]);
        m = max(m, pv[gq * 4 + 3][1][0]);
        if (gq == 3) { asm volatile("" : "+v"(zv0), "+v"(zv1)); m = umax3(m, zv0[0], zv1[0]); }
        return __ballot(m == POIS) != 0ull;
      };
      if (__builtin_expect(gbad(), 0)) {
        _Pragma("unroll 1") for (unsigned it = 0; it < SPIN; ++it) {
          RETRY();
          SLEEP(2);
          if (gq == 3) ZLD();
          GLD(gq);
          WAITV(0);
          if (!gbad()) break;
        }
      }
      if (gq == 3) {
#pragma unroll
        for (int e = 0; e < 4; ++e) { z[e] = __uint_as_float(zv0[e]); z[4 + e] = __uint_as_float(zv1[e]); }
        SBAR();
      }
#pragma unroll
      for (int i = 0; i < 4; ++i)
#pragma unroll
        for (int t = 0; t < 4; ++t) {
          gs[gq][t] = MFMA16(S8(sel[(t ^ (t >> 1)) & 1]), S8(pv[gq * 4 + i][t >> 1]), i ? gs[gq][t] : (f4)(0.f));
          if (gq == 3) {
            SBAR();
            if (i + t == 0) asm volatile("s_nop 1");
            asm volatile("v_add_f32_dpp %0, %0, %0 row_ror:%2 row_mask:0xf bank_mask:0xf\n v_add_f32_dpp %1, %1, %1 row_ror:%2 row_mask:0xf bank_mask:0xf" : "+v"(z[2 * t]), "+v"(z[2 * t + 1]) : "i"(8 >> i));
            SBAR();
          }
        }
      if (gq == 2) {
#pragma unroll
        for (int t = 0; t < 4; ++t) { gs[0][t] += gs[1][t]; asm volatile("" : "+v"(gs[0][t])); }
      }
    }
    STMP(3);
    WAITL(0);
#pragma unroll
    for (int i = 0; i < 8; i += 2) asm volatile("" : "+v"(pq[i]), "+v"(pq[i + 1]));
    u4 kA[2], zA;
#pragma unroll
    for (int e = 0; e < 4; ++e) zA[e] = pk2(z[2 * e], z[2 * e + 1]);
    SBAR();
#pragma unroll
    for (int t = 0; t < 4; ++t) gs[0][t] += gs[2][t] + gs[3][t];
    SBAR();
    f4 dn[8], o0[8], o1[8]; float inv[8];
#pragma unroll
    for (int i = 0; i < 8; ++i) {
      dn[i] = MFMA16(S8(zA), S8(pq[i]), eps);
      SBAR();
      kA[i >> 2][i & 3] = pk2(gs[0][i >> 1][(i & 1) * 2], gs[0][i >> 1][(i & 1) * 2 + 1]);
      SBAR();
    }
#pragma unroll
    for (int i = 0; i < 8; ++i) {
      o0[i] = MFMA16(S8(kA[0]), S8(pq[i]), (f4)(0.f)); SBAR();
      o1[i] = MFMA16(S8(kA[1]), S8(pq[i]), (f4)(0.f)); SBAR();
      inv[i] = __builtin_amdgcn_rcpf(dn[i][0]); asm volatile("" ::"v"(dn[i])); SBAR();
    }
    BST(18);
    u4 op = sel[0];
#pragma unroll
    for (int i = 0; i < 8; ++i) {
      o0[i] *= inv[i]; o1[i] *= inv[i];
      u4 o; o[0] = min(pk2(o0[i][0], o0[i][1]), POIS - 1u); o[1] = pk2(o0[i][2], o0[i][3]); o[2] = pk2(o1[i][0], o1[i][1]); o[3] = pk2(o1[i][2], o1[i][3]);
      asm volatile("" : "+v"(o) : "v"(op));
      ST128(o, (i & 4 ? ofp : ofo) + (i & 1) * 256 + (i & 2) * 1024);
      op = o;
      SBAR();
    }
    lsig(myFL, 4);
    STMP(4);
    STFLUSH();
    asm volatile(".Lend_l:");
  }

}

extern "C" void kernel_launch(void* const* d_in, const int* in_sizes, int n_in, void* d_out, int out_size,
                              void* d_ws, size_t ws_size, hipStream_t stream) {
  (void)hipFuncSetAttribute((const void*)fused, hipFuncAttributeMaxDynamicSharedMemorySize, LDS_SZ);
  fused<<<256, 512, LDS_SZ, stream>>>((const u16*)d_in[0], (const u16*)d_in[1], (const u16*)d_in[4], (char*)d_ws, (const u16*)d_in[2],
                                      (const u16*)d_in[5], (const u16*)d_in[6], (const u16*)d_in[7], (u16*)d_out);
}
```

```cpp
#define EVS 4
#define EVR 4
#define EVO 0.f
#define EVM 0
#define EVG (!sp_&&ph_==0)
#include <hip/hip_runtime.h>
#include <stdint.h>

typedef unsigned short u16;
typedef short s4 __attribute__((ext_vector_type(4)));
typedef short s8 __attribute__((ext_vector_type(8)));
typedef float f4 __attribute__((ext_vector_type(4)));
typedef float f16v __attribute__((ext_vector_type(16)));
typedef float f2 __attribute__((ext_vector_type(2)));
typedef unsigned u4 __attribute__((ext_vector_type(4)));
typedef unsigned u2 __attribute__((ext_vector_type(2)));
typedef unsigned u8v __attribute__((ext_vector_type(8)));
typedef __bf16 bf2 __attribute__((ext_vector_type(2)));
#define LAS __attribute__((address_space(3)))
#define DEV __device__ __forceinline__

constexpr int D = 1024, F = 3584;
#define NOMOE 0
#define KD 220
#define KS 214
#define OD 180
#define OHP 1
#define FS 128
#define GSL 8
#define YSL 16
#define TP1 0
#define TP2 3
#define TLBW 0
#define FULL 1
#define HPRIO 1
#define OWN 1
#define NOPOIS 0
#define CAP 7
#define RD 4
#define DYN 1
#define JX 0
#define NOSKIP 0
#define GRP 8
#define ADV 15
#define HB 1
#define RT 1
#define WD 0
#define WU1 12
#define WU2 22
#define PL 0
#define PO 0
#define WG 0
#define DS 0
#define D1 16
#define D2 2
#define HKS 2
#define LW1 1
#define HW1 2
#define HS0 0
#define GPV 2
#define XO 1
#define TP0 3
#define G1 0x31
#define G1S 4
#define G2S 8
#define TD 1
#define YD 170
#define WU0 4
#define WE 12
#define HT 115
#define CF 19
#define KL 145
#define OL 115
#define CL 0
#define WS0 4
#define OM 0
#define HG 128
#define U0 0
#define U3 5
#define TL 0
#define MH 0
#define HWT 3
#define YG 0
#define GG 1
#define PV 1
#define U1 4
#define U2 2
#define QD 1
#define LP0 3
#define LP1 3
#define WA 8
#define PW 3
#define WB 24
#define PF 1
#define PFS 6
#define PA 0
#define PAS 0
#define ROT 0

constexpr unsigned WS_E = 0;
constexpr unsigned WS_YF = 524288;
constexpr unsigned WS_CAND = 12288;
constexpr unsigned WS_RG = 49152;
constexpr unsigned WS_ST = 65536;
constexpr unsigned WS_HK = 589824;
constexpr unsigned WS_ZP = 131072;
constexpr unsigned WS_CS = 196608;
constexpr unsigned WS_OH = 458752;
constexpr unsigned WS_KVP = 1u << 20;
constexpr unsigned WS_GP = 2u << 20;
constexpr unsigned WS_YP = 3u << 20;
constexpr unsigned WS_OF = 5u << 20;
constexpr unsigned WS_SZ = 9u << 20;
#define NR 4
#define RGO(tag) ((64u << 20) + ((tag) & (NR - 1u)) * (10u << 20))
#define MKWS(tag) char* const wsx = ws + RGO(tag); const __amdgpu_buffer_rsrc_t wsr = __builtin_amdgcn_make_buffer_rsrc(wsx, 0, WS_SZ, 0x00020000)

constexpr int STG = 40960;
constexpr int L_KVX = 0;
constexpr int L_PQ = 12288;
constexpr int L_M = 28672, L_RS = L_M + 512, L_CS = L_RS + 512, L_SEL = L_CS + 768, L_FL = L_SEL + 64;
constexpr int L_HX = 30720, L_RL = 31744, L_WO = 32768;
constexpr int LDS_SZ = 163840;
constexpr unsigned POIS = 0xFFFFFFFFu;

DEV unsigned pk2(float a, float b) { bf2 v; v[0] = (__bf16)a; v[1] = (__bf16)b; return __builtin_bit_cast(unsigned, v); }
DEV float lo(unsigned u) { return __uint_as_float(u << 16); }
DEV float hi(unsigned u) { return __uint_as_float(u & 0xFFFF0000u); }
DEV float rbf(float a) { return lo(pk2(a, a)); }
DEV float dot2(unsigned a, unsigned b, float c) { return __builtin_amdgcn_fdot2_f32_bf16(__builtin_bit_cast(bf2, a), __builtin_bit_cast(bf2, b), c, false); }
#define RD128(dst, addr, OFF) asm volatile("ds_read_b128 %0, %1 offset:%2" : "=v"(dst) : "v"(addr), "i"(OFF) : "memory")
#define RDTR(dst, addr, OFF) asm volatile("ds_read_b64_tr_b16 %0, %1 offset:%2" : "=v"(dst) : "v"(addr), "i"(OFF) : "memory")
#define SBAR() __builtin_amdgcn_sched_barrier(0)
#define CAT(x, y) __builtin_shufflevector(x, y, 0, 1, 2, 3, 4, 5, 6, 7)
#define WAITV(n) asm volatile("s_waitcnt vmcnt(%0)" ::"i"(n) : "memory")
#define WAITL(n) asm volatile("s_waitcnt lgkmcnt(%0)" ::"i"(n) : "memory")
#define MEMB() asm volatile("" ::: "memory")
#define SLEEP(n) asm volatile("s_sleep %0" ::"i"(n) : "memory")
#define BAR() asm volatile("s_barrier" ::: "memory")
#define GL0(vo, sb, m0v) asm volatile("s_mov_b32 m0, %2\n s_nop 0\n global_load_lds_dwordx4 %0, %1" ::"v"(vo), "s"(sb), "s"(m0v) : "memory")
#define GLI(vo, sb, OFF) asm volatile("global_load_lds_dwordx4 %0, %1 offset:%2" ::"v"(vo), "s"(sb), "i"(OFF) : "memory")
#define GL4(v, sb, mm, MO) do { asm volatile("s_add_i32 m0, %2, %3\n s_nop 0\n global_load_lds_dwordx4 %0, %1" ::"v"((v)[0]), "s"(sb), "s"(mm), "i"(MO) : "memory", "scc"); \
      GLI((v)[1], sb, 1024); GLI((v)[2], sb, 2048); GLI((v)[3], sb, 3072); } while (0)
#define VFW(i) ((8 * (i) + (l >> 3)) * 2048 + (((l & 7) ^ (((l >> 4) & 1) << 2)) * 16) + (3 - (i)) * 1024)
#define MFMA16(a, b, c) __builtin_amdgcn_mfma_f32_16x16x32_bf16(a, b, c, 0, 0, 0)
#define S8(x) __builtin_bit_cast(s8, x)
#define LD128(off) __builtin_amdgcn_raw_buffer_load_b128(wsr, (off), 0, 16)
#define LD64(off) __builtin_amdgcn_raw_buffer_load_b64(wsr, (off), 0, 16)
#define ST128(v, off) __builtin_amdgcn_raw_buffer_store_b128((v), wsr, (off), 0, 16)
#define ST64(v, off) __builtin_amdgcn_raw_buffer_store_b64((v), wsr, (off), 0, 16)
#define OFA(i) (WS_OF + ((mb * 32 + hd) * 8 + (i)) * 1024 + l * 16)
#define POISON(i) ST128(pz, (i) < 2 ? WS_KVP + (hd * 16 + mb) * 2048 + (i) * 1024 + l * 16 : (i) == 2 ? WS_ZP + (hd * 16 + mb) * 128 + (l & 7) * 16 : OFA((i) - 3))
#define NOW() ((unsigned)__builtin_amdgcn_s_memrealtime())
constexpr unsigned SPIN = 1u << 21;
DEV const char* uptr(const char* p) {
  unsigned long long v = (unsigned long long)p;
  unsigned lo_ = __builtin_amdgcn_readfirstlane((unsigned)v), hi_ = __builtin_amdgcn_readfirstlane((unsigned)(v >> 32));
  return (const char*)(((unsigned long long)hi_ << 32) | lo_);
}
DEV const char* padd(const char* p, unsigned o) {
  unsigned lo_, hi_;
  asm("s_add_u32 %0, %2, %4\n s_addc_u32 %1, %3, 0" : "=&s"(lo_), "=s"(hi_) : "s"((unsigned)(unsigned long long)p), "s"((unsigned)((unsigned long long)p >> 32)), "s"(o) : "scc");
  return (const char*)(((unsigned long long)hi_ << 32) | lo_);
}
DEV void lsig(unsigned addr, unsigned val) { asm volatile("s_waitcnt lgkmcnt(0)\n ds_write_b32 %0, %1" ::"v"(addr), "v"(val) : "memory"); }
template <int SL = 0> DEV void lwait(unsigned addr, unsigned mask, unsigned target) {
  _Pragma("unroll 1") for (unsigned it = 0; it < SPIN; ++it) {
    unsigned v;
    asm volatile("ds_read_b32 %0, %1\n s_waitcnt lgkmcnt(0)" : "=v"(v) : "v"(addr) : "memory");
    if (((unsigned)__ballot(v < target) & mask) == 0u) break;
    if (SL) SLEEP(SL);
  }
}
DEV unsigned nopois(unsigned a) { return min(a, POIS - 1u); }
DEV u4 tagf(f4 v, unsigned tag) {
  u4 r;
#pragma unroll
  for (int e = 0; e < 4; ++e) r[e] = __builtin_amdgcn_perm(__float_as_uint(v[e]), tag, 0x07060500u + e);
  return r;
}
DEV void untag(u4& v, unsigned tag, unsigned& bad) {
#pragma unroll
  for (int e = 0; e < 4; ++e) v[e] ^= (tag >> (8 * e)) & 255u;
  bad |= v[0] | v[1]; bad |= v[2] | v[3];
}
DEV unsigned umax3(unsigned a, unsigned b, unsigned c) { return max(max(a, b), c); }
template <int C> DEV float dppf(float v) { return __int_as_float(__builtin_amdgcn_update_dpp(0, __float_as_int(v), C, 0xf, 0xf, false)); }
DEV unsigned wave_max(unsigned v) {
  v = max(v, (unsigned)__builtin_amdgcn_update_dpp(0, (int)v, 0x111, 0xf, 0xf, false));
  v = max(v, (unsigned)__builtin_amdgcn_update_dpp(0, (int)v, 0x112, 0xf, 0xf, false));
  v = max(v, (unsigned)__builtin_amdgcn_update_dpp(0, (int)v, 0x114, 0xf, 0xf, false));
  v = max(v, (unsigned)__builtin_amdgcn_update_dpp(0, (int)v, 0x118, 0xf, 0xf, false));
  v = max(v, (unsigned)__builtin_amdgcn_update_dpp(0, (int)v, 0x142, 0xa, 0xf, false));
  v = max(v, (unsigned)__builtin_amdgcn_update_dpp(0, (int)v, 0x143, 0xc, 0xf, false));
  return (unsigned)__builtin_amdgcn_readlane((int)v, 63);
}
DEV unsigned phi2(f2 u, f2 rs, f2 rl) {
  f2 a = u * rl, p = u * rs + 1.f, r;
  r[0] = __builtin_amdgcn_fmed3f(__builtin_amdgcn_exp2f(a[0]), p[0], 1.f);
  r[1] = __builtin_amdgcn_fmed3f(__builtin_amdgcn_exp2f(a[1]), p[1], 1.f);
  r += 1e-6f;
  return pk2(r[0], r[1]);
}

#define STDECL unsigned st_[16] = {0, 0, 0, 0, 0, 0, 0, 0, 0, 0, 0, 0, 0, 0, 0, 0}
#define STMP(i) st_[i] = (unsigned)__builtin_amdgcn_s_memrealtime()
#define BST(i)
#undef HB
#define HB 0
#define BSTV cw
#define RETRY()
#define RETRY2()
#define STFLUSH() do { if (w == 0) { const __amdgpu_buffer_rsrc_t dr_ = __builtin_amdgcn_make_buffer_rsrc(ws + (40u << 20), 0, 1u << 20, 0x00020000); \
  if (l == 0) __builtin_amdgcn_raw_buffer_store_b64(((u2){st_[EVR], st_[EVS]}), dr_, blockIdx.x * 128, 0, 16); \
  if (blockIdx.x == 0) { while (NOW() - st_[0] < 3500u) SLEEP(1); \
    float sa_ = 0, sg_ = 0, n_ = 0; unsigned mx_ = 0; \
    for (int i_ = 0; i_ < 8; ++i_) { const int b_ = i_ * 32 + (l & 31), x_ = b_ & 7, j_ = b_ >> 3, hp_ = (x_ >> 1) * 4 + (j_ & 3), ph_ = (int)(PHT >> (2 * j_)) & 3; const bool sp_ = (hp_ & 7) == 3; \
      const u2 a_ = __builtin_amdgcn_raw_buffer_load_b64(dr_, b_ * 128, 0, 16); const unsigned t_ = a_[1] - st_[0] + 1000u; \
      sa_ += (float)(a_[0] - st_[0] + 1000u); if (EVG) { sg_ += (float)t_; n_ += 1.f; mx_ = max(mx_, t_); } } \
    sa_ = half_sum(sa_); sg_ = half_sum(sg_); n_ = half_sum(n_); mx_ = wave_max(mx_); \
    const float d_ = ((EVM ? (float)mx_ : sg_ / n_) - sa_ / 256.f + 200.f - EVO) * 10.f; \
    const unsigned v_ = min((unsigned)__builtin_amdgcn_readfirstlane((int)fmaxf(d_, 0.f)), 300000u); \
    _Pragma("unroll 1") for (unsigned it = 0; it < SPIN; ++it) { if (NOW() - st_[0] >= 4500u + v_) break; SLEEP(1); } } } } while (0)

DEV float half_sum(float v) {
  asm volatile("s_nop 1\n v_add_f32_dpp %0, %0, %0 row_ror:8 row_mask:0xf bank_mask:0xf\n s_nop 1\n v_add_f32_dpp %0, %0, %0 row_ror:4 row_mask:0xf bank_mask:0xf\n"
               "s_nop 1\n v_add_f32_dpp %0, %0, %0 row_ror:2 row_mask:0xf bank_mask:0xf\n s_nop 1\n v_add_f32_dpp %0, %0, %0 row_ror:1 row_mask:0xf bank_mask:0xf\n s_nop 1" : "+v"(v));
  auto r = __builtin_amdgcn_permlane16_swap(__float_as_uint(v), __float_as_uint(v), false, false);
  return __uint_as_float(r[0]) + __uint_as_float(r[1]);
}

struct FQ { s8 a[4]; s4 b[12]; };
template <bool B> struct BC { static constexpr bool v = B; };
template <int I> struct IC { static constexpr int v = I; };

__global__ __launch_bounds__(512) void fused(const u16* __restrict__ x, const u16* __restrict__ wqkv, const u16* __restrict__ wrt,
                                             char* __restrict__ ws_, const u16* __restrict__ wo_, const u16* __restrict__ wg,
                                             const u16* __restrict__ wu, const u16* __restrict__ wd, u16* __restrict__ out) {
  extern __shared__ __attribute__((aligned(16))) char lds[];
  const char *kx, *kq, *kr, *wo; char* ws;
  asm volatile("s_load_dwordx2 %0, %5, 0\n s_load_dwordx2 %1, %5, 8\n s_load_dwordx2 %2, %5, 16\n s_load_dwordx2 %3, %5, 24\n s_load_dwordx2 %4, %5, 32"
               : "=&s"(kx), "=&s"(kq), "=&s"(kr), "=&s"(ws), "=&s"(wo) : "s"(__builtin_amdgcn_kernarg_segment_ptr()));
  SBAR();
  const int w = __builtin_amdgcn_readfirstlane(threadIdx.x >> 6), l = threadIdx.x & 63;
  STDECL;
  STMP(0);
  const int g = blockIdx.x, xcd = g & 7, j = g >> 3, ii = j >> 2;
  const int mb = (xcd & 1) * 8 + ii, hp = (xcd >> 1) * 4 + (j & 3);
  unsigned cw, cwo = (l * 32 + j) * 64; unsigned long long cwb;
  asm volatile("s_getpc_b64 %2\n.Lcode_here:\n v_min_u32 %1, max(.Lend_c - .Lcode_here, .Lend_l - .Lcode_here, .Lend_h - .Lcode_here, .Lend_s - .Lcode_here) - 4, %1\n"
               "s_cmp_lg_u32 %3, 7\n s_cbranch_scc1 .Lskip_w\n global_load_dword %0, %1, %2\n.Lskip_w:" : "=&v"(cw), "+v"(cwo), "=&s"(cwb) : "s"(w) : "memory", "scc");
  SBAR();
  const int lw = w & 1, rot = 2 * xcd + 4 * ((ii + JX * j) & 3);
  const unsigned ldsb = (unsigned)(size_t)(LAS char*)lds;
  unsigned va[4];
#pragma unroll
  for (int i = 0; i < 4; ++i) va[i] = (8 * i + (l >> 3)) * 2048 + (((l & 7) ^ ((i & 1) * 4 + (l >> 4))) * 16) + (3 - i) * 1024;
  const int ao = (mb * 128 + lw * 64) * 2048 - 3072;
  long long ao0 = ao + (rot & 15) * 128;
  unsigned mba = ldsb + lw * 8192;
  const int srow = mb * 128 + hp * 8 + 2 * w + (l >> 5);
  unsigned xo = srow * 2048 + (l & 31) * 16, ro = (l & 31) * 16;
  asm volatile("" : "+v"(va[0]), "+v"(va[1]), "+v"(va[2]), "+v"(va[3]), "+v"(xo), "+v"(ro));
  asm volatile("s_waitcnt lgkmcnt(0)" : "+s"(kx), "+s"(kq), "+s"(kr), "+s"(ws), "+s"(wo), "+s"(ao0), "+s"(mba));
  SBAR();
  asm volatile("s_sub_u32 m0, %6, 4\n s_cmp_gt_u32 m0, 1\n s_cbranch_scc1 .Lskip_l\n s_mov_b32 m0, %5\n s_nop 0\n global_load_lds_dwordx4 %0, %4\n global_load_lds_dwordx4 %1, %4 offset:1024\n"
               "global_load_lds_dwordx4 %2, %4 offset:2048\n global_load_lds_dwordx4 %3, %4 offset:3072\n.Lskip_l:"
               :: "v"(va[0]), "v"(va[1]), "v"(va[2]), "v"(va[3]), "s"(kx + ao0), "s"(mba), "s"(w) : "memory", "scc");
  u4 xa[4], rw[4]; unsigned ep;
  asm volatile("s_cmp_gt_u32 %14, 3\n s_cbranch_scc1 .Lskip_x\n"
               "global_load_dwordx4 %0, %9, %11\n global_load_dwordx4 %1, %9, %11 offset:512\n global_load_dwordx4 %2, %9, %11 offset:1024\n global_load_dwordx4 %3, %9, %11 offset:1536\n"
               "global_load_dwordx4 %4, %10, %12\n global_load_dwordx4 %5, %10, %12 offset:512\n global_load_dwordx4 %6, %10, %12 offset:1024\n global_load_dwordx4 %7, %10, %12 offset:1536\n"
               ".Lskip_x:\n s_load_dword %8, %13, 0"
               : "=&v"(xa[0]), "=&v"(xa[1]), "=&v"(xa[2]), "=&v"(xa[3]), "=&v"(rw[0]), "=&v"(rw[1]), "=&v"(rw[2]), "=&v"(rw[3]), "=&s"(ep)
               : "v"(xo), "v"(ro), "s"(kx), "s"(kr), "s"(ws), "s"(w) : "memory", "scc");
#define GETTAG(t) unsigned t; asm volatile("s_waitcnt lgkmcnt(0)\n s_add_u32 %0, %1, 1" : "=s"(t) : "s"(ep) : "memory", "scc")
  SBAR();
#define PTAB 0xA98FEDC1045B7623ull
#define PHT 0x6624924999924924ull
#define CX 4
  const bool sp = (hp & 7) == 3;
  const int nb = hp;
#define PAIRAT(p) ((int)(PTAB >> (4 * (p))) & 15)
#define HEADAT(s) (2 * PAIRAT((s) >> 1) + ((s) & 1))
  const int ph = (int)(PHT >> (2 * j)) & 3;
#define HQT 0xDCBA098765320410ull
#define CXA 1
#define CHUNK(b) ((b) < 14 ? 4 * (b) + 3 : ((b) - 14) + ((((b) - 14) * 43) >> 7))
  const unsigned adFL = ldsb + L_FL + 4 * (l & 7), myFL = ldsb + L_FL + 4 * w;
  const __amdgpu_buffer_rsrc_t wse = __builtin_amdgcn_make_buffer_rsrc(ws, 0, 64, 0x00020000);
  const int r15 = l & 15, g4 = l >> 4, q = (l & 15) >> 2, p = l & 3;

  if (w != 4 && w != 5) {
  if (w >= 6) {
    const int mw = w - 6;
    unsigned pf = 0;
    if (PF && !(xcd & 2) && !sp && (mw == 0 || PF == 3 || (PF == 2 && (ii >> 2)))) {
      const unsigned po = (((rot + 2 - 2 * mw + (ii >> 2)) & 15) * 64 + l) * 6144 + (j & 3) * 2048 + (hp | 3) * 128;
      if (PFS) SLEEP(PFS);
      asm volatile("global_load_dword %0, %1, %2" : "+v"(pf) : "v"(po), "s"(kq) : "memory");
    }
    if (PA && mw == 1) {
      const unsigned po = (mb * 128 + (j & 3) * 32 + (l & 31)) * 2048 + (l >> 5) * 1024 + 384;
      if (PAS) SLEEP(PAS);
      asm volatile("global_load_dword %0, %1, %2" : "+v"(pf) : "v"(po), "s"(kx) : "memory");
    }
    GETTAG(tag);
    const int bq = ((int)(HQT >> (4 * hp)) & 15) * 4 + (ii & 3), ksp = (xcd & 1) * 2 + (ii >> 2);
    const int chunk = CHUNK(bq), bg = (CXA || bq < 14) ? bq ^ (w & 1) : bq, chg = CHUNK(bg);
    auto helper = [&](auto SP) {
    constexpr bool spare = decltype(SP)::v, moe = !spare && !NOMOE;
    int nbar = 0;
#define TICK() do { if (HB) { BST(nbar); asm volatile("" : "+v"(BSTV)); } SBAR(); BAR(); SBAR(); ++nbar; } while (0)
#define DTICK(lim) do { if (nbar < (lim)) TICK(); else SLEEP(2); } while (0)
    const int hd = hp * 2 + mw;
    const u4 pz = (u4)(POIS);
    MKWS(tag);
    const int np = l & 7, kset = (l >> 3) & 1;
    u4 gw[4][8], dw[2][8];
    const __amdgpu_buffer_rsrc_t wgr = __builtin_amdgcn_make_buffer_rsrc((void*)(mw ? wu : wg), 0, D * F * 2, 0x00020000);
    const __amdgpu_buffer_rsrc_t wdr = __builtin_amdgcn_make_buffer_rsrc((void*)wd, 0, D * F * 2, 0x00020000);
    const __amdgpu_buffer_rsrc_t wor = __builtin_amdgcn_make_buffer_rsrc((void*)wo, 0, D * D * 2, 0x00020000);
    const unsigned gvo = ((256 * ksp + 32 * kset + 8 * g4) * F + 64 * chg + 8 * np) * 2;
    const unsigned dvo = ((64 * chunk + 8 * g4) * D + 256 * ksp + 128 * mw + 8 * r15) * 2;
    auto burst = [&](int k, int i0 = 0, int i1 = 16) {
      if (moe) {
#pragma unroll
        for (int i = i0; i < i1; ++i) {
          if (k < 2) gw[(2 * k + (i >> 3)) & 3][i & 7] = __builtin_amdgcn_raw_buffer_load_b128(wgr, gvo, (128 * k + 64 * (i >> 3) + (i & 7)) * F * 2, 2);
          else dw[i >> 3][i & 7] = __builtin_amdgcn_raw_buffer_load_b128(wdr, dvo, (32 * (i >> 3) + (i & 7)) * D * 2, 2);
        }
      }
    };
    auto transp = [&](u4(&v)[8]) {
      if (moe) {
        u4 f[8];
#pragma unroll
        for (int c = 0; c < 8; ++c)
#pragma unroll
          for (int e = 0; e < 4; ++e) {
            const unsigned ev = v[2 * e][c >> 1], od = v[2 * e + 1][c >> 1];
            f[c][e] = (c & 1) ? __builtin_amdgcn_perm(od, ev, 0x07060302u) : __builtin_amdgcn_perm(od, ev, 0x05040100u);
          }
#pragma unroll
        for (int c = 0; c < 8; ++c) { v[c] = f[c]; asm volatile("" : "+v"(v[c])); }
      }
    };
    auto pois = [&]() {
      ST128(pz, WS_KVP + (hd * 16 + mb) * 2048 + l * 16); ST128(pz, WS_KVP + (hd * 16 + mb) * 2048 + 1024 + l * 16); ST128(pz, WS_ZP + (hd * 16 + mb) * 128 + (l & 7) * 16);
    };
    unsigned wt0, wt1, wt2 = 0, wt3 = 0;
    auto touch = [&]() {
      const unsigned to = (j * 32 + mw * 16 + (l >> 3)) * 2048 + (xcd >> 1) * 512 + (l & 7) * 64;
      wt0 = __builtin_amdgcn_raw_buffer_load_b32(wor, to, 0, 0); wt1 = __builtin_amdgcn_raw_buffer_load_b32(wor, to, 16384, 0);
      if (TLBW) {
        const __amdgpu_buffer_rsrc_t outr = __builtin_amdgcn_make_buffer_rsrc(out, 0, 2048 * D * 2, 0x00020000);
        wt2 = __builtin_amdgcn_raw_buffer_load_b32(outr, (mb * 128 + mw * 64 + (l & 3) * 16) * 2048 + nb * 128, 0, 0);
        wt3 = __builtin_amdgcn_raw_buffer_load_b32(wsr, (l & 1) ? WS_GP + g * 4096 + mw * 2048 : WS_YP + (g >> 2) * 32768 + (l & 2) * 8192, 0, 0);
      }
    };
    s4 sb[24];
    auto colrd = [&](int h) {
      const unsigned bo = ldsb + (1 + h) * STG + 16384 + (8 * g4 + q) * 128 + (p & 1) * 8;
      const int sw = ((q >> 1) << 2) | ((g4 & 1) << 1);
      const unsigned b0 = bo + (((mw * 4 + (p >> 1)) ^ sw) * 16), b1 = bo + (((mw * 4 + 2 + (p >> 1)) ^ sw) * 16);
#pragma unroll
      for (int n = 0; n < 12; ++n) { RDTR(sb[2 * n], (n & 1) ? b1 : b0, ((n % 6) >> 1) * 8192 + (n / 6) * 4096); RDTR(sb[2 * n + 1], (n & 1) ? b1 : b0, ((n % 6) >> 1) * 8192 + (n / 6) * 4096 + 512); }
      WAITL(0);
      SBAR();
    };
    auto colsum = [&]() {
      const u4 ones = (u4)(0x3F803F80u);
      f4 c[6];
#pragma unroll
      for (int n = 0; n < 12; ++n) c[n % 6] = MFMA16(S8(ones), CAT(sb[2 * n], sb[2 * n + 1]), n >= 6 ? c[n % 6] : (f4)(0.f));
      const u4 csg = (u4){__float_as_uint(g4 ? c[3][0] : c[0][0]), __float_as_uint(g4 ? c[4][0] : c[1][0]), __float_as_uint(g4 ? c[5][0] : c[2][0]), tag};
      WAITV(0);
      asm volatile("" : "+v"(cw) : "v"(pf));
      ST128(csg, l < 32 ? WS_CS + (hd * 16 + mb) * 512 + l * 16 : WS_SZ);
    };
    u4 stv, cv[8], cd; float cs[3];
    auto gload = [&]() {
      stv = LD128(WS_ST + (mb * 128 + mw * 64 + l) * 16);
#pragma unroll
      for (int i = 0; i < 8; ++i) cv[i] = LD128(WS_CS + hd * 8192 + i * 1024 + l * 16);
    };
    auto gbad = [&]() {
      unsigned bad = (stv[1] ^ tag) | (stv[3] ^ tag);
#pragma unroll
      for (int i = 0; i < 8; ++i) bad |= cv[i][3] ^ tag;
      return __ballot(bad != 0u) != 0ull;
    };
    int idx;
    auto gfin = [&]() {
      if (gbad()) { RETRY(); _Pragma("unroll 1") for (unsigned it = 0; it < SPIN; ++it) { SLEEP(2); gload(); if (!gbad()) break; } }
#pragma unroll
      for (int e = 0; e < 3; ++e) {
        float a = __uint_as_float(cv[0][e]);
#pragma unroll
        for (int i = 1; i < 8; ++i) a += __uint_as_float(cv[i][e]);
        auto r = __builtin_amdgcn_permlane32_swap(__float_as_uint(a), __float_as_uint(a), false, false);
        cs[e] = __uint_as_float(r[0]) + __uint_as_float(r[1]);
      }
      asm volatile("" : "+v"(cs[0]), "+v"(cs[1]), "+v"(cs[2]) : "v"(wt0), "v"(wt1), "v"(wt2), "v"(wt3));
    };
    auto cload = [&]() { cd = LD128(WS_CAND + (((hp >> 2) == 2) * 16 + mb) * 128 + (l & 7) * 16); };
    auto cfin = [&]() {
      if (__builtin_expect(__ballot(cd[1] != tag || cd[3] != tag) != 0ull, 0)) {
        RETRY();
        _Pragma("unroll 1") for (unsigned it = 0; it < SPIN; ++it) { SLEEP(4); cload(); if (__ballot(cd[1] != tag || cd[3] != tag) == 0ull) break; }
      }
      cd[0] = max(cd[0], cd[2]);
      idx = (int)(cd[0] & 2047u);
    };
    auto lpub = [&]() {
      *(LAS float*)(lds + L_M + (mw * 64 + l) * 4) = __uint_as_float(stv[0]);
      *(LAS float*)(lds + L_RS + (mw * 64 + l) * 4) = __uint_as_float(stv[2]);
      *(LAS float*)(lds + L_RL + (mw * 64 + l) * 4) = __uint_as_float(stv[2]) * 1.44269504f;
      if (l < 32) {
#pragma unroll
        for (int e = 0; e < 3; ++e) *(LAS float*)(lds + L_CS + mw * 384 + ((3 * g4 + e) * 16 + r15) * 4) = -cs[e];
      }
      if (mw == 0 && l < 8) *(LAS int*)(lds + L_SEL + l * 4) = idx;
      lsig(myFL, 1u);
    };
    u4 xs[4]; float xrs;
    asm volatile("" ::"s"(wg), "s"(wu), "s"(wd), "v"(cw));
    if (HPRIO) __builtin_amdgcn_s_setprio(HPRIO);
    TICK();
    if (spare) {
      TICK();
      pois();
      if (ii >> 2) TICK();
      colrd(ii >> 2);
      if (!(ii >> 2)) TICK();
      u4 kk[8];
      auto kload = [&]() {
#pragma unroll
        for (int i = 0; i < 8; ++i) kk[i] = LD128(WS_RG + mw * 8192 + i * 1024 + l * 16);
      };
      kload();
      TICK();
      _Pragma("unroll 1") for (unsigned it = 0; it < SPIN; ++it) {
        unsigned bad = 0;
#pragma unroll
        for (int i = 0; i < 8; ++i) { bad |= (kk[i][1] ^ tag) | (kk[i][3] ^ tag); asm volatile("" : "+v"(bad)); }
        if (__ballot(bad != 0u) == 0ull) break;
        RETRY();
        kload();
        DTICK(7);
      }
      __builtin_amdgcn_s_setprio(3);
      unsigned m1 = max(kk[0][0], kk[0][2]), m2 = min(kk[0][0], kk[0][2]), m3 = 0;
#pragma unroll
      for (int i = 2; i < 16; ++i) {
        const unsigned a = kk[i >> 1][(i & 1) * 2];
        asm("v_med3_u32 %0, %1, %2, %0" : "+v"(m3) : "v"(m2), "v"(a));
        asm("v_med3_u32 %0, %1, %2, %0" : "+v"(m2) : "v"(m1), "v"(a));
        m1 = max(m1, a);
      }
      unsigned mine8 = 0;
#pragma unroll
      for (int it = 0; it < 8; ++it) {
        const unsigned km = wave_max(m1);
        mine8 = l == it ? km : mine8;
        const bool win = m1 == km;
        if (it < 7) { m1 = win ? m2 : m1; m2 = win ? m3 : m2; m3 = win ? 0u : m3; }
        if (it == 3 && nbar < 7) TICK();
      }
      if (__builtin_expect(__ballot(m1 == 0u) != 0ull, 0)) {
        unsigned lim = 0;
#pragma unroll 1
        for (int it = 0; it < 8; ++it) {
          unsigned m = 0;
#pragma unroll
          for (int i = 0; i < 8; ++i) m = umax3(m, kk[i][0] - lim, kk[i][2] - lim);
          lim += wave_max(m);
          mine8 = l == it ? lim : mine8;
        }
      }
      ST64(((u2){mine8, tag}), l < 8 ? WS_CAND + ((hp >> 3) * 16 + mb) * 128 + (mw ? 7 - l : l) * 16 + mw * 8 : WS_SZ);
      __builtin_amdgcn_s_setprio(HPRIO);
      STMP(2);
      TICK();
      colsum();
      DTICK(9);
      touch();
      while (nbar < 10) TICK();
      gload(); cload();
      if (RT && mw) asm volatile("global_load_dword %0, %1, %2 sc1" : "+v"(cw) : "v"(cwo), "s"(cwb) : "memory");
      TICK(); TICK();
      WAITV(0);
      asm volatile("" : "+v"(cw));
      gfin(); cfin();
      STMP(3);
      TICK(); TICK(); TICK();
      lpub();
    } else {
      if (PV == 0) pois();
      TICK();
      if (PV == 1) pois();
      if (ii >> 2) TICK();
      colrd(ii >> 2);
      TICK();
      if (CL && !(ii >> 2)) TICK();
      colsum();
      if (!CL && !(ii >> 2)) TICK();
      u4 sv; f4 acc[8];
      auto xload = [&]() {
        const u16* xp = x + (size_t)idx * D + 256 * ksp + 32 * kset + 8 * g4;
        sv = LD128(WS_ST + idx * 16);
#pragma unroll
        for (int it = 0; it < 4; ++it) xs[it] = *(const u4*)(xp + 64 * it);
        SBAR();
      };
      auto xnorm = [&]() {
        if (__builtin_expect(__ballot(sv[1] != tag || sv[3] != tag) != 0ull, 0)) {
          _Pragma("unroll 1") for (unsigned it = 0; it < SPIN; ++it) { SLEEP(1); sv = LD128(WS_ST + idx * 16); if (__ballot(sv[1] != tag || sv[3] != tag) == 0ull) break; }
        }
        const float nm = -__uint_as_float(sv[0]);
        xrs = __uint_as_float(sv[2]);
#pragma unroll
        for (int it = 0; it < 4; ++it) {
          float t0, t1, t2, t3; unsigned x0 = xs[it][0], x1 = xs[it][1], x2 = xs[it][2], x3 = xs[it][3];
          asm volatile("v_dot2_f32_bf16 %4, %0, %8, %10\n v_dot2_f32_bf16 %0, %0, %9, %10\n v_dot2_f32_bf16 %5, %1, %8, %10\n v_dot2_f32_bf16 %1, %1, %9, %10\n"
                       "v_dot2_f32_bf16 %6, %2, %8, %10\n v_dot2_f32_bf16 %2, %2, %9, %10\n v_dot2_f32_bf16 %7, %3, %8, %10\n v_dot2_f32_bf16 %3, %3, %9, %10\n"
                       "v_cvt_pk_bf16_f32 %0, %4, %0\n v_cvt_pk_bf16_f32 %1, %5, %1\n v_cvt_pk_bf16_f32 %2, %6, %2\n s_nop 0\n v_cvt_pk_bf16_f32 %3, %7, %3"
                       : "+v"(x0), "+v"(x1), "+v"(x2), "+v"(x3), "=&v"(t0), "=&v"(t1), "=&v"(t2), "=&v"(t3) : "s"(0x00003F80u), "s"(0x3F800000u), "v"(nm));
          xs[it] = (u4){x0, x1, x2, x3};
        }
        asm volatile("s_nop 1" : "+v"(xs[0]), "+v"(xs[1]), "+v"(xs[2]), "+v"(xs[3]), "+v"(xrs));
        SBAR();
      };
      auto gmul = [&](int i0, int i1) {
#pragma unroll
        for (int it = i0; it < i1; ++it)
#pragma unroll
          for (int c = 0; c < 8; ++c) acc[c] = MFMA16(S8(gw[it][c]), S8(xs[it]), it ? acc[c] : (f4)(0.f));
#pragma unroll
        for (int c = 0; c < 8; ++c) asm volatile("" : "+v"(acc[c]));
        SBAR();
      };
      auto gout = [&]() {
        unsigned go = r15 < 8 ? WS_GP + ((chg * 4 + ksp) * 2 + mw) * 2048 + (g4 >> 1) * 1024 + (g4 & 1) * 512 + r15 * 16 : WS_SZ;
        asm volatile("" : "+v"(go));
#pragma unroll
        for (int i = 0; i < 4; ++i) {
          unsigned a[4], b[4];
#pragma unroll
          for (int c = 0; c < 4; ++c) {
            auto r = __builtin_amdgcn_permlane32_swap(__float_as_uint(acc[c][i]), __float_as_uint(acc[c + 4][i]), false, false);
            a[c] = r[0]; b[c] = r[1];
          }
          unsigned s0, s1, s2, s3;
#define AR(d, x, y) "v_add_f32_dpp %" #d ", %" #x ", %" #y " row_ror:8 row_mask:0xf bank_mask:0xf\n"
          asm volatile("s_nop 1\n" AR(0, 8, 4) AR(1, 9, 5) AR(2, 10, 6) AR(3, 11, 7) : "=&v"(s0), "=&v"(s1), "=&v"(s2), "=&v"(s3)
                       : "v"(a[0]), "v"(a[1]), "v"(a[2]), "v"(a[3]), "v"(b[0]), "v"(b[1]), "v"(b[2]), "v"(b[3]));
          ST128(tagf(__builtin_bit_cast(f4, ((u4){s0, s1, s2, s3})), tag), go + i * 128);
        }
        SBAR();
      };
      for (int i = 0; i < ph; ++i) TICK();
      burst(0); touch();
      TICK();
      TICK();
      if (TL) { transp(gw[0]); transp(gw[1]); }
      TICK();
      if (!TL) transp(gw[0]);
      TICK();
      if (!TL) transp(gw[1]);
      TICK();
      if (ph) cload();
      if (RT && mw) asm volatile("global_load_dword %0, %1, %2 sc1" : "+v"(cw) : "v"(cwo), "s"(cwb) : "memory");
      SBAR();
      burst(1);
      TICK();
      TICK(); TICK();
      transp(gw[2]);
      WAITV(0);
      asm volatile("" : "+v"(cw));
      if (ph < 2) TICK();
      transp(gw[3]);
      if (ph < 1) { SBAR(); cload(); TICK(); }
      SBAR();
      if (XO == 0) { cfin(); xload(); gload(); } else { gload(); SBAR(); cfin(); xload(); }
      TICK();
      gfin();
      lpub();
      STMP(12);
      SBAR();
      __builtin_amdgcn_s_setprio(0);
      burst(2, 0, D1);
      STMP(3);
      SBAR();
      if (HS0) SLEEP(HS0);
      if (G1 & 1) { lwait<HW1>(adFL, 0x0Fu, G1 >> 4); if (G1S) SLEEP(G1S); __builtin_amdgcn_s_setprio(TP0); }
      xnorm();
      if (G1 & 2) { lwait<2>(adFL, 0x0Fu, G1 >> 4); if (G1S) SLEEP(G1S); __builtin_amdgcn_s_setprio(TP0); }
      STMP(5);
      gmul(0, 4);
      gout();
      STMP(6);
      SBAR();
    }
    __builtin_amdgcn_s_setprio(TP1);
    STMP(4);
    if (moe) {
      if (D2 == 0) { burst(2, D1, 16); SBAR(); }
      if (D1 >= 8 || D2 == 0) transp(dw[0]); if (D1 == 16 || D2 == 0) transp(dw[1]);
      SBAR();
      lwait<2>(adFL, 0x30u, 3);
      if (G2S) SLEEP(G2S);
      STMP(7);
      if (D2 == 1) { burst(2, D1, 16); SBAR(); }
      f4 acc[8];
      u4 ha[2];
      {
        u4 pg[4][2];
        auto pload = [&]() {
#pragma unroll
          for (int ks = 0; ks < 4; ++ks)
#pragma unroll
            for (int t = 0; t < 2; ++t) pg[ks][t] = LD128(WS_GP + ((chunk * 4 + ks) * 4 + 2 * t + mw) * 1024 + (4 * kset + g4) * 128 + np * 16);
        };
        auto pbad = [&]() {
          unsigned bad = 0;
#pragma unroll
          for (int i = 0; i < 8; ++i) untag(pg[i >> 1][i & 1], tag, bad);
          return __ballot((bad & 255u) != 0u) != 0ull;
        };
        pload();
        SBAR();
        if (D2 == 2) { burst(2, D1, 16); SBAR(); }
        __builtin_amdgcn_s_setprio(TP2);
        if (D1 < 8 && D2 == 1) transp(dw[0]);
        if (D1 < 16 && D2 == 1) transp(dw[1]);
        SBAR();
        if (__builtin_expect(pbad(), 0)) { _Pragma("unroll 1") for (unsigned it = 0; it < SPIN; ++it) { RETRY2(); SLEEP(4); pload(); if (!pbad()) break; } }
        STMP(10);
        f4 gq[2];
#pragma unroll
        for (int t = 0; t < 2; ++t)
          gq[t] = ((__builtin_bit_cast(f4, pg[0][t]) + __builtin_bit_cast(f4, pg[1][t])) + (__builtin_bit_cast(f4, pg[2][t]) + __builtin_bit_cast(f4, pg[3][t]))) * xrs;
        float hv[4];
#pragma unroll
        for (int e = 0; e < 4; ++e) {
          const float gg = rbf(gq[0][e]), uu = rbf(gq[1][e]);
          hv[e] = rbf(gg / (1.f + __expf(-gg))) * uu;
        }
        *(LAS u2*)(lds + L_HX + l * 16 + mw * 8) = (u2){pk2(hv[0], hv[1]), pk2(hv[2], hv[3])};
        lsig(myFL, 2u);
        if (D1 < 16 && D2 == 2) { SBAR(); if (D1 < 8) transp(dw[0]); transp(dw[1]); SBAR(); }
        lwait(adFL, 0xC0u, 2);
        ha[0] = *(const LAS u4*)(lds + L_HX + l * 16); ha[1] = *(const LAS u4*)(lds + L_HX + (l ^ 8) * 16);
      }
#pragma unroll
      for (int it = 0; it < 2; ++it)
#pragma unroll
        for (int c = 0; c < 8; ++c) acc[c] = MFMA16(S8(ha[it]), S8(dw[it][c]), it ? acc[c] : (f4)(0.f));
      if (g4 < 2) {
#pragma unroll
        for (int i = 0; i < 4; ++i) {
          const unsigned yo = WS_YP + ((chunk * 8 + 4 * g4 + i) * D + 256 * ksp + 128 * mw + 4 * r15) * 4;
          ST128(tagf((f4){acc[0][i], acc[1][i], acc[2][i], acc[3][i]}, tag), yo);
          ST128(tagf((f4){acc[4][i], acc[5][i], acc[6][i], acc[7][i]}, tag), yo + 256);
        }
      }
      STMP(11);
    }
    float sg;
    {
      const unsigned mono = cd[0] >> 16, rb = (mono & 0x8000u) ? (mono & 0x7FFFu) : (~mono & 0xFFFFu);
      sg = rbf(1.f / (1.f + __expf(-lo(rb))));
    }
    unsigned mine = (unsigned)__ballot(!NOMOE && l < 8 && (idx >> 7) == mb && (l & 1) == mw);
    if (mine) {
      if (spare) lwait<4>(adFL, 0x30u, 3);
      {
        const u2 f = LD64(WS_OH + (mb * 16 + r15) * 128);
        const unsigned t0 = NOW();
        unsigned rel = f[1] + YD - t0;
        rel = wave_max(f[0] == tag && rel < 600u ? rel : 0u);
        _Pragma("unroll 1") for (unsigned it = 0; it < SPIN; ++it) { if (NOW() - t0 >= rel) break; SLEEP(2); }
      }
      STMP(8);
      lwait<1>(adFL, 0x0Fu, 4);
      __builtin_amdgcn_s_setprio(TP2);
      while (mine) {
        const int t = __builtin_ctz(mine); mine &= mine - 1;
        u4 yv[14];
        _Pragma("unroll 1") for (unsigned it = 0; it < SPIN; ++it) {
          unsigned bad = 0;
#pragma unroll
          for (int i = 0; i < 14; ++i) yv[i] = LD128(WS_YP + (((i * 4 + g4) * 8 + t) * D + (nb >> 1) * 128 + (r15 >> 3) * 64 + (nb & 1) * 32 + (r15 & 7) * 4) * 4);
          SBAR();
#pragma unroll
          for (int i = 0; i < 14; ++i) untag(yv[i], tag, bad);
          if (__ballot((bad & 255u) != 0u) == 0ull) break;
          RETRY();
          SLEEP(YSL);
        }
        f4 y = __builtin_bit_cast(f4, yv[0]);
#pragma unroll
        for (int i = 1; i < 14; ++i) y += __builtin_bit_cast(f4, yv[i]);
        const float gt = __int_as_float(__builtin_amdgcn_readlane(__float_as_int(sg), t));
#pragma unroll
        for (int e = 0; e < 4; ++e) {
          auto r = __builtin_amdgcn_permlane32_swap(__float_as_uint(y[e]), __float_as_uint(y[e]), false, false);
          const float a = __uint_as_float(r[0]) + __uint_as_float(r[1]);
          auto r2 = __builtin_amdgcn_permlane16_swap(__float_as_uint(a), __float_as_uint(a), false, false);
          y[e] = rbf(rbf(__uint_as_float(r2[0]) + __uint_as_float(r2[1])) * gt);
        }
        if (l < 16) *(LAS f4*)(lds + L_KVX + t * 256 + (l & 7) * 32 + (l >> 3) * 16) = y;
      }
    }
    lsig(myFL, 3u);
    STMP(9);
    STFLUSH();
    if (spare) asm volatile(".Lend_s:"); else asm volatile(".Lend_h:");
    };
    if (sp) helper(BC<true>()); else helper(BC<false>());
    return;
  }

  const int wr = w >> 1, wc = w & 1;
  u2 kg; u4 sg4; unsigned tag;
  {
    const int row = srow;
    SBAR(); BST(0); SBAR();
    asm volatile("s_waitcnt vmcnt(4)" : "+v"(xa[0]), "+v"(xa[1]), "+v"(xa[2]), "+v"(xa[3]));
    SBAR(); BST(1); SBAR();
    float s0 = 0, s1 = 0;
#pragma unroll
    for (int c = 0; c < 4; ++c)
#pragma unroll
      for (int e = 0; e < 4; e += 2) { unsigned x0 = xa[c][e], x1 = xa[c][e + 1]; s0 = dot2(x0, 0x3F803F80u, s0); s1 = dot2(x1, 0x3F803F80u, s1); }
    const float m = half_sum(s0 + s1) * (1.f / D);
    f2 v[16], q2 = (f2)(0.f), q3 = (f2)(0.f);
#pragma unroll
    for (int c = 0; c < 16; ++c) v[c] = (f2){lo(xa[c >> 2][c & 3]), hi(xa[c >> 2][c & 3])} - m;
#pragma unroll
    for (int c = 0; c < 16; c += 2) { q2 += v[c] * v[c]; q3 += v[c + 1] * v[c + 1]; }
    q2 += q3;
    const float mr = rbf(m);
    const float rs = 1.f / rbf(sqrtf(rbf(rbf(half_sum(q2[0] + q2[1]) * (1.f / D)) + 1e-5f)));
    float d0 = 0, d1 = 0;
    asm volatile("s_waitcnt vmcnt(0)" : "+v"(rw[0]), "+v"(rw[1]), "+v"(rw[2]), "+v"(rw[3]), "+v"(d0) : "v"(rs));
#pragma unroll
    for (int c = 0; c < 16; c += 2) {
      const f2 e0 = (f2){lo(xa[c >> 2][c & 3]), hi(xa[c >> 2][c & 3])} - mr, e1 = (f2){lo(xa[c >> 2][(c & 3) + 1]), hi(xa[c >> 2][(c & 3) + 1])} - mr;
      const unsigned p0 = pk2(e0[0], e0[1]), p1 = pk2(e1[0], e1[1]);
      const f2 n0 = (f2){lo(p0), hi(p0)} * rs, n1 = (f2){lo(p1), hi(p1)} * rs;
      unsigned c0 = rw[c >> 2][c & 3], c1 = rw[c >> 2][(c & 3) + 1];
      d0 = dot2(pk2(n0[0], n0[1]), c0, d0); d1 = dot2(pk2(n1[0], n1[1]), c1, d1);
    }
    const float d = half_sum(d0 + d1);
    const unsigned rb = pk2(d, d) & 0xFFFFu;
    const unsigned mono = (rb & 0x8000u) ? (~rb & 0xFFFFu) : (rb | 0x8000u);
    SBAR();
    GETTAG(tg); tag = tg;
    kg = (u2){(mono << 16) | (unsigned)row, tag};
    sg4 = (u4){__float_as_uint(mr), tag, __float_as_uint(rs), tag};
  }
  MKWS(tag);
  SBAR(); BST(2); SBAR();
  if ((l & 31) == 0) { ST64(kg, WS_RG + srow * 8); ST128(sg4, WS_ST + srow * 16); }
  unsigned aad[2], bad[2];
#pragma unroll
  for (int s = 0; s < 2; ++s) aad[s] = ldsb + (wr * 64 + r15) * 128 + (((4 * s + g4) ^ ((r15 >> 1) & 7)) * 16);
#pragma unroll
  for (int nh = 0; nh < 2; ++nh) bad[nh] = ldsb + 16384 + (8 * g4 + q) * 128 + (((wc * 4 + 2 * (nh ^ wr) + (p >> 1)) ^ (((q >> 1) << 2) | ((g4 & 1) << 1))) * 16) + (p & 1) * 8;
  f4 acc[4][6];
#pragma unroll
  for (int m = 0; m < 4; ++m)
#pragma unroll
    for (int n = 0; n < 6; ++n) { acc[m][n] = (f4)(0.f); asm volatile("" : "+v"(acc[m][n])); }
#define MF(m, n, Fq) do { if (n < 2) acc[m][n] = MFMA16(CAT(Fq.b[2 * n], Fq.b[2 * n + 1]), Fq.a[m], acc[m][n]); \
     else acc[m][n] = MFMA16(Fq.a[m], CAT(Fq.b[2 * n], Fq.b[2 * n + 1]), acc[m][n]); SBAR(); } while (0)
#define RB(G, n, vb0, vb1, SS, hf) RDTR(G.b[2 * (n) + (hf)], (((n) & 1) ? vb1 : vb0), ((n) >> 1) * 8192 + (SS) * 4096 + (hf) * 512)
#define RA(G, m, va) RD128(G.a[m], va, (m) * 2048)
#define SUB(Fq, G, va, vb0, vb1, SS, PRE) do { \
    WAITL(13); SBAR(); \
    MF(0, 0, Fq); PRE; RA(G, 0, va); SBAR(); \
    WAITL(11); SBAR(); \
    MF(1, 0, Fq); RB(G, 0, vb0, vb1, SS, 0); SBAR(); MF(2, 0, Fq); RB(G, 0, vb0, vb1, SS, 1); SBAR(); MF(3, 0, Fq); RA(G, 1, va); SBAR(); \
    WAITL(12); SBAR(); \
    MF(0, 1, Fq); RA(G, 2, va); SBAR(); MF(1, 1, Fq); RA(G, 3, va); SBAR(); MF(2, 1, Fq); RB(G, 1, vb0, vb1, SS, 0); SBAR(); MF(3, 1, Fq); RB(G, 1, vb0, vb1, SS, 1); SBAR(); \
    WAITL(8); SBAR(); \
    MF(0, 2, Fq); RB(G, 2, vb0, vb1, SS, 0); SBAR(); MF(1, 2, Fq); RB(G, 2, vb0, vb1, SS, 1); SBAR(); MF(2, 2, Fq); RB(G, 3, vb0, vb1, SS, 0); SBAR(); MF(3, 2, Fq); RB(G, 3, vb0, vb1, SS, 1); SBAR(); \
    MF(0, 3, Fq); RB(G, 4, vb0, vb1, SS, 0); SBAR(); MF(1, 3, Fq); RB(G, 4, vb0, vb1, SS, 1); SBAR(); MF(2, 3, Fq); RB(G, 5, vb0, vb1, SS, 0); SBAR(); MF(3, 3, Fq); RB(G, 5, vb0, vb1, SS, 1); SBAR(); \
    MF(0, 4, Fq); MF(1, 4, Fq); MF(2, 4, Fq); MF(3, 4, Fq); MF(0, 5, Fq); MF(1, 5, Fq); MF(2, 5, Fq); MF(3, 5, Fq); } while (0)
#define KSTEP(MODE) do { \
    if (kt == sx) { const unsigned xo = xad + buf * STG; \
      _Pragma("unroll") for (int t = 0; t < 8; ++t) { const unsigned a_ = xo ^ (t * 16); \
        asm volatile("ds_read_b64 %0, %1" : "+v"(xv[t]) : "v"(a_) : "memory"); } \
      WAITL(0); SBAR(); } \
    const int nbuf = (buf + 1) & 3; \
    const unsigned bo = buf * STG, nbo = nbuf * STG; \
    { unsigned va = aad[1] + bo, vb0 = bad[0] + bo, vb1 = bad[1] + bo; SUB(f0, f1, va, vb0, vb1, 1, ); } \
    if (MODE < 2) { \
      unsigned va = aad[0] + nbo, vb0 = bad[0] + nbo, vb1 = bad[1] + nbo; \
      SUB(f1, f0, va, vb0, vb1, 0, if (MODE == 0) { WAITL(0); BAR(); } else asm volatile("ds_write_b32 %0, %1" ::"v"(myFL), "v"(1u) : "memory"); SBAR()); \
    } else { asm volatile("ds_write_b32 %1, %2\n ds_read_b32 %0, %3\n s_waitcnt lgkmcnt(2)" : "=&v"(fl0) : "v"(myFL), "v"(2u), "v"(adFL) : "memory"); SBAR(); \
      MF(0, 2, f1); MF(1, 2, f1); MF(2, 2, f1); MF(3, 2, f1); MF(0, 3, f1); MF(1, 3, f1); MF(2, 3, f1); MF(3, 3, f1); \
      MF(0, 4, f1); MF(1, 4, f1); MF(2, 4, f1); MF(3, 4, f1); MF(0, 5, f1); MF(1, 5, f1); MF(2, 5, f1); MF(3, 5, f1); \
      MF(0, 0, f1); MF(1, 0, f1); MF(2, 0, f1); MF(3, 0, f1); MF(0, 1, f1); MF(1, 1, f1); MF(2, 1, f1); MF(3, 1, f1); } \
    buf = nbuf; } while (0)
#define SUBK(Fq, G, va, vb0, vb1, SS, W0, PRE) do { \
    WAITL(W0); SBAR(); \
    MF(0, 2, Fq); PRE; RA(G, 0, va); SBAR(); \
    WAITL(7); SBAR(); \
    MF(1, 2, Fq); RB(G, 2, vb0, vb1, SS, 0); SBAR(); MF(2, 2, Fq); RB(G, 2, vb0, vb1, SS, 1); SBAR(); MF(3, 2, Fq); RA(G, 1, va); SBAR(); \
    WAITL(8); SBAR(); \
    MF(0, 3, Fq); RA(G, 2, va); SBAR(); MF(1, 3, Fq); RA(G, 3, va); SBAR(); MF(2, 3, Fq); RB(G, 3, vb0, vb1, SS, 0); SBAR(); MF(3, 3, Fq); RB(G, 3, vb0, vb1, SS, 1); SBAR(); \
    WAITL(10); SBAR(); \
    MF(0, 4, Fq); RB(G, 4, vb0, vb1, SS, 0); SBAR(); MF(1, 4, Fq); RB(G, 4, vb0, vb1, SS, 1); SBAR(); MF(2, 4, Fq); RB(G, 5, vb0, vb1, SS, 0); SBAR(); MF(3, 4, Fq); RB(G, 5, vb0, vb1, SS, 1); SBAR(); \
    WAITL(12); SBAR(); \
    MF(0, 5, Fq); MF(1, 5, Fq); MF(2, 5, Fq); MF(3, 5, Fq); } while (0)
#define RQ(G, va, vb0, vb1, SS) do { RA(G, 0, va); RB(G, 0, vb0, vb1, SS, 0); RB(G, 0, vb0, vb1, SS, 1); RA(G, 1, va); RA(G, 2, va); RA(G, 3, va); \
    RB(G, 1, vb0, vb1, SS, 0); RB(G, 1, vb0, vb1, SS, 1); SBAR(); } while (0)
#define MQ(Fq) do { MF(0, 0, Fq); MF(1, 0, Fq); MF(2, 0, Fq); MF(3, 0, Fq); MF(0, 1, Fq); MF(1, 1, Fq); MF(2, 1, Fq); MF(3, 1, Fq); } while (0)
  u2 xv[8];
#pragma unroll
  for (int t = 0; t < 8; ++t) asm volatile("" : "=v"(xv[t]));
  const int sx = (nb - rot) & 15;
  const unsigned xad = ldsb + (w * 32 + (l & 31)) * 128 + ((r15 >> 1) & 7) * 16 + (l >> 5) * 8;
  STMP(1);
  WAITL(0);
  BAR();
  STMP(2);
  WAITL(0);
  FQ f0, f1;
  { unsigned va = aad[0], vb0 = bad[0], vb1 = bad[1];
    RA(f0, 0, va); RB(f0, 0, vb0, vb1, 0, 0); RB(f0, 0, vb0, vb1, 0, 1); RA(f0, 1, va); RA(f0, 2, va); RA(f0, 3, va);
    RB(f0, 1, vb0, vb1, 0, 0); RB(f0, 1, vb0, vb1, 0, 1); RB(f0, 2, vb0, vb1, 0, 0); RB(f0, 2, vb0, vb1, 0, 1); RB(f0, 3, vb0, vb1, 0, 0); RB(f0, 3, vb0, vb1, 0, 1);
    RB(f0, 4, vb0, vb1, 0, 0); RB(f0, 4, vb0, vb1, 0, 1); RB(f0, 5, vb0, vb1, 0, 0); RB(f0, 5, vb0, vb1, 0, 1); SBAR(); }
  unsigned sad = ldsb + L_M + wr * 256 + g4 * 16, cad = ldsb + L_CS + wc * 384 + r15 * 4 + wr * 64;
  const int hd = hp * 2 + wc;
  unsigned kvo = WS_KVP + (hd * 16 + mb) * 2048 + wr * 1024 + l * 16, zo = r15 == 0 ? WS_ZP + (hd * 16 + mb) * 128 + g4 * 32 + wr * 16 : WS_SZ;
  unsigned xw = ldsb + L_KVX + w * 3072 + l * 16, xr = ldsb + L_KVX + (w ^ 2) * 3072 + l * 16;
  asm volatile("s_waitcnt lgkmcnt(0)" : "+v"(sad), "+v"(cad), "+v"(kvo), "+v"(zo), "+v"(xw), "+v"(xr) : : "memory"); SBAR();
  int buf = 0; unsigned fl0;
#pragma unroll 1
  for (int kt = 0; kt < 14; ++kt) KSTEP(0);
  STMP(14); SBAR();
  f4 sm[4], sr[4], sl[4]; f2 ck, cv;
  unsigned c2 = 2u, c3 = 3u, vfl = myFL, b30, b31;
  u4 ones = (u4)(0x3F803F80u);
#define SC0() asm volatile("ds_read_b32 %0, %2 offset:128\n ds_read_b32 %1, %2 offset:256" : "=&v"(ck[0]), "=&v"(cv[0]) : "v"(cad) : "memory")
#define SC1() asm volatile("ds_read_b32 %0, %2 offset:128\n ds_read_b32 %1, %2 offset:256" : "=&v"(ck[1]), "=&v"(cv[1]) : "v"(cad ^ 64) : "memory")
  const unsigned b20 = bad[0] + 2 * STG, b21 = bad[1] + 2 * STG;
#define SM(m) do { RD128(sm[m], sad, (m) * 64); RD128(sl[m], sad, L_RL - L_M + (m) * 64); RD128(sr[m], sad, L_RS - L_M + (m) * 64); } while (0)
  {
    { const unsigned va = aad[1] + 2 * STG; SUBK(f0, f1, va, b20, b21, 1, 6, asm volatile("" ::"v"(f0.b[0]), "v"(f0.b[1]), "v"(f0.b[2]), "v"(f0.b[3]))); }
    { const unsigned va = aad[0] + 3 * STG; b30 = bad[0] + 3 * STG; b31 = bad[1] + 3 * STG; SUBK(f1, f0, va, b30, b31, 0, 9, asm volatile("" : "+v"(c2), "+v"(c3), "+v"(vfl))); }
    STMP(7); SBAR();
    { const unsigned va = aad[1] + 3 * STG; SUBK(f0, f1, va, b30, b31, 1, 9, asm volatile("" : "+v"(ones))); }
    asm volatile("ds_write_b32 %1, %2\n ds_read_b32 %0, %3\n s_waitcnt lgkmcnt(8)" : "=&v"(fl0) : "v"(vfl), "v"(c2), "v"(adFL) : "memory"); SBAR();
    MF(0, 2, f1); SC0(); SBAR(); MF(1, 2, f1); SC1(); SBAR(); MF(2, 2, f1); MF(3, 2, f1); WAITL(10); SBAR();
    MF(0, 3, f1); SM(0); SBAR(); MF(1, 3, f1); MF(2, 3, f1); SM(1); SBAR(); MF(3, 3, f1);
    WAITL(12); SBAR();
    MF(0, 4, f1); SM(2); SBAR(); MF(1, 4, f1); MF(2, 4, f1); SM(3); SBAR(); MF(3, 4, f1); MF(0, 5, f1); MF(1, 5, f1); MF(2, 5, f1); MF(3, 5, f1);
  }
  asm volatile("s_waitcnt lgkmcnt(0)" : "+v"(fl0) : : "memory");
  if (__builtin_expect(((unsigned)__ballot(fl0 < 1u) & 0xC0u) != 0u, 0)) {
    lwait(adFL, 0xC0u, 1);
    SC0(); SC1(); SM(0); SM(1); SM(2); SM(3); WAITL(0);
  }
  asm volatile("" : "+v"(ck), "+v"(cv), "+v"(sm[0]), "+v"(sm[1]), "+v"(sm[2]), "+v"(sm[3]), "+v"(sr[0]), "+v"(sr[1]), "+v"(sr[2]), "+v"(sr[3]));
  asm volatile("" : "+v"(sl[0]), "+v"(sl[1]), "+v"(sl[2]), "+v"(sl[3]));
  STMP(3);
  __builtin_amdgcn_s_setprio(2);
  {
    f4 kv[4], zt[2];
    u4 kf[2][2], vf[2][2];
#define MKV(t, i) do { if ((i) == 2 || (i) == 5) zt[(i) == 2] = MFMA16(S8(kf[t][(i) == 2]), S8(ones), (t) ? zt[(i) == 2] : (f4)(0.f)); \
      else kv[((i) + 2) % 5] = MFMA16(S8(kf[t][(((i) + 2) % 5) & 1]), S8(vf[t][(((i) + 2) % 5) >> 1]), (t) ? kv[((i) + 2) % 5] : (f4)(0.f)); SBAR(); } while (0)
#pragma unroll
    for (int m = 0; m < 4; ++m) {
      const int t = m >> 1, mm = m & 1;
      f4 ea[2], pp[2];
#pragma unroll
      for (int a = 0; a < 2; ++a) {
        const f4 uv = (acc[m][4 + a] + sm[m] * cv[a]) * sr[m];
        vf[t][a][2 * mm] = pk2(uv[0], uv[1]); vf[t][a][2 * mm + 1] = pk2(uv[2], uv[3]);
        const f4 uk = acc[m][2 + a] + sm[m] * ck[a];
        ea[a] = uk * sl[m]; pp[a] = uk * sr[m] + 1.f;
      }
      SBAR();
#pragma unroll
      for (int e = 0; e < 8; ++e) { if (t && (e == 0 || e == 3 || e == 6)) MKV(0, 3 * mm + e / 3); ea[e >> 2][e & 3] = __builtin_amdgcn_exp2f(ea[e >> 2][e & 3]); SBAR(); }
#pragma unroll
      for (int e = 0; e < 8; ++e) pp[e >> 2][e & 3] = __builtin_amdgcn_fmed3f(ea[e >> 2][e & 3], pp[e >> 2][e & 3], 1.f);
      SBAR();
#pragma unroll
      for (int a = 0; a < 2; ++a) { pp[a] += 1e-6f; kf[t][a][2 * mm] = pk2(pp[a][0], pp[a][1]); kf[t][a][2 * mm + 1] = pk2(pp[a][2], pp[a][3]); }
      SBAR();
    }
    MKV(1, 0); MKV(1, 1); MKV(1, 2); MKV(1, 3);
    asm volatile("ds_write_b128 %0, %1" ::"v"(xw), "v"(kv[2]) : "memory"); SBAR();
    MKV(1, 4);
    asm volatile("ds_write_b128 %0, %1 offset:1024" ::"v"(xw), "v"(kv[3]) : "memory"); SBAR();
    MKV(1, 5);
    asm volatile("ds_write_b128 %0, %1 offset:2048\n ds_write_b32 %2, %3" ::"v"(xw), "v"(zt[1]), "v"(vfl), "v"(c3) : "memory");
    f4 r0, r1, rz;
    _Pragma("unroll 1") for (unsigned it = 0; it < SPIN; ++it) {
      unsigned v;
      asm volatile("ds_read_b32 %0, %4\n ds_read_b128 %1, %5 offset:1024\n ds_read_b128 %2, %5\n ds_read_b128 %3, %5 offset:2048\n s_waitcnt lgkmcnt(0)"
                   : "=&v"(v), "=&v"(r0), "=&v"(r1), "=&v"(rz) : "v"(adFL), "v"(xr) : "memory");
      if ((((unsigned)__ballot(v < 3u) >> (w ^ 2)) & 1u) == 0u) break;
    }
    const f4 s0 = kv[0] + r0, s1 = kv[1] + r1, sz = zt[0] + rz;
    u4 o; o[0] = min(pk2(s0[0], s0[1]), POIS - 1u); o[1] = pk2(s0[2], s0[3]); o[2] = pk2(s1[0], s1[1]); o[3] = pk2(s1[2], s1[3]);
    ST128(o, kvo);
    u4 zq = __builtin_bit_cast(u4, sz); zq[0] = min(zq[0], POIS - 1u);
    ST128(zq, zo);
  }
  STMP(4);
  {
    SBAR();
    const LAS float* Lm = (const LAS float*)(lds + L_M) + wr * 64 + r15;
    const LAS float* Lc = (const LAS float*)(lds + L_CS) + wc * 96 + 4 * g4;
    { const unsigned va = aad[0] + 2 * STG; RQ(f0, va, b20, b21, 0); }
    { const unsigned va = aad[1] + 2 * STG; RQ(f1, va, b20, b21, 1); }
    WAITL(8); SBAR(); MQ(f0);
    { const unsigned va = aad[0] + 3 * STG; RQ(f0, va, b30, b31, 0); }
    WAITL(8); SBAR(); MQ(f1);
    { const unsigned va = aad[1] + 3 * STG; RQ(f1, va, b30, b31, 1); }
    WAITL(8); SBAR();
    const f4 c0 = *(const LAS f4*)(Lc + 16 * wr), c1 = *(const LAS f4*)(Lc + 16 - 16 * wr);
    float mv[4], rv[4], rl[4];
#pragma unroll
    for (int m = 0; m < 4; ++m) { mv[m] = Lm[m * 16]; rv[m] = Lm[m * 16 + (L_RS - L_M) / 4]; rl[m] = Lm[m * 16 + (L_RL - L_M) / 4]; }
    SBAR(); MQ(f0);
    if (sx >= 14) {
      const unsigned xo = xad + (sx - 12) * STG;
#pragma unroll
      for (int t = 0; t < 8; ++t) { const unsigned a_ = xo ^ (t * 16); asm volatile("ds_read_b64 %0, %1" : "+v"(xv[t]) : "v"(a_) : "memory"); }
    }
    WAITL(0); SBAR(); MQ(f1);
    LAS char* pw = (LAS char*)lds + L_PQ + wr * 8192 + wc * 4096 + l * 16;
#pragma unroll
    for (int m = 0; m < 4; ++m) {
      const f4 u0 = acc[m][0] + c0 * mv[m], u1 = acc[m][1] + c1 * mv[m];
      *(LAS u2*)(pw + wr * 8 + m * 1024) = (u2){phi2((f2){u0[0], u0[1]}, (f2)(rv[m]), (f2)(rl[m])), phi2((f2){u0[2], u0[3]}, (f2)(rv[m]), (f2)(rl[m]))};
      *(LAS u2*)(pw + 8 - wr * 8 + m * 1024) = (u2){phi2((f2){u1[0], u1[1]}, (f2)(rv[m]), (f2)(rl[m])), phi2((f2){u1[2], u1[3]}, (f2)(rv[m]), (f2)(rl[m]))};
    }
  }
  lsig(myFL, 4);
  __builtin_amdgcn_s_setprio(0);
  STMP(5);
  {
  int l;
  asm volatile("v_mbcnt_lo_u32_b32 %0, -1, 0\n v_mbcnt_hi_u32_b32 %0, -1, %0" : "=v"(l));
  __builtin_assume(l >= 0 && l < 64);
  const int r15 = l & 15, g4 = l >> 4, q = (l & 15) >> 2, p = l & 3;
  const int trow = mb * 128 + w * 32 + (l & 31);
  u2 oh;
  const unsigned flo = PAIRAT(r15) * 128;
  const char* fb = uptr(wsx + WS_OH + mb * 2048);
  int sel = -1;
  {
    const u4 s0 = *(const LAS u4*)(lds + L_SEL), s1 = *(const LAS u4*)(lds + L_SEL + 16);
#pragma unroll
    for (int i = 0; i < 8; ++i) { const int v = (int)(i < 4 ? s0[i & 3] : s1[i & 3]); sel = v == trow ? i : sel; }
  }
  const bool anysel = __ballot(sel >= 0) != 0ull;
  f16v oc[2][2];
  {
    const float m = *(const LAS float*)(lds + L_M + (w * 32 + (l & 31)) * 4), rs = *(const LAS float*)(lds + L_RS + (w * 32 + (l & 31)) * 4);
#pragma unroll
    for (int t = 0; t < 8; ++t) {
      const f4 xf = (f4){lo(xv[t][0]), hi(xv[t][0]), lo(xv[t][1]), hi(xv[t][1])};
      const f4 y = (xf - m) * rs + xf;
#pragma unroll
      for (int i = 0; i < 4; ++i) oc[0][t >> 2][(t & 3) * 4 + i] = y[i];
    }
    asm volatile("" : "+v"(oc[0][0]), "+v"(oc[0][1]));
  }
  unsigned wa[2];
#pragma unroll
  for (int t = 0; t < 2; ++t) wa[t] = ldsb + L_WO + wc * 4096 + (4 * (g4 >> 1) + q) * 128 + (((4 * t + 2 * (g4 & 1) + (p >> 1)) ^ ((q >> 1) << 2)) * 16) + (p & 1) * 8;
  u4 a[RD][4];
#pragma unroll
  for (int k = 0; k < RD; ++k) asm volatile("" : "=v"(a[k][0]), "=v"(a[k][1]), "=v"(a[k][2]), "=v"(a[k][3]));
  const char* ob = uptr(wsx + WS_OF + mb * 262144 + wc * 8192 + wr * 4096);
  const unsigned ov0 = l * 16 + wc * 2048, ov1 = ov0 ^ 2048;
  STMP(6);
  unsigned vfw[4];
#pragma unroll
  for (int i = 0; i < 4; ++i) { vfw[i] = VFW(i); asm volatile("" : "+v"(vfw[i])); }
  auto wo_part = [&](int s0, int s1) {
#pragma unroll
    for (int i = s0 / 2; i < s1 / 2; i += 2) {
      const int pp = i + wr;
      const char* wp = (const char*)wo + (nb * 128 - 3072) + (2 * PAIRAT(pp) + wc) * 65536; const unsigned wm = ldsb + (2 * pp + wc) * 4096;
      GL4(vfw, wp, wm, L_WO);
    }
  };
  lwait<2>(adFL, 0x3Fu, (l & 4) ? 3u : 4u);
  asm volatile("global_load_dwordx2 %0, %1, %2 sc1" : "=&v"(oh) : "v"(flo), "s"(fb) : "memory");
  wo_part(WE, WB);
  WAITV(WB - WE);
  asm volatile("" : "+v"(oh));
  unsigned seen = (unsigned)__ballot(oh[0] == tag), go = 0;
  auto need = [&](unsigned mask) {
    if (__builtin_expect((go & mask) != mask, 0)) {
      RETRY2();
      _Pragma("unroll 1") for (unsigned it = 0; it < SPIN; ++it) {
        if ((seen & mask) != mask) {
          asm volatile("global_load_dwordx2 %0, %1, %2 sc1\n s_waitcnt vmcnt(0)" : "+v"(oh) : "v"(flo), "s"(fb) : "memory");
          seen = (unsigned)__ballot(oh[0] == tag);
        }
        go = seen & (unsigned)__ballot(oh[1] - NOW() - 1u >= 1000u);
        if ((go & mask) == mask) break;
        SLEEP(1);
      }
    }
  };
#define OX 0
#define OSC "sc1"
#define OL1(pos, i) do { if (OX == 2 && (pos) >= RD) break; \
    asm volatile("global_load_dwordx4 %0, %1, %2 offset:%3 " OSC : "+v"(a[(pos) % RD][i]) : "v"((i) & 1 ? ov1 : ov0), "s"(ob + (OX == 1 ? ((pos) & 1) : PAIRAT(pos)) * 16384), "i"(((i) >> 1) * 1024) : "memory"); } while (0)
#define OLR(pos) do { _Pragma("unroll") for (int i_ = 0; i_ < 4; ++i_) asm volatile("global_load_dwordx4 %0, %1, %2 sc1" : "+v"(a[(pos) % RD][i_]) : "v"((i_ & 1 ? ov1 : ov0) + (i_ >> 1) * 1024), "s"(ob + PAIRAT(pos) * 16384) : "memory"); } while (0)
#define OLD(pos) do { OL1(pos, 0); OL1(pos, 1); OL1(pos, 2); OL1(pos, 3); } while (0)
  auto omax = [&](u4(&v)[4]) {
    asm volatile("" : "+v"(v[0]), "+v"(v[1]), "+v"(v[2]), "+v"(v[3]));
    return max(umax3(v[0][0], v[1][0], v[2][0]), v[3][0]);
  };
#define OFIX(pos, m) do { \
    if (__builtin_expect(__ballot((m) == POIS) != 0ull, 0)) { RETRY(); _Pragma("unroll 1") for (unsigned it = 0; it < SPIN; ++it) { SLEEP(4); OLR(pos); WAITV(0); if (__ballot(omax(a[(pos) % RD]) == POIS) == 0ull) break; } } } while (0)
#define OS1(i)
#define OS2(i)
  lwait<1>(adFL, 0x30u, 4);
  wo_part(WB, 32);
  __builtin_amdgcn_s_setprio(2);
  go = seen & (unsigned)__ballot(oh[1] - NOW() - 1u >= 1000u);
  need(1u);
  STMP(15);
  u2 oh2 = oh;
  if (__builtin_expect(seen != ~0u, 0)) asm volatile("global_load_dwordx2 %0, %1, %2 sc1" : "+v"(oh2) : "v"(flo), "s"(fb) : "memory");
  OS1(0); OLD(0);
#pragma unroll
  for (int i = 1; i < RD; ++i) { need(1u << i); OS1(i); OLD(i); }
  STMP(10);
  s4 b[8];
#define ORD(pos, f) do { if ((f) < 2 && (pos) == 8) { wa[(f) & 1] += 65536; asm volatile("" : "+v"(wa[(f) & 1])); } \
    RDTR(b[2 * (f)], wa[(f) & 1], ((pos) & 7) * 8192 + ((f) >> 1) * 1024); RDTR(b[2 * (f) + 1], wa[(f) & 1], ((pos) & 7) * 8192 + ((f) >> 1) * 1024 + 2048); } while (0)
#define OMF(pos, f, r) do { if ((f) < 2 && (pos) == 0 && (r) == 1) asm volatile("v_mfma_f32_32x32x16_bf16 %0, %1, %2, 0" : "=&v"(oc[r][(f) & 1]) \
      : "v"(CAT(b[2 * (f)], b[2 * (f) + 1])), "v"(a[(pos) % RD][((f) & 2) + (r)])); \
    else asm volatile("v_mfma_f32_32x32x16_bf16 %0, %1, %2, %0" : "+v"(oc[r][(f) & 1]) \
      : "v"(CAT(b[2 * (f)], b[2 * (f) + 1])), "v"(a[(pos) % RD][((f) & 2) + (r)])); \
    SBAR(); if ((r) == 1 && (pos) < 15) ORD(((pos) + 1) & 15, f); SBAR(); } while (0)
#define ONX(pos) ((pos) > 0 && (pos) + RD - 1 < 16)
#define ONL(pos, i) do { if (ONX(pos)) OL1(((pos) + RD - 1) & 15, i); SBAR(); } while (0)
#define OWL(pos, f) do { WAITL((pos) < 15 ? 6 : 6 - 2 * (f)); SBAR(); } while (0)
#define OHEAD(pos) do { unsigned m_ = 0; \
    if ((pos) == WE / 2 - 1 && WE < 32) { asm volatile("" : "+v"(fl3)); if (__builtin_expect(((unsigned)__ballot(fl3 < 5u) >> (w ^ 2)) & 1u, 0)) lwait(adFL, 1u << (w ^ 2), 5); SBAR(); } \
    OWL(pos, 0); OMF(pos, 0, 0); if (ONX(pos)) { need(1u << (((pos) + RD - 1) & 15)); OS1(((pos) + RD - 1) & 15); } SBAR(); \
    OMF(pos, 0, 1); ONL(pos, 0); \
    OWL(pos, 1); OMF(pos, 1, 0); ONL(pos, 1); \
    OMF(pos, 1, 1); ONL(pos, 2); \
    OWL(pos, 2); OMF(pos, 2, 0); ONL(pos, 3); \
    if ((pos) == 0 || (pos) == 4) { asm volatile("ds_write_b32 %0, %1" ::"v"(myFL), "v"((pos) ? 6u : 5u) : "memory"); SBAR(); } \
    if ((pos) == WE / 2 - 3) { asm volatile("ds_read_b32 %0, %1" : "=v"(fl3) : "v"(adFL) : "memory"); SBAR(); } \
    if ((pos) == 13) { asm volatile("ds_read_b32 %0, %1" : "=v"(fl3) : "v"(adFL) : "memory"); SBAR(); } \
    OMF(pos, 2, 1); \
    OWL(pos, 3); OMF(pos, 3, 0); \
    if ((pos) < 15) { WAITV(4 * ((pos) + RD - 1 < 16 ? RD - 2 : 14 - (pos))); m_ = omax(a[((pos) + 1) % RD]); asm volatile("" : "+v"(m_)); SBAR(); } \
    OMF(pos, 3, 1); \
    if ((pos) < 15) { OFIX(((pos) + 1) & 15, m_); OS2(((pos) + 1) & 15); } SBAR(); } while (0)
  unsigned fl3;
  ORD(0, 0); ORD(0, 1); ORD(0, 2); ORD(0, 3);
  { WAITV(4 * (RD - 1)); const unsigned m_ = omax(a[0]); OFIX(0, m_); OS2(0);
    asm volatile("" : "+v"(oh2));
    if (oh2[0] == tag) oh = oh2;
    seen = (unsigned)__ballot(oh[0] == tag);
    go = seen & (unsigned)__ballot(oh[1] - NOW() - max(r15 - (RD - 1), 0) * ADV - 1u >= 1000u);
    SBAR(); }
  OHEAD(0); OHEAD(1); STMP(11); OHEAD(2); OHEAD(3); OHEAD(4); OHEAD(5); OHEAD(6); OHEAD(7); STMP(12);
  OHEAD(8); OHEAD(9); STMP(13); OHEAD(10); OHEAD(11); OHEAD(12); OHEAD(13); OHEAD(14);
  f4 xi[8];
  {
    asm volatile("" : "+v"(fl3));
    if (__builtin_expect(((unsigned)__ballot(fl3 < 6u) >> (w ^ 2)) & 1u, 0)) lwait(adFL, 1u << (w ^ 2), 6);
    if (__builtin_expect(((unsigned)__ballot(fl3 < 4u) & 0x30u) != 0u, 0)) lwait(adFL, 0x30u, 4);
    const unsigned xw = ldsb + L_WO + (4 * wr + wc) * 4096 + l * 16, xr = xw ^ 4096;
#define XW(t) asm volatile("ds_write_b128 %0, %1 offset:%2" ::"v"(xw), "v"(__builtin_shufflevector(oc[1][(t) >> 2], oc[1][(t) >> 2], ((t) & 3) * 4, ((t) & 3) * 4 + 1, ((t) & 3) * 4 + 2, ((t) & 3) * 4 + 3)), \
      "i"(((t) >> 2) * 8192 + ((t) & 3) * 1024) : "memory")
    SBAR();
    OWL(15, 0); OMF(15, 0, 1); OWL(15, 1); OMF(15, 1, 1); OWL(15, 2); OMF(15, 2, 1); OWL(15, 3); OMF(15, 3, 1);
    OMF(15, 0, 0); OMF(15, 1, 0); XW(0); XW(1); XW(2); XW(3); SBAR();
    OMF(15, 2, 0); XW(4); XW(5); XW(6); XW(7); SBAR();
    OMF(15, 3, 0);
    asm volatile("ds_write_b32 %0, %1" ::"v"(myFL), "v"(7u) : "memory");
    _Pragma("unroll 1") for (unsigned it = 0; it < SPIN; ++it) {
      unsigned v;
      asm volatile("ds_read_b32 %0, %1" : "=v"(v) : "v"(adFL) : "memory");
#pragma unroll
      for (int t = 0; t < 8; ++t) RD128(xi[t], xr, (t >> 2) * 8192 + (t & 3) * 1024);
      WAITL(0);
      asm volatile("" : "+v"(v), "+v"(xi[0]), "+v"(xi[1]), "+v"(xi[2]), "+v"(xi[3]), "+v"(xi[4]), "+v"(xi[5]), "+v"(xi[6]), "+v"(xi[7]));
      if ((((unsigned)__ballot(v < 7u) >> (w ^ 1)) & 1u) == 0u) break;
    }
    asm volatile("" : "+v"(oc[0][0]), "+v"(oc[0][1]));
#pragma unroll
    for (int t = 0; t < 8; ++t)
#pragma unroll
      for (int i = 0; i < 4; ++i) xi[t][i] += oc[0][t >> 2][(t & 3) * 4 + i];
  }
  STMP(8);
  if (anysel) {
    lwait(adFL, 0xC0u, 3);
    if (sel >= 0) {
#pragma unroll
      for (int t = 0; t < 8; ++t) xi[t] += *(const LAS f4*)(lds + L_KVX + sel * 256 + (8 * t + 4 * (l >> 5)) * 4);
    }
  }
  const __amdgpu_buffer_rsrc_t outr = __builtin_amdgcn_make_buffer_rsrc(out, 0, 2048 * D * 2, 0x00020000);
  if (FULL) {
    LAS char* tb = (LAS char*)lds + L_PQ + w * 4096;
#pragma unroll
    for (int t = 0; t < 8; ++t) *(LAS u2*)(tb + (l & 31) * 128 + ((t ^ (l & 7)) * 16) + (l >> 5) * 8) = (u2){pk2(xi[t][0], xi[t][1]), pk2(xi[t][2], xi[t][3])};
#pragma unroll
    for (int i = 0; i < 4; ++i) {
      const u4 v = *(const LAS u4*)(tb + i * 1024 + (l >> 3) * 128 + (((l ^ (l >> 3)) & 7) * 16));
      __builtin_amdgcn_raw_buffer_store_b128(v, outr, (mb * 128 + w * 32 + i * 8 + (l >> 3)) * 2048 + nb * 128 + (l & 7) * 16, 0, 16);
    }
  } else {
#pragma unroll
  for (int hh = 0; hh < 4; ++hh) {
    const f4 ta = xi[2 * hh], tb = xi[2 * hh + 1];
    auto r0 = __builtin_amdgcn_permlane32_swap(pk2(ta[0], ta[1]), pk2(tb[0], tb[1]), false, false);
    auto r1 = __builtin_amdgcn_permlane32_swap(pk2(ta[2], ta[3]), pk2(tb[2], tb[3]), false, false);
    __builtin_amdgcn_raw_buffer_store_b128(((u4){r0[0], r1[0], r0[1], r1[1]}), outr, (trow * D + nb * 64 + 16 * hh + 8 * (l >> 5)) * 2, 0, 16);
  }
  }
  if (g == 0 && w == 0 && l == 0) __builtin_amdgcn_raw_buffer_store_b32(tag, wse, WS_E, 0, 16);
  STMP(9);
  STFLUSH();
  asm volatile(".Lend_c:");
  }
  return;
  }
  {
    unsigned vb[4], vfw[4];
    STMP(6); SBAR();
#pragma unroll
    for (int i = 0; i < 4; ++i) vb[i] = (8 * i + (l >> 3)) * 6144 + (((l & 7) ^ ((((l >> 4) & 1) << 2) | ((i & 1) << 1))) * 16) + (3 - i) * 1024;
    const unsigned lb0 = lw * 196608, lb1 = 196608 - lw * 194560, mbb = ldsb + 16384 + lw * 12288;
    const char* Ab = kx + ao;
    const char* Bb = kq + (hp * 128 + lw * 2048 - 3072);
    auto issue = [&](int s, int u0, int u1) {
      const int kt = (s + rot) & 15;
      const char* sa = Ab + kt * 128; const char* sb = Bb + kt * (64 * 6144);
      const unsigned so = (s & 3) * STG, ma = mba + so, mq = mbb + so;
#pragma unroll
      for (int u = u0; u < u1; ++u) { if (u < 2) GL4(va, sa + u * 65536, ma, u * 4096); else GL4(vb, sb + (u == 2 ? lb0 : u == 3 ? lb1 : lb0 + 2048), mq, (u - 2) * 4096); }
    };
    int tk = 0;
#define LBAR() do { BST(tk + 1); BAR(); BST(tk + 21); ++tk; } while (0)
    issue(0, 2, 3); issue(0, 1, 2); issue(0, 3, 5); SBAR(); STMP(8); SBAR(); issue(1, 0, 5); issue(2, 0, U0);
    BST(60);
    WAITV(20 + 4 * U0); BST(0); BAR(); BST(20);
    issue(2, U0, 5);
    GETTAG(tag);
    MKWS(tag);
    const unsigned ck0 = (unsigned)__builtin_amdgcn_s_memtime(), rt0 = NOW();
    int s0 = 3;
    if (U3 < 5) { issue(3, 0, U3); BST(tk + 40); WAITV(20 + 4 * U3); LBAR(); issue(3, U3, 5); s0 = 4; }
#pragma unroll 1
    for (; s0 < (sp ? 16 : 6 + ph); ++s0) { issue(s0, 0, 5); BST(tk + 40); WAITV(40); LBAR(); }
#pragma unroll 1
    for (; s0 < 16; s0 += 5) {
      WAITV(20); LBAR();
      WAITV(0); LBAR();
      issue(s0, 0, 5); issue(s0 + 1, 0, U1); BST(tk + 40); WAITV(4 * U1); LBAR();
      issue(s0 + 1, U1, 5); issue(s0 + 2, 0, 5);
      if (s0 + 3 >= 16) break;
      issue(s0 + 3, 0, U2); BST(tk + 40); WAITV(20 + 4 * U2); LBAR();
      issue(s0 + 3, U2, 5);
      if (s0 + 4 >= 16) break;
      issue(s0 + 4, 0, 5); BST(tk + 40); WAITV(40); LBAR();
    }
    int wq = 0;
#pragma unroll
    for (int i = 0; i < 4; ++i) vfw[i] = VFW(i);
    auto wo_upto = [&](int lim) {
      _Pragma("unroll 1") for (; wq < lim; ++wq) { const char* wp = (const char*)wo + (nb * 128 - 3072) + HEADAT(wq) * 65536; const unsigned wm = ldsb + wq * 4096; GL4(vfw, wp, wm, L_WO); }
    };
    asm volatile("ds_write_b32 %0, %1" ::"v"(adFL), "v"(0u) : "memory");
    unsigned kd = sp ? KS : KD, od = OD;
    if (CF) {
      const unsigned dc = (unsigned)__builtin_amdgcn_s_memtime() - ck0, dr = NOW() - rt0;
      const float sc = fminf((float)dr * (float)CF * __builtin_amdgcn_rcpf((float)dc), 2.f);
      kd = KL + (unsigned)((float)(kd - KL) * sc); od = OL + (unsigned)((float)(OD - OL) * sc);
      asm volatile("" : "+v"(kd), "+v"(od));
    }
    const int hd = hp * 2 + lw;
    u4 sel[2];
#pragma unroll
    for (int e = 0; e < 2; ++e) {
      unsigned v = (r15 >> 2) == g4 ? (((r15 & 3) == 2 * e ? 0x3F80u : 0u) | ((r15 & 3) == 2 * e + 1 ? 0x3F800000u : 0u)) : 0u;
      asm volatile("" : "+v"(v));
      sel[0][e] = v; sel[0][2 + e] = 0u; sel[1][e] = 0u; sel[1][2 + e] = v;
    }
    const char* kvb = uptr(wsx + WS_KVP + (size_t)hd * 32768);
    const char* zb = uptr(wsx + WS_ZP + (size_t)hd * 2048);
    unsigned zoff = r15 * 128 + g4 * 32, oho = l == 0 ? WS_OH + (mb * 16 + hp) * 128 : WS_SZ, fla = ldsb + L_FL;
    asm volatile("" : "+v"(zoff), "+v"(oho), "+v"(fla), "+s"(kvb), "+s"(zb));
    WAITV(0); WAITL(0); LBAR();
    u4 dg;
    unsigned now;
    if (lw) {
      const u4 pz = (u4)(POIS);
      const unsigned tb = NOW();
      __builtin_amdgcn_raw_buffer_store_b32(tb + kd, wsr, l == 0 ? WS_HK + hp * 512 + (mb >> 2) * 128 + (mb & 3) * 4 : WS_SZ, 0, 16);
#pragma unroll
      for (int i = 0; i < 16; ++i) ST128(pz, WS_OF + (mb * 32 + hp * 2) * 8192 + i * 1024 + l * 16);
      BST(61);
      const char* hb = uptr(wsx + WS_HK + hp * 512);
      __builtin_amdgcn_s_setprio(LP1);
      lwait<4>(adFL, 0xC0u, 1u);
      while (NOW() - tb < HT) SLEEP(1);
      STMP(1);
      unsigned t0, rq[4], m0, m1, nt;
#define HG4(d, a, b, c, e) "s_sub_u32 s" #a ", s" #a ", %0\n s_sub_u32 s" #b ", s" #b ", %0\n s_sub_u32 s" #c ", s" #c ", %0\n s_sub_u32 s" #e ", s" #e ", %0\n" \
      "s_max_u32 s" #a ", s" #a ", s" #b "\n s_max_u32 s" #c ", s" #c ", s" #e "\n s_max_u32 %" #d ", s" #a ", s" #c "\n"
      asm volatile("s_mov_b32 %7, 15\n.Lhint:\n s_load_dwordx4 s[72:75], %8, 0 glc\n s_load_dwordx4 s[76:79], %8, 128 glc\n s_load_dwordx4 s[80:83], %8, 256 glc\n"
                   "s_load_dwordx4 s[84:87], %8, 384 glc\n s_memrealtime s[88:89]\n s_waitcnt lgkmcnt(0)\n s_sub_u32 %0, s88, 0x800\n"
                   HG4(1, 72, 73, 74, 75) HG4(2, 76, 77, 78, 79) HG4(3, 80, 81, 82, 83) HG4(4, 84, 85, 86, 87)
                   "s_max_u32 %5, %1, %2\n s_max_u32 %6, %3, %4\n s_max_u32 s72, %5, %6\n s_cmpk_lt_u32 s72, 0xc00\n s_cbranch_scc1 .Lhdone\n"
                   "s_sleep %9\n s_sub_u32 %7, %7, 1\n s_cbranch_scc0 .Lhint\n"
                   "s_mov_b32 %1, 0\n s_mov_b32 %2, 0\n s_mov_b32 %3, 0\n s_mov_b32 %4, 0\n s_mov_b32 %5, 0\n s_mov_b32 %6, 0\n.Lhdone:"
                   : "=&s"(t0), "=&s"(rq[0]), "=&s"(rq[1]), "=&s"(rq[2]), "=&s"(rq[3]), "=&s"(m0), "=&s"(m1), "=&s"(nt) : "s"(hb), "i"(HKS)
                   : "memory", "scc", "s72", "s73", "s74", "s75", "s76", "s77", "s78", "s79", "s80", "s81", "s82", "s83", "s84", "s85", "s86", "s87", "s88", "s89");
      BST(16);
      const bool up = m0 > m1;
      const unsigned a0 = up ? rq[2] : rq[0], a1 = up ? rq[3] : rq[1], a2 = up ? rq[0] : rq[2], a3 = up ? rq[1] : rq[3], gu = up ? 2u : 0u;
      unsigned e0, e1, go;
      asm("s_cmp_gt_u32 %3, %4\n s_cselect_b32 %0, %6, %5\n s_cselect_b32 %1, %5, %6\n s_addc_u32 %2, %7, 0" : "=&s"(e0), "=&s"(e1), "=&s"(go) : "s"(a2), "s"(a3), "s"(a0), "s"(a1), "s"(gu) : "scc");
      dg = (u4){((t0 + 3u + e0) & ~3u) | go, t0 + e1, t0 + min(a2, a3), t0 + max(m0, m1)};
      asm volatile("ds_write_b128 %0, %1 offset:32\n ds_write_b32 %0, %2 offset:20" ::"v"(fla), "v"(dg), "v"(1u) : "memory");
      BST(15);
      now = NOW();
      WAITV(0);
      ST64(((u2){tag, ((int)(dg[3] - now) > 0 ? dg[3] : now) + od}), oho);
    } else {
      wo_upto(WA);
      BST(15);
      if (WA < WE) { lwait<2>(adFL, 0x0Fu, 3); if (WS0) SLEEP(WS0); wo_upto(WE); }
      STMP(1);
      __builtin_amdgcn_s_setprio(LP0);
      u4 v;
      _Pragma("unroll 1") for (unsigned it = 0; it < SPIN; ++it) {
        unsigned f;
        asm volatile("ds_read_b32 %0, %2 offset:20\n ds_read_b128 %1, %2 offset:32\n s_waitcnt lgkmcnt(0)" : "=&v"(f), "=&v"(v) : "v"(fla) : "memory");
        if (__builtin_amdgcn_readfirstlane(f)) break;
        SLEEP(2);
      }
#pragma unroll
      for (int e = 0; e < 4; ++e) dg[e] = __builtin_amdgcn_readfirstlane(v[e]);
      now = NOW();
    }
    unsigned gx = (dg[0] & 3u) * 8192u;
    asm volatile("" : "+s"(gx));
    __builtin_amdgcn_s_setprio(3);
    u4 pv[16][2], zv0, zv1, pq[8];
    const unsigned lv16 = l * 16;
    const unsigned pa = ldsb + L_PQ + lw * 4096 + l * 16, omask = lw ? 0x0Au : 0x25u;
    STMP(2);
#define GLD(gq) do { _Pragma("unroll") for (int i = 0; i < 4; ++i) { const char* sb = padd(kvb, (((gq) * 8192u + (i & 2) * 2048u) ^ gx)); \
      asm volatile("global_load_dwordx4 %0, %1, %2 offset:%3 sc1" : "=v"(pv[(gq) * 4 + i][0]) : "v"(lv16), "s"(sb), "i"((i & 1) * 2048) : "memory"); \
      asm volatile("global_load_dwordx4 %0, %1, %2 offset:%3 sc1" : "=v"(pv[(gq) * 4 + i][1]) : "v"(lv16), "s"(sb), "i"((i & 1) * 2048 + 1024) : "memory"); } } while (0)
#define ZLD() do { asm volatile("global_load_dwordx4 %0, %1, %2 sc1" : "=v"(zv0) : "v"(zoff), "s"(zb) : "memory"); \
      asm volatile("global_load_dwordx4 %0, %1, %2 offset:16 sc1" : "=v"(zv1) : "v"(zoff), "s"(zb) : "memory"); } while (0)
#pragma unroll
    for (int gq = 0; gq < 4; ++gq) {
      while ((int)(now - dg[gq]) < 0) now = NOW();
      if (gq == 3) ZLD();
      GLD(gq);
    }
    lsig(myFL, 3);
    STMP(5);
    unsigned ofo = WS_OF + (mb * 32 + hd) * 8192 + g4 * 512 + r15 * 16;
    unsigned ofp = ofo + 4096;
    f4 eps = (f4)(1e-6f);
    asm volatile("" : "+v"(ofo), "+v"(ofp), "+v"(eps), "+v"(sel[0]), "+v"(sel[1]));
    f4 gs[4][4]; float z[8];
#pragma unroll
    for (int gq = 0; gq < 4; ++gq) {
      if (gq == PW) {
        SBAR();
        lwait(adFL, omask, (l & 4) ? 3u : 4u);
#pragma unroll
        for (int i = 0; i < 8; ++i) RD128(pq[i], pa, (i >> 2) * 8192 + (i & 3) * 1024);
      }
      SBAR(); WAITV(gq == 0 ? 26 : gq == 1 ? 18 : gq == 2 ? 10 : 0);
      if (gq == 3) BST(17);
      auto gbad = [&]() {
#pragma unroll
        for (int i = 0; i < 4; ++i) asm volatile("" : "+v"(pv[gq * 4 + i][0]), "+v"(pv[gq * 4 + i][1]));
        unsigned m = umax3(pv[gq * 4][0][0], pv[gq * 4][1][0], pv[gq * 4 + 1][0][0]);
        m = umax3(m, pv[gq * 4 + 1][1][0], pv[gq * 4 + 2][0][0]);
        m = umax3(m, pv[gq * 4 + 2][1][0], pv[gq * 4 + 3][0][0]);
        m = max(m, pv[gq * 4 + 3][1][0]);
        if (gq == 3) { asm volatile("" : "+v"(zv0), "+v"(zv1)); m = umax3(m, zv0[0], zv1[0]); }
        return __ballot(m == POIS) != 0ull;
      };
      if (__builtin_expect(gbad(), 0)) {
        _Pragma("unroll 1") for (unsigned it = 0; it < SPIN; ++it) {
          RETRY();
          SLEEP(2);
          if (gq == 3) ZLD();
          GLD(gq);
          WAITV(0);
          if (!gbad()) break;
        }
      }
      if (gq == 3) {
#pragma unroll
        for (int e = 0; e < 4; ++e) { z[e] = __uint_as_float(zv0[e]); z[4 + e] = __uint_as_float(zv1[e]); }
        SBAR();
      }
#pragma unroll
      for (int i = 0; i < 4; ++i)
#pragma unroll
        for (int t = 0; t < 4; ++t) {
          gs[gq][t] = MFMA16(S8(sel[(t ^ (t >> 1)) & 1]), S8(pv[gq * 4 + i][t >> 1]), i ? gs[gq][t] : (f4)(0.f));
          if (gq == 3) {
            SBAR();
            if (i + t == 0) asm volatile("s_nop 1");
            asm volatile("v_add_f32_dpp %0, %0, %0 row_ror:%2 row_mask:0xf bank_mask:0xf\n v_add_f32_dpp %1, %1, %1 row_ror:%2 row_mask:0xf bank_mask:0xf" : "+v"(z[2 * t]), "+v"(z[2 * t + 1]) : "i"(8 >> i));
            SBAR();
          }
        }
      if (gq == 2) {
#pragma unroll
        for (int t = 0; t < 4; ++t) { gs[0][t] += gs[1][t]; asm volatile("" : "+v"(gs[0][t])); }
      }
    }
    STMP(3);
    WAITL(0);
#pragma unroll
    for (int i = 0; i < 8; i += 2) asm volatile("" : "+v"(pq[i]), "+v"(pq[i + 1]));
    u4 kA[2], zA;
#pragma unroll
    for (int e = 0; e < 4; ++e) zA[e] = pk2(z[2 * e], z[2 * e + 1]);
    SBAR();
#pragma unroll
    for (int t = 0; t < 4; ++t) gs[0][t] += gs[2][t] + gs[3][t];
    SBAR();
    f4 dn[8], o0[8], o1[8]; float inv[8];
#pragma unroll
    for (int i = 0; i < 8; ++i) {
      dn[i] = MFMA16(S8(zA), S8(pq[i]), eps);
      SBAR();
      kA[i >> 2][i & 3] = pk2(gs[0][i >> 1][(i & 1) * 2], gs[0][i >> 1][(i & 1) * 2 + 1]);
      SBAR();
    }
#pragma unroll
    for (int i = 0; i < 8; ++i) {
      o0[i] = MFMA16(S8(kA[0]), S8(pq[i]), (f4)(0.f)); SBAR();
      o1[i] = MFMA16(S8(kA[1]), S8(pq[i]), (f4)(0.f)); SBAR();
      inv[i] = __builtin_amdgcn_rcpf(dn[i][0]); asm volatile("" ::"v"(dn[i])); SBAR();
    }
    BST(18);
    u4 op = sel[0];
#pragma unroll
    for (int i = 0; i < 8; ++i) {
      o0[i] *= inv[i]; o1[i] *= inv[i];
      u4 o; o[0] = min(pk2(o0[i][0], o0[i][1]), POIS - 1u); o[1] = pk2(o0[i][2], o0[i][3]); o[2] = pk2(o1[i][0], o1[i][1]); o[3] = pk2(o1[i][2], o1[i][3]);
      asm volatile("" : "+v"(o) : "v"(op));
      ST128(o, (i & 4 ? ofp : ofo) + (i & 1) * 256 + (i & 2) * 1024);
      op = o;
      SBAR();
    }
    lsig(myFL, 4);
    STMP(4);
    STFLUSH();
    asm volatile(".Lend_l:");
  }

}

extern "C" void kernel_launch(void* const* d_in, const int* in_sizes, int n_in, void* d_out, int out_size,
                              void* d_ws, size_t ws_size, hipStream_t stream) {
  (void)hipFuncSetAttribute((const void*)fused, hipFuncAttributeMaxDynamicSharedMemorySize, LDS_SZ);
  fused<<<256, 512, LDS_SZ, stream>>>((const u16*)d_in[0], (const u16*)d_in[1], (const u16*)d_in[4], (char*)d_ws, (const u16*)d_in[2],
                                      (const u16*)d_in[5], (const u16*)d_in[6], (const u16*)d_in[7], (u16*)d_out);
}
```

```cpp
#include <hip/hip_runtime.h>
#include <stdint.h>

typedef unsigned short u16;
typedef short s4 __attribute__((ext_vector_type(4)));
typedef short s8 __attribute__((ext_vector_type(8)));
typedef float f4 __attribute__((ext_vector_type(4)));
typedef float f16v __attribute__((ext_vector_type(16)));
typedef float f2 __attribute__((ext_vector_type(2)));
typedef unsigned u4 __attribute__((ext_vector_type(4)));
typedef unsigned u2 __attribute__((ext_vector_type(2)));
typedef unsigned u8v __attribute__((ext_vector_type(8)));
typedef __bf16 bf2 __attribute__((ext_vector_type(2)));
#define LAS __attribute__((address_space(3)))
#define DEV __device__ __forceinline__

constexpr int D = 1024, F = 3584;
#define NOMOE 0
#define KD 230
#define KS 224
#define OD 176
#define OHP 1
#define FS 128
#define GSL 8
#define YSL 16
#define TP1 0
#define TP2 3
#define TLBW 0
#define FULL 1
#define HPRIO 1
#define OWN 1
#define NOPOIS 0
#define CAP 7
#define RD 4
#define DYN 1
#define JX 0
#define NOSKIP 0
#define GRP 8
#define ADV 15
#define HB 1
#define RT 1
#define WD 0
#define WU1 12
#define WU2 22
#define PL 0
#define PO 0
#define WG 0
#define DS 0
#define D1 16
#define D2 2
#define HKS 2
#define LW1 1
#define HW1 2
#define HS0 0
#define GPV 2
#define XO 1
#define TP0 3
#define G1 0x31
#define G1S 4
#define G2S 8
#define TD 1
#define YD 170
#define WU0 4
#define WE 8
#define HT 115
#define CF 19
#define KL 145
#define OL 115
#define CL 0
#define WS0 0
#define OM 0
#define HG 128
#define U0 0
#define U3 5
#define TL 0
#define MH 0
#define HWT 3
#define YG 0
#define GG 1
#define PV 1
#define U1 4
#define U2 2
#define QD 1
#define LP0 3
#define LP1 3
#define WA 8
#define PW 3
#define WB 20
#define PF 1
#define PFS 6
#define PA 0
#define PAS 0
#define LPL 0
#define WP 1
#define E0 0
#define ES 0
#define IL 1
#define LW4 1
#define WF 1
#define LW3 2
#define ROT 0
#define LF 0
#define SL5 1
#define GE 0
#define ZH 1
#define TW 0
#define TWS 0
#define TCH 8
#define SPW 50
#define SP2 0
#define SP0 1
#define SPS 35
#define TH 0
#define TE 1
#define TN 4
#define TS 1
#define SE 1
#define TLP 1
#define SSL 64
#define T2 0
#define GI 0
#define GO 0
#define ZF 0
#define GW 0
#define F5 1
#define WT 0xFEDCBA9876543210ull
#define PE 0
#define KX 50

constexpr unsigned WS_E = 0;
constexpr unsigned WS_YF = 524288;
constexpr unsigned WS_CAND = 12288;
constexpr unsigned WS_RG = 49152;
constexpr unsigned WS_ST = 65536;
constexpr unsigned WS_HK = 589824;
constexpr unsigned WS_ZP = 131072;
constexpr unsigned WS_CS = 196608;
constexpr unsigned WS_OH = 458752;
constexpr unsigned WS_KVP = 1u << 20;
constexpr unsigned WS_GP = 2u << 20;
constexpr unsigned WS_YP = 3u << 20;
constexpr unsigned WS_OF = 5u << 20;
constexpr unsigned WS_SZ = 9u << 20;
#define NR 4
#define RGO(tag) ((64u << 20) + ((tag) & (NR - 1u)) * (10u << 20))
#define MKWS(tag) char* const wsx = ws + RGO(tag); const __amdgpu_buffer_rsrc_t wsr = __builtin_amdgcn_make_buffer_rsrc(wsx, 0, WS_SZ, 0x00020000)

constexpr int STG = 40960;
constexpr int L_KVX = 0;
constexpr int L_PQ = 12288;
constexpr int L_M = 28672, L_RS = L_M + 512, L_CS = L_RS + 512, L_SEL = L_CS + 768, L_FL = L_SEL + 64;
constexpr int L_HX = 30720, L_RL = 31744, L_WO = 32768;
constexpr int LDS_SZ = 163840;
constexpr unsigned POIS = 0xFFFFFFFFu;

DEV unsigned pk2(float a, float b) { bf2 v; v[0] = (__bf16)a; v[1] = (__bf16)b; return __builtin_bit_cast(unsigned, v); }
DEV float lo(unsigned u) { return __uint_as_float(u << 16); }
DEV float hi(unsigned u) { return __uint_as_float(u & 0xFFFF0000u); }
DEV float rbf(float a) { return lo(pk2(a, a)); }
DEV float dot2(unsigned a, unsigned b, float c) { return __builtin_amdgcn_fdot2_f32_bf16(__builtin_bit_cast(bf2, a), __builtin_bit_cast(bf2, b), c, false); }
#define RD128(dst, addr, OFF) asm volatile("ds_read_b128 %0, %1 offset:%2" : "=v"(dst) : "v"(addr), "i"(OFF) : "memory")
#define RDTR(dst, addr, OFF) asm volatile("ds_read_b64_tr_b16 %0, %1 offset:%2" : "=v"(dst) : "v"(addr), "i"(OFF) : "memory")
#define AL8() asm volatile(".p2align 3")
#define SBAR() __builtin_amdgcn_sched_barrier(0)
#define CAT(x, y) __builtin_shufflevector(x, y, 0, 1, 2, 3, 4, 5, 6, 7)
#define WAITV(n) asm volatile("s_waitcnt vmcnt(%0)" ::"i"(n) : "memory")
#define WAITL(n) asm volatile("s_waitcnt lgkmcnt(%0)" ::"i"(n) : "memory")
#define MEMB() asm volatile("" ::: "memory")
#define SLEEP(n) asm volatile("s_sleep %0" ::"i"(n) : "memory")
#define BAR() asm volatile("s_barrier" ::: "memory")
#define GL0(vo, sb, m0v) asm volatile("s_mov_b32 m0, %2\n s_nop 0\n global_load_lds_dwordx4 %0, %1" ::"v"(vo), "s"(sb), "s"(m0v) : "memory")
#define GLI(vo, sb, OFF) asm volatile("global_load_lds_dwordx4 %0, %1 offset:%2" ::"v"(vo), "s"(sb), "i"(OFF) : "memory")
#define GL4(v, sb, mm, MO) do { asm volatile("s_add_i32 m0, %2, %3\n s_nop 0\n global_load_lds_dwordx4 %0, %1" ::"v"((v)[0]), "s"(sb), "s"(mm), "i"(MO) : "memory", "scc"); \
      GLI((v)[1], sb, 1024); GLI((v)[2], sb, 2048); GLI((v)[3], sb, 3072); } while (0)
#define VFW(i) ((8 * (i) + (l >> 3)) * 2048 + (((l & 7) ^ (((l >> 4) & 1) << 2)) * 16) + (3 - (i)) * 1024)
#define MFMA16(a, b, c) __builtin_amdgcn_mfma_f32_16x16x32_bf16(a, b, c, 0, 0, 0)
#define S8(x) __builtin_bit_cast(s8, x)
#define LD128(off) __builtin_amdgcn_raw_buffer_load_b128(wsr, (off), 0, 16)
#define LD64(off) __builtin_amdgcn_raw_buffer_load_b64(wsr, (off), 0, 16)
#define ST128(v, off) __builtin_amdgcn_raw_buffer_store_b128((v), wsr, (off), 0, 16)
#define ST64(v, off) __builtin_amdgcn_raw_buffer_store_b64((v), wsr, (off), 0, 16)
#define OFA(i) (WS_OF + ((mb * 32 + hd) * 8 + (i)) * 1024 + l * 16)
#define POISON(i) ST128(pz, (i) < 2 ? WS_KVP + (hd * 16 + mb) * 2048 + (i) * 1024 + l * 16 : (i) == 2 ? WS_ZP + (hd * 16 + mb) * 128 + (l & 7) * 16 : OFA((i) - 3))
#define NOW() ((unsigned)__builtin_amdgcn_s_memrealtime())
constexpr unsigned SPIN = 1u << 21;
DEV const char* uptr(const char* p) {
  unsigned long long v = (unsigned long long)p;
  unsigned lo_ = __builtin_amdgcn_readfirstlane((unsigned)v), hi_ = __builtin_amdgcn_readfirstlane((unsigned)(v >> 32));
  return (const char*)(((unsigned long long)hi_ << 32) | lo_);
}
DEV const char* padd(const char* p, unsigned o) {
  unsigned lo_, hi_;
  asm("s_add_u32 %0, %2, %4\n s_addc_u32 %1, %3, 0" : "=&s"(lo_), "=s"(hi_) : "s"((unsigned)(unsigned long long)p), "s"((unsigned)((unsigned long long)p >> 32)), "s"(o) : "scc");
  return (const char*)(((unsigned long long)hi_ << 32) | lo_);
}
DEV void lsig(unsigned addr, unsigned val) { asm volatile("s_waitcnt lgkmcnt(0)\n ds_write_b32 %0, %1" ::"v"(addr), "v"(val) : "memory"); }
template <int SL = 0> DEV void lwait(unsigned addr, unsigned mask, unsigned target) {
  _Pragma("unroll 1") for (unsigned it = 0; it < SPIN; ++it) {
    unsigned v;
    asm volatile("ds_read_b32 %0, %1\n s_waitcnt lgkmcnt(0)" : "=v"(v) : "v"(addr) : "memory");
    if (((unsigned)__ballot(v < target) & mask) == 0u) break;
    if (SL) SLEEP(SL);
  }
}
DEV unsigned nopois(unsigned a) { return min(a, POIS - 1u); }
DEV u4 tagf(f4 v, unsigned tag) {
  u4 r;
#pragma unroll
  for (int e = 0; e < 4; ++e) r[e] = __builtin_amdgcn_perm(__float_as_uint(v[e]), tag, 0x07060500u + e);
  return r;
}
DEV void untag(u4& v, unsigned tag, unsigned& bad) {
#pragma unroll
  for (int e = 0; e < 4; ++e) v[e] ^= (tag >> (8 * e)) & 255u;
  bad |= v[0] | v[1]; bad |= v[2] | v[3];
}
DEV unsigned umax3(unsigned a, unsigned b, unsigned c) { return max(max(a, b), c); }
template <int C> DEV float dppf(float v) { return __int_as_float(__builtin_amdgcn_update_dpp(0, __float_as_int(v), C, 0xf, 0xf, false)); }
DEV unsigned wave_max(unsigned v) {
  v = max(v, (unsigned)__builtin_amdgcn_update_dpp(0, (int)v, 0x111, 0xf, 0xf, false));
  v = max(v, (unsigned)__builtin_amdgcn_update_dpp(0, (int)v, 0x112, 0xf, 0xf, false));
  v = max(v, (unsigned)__builtin_amdgcn_update_dpp(0, (int)v, 0x114, 0xf, 0xf, false));
  v = max(v, (unsigned)__builtin_amdgcn_update_dpp(0, (int)v, 0x118, 0xf, 0xf, false));
  v = max(v, (unsigned)__builtin_amdgcn_update_dpp(0, (int)v, 0x142, 0xa, 0xf, false));
  v = max(v, (unsigned)__builtin_amdgcn_update_dpp(0, (int)v, 0x143, 0xc, 0xf, false));
  return (unsigned)__builtin_amdgcn_readlane((int)v, 63);
}
DEV unsigned phi2(f2 u, f2 rs, f2 rl) {
  f2 a = u * rl, p = u * rs + 1.f, r;
  r[0] = __builtin_amdgcn_fmed3f(__builtin_amdgcn_exp2f(a[0]), p[0], 1.f);
  r[1] = __builtin_amdgcn_fmed3f(__builtin_amdgcn_exp2f(a[1]), p[1], 1.f);
  r += 1e-6f;
  return pk2(r[0], r[1]);
}

#define STDECL
#undef HB
#define HB 0
#define BSTV cw
#define STMP(i)
#define BST(i)
#define STFLUSH()
#define RETRY()
#define RETRY2()

DEV float half_sum(float v) {
  asm volatile("s_nop 1\n v_add_f32_dpp %0, %0, %0 row_ror:8 row_mask:0xf bank_mask:0xf\n s_nop 1\n v_add_f32_dpp %0, %0, %0 row_ror:4 row_mask:0xf bank_mask:0xf\n"
               "s_nop 1\n v_add_f32_dpp %0, %0, %0 row_ror:2 row_mask:0xf bank_mask:0xf\n s_nop 1\n v_add_f32_dpp %0, %0, %0 row_ror:1 row_mask:0xf bank_mask:0xf\n s_nop 1" : "+v"(v));
  auto r = __builtin_amdgcn_permlane16_swap(__float_as_uint(v), __float_as_uint(v), false, false);
  return __uint_as_float(r[0]) + __uint_as_float(r[1]);
}

constexpr unsigned long long ownat(unsigned long long a, unsigned long long b, unsigned long long c, unsigned long long d) {
  unsigned long long r = 0;
  for (int h = 0; h < 16; ++h) {
    const unsigned long long t = (h & 2) ? ((h & 1) ? d : c) : ((h & 1) ? b : a);
    for (int i = 0; i < 16; ++i) if ((int)((t >> (4 * i)) & 15) == h) r |= (unsigned long long)i << (4 * h);
  }
  return r;
}
constexpr int wtat(int i) { return (int)(WT >> (4 * i)) & 15; }
constexpr int wtmin(int a, int b) { int m = 99; for (int i = a; i < b; ++i) m = wtat(i) < m ? wtat(i) : m; return m; }
struct FQ { s8 a[4]; s4 b[12]; };
template <bool B> struct BC { static constexpr bool v = B; };
template <int I> struct IC { static constexpr int v = I; };

__global__ __launch_bounds__(512) void fused(const u16* __restrict__ x, const u16* __restrict__ wqkv, const u16* __restrict__ wrt,
                                             char* __restrict__ ws_, const u16* __restrict__ wo_, const u16* __restrict__ wg,
                                             const u16* __restrict__ wu, const u16* __restrict__ wd, u16* __restrict__ out) {
  extern __shared__ __attribute__((aligned(16))) char lds[];
  const char *kx, *kq, *kr, *wo; char* ws;
  asm volatile("s_load_dwordx2 %0, %5, 0\n s_load_dwordx2 %1, %5, 8\n s_load_dwordx2 %2, %5, 16\n s_load_dwordx2 %3, %5, 24\n s_load_dwordx2 %4, %5, 32"
               : "=&s"(kx), "=&s"(kq), "=&s"(kr), "=&s"(ws), "=&s"(wo) : "s"(__builtin_amdgcn_kernarg_segment_ptr()));
  SBAR();
  const int w = __builtin_amdgcn_readfirstlane(threadIdx.x >> 6), l = threadIdx.x & 63;
  STDECL;
  STMP(0);
  const int g = blockIdx.x, xcd = g & 7, j = g >> 3, ii = j >> 2;
  const int mb = (xcd & 1) * 8 + ii, hp = (xcd >> 1) * 4 + (j & 3);
  unsigned cw, cwo = (l * 32 + j) * 64; unsigned long long cwb;
  asm volatile("s_getpc_b64 %2\n.Lcode_here:\n v_min_u32 %1, max(.Lend_c - .Lcode_here, .Lend_l - .Lcode_here, .Lend_h - .Lcode_here, .Lend_s - .Lcode_here) - 4, %1\n"
               "s_cmp_lg_u32 %3, 7\n s_cbranch_scc1 .Lskip_w\n global_load_dword %0, %1, %2\n.Lskip_w:" : "=&v"(cw), "+v"(cwo), "=&s"(cwb) : "s"(w) : "memory", "scc");
  SBAR();
  const int lw = w & 1, rot = ROT == 0 ? 2 * xcd + 4 * (ii & 3) : ROT == 1 ? 2 * xcd + 8 * (ii & 1) : ROT == 2 ? 2 * xcd : ROT == 3 ? 0 : ROT == 4 ? 4 * (xcd & 1) + 8 * (ii & 1) : ROT == 7 ? 2 * ii + (xcd & 1) + 2 * (xcd & 6) : ROT == 8 ? 2 * ii + 2 * (xcd & 6) : ROT == 9 ? 2 * ii + (xcd & 1) + 2 * (xcd & 6) + 8 * (j & 1) : ROT == 10 ? 2 * ii + (xcd & 1) : 2 * (xcd & 1) + 4 * (ii & 3);
  const unsigned ldsb = (unsigned)(size_t)(LAS char*)lds;
  unsigned va[4];
#pragma unroll
  for (int i = 0; i < 4; ++i) va[i] = (8 * i + (l >> 3)) * 2048 + (((l & 7) ^ ((i & 1) * 4 + (l >> 4))) * 16) + (3 - i) * 1024;
  const int ao = (mb * 128 + lw * 64) * 2048 - 3072;
  long long ao0 = ao + (rot & 15) * 128;
  unsigned mba = ldsb + lw * 8192;
  const int srow = mb * 128 + hp * 8 + 2 * w + (l >> 5);
  unsigned xo = srow * 2048 + (l & 31) * 16, ro = (l & 31) * 16;
  asm volatile("" : "+v"(va[0]), "+v"(va[1]), "+v"(va[2]), "+v"(va[3]), "+v"(xo), "+v"(ro));
  asm volatile("s_waitcnt lgkmcnt(0)" : "+s"(kx), "+s"(kq), "+s"(kr), "+s"(ws), "+s"(wo), "+s"(ao0), "+s"(mba));
  SBAR();
  asm volatile("s_sub_u32 m0, %6, 4\n s_cmp_gt_u32 m0, 1\n s_cbranch_scc1 .Lskip_l\n s_mov_b32 m0, %5\n s_nop 0\n global_load_lds_dwordx4 %0, %4\n global_load_lds_dwordx4 %1, %4 offset:1024\n"
               "global_load_lds_dwordx4 %2, %4 offset:2048\n global_load_lds_dwordx4 %3, %4 offset:3072\n.Lskip_l:"
               :: "v"(va[0]), "v"(va[1]), "v"(va[2]), "v"(va[3]), "s"(kx + ao0), "s"(mba), "s"(w) : "memory", "scc");
  u4 xa[4], rw[4]; unsigned ep;
  asm volatile("s_cmp_gt_u32 %14, 3\n s_cbranch_scc1 .Lskip_x\n"
               "global_load_dwordx4 %0, %9, %11\n global_load_dwordx4 %1, %9, %11 offset:512\n global_load_dwordx4 %2, %9, %11 offset:1024\n global_load_dwordx4 %3, %9, %11 offset:1536\n"
               "global_load_dwordx4 %4, %10, %12\n global_load_dwordx4 %5, %10, %12 offset:512\n global_load_dwordx4 %6, %10, %12 offset:1024\n global_load_dwordx4 %7, %10, %12 offset:1536\n"
               ".Lskip_x:\n s_load_dword %8, %13, 0"
               : "=&v"(xa[0]), "=&v"(xa[1]), "=&v"(xa[2]), "=&v"(xa[3]), "=&v"(rw[0]), "=&v"(rw[1]), "=&v"(rw[2]), "=&v"(rw[3]), "=&s"(ep)
               : "v"(xo), "v"(ro), "s"(kx), "s"(kr), "s"(ws), "s"(w) : "memory", "scc");
#define GETTAG(t) unsigned t; asm volatile("s_waitcnt lgkmcnt(0)\n s_add_u32 %0, %1, 1" : "=s"(t) : "s"(ep) : "memory", "scc")
  SBAR();
#define PTAB 0xA98FEDC1045B7623ull
#define PHT 0x6624924999924924ull
#define CX 4
  const bool sp = (hp & 7) == 3;
  const int nb = hp;
#define PT0 PTAB
#define PT1 PTAB
#define PT2 PTAB
#define PT3 PTAB
  auto mktab = [&]() {
    const unsigned long long t = (hp & 2) ? ((hp & 1) ? PT3 : PT2) : ((hp & 1) ? PT1 : PT0), m = ~0ull << (4 * ((int)(ownat(PT0, PT1, PT2, PT3) >> (4 * hp)) & 15));
    unsigned long long r = (t & (m << 4)) | ((t & ~m) << 4) | (unsigned)hp;
    asm volatile("" : "+s"(r));
    return r;
  };
#define PAIRAT(p) ((int)(ptab >> (4 * (p))) & 15)
#define HEADAT(s) (2 * PAIRAT((s) >> 1) + ((s) & 1))
  const int ph = (int)(PHT >> (2 * j)) & 3;
#define HQT 0xDC1A09806572043Bull
#define CXA 1
#define CHUNK(b) ((b) < 14 ? 4 * (b) + 3 : ((b) - 14) + ((((b) - 14) * 43) >> 7))
  const unsigned adFL = ldsb + L_FL + 4 * (l & 7), myFL = ldsb + L_FL + 4 * w;
  const __amdgpu_buffer_rsrc_t wse = __builtin_amdgcn_make_buffer_rsrc(ws, 0, 64, 0x00020000);
  const int r15 = l & 15, g4 = l >> 4, q = (l & 15) >> 2, p = l & 3;
  unsigned pfo = 0, pfd = 0, pfl;
#define PFL(k) "s_load_dwordx2 s[100:101], %3, %1 offset:" #k " * 2048 + 60\n"
  auto spf = [&](const char* pb) {
    unsigned t;
    if (TCH == 3) asm volatile("s_getreg_b32 %0, hwreg(HW_REG_IB_STS, 8, 4)\n s_or_b32 %0, %0, %2\n s_cbranch_scc1 1f\n" PFL(0) PFL(1) PFL(2) PFL(3) PFL(4) PFL(5) PFL(6)
                 "s_cmp_eq_u32 %1, %4\n s_cselect_b32 %2, 1, 0\n s_add_u32 %1, %1, 7 * 2048\n s_min_u32 %1, %1, %4\n1:"
                 : "=&s"(t), "+s"(pfo), "+s"(pfd) : "s"(pb), "s"(pfl) : "scc", "s100", "s101");
  };
#define PFB(n, s) const char* pfb = wo + hp * 128 + (ii * 128 + (s) + (w & 1) * (n)) * 2048; pfl = ((n) - 7) * 2048; asm volatile("" : "+s"(pfb), "+s"(pfl))

  if (w != 4 && w != 5) {
  if (w >= 6) {
    const int mw = w - 6;
    unsigned pf = 0;
    if (PF && !(xcd & 2) && !sp && (mw == 0 || PF == 3 || (PF == 2 && (ii >> 2)))) {
      const unsigned po = (((rot + 2 - 2 * mw + (ii >> 2)) & 15) * 64 + l) * 6144 + (j & 3) * 2048 + (hp | 3) * 128;
      if (PFS) SLEEP(PFS);
      asm volatile("global_load_dword %0, %1, %2" : "+v"(pf) : "v"(po), "s"(kq) : "memory");
    }
    if (PA && mw == 1) {
      const unsigned po = (mb * 128 + (j & 3) * 32 + (l & 31)) * 2048 + (l >> 5) * 1024 + 384;
      if (PAS) SLEEP(PAS);
      asm volatile("global_load_dword %0, %1, %2" : "+v"(pf) : "v"(po), "s"(kx) : "memory");
    }
    GETTAG(tag);
    const int bq = ((int)(HQT >> (4 * hp)) & 15) * 4 + (ii & 3), ksp = (xcd & 1) * 2 + (ii >> 2);
    const int chunk = CHUNK(bq), bg = (CXA || bq < 14) ? bq ^ (w & 1) : bq, chg = CHUNK(bg);
    auto helper = [&](auto SP) {
    constexpr bool spare = decltype(SP)::v, moe = !spare && !NOMOE;
    int nbar = 0;
#define TICK() do { if (HB) { BST(nbar); asm volatile("" : "+v"(BSTV)); } SBAR(); BAR(); SBAR(); ++nbar; } while (0)
#define ZFL() do { if (ZH) asm volatile("ds_write_b32 %0, %1\n s_waitcnt lgkmcnt(0)" ::"v"(adFL), "v"(0u) : "memory"); } while (0)
#define TICKP() do { TICK(); spf(pfb); SBAR(); } while (0)
#define TICKQ() do { TICK(); spf(pfb); if (SP2) { SLEEP(SP2); spf(pfb); } SBAR(); } while (0)
#define DTICK(lim) do { if (nbar < (lim)) TICK(); else SLEEP(2); } while (0)
    const int hd = hp * 2 + mw;
    const u4 pz = (u4)(POIS);
    MKWS(tag);
    const int np = l & 7, kset = (l >> 3) & 1;
    u4 gw[4][8], dw[2][8];
    const __amdgpu_buffer_rsrc_t wgr = __builtin_amdgcn_make_buffer_rsrc((void*)(mw ? wu : wg), 0, D * F * 2, 0x00020000);
    const __amdgpu_buffer_rsrc_t wdr = __builtin_amdgcn_make_buffer_rsrc((void*)wd, 0, D * F * 2, 0x00020000);
    const __amdgpu_buffer_rsrc_t wor = __builtin_amdgcn_make_buffer_rsrc((void*)wo, 0, D * D * 2, 0x00020000);
    const unsigned gvo = ((256 * ksp + 32 * kset + 8 * g4) * F + 64 * chg + 8 * np) * 2;
    const unsigned dvo = ((64 * chunk + 8 * g4) * D + 256 * ksp + 128 * mw + 8 * r15) * 2;
    auto burst = [&](int k, int i0 = 0, int i1 = 16) {
      if (moe) {
#pragma unroll
        for (int i = i0; i < i1; ++i) {
          if (k < 2) gw[(2 * k + (i >> 3)) & 3][i & 7] = __builtin_amdgcn_raw_buffer_load_b128(wgr, gvo, (128 * k + 64 * (i >> 3) + (i & 7)) * F * 2, 2);
          else dw[i >> 3][i & 7] = __builtin_amdgcn_raw_buffer_load_b128(wdr, dvo, (32 * (i >> 3) + (i & 7)) * D * 2, 2);
        }
      }
    };
    auto transp = [&](u4(&v)[8]) {
      if (moe) {
        u4 f[8];
#pragma unroll
        for (int c = 0; c < 8; ++c)
#pragma unroll
          for (int e = 0; e < 4; ++e) {
            const unsigned ev = v[2 * e][c >> 1], od = v[2 * e + 1][c >> 1];
            f[c][e] = (c & 1) ? __builtin_amdgcn_perm(od, ev, 0x07060302u) : __builtin_amdgcn_perm(od, ev, 0x05040100u);
          }
#pragma unroll
        for (int c = 0; c < 8; ++c) { v[c] = f[c]; asm volatile("" : "+v"(v[c])); }
      }
    };
    auto pois = [&]() {
      ST128(pz, WS_KVP + (hd * 16 + mb) * 2048 + l * 16); ST128(pz, WS_KVP + (hd * 16 + mb) * 2048 + 1024 + l * 16); ST128(pz, WS_ZP + (hd * 16 + mb) * 128 + (l & 7) * 16);
    };
    unsigned wt0 = 0, wt1 = 0, wt2 = 0, wt3 = 0;
    auto touch = [&](int part = 3) {
      const unsigned to = (j * 32 + mw * 16 + (l >> 3)) * 2048 + (xcd >> 1) * 512 + (l & 7) * 64;
      if (TH) wt0 = __builtin_amdgcn_raw_buffer_load_b32(wor, to, (xcd & 1) * 16384, 0);
      else {
        if (part & 1) wt0 = __builtin_amdgcn_raw_buffer_load_b32(wor, to, 0, 0);
        if (part & 2) wt1 = __builtin_amdgcn_raw_buffer_load_b32(wor, to, 16384, 0);
      }
      if (TLBW) {
        const __amdgpu_buffer_rsrc_t outr = __builtin_amdgcn_make_buffer_rsrc(out, 0, 2048 * D * 2, 0x00020000);
        wt2 = __builtin_amdgcn_raw_buffer_load_b32(outr, (mb * 128 + mw * 64 + (l & 3) * 16) * 2048 + nb * 128, 0, 0);
        wt3 = __builtin_amdgcn_raw_buffer_load_b32(wsr, (l & 1) ? WS_GP + g * 4096 + mw * 2048 : WS_YP + (g >> 2) * 32768 + (l & 2) * 8192, 0, 0);
      }
    };
    unsigned teo;
    {
      const int pr = l < 8 ? (hp & 12) + (l & 3) : (int)(PTAB >> (4 * ((l >> 3) - 1))) & 15;
      teo = l < 8 * TN + 8 && mw == 0 ? (pr * 64 + j * 2 + ((l >> 2) & 1)) * 2048 + (xcd >> 1) * 512 + (l & 3) * 128 : D * D * 2;
      if (T2) {
        const int pr2 = l < 8 ? (hp & 12) + ((l >> 1) & 3) : pr;
        teo = l < 8 * TN + 8 ? (pr2 * 64 + j * 2 + mw) * 2048 + (xcd >> 1) * 512 + (l & 7) * 64 : D * D * 2;
      }
      asm volatile("" : "+v"(teo));
    }
    auto tearly = [&]() { wt0 = __builtin_amdgcn_raw_buffer_load_b32(wor, teo, 0, 0); };
    unsigned tlo;
    {
      const int ps = TN + (l >> 3) * 2 + mw, pr = (int)(PTAB >> (4 * (ps & 15))) & 15;
      tlo = ps < 16 ? (pr * 64 + j * 2 + ((l >> 2) & 1)) * 2048 + (xcd >> 1) * 512 + (l & 3) * 128 : D * D * 2;
      asm volatile("" : "+v"(tlo));
    }
    auto tlate = [&]() { wt1 = __builtin_amdgcn_raw_buffer_load_b32(wor, tlo, 0, 0); };
    s4 sb[24];
    auto colrd = [&](int h) {
      const unsigned bo = ldsb + (1 + h) * STG + 16384 + (8 * g4 + q) * 128 + (p & 1) * 8;
      const int sw = ((q >> 1) << 2) | ((g4 & 1) << 1);
      const unsigned b0 = bo + (((mw * 4 + (p >> 1)) ^ sw) * 16), b1 = bo + (((mw * 4 + 2 + (p >> 1)) ^ sw) * 16);
#pragma unroll
      for (int n = 0; n < 12; ++n) { RDTR(sb[2 * n], (n & 1) ? b1 : b0, ((n % 6) >> 1) * 8192 + (n / 6) * 4096); RDTR(sb[2 * n + 1], (n & 1) ? b1 : b0, ((n % 6) >> 1) * 8192 + (n / 6) * 4096 + 512); }
      WAITL(0);
      SBAR();
    };
    auto colsum = [&]() {
      const u4 ones = (u4)(0x3F803F80u);
      f4 c[6];
#pragma unroll
      for (int n = 0; n < 12; ++n) c[n % 6] = MFMA16(S8(ones), CAT(sb[2 * n], sb[2 * n + 1]), n >= 6 ? c[n % 6] : (f4)(0.f));
      const u4 csg = (u4){__float_as_uint(g4 ? c[3][0] : c[0][0]), __float_as_uint(g4 ? c[4][0] : c[1][0]), __float_as_uint(g4 ? c[5][0] : c[2][0]), tag};
      WAITV(0);
      asm volatile("" : "+v"(cw) : "v"(pf));
      ST128(csg, l < 32 ? WS_CS + (hd * 16 + mb) * 512 + l * 16 : WS_SZ);
    };
    u4 stv, cv[8], cd; float cs[3];
    auto gload = [&]() {
      stv = LD128(WS_ST + (mb * 128 + mw * 64 + l) * 16);
#pragma unroll
      for (int i = 0; i < 8; ++i) cv[i] = LD128(WS_CS + hd * 8192 + i * 1024 + l * 16);
    };
    auto gbad = [&]() {
      unsigned bad = (stv[1] ^ tag) | (stv[3] ^ tag);
#pragma unroll
      for (int i = 0; i < 8; ++i) bad |= cv[i][3] ^ tag;
      return __ballot(bad != 0u) != 0ull;
    };
    int idx;
    auto gfin = [&]() {
      if (gbad()) { RETRY(); _Pragma("unroll 1") for (unsigned it = 0; it < SPIN; ++it) { SLEEP(2); gload(); if (!gbad()) break; } }
#pragma unroll
      for (int e = 0; e < 3; ++e) {
        float a = __uint_as_float(cv[0][e]);
#pragma unroll
        for (int i = 1; i < 8; ++i) a += __uint_as_float(cv[i][e]);
        auto r = __builtin_amdgcn_permlane32_swap(__float_as_uint(a), __float_as_uint(a), false, false);
        cs[e] = __uint_as_float(r[0]) + __uint_as_float(r[1]);
      }
      if ((TCH == 2 && !spare) || TCH == 8) asm volatile("" : "+v"(cs[0]), "+v"(cs[1]), "+v"(cs[2]));
      else asm volatile("" : "+v"(cs[0]), "+v"(cs[1]), "+v"(cs[2]) : "v"(wt0), "v"(wt1), "v"(wt2), "v"(wt3));
    };
    auto cload = [&]() { cd = LD128(WS_CAND + (((hp >> 2) == 2) * 16 + mb) * 128 + (l & 7) * 16); };
    auto cfin = [&]() {
      if (__builtin_expect(__ballot(cd[1] != tag || cd[3] != tag) != 0ull, 0)) {
        RETRY();
        _Pragma("unroll 1") for (unsigned it = 0; it < SPIN; ++it) { SLEEP(4); cload(); if (__ballot(cd[1] != tag || cd[3] != tag) == 0ull) break; }
      }
      cd[0] = max(cd[0], cd[2]);
      idx = (int)(cd[0] & 2047u);
    };
    auto lpub = [&](bool sig = true) {
      *(LAS float*)(lds + L_M + (mw * 64 + l) * 4) = __uint_as_float(stv[0]);
      *(LAS float*)(lds + L_RS + (mw * 64 + l) * 4) = __uint_as_float(stv[2]);
      *(LAS float*)(lds + L_RL + (mw * 64 + l) * 4) = __uint_as_float(stv[2]) * 1.44269504f;
      if (l < 32) {
#pragma unroll
        for (int e = 0; e < 3; ++e) *(LAS float*)(lds + L_CS + mw * 384 + ((3 * g4 + e) * 16 + r15) * 4) = -cs[e];
      }
      if (mw == 0 && l < 8) *(LAS int*)(lds + L_SEL + l * 4) = idx;
      if (sig) lsig(myFL, 1u);
    };
    u4 xs[4]; float xrs;
    asm volatile("" ::"s"(wg), "s"(wu), "s"(wd), "v"(cw));
    unsigned tw[8] = {0, 0, 0, 0, 0, 0, 0, 0};
    if (TW && moe) {
      if (TWS) SLEEP(TWS);
      const char* gb = (const char*)(mw ? wu : wg) + (256 * ksp * F + 64 * chg) * 2;
      if (TW & 1) asm volatile("s_load_dword %0, %5, 0\n s_load_dword %1, %5, 458752\n s_load_dword %2, %6, 0\n s_load_dword %3, %6, 458752\n s_load_dword %4, %6, 910336"
                               : "+s"(tw[0]), "+s"(tw[1]), "+s"(tw[2]), "+s"(tw[3]), "+s"(tw[4]) : "s"(gb), "s"(gb + 917504) : "memory");
      if (TW & 2) asm volatile("s_load_dword %0, %2, 0\n s_load_dword %1, %3, 0" : "+s"(tw[5]), "+s"(tw[6])
                               : "s"((const char*)wd + ((64 * chunk + 63 * mw) * D + 256 * ksp) * 2), "s"(wo + (j * 32 + mw * 16) * 2048 + (xcd >> 1) * 512) : "memory");
      if (TW & 4) asm volatile("s_load_dword %0, %1, 0" : "+s"(tw[7]) : "s"((const char*)out + (mb * 128 + mw * 127) * 2048 + nb * 128) : "memory");
    }
    PFB(spare ? SPS : SPW, 0);
    if (SP0) { WAITL(0); spf(pfb); }
    if (HPRIO) __builtin_amdgcn_s_setprio(HPRIO);
    TICK();
    if (spare) {
      TICK();
      pois();
      if (ii >> 2) TICK();
      colrd(ii >> 2);
      spf(pfb);
      if (!(ii >> 2)) TICK();
      u4 kk[8];
      auto kload = [&]() {
#pragma unroll
        for (int i = 0; i < 8; ++i) kk[i] = LD128(WS_RG + mw * 8192 + i * 1024 + l * 16);
      };
      kload();
      TICKP();
      _Pragma("unroll 1") for (unsigned it = 0; it < SPIN; ++it) {
        unsigned bad = 0;
#pragma unroll
        for (int i = 0; i < 8; ++i) { bad |= (kk[i][1] ^ tag) | (kk[i][3] ^ tag); asm volatile("" : "+v"(bad)); }
        if (__ballot(bad != 0u) == 0ull) break;
        RETRY();
        kload();
        DTICK(7);
      }
      __builtin_amdgcn_s_setprio(3);
      unsigned m1 = max(kk[0][0], kk[0][2]), m2 = min(kk[0][0], kk[0][2]), m3 = 0;
#pragma unroll
      for (int i = 2; i < 16; ++i) {
        const unsigned a = kk[i >> 1][(i & 1) * 2];
        asm("v_med3_u32 %0, %1, %2, %0" : "+v"(m3) : "v"(m2), "v"(a));
        asm("v_med3_u32 %0, %1, %2, %0" : "+v"(m2) : "v"(m1), "v"(a));
        m1 = max(m1, a);
      }
      unsigned mine8 = 0;
#pragma unroll
      for (int it = 0; it < 8; ++it) {
        const unsigned km = wave_max(m1);
        mine8 = l == it ? km : mine8;
        const bool win = m1 == km;
        if (it < 7) { m1 = win ? m2 : m1; m2 = win ? m3 : m2; m3 = win ? 0u : m3; }
        if (it == 3 && nbar < 7) TICK();
      }
      if (__builtin_expect(__ballot(m1 == 0u) != 0ull, 0)) {
        unsigned lim = 0;
#pragma unroll 1
        for (int it = 0; it < 8; ++it) {
          unsigned m = 0;
#pragma unroll
          for (int i = 0; i < 8; ++i) m = umax3(m, kk[i][0] - lim, kk[i][2] - lim);
          lim += wave_max(m);
          mine8 = l == it ? lim : mine8;
        }
      }
      ST64(((u2){mine8, tag}), l < 8 ? WS_CAND + ((hp >> 3) * 16 + mb) * 128 + (mw ? 7 - l : l) * 16 + mw * 8 : WS_SZ);
      __builtin_amdgcn_s_setprio(HPRIO);
      STMP(2);
      TICKP();
      colsum();
      DTICK(9);
      if (TCH == 8) { tearly(); if (!TS) tlate(); } else if (TCH && TCH != 3) touch();
      spf(pfb);
      while (nbar < 10) TICKQ();
      gload(); cload();
      if (RT && mw) asm volatile("global_load_dword %0, %1, %2 sc1" : "+v"(cw) : "v"(cwo), "s"(cwb) : "memory");
      TICKQ(); TICKP();
      WAITV(0);
      asm volatile("" : "+v"(cw));
      gfin(); cfin();
      STMP(3);
      TICK(); TICK();
      if (TCH == 8 && TS) tlate();
      if (SE) { lpub(false); asm volatile("ds_write_b32 %0, %1\n s_waitcnt lgkmcnt(0)" ::"v"(adFL), "v"((l & 6) == 6 ? 1u : 0u) : "memory"); } else ZFL();
      TICK();
      if (!SE) lpub();
      if (SSL) SLEEP(SSL);
      if (TCH == 8) asm volatile("" ::"v"(wt0), "v"(wt1));
    } else {
      if (PV == 0) pois();
      TICK();
      if (PV == 1) pois();
      if (ii >> 2) TICK();
      colrd(ii >> 2);
      if (TW) asm volatile("" ::"s"(tw[0]), "s"(tw[1]), "s"(tw[2]), "s"(tw[3]), "s"(tw[4]), "s"(tw[5]), "s"(tw[6]), "s"(tw[7]));
      spf(pfb);
      TICK();
      if (CL && !(ii >> 2)) TICK();
      colsum();
      spf(pfb);
      if (!CL && !(ii >> 2)) TICK();
      u4 sv; f4 acc[8];
      auto xload = [&]() {
        const u16* xp = x + (size_t)idx * D + 256 * ksp + 32 * kset + 8 * g4;
        sv = LD128(WS_ST + idx * 16);
#pragma unroll
        for (int it = 0; it < 4; ++it) xs[it] = *(const u4*)(xp + 64 * it);
        SBAR();
      };
      auto xnorm = [&]() {
        if (__builtin_expect(__ballot(sv[1] != tag || sv[3] != tag) != 0ull, 0)) {
          _Pragma("unroll 1") for (unsigned it = 0; it < SPIN; ++it) { SLEEP(1); sv = LD128(WS_ST + idx * 16); if (__ballot(sv[1] != tag || sv[3] != tag) == 0ull) break; }
        }
        const float nm = -__uint_as_float(sv[0]);
        xrs = __uint_as_float(sv[2]);
#pragma unroll
        for (int it = 0; it < 4; ++it) {
          float t0, t1, t2, t3; unsigned x0 = xs[it][0], x1 = xs[it][1], x2 = xs[it][2], x3 = xs[it][3];
          asm volatile("v_dot2_f32_bf16 %4, %0, %8, %10\n v_dot2_f32_bf16 %0, %0, %9, %10\n v_dot2_f32_bf16 %5, %1, %8, %10\n v_dot2_f32_bf16 %1, %1, %9, %10\n"
                       "v_dot2_f32_bf16 %6, %2, %8, %10\n v_dot2_f32_bf16 %2, %2, %9, %10\n v_dot2_f32_bf16 %7, %3, %8, %10\n v_dot2_f32_bf16 %3, %3, %9, %10\n"
                       "v_cvt_pk_bf16_f32 %0, %4, %0\n v_cvt_pk_bf16_f32 %1, %5, %1\n v_cvt_pk_bf16_f32 %2, %6, %2\n s_nop 0\n v_cvt_pk_bf16_f32 %3, %7, %3"
                       : "+v"(x0), "+v"(x1), "+v"(x2), "+v"(x3), "=&v"(t0), "=&v"(t1), "=&v"(t2), "=&v"(t3) : "s"(0x00003F80u), "s"(0x3F800000u), "v"(nm));
          xs[it] = (u4){x0, x1, x2, x3};
        }
        asm volatile("s_nop 1" : "+v"(xs[0]), "+v"(xs[1]), "+v"(xs[2]), "+v"(xs[3]), "+v"(xrs));
        SBAR();
      };
      auto gmul = [&](int i0, int i1) {
#pragma unroll
        for (int it = i0; it < i1; ++it)
#pragma unroll
          for (int c = 0; c < 8; ++c) acc[c] = MFMA16(S8(gw[it][c]), S8(xs[it]), it ? acc[c] : (f4)(0.f));
#pragma unroll
        for (int c = 0; c < 8; ++c) asm volatile("" : "+v"(acc[c]));
        SBAR();
      };
      auto gout = [&]() {
        unsigned go = r15 < 8 ? WS_GP + ((chg * 4 + ksp) * 2 + mw) * 2048 + (g4 >> 1) * 1024 + (g4 & 1) * 512 + r15 * 16 : WS_SZ;
        asm volatile("" : "+v"(go));
#pragma unroll
        for (int i = 0; i < 4; ++i) {
          unsigned a[4], b[4];
#pragma unroll
          for (int c = 0; c < 4; ++c) {
            auto r = __builtin_amdgcn_permlane32_swap(__float_as_uint(acc[c][i]), __float_as_uint(acc[c + 4][i]), false, false);
            a[c] = r[0]; b[c] = r[1];
          }
          unsigned s0, s1, s2, s3;
#define AR(d, x, y) "v_add_f32_dpp %" #d ", %" #x ", %" #y " row_ror:8 row_mask:0xf bank_mask:0xf\n"
          asm volatile("s_nop 1\n" AR(0, 8, 4) AR(1, 9, 5) AR(2, 10, 6) AR(3, 11, 7) : "=&v"(s0), "=&v"(s1), "=&v"(s2), "=&v"(s3)
                       : "v"(a[0]), "v"(a[1]), "v"(a[2]), "v"(a[3]), "v"(b[0]), "v"(b[1]), "v"(b[2]), "v"(b[3]));
          ST128(tagf(__builtin_bit_cast(f4, ((u4){s0, s1, s2, s3})), tag), go + i * 128);
        }
        SBAR();
      };
      for (int i = 0; i < ph; ++i) { spf(pfb); TICK(); }
      burst(0); if (TCH == 1) touch(); if (TCH == 7) touch(1); if (TCH == 8 && TE == 0) tearly();
      spf(pfb);
      TICKQ();
      TICKQ();
      if (TL) { transp(gw[0]); transp(gw[1]); }
      TICKP();
      if (!TL) transp(gw[0]);
      if (SP2) spf(pfb);
      TICKP();
      if (!TL) transp(gw[1]);
      if (SP2) spf(pfb);
      TICK();
      if (ph) cload();
      if (GE == 1) gload();
      if (RT && mw) asm volatile("global_load_dword %0, %1, %2 sc1" : "+v"(cw) : "v"(cwo), "s"(cwb) : "memory");
      SBAR();
      burst(1); if (TCH == 4) touch(); if (TCH == 8 && TE == 1) tearly();
      spf(pfb);
      TICKQ();
      if (TCH == 5) touch();
      if (TCH == 7) touch(2);
      if (TCH == 8 && TE == 2) tearly();
      if (GE && !ph) cload();
      if (GE == 2) gload();
      TICK();
      if (ph < 2) spf(pfb);
      if (GE == 1) gfin();
      TICK();
      transp(gw[2]);
      WAITV(0);
      asm volatile("" : "+v"(cw));
      if (GE == 2) gfin();
      if (ph < 2) TICK();
      transp(gw[3]);
      if (GE) {
        if (ph < 1) TICK();
        cfin();
        ZFL();
        TICK();
        xload();
      } else {
        if (ph < 1) { SBAR(); cload(); TICK(); }
        SBAR();
        if (XO == 0) { cfin(); xload(); gload(); } else { gload(); SBAR(); cfin(); xload(); }
        if (TCH == 2) { touch(); SBAR(); }
        if (TCH == 8 && (TLP == 0 || (TLP == 3 && ph == 0))) { tlate(); SBAR(); }
        ZFL();
        TICK();
        if (TCH == 8 && (TLP == 1 || (TLP == 3 && ph != 0))) { tlate(); SBAR(); }
        gfin();
      }
      lpub();
      STMP(12);
      SBAR();
      __builtin_amdgcn_s_setprio(0);
      if (TCH == 8 && TLP == 4) { tlate(); SBAR(); }
      burst(2, 0, D1);
      if (TCH == 8 && TLP == 2) tlate();
      STMP(3);
      SBAR();
      if (HS0) SLEEP(HS0);
      if (G1 & 1) { lwait<HW1>(adFL, 0x0Fu, G1 >> 4); if (G1S) SLEEP(G1S); __builtin_amdgcn_s_setprio(TP0); }
      xnorm();
      if (G1 & 2) { lwait<2>(adFL, 0x0Fu, G1 >> 4); if (G1S) SLEEP(G1S); __builtin_amdgcn_s_setprio(TP0); }
      STMP(5);
      gmul(0, 4);
      gout();
      if (TCH == 2 || TCH == 8) asm volatile("" ::"v"(wt0), "v"(wt1));
      STMP(6);
      SBAR();
    }
    __builtin_amdgcn_s_setprio(TP1);
    STMP(4);
    if (moe) {
      if (D2 == 0) { burst(2, D1, 16); SBAR(); }
      if (D1 >= 8 || D2 == 0) transp(dw[0]); if (D1 == 16 || D2 == 0) transp(dw[1]);
      SBAR();
      lwait<2>(adFL, 0x30u, 3);
      if (G2S) SLEEP(G2S);
      STMP(7);
      if (D2 == 1) { burst(2, D1, 16); SBAR(); }
      f4 acc[8];
      u4 ha[2];
      {
        u4 pg[4][2];
        auto pload = [&]() {
#pragma unroll
          for (int ks = 0; ks < 4; ++ks)
#pragma unroll
            for (int t = 0; t < 2; ++t) pg[ks][t] = LD128(WS_GP + ((chunk * 4 + ks) * 4 + 2 * t + mw) * 1024 + (4 * kset + g4) * 128 + np * 16);
        };
        auto pbad = [&]() {
          unsigned bad = 0;
#pragma unroll
          for (int i = 0; i < 8; ++i) untag(pg[i >> 1][i & 1], tag, bad);
          return __ballot((bad & 255u) != 0u) != 0ull;
        };
        pload();
        SBAR();
        if (D2 == 2) { burst(2, D1, 16); SBAR(); }
        __builtin_amdgcn_s_setprio(TP2);
        if (D1 < 8 && D2 == 1) transp(dw[0]);
        if (D1 < 16 && D2 == 1) transp(dw[1]);
        SBAR();
        if (__builtin_expect(pbad(), 0)) { _Pragma("unroll 1") for (unsigned it = 0; it < SPIN; ++it) { RETRY2(); SLEEP(4); pload(); if (!pbad()) break; } }
        STMP(10);
        f4 gq[2];
#pragma unroll
        for (int t = 0; t < 2; ++t)
          gq[t] = ((__builtin_bit_cast(f4, pg[0][t]) + __builtin_bit_cast(f4, pg[1][t])) + (__builtin_bit_cast(f4, pg[2][t]) + __builtin_bit_cast(f4, pg[3][t]))) * xrs;
        float hv[4];
#pragma unroll
        for (int e = 0; e < 4; ++e) {
          const float gg = rbf(gq[0][e]), uu = rbf(gq[1][e]);
          hv[e] = rbf(gg / (1.f + __expf(-gg))) * uu;
        }
        *(LAS u2*)(lds + L_HX + l * 16 + mw * 8) = (u2){pk2(hv[0], hv[1]), pk2(hv[2], hv[3])};
        lsig(myFL, 2u);
        if (D1 < 16 && D2 == 2) { SBAR(); if (D1 < 8) transp(dw[0]); transp(dw[1]); SBAR(); }
        lwait(adFL, 0xC0u, 2);
        ha[0] = *(const LAS u4*)(lds + L_HX + l * 16); ha[1] = *(const LAS u4*)(lds + L_HX + (l ^ 8) * 16);
      }
#pragma unroll
      for (int it = 0; it < 2; ++it)
#pragma unroll
        for (int c = 0; c < 8; ++c) acc[c] = MFMA16(S8(ha[it]), S8(dw[it][c]), it ? acc[c] : (f4)(0.f));
      if (g4 < 2) {
#pragma unroll
        for (int i = 0; i < 4; ++i) {
          const unsigned yo = WS_YP + ((chunk * 8 + 4 * g4 + i) * D + 256 * ksp + 128 * mw + 4 * r15) * 4;
          ST128(tagf((f4){acc[0][i], acc[1][i], acc[2][i], acc[3][i]}, tag), yo);
          ST128(tagf((f4){acc[4][i], acc[5][i], acc[6][i], acc[7][i]}, tag), yo + 256);
        }
      }
      STMP(11);
    }
    float sg;
    {
      const unsigned mono = cd[0] >> 16, rb = (mono & 0x8000u) ? (mono & 0x7FFFu) : (~mono & 0xFFFFu);
      sg = rbf(1.f / (1.f + __expf(-lo(rb))));
    }
    unsigned mine = (unsigned)__ballot(!NOMOE && l < 8 && (idx >> 7) == mb && (l & 1) == mw);
    if (mine) {
      if (spare) lwait<4>(adFL, 0x30u, 3);
      {
        const u2 f = LD64(WS_OH + (mb * 16 + r15) * 128);
        const unsigned t0 = NOW();
        unsigned rel = f[1] + YD - t0;
        rel = wave_max(f[0] == tag && rel < 600u ? rel : 0u);
        _Pragma("unroll 1") for (unsigned it = 0; it < SPIN; ++it) { if (NOW() - t0 >= rel) break; SLEEP(2); }
      }
      STMP(8);
      lwait<1>(adFL, 0x0Fu, 4);
      __builtin_amdgcn_s_setprio(TP2);
      while (mine) {
        const int t = __builtin_ctz(mine); mine &= mine - 1;
        u4 yv[14];
        _Pragma("unroll 1") for (unsigned it = 0; it < SPIN; ++it) {
          unsigned bad = 0;
#pragma unroll
          for (int i = 0; i < 14; ++i) yv[i] = LD128(WS_YP + (((i * 4 + g4) * 8 + t) * D + (nb >> 1) * 128 + (r15 >> 3) * 64 + (nb & 1) * 32 + (r15 & 7) * 4) * 4);
          SBAR();
#pragma unroll
          for (int i = 0; i < 14; ++i) untag(yv[i], tag, bad);
          if (__ballot((bad & 255u) != 0u) == 0ull) break;
          RETRY();
          SLEEP(YSL);
        }
        f4 y = __builtin_bit_cast(f4, yv[0]);
#pragma unroll
        for (int i = 1; i < 14; ++i) y += __builtin_bit_cast(f4, yv[i]);
        const float gt = __int_as_float(__builtin_amdgcn_readlane(__float_as_int(sg), t));
#pragma unroll
        for (int e = 0; e < 4; ++e) {
          auto r = __builtin_amdgcn_permlane32_swap(__float_as_uint(y[e]), __float_as_uint(y[e]), false, false);
          const float a = __uint_as_float(r[0]) + __uint_as_float(r[1]);
          auto r2 = __builtin_amdgcn_permlane16_swap(__float_as_uint(a), __float_as_uint(a), false, false);
          y[e] = rbf(rbf(__uint_as_float(r2[0]) + __uint_as_float(r2[1])) * gt);
        }
        if (l < 16) *(LAS f4*)(lds + L_KVX + t * 256 + (l & 7) * 32 + (l >> 3) * 16) = y;
      }
    }
    lsig(myFL, 3u);
    STMP(9);
    STFLUSH();
    if (spare) asm volatile(".Lend_s:"); else asm volatile(".Lend_h:");
    };
    if (sp) helper(BC<true>()); else helper(BC<false>());
    return;
  }

  const int wr = w >> 1, wc = w & 1;
  u2 kg; u4 sg4; unsigned tag;
  {
    const int row = srow;
    SBAR(); BST(0); SBAR();
    asm volatile("s_waitcnt vmcnt(4)" : "+v"(xa[0]), "+v"(xa[1]), "+v"(xa[2]), "+v"(xa[3]));
    SBAR(); BST(1); SBAR();
    float s0 = 0, s1 = 0;
#pragma unroll
    for (int c = 0; c < 4; ++c)
#pragma unroll
      for (int e = 0; e < 4; e += 2) { unsigned x0 = xa[c][e], x1 = xa[c][e + 1]; s0 = dot2(x0, 0x3F803F80u, s0); s1 = dot2(x1, 0x3F803F80u, s1); }
    const float m = half_sum(s0 + s1) * (1.f / D);
    f2 v[16], q2 = (f2)(0.f), q3 = (f2)(0.f);
#pragma unroll
    for (int c = 0; c < 16; ++c) v[c] = (f2){lo(xa[c >> 2][c & 3]), hi(xa[c >> 2][c & 3])} - m;
#pragma unroll
    for (int c = 0; c < 16; c += 2) { q2 += v[c] * v[c]; q3 += v[c + 1] * v[c + 1]; }
    q2 += q3;
    const float mr = rbf(m);
    const float rs = 1.f / rbf(sqrtf(rbf(rbf(half_sum(q2[0] + q2[1]) * (1.f / D)) + 1e-5f)));
    float d0 = 0, d1 = 0;
    asm volatile("s_waitcnt vmcnt(0)" : "+v"(rw[0]), "+v"(rw[1]), "+v"(rw[2]), "+v"(rw[3]), "+v"(d0) : "v"(rs));
#pragma unroll
    for (int c = 0; c < 16; c += 2) {
      const f2 e0 = (f2){lo(xa[c >> 2][c & 3]), hi(xa[c >> 2][c & 3])} - mr, e1 = (f2){lo(xa[c >> 2][(c & 3) + 1]), hi(xa[c >> 2][(c & 3) + 1])} - mr;
      const unsigned p0 = pk2(e0[0], e0[1]), p1 = pk2(e1[0], e1[1]);
      const f2 n0 = (f2){lo(p0), hi(p0)} * rs, n1 = (f2){lo(p1), hi(p1)} * rs;
      unsigned c0 = rw[c >> 2][c & 3], c1 = rw[c >> 2][(c & 3) + 1];
      d0 = dot2(pk2(n0[0], n0[1]), c0, d0); d1 = dot2(pk2(n1[0], n1[1]), c1, d1);
    }
    const float d = half_sum(d0 + d1);
    const unsigned rb = pk2(d, d) & 0xFFFFu;
    const unsigned mono = (rb & 0x8000u) ? (~rb & 0xFFFFu) : (rb | 0x8000u);
    SBAR();
    GETTAG(tg); tag = tg;
    kg = (u2){(mono << 16) | (unsigned)row, tag};
    sg4 = (u4){__float_as_uint(mr), tag, __float_as_uint(rs), tag};
  }
  MKWS(tag);
  SBAR(); BST(2); SBAR();
  if ((l & 31) == 0) { ST64(kg, WS_RG + srow * 8); ST128(sg4, WS_ST + srow * 16); }
  unsigned aad[2], bad[2];
#pragma unroll
  for (int s = 0; s < 2; ++s) aad[s] = ldsb + (wr * 64 + r15) * 128 + (((4 * s + g4) ^ ((r15 >> 1) & 7)) * 16);
#pragma unroll
  for (int nh = 0; nh < 2; ++nh) bad[nh] = ldsb + 16384 + (8 * g4 + q) * 128 + (((wc * 4 + 2 * (nh ^ wr) + (p >> 1)) ^ (((q >> 1) << 2) | ((g4 & 1) << 1))) * 16) + (p & 1) * 8;
  f4 acc[4][6];
#pragma unroll
  for (int m = 0; m < 4; ++m)
#pragma unroll
    for (int n = 0; n < 6; ++n) { acc[m][n] = (f4)(0.f); asm volatile("" : "+v"(acc[m][n])); }
#define MF(m, n, Fq) do { if (n < 2) acc[m][n] = MFMA16(CAT(Fq.b[2 * n], Fq.b[2 * n + 1]), Fq.a[m], acc[m][n]); \
     else acc[m][n] = MFMA16(Fq.a[m], CAT(Fq.b[2 * n], Fq.b[2 * n + 1]), acc[m][n]); SBAR(); } while (0)
#define RB(G, n, vb0, vb1, SS, hf) RDTR(G.b[2 * (n) + (hf)], (((n) & 1) ? vb1 : vb0), ((n) >> 1) * 8192 + (SS) * 4096 + (hf) * 512)
#define RA(G, m, va) RD128(G.a[m], va, (m) * 2048)
#define SUB(Fq, G, va, vb0, vb1, SS, PRE) do { \
    WAITL(13); SBAR(); \
    MF(0, 0, Fq); PRE; RA(G, 0, va); SBAR(); \
    WAITL(11); SBAR(); \
    MF(1, 0, Fq); RB(G, 0, vb0, vb1, SS, 0); SBAR(); MF(2, 0, Fq); RB(G, 0, vb0, vb1, SS, 1); SBAR(); MF(3, 0, Fq); RA(G, 1, va); SBAR(); \
    WAITL(12); SBAR(); \
    MF(0, 1, Fq); RA(G, 2, va); SBAR(); MF(1, 1, Fq); RA(G, 3, va); SBAR(); MF(2, 1, Fq); RB(G, 1, vb0, vb1, SS, 0); SBAR(); MF(3, 1, Fq); RB(G, 1, vb0, vb1, SS, 1); SBAR(); \
    WAITL(8); SBAR(); \
    MF(0, 2, Fq); RB(G, 2, vb0, vb1, SS, 0); SBAR(); MF(1, 2, Fq); RB(G, 2, vb0, vb1, SS, 1); SBAR(); MF(2, 2, Fq); RB(G, 3, vb0, vb1, SS, 0); SBAR(); MF(3, 2, Fq); RB(G, 3, vb0, vb1, SS, 1); SBAR(); \
    MF(0, 3, Fq); RB(G, 4, vb0, vb1, SS, 0); SBAR(); MF(1, 3, Fq); RB(G, 4, vb0, vb1, SS, 1); SBAR(); MF(2, 3, Fq); RB(G, 5, vb0, vb1, SS, 0); SBAR(); MF(3, 3, Fq); RB(G, 5, vb0, vb1, SS, 1); SBAR(); \
    MF(0, 4, Fq); MF(1, 4, Fq); MF(2, 4, Fq); MF(3, 4, Fq); MF(0, 5, Fq); MF(1, 5, Fq); MF(2, 5, Fq); MF(3, 5, Fq); } while (0)
#define KSTEP(MODE) do { \
    if (kt == sx) { const unsigned xo = xad + buf * STG; \
      _Pragma("unroll") for (int t = 0; t < 8; ++t) { const unsigned a_ = xo ^ (t * 16); \
        asm volatile("ds_read_b64 %0, %1" : "+v"(xv[t]) : "v"(a_) : "memory"); } \
      WAITL(0); SBAR(); } \
    const int nbuf = (buf + 1) & 3; \
    const unsigned bo = buf * STG, nbo = nbuf * STG; \
    { unsigned va = aad[1] + bo, vb0 = bad[0] + bo, vb1 = bad[1] + bo; SUB(f0, f1, va, vb0, vb1, 1, ); } \
    if (MODE < 2) { \
      unsigned va = aad[0] + nbo, vb0 = bad[0] + nbo, vb1 = bad[1] + nbo; \
      SUB(f1, f0, va, vb0, vb1, 0, if (MODE == 0) { WAITL(0); BAR(); } else asm volatile("ds_write_b32 %0, %1" ::"v"(myFL), "v"(1u) : "memory"); SBAR()); \
    } else { asm volatile("ds_write_b32 %1, %2\n ds_read_b32 %0, %3\n s_waitcnt lgkmcnt(2)" : "=&v"(fl0) : "v"(myFL), "v"(2u), "v"(adFL) : "memory"); SBAR(); \
      MF(0, 2, f1); MF(1, 2, f1); MF(2, 2, f1); MF(3, 2, f1); MF(0, 3, f1); MF(1, 3, f1); MF(2, 3, f1); MF(3, 3, f1); \
      MF(0, 4, f1); MF(1, 4, f1); MF(2, 4, f1); MF(3, 4, f1); MF(0, 5, f1); MF(1, 5, f1); MF(2, 5, f1); MF(3, 5, f1); \
      MF(0, 0, f1); MF(1, 0, f1); MF(2, 0, f1); MF(3, 0, f1); MF(0, 1, f1); MF(1, 1, f1); MF(2, 1, f1); MF(3, 1, f1); } \
    buf = nbuf; } while (0)
#define SUBK(Fq, G, va, vb0, vb1, SS, W0, X, PRE, POST) do { \
    WAITL(W0); SBAR(); \
    MF(0, 2, Fq); PRE; RA(G, 0, va); SBAR(); \
    WAITL(7 + (X)); SBAR(); \
    MF(1, 2, Fq); RB(G, 2, vb0, vb1, SS, 0); SBAR(); MF(2, 2, Fq); RB(G, 2, vb0, vb1, SS, 1); SBAR(); MF(3, 2, Fq); RA(G, 1, va); SBAR(); \
    WAITL(8 + (X)); SBAR(); \
    MF(0, 3, Fq); RA(G, 2, va); SBAR(); MF(1, 3, Fq); RA(G, 3, va); SBAR(); MF(2, 3, Fq); RB(G, 3, vb0, vb1, SS, 0); SBAR(); MF(3, 3, Fq); RB(G, 3, vb0, vb1, SS, 1); SBAR(); \
    WAITL(10 + (X)); SBAR(); \
    MF(0, 4, Fq); RB(G, 4, vb0, vb1, SS, 0); SBAR(); MF(1, 4, Fq); RB(G, 4, vb0, vb1, SS, 1); SBAR(); MF(2, 4, Fq); RB(G, 5, vb0, vb1, SS, 0); SBAR(); MF(3, 4, Fq); RB(G, 5, vb0, vb1, SS, 1); SBAR(); \
    WAITL(12); SBAR(); \
    MF(0, 5, Fq); POST; MF(1, 5, Fq); MF(2, 5, Fq); MF(3, 5, Fq); } while (0)
#define RQ(G, va, vb0, vb1, SS) do { RA(G, 0, va); RB(G, 0, vb0, vb1, SS, 0); RB(G, 0, vb0, vb1, SS, 1); RA(G, 1, va); RA(G, 2, va); RA(G, 3, va); \
    RB(G, 1, vb0, vb1, SS, 0); RB(G, 1, vb0, vb1, SS, 1); SBAR(); } while (0)
#define MQ(Fq) do { MF(0, 0, Fq); MF(1, 0, Fq); MF(2, 0, Fq); MF(3, 0, Fq); MF(0, 1, Fq); MF(1, 1, Fq); MF(2, 1, Fq); MF(3, 1, Fq); } while (0)
  u2 xv[8];
#pragma unroll
  for (int t = 0; t < 8; ++t) asm volatile("" : "=v"(xv[t]));
  const int sx = (nb - rot) & 15;
  const unsigned xad = ldsb + (w * 32 + (l & 31)) * 128 + ((r15 >> 1) & 7) * 16 + (l >> 5) * 8;
  STMP(1);
  WAITL(0);
  BAR();
  STMP(2);
  WAITL(0);
  FQ f0, f1;
  { unsigned va = aad[0], vb0 = bad[0], vb1 = bad[1];
    RA(f0, 0, va); RB(f0, 0, vb0, vb1, 0, 0); RB(f0, 0, vb0, vb1, 0, 1); RA(f0, 1, va); RA(f0, 2, va); RA(f0, 3, va);
    RB(f0, 1, vb0, vb1, 0, 0); RB(f0, 1, vb0, vb1, 0, 1); RB(f0, 2, vb0, vb1, 0, 0); RB(f0, 2, vb0, vb1, 0, 1); RB(f0, 3, vb0, vb1, 0, 0); RB(f0, 3, vb0, vb1, 0, 1);
    RB(f0, 4, vb0, vb1, 0, 0); RB(f0, 4, vb0, vb1, 0, 1); RB(f0, 5, vb0, vb1, 0, 0); RB(f0, 5, vb0, vb1, 0, 1); SBAR(); }
  unsigned sad = ldsb + L_M + wr * 256 + g4 * 16, cad = ldsb + L_CS + wc * 384 + r15 * 4 + wr * 64;
  const int hd = hp * 2 + wc;
  unsigned kvo = WS_KVP + (hd * 16 + mb) * 2048 + wr * 1024 + l * 16, zo = r15 == 0 ? WS_ZP + (hd * 16 + mb) * 128 + g4 * 32 + wr * 16 : WS_SZ;
  unsigned xw = ldsb + L_KVX + w * 3072 + l * 16, xr = ldsb + L_KVX + (w ^ 2) * 3072 + l * 16;
  asm volatile("s_waitcnt lgkmcnt(0)" : "+v"(sad), "+v"(cad), "+v"(kvo), "+v"(zo), "+v"(xw), "+v"(xr) : : "memory"); SBAR();
  int buf = 0; unsigned fl0;
#pragma unroll 1
  for (int kt = 0; kt < 14; ++kt) KSTEP(0);
  STMP(14); SBAR();
  f4 sm[4], sr[4], sl[4]; f2 ck, cv;
  unsigned c2 = 2u, c3 = 3u, vfl = myFL, b30, b31;
  u4 ones = (u4)(0x3F803F80u);
#define SC0() asm volatile("ds_read_b32 %0, %2 offset:128\n ds_read_b32 %1, %2 offset:256" : "=&v"(ck[0]), "=&v"(cv[0]) : "v"(cad) : "memory")
#define SC1() asm volatile("ds_read_b32 %0, %2 offset:128\n ds_read_b32 %1, %2 offset:256" : "=&v"(ck[1]), "=&v"(cv[1]) : "v"(cad ^ 64) : "memory")
  const unsigned b20 = bad[0] + 2 * STG, b21 = bad[1] + 2 * STG, a21 = aad[1] + 2 * STG;
  unsigned a20, a30, a31, pfl2, lm, lc, ld, pw0, pw1, pws = ldsb + L_PQ + wr * 8200 + wc * 4096; int sxl = sx;
#define SM(m) do { RD128(sm[m], sad, (m) * 64); RD128(sl[m], sad, L_RL - L_M + (m) * 64); RD128(sr[m], sad, L_RS - L_M + (m) * 64); } while (0)
  {
    { const unsigned va = a21; SUBK(f0, f1, va, b20, b21, 1, 6, 0, asm volatile("" ::"v"(f0.b[0]), "v"(f0.b[1]), "v"(f0.b[2]), "v"(f0.b[3])), ); }
    { const unsigned va = a30 = aad[0] + 3 * STG; b30 = bad[0] + 3 * STG; b31 = bad[1] + 3 * STG;
      unsigned ft, late = 0;
      SUBK(f1, f0, va, b30, b31, 0, 9, LF, asm volatile("" : "+v"(c2), "+v"(c3), "+v"(vfl)); if (LF) asm volatile("ds_read_b32 %0, %1" : "=v"(ft) : "v"(adFL) : "memory"),
           if (LF) { asm volatile("" : "+v"(ft)); late = (unsigned)__ballot(ft < 1u) & 0x30u; asm volatile("" : "+s"(late)); SBAR(); });
      if (__builtin_expect(late != 0u, 0)) {
        RETRY2();
        lwait(adFL, 0x30u, 1);
#define RAP(G, m, va) asm volatile("ds_read_b128 %0, %1 offset:%2" : "+v"(G.a[m]) : "v"(va), "i"((m) * 2048) : "memory")
#define RBP(G, n, hf) asm volatile("ds_read_b64_tr_b16 %0, %1 offset:%2" : "+v"(G.b[2 * (n) + (hf)]) : "v"(((n) & 1) ? b31 : b30), "i"(((n) >> 1) * 8192 + (hf) * 512) : "memory")
        RAP(f0, 0, va); RBP(f0, 2, 0); RBP(f0, 2, 1); RAP(f0, 1, va); RAP(f0, 2, va); RAP(f0, 3, va);
        RBP(f0, 3, 0); RBP(f0, 3, 1); RBP(f0, 4, 0); RBP(f0, 4, 1); RBP(f0, 5, 0); RBP(f0, 5, 1);
      }
      SBAR();
    }
    STMP(7); SBAR();
    { const unsigned va = a31 = aad[1] + 3 * STG; SUBK(f0, f1, va, b30, b31, 1, 9, 0, asm volatile("" : "+v"(ones)), a20 = aad[0] + 2 * STG; pfl2 = vfl ^ 8; asm volatile("" : "+v"(a20), "+v"(pfl2), "+s"(pws), "+s"(sxl)); SBAR()); }
    asm volatile("ds_write_b32 %1, %2\n ds_read_b32 %0, %3\n s_waitcnt lgkmcnt(8)" : "=&v"(fl0) : "v"(vfl), "v"(c2), "v"(adFL) : "memory"); SBAR();
    MF(0, 2, f1); SC0(); SBAR(); MF(1, 2, f1); SC1(); SBAR(); MF(2, 2, f1); MF(3, 2, f1); WAITL(10); SBAR();
    MF(0, 3, f1); SM(0); SBAR(); MF(1, 3, f1); MF(2, 3, f1); SM(1); SBAR(); MF(3, 3, f1);
    WAITL(12); SBAR();
    MF(0, 4, f1); SM(2); SBAR(); MF(1, 4, f1); MF(2, 4, f1); SM(3); SBAR(); MF(3, 4, f1); MF(0, 5, f1); MF(1, 5, f1); MF(2, 5, f1); MF(3, 5, f1);
  }
  asm volatile("s_waitcnt lgkmcnt(0)" : "+v"(fl0) : : "memory");
  if (__builtin_expect(((unsigned)__ballot(fl0 < 1u) & 0xC0u) != 0u, 0)) {
    lwait(adFL, 0xC0u, 1);
    SC0(); SC1(); SM(0); SM(1); SM(2); SM(3); WAITL(0);
  }
  asm volatile("" : "+v"(ck), "+v"(cv), "+v"(sm[0]), "+v"(sm[1]), "+v"(sm[2]), "+v"(sm[3]), "+v"(sr[0]), "+v"(sr[1]), "+v"(sr[2]), "+v"(sr[3]));
  asm volatile("" : "+v"(sl[0]), "+v"(sl[1]), "+v"(sl[2]), "+v"(sl[3]));
  STMP(3);
  __builtin_amdgcn_s_setprio(2);
  SBAR(); AL8(); SBAR();
  {
    f4 kv[4], zt[2];
    u4 kf[2][2], vf[2][2];
#define MKV(t, i) do { if ((i) == 2 || (i) == 5) zt[(i) == 2] = MFMA16(S8(kf[t][(i) == 2]), S8(ones), (t) ? zt[(i) == 2] : (f4)(0.f)); \
      else kv[((i) + 2) % 5] = MFMA16(S8(kf[t][(((i) + 2) % 5) & 1]), S8(vf[t][(((i) + 2) % 5) >> 1]), (t) ? kv[((i) + 2) % 5] : (f4)(0.f)); SBAR(); } while (0)
#pragma unroll
    for (int m = 0; m < 4; ++m) {
      const int t = m >> 1, mm = m & 1;
      f4 ea[2], pp[2];
#pragma unroll
      for (int a = 0; a < 2; ++a) {
        const f4 uv = (acc[m][4 + a] + sm[m] * cv[a]) * sr[m];
        vf[t][a][2 * mm] = pk2(uv[0], uv[1]); vf[t][a][2 * mm + 1] = pk2(uv[2], uv[3]);
        const f4 uk = acc[m][2 + a] + sm[m] * ck[a];
        ea[a] = uk * sl[m]; pp[a] = uk * sr[m] + 1.f;
      }
      SBAR();
#pragma unroll
      for (int e = 0; e < 8; ++e) { if (t && (e == 0 || e == 3 || e == 6)) MKV(0, 3 * mm + e / 3); ea[e >> 2][e & 3] = __builtin_amdgcn_exp2f(ea[e >> 2][e & 3]); SBAR(); }
      AL8(); SBAR();
#pragma unroll
      for (int e = 0; e < 8; ++e) pp[e >> 2][e & 3] = __builtin_amdgcn_fmed3f(ea[e >> 2][e & 3], pp[e >> 2][e & 3], 1.f);
      SBAR();
#pragma unroll
      for (int a = 0; a < 2; ++a) { pp[a] += 1e-6f; kf[t][a][2 * mm] = pk2(pp[a][0], pp[a][1]); kf[t][a][2 * mm + 1] = pk2(pp[a][2], pp[a][3]); }
      SBAR();
    }
    MKV(1, 0); MKV(1, 1); MKV(1, 2); MKV(1, 3);
    asm volatile("ds_write_b128 %0, %1" ::"v"(xw), "v"(kv[2]) : "memory"); SBAR();
    MKV(1, 4);
    asm volatile("ds_write_b128 %0, %1 offset:1024" ::"v"(xw), "v"(kv[3]) : "memory"); SBAR();
    MKV(1, 5);
    asm volatile("ds_write_b128 %0, %1 offset:2048\n ds_write_b32 %2, %3" ::"v"(xw), "v"(zt[1]), "v"(vfl), "v"(c3) : "memory");
    f4 r0, r1, rz; unsigned v;
    asm volatile("ds_read_b32 %0, %4\n ds_read_b128 %1, %5 offset:1024\n ds_read_b128 %2, %5\n ds_read_b128 %3, %5 offset:2048"
                 : "=&v"(v), "=&v"(r0), "=&v"(r1), "=&v"(rz) : "v"(pfl2), "v"(xr) : "memory");
    RQ(f0, a20, b20, b21, 0);
    lm = ldsb + L_M + wr * 256 + r15 * 4; lc = ldsb + L_CS + wc * 384 + wr * 64 + g4 * 16; pw0 = pws + l * 16; pw1 = pw0 ^ 8; ld = lc ^ 64;
    asm volatile("" : "+v"(lm), "+v"(lc), "+v"(pw0), "+v"(pw1), "+v"(ld)); SBAR();
    unsigned nx;
    asm volatile("s_mov_b32 %4, 0x200000\n s_branch .Lxw\n .p2align 3\n s_nop 0\n.Lxl:\n ds_read_b32 %0, %5\n ds_read_b128 %1, %6 offset:1024\n ds_read_b128 %2, %6\n ds_read_b128 %3, %6 offset:2048\n"
                 "s_sub_u32 %4, %4, 1\n s_waitcnt lgkmcnt(0)\n s_cbranch_scc1 .Lxd\n.Lxw:\n s_waitcnt lgkmcnt(8)\n v_cmp_gt_u32 vcc, 3, %0\n s_cbranch_vccnz .Lxl\n.Lxd:"
                 : "+v"(v), "+v"(r0), "+v"(r1), "+v"(rz), "=&s"(nx) : "v"(pfl2), "v"(xr) : "memory", "scc", "vcc");
    const f4 s0 = kv[0] + r0, s1 = kv[1] + r1, sz = zt[0] + rz;
    if (ZF) { ST128(__builtin_bit_cast(u4, sz), zo); SBAR(); }
    u4 o; o[0] = pk2(s0[0], s0[1]); o[1] = pk2(s0[2], s0[3]); o[2] = pk2(s1[0], s1[1]); o[3] = pk2(s1[2], s1[3]);
    ST128(o, kvo);
    if (!ZF) ST128(__builtin_bit_cast(u4, sz), zo);
  }
  STMP(4);
  {
    SBAR();
#define STQ(Fq, G, va, vb0, vb1, SS, POST) do { WAITL(2); SBAR(); \
      MF(0, 0, Fq); RA(G, 0, va); RB(G, 0, vb0, vb1, SS, 0); SBAR(); MF(1, 0, Fq); RB(G, 0, vb0, vb1, SS, 1); RA(G, 1, va); SBAR(); \
      MF(2, 0, Fq); RA(G, 2, va); RA(G, 3, va); SBAR(); MF(3, 0, Fq); RB(G, 1, vb0, vb1, SS, 0); RB(G, 1, vb0, vb1, SS, 1); WAITL(8); SBAR(); \
      MF(0, 1, Fq); POST; SBAR(); MF(1, 1, Fq); MF(2, 1, Fq); MF(3, 1, Fq); } while (0)
    unsigned c4 = 4u;
    STQ(f0, f1, a21, b20, b21, 1, );
    STQ(f1, f0, a30, b30, b31, 0, );
    unsigned lr;
    STQ(f0, f1, a31, b30, b31, 1, lr = lm + (L_RL - L_M); asm volatile("" : "+v"(lr)));
    f4 c0, c1; f2 mv[2], rv[2], rl[2];
#define RD2(d, a, o) asm volatile("ds_read2_b32 %0, %1 offset0:%2 offset1:%3" : "=v"(d) : "v"(a), "i"(o), "i"((o) + 16) : "memory")
    WAITL(2); SBAR();
    MF(0, 0, f1); RD128(c0, lc, 0); RD128(c1, ld, 0); SBAR(); MF(1, 0, f1); RD2(mv[0], lm, 0); RD2(rl[0], lr, 0); SBAR();
    MF(2, 0, f1); RD2(rv[0], lm, 128); RD2(mv[1], lm, 32); SBAR(); MF(3, 0, f1); RD2(rl[1], lr, 32); RD2(rv[1], lm, 160); WAITL(8); SBAR();
    MF(0, 1, f1); asm volatile("" : "+v"(c4)); SBAR(); MF(1, 1, f1); MF(2, 1, f1); MF(3, 1, f1);
    asm volatile("s_waitcnt lgkmcnt(0)" : "+v"(c0), "+v"(c1), "+v"(mv[0]), "+v"(mv[1]), "+v"(rv[0]), "+v"(rv[1]), "+v"(rl[0]), "+v"(rl[1]) : : "memory");
    if (__builtin_expect(__builtin_amdgcn_readfirstlane(sxl) >= 14, 0)) {
      const unsigned xo = xad + (sxl - 12) * STG;
#pragma unroll
      for (int t = 0; t < 8; ++t) { const unsigned a_ = xo ^ (t * 16); asm volatile("ds_read_b64 %0, %1" : "+v"(xv[t]) : "v"(a_) : "memory"); }
    }
    SBAR(); AL8(); SBAR();
    u2 ph[2][2];
#pragma unroll
    for (int m = 0; m < 4; ++m) {
      f4 ea[2], pp[2];
#pragma unroll
      for (int a = 0; a < 2; ++a) {
        const f4 u = acc[m][a] + (a ? c1 : c0) * mv[m >> 1][m & 1];
        ea[a] = u * rl[m >> 1][m & 1]; pp[a] = u * rv[m >> 1][m & 1] + 1.f;
      }
      SBAR();
#pragma unroll
      for (int e = 0; e < 8; ++e) ea[e >> 2][e & 3] = __builtin_amdgcn_exp2f(ea[e >> 2][e & 3]);
      asm volatile(".p2align 3" ::"v"(ea[0]), "v"(ea[1][0]), "v"(ea[1][1]), "v"(ea[1][2]), "v"(pp[0]), "v"(pp[1])); SBAR();
#pragma unroll
      for (int e = 0; e < 8; ++e) pp[e >> 2][e & 3] = __builtin_amdgcn_fmed3f(ea[e >> 2][e & 3], pp[e >> 2][e & 3], 1.f);
      asm volatile("" ::"v"(pp[0]), "v"(pp[1])); SBAR();
      pp[0] += 1e-6f; pp[1] += 1e-6f;

#pragma unroll
      for (int a = 0; a < 2; ++a) ph[a][m & 1] = (u2){pk2(pp[a][0], pp[a][1]), pk2(pp[a][2], pp[a][3])};
      SBAR();
      if (m & 1) asm volatile("ds_write2st64_b64 %0, %2, %3 offset0:%6 offset1:%7\n ds_write2st64_b64 %1, %4, %5 offset0:%6 offset1:%7"
                              ::"v"(pw0), "v"(pw1), "v"(ph[0][0]), "v"(ph[0][1]), "v"(ph[1][0]), "v"(ph[1][1]), "i"(2 * m - 2), "i"(2 * m) : "memory");
      SBAR();
    }
    asm volatile("ds_write_b32 %8, %9\n s_waitcnt lgkmcnt(0)" : "+v"(xv[0]), "+v"(xv[1]), "+v"(xv[2]), "+v"(xv[3]), "+v"(xv[4]), "+v"(xv[5]), "+v"(xv[6]), "+v"(xv[7]) : "v"(vfl), "v"(c4) : "memory");
  }
  __builtin_amdgcn_s_setprio(0);
  STMP(5);
  {
  int l;
  asm volatile("v_mbcnt_lo_u32_b32 %0, -1, 0\n v_mbcnt_hi_u32_b32 %0, -1, %0" : "=v"(l));
  __builtin_assume(l >= 0 && l < 64);
  const int r15 = l & 15, g4 = l >> 4, q = (l & 15) >> 2, p = l & 3;
  const int trow = mb * 128 + w * 32 + (l & 31);
  u2 oh;
  const unsigned long long ptab = mktab();
  const unsigned flo = PAIRAT(r15) * 128;
  const char* fb = uptr(wsx + WS_OH + mb * 2048);
  unsigned vfw[4];
#pragma unroll
  for (int i = 0; i < 4; ++i) { vfw[i] = VFW(i); asm volatile("" : "+v"(vfw[i])); }
  auto wo_part = [&](int s0, int s1) {
#pragma unroll
    for (int i = s0 / 2; i < s1 / 2; i += 2) {
      const int pp = wr ? wtat(i + 1) : wtat(i);
      const char* wp = (const char*)wo + (nb * 128 - 3072) + (2 * PAIRAT(pp) + wc) * 65536; const unsigned wm = ldsb + (2 * pp + wc) * 4096;
      GL4(vfw, wp, wm, L_WO);
    }
  };
  if (WF) {
  lwait<LW3>(adFL, 0x3Fu, (l & 4) ? 3u : 4u);
  asm volatile("global_load_dwordx2 %0, %1, %2 sc1" : "=&v"(oh) : "v"(flo), "s"(fb) : "memory");
  wo_part(WE, WB);
  SBAR();
  }
  int sel = -1;
  {
    const u4 s0 = *(const LAS u4*)(lds + L_SEL), s1 = *(const LAS u4*)(lds + L_SEL + 16);
#pragma unroll
    for (int i = 0; i < 8; ++i) { const int v = (int)(i < 4 ? s0[i & 3] : s1[i & 3]); sel = v == trow ? i : sel; }
  }
  const bool anysel = __ballot(sel >= 0) != 0ull;
  f16v oc[2][2];
  {
    const float m = *(const LAS float*)(lds + L_M + (w * 32 + (l & 31)) * 4), rs = *(const LAS float*)(lds + L_RS + (w * 32 + (l & 31)) * 4);
#pragma unroll
    for (int t = 0; t < 8; ++t) {
      const f4 xf = (f4){lo(xv[t][0]), hi(xv[t][0]), lo(xv[t][1]), hi(xv[t][1])};
      const f4 y = (xf - m) * rs + xf;
#pragma unroll
      for (int i = 0; i < 4; ++i) oc[0][t >> 2][(t & 3) * 4 + i] = y[i];
    }
    asm volatile("" : "+v"(oc[0][0]), "+v"(oc[0][1]));
  }
  unsigned wa[2];
#pragma unroll
  for (int t = 0; t < 2; ++t) wa[t] = ldsb + L_WO + wc * 4096 + (4 * (g4 >> 1) + q) * 128 + (((4 * t + 2 * (g4 & 1) + (p >> 1)) ^ ((q >> 1) << 2)) * 16) + (p & 1) * 8;
  u4 a[RD][4];
#pragma unroll
  for (int k = 0; k < RD; ++k) asm volatile("" : "=v"(a[k][0]), "=v"(a[k][1]), "=v"(a[k][2]), "=v"(a[k][3]));
  const char* ob = uptr(wsx + WS_OF + mb * 262144 + wc * 8192 + wr * 4096);
  const unsigned ov0 = l * 16 + wc * 2048, ov1 = ov0 ^ 2048;
  STMP(6);
  if (!WF) {
  lwait<2>(adFL, 0x3Fu, (l & 4) ? 3u : 4u);
  asm volatile("global_load_dwordx2 %0, %1, %2 sc1" : "=&v"(oh) : "v"(flo), "s"(fb) : "memory");
  wo_part(WE, WB);
  }
  WAITV(WB - WE);
  asm volatile("" : "+v"(oh));
  unsigned seen = (unsigned)__ballot(oh[0] == tag), go = 0;
  auto need = [&](unsigned mask) {
    if (__builtin_expect((go & mask) != mask, 0)) {
      RETRY2();
      _Pragma("unroll 1") for (unsigned it = 0; it < SPIN; ++it) {
        if ((seen & mask) != mask) {
          asm volatile("global_load_dwordx2 %0, %1, %2 sc1\n s_waitcnt vmcnt(0)" : "+v"(oh) : "v"(flo), "s"(fb) : "memory");
          seen = (unsigned)__ballot(oh[0] == tag);
        }
        go = seen & (unsigned)__ballot(oh[1] - NOW() - 1u >= 1000u);
        if ((go & mask) == mask) break;
        SLEEP(1);
      }
    }
  };
#define OX 0
#define OSC "sc1"
#define OL1(pos, i) do { if (OX == 2 && (pos) >= RD) break; \
    asm volatile("global_load_dwordx4 %0, %1, %2 offset:%3 " OSC : "+v"(a[(pos) % RD][i]) : "v"((i) & 1 ? ov1 : ov0), "s"(ob + (OX == 1 ? PAIRAT((pos) & 3) : OX == 3 ? PAIRAT((pos) < 4 || ((pos) & 3) == 0 ? (pos) : (pos) & 3) : PAIRAT(pos)) * 16384), "i"(((i) >> 1) * 1024) : "memory"); } while (0)
#define OLR(pos) do { _Pragma("unroll") for (int i_ = 0; i_ < 4; ++i_) asm volatile("global_load_dwordx4 %0, %1, %2 sc1" : "+v"(a[(pos) % RD][i_]) : "v"((i_ & 1 ? ov1 : ov0) + (i_ >> 1) * 1024), "s"(ob + PAIRAT(pos) * 16384) : "memory"); } while (0)
#define OLD(pos) do { OL1(pos, 0); OL1(pos, 1); OL1(pos, 2); OL1(pos, 3); } while (0)
  auto omax = [&](u4(&v)[4]) {
    asm volatile("" : "+v"(v[0]), "+v"(v[1]), "+v"(v[2]), "+v"(v[3]));
    return max(umax3(v[0][0], v[1][0], v[2][0]), v[3][0]);
  };
#define OFIX(pos, m) do { \
    if (__builtin_expect(__ballot((m) == POIS) != 0ull, 0)) { RETRY(); _Pragma("unroll 1") for (unsigned it = 0; it < SPIN; ++it) { SLEEP(4); OLR(pos); WAITV(0); if (__ballot(omax(a[(pos) % RD]) == POIS) == 0ull) break; } } } while (0)
#define OS1(i)
#define OS2(i)
  u2 oh2 = oh;
  s4 b[8];
#define ORD(pos, f) do { if ((f) < 2 && (pos) == 8) { wa[(f) & 1] += 65536; asm volatile("" : "+v"(wa[(f) & 1])); } \
    RDTR(b[2 * (f)], wa[(f) & 1], ((pos) & 7) * 8192 + ((f) >> 1) * 1024); RDTR(b[2 * (f) + 1], wa[(f) & 1], ((pos) & 7) * 8192 + ((f) >> 1) * 1024 + 2048); } while (0)
  const unsigned lo0 = ldsb + L_PQ + w * 4096 + ov0, lo1 = lo0 ^ 2048;
#pragma unroll
  for (int i = 1; i < RD; ++i) {
    if (i == E0 + 1) {
      lwait<LW4>(adFL, 0x30u, 4);
      if (F5) asm volatile("s_waitcnt vmcnt(0)\n ds_write_b32 %0, %1" ::"v"(myFL), "v"(5u) : "memory");
    }
    if (i == E0 + 2) {
      RD128(a[0][0], lo0, 0); RD128(a[0][1], lo1, 0); RD128(a[0][2], lo0, 1024); RD128(a[0][3], lo1, 1024);
      ORD(0, 0); ORD(0, 1); ORD(0, 2); ORD(0, 3);
    }
    if (i == WP) wo_part(WB, 32);
    if (i == RD - 1) break;
    if (i == 1) {
      __builtin_amdgcn_s_setprio(2);
      if (E0 && ES) SLEEP(ES);
    }
    if (i == 1 || i == E0 + 1) go = seen & (unsigned)__ballot(oh[1] - NOW() - 1u >= 1000u);
    need(1u << i);
    if (i == 1) {
      STMP(15);
      oh2 = oh;
      if (__builtin_expect(seen != ~0u, 0)) asm volatile("global_load_dwordx2 %0, %1, %2 sc1" : "+v"(oh2) : "v"(flo), "s"(fb) : "memory");
    }
    OS1(i); OLD(i);
  }
#define WXT(pos) ((pos) < WP ? 32 - WB : 0)
  STMP(10);
#define OMF(pos, f, r) do { if ((f) < 2 && (pos) == 0 && (r) == 1) asm volatile("v_mfma_f32_32x32x16_bf16 %0, %1, %2, 0" : "=&v"(oc[r][(f) & 1]) \
      : "v"(CAT(b[2 * (f)], b[2 * (f) + 1])), "v"(a[(pos) % RD][((f) & 2) + (r)])); \
    else asm volatile("v_mfma_f32_32x32x16_bf16 %0, %1, %2, %0" : "+v"(oc[r][(f) & 1]) \
      : "v"(CAT(b[2 * (f)], b[2 * (f) + 1])), "v"(a[(pos) % RD][((f) & 2) + (r)])); \
    SBAR(); if ((r) == 1 && (pos) < 15) ORD(((pos) + 1) & 15, f); SBAR(); } while (0)
#define ONX(pos) ((pos) + RD - 1 < 16)
#define ONL(pos, i) do { if (ONX(pos)) OL1(((pos) + RD - 1) & 15, i); SBAR(); } while (0)
#define OWL(pos, f) do { WAITL((pos) < 15 ? 6 : 6 - 2 * (f)); SBAR(); } while (0)
#define OHEAD(pos) do { unsigned m_ = 0; \
    if ((pos) == wtmin(WE / 2, WB / 2) - 1) { asm volatile("" : "+v"(fl3)); if (__builtin_expect(((unsigned)__ballot(fl3 < 5u) >> (w ^ 2)) & 1u, 0)) lwait(adFL, 1u << (w ^ 2), 5); SBAR(); } \
    if ((pos) == wtmin(WB / 2, 16) - 1) { asm volatile("" : "+v"(fl3)); if (__builtin_expect(((unsigned)__ballot(fl3 < 6u) >> (w ^ 2)) & 1u, 0)) lwait(adFL, 1u << (w ^ 2), 6); SBAR(); } \
    OWL(pos, 0); OMF(pos, 0, 0); if (ONX(pos)) { need(1u << (((pos) + RD - 1) & 15)); OS1(((pos) + RD - 1) & 15); } SBAR(); \
    OMF(pos, 0, 1); ONL(pos, 0); \
    OWL(pos, 1); OMF(pos, 1, 0); ONL(pos, 1); \
    OMF(pos, 1, 1); ONL(pos, 2); \
    OWL(pos, 2); OMF(pos, 2, 0); ONL(pos, 3); \
    if (((pos) == 1 && !F5) || (pos) == 4) { asm volatile("ds_write_b32 %0, %1" ::"v"(myFL), "v"((pos) > 1 ? 6u : 5u) : "memory"); SBAR(); } \
    if ((pos) == wtmin(WE / 2, WB / 2) - 3 || (pos) == wtmin(WB / 2, 16) - 3) { asm volatile("ds_read_b32 %0, %1" : "=v"(fl3) : "v"(adFL) : "memory"); SBAR(); } \
    if ((pos) == 13) { asm volatile("ds_read_b32 %0, %1" : "=v"(fl3) : "v"(adFL) : "memory"); SBAR(); } \
    OMF(pos, 2, 1); \
    OWL(pos, 3); OMF(pos, 3, 0); \
    if ((pos) < 15) { WAITV(4 * ((pos) + RD - 1 < 16 ? RD - 2 : 14 - (pos)) + WXT((pos) + 1)); m_ = omax(a[((pos) + 1) % RD]); asm volatile("" : "+v"(m_)); SBAR(); } \
    OMF(pos, 3, 1); \
    if ((pos) < 15) { OFIX(((pos) + 1) & 15, m_); OS2(((pos) + 1) & 15); } SBAR(); } while (0)
  static_assert(F5 && wtmin(WE / 2, WB / 2) >= 4 && wtmin(WB / 2, 16) >= wtmin(WE / 2, WB / 2) + 2 && wtmin(WB / 2, 16) >= 8, "");
  unsigned fl3;
  OHEAD(0);
  {
    asm volatile("" : "+v"(oh2));
    if (oh2[0] == tag) oh = oh2;
    seen = (unsigned)__ballot(oh[0] == tag);
    go = seen & (unsigned)__ballot(oh[1] - NOW() - max(r15 - RD, 0) * ADV - 1u >= 1000u);
    SBAR(); }
  OHEAD(1); STMP(11); OHEAD(2); OHEAD(3); OHEAD(4); OHEAD(5); OHEAD(6); OHEAD(7); STMP(12);
  OHEAD(8); OHEAD(9); STMP(13); OHEAD(10); OHEAD(11); OHEAD(12); OHEAD(13); OHEAD(14);
  f4 xi[8];
  {
    asm volatile("" : "+v"(fl3));
    if (__builtin_expect(((unsigned)__ballot(fl3 < 6u) >> (w ^ 2)) & 1u, 0)) lwait(adFL, 1u << (w ^ 2), 6);
    if (__builtin_expect(((unsigned)__ballot(fl3 < 4u) & 0x30u) != 0u, 0)) lwait(adFL, 0x30u, 4);
    const unsigned xw = ldsb + L_WO + (4 * wr + wc) * 4096 + l * 16, xr = xw ^ 4096;
#define XW(t) asm volatile("ds_write_b128 %0, %1 offset:%2" ::"v"(xw), "v"(__builtin_shufflevector(oc[1][(t) >> 2], oc[1][(t) >> 2], ((t) & 3) * 4, ((t) & 3) * 4 + 1, ((t) & 3) * 4 + 2, ((t) & 3) * 4 + 3)), \
      "i"(((t) >> 2) * 8192 + ((t) & 3) * 1024) : "memory")
    SBAR();
    OWL(15, 0); OMF(15, 0, 1); OWL(15, 1); OMF(15, 1, 1); OWL(15, 2); OMF(15, 2, 1); OWL(15, 3); OMF(15, 3, 1);
    OMF(15, 0, 0); OMF(15, 1, 0); XW(0); XW(1); XW(2); XW(3); SBAR();
    OMF(15, 2, 0); XW(4); XW(5); XW(6); XW(7); SBAR();
    OMF(15, 3, 0);
    asm volatile("ds_write_b32 %0, %1" ::"v"(myFL), "v"(7u) : "memory");
    _Pragma("unroll 1") for (unsigned it = 0; it < SPIN; ++it) {
      unsigned v;
      asm volatile("ds_read_b32 %0, %1" : "=v"(v) : "v"(adFL) : "memory");
#pragma unroll
      for (int t = 0; t < 8; ++t) RD128(xi[t], xr, (t >> 2) * 8192 + (t & 3) * 1024);
      WAITL(0);
      asm volatile("" : "+v"(v), "+v"(xi[0]), "+v"(xi[1]), "+v"(xi[2]), "+v"(xi[3]), "+v"(xi[4]), "+v"(xi[5]), "+v"(xi[6]), "+v"(xi[7]));
      if ((((unsigned)__ballot(v < 7u) >> (w ^ 1)) & 1u) == 0u) break;
    }
    asm volatile("" : "+v"(oc[0][0]), "+v"(oc[0][1]));
#pragma unroll
    for (int t = 0; t < 8; ++t)
#pragma unroll
      for (int i = 0; i < 4; ++i) xi[t][i] += oc[0][t >> 2][(t & 3) * 4 + i];
  }
  STMP(8);
  if (anysel) {
    lwait(adFL, 0xC0u, 3);
    if (sel >= 0) {
#pragma unroll
      for (int t = 0; t < 8; ++t) xi[t] += *(const LAS f4*)(lds + L_KVX + sel * 256 + (8 * t + 4 * (l >> 5)) * 4);
    }
  }
  const __amdgpu_buffer_rsrc_t outr = __builtin_amdgcn_make_buffer_rsrc(out, 0, 2048 * D * 2, 0x00020000);
  if (FULL) {
    LAS char* tb = (LAS char*)lds + L_PQ + w * 4096;
#pragma unroll
    for (int t = 0; t < 8; ++t) *(LAS u2*)(tb + (l & 31) * 128 + ((t ^ (l & 7)) * 16) + (l >> 5) * 8) = (u2){pk2(xi[t][0], xi[t][1]), pk2(xi[t][2], xi[t][3])};
#pragma unroll
    for (int i = 0; i < 4; ++i) {
      const u4 v = *(const LAS u4*)(tb + i * 1024 + (l >> 3) * 128 + (((l ^ (l >> 3)) & 7) * 16));
      __builtin_amdgcn_raw_buffer_store_b128(v, outr, (mb * 128 + w * 32 + i * 8 + (l >> 3)) * 2048 + nb * 128 + (l & 7) * 16, 0, 16);
    }
  } else {
#pragma unroll
  for (int hh = 0; hh < 4; ++hh) {
    const f4 ta = xi[2 * hh], tb = xi[2 * hh + 1];
    auto r0 = __builtin_amdgcn_permlane32_swap(pk2(ta[0], ta[1]), pk2(tb[0], tb[1]), false, false);
    auto r1 = __builtin_amdgcn_permlane32_swap(pk2(ta[2], ta[3]), pk2(tb[2], tb[3]), false, false);
    __builtin_amdgcn_raw_buffer_store_b128(((u4){r0[0], r1[0], r0[1], r1[1]}), outr, (trow * D + nb * 64 + 16 * hh + 8 * (l >> 5)) * 2, 0, 16);
  }
  }
  if (g == 0 && w == 0 && l == 0) __builtin_amdgcn_raw_buffer_store_b32(tag, wse, WS_E, 0, 16);
  STMP(9);
  STFLUSH();
  asm volatile(".Lend_c:");
  }
  return;
  }
  {
#define LT2(i) BST(i)
    unsigned vb[4], vfw[4];
    STMP(6); SBAR();
#pragma unroll
    for (int i = 0; i < 4; ++i) vb[i] = (8 * i + (l >> 3)) * 6144 + (((l & 7) ^ ((((l >> 4) & 1) << 2) | ((i & 1) << 1))) * 16) + (3 - i) * 1024;
    const unsigned lb0 = lw * 196608, lb1 = 196608 - lw * 194560, mbb = ldsb + 16384 + lw * 12288;
    const char* Ab = kx + ao;
    const char* Bb = kq + (hp * 128 + lw * 2048 - 3072);
    auto issue = [&](int s, int u0, int u1) {
      const int kt = (s + rot) & 15;
      const char* sa = Ab + kt * 128; const char* sb = Bb + kt * (64 * 6144);
      const unsigned so = (s & 3) * STG, ma = mba + so, mq = mbb + so;
#pragma unroll
      for (int u = u0; u < u1; ++u) { if (u < 2) GL4(va, sa + u * 65536, ma, u * 4096); else GL4(vb, sb + (u == 2 ? lb0 : u == 3 ? lb1 : lb0 + 2048), mq, (u - 2) * 4096); }
    };
    int tk = 0;
#define LST(i) BST(i)
#define LBAR() do { LST(tk + 1); BAR(); LST(tk + 21); ++tk; } while (0)
    issue(0, 2, 3); issue(0, 1, 2); issue(0, 3, 5); SBAR(); STMP(8); SBAR(); issue(1, 0, 5); issue(2, 0, U0);
    LT2(60);
    SBAR();
    GETTAG(tag);
    MKWS(tag);
    const unsigned long long ptab = mktab();
#pragma unroll
    for (int i = 0; i < 4; ++i) vfw[i] = VFW(i);
    const int hd = hp * 2 + lw;
    u4 sel;
#pragma unroll
    for (int e = 0; e < 2; ++e) {
      unsigned v = (r15 >> 2) == g4 ? (((r15 & 3) == 2 * e ? 0x3F80u : 0u) | ((r15 & 3) == 2 * e + 1 ? 0x3F800000u : 0u)) : 0u;
      sel[e] = v; sel[2 + e] = v;
    }
    asm volatile("" : "+v"(sel));
    const char* kvb = uptr(wsx + WS_KVP + 4096 + (size_t)hd * 32768);
    const char* zb = uptr(wsx + WS_ZP + (size_t)hd * 2048);
    unsigned zoff = q * 128 + g4 * 32 + p * 8, oho = l == 0 ? WS_OH + (mb * 16 + hp) * 128 : WS_SZ, fla = ldsb + L_FL, v0 = 0u, v1 = 1u, afl = adFL, mfl = myFL;
    unsigned hko = l == 0 ? WS_HK + hp * 512 + (mb >> 2) * 128 + (mb & 3) * 4 : WS_SZ, pzo = WS_OF + (mb * 32 + hp * 2 + (PE == 2 ? lw : 0)) * 8192 + l * 16;
    u4 pz = (u4)(POIS);
    asm volatile("" : "+v"(zoff), "+v"(oho), "+v"(fla), "+s"(kvb), "+s"(zb), "+v"(v0), "+v"(v1), "+v"(afl), "+v"(mfl), "+v"(hko), "+v"(pzo), "+v"(pz));
    unsigned lv16 = l * 16, pa = ldsb + L_PQ + lw * 4096 + l * 16, thr = (l & 4) ? 3u : 4u;
    unsigned ofo = WS_OF + (mb * 32 + hd) * 8192 + g4 * 512 + r15 * 16, lwa = ldsb + L_PQ + lw * 4096 + g4 * 512 + r15 * 16;
    asm volatile("" : "+v"(vfw[0]), "+v"(vfw[1]), "+v"(vfw[2]), "+v"(vfw[3]), "+v"(lv16), "+v"(pa), "+v"(thr), "+v"(ofo), "+v"(lwa));
    PFB(sp ? 64 - SPS : 64 - SPW, sp ? 2 * SPS : 2 * SPW);
    SBAR();
    WAITV(20 + 4 * U0); LT2(0); BAR(); LT2(20);
    issue(2, U0, 5);
    unsigned ck0 = (unsigned)__builtin_amdgcn_s_memtime(), rt0 = NOW();
    asm volatile("" : "+s"(ck0), "+s"(rt0));
#define LPF() spf(pfb)
    int s0 = 3;
    if (LPL) __builtin_amdgcn_s_setprio(LPL);
    if (U3 < 5) { issue(3, 0, U3); LST(tk + 40); WAITV(20 + 4 * U3); LBAR(); issue(3, U3, 5); s0 = 4; }
#pragma unroll 1
    for (; s0 < (sp ? (LF ? SL5 + 2 : 16) : 6 + ph); ++s0) { issue(s0, 0, 5); LST(tk + 40); if (sp && s0 < 14) LPF(); WAITV(40); LBAR(); }
    if (!sp) {
#pragma unroll 1
    for (; s0 < 16; s0 += 5) {
      LPF(); WAITV(20); LBAR();
      LPF(); WAITV(0); LBAR();
      issue(s0, 0, 5); issue(s0 + 1, 0, U1); LST(tk + 40); WAITV(4 * U1); LBAR();
      issue(s0 + 1, U1, 5); issue(s0 + 2, 0, 5);
      if (LF && s0 > 10) { s0 += 3; break; }
      if (s0 + 3 >= 16) break;
      issue(s0 + 3, 0, U2); LST(tk + 40); WAITV(20 + 4 * U2); LBAR();
      issue(s0 + 3, U2, 5);
      if (s0 + 4 >= 16) break;
      issue(s0 + 4, 0, 5); LST(tk + 40); WAITV(40); LBAR();
    }
    }
    if (LF) {
#pragma unroll 1
      for (; s0 < 16; ++s0) { LST(tk + 40); WAITV(20); LBAR(); issue(s0, 0, 5); }
    }
    if (LPL) __builtin_amdgcn_s_setprio(0);
    int wq = 0;
    auto wo_upto = [&](int lim) {
      _Pragma("unroll 1") for (; wq < lim; ++wq) {
        const int sl = 2 * ((int)(WT >> (4 * (wq >> 1))) & 15) + (wq & 1);
        const char* wp = (const char*)wo + (nb * 128 - 3072) + HEADAT(sl) * 65536; const unsigned wm = ldsb + sl * 4096; GL4(vfw, wp, wm, L_WO); }
    };
    if (!ZH) asm volatile("ds_write_b32 %0, %1" ::"v"(afl), "v"(v0) : "memory");
    if (PE == 1 && lw) {
#pragma unroll
      for (int i = 0; i < 16; ++i) __builtin_amdgcn_raw_buffer_store_b128(pz, wsr, pzo + (i & 3) * 1024, (i >> 2) * 4096, 16);
    }
    if (PE == 2) {
#pragma unroll
      for (int i = 0; i < 8; ++i) __builtin_amdgcn_raw_buffer_store_b128(pz, wsr, pzo + (i & 3) * 1024, (i >> 2) * 4096, 16);
    }
    SBAR();
    unsigned kd = sp ? KS : KD, od = OD;
    if (CF && lw) {
      const unsigned dc = (unsigned)__builtin_amdgcn_s_memtime() - ck0, dr = NOW() - rt0;
      const float sc = fminf((float)dr * (float)CF * __builtin_amdgcn_rcpf((float)dc), 2.f);
      kd = KL + (unsigned)((float)(kd - KL) * sc); od = OL + (unsigned)((float)(OD - OL) * sc);
      asm volatile("" : "+v"(kd), "+v"(od));
    }
    SBAR();
    if (PE == 1 && lw) WAITV(16); else WAITV(PE == 2 ? 8 : 0);
    WAITL(0); LBAR();
    SBAR();
    u4 dg;
    unsigned now;
    if (lw) {
      const unsigned tb = NOW();
      __builtin_amdgcn_raw_buffer_store_b32(tb + kd, wsr, hko, 0, 16);
#pragma unroll
      for (int i = 0; i < (PE ? 0 : 16); ++i) __builtin_amdgcn_raw_buffer_store_b128(pz, wsr, pzo + (i & 3) * 1024, (i >> 2) * 4096, 16);
      LT2(61);
      const char* hb = uptr(wsx + WS_HK + hp * 512);
      __builtin_amdgcn_s_setprio(LP1);
      lwait<4>(adFL, PE == 2 ? 0xD0u : 0xC0u, 1u);
      while (NOW() - tb < HT) SLEEP(1);
      STMP(1);
      unsigned t0, rq[4], m0, m1, nt;
#define HG4(d, a, b, c, e) "s_sub_u32 s" #a ", s" #a ", %0\n s_sub_u32 s" #b ", s" #b ", %0\n s_sub_u32 s" #c ", s" #c ", %0\n s_sub_u32 s" #e ", s" #e ", %0\n" \
      "s_max_u32 s" #a ", s" #a ", s" #b "\n s_max_u32 s" #c ", s" #c ", s" #e "\n s_max_u32 %" #d ", s" #a ", s" #c "\n"
      asm volatile("s_mov_b32 %7, 15\n.Lhint:\n s_load_dwordx4 s[72:75], %8, 0 glc\n s_load_dwordx4 s[76:79], %8, 128 glc\n s_load_dwordx4 s[80:83], %8, 256 glc\n"
                   "s_load_dwordx4 s[84:87], %8, 384 glc\n s_memrealtime s[88:89]\n s_waitcnt lgkmcnt(0)\n s_sub_u32 %0, s88, 0x800\n"
                   HG4(1, 72, 73, 74, 75) HG4(2, 76, 77, 78, 79) HG4(3, 80, 81, 82, 83) HG4(4, 84, 85, 86, 87)
                   "s_max_u32 %5, %1, %2\n s_max_u32 %6, %3, %4\n s_max_u32 s72, %5, %6\n s_cmpk_lt_u32 s72, 0xc00\n s_cbranch_scc1 .Lhdone\n"
                   "s_sleep %9\n s_sub_u32 %7, %7, 1\n s_cbranch_scc0 .Lhint\n"
                   "s_mov_b32 %1, 0\n s_mov_b32 %2, 0\n s_mov_b32 %3, 0\n s_mov_b32 %4, 0\n s_mov_b32 %5, 0\n s_mov_b32 %6, 0\n.Lhdone:"
                   : "=&s"(t0), "=&s"(rq[0]), "=&s"(rq[1]), "=&s"(rq[2]), "=&s"(rq[3]), "=&s"(m0), "=&s"(m1), "=&s"(nt) : "s"(hb), "i"(HKS)
                   : "memory", "scc", "s72", "s73", "s74", "s75", "s76", "s77", "s78", "s79", "s80", "s81", "s82", "s83", "s84", "s85", "s86", "s87", "s88", "s89");
      LT2(16);
      const bool up = m0 > m1;
      const unsigned a0 = up ? rq[2] : rq[0], a1 = up ? rq[3] : rq[1], a2 = up ? rq[0] : rq[2], a3 = up ? rq[1] : rq[3], gu = up ? 2u : 0u;
      unsigned e0, e1, go;
      asm("s_cmp_gt_u32 %3, %4\n s_cselect_b32 %0, %6, %5\n s_cselect_b32 %1, %5, %6\n s_addc_u32 %2, %7, 0" : "=&s"(e0), "=&s"(e1), "=&s"(go) : "s"(a2), "s"(a3), "s"(a0), "s"(a1), "s"(gu) : "scc");
      dg = (u4){((t0 + 3u + e0) & ~3u) | go, t0 + e1, t0 + min(a2, a3), t0 + max(m0, m1)};
      asm volatile("ds_write_b128 %0, %1 offset:32\n ds_write_b32 %0, %2 offset:20" ::"v"(fla), "v"(dg), "v"(2u) : "memory");
      LT2(15);
      now = NOW();
      WAITV(0);
      unsigned de = dg[3] - t0;
      if (GI) { de = max(max(de, dg[2] - t0 + GI), max(dg[1] - t0 + 2 * GI, max(dg[0] - t0, tb + (unsigned)__builtin_amdgcn_readfirstlane(kd) + GO - t0) + 3 * GI)); }
      de += t0;
      ST64(((u2){tag, ((int)(de - now) > 0 ? de : now) + od}), oho);
    } else {
      wo_upto(WA);
      if (PE == 2) asm volatile("s_waitcnt vmcnt(%2)\n ds_write_b32 %0, %1" ::"v"(mfl), "v"(v1), "i"(4 * WA) : "memory");
      LT2(15);
      if (WA < WE || GW) { lwait<2>(adFL, 0x0Fu, 3); if (WS0) SLEEP(WS0); wo_upto(WE); }
      STMP(1);
      __builtin_amdgcn_s_setprio(LP0);
      u4 v;
      _Pragma("unroll 1") for (unsigned it = 0; it < SPIN; ++it) {
        unsigned f;
        asm volatile("ds_read_b32 %0, %2 offset:20\n ds_read_b128 %1, %2 offset:32\n s_waitcnt lgkmcnt(0)" : "=&v"(f), "=&v"(v) : "v"(fla) : "memory");
        if (__builtin_amdgcn_readfirstlane(f) >= 2u) break;
        SLEEP(2);
      }
#pragma unroll
      for (int e = 0; e < 4; ++e) dg[e] = __builtin_amdgcn_readfirstlane(v[e]);
      now = NOW();
    }
    unsigned gx = (dg[0] & 3u) * 8192u;
    asm volatile("" : "+s"(gx));
    __builtin_amdgcn_s_setprio(3);
    STMP(2);
    unsigned nr;
#define OST_(i)
#define OSTE :
    asm volatile(
      "s_lshr_b32 s72, %[gx], 4\n v_xor_b32 v252, %[gx], %[lv]\n v_xor_b32 v253, s72, %[zo]\n s_mov_b32 s74, %[now]\n s_sub_i32 s72, s74, %[d0]\n s_cmp_gt_i32 s72, -1\n "
      "s_cbranch_scc1 .Lgo0\n .Lgw0:\n s_memrealtime s[74:75]\n s_waitcnt lgkmcnt(0)\n s_sub_i32 s72, s74, %[d0]\n s_cmp_lt_i32 s72, 0\n s_cbranch_scc1 .Lgw0\n .Lgo0:\n "
      "global_load_dwordx2 v[240:241], v253, %[zb] sc1\n global_load_dwordx4 v[80:83], v252, %[kb] offset:-4096 sc1\n global_load_dwordx4 v[84:87], v252, %[kb] offset:-2048 sc1\n "
      "global_load_dwordx4 v[90:93], v252, %[kb] offset:-3072 sc1\n global_load_dwordx4 v[94:97], v252, %[kb] offset:-1024 sc1\n global_load_dwordx4 v[100:103], v252, %[kb] offset:0 sc1\n "
      "global_load_dwordx4 v[104:107], v252, %[kb] offset:2048 sc1\n global_load_dwordx4 v[110:113], v252, %[kb] offset:1024 sc1\n "
      "global_load_dwordx4 v[114:117], v252, %[kb] offset:3072 sc1\n v_xor_b32 v252, 0x2000, v252\n v_xor_b32 v253, 0x200, v253\n s_sub_i32 s72, s74, %[d1]\n s_cmp_gt_i32 s72, -1\n "
      "s_cbranch_scc1 .Lgo1\n .Lgw1:\n s_memrealtime s[74:75]\n s_waitcnt lgkmcnt(0)\n s_sub_i32 s72, s74, %[d1]\n s_cmp_lt_i32 s72, 0\n s_cbranch_scc1 .Lgw1\n .Lgo1:\n "
      "global_load_dwordx2 v[242:243], v253, %[zb] sc1\n global_load_dwordx4 v[120:123], v252, %[kb] offset:-4096 sc1\n global_load_dwordx4 v[124:127], v252, %[kb] offset:-2048 sc1\n "
      "global_load_dwordx4 v[130:133], v252, %[kb] offset:-3072 sc1\n global_load_dwordx4 v[134:137], v252, %[kb] offset:-1024 sc1\n "
      "global_load_dwordx4 v[140:143], v252, %[kb] offset:0 sc1\n global_load_dwordx4 v[144:147], v252, %[kb] offset:2048 sc1\n global_load_dwordx4 v[150:153], v252, %[kb] offset:1024 sc1\n "
      "global_load_dwordx4 v[154:157], v252, %[kb] offset:3072 sc1\n v_xor_b32 v252, 0x6000, v252\n v_xor_b32 v253, 0x600, v253\n s_sub_i32 s72, s74, %[d2]\n s_cmp_gt_i32 s72, -1\n "
      "s_cbranch_scc1 .Lgo2\n .Lgw2:\n s_memrealtime s[74:75]\n s_waitcnt lgkmcnt(0)\n s_sub_i32 s72, s74, %[d2]\n s_cmp_lt_i32 s72, 0\n s_cbranch_scc1 .Lgw2\n .Lgo2:\n "
      "global_load_dwordx2 v[244:245], v253, %[zb] sc1\n global_load_dwordx4 v[160:163], v252, %[kb] offset:-4096 sc1\n global_load_dwordx4 v[164:167], v252, %[kb] offset:-2048 sc1\n "
      "global_load_dwordx4 v[170:173], v252, %[kb] offset:-3072 sc1\n global_load_dwordx4 v[174:177], v252, %[kb] offset:-1024 sc1\n "
      "global_load_dwordx4 v[180:183], v252, %[kb] offset:0 sc1\n global_load_dwordx4 v[184:187], v252, %[kb] offset:2048 sc1\n global_load_dwordx4 v[190:193], v252, %[kb] offset:1024 sc1\n "
      "global_load_dwordx4 v[194:197], v252, %[kb] offset:3072 sc1\n v_xor_b32 v252, 0x2000, v252\n v_xor_b32 v253, 0x200, v253\n v_mov_b32 v248, 3\n s_sub_i32 s72, s74, %[d3]\n "
      "s_cmp_gt_i32 s72, -1\n s_cbranch_scc1 .Lgo3\n .Lgw3:\n s_memrealtime s[74:75]\n s_waitcnt lgkmcnt(0)\n s_sub_i32 s72, s74, %[d3]\n s_cmp_lt_i32 s72, 0\n s_cbranch_scc1 .Lgw3\n "
      ".Lgo3:\n "
      OST_(0)
      "global_load_dwordx2 v[246:247], v253, %[zb] sc1\n global_load_dwordx4 v[200:203], v252, %[kb] offset:-4096 sc1\n global_load_dwordx4 v[204:207], v252, %[kb] offset:-2048 sc1\n "
      "global_load_dwordx4 v[210:213], v252, %[kb] offset:-3072 sc1\n global_load_dwordx4 v[214:217], v252, %[kb] offset:-1024 sc1\n "
      "global_load_dwordx4 v[220:223], v252, %[kb] offset:0 sc1\n global_load_dwordx4 v[224:227], v252, %[kb] offset:2048 sc1\n global_load_dwordx4 v[230:233], v252, %[kb] offset:1024 sc1\n "
      "global_load_dwordx4 v[234:237], v252, %[kb] offset:3072 sc1\n ds_write_b32 %[mfl], v248\n "
      OST_(1)
      "s_mov_b32 %[nr], 0\n s_movk_i32 s73, 0x1000\n s_waitcnt vmcnt(27)\n .Lck0:\n v_max3_u32 v254, v240, v80, v84\n v_max3_u32 v254, v254, v90, v94\n v_max3_u32 v254, v254, v100, v104\n "
      "v_max3_u32 v254, v254, v110, v114\n v_cmp_eq_u32 vcc, -1, v254\n s_cbranch_vccnz .Lrt0\n .Lok0:\n v_mov_b64 v[88:89], v[82:83]\n v_mov_b64 v[82:83], v[84:85]\n "
      "v_mov_b64 v[98:99], v[92:93]\n v_mov_b64 v[92:93], v[94:95]\n v_mov_b64 v[108:109], v[102:103]\n v_mov_b64 v[102:103], v[104:105]\n v_mov_b64 v[118:119], v[112:113]\n "
      "v_mov_b64 v[112:113], v[114:115]\n v_mfma_f32_16x16x32_bf16 v[68:71], %[sel], v[86:89], 0\n v_mfma_f32_16x16x32_bf16 v[64:67], %[sel], v[80:83], 0\n "
      "v_mfma_f32_16x16x32_bf16 v[72:75], %[sel], v[96:99], 0\n v_mfma_f32_16x16x32_bf16 v[76:79], %[sel], v[90:93], 0\n v_mfma_f32_16x16x32_bf16 v[68:71], %[sel], v[106:109], v[68:71]\n "
      "v_mfma_f32_16x16x32_bf16 v[64:67], %[sel], v[100:103], v[64:67]\n v_mfma_f32_16x16x32_bf16 v[72:75], %[sel], v[116:119], v[72:75]\n "
      "v_mfma_f32_16x16x32_bf16 v[76:79], %[sel], v[110:113], v[76:79]\n s_waitcnt vmcnt(18)\n .Lck1:\n v_max3_u32 v254, v242, v120, v124\n v_max3_u32 v254, v254, v130, v134\n "
      "v_max3_u32 v254, v254, v140, v144\n v_max3_u32 v254, v254, v150, v154\n v_cmp_eq_u32 vcc, -1, v254\n s_cbranch_vccnz .Lrt1\n .Lok1:\n v_mov_b64 v[128:129], v[122:123]\n "
      "v_mov_b64 v[122:123], v[124:125]\n v_mov_b64 v[138:139], v[132:133]\n v_mov_b64 v[132:133], v[134:135]\n v_mov_b64 v[148:149], v[142:143]\n v_mov_b64 v[142:143], v[144:145]\n "
      "v_mov_b64 v[158:159], v[152:153]\n v_mov_b64 v[152:153], v[154:155]\n v_mfma_f32_16x16x32_bf16 v[84:87], %[sel], v[126:129], 0\n "
      "v_mfma_f32_16x16x32_bf16 v[80:83], %[sel], v[120:123], 0\n v_mfma_f32_16x16x32_bf16 v[88:91], %[sel], v[136:139], 0\n v_mfma_f32_16x16x32_bf16 v[92:95], %[sel], v[130:133], 0\n "
      "v_mfma_f32_16x16x32_bf16 v[84:87], %[sel], v[146:149], v[84:87]\n v_mfma_f32_16x16x32_bf16 v[80:83], %[sel], v[140:143], v[80:83]\n "
      "v_mfma_f32_16x16x32_bf16 v[88:91], %[sel], v[156:159], v[88:91]\n v_mfma_f32_16x16x32_bf16 v[92:95], %[sel], v[150:153], v[92:95]\n s_waitcnt vmcnt(9)\n .Lck2:\n "
      "v_max3_u32 v254, v244, v160, v164\n v_max3_u32 v254, v254, v170, v174\n v_max3_u32 v254, v254, v180, v184\n v_max3_u32 v254, v254, v190, v194\n v_cmp_eq_u32 vcc, -1, v254\n "
      "s_cbranch_vccnz .Lrt2\n .Lok2:\n v_mov_b64 v[168:169], v[162:163]\n v_mov_b64 v[162:163], v[164:165]\n v_mov_b64 v[178:179], v[172:173]\n v_mov_b64 v[172:173], v[174:175]\n "
      "v_mov_b64 v[188:189], v[182:183]\n v_mov_b64 v[182:183], v[184:185]\n v_mov_b64 v[198:199], v[192:193]\n v_mov_b64 v[192:193], v[194:195]\n "
      "v_pk_add_f32 v[64:65], v[64:65], v[80:81]\n v_pk_add_f32 v[66:67], v[66:67], v[82:83]\n v_pk_add_f32 v[68:69], v[68:69], v[84:85]\n v_pk_add_f32 v[70:71], v[70:71], v[86:87]\n "
      "v_pk_add_f32 v[72:73], v[72:73], v[88:89]\n v_pk_add_f32 v[74:75], v[74:75], v[90:91]\n v_pk_add_f32 v[76:77], v[76:77], v[92:93]\n v_pk_add_f32 v[78:79], v[78:79], v[94:95]\n "
      "v_pk_add_f32 v[250:251], v[240:241], v[242:243]\n v_mfma_f32_16x16x32_bf16 v[100:103], %[sel], v[166:169], 0\n v_mfma_f32_16x16x32_bf16 v[96:99], %[sel], v[160:163], 0\n "
      "v_mfma_f32_16x16x32_bf16 v[104:107], %[sel], v[176:179], 0\n v_mfma_f32_16x16x32_bf16 v[108:111], %[sel], v[170:173], 0\n "
      "v_mfma_f32_16x16x32_bf16 v[100:103], %[sel], v[186:189], v[100:103]\n v_mfma_f32_16x16x32_bf16 v[96:99], %[sel], v[180:183], v[96:99]\n "
      "v_mfma_f32_16x16x32_bf16 v[104:107], %[sel], v[196:199], v[104:107]\n v_mfma_f32_16x16x32_bf16 v[108:111], %[sel], v[190:193], v[108:111]\n v_mov_b32 v156, 0x358637bd\n "
      "v_mov_b32 v157, 0x358637bd\n v_mov_b32 v158, 0x358637bd\n v_mov_b32 v159, 0x358637bd\n s_mov_b32 s72, 0x200000\n .Lpq:\n ds_read_b32 v248, %[afl]\n s_waitcnt lgkmcnt(0)\n "
      "v_cmp_lt_u32 vcc, v248, %[thr]\n s_and_b32 s76, vcc_lo, %[om]\n s_cbranch_scc0 .Lpd\n s_sub_u32 s72, s72, 1\n s_cbranch_scc0 .Lpq\n .Lpd:\n ds_read_b128 v[120:123], %[pa] offset:0\n "
      "ds_read_b128 v[124:127], %[pa] offset:1024\n ds_read_b128 v[128:131], %[pa] offset:2048\n ds_read_b128 v[132:135], %[pa] offset:3072\n ds_read_b128 v[136:139], %[pa] offset:8192\n "
      "ds_read_b128 v[140:143], %[pa] offset:9216\n ds_read_b128 v[144:147], %[pa] offset:10240\n ds_read_b128 v[148:151], %[pa] offset:11264\n .p2align 3\n .Lg3:\n s_waitcnt vmcnt(8)\n "
      "v_add_f32 v248, v244, v246\n v_add_f32 v249, v245, v247\n v_add_f32 v248, v250, v248\n v_add_f32 v249, v251, v249\n s_nop 0\n "
      "v_add_f32_dpp v248, v248, v248 row_ror:8 row_mask:0xf bank_mask:0xf\n v_add_f32_dpp v249, v249, v249 row_ror:8 row_mask:0xf bank_mask:0xf\n s_nop 0\n "
      "v_add_f32_dpp v248, v248, v248 row_ror:4 row_mask:0xf bank_mask:0xf\n v_add_f32_dpp v249, v249, v249 row_ror:4 row_mask:0xf bank_mask:0xf\n v_cvt_pk_bf16_f32 v248, v248, v249\n "
      "s_nop 1\n v_mov_b32_dpp v112, v248 quad_perm:[0,0,0,0] row_mask:0xf bank_mask:0xf\n v_mov_b32_dpp v113, v248 quad_perm:[1,1,1,1] row_mask:0xf bank_mask:0xf\n "
      "v_mov_b32_dpp v114, v248 quad_perm:[2,2,2,2] row_mask:0xf bank_mask:0xf\n v_mov_b32_dpp v115, v248 quad_perm:[3,3,3,3] row_mask:0xf bank_mask:0xf\n s_waitcnt vmcnt(7) lgkmcnt(0)\n "
      "v_mov_b64 v[208:209], v[202:203]\n v_mfma_f32_16x16x32_bf16 v[176:179], v[112:115], v[120:123], v[156:159]\n v_mfma_f32_16x16x32_bf16 v[180:183], v[112:115], v[124:127], v[156:159]\n "
      "v_mfma_f32_16x16x32_bf16 v[184:187], v[112:115], v[128:131], v[156:159]\n v_rcp_f32_e64 v192, v176\n v_mfma_f32_16x16x32_bf16 v[188:191], v[112:115], v[132:135], v[156:159]\n "
      "v_rcp_f32 v193, v180\n s_waitcnt vmcnt(6)\n v_mfma_f32_16x16x32_bf16 v[164:167], %[sel], v[206:209], 0\n v_mov_b64_e64 v[202:203], v[204:205]\n v_max3_u32 v254, v246, v200, v204\n "
      "v_mfma_f32_16x16x32_bf16 v[176:179], v[112:115], v[136:139], v[156:159]\n v_rcp_f32_e64 v194, v184\n v_mfma_f32_16x16x32_bf16 v[160:163], %[sel], v[200:203], 0\n s_waitcnt vmcnt(5)\n "
      "v_mov_b64 v[218:219], v[212:213]\n v_mfma_f32_16x16x32_bf16 v[180:183], v[112:115], v[140:143], v[156:159]\n v_rcp_f32 v195, v188\n s_waitcnt vmcnt(4)\n "
      "v_mfma_f32_16x16x32_bf16 v[168:171], %[sel], v[216:219], 0\n v_mov_b64_e64 v[212:213], v[214:215]\n v_max3_u32 v254, v254, v210, v214\n "
      "v_mfma_f32_16x16x32_bf16 v[184:187], v[112:115], v[144:147], v[156:159]\n v_rcp_f32_e64 v196, v176\n v_mfma_f32_16x16x32_bf16 v[172:175], %[sel], v[210:213], 0\n s_waitcnt vmcnt(3)\n "
      "v_mov_b64 v[228:229], v[222:223]\n v_mfma_f32_16x16x32_bf16 v[188:191], v[112:115], v[148:151], v[156:159]\n v_rcp_f32 v197, v180\n s_waitcnt vmcnt(2)\n "
      "v_mfma_f32_16x16x32_bf16 v[164:167], %[sel], v[226:229], v[164:167]\n v_mov_b64_e64 v[222:223], v[224:225]\n v_max3_u32 v254, v254, v220, v224\n v_rcp_f32_e64 v198, v184\n "
      "v_mfma_f32_16x16x32_bf16 v[160:163], %[sel], v[220:223], v[160:163]\n v_rcp_f32_e64 v199, v188\n s_waitcnt vmcnt(1)\n v_mov_b64 v[238:239], v[232:233]\n "
      "v_pk_add_f32 v[164:165], v[100:101], v[164:165]\n v_pk_add_f32 v[166:167], v[102:103], v[166:167]\n v_pk_add_f32 v[164:165], v[68:69], v[164:165]\n "
      "v_pk_add_f32 v[166:167], v[70:71], v[166:167]\n v_pk_add_f32 v[160:161], v[96:97], v[160:161]\n v_pk_add_f32 v[162:163], v[98:99], v[162:163]\n "
      "v_pk_add_f32 v[160:161], v[64:65], v[160:161]\n v_pk_add_f32 v[162:163], v[66:67], v[162:163]\n s_waitcnt vmcnt(0)\n "
      OST_(2)
      "v_mov_b64 v[232:233], v[234:235]\n v_mfma_f32_16x16x32_bf16 v[168:171], %[sel], v[236:239], v[168:171]\n v_max3_u32 v254, v254, v230, v234\n v_cvt_pk_bf16_f32 v118, v164, v165\n "
      "v_mfma_f32_16x16x32_bf16 v[172:175], %[sel], v[230:233], v[172:175]\n v_cvt_pk_bf16_f32 v119, v166, v167\n v_cvt_pk_bf16_f32 v116, v160, v161\n v_cvt_pk_bf16_f32 v117, v162, v163\n "
      "v_cmp_eq_u32 vcc, -1, v254\n s_cbranch_vccnz .Lrt3\n .Lok3:\n "
      OST_(3)
      "v_mfma_f32_16x16x32_bf16 v[200:203], v[116:119], v[120:123], 0\n v_add_f32 v168, v104, v168\n v_add_f32 v169, v105, v169\n "
      "v_mfma_f32_16x16x32_bf16 v[204:207], v[116:119], v[124:127], 0\n v_add_f32 v170, v106, v170\n v_add_f32 v171, v107, v171\n "
      "v_mfma_f32_16x16x32_bf16 v[208:211], v[116:119], v[128:131], 0\n v_add_f32 v168, v72, v168\n v_add_f32 v169, v73, v169\n "
      "v_mfma_f32_16x16x32_bf16 v[212:215], v[116:119], v[132:135], 0\n v_add_f32 v170, v74, v170\n v_add_f32 v171, v75, v171\n "
      "v_mfma_f32_16x16x32_bf16 v[216:219], v[116:119], v[136:139], 0\n v_add_f32 v172, v108, v172\n v_add_f32 v173, v109, v173\n v_cvt_pk_bf16_f32 v152, v168, v169\n "
      "v_mfma_f32_16x16x32_bf16 v[220:223], v[116:119], v[140:143], 0\n v_add_f32 v174, v110, v174\n v_add_f32 v175, v111, v175\n v_cvt_pk_bf16_f32 v153, v170, v171\n "
      "v_mfma_f32_16x16x32_bf16 v[224:227], v[116:119], v[144:147], 0\n v_add_f32 v172, v76, v172\n v_add_f32 v173, v77, v173\n v_add_f32 v174, v78, v174\n v_add_f32 v175, v79, v175\n "
      "v_cvt_pk_bf16_f32 v154, v172, v173\n v_cvt_pk_bf16_f32 v155, v174, v175\n v_mfma_f32_16x16x32_bf16 v[228:231], v[116:119], v[148:151], 0\n v_mul_f32 v200, v200, v192\n "
      "v_mul_f32 v201, v201, v192\n v_mfma_f32_16x16x32_bf16 v[64:67], v[152:155], v[120:123], 0\n v_mul_f32 v202, v202, v192\n v_mul_f32 v203, v203, v192\n "
      "v_mfma_f32_16x16x32_bf16 v[68:71], v[152:155], v[124:127], 0\n v_cvt_pk_bf16_f32 v96, v200, v201\n v_cvt_pk_bf16_f32 v97, v202, v203\n "
      "v_mfma_f32_16x16x32_bf16 v[72:75], v[152:155], v[128:131], 0\n v_mul_f32 v204, v204, v193\n v_mul_f32 v205, v205, v193\n "
      "v_mfma_f32_16x16x32_bf16 v[76:79], v[152:155], v[132:135], 0\n v_mul_f32 v206, v206, v193\n v_mul_f32 v207, v207, v193\n "
      "v_mfma_f32_16x16x32_bf16 v[80:83], v[152:155], v[136:139], 0\n v_cvt_pk_bf16_f32 v100, v204, v205\n v_cvt_pk_bf16_f32 v101, v206, v207\n "
      "v_mfma_f32_16x16x32_bf16 v[84:87], v[152:155], v[140:143], 0\n v_mul_f32 v208, v208, v194\n v_mul_f32 v209, v209, v194\n "
      "v_mfma_f32_16x16x32_bf16 v[88:91], v[152:155], v[144:147], 0\n v_mul_f32 v210, v210, v194\n v_mul_f32 v211, v211, v194\n "
      "v_mfma_f32_16x16x32_bf16 v[92:95], v[152:155], v[148:151], 0\n v_cvt_pk_bf16_f32 v104, v208, v209\n v_cvt_pk_bf16_f32 v105, v210, v211\n "
      OST_(4)
      "v_mul_f32 v64, v64, v192\n v_mul_f32 v65, v65, v192\n v_mul_f32 v66, v66, v192\n v_mul_f32 v67, v67, v192\n v_cvt_pk_bf16_f32 v98, v64, v65\n v_cvt_pk_bf16_f32 v99, v66, v67\n "
      "buffer_store_dwordx4 v[96:99], %[ofo], %[ws], 0 offen offset:0 sc1\n v_pk_mul_f32 v[68:69], v[68:69], v[192:193] op_sel:[0,1]\n "
      "v_pk_mul_f32 v[70:71], v[70:71], v[192:193] op_sel:[0,1]\n v_cvt_pk_bf16_f32 v102, v68, v69\n v_cvt_pk_bf16_f32 v103, v70, v71\n "
      "buffer_store_dwordx4 v[100:103], %[ofo], %[ws], 0 offen offset:256 sc1\n v_pk_mul_f32 v[72:73], v[72:73], v[194:195] op_sel_hi:[1,0]\n "
      "v_pk_mul_f32 v[74:75], v[74:75], v[194:195] op_sel_hi:[1,0]\n v_cvt_pk_bf16_f32 v106, v72, v73\n v_cvt_pk_bf16_f32 v107, v74, v75\n "
      "buffer_store_dwordx4 v[104:107], %[ofo], %[ws], 0 offen offset:2048 sc1\n v_pk_mul_f32 v[76:77], v[76:77], v[194:195] op_sel:[0,1]\n "
      "v_pk_mul_f32 v[78:79], v[78:79], v[194:195] op_sel:[0,1]\n v_pk_mul_f32 v[212:213], v[212:213], v[194:195] op_sel:[0,1]\n "
      "v_pk_mul_f32 v[214:215], v[214:215], v[194:195] op_sel:[0,1]\n v_cvt_pk_bf16_f32 v108, v212, v213\n v_cvt_pk_bf16_f32 v109, v214, v215\n v_cvt_pk_bf16_f32 v110, v76, v77\n "
      "v_cvt_pk_bf16_f32 v111, v78, v79\n buffer_store_dwordx4 v[108:111], %[ofo], %[ws], 0 offen offset:2304 sc1\n v_pk_mul_f32 v[80:81], v[80:81], v[196:197] op_sel_hi:[1,0]\n "
      "v_pk_mul_f32 v[82:83], v[82:83], v[196:197] op_sel_hi:[1,0]\n v_pk_mul_f32 v[216:217], v[216:217], v[196:197] op_sel_hi:[1,0]\n "
      "v_pk_mul_f32 v[218:219], v[218:219], v[196:197] op_sel_hi:[1,0]\n v_cvt_pk_bf16_f32 v176, v216, v217\n v_cvt_pk_bf16_f32 v177, v218, v219\n v_cvt_pk_bf16_f32 v178, v80, v81\n "
      "v_cvt_pk_bf16_f32 v179, v82, v83\n buffer_store_dwordx4 v[176:179], %[ofo], %[ws], s73 offen offset:0 sc1\n v_pk_mul_f32 v[84:85], v[84:85], v[196:197] op_sel:[0,1]\n "
      "v_pk_mul_f32 v[86:87], v[86:87], v[196:197] op_sel:[0,1]\n v_pk_mul_f32 v[220:221], v[220:221], v[196:197] op_sel:[0,1]\n "
      "v_pk_mul_f32 v[222:223], v[222:223], v[196:197] op_sel:[0,1]\n v_cvt_pk_bf16_f32 v180, v220, v221\n v_cvt_pk_bf16_f32 v181, v222, v223\n v_cvt_pk_bf16_f32 v182, v84, v85\n "
      "v_cvt_pk_bf16_f32 v183, v86, v87\n buffer_store_dwordx4 v[180:183], %[ofo], %[ws], s73 offen offset:256 sc1\n v_pk_mul_f32 v[88:89], v[88:89], v[198:199] op_sel_hi:[1,0]\n "
      "v_pk_mul_f32 v[90:91], v[90:91], v[198:199] op_sel_hi:[1,0]\n v_pk_mul_f32 v[224:225], v[224:225], v[198:199] op_sel_hi:[1,0]\n "
      "v_pk_mul_f32 v[226:227], v[226:227], v[198:199] op_sel_hi:[1,0]\n v_cvt_pk_bf16_f32 v184, v224, v225\n v_cvt_pk_bf16_f32 v185, v226, v227\n v_cvt_pk_bf16_f32 v186, v88, v89\n "
      "v_cvt_pk_bf16_f32 v187, v90, v91\n buffer_store_dwordx4 v[184:187], %[ofo], %[ws], s73 offen offset:2048 sc1\n v_pk_mul_f32 v[92:93], v[92:93], v[198:199] op_sel:[0,1]\n "
      "v_pk_mul_f32 v[94:95], v[94:95], v[198:199] op_sel:[0,1]\n v_pk_mul_f32 v[228:229], v[228:229], v[198:199] op_sel:[0,1]\n "
      "v_pk_mul_f32 v[230:231], v[230:231], v[198:199] op_sel:[0,1]\n v_cvt_pk_bf16_f32 v188, v228, v229\n v_cvt_pk_bf16_f32 v189, v230, v231\n v_cvt_pk_bf16_f32 v190, v92, v93\n "
      "v_cvt_pk_bf16_f32 v191, v94, v95\n buffer_store_dwordx4 v[188:191], %[ofo], %[ws], s73 offen offset:2304 sc1\n "
      OST_(5)
      "v_mov_b32 v248, 4\n ds_write_b128 %[lwa], v[96:99] offset:0\n ds_write_b128 %[lwa], v[100:103] offset:256\n ds_write_b128 %[lwa], v[104:107] offset:2048\n "
      "ds_write_b128 %[lwa], v[108:111] offset:2304\n ds_write_b128 %[lwa], v[176:179] offset:8192\n ds_write_b128 %[lwa], v[180:183] offset:8448\n "
      "ds_write_b128 %[lwa], v[184:187] offset:10240\n ds_write_b128 %[lwa], v[188:191] offset:10496\n ds_write_b32 %[mfl], v248\n s_branch .Lgd\n .Lrt0:\n s_add_u32 %[nr], %[nr], 1\n "
      "s_bitcmp1_b32 %[nr], 21\n s_cbranch_scc1 .Lok0\n s_sleep 2\n v_xor_b32 v248, 0x6000, v252\n v_xor_b32 v249, 0x600, v253\n global_load_dwordx2 v[240:241], v249, %[zb] sc1\n "
      "global_load_dwordx4 v[80:83], v248, %[kb] offset:-4096 sc1\n global_load_dwordx4 v[84:87], v248, %[kb] offset:-2048 sc1\n global_load_dwordx4 v[90:93], v248, %[kb] offset:-3072 sc1\n "
      "global_load_dwordx4 v[94:97], v248, %[kb] offset:-1024 sc1\n global_load_dwordx4 v[100:103], v248, %[kb] offset:0 sc1\n global_load_dwordx4 v[104:107], v248, %[kb] offset:2048 sc1\n "
      "global_load_dwordx4 v[110:113], v248, %[kb] offset:1024 sc1\n global_load_dwordx4 v[114:117], v248, %[kb] offset:3072 sc1\n s_waitcnt vmcnt(0)\n s_branch .Lck0\n .Lrt1:\n "
      "s_add_u32 %[nr], %[nr], 1\n s_bitcmp1_b32 %[nr], 21\n s_cbranch_scc1 .Lok1\n s_sleep 2\n v_xor_b32 v248, 0x4000, v252\n v_xor_b32 v249, 0x400, v253\n "
      "global_load_dwordx2 v[242:243], v249, %[zb] sc1\n global_load_dwordx4 v[120:123], v248, %[kb] offset:-4096 sc1\n global_load_dwordx4 v[124:127], v248, %[kb] offset:-2048 sc1\n "
      "global_load_dwordx4 v[130:133], v248, %[kb] offset:-3072 sc1\n global_load_dwordx4 v[134:137], v248, %[kb] offset:-1024 sc1\n "
      "global_load_dwordx4 v[140:143], v248, %[kb] offset:0 sc1\n global_load_dwordx4 v[144:147], v248, %[kb] offset:2048 sc1\n global_load_dwordx4 v[150:153], v248, %[kb] offset:1024 sc1\n "
      "global_load_dwordx4 v[154:157], v248, %[kb] offset:3072 sc1\n s_waitcnt vmcnt(0)\n s_branch .Lck1\n .Lrt2:\n s_add_u32 %[nr], %[nr], 1\n s_bitcmp1_b32 %[nr], 21\n "
      "s_cbranch_scc1 .Lok2\n s_sleep 2\n v_xor_b32 v248, 0x2000, v252\n v_xor_b32 v249, 0x200, v253\n global_load_dwordx2 v[244:245], v249, %[zb] sc1\n "
      "global_load_dwordx4 v[160:163], v248, %[kb] offset:-4096 sc1\n global_load_dwordx4 v[164:167], v248, %[kb] offset:-2048 sc1\n "
      "global_load_dwordx4 v[170:173], v248, %[kb] offset:-3072 sc1\n global_load_dwordx4 v[174:177], v248, %[kb] offset:-1024 sc1\n "
      "global_load_dwordx4 v[180:183], v248, %[kb] offset:0 sc1\n global_load_dwordx4 v[184:187], v248, %[kb] offset:2048 sc1\n global_load_dwordx4 v[190:193], v248, %[kb] offset:1024 sc1\n "
      "global_load_dwordx4 v[194:197], v248, %[kb] offset:3072 sc1\n s_waitcnt vmcnt(0)\n s_branch .Lck2\n .Lrt3:\n s_add_u32 %[nr], %[nr], 1\n s_bitcmp1_b32 %[nr], 21\n "
      "s_cbranch_scc1 .Lok3\n s_sleep 2\n global_load_dwordx2 v[246:247], v253, %[zb] sc1\n global_load_dwordx4 v[200:203], v252, %[kb] offset:-4096 sc1\n "
      "global_load_dwordx4 v[204:207], v252, %[kb] offset:-2048 sc1\n global_load_dwordx4 v[210:213], v252, %[kb] offset:-3072 sc1\n "
      "global_load_dwordx4 v[214:217], v252, %[kb] offset:-1024 sc1\n global_load_dwordx4 v[220:223], v252, %[kb] offset:0 sc1\n "
      "global_load_dwordx4 v[224:227], v252, %[kb] offset:2048 sc1\n global_load_dwordx4 v[230:233], v252, %[kb] offset:1024 sc1\n "
      "global_load_dwordx4 v[234:237], v252, %[kb] offset:3072 sc1\n s_branch .Lg3\n .Lgd:\n "
      OSTE
      [nr] "=&s"(nr)
      : [gx] "s"(gx), [now] "s"(now), [d0] "s"(dg[0]), [d1] "s"(dg[1]), [d2] "s"(dg[2]), [d3] "s"(dg[3]), [kb] "s"(kvb), [zb] "s"(zb), [ws] "s"(wsr), [om] "s"(lw ? 0x0Au : 0x25u),
        [lv] "v"(lv16), [zo] "v"(zoff), [mfl] "v"(mfl), [afl] "v"(afl), [thr] "v"(thr), [pa] "v"(pa), [ofo] "v"(ofo), [lwa] "v"(lwa), [sel] "v"(sel)
      : "memory", "scc", "vcc", "s72", "s73", "s74", "s75", "s76",
        "v64", "v65", "v66", "v67", "v68", "v69", "v70", "v71", "v72", "v73", "v74", "v75", "v76", "v77", "v78", "v79", "v80", "v81", "v82", "v83", "v84", "v85", "v86", "v87", "v88", "v89",
        "v90", "v91", "v92", "v93", "v94", "v95", "v96", "v97", "v98", "v99", "v100", "v101", "v102", "v103", "v104", "v105", "v106", "v107", "v108", "v109", "v110", "v111", "v112", "v113",
        "v114", "v115", "v116", "v117", "v118", "v119", "v120", "v121", "v122", "v123", "v124", "v125", "v126", "v127", "v128", "v129", "v130", "v131", "v132", "v133", "v134", "v135",
        "v136", "v137", "v138", "v139", "v140", "v141", "v142", "v143", "v144", "v145", "v146", "v147", "v148", "v149", "v150", "v151", "v152", "v153", "v154", "v155", "v156", "v157",
        "v158", "v159", "v160", "v161", "v162", "v163", "v164", "v165", "v166", "v167", "v168", "v169", "v170", "v171", "v172", "v173", "v174", "v175", "v176", "v177", "v178", "v179",
        "v180", "v181", "v182", "v183", "v184", "v185", "v186", "v187", "v188", "v189", "v190", "v191", "v192", "v193", "v194", "v195", "v196", "v197", "v198", "v199", "v200", "v201",
        "v202", "v203", "v204", "v205", "v206", "v207", "v208", "v209", "v210", "v211", "v212", "v213", "v214", "v215", "v216", "v217", "v218", "v219", "v220", "v221", "v222", "v223",
        "v224", "v225", "v226", "v227", "v228", "v229", "v230", "v231", "v232", "v233", "v234", "v235", "v236", "v237", "v238", "v239", "v240", "v241", "v242", "v243", "v244", "v245",
        "v246", "v247", "v248", "v249", "v250", "v251", "v252", "v253", "v254");
    STMP(4);
    STFLUSH();
    asm volatile(".Lend_l:");
  }

}

extern "C" void kernel_launch(void* const* d_in, const int* in_sizes, int n_in, void* d_out, int out_size,
                              void* d_ws, size_t ws_size, hipStream_t stream) {
  (void)hipFuncSetAttribute((const void*)fused, hipFuncAttributeMaxDynamicSharedMemorySize, LDS_SZ);
  fused<<<256, 512, LDS_SZ, stream>>>((const u16*)d_in[0], (const u16*)d_in[1], (const u16*)d_in[4], (char*)d_ws, (const u16*)d_in[2],
                                      (const u16*)d_in[5], (const u16*)d_in[6], (const u16*)d_in[7], (u16*)d_out);
}
```

```cpp
#include <hip/hip_runtime.h>
#include <stdint.h>

typedef unsigned short u16;
typedef short s4 __attribute__((ext_vector_type(4)));
typedef short s8 __attribute__((ext_vector_type(8)));
typedef float f4 __attribute__((ext_vector_type(4)));
typedef float f16v __attribute__((ext_vector_type(16)));
typedef float f2 __attribute__((ext_vector_type(2)));
typedef unsigned u4 __attribute__((ext_vector_type(4)));
typedef unsigned u2 __attribute__((ext_vector_type(2)));
typedef unsigned u8v __attribute__((ext_vector_type(8)));
typedef __bf16 bf2 __attribute__((ext_vector_type(2)));
#define LAS __attribute__((address_space(3)))
#define DEV __device__ __forceinline__

constexpr int D = 1024, F = 3584;
#define NOMOE 0
#define KD 224
#define KS 222
#define OD 184
#define OHP 1
#define FS 128
#define GSL 8
#define YSL 16
#define TP1 0
#define TP2 3
#define TLBW 0
#define FULL 1
#define HPRIO 1
#define OWN 1
#define NOPOIS 0
#define CAP 7
#define RD 4
#define DYN 1
#define JX 0
#define NOSKIP 0
#define GRP 8
#define ADV 15
#define HB 1
#define RT 1
#define WD 0
#define WU1 12
#define WU2 22
#define PL 0
#define PO 0
#define WG 0
#define DS 0
#define D1 16
#define D2 2
#define HKS 2
#define LW1 1
#define HW1 2
#define HS0 0
#define GPV 2
#define XO 1
#define TP0 3
#define G1 0x31
#define G1S 4
#define G2S 8
#define TD 1
#define YD 170
#define WU0 4
#define WE 16
#define HT 115
#define CF 19
#define KL 145
#define OL 115
#define CL 0
#define WS0 0
#define OM 0
#define HG 128
#define U0 0
#define U3 5
#define TL 0
#define MH 0
#define HWT 3
#define YG 0
#define GG 1
#define PV 1
#define U1 4
#define U2 2
#define QD 1
#define LP0 3
#define LP1 3
#define WA 8
#define PW 3
#define WB 28
#define PF 1
#define PFS 6
#define PA 0
#define PAS 0
#define LPL 0
#define WP 1
#define E0 0
#define ES 0
#define IL 1
#define LW4 1
#define WF 1
#define LW3 2
#define ROT 0
#define LF 0
#define SL5 1
#define GE 0
#define ZH 1
#define TW 0
#define TWS 0
#define TCH 8
#define SPW 50
#define SP2 0
#define SP0 1
#define SPS 35
#define TH 0
#define TE 1
#define TN 4
#define TS 1
#define SE 1
#define TLP 4
#define SSL 64
#define T2 0
#define GI 0
#define GO 0
#define ZF 0
#define GW 1
#define F5 1
#define WT 0xDCB87654FEA93210ull
#define PE 3
#define GX 0
#define GS 0
#define C2 1
#define CB 4
#define DE 0
#define HS1 0
#define WD0 0
#define W00 0
#define KX 50

constexpr unsigned WS_E = 0;
constexpr unsigned WS_CT = 524288;
constexpr unsigned WS_CAND = 12288;
constexpr unsigned WS_RG = 49152;
constexpr unsigned WS_ST = 65536;
constexpr unsigned WS_HK = 589824;
constexpr unsigned WS_ZP = 131072;
constexpr unsigned WS_CS = 196608;
constexpr unsigned WS_OH = 458752;
constexpr unsigned WS_KVP = 1u << 20;
constexpr unsigned WS_GP = 2u << 20;
constexpr unsigned WS_YP = 3u << 20;
constexpr unsigned WS_OF = 5u << 20;
constexpr unsigned WS_SZ = 9u << 20;
#define NR 4
#define RGO(tag) ((64u << 20) + ((tag) & (NR - 1u)) * (10u << 20))
#define MKWS(tag) char* const wsx = ws + RGO(tag); const __amdgpu_buffer_rsrc_t wsr = __builtin_amdgcn_make_buffer_rsrc(wsx, 0, WS_SZ, 0x00020000)

constexpr int STG = 40960;
constexpr int L_KVX = 0;
constexpr int L_PQ = 12288;
constexpr int L_M = 28672, L_RS = L_M + 512, L_CS = L_RS + 512, L_SEL = L_CS + 768, L_FL = L_SEL + 64;
constexpr int L_HX = 30720, L_RL = 31744, L_WO = 32768;
constexpr int LDS_SZ = 163840;
constexpr unsigned POIS = 0xFFFFFFFFu;

DEV unsigned pk2(float a, float b) { bf2 v; v[0] = (__bf16)a; v[1] = (__bf16)b; return __builtin_bit_cast(unsigned, v); }
DEV float lo(unsigned u) { return __uint_as_float(u << 16); }
DEV float hi(unsigned u) { return __uint_as_float(u & 0xFFFF0000u); }
DEV float rbf(float a) { return lo(pk2(a, a)); }
DEV float dot2(unsigned a, unsigned b, float c) { return __builtin_amdgcn_fdot2_f32_bf16(__builtin_bit_cast(bf2, a), __builtin_bit_cast(bf2, b), c, false); }
#define RD128(dst, addr, OFF) asm volatile("ds_read_b128 %0, %1 offset:%2" : "=v"(dst) : "v"(addr), "i"(OFF) : "memory")
#define RDTR(dst, addr, OFF) asm volatile("ds_read_b64_tr_b16 %0, %1 offset:%2" : "=v"(dst) : "v"(addr), "i"(OFF) : "memory")
#define AL8() asm volatile(".p2align 3")
#define SBAR() __builtin_amdgcn_sched_barrier(0)
#define CAT(x, y) __builtin_shufflevector(x, y, 0, 1, 2, 3, 4, 5, 6, 7)
#define WAITV(n) asm volatile("s_waitcnt vmcnt(%0)" ::"i"(n) : "memory")
#define WAITL(n) asm volatile("s_waitcnt lgkmcnt(%0)" ::"i"(n) : "memory")
#define MEMB() asm volatile("" ::: "memory")
#define SLEEP(n) asm volatile("s_sleep %0" ::"i"(n) : "memory")
#define BAR() asm volatile("s_barrier" ::: "memory")
#define GL0(vo, sb, m0v) asm volatile("s_mov_b32 m0, %2\n s_nop 0\n global_load_lds_dwordx4 %0, %1" ::"v"(vo), "s"(sb), "s"(m0v) : "memory")
#define GLI(vo, sb, OFF) asm volatile("global_load_lds_dwordx4 %0, %1 offset:%2" ::"v"(vo), "s"(sb), "i"(OFF) : "memory")
#define GL4(v, sb, mm, MO) do { asm volatile("s_add_i32 m0, %2, %3\n s_nop 0\n global_load_lds_dwordx4 %0, %1" ::"v"((v)[0]), "s"(sb), "s"(mm), "i"(MO) : "memory", "scc"); \
      GLI((v)[1], sb, 1024); GLI((v)[2], sb, 2048); GLI((v)[3], sb, 3072); } while (0)
#define VFW(i) ((8 * (i) + (l >> 3)) * 2048 + (((l & 7) ^ (((l >> 4) & 1) << 2)) * 16) + (3 - (i)) * 1024)
#define MFMA16(a, b, c) __builtin_amdgcn_mfma_f32_16x16x32_bf16(a, b, c, 0, 0, 0)
#define S8(x) __builtin_bit_cast(s8, x)
#define LD128(off) __builtin_amdgcn_raw_buffer_load_b128(wsr, (off), 0, 16)
#define LD64(off) __builtin_amdgcn_raw_buffer_load_b64(wsr, (off), 0, 16)
#define ST128(v, off) __builtin_amdgcn_raw_buffer_store_b128((v), wsr, (off), 0, 16)
#define ST64(v, off) __builtin_amdgcn_raw_buffer_store_b64((v), wsr, (off), 0, 16)
#define OFA(i) (WS_OF + ((mb * 32 + hd) * 8 + (i)) * 1024 + l * 16)
#define POISON(i) ST128(pz, (i) < 2 ? WS_KVP + (hd * 16 + mb) * 2048 + (i) * 1024 + l * 16 : (i) == 2 ? WS_ZP + (hd * 16 + mb) * 128 + (l & 7) * 16 : OFA((i) - 3))
#define NOW() ((unsigned)__builtin_amdgcn_s_memrealtime())
constexpr unsigned SPIN = 1u << 21;
DEV const char* uptr(const char* p) {
  unsigned long long v = (unsigned long long)p;
  unsigned lo_ = __builtin_amdgcn_readfirstlane((unsigned)v), hi_ = __builtin_amdgcn_readfirstlane((unsigned)(v >> 32));
  return (const char*)(((unsigned long long)hi_ << 32) | lo_);
}
DEV const char* padd(const char* p, unsigned o) {
  unsigned lo_, hi_;
  asm("s_add_u32 %0, %2, %4\n s_addc_u32 %1, %3, 0" : "=&s"(lo_), "=s"(hi_) : "s"((unsigned)(unsigned long long)p), "s"((unsigned)((unsigned long long)p >> 32)), "s"(o) : "scc");
  return (const char*)(((unsigned long long)hi_ << 32) | lo_);
}
DEV void lsig(unsigned addr, unsigned val) { asm volatile("s_waitcnt lgkmcnt(0)\n ds_write_b32 %0, %1" ::"v"(addr), "v"(val) : "memory"); }
template <int SL = 0> DEV void lwait(unsigned addr, unsigned mask, unsigned target) {
  _Pragma("unroll 1") for (unsigned it = 0; it < SPIN; ++it) {
    unsigned v;
    asm volatile("ds_read_b32 %0, %1\n s_waitcnt lgkmcnt(0)" : "=v"(v) : "v"(addr) : "memory");
    if (((unsigned)__ballot(v < target) & mask) == 0u) break;
    if (SL) SLEEP(SL);
  }
}
DEV unsigned nopois(unsigned a) { return min(a, POIS - 1u); }
DEV u4 tagf(f4 v, unsigned tag) {
  u4 r;
#pragma unroll
  for (int e = 0; e < 4; ++e) r[e] = __builtin_amdgcn_perm(__float_as_uint(v[e]), tag, 0x07060500u + e);
  return r;
}
DEV void untag(u4& v, unsigned tag, unsigned& bad) {
#pragma unroll
  for (int e = 0; e < 4; ++e) v[e] ^= (tag >> (8 * e)) & 255u;
  bad |= v[0] | v[1]; bad |= v[2] | v[3];
}
DEV unsigned umax3(unsigned a, unsigned b, unsigned c) { return max(max(a, b), c); }
template <int C> DEV float dppf(float v) { return __int_as_float(__builtin_amdgcn_update_dpp(0, __float_as_int(v), C, 0xf, 0xf, false)); }
DEV unsigned wave_max(unsigned v) {
  v = max(v, (unsigned)__builtin_amdgcn_update_dpp(0, (int)v, 0x111, 0xf, 0xf, false));
  v = max(v, (unsigned)__builtin_amdgcn_update_dpp(0, (int)v, 0x112, 0xf, 0xf, false));
  v = max(v, (unsigned)__builtin_amdgcn_update_dpp(0, (int)v, 0x114, 0xf, 0xf, false));
  v = max(v, (unsigned)__builtin_amdgcn_update_dpp(0, (int)v, 0x118, 0xf, 0xf, false));
  v = max(v, (unsigned)__builtin_amdgcn_update_dpp(0, (int)v, 0x142, 0xa, 0xf, false));
  v = max(v, (unsigned)__builtin_amdgcn_update_dpp(0, (int)v, 0x143, 0xc, 0xf, false));
  return (unsigned)__builtin_amdgcn_readlane((int)v, 63);
}
DEV unsigned phi2(f2 u, f2 rs, f2 rl) {
  f2 a = u * rl, p = u * rs + 1.f, r;
  r[0] = __builtin_amdgcn_fmed3f(__builtin_amdgcn_exp2f(a[0]), p[0], 1.f);
  r[1] = __builtin_amdgcn_fmed3f(__builtin_amdgcn_exp2f(a[1]), p[1], 1.f);
  r += 1e-6f;
  return pk2(r[0], r[1]);
}

#define STDECL
#undef HB
#define HB 0
#define BSTV cw
#define STMP(i)
#define BST(i)
#define STFLUSH()
#define RETRY()
#define RETRY2()

DEV float half_sum(float v) {
  asm volatile("s_nop 1\n v_add_f32_dpp %0, %0, %0 row_ror:8 row_mask:0xf bank_mask:0xf\n s_nop 1\n v_add_f32_dpp %0, %0, %0 row_ror:4 row_mask:0xf bank_mask:0xf\n"
               "s_nop 1\n v_add_f32_dpp %0, %0, %0 row_ror:2 row_mask:0xf bank_mask:0xf\n s_nop 1\n v_add_f32_dpp %0, %0, %0 row_ror:1 row_mask:0xf bank_mask:0xf\n s_nop 1" : "+v"(v));
  auto r = __builtin_amdgcn_permlane16_swap(__float_as_uint(v), __float_as_uint(v), false, false);
  return __uint_as_float(r[0]) + __uint_as_float(r[1]);
}

constexpr unsigned long long ownat(unsigned long long a, unsigned long long b, unsigned long long c, unsigned long long d) {
  unsigned long long r = 0;
  for (int h = 0; h < 16; ++h) {
    const unsigned long long t = (h & 2) ? ((h & 1) ? d : c) : ((h & 1) ? b : a);
    for (int i = 0; i < 16; ++i) if ((int)((t >> (4 * i)) & 15) == h) r |= (unsigned long long)i << (4 * h);
  }
  return r;
}
constexpr int wtat(int i) { return (int)(WT >> (4 * i)) & 15; }
constexpr int wtmin(int a, int b) { int m = 99; for (int i = a; i < b; ++i) m = wtat(i) < m ? wtat(i) : m; return m; }
constexpr int wtmax(int a, int b) { int m = 0; for (int i = a; i < b; ++i) m = wtat(i) > m ? wtat(i) : m; return m; }
struct FQ { s8 a[4]; s4 b[12]; };
template <bool B> struct BC { static constexpr bool v = B; };
template <int I> struct IC { static constexpr int v = I; };

__global__ __launch_bounds__(512) void fused(const u16* __restrict__ x, const u16* __restrict__ wqkv, const u16* __restrict__ wrt,
                                             char* __restrict__ ws_, const u16* __restrict__ wo_, const u16* __restrict__ wg,
                                             const u16* __restrict__ wu, const u16* __restrict__ wd, u16* __restrict__ out) {
  extern __shared__ __attribute__((aligned(16))) char lds[];
  const char *kx, *kq, *kr, *wo; char* ws;
  asm volatile("s_load_dwordx2 %0, %5, 0\n s_load_dwordx2 %1, %5, 8\n s_load_dwordx2 %2, %5, 16\n s_load_dwordx2 %3, %5, 24\n s_load_dwordx2 %4, %5, 32"
               : "=&s"(kx), "=&s"(kq), "=&s"(kr), "=&s"(ws), "=&s"(wo) : "s"(__builtin_amdgcn_kernarg_segment_ptr()));
  SBAR();
  const int w = __builtin_amdgcn_readfirstlane(threadIdx.x >> 6), l = threadIdx.x & 63;
  STDECL;
  STMP(0);
  const int g = blockIdx.x, xcd = g & 7, j = g >> 3, ii = j >> 2;
  const int mb = (xcd & 1) * 8 + ii, hp = (xcd >> 1) * 4 + (j & 3);
  unsigned cw, cwo = (l * 32 + j) * 64; unsigned long long cwb;
  asm volatile("s_getpc_b64 %2\n.Lcode_here:\n v_min_u32 %1, max(.Lend_c - .Lcode_here, .Lend_l - .Lcode_here, .Lend_h - .Lcode_here, .Lend_s - .Lcode_here) - 4, %1\n"
               "s_cmp_lg_u32 %3, 7\n s_cbranch_scc1 .Lskip_w\n global_load_dword %0, %1, %2\n.Lskip_w:" : "=&v"(cw), "+v"(cwo), "=&s"(cwb) : "s"(w) : "memory", "scc");
  SBAR();
  const int lw = w & 1, rot = ROT == 0 ? 2 * xcd + 4 * (ii & 3) : ROT == 1 ? 2 * xcd + 8 * (ii & 1) : ROT == 2 ? 2 * xcd : ROT == 3 ? 0 : ROT == 4 ? 4 * (xcd & 1) + 8 * (ii & 1) : ROT == 7 ? 2 * ii + (xcd & 1) + 2 * (xcd & 6) : ROT == 8 ? 2 * ii + 2 * (xcd & 6) : ROT == 9 ? 2 * ii + (xcd & 1) + 2 * (xcd & 6) + 8 * (j & 1) : ROT == 10 ? 2 * ii + (xcd & 1) : 2 * (xcd & 1) + 4 * (ii & 3);
  const unsigned ldsb = (unsigned)(size_t)(LAS char*)lds;
  unsigned va[4];
#pragma unroll
  for (int i = 0; i < 4; ++i) va[i] = (8 * i + (l >> 3)) * 2048 + (((l & 7) ^ ((i & 1) * 4 + (l >> 4))) * 16) + (3 - i) * 1024;
  const int ao = (mb * 128 + lw * 64) * 2048 - 3072;
  long long ao0 = ao + (rot & 15) * 128;
  unsigned mba = ldsb + lw * 8192;
  const int srow = mb * 128 + hp * 8 + 2 * w + (l >> 5);
  unsigned xo = srow * 2048 + (l & 31) * 16, ro = (l & 31) * 16;
  asm volatile("" : "+v"(va[0]), "+v"(va[1]), "+v"(va[2]), "+v"(va[3]), "+v"(xo), "+v"(ro));
  asm volatile("s_waitcnt lgkmcnt(0)" : "+s"(kx), "+s"(kq), "+s"(kr), "+s"(ws), "+s"(wo), "+s"(ao0), "+s"(mba));
  SBAR();
  asm volatile("s_sub_u32 m0, %6, 4\n s_cmp_gt_u32 m0, 1\n s_cbranch_scc1 .Lskip_l\n s_mov_b32 m0, %5\n s_nop 0\n global_load_lds_dwordx4 %0, %4\n global_load_lds_dwordx4 %1, %4 offset:1024\n"
               "global_load_lds_dwordx4 %2, %4 offset:2048\n global_load_lds_dwordx4 %3, %4 offset:3072\n.Lskip_l:"
               :: "v"(va[0]), "v"(va[1]), "v"(va[2]), "v"(va[3]), "s"(kx + ao0), "s"(mba), "s"(w) : "memory", "scc");
  u4 xa[4], rw[4]; unsigned ep;
  asm volatile("s_cmp_gt_u32 %14, 3\n s_cbranch_scc1 .Lskip_x\n"
               "global_load_dwordx4 %0, %9, %11\n global_load_dwordx4 %1, %9, %11 offset:512\n global_load_dwordx4 %2, %9, %11 offset:1024\n global_load_dwordx4 %3, %9, %11 offset:1536\n"
               "global_load_dwordx4 %4, %10, %12\n global_load_dwordx4 %5, %10, %12 offset:512\n global_load_dwordx4 %6, %10, %12 offset:1024\n global_load_dwordx4 %7, %10, %12 offset:1536\n"
               ".Lskip_x:\n s_load_dword %8, %13, 0"
               : "=&v"(xa[0]), "=&v"(xa[1]), "=&v"(xa[2]), "=&v"(xa[3]), "=&v"(rw[0]), "=&v"(rw[1]), "=&v"(rw[2]), "=&v"(rw[3]), "=&s"(ep)
               : "v"(xo), "v"(ro), "s"(kx), "s"(kr), "s"(ws), "s"(w) : "memory", "scc");
#define GETTAG(t) unsigned t; asm volatile("s_waitcnt lgkmcnt(0)\n s_add_u32 %0, %1, 1" : "=s"(t) : "s"(ep) : "memory", "scc")
  SBAR();
#define PTAB 0xA98FEDC1045B7623ull
#define PHT 0x6624924999924924ull
#define CX 4
  const bool sp = (hp & 7) == 3;
  const int nb = hp;
#define PT0 PTAB
#define PT1 PTAB
#define PT2 PTAB
#define PT3 PTAB
  auto mktab = [&]() {
    const unsigned long long t = (hp & 2) ? ((hp & 1) ? PT3 : PT2) : ((hp & 1) ? PT1 : PT0), m = ~0ull << (4 * ((int)(ownat(PT0, PT1, PT2, PT3) >> (4 * hp)) & 15));
    unsigned long long r = (t & (m << 4)) | ((t & ~m) << 4) | (unsigned)hp;
    asm volatile("" : "+s"(r));
    return r;
  };
#define PAIRAT(p) ((int)(ptab >> (4 * (p))) & 15)
#define HEADAT(s) (2 * PAIRAT((s) >> 1) + ((s) & 1))
  const int ph = (int)(PHT >> (2 * j)) & 3;
#define HQT 0xDC1A09806572043Bull
#define CXA 1
#define CHUNK(b) ((b) < 14 ? 4 * (b) + 3 : ((b) - 14) + ((((b) - 14) * 43) >> 7))
  const unsigned adFL = ldsb + L_FL + 4 * (l & 7), myFL = ldsb + L_FL + 4 * w;
  const __amdgpu_buffer_rsrc_t wse = __builtin_amdgcn_make_buffer_rsrc(ws, 0, 64, 0x00020000);
  const int r15 = l & 15, g4 = l >> 4, q = (l & 15) >> 2, p = l & 3;
  unsigned pfo = 0, pfd = 0, pfl;
#define PFL(k) "s_load_dwordx2 s[100:101], %3, %1 offset:" #k " * 2048 + 60\n"
  auto spf = [&](const char* pb) {
    unsigned t;
    if (TCH == 3) asm volatile("s_getreg_b32 %0, hwreg(HW_REG_IB_STS, 8, 4)\n s_or_b32 %0, %0, %2\n s_cbranch_scc1 1f\n" PFL(0) PFL(1) PFL(2) PFL(3) PFL(4) PFL(5) PFL(6)
                 "s_cmp_eq_u32 %1, %4\n s_cselect_b32 %2, 1, 0\n s_add_u32 %1, %1, 7 * 2048\n s_min_u32 %1, %1, %4\n1:"
                 : "=&s"(t), "+s"(pfo), "+s"(pfd) : "s"(pb), "s"(pfl) : "scc", "s100", "s101");
  };
#define PFB(n, s) const char* pfb = wo + hp * 128 + (ii * 128 + (s) + (w & 1) * (n)) * 2048; pfl = ((n) - 7) * 2048; asm volatile("" : "+s"(pfb), "+s"(pfl))

  if (w != 4 && w != 5) {
  if (w >= 6) {
    const int mw = w - 6;
    unsigned pf = 0;
    if (PF && !(xcd & 2) && !sp && (mw == 0 || PF == 3 || (PF == 2 && (ii >> 2)))) {
      const unsigned po = (((rot + 2 - 2 * mw + (ii >> 2)) & 15) * 64 + l) * 6144 + (j & 3) * 2048 + (hp | 3) * 128;
      if (PFS) SLEEP(PFS);
      asm volatile("global_load_dword %0, %1, %2" : "+v"(pf) : "v"(po), "s"(kq) : "memory");
    }
    if (PA && mw == 1) {
      const unsigned po = (mb * 128 + (j & 3) * 32 + (l & 31)) * 2048 + (l >> 5) * 1024 + 384;
      if (PAS) SLEEP(PAS);
      asm volatile("global_load_dword %0, %1, %2" : "+v"(pf) : "v"(po), "s"(kx) : "memory");
    }
    GETTAG(tag);
    const int bq = ((int)(HQT >> (4 * hp)) & 15) * 4 + (ii & 3), ksp = (xcd & 1) * 2 + (ii >> 2);
    const int chunk = CHUNK(bq), bg = (CXA || bq < 14) ? bq ^ (w & 1) : bq, chg = CHUNK(bg);
    auto helper = [&](auto SP) {
    constexpr bool spare = decltype(SP)::v, moe = !spare && !NOMOE;
    int nbar = 0;
#define TICK() do { if (HB) { BST(nbar); asm volatile("" : "+v"(BSTV)); } SBAR(); BAR(); SBAR(); ++nbar; } while (0)
#define ZFL() do { if (ZH) asm volatile("ds_write_b32 %0, %1\n s_waitcnt lgkmcnt(0)" ::"v"(adFL), "v"(0u) : "memory"); } while (0)
#define TICKP() do { TICK(); spf(pfb); SBAR(); } while (0)
#define TICKQ() do { TICK(); spf(pfb); if (SP2) { SLEEP(SP2); spf(pfb); } SBAR(); } while (0)
#define DTICK(lim) do { if (nbar < (lim)) TICK(); else SLEEP(2); } while (0)
    const int hd = hp * 2 + mw;
    const u4 pz = (u4)(POIS);
    MKWS(tag);
    const int np = l & 7, kset = (l >> 3) & 1;
    u4 gw[4][8], dw[2][8];
    const __amdgpu_buffer_rsrc_t wgr = __builtin_amdgcn_make_buffer_rsrc((void*)(mw ? wu : wg), 0, D * F * 2, 0x00020000);
    const __amdgpu_buffer_rsrc_t wdr = __builtin_amdgcn_make_buffer_rsrc((void*)wd, 0, D * F * 2, 0x00020000);
    const __amdgpu_buffer_rsrc_t wor = __builtin_amdgcn_make_buffer_rsrc((void*)wo, 0, D * D * 2, 0x00020000);
    const unsigned gvo = ((256 * ksp + 32 * kset + 8 * g4) * F + 64 * chg + 8 * np) * 2;
    const unsigned dvo = ((64 * chunk + 8 * g4) * D + 256 * ksp + 128 * mw + 8 * r15) * 2;
    auto burst = [&](int k, int i0 = 0, int i1 = 16) {
      if (moe) {
#pragma unroll
        for (int i = i0; i < i1; ++i) {
          if (k < 2) gw[(2 * k + (i >> 3)) & 3][i & 7] = __builtin_amdgcn_raw_buffer_load_b128(wgr, gvo, (128 * k + 64 * (i >> 3) + (i & 7)) * F * 2, 2);
          else dw[i >> 3][i & 7] = __builtin_amdgcn_raw_buffer_load_b128(wdr, dvo, (32 * (i >> 3) + (i & 7)) * D * 2, 2);
        }
      }
    };
    auto transp = [&](u4(&v)[8]) {
      if (moe) {
        u4 f[8];
#pragma unroll
        for (int c = 0; c < 8; ++c)
#pragma unroll
          for (int e = 0; e < 4; ++e) {
            const unsigned ev = v[2 * e][c >> 1], od = v[2 * e + 1][c >> 1];
            f[c][e] = (c & 1) ? __builtin_amdgcn_perm(od, ev, 0x07060302u) : __builtin_amdgcn_perm(od, ev, 0x05040100u);
          }
#pragma unroll
        for (int c = 0; c < 8; ++c) { v[c] = f[c]; asm volatile("" : "+v"(v[c])); }
      }
    };
    auto pois = [&]() {
      ST128(pz, WS_KVP + (hd * 16 + mb) * 2048 + l * 16); ST128(pz, WS_KVP + (hd * 16 + mb) * 2048 + 1024 + l * 16); ST128(pz, WS_ZP + (hd * 16 + mb) * 128 + (l & 7) * 16);
    };
    unsigned wt0 = 0, wt1 = 0, wt2 = 0, wt3 = 0;
    auto touch = [&](int part = 3) {
      const unsigned to = (j * 32 + mw * 16 + (l >> 3)) * 2048 + (xcd >> 1) * 512 + (l & 7) * 64;
      if (TH) wt0 = __builtin_amdgcn_raw_buffer_load_b32(wor, to, (xcd & 1) * 16384, 0);
      else {
        if (part & 1) wt0 = __builtin_amdgcn_raw_buffer_load_b32(wor, to, 0, 0);
        if (part & 2) wt1 = __builtin_amdgcn_raw_buffer_load_b32(wor, to, 16384, 0);
      }
      if (TLBW) {
        const __amdgpu_buffer_rsrc_t outr = __builtin_amdgcn_make_buffer_rsrc(out, 0, 2048 * D * 2, 0x00020000);
        wt2 = __builtin_amdgcn_raw_buffer_load_b32(outr, (mb * 128 + mw * 64 + (l & 3) * 16) * 2048 + nb * 128, 0, 0);
        wt3 = __builtin_amdgcn_raw_buffer_load_b32(wsr, (l & 1) ? WS_GP + g * 4096 + mw * 2048 : WS_YP + (g >> 2) * 32768 + (l & 2) * 8192, 0, 0);
      }
    };
    unsigned teo;
    {
      const int pr = l < 8 ? (hp & 12) + (l & 3) : (int)(PTAB >> (4 * ((l >> 3) - 1))) & 15;
      teo = l < 8 * TN + 8 && mw == 0 ? (pr * 64 + j * 2 + ((l >> 2) & 1)) * 2048 + (xcd >> 1) * 512 + (l & 3) * 128 : D * D * 2;
      if (T2) {
        const int pr2 = l < 8 ? (hp & 12) + ((l >> 1) & 3) : pr;
        teo = l < 8 * TN + 8 ? (pr2 * 64 + j * 2 + mw) * 2048 + (xcd >> 1) * 512 + (l & 7) * 64 : D * D * 2;
      }
      asm volatile("" : "+v"(teo));
    }
    auto tearly = [&]() { wt0 = __builtin_amdgcn_raw_buffer_load_b32(wor, teo, 0, 0); };
    unsigned tlo;
    {
      const int ps = TN + (l >> 3) * 2 + mw, pr = (int)(PTAB >> (4 * (ps & 15))) & 15;
      tlo = ps < 16 ? (pr * 64 + j * 2 + ((l >> 2) & 1)) * 2048 + (xcd >> 1) * 512 + (l & 3) * 128 : D * D * 2;
      asm volatile("" : "+v"(tlo));
    }
    auto tlate = [&]() { wt1 = __builtin_amdgcn_raw_buffer_load_b32(wor, tlo, 0, 0); };
    s4 sb[24];
    auto colrd = [&](int h) {
      const unsigned bo = ldsb + (1 + h) * STG + 16384 + (8 * g4 + q) * 128 + (p & 1) * 8;
      const int sw = ((q >> 1) << 2) | ((g4 & 1) << 1);
      const unsigned b0 = bo + (((mw * 4 + (p >> 1)) ^ sw) * 16), b1 = bo + (((mw * 4 + 2 + (p >> 1)) ^ sw) * 16);
#pragma unroll
      for (int n = 0; n < 12; ++n) { RDTR(sb[2 * n], (n & 1) ? b1 : b0, ((n % 6) >> 1) * 8192 + (n / 6) * 4096); RDTR(sb[2 * n + 1], (n & 1) ? b1 : b0, ((n % 6) >> 1) * 8192 + (n / 6) * 4096 + 512); }
      WAITL(0);
      SBAR();
    };
    auto colsum = [&]() {
      const u4 ones = (u4)(0x3F803F80u);
      f4 c[6];
#pragma unroll
      for (int n = 0; n < 12; ++n) c[n % 6] = MFMA16(S8(ones), CAT(sb[2 * n], sb[2 * n + 1]), n >= 6 ? c[n % 6] : (f4)(0.f));
      const u4 csg = (u4){__float_as_uint(g4 ? c[3][0] : c[0][0]), __float_as_uint(g4 ? c[4][0] : c[1][0]), __float_as_uint(g4 ? c[5][0] : c[2][0]), tag};
      WAITV(0);
      asm volatile("" : "+v"(cw) : "v"(pf));
      ST128(csg, l < 32 ? WS_CS + (hd * 16 + mb) * 512 + l * 16 : WS_SZ);
    };
    u4 stv, cv[8], cd; float cs[3];
    auto gload = [&]() {
      stv = LD128(WS_ST + (mb * 128 + mw * 64 + l) * 16);
      if (C2 && !spare) cv[0] = LD128(WS_CT + hd * 512 + (l & 31) * 16);
      else {
#pragma unroll
        for (int i = 0; i < 8; ++i) cv[i] = LD128(WS_CS + hd * 8192 + i * 1024 + l * 16);
      }
    };
    u4 ra, rb;
    auto rload = [&]() { const unsigned ro = WS_CS + hd * 8192 + (l >> 3) * 512 + (mb & 3) * 128 + (l & 7) * 16; ra = LD128(ro); rb = LD128(ro + 4096); };
    auto rfin = [&]() {
      if (__builtin_expect(__ballot(ra[3] != tag || rb[3] != tag) != 0ull, 0)) {
        RETRY2();
        _Pragma("unroll 1") for (unsigned it = 0; it < SPIN; ++it) { SLEEP(4); rload(); if (__ballot(ra[3] != tag || rb[3] != tag) == 0ull) break; }
      }
#pragma unroll
      for (int e = 0; e < 3; ++e) {
        const float a = __uint_as_float(ra[e]) + __uint_as_float(rb[e]);
        auto r = __builtin_amdgcn_permlane32_swap(__float_as_uint(a), __float_as_uint(a), false, false);
        const float b = __uint_as_float(r[0]) + __uint_as_float(r[1]);
        auto r2 = __builtin_amdgcn_permlane16_swap(__float_as_uint(b), __float_as_uint(b), false, false);
        const float c = __uint_as_float(r2[0]) + __uint_as_float(r2[1]);
        ra[e] = __float_as_uint(c + dppf<0x128>(c));
      }
      ST128(ra, l < 8 ? WS_CT + hd * 512 + (mb & 3) * 128 + l * 16 : WS_SZ);
    };
    auto gbad = [&]() {
      unsigned bad = (stv[1] ^ tag) | (stv[3] ^ tag);
#pragma unroll
      for (int i = 0; i < (C2 && !spare ? 1 : 8); ++i) bad |= cv[i][3] ^ tag;
      return __ballot(bad != 0u) != 0ull;
    };
    int idx;
    auto gfin = [&]() {
      if (gbad()) { RETRY(); _Pragma("unroll 1") for (unsigned it = 0; it < SPIN; ++it) { SLEEP(2); gload(); if (!gbad()) break; } }
#pragma unroll
      for (int e = 0; e < 3; ++e) {
        float a = __uint_as_float(cv[0][e]);
        if (C2 && !spare) { cs[e] = a; continue; }
#pragma unroll
        for (int i = 1; i < 8; ++i) a += __uint_as_float(cv[i][e]);
        auto r = __builtin_amdgcn_permlane32_swap(__float_as_uint(a), __float_as_uint(a), false, false);
        cs[e] = __uint_as_float(r[0]) + __uint_as_float(r[1]);
      }
      if ((TCH == 2 && !spare) || TCH == 8) asm volatile("" : "+v"(cs[0]), "+v"(cs[1]), "+v"(cs[2]));
      else asm volatile("" : "+v"(cs[0]), "+v"(cs[1]), "+v"(cs[2]) : "v"(wt0), "v"(wt1), "v"(wt2), "v"(wt3));
    };
    auto cload = [&]() { cd = LD128(WS_CAND + (((hp >> 2) == 2) * 16 + mb) * 128 + (l & 7) * 16); };
    auto cfin = [&]() {
      if (__builtin_expect(__ballot(cd[1] != tag || cd[3] != tag) != 0ull, 0)) {
        RETRY();
        _Pragma("unroll 1") for (unsigned it = 0; it < SPIN; ++it) { SLEEP(4); cload(); if (__ballot(cd[1] != tag || cd[3] != tag) == 0ull) break; }
      }
      cd[0] = max(cd[0], cd[2]);
      idx = (int)(cd[0] & 2047u);
    };
    auto lpub = [&](bool sig = true) {
      *(LAS float*)(lds + L_M + (mw * 64 + l) * 4) = __uint_as_float(stv[0]);
      *(LAS float*)(lds + L_RS + (mw * 64 + l) * 4) = __uint_as_float(stv[2]);
      *(LAS float*)(lds + L_RL + (mw * 64 + l) * 4) = __uint_as_float(stv[2]) * 1.44269504f;
      if (l < 32) {
#pragma unroll
        for (int e = 0; e < 3; ++e) *(LAS float*)(lds + L_CS + mw * 384 + ((3 * g4 + e) * 16 + r15) * 4) = -cs[e];
      }
      if (mw == 0 && l < 8) *(LAS int*)(lds + L_SEL + l * 4) = idx;
      if (sig) lsig(myFL, 1u);
    };
    u4 xs[4]; float xrs;
    asm volatile("" ::"s"(wg), "s"(wu), "s"(wd), "v"(cw));
    unsigned tw[8] = {0, 0, 0, 0, 0, 0, 0, 0};
    if (TW && moe) {
      if (TWS) SLEEP(TWS);
      const char* gb = (const char*)(mw ? wu : wg) + (256 * ksp * F + 64 * chg) * 2;
      if (TW & 1) asm volatile("s_load_dword %0, %5, 0\n s_load_dword %1, %5, 458752\n s_load_dword %2, %6, 0\n s_load_dword %3, %6, 458752\n s_load_dword %4, %6, 910336"
                               : "+s"(tw[0]), "+s"(tw[1]), "+s"(tw[2]), "+s"(tw[3]), "+s"(tw[4]) : "s"(gb), "s"(gb + 917504) : "memory");
      if (TW & 2) asm volatile("s_load_dword %0, %2, 0\n s_load_dword %1, %3, 0" : "+s"(tw[5]), "+s"(tw[6])
                               : "s"((const char*)wd + ((64 * chunk + 63 * mw) * D + 256 * ksp) * 2), "s"(wo + (j * 32 + mw * 16) * 2048 + (xcd >> 1) * 512) : "memory");
      if (TW & 4) asm volatile("s_load_dword %0, %1, 0" : "+s"(tw[7]) : "s"((const char*)out + (mb * 128 + mw * 127) * 2048 + nb * 128) : "memory");
    }
    PFB(spare ? SPS : SPW, 0);
    if (SP0) { WAITL(0); spf(pfb); }
    if (HPRIO) __builtin_amdgcn_s_setprio(HPRIO);
    TICK();
    if (spare) {
      TICK();
      pois();
      if (ii >> 2) TICK();
      colrd(ii >> 2);
      spf(pfb);
      if (!(ii >> 2)) TICK();
      u4 kk[8];
      auto kload = [&]() {
#pragma unroll
        for (int i = 0; i < 8; ++i) kk[i] = LD128(WS_RG + mw * 8192 + i * 1024 + l * 16);
      };
      kload();
      TICKP();
      _Pragma("unroll 1") for (unsigned it = 0; it < SPIN; ++it) {
        unsigned bad = 0;
#pragma unroll
        for (int i = 0; i < 8; ++i) { bad |= (kk[i][1] ^ tag) | (kk[i][3] ^ tag); asm volatile("" : "+v"(bad)); }
        if (__ballot(bad != 0u) == 0ull) break;
        RETRY();
        kload();
        DTICK(7);
      }
      __builtin_amdgcn_s_setprio(3);
      unsigned m1 = max(kk[0][0], kk[0][2]), m2 = min(kk[0][0], kk[0][2]), m3 = 0;
#pragma unroll
      for (int i = 2; i < 16; ++i) {
        const unsigned a = kk[i >> 1][(i & 1) * 2];
        asm("v_med3_u32 %0, %1, %2, %0" : "+v"(m3) : "v"(m2), "v"(a));
        asm("v_med3_u32 %0, %1, %2, %0" : "+v"(m2) : "v"(m1), "v"(a));
        m1 = max(m1, a);
      }
      unsigned mine8 = 0;
#pragma unroll
      for (int it = 0; it < 8; ++it) {
        const unsigned km = wave_max(m1);
        mine8 = l == it ? km : mine8;
        const bool win = m1 == km;
        if (it < 7) { m1 = win ? m2 : m1; m2 = win ? m3 : m2; m3 = win ? 0u : m3; }
        if (it == 3 && nbar < 7) TICK();
      }
      if (__builtin_expect(__ballot(m1 == 0u) != 0ull, 0)) {
        unsigned lim = 0;
#pragma unroll 1
        for (int it = 0; it < 8; ++it) {
          unsigned m = 0;
#pragma unroll
          for (int i = 0; i < 8; ++i) m = umax3(m, kk[i][0] - lim, kk[i][2] - lim);
          lim += wave_max(m);
          mine8 = l == it ? lim : mine8;
        }
      }
      ST64(((u2){mine8, tag}), l < 8 ? WS_CAND + ((hp >> 3) * 16 + mb) * 128 + (mw ? 7 - l : l) * 16 + mw * 8 : WS_SZ);
      __builtin_amdgcn_s_setprio(HPRIO);
      STMP(2);
      TICKP();
      colsum();
      DTICK(9);
      if (TCH == 8) { tearly(); if (!TS) tlate(); } else if (TCH && TCH != 3) touch();
      spf(pfb);
      while (nbar < 10) TICKQ();
      gload(); cload();
      if (RT && mw) asm volatile("global_load_dword %0, %1, %2 sc1" : "+v"(cw) : "v"(cwo), "s"(cwb) : "memory");
      TICKQ(); TICKP();
      WAITV(0);
      asm volatile("" : "+v"(cw));
      gfin(); cfin();
      STMP(3);
      TICK(); TICK();
      if (TCH == 8 && TS) tlate();
      if (SE) { lpub(false); asm volatile("ds_write_b32 %0, %1\n s_waitcnt lgkmcnt(0)" ::"v"(adFL), "v"((l & 6) == 6 ? 1u : 0u) : "memory"); } else ZFL();
      TICK();
      if (!SE) lpub();
      if (SSL) SLEEP(SSL);
      if (TCH == 8) asm volatile("" ::"v"(wt0), "v"(wt1));
    } else {
      if (PV == 0) pois();
      TICK();
      if (PV == 1) pois();
      if (ii >> 2) TICK();
      colrd(ii >> 2);
      if (TW) asm volatile("" ::"s"(tw[0]), "s"(tw[1]), "s"(tw[2]), "s"(tw[3]), "s"(tw[4]), "s"(tw[5]), "s"(tw[6]), "s"(tw[7]));
      spf(pfb);
      TICK();
      if (CL && !(ii >> 2)) TICK();
      colsum();
      spf(pfb);
      if (!CL && !(ii >> 2)) TICK();
      u4 sv; f4 acc[8];
      auto xload = [&]() {
        const u16* xp = x + (size_t)idx * D + 256 * ksp + 32 * kset + 8 * g4;
        sv = LD128(WS_ST + idx * 16);
#pragma unroll
        for (int it = 0; it < 4; ++it) xs[it] = *(const u4*)(xp + 64 * it);
        SBAR();
      };
      auto xnorm = [&]() {
        if (__builtin_expect(__ballot(sv[1] != tag || sv[3] != tag) != 0ull, 0)) {
          _Pragma("unroll 1") for (unsigned it = 0; it < SPIN; ++it) { SLEEP(1); sv = LD128(WS_ST + idx * 16); if (__ballot(sv[1] != tag || sv[3] != tag) == 0ull) break; }
        }
        const float nm = -__uint_as_float(sv[0]);
        xrs = __uint_as_float(sv[2]);
#pragma unroll
        for (int it = 0; it < 4; ++it) {
          float t0, t1, t2, t3; unsigned x0 = xs[it][0], x1 = xs[it][1], x2 = xs[it][2], x3 = xs[it][3];
          asm volatile("v_dot2_f32_bf16 %4, %0, %8, %10\n v_dot2_f32_bf16 %0, %0, %9, %10\n v_dot2_f32_bf16 %5, %1, %8, %10\n v_dot2_f32_bf16 %1, %1, %9, %10\n"
                       "v_dot2_f32_bf16 %6, %2, %8, %10\n v_dot2_f32_bf16 %2, %2, %9, %10\n v_dot2_f32_bf16 %7, %3, %8, %10\n v_dot2_f32_bf16 %3, %3, %9, %10\n"
                       "v_cvt_pk_bf16_f32 %0, %4, %0\n v_cvt_pk_bf16_f32 %1, %5, %1\n v_cvt_pk_bf16_f32 %2, %6, %2\n s_nop 0\n v_cvt_pk_bf16_f32 %3, %7, %3"
                       : "+v"(x0), "+v"(x1), "+v"(x2), "+v"(x3), "=&v"(t0), "=&v"(t1), "=&v"(t2), "=&v"(t3) : "s"(0x00003F80u), "s"(0x3F800000u), "v"(nm));
          xs[it] = (u4){x0, x1, x2, x3};
        }
        asm volatile("s_nop 1" : "+v"(xs[0]), "+v"(xs[1]), "+v"(xs[2]), "+v"(xs[3]), "+v"(xrs));
        SBAR();
      };
      auto gmul = [&](int i0, int i1) {
#pragma unroll
        for (int it = i0; it < i1; ++it)
#pragma unroll
          for (int c = 0; c < 8; ++c) acc[c] = MFMA16(S8(gw[it][c]), S8(xs[it]), it ? acc[c] : (f4)(0.f));
#pragma unroll
        for (int c = 0; c < 8; ++c) asm volatile("" : "+v"(acc[c]));
        SBAR();
      };
      auto gout = [&]() {
        unsigned go = r15 < 8 ? WS_GP + ((chg * 4 + ksp) * 2 + mw) * 2048 + (g4 >> 1) * 1024 + (g4 & 1) * 512 + r15 * 16 : WS_SZ;
        asm volatile("" : "+v"(go));
#pragma unroll
        for (int i = 0; i < 4; ++i) {
          unsigned a[4], b[4];
#pragma unroll
          for (int c = 0; c < 4; ++c) {
            auto r = __builtin_amdgcn_permlane32_swap(__float_as_uint(acc[c][i]), __float_as_uint(acc[c + 4][i]), false, false);
            a[c] = r[0]; b[c] = r[1];
          }
          unsigned s0, s1, s2, s3;
#define AR(d, x, y) "v_add_f32_dpp %" #d ", %" #x ", %" #y " row_ror:8 row_mask:0xf bank_mask:0xf\n"
          asm volatile("s_nop 1\n" AR(0, 8, 4) AR(1, 9, 5) AR(2, 10, 6) AR(3, 11, 7) : "=&v"(s0), "=&v"(s1), "=&v"(s2), "=&v"(s3)
                       : "v"(a[0]), "v"(a[1]), "v"(a[2]), "v"(a[3]), "v"(b[0]), "v"(b[1]), "v"(b[2]), "v"(b[3]));
          ST128(tagf(__builtin_bit_cast(f4, ((u4){s0, s1, s2, s3})), tag), go + i * 128);
        }
        SBAR();
      };
      for (int i = 0; i < ph; ++i) { spf(pfb); TICK(); }
      burst(0); if (TCH == 1) touch(); if (TCH == 7) touch(1); if (TCH == 8 && TE == 0) tearly();
      spf(pfb);
      TICKQ();
      TICKQ();
      if (TL) { transp(gw[0]); transp(gw[1]); }
      TICKP();
      if (!TL) transp(gw[0]);
      if (C2 && (mb >> 2) == (CB == 4 ? 2 * mw : CB)) { SBAR(); rload(); }
      if (SP2) spf(pfb);
      TICKP();
      if (!TL) transp(gw[1]);
      if (C2 && (mb >> 2) == (CB == 4 ? 2 * mw : CB)) { SBAR(); rfin(); }
      if (SP2) spf(pfb);
      TICK();
      if (ph) cload();
      if (GE == 1) gload();
      if (RT && mw) asm volatile("global_load_dword %0, %1, %2 sc1" : "+v"(cw) : "v"(cwo), "s"(cwb) : "memory");
      SBAR();
      burst(1); if (TCH == 4) touch(); if (TCH == 8 && TE == 1) tearly();
      spf(pfb);
      TICKQ();
      if (TCH == 5) touch();
      if (TCH == 7) touch(2);
      if (TCH == 8 && TE == 2) tearly();
      if (GE && !ph) cload();
      if (GE == 2) gload();
      TICK();
      if (ph < 2) spf(pfb);
      if (GE == 1) gfin();
      TICK();
      transp(gw[2]);
      WAITV(0);
      asm volatile("" : "+v"(cw));
      if (GE == 2) gfin();
      if (ph < 2) TICK();
      transp(gw[3]);
      if (GE) {
        if (ph < 1) TICK();
        cfin();
        if (GX) xload();
        if (GS) { lpub(false); asm volatile("ds_write_b32 %0, %1\n s_waitcnt lgkmcnt(0)" ::"v"(adFL), "v"((l & 6) == 6 ? 1u : 0u) : "memory"); } else ZFL();
        TICK();
        if (!GX) xload();
        if (TCH == 8) { tlate(); SBAR(); }
      } else {
        if (ph < 1) { SBAR(); cload(); TICK(); }
        SBAR();
        if (XO == 0) { cfin(); xload(); gload(); } else { gload(); SBAR(); cfin(); xload(); }
        if (TCH == 2) { touch(); SBAR(); }
        if (TCH == 8 && (TLP == 0 || (TLP == 3 && ph == 0))) { tlate(); SBAR(); }
        ZFL();
        TICK();
        if (TCH == 8 && (TLP == 1 || (TLP == 3 && ph != 0))) { tlate(); SBAR(); }
        if (DE) { burst(2, 0, DE); SBAR(); }
        gfin();
      }
      if (!(GE && GS)) lpub();
      STMP(12);
      SBAR();
      __builtin_amdgcn_s_setprio(0);
      if (TCH == 8 && TLP == 4) { tlate(); SBAR(); }
      if (HS1) SLEEP(HS1);
      if (TCH == 8 && TLP == 5) { tlate(); SBAR(); }
      burst(2, DE, D1);
      if (TCH == 8 && TLP == 2) tlate();
      STMP(3);
      SBAR();
      if (HS0) SLEEP(HS0);
      if (G1 & 1) { lwait<HW1>(adFL, 0x0Fu, G1 >> 4); if (G1S) SLEEP(G1S); __builtin_amdgcn_s_setprio(TP0); }
      xnorm();
      if (G1 & 2) { lwait<2>(adFL, 0x0Fu, G1 >> 4); if (G1S) SLEEP(G1S); __builtin_amdgcn_s_setprio(TP0); }
      STMP(5);
      gmul(0, 4);
      gout();
      if (TCH == 2 || TCH == 8) asm volatile("" ::"v"(wt0), "v"(wt1));
      STMP(6);
      SBAR();
    }
    __builtin_amdgcn_s_setprio(TP1);
    STMP(4);
    if (moe) {
      if (D2 == 0) { burst(2, D1, 16); SBAR(); }
      if (D1 >= 8 || D2 == 0) transp(dw[0]); if (D1 == 16 || D2 == 0) transp(dw[1]);
      SBAR();
      lwait<2>(adFL, 0x30u, 3);
      if (G2S) SLEEP(G2S);
      STMP(7);
      if (D2 == 1) { burst(2, D1, 16); SBAR(); }
      f4 acc[8];
      u4 ha[2];
      {
        u4 pg[4][2];
        auto pload = [&]() {
#pragma unroll
          for (int ks = 0; ks < 4; ++ks)
#pragma unroll
            for (int t = 0; t < 2; ++t) pg[ks][t] = LD128(WS_GP + ((chunk * 4 + ks) * 4 + 2 * t + mw) * 1024 + (4 * kset + g4) * 128 + np * 16);
        };
        auto pbad = [&]() {
          unsigned bad = 0;
#pragma unroll
          for (int i = 0; i < 8; ++i) untag(pg[i >> 1][i & 1], tag, bad);
          return __ballot((bad & 255u) != 0u) != 0ull;
        };
        pload();
        SBAR();
        if (D2 == 2) { burst(2, D1, 16); SBAR(); }
        __builtin_amdgcn_s_setprio(TP2);
        if (D1 < 8 && D2 == 1) transp(dw[0]);
        if (D1 < 16 && D2 == 1) transp(dw[1]);
        SBAR();
        if (__builtin_expect(pbad(), 0)) { _Pragma("unroll 1") for (unsigned it = 0; it < SPIN; ++it) { RETRY2(); SLEEP(4); pload(); if (!pbad()) break; } }
        STMP(10);
        f4 gq[2];
#pragma unroll
        for (int t = 0; t < 2; ++t)
          gq[t] = ((__builtin_bit_cast(f4, pg[0][t]) + __builtin_bit_cast(f4, pg[1][t])) + (__builtin_bit_cast(f4, pg[2][t]) + __builtin_bit_cast(f4, pg[3][t]))) * xrs;
        float hv[4];
#pragma unroll
        for (int e = 0; e < 4; ++e) {
          const float gg = rbf(gq[0][e]), uu = rbf(gq[1][e]);
          hv[e] = rbf(gg / (1.f + __expf(-gg))) * uu;
        }
        *(LAS u2*)(lds + L_HX + l * 16 + mw * 8) = (u2){pk2(hv[0], hv[1]), pk2(hv[2], hv[3])};
        lsig(myFL, 2u);
        if (D1 < 16 && D2 == 2) { SBAR(); if (D1 < 8) transp(dw[0]); transp(dw[1]); SBAR(); }
        lwait(adFL, 0xC0u, 2);
        ha[0] = *(const LAS u4*)(lds + L_HX + l * 16); ha[1] = *(const LAS u4*)(lds + L_HX + (l ^ 8) * 16);
      }
#pragma unroll
      for (int it = 0; it < 2; ++it)
#pragma unroll
        for (int c = 0; c < 8; ++c) acc[c] = MFMA16(S8(ha[it]), S8(dw[it][c]), it ? acc[c] : (f4)(0.f));
      if (g4 < 2) {
#pragma unroll
        for (int i = 0; i < 4; ++i) {
          const unsigned yo = WS_YP + ((chunk * 8 + 4 * g4 + i) * D + 256 * ksp + 128 * mw + 4 * r15) * 4;
          ST128(tagf((f4){acc[0][i], acc[1][i], acc[2][i], acc[3][i]}, tag), yo);
          ST128(tagf((f4){acc[4][i], acc[5][i], acc[6][i], acc[7][i]}, tag), yo + 256);
        }
      }
      STMP(11);
    }
    float sg;
    {
      const unsigned mono = cd[0] >> 16, rb = (mono & 0x8000u) ? (mono & 0x7FFFu) : (~mono & 0xFFFFu);
      sg = rbf(1.f / (1.f + __expf(-lo(rb))));
    }
    unsigned mine = (unsigned)__ballot(!NOMOE && l < 8 && (idx >> 7) == mb && (l & 1) == mw);
    if (mine) {
      if (spare) lwait<4>(adFL, 0x30u, 3);
      {
        const u2 f = LD64(WS_OH + (mb * 16 + r15) * 128);
        const unsigned t0 = NOW();
        unsigned rel = f[1] + YD - t0;
        rel = wave_max(f[0] == tag && rel < 600u ? rel : 0u);
        _Pragma("unroll 1") for (unsigned it = 0; it < SPIN; ++it) { if (NOW() - t0 >= rel) break; SLEEP(2); }
      }
      STMP(8);
      lwait<1>(adFL, 0x0Fu, 4);
      __builtin_amdgcn_s_setprio(TP2);
      while (mine) {
        const int t = __builtin_ctz(mine); mine &= mine - 1;
        u4 yv[14];
        _Pragma("unroll 1") for (unsigned it = 0; it < SPIN; ++it) {
          unsigned bad = 0;
#pragma unroll
          for (int i = 0; i < 14; ++i) yv[i] = LD128(WS_YP + (((i * 4 + g4) * 8 + t) * D + (nb >> 1) * 128 + (r15 >> 3) * 64 + (nb & 1) * 32 + (r15 & 7) * 4) * 4);
          SBAR();
#pragma unroll
          for (int i = 0; i < 14; ++i) untag(yv[i], tag, bad);
          if (__ballot((bad & 255u) != 0u) == 0ull) break;
          RETRY();
          SLEEP(YSL);
        }
        f4 y = __builtin_bit_cast(f4, yv[0]);
#pragma unroll
        for (int i = 1; i < 14; ++i) y += __builtin_bit_cast(f4, yv[i]);
        const float gt = __int_as_float(__builtin_amdgcn_readlane(__float_as_int(sg), t));
#pragma unroll
        for (int e = 0; e < 4; ++e) {
          auto r = __builtin_amdgcn_permlane32_swap(__float_as_uint(y[e]), __float_as_uint(y[e]), false, false);
          const float a = __uint_as_float(r[0]) + __uint_as_float(r[1]);
          auto r2 = __builtin_amdgcn_permlane16_swap(__float_as_uint(a), __float_as_uint(a), false, false);
          y[e] = rbf(rbf(__uint_as_float(r2[0]) + __uint_as_float(r2[1])) * gt);
        }
        if (l < 16) *(LAS f4*)(lds + L_KVX + t * 256 + (l & 7) * 32 + (l >> 3) * 16) = y;
      }
    }
    lsig(myFL, 3u);
    STMP(9);
    STFLUSH();
    if (spare) asm volatile(".Lend_s:"); else asm volatile(".Lend_h:");
    };
    if (sp) helper(BC<true>()); else helper(BC<false>());
    return;
  }

  const int wr = w >> 1, wc = w & 1;
  u2 kg; u4 sg4; unsigned tag;
  {
    const int row = srow;
    SBAR(); BST(0); SBAR();
    asm volatile("s_waitcnt vmcnt(4)" : "+v"(xa[0]), "+v"(xa[1]), "+v"(xa[2]), "+v"(xa[3]));
    SBAR(); BST(1); SBAR();
    float s0 = 0, s1 = 0;
#pragma unroll
    for (int c = 0; c < 4; ++c)
#pragma unroll
      for (int e = 0; e < 4; e += 2) { unsigned x0 = xa[c][e], x1 = xa[c][e + 1]; s0 = dot2(x0, 0x3F803F80u, s0); s1 = dot2(x1, 0x3F803F80u, s1); }
    const float m = half_sum(s0 + s1) * (1.f / D);
    f2 v[16], q2 = (f2)(0.f), q3 = (f2)(0.f);
#pragma unroll
    for (int c = 0; c < 16; ++c) v[c] = (f2){lo(xa[c >> 2][c & 3]), hi(xa[c >> 2][c & 3])} - m;
#pragma unroll
    for (int c = 0; c < 16; c += 2) { q2 += v[c] * v[c]; q3 += v[c + 1] * v[c + 1]; }
    q2 += q3;
    const float mr = rbf(m);
    const float rs = 1.f / rbf(sqrtf(rbf(rbf(half_sum(q2[0] + q2[1]) * (1.f / D)) + 1e-5f)));
    float d0 = 0, d1 = 0;
    asm volatile("s_waitcnt vmcnt(0)" : "+v"(rw[0]), "+v"(rw[1]), "+v"(rw[2]), "+v"(rw[3]), "+v"(d0) : "v"(rs));
#pragma unroll
    for (int c = 0; c < 16; c += 2) {
      const f2 e0 = (f2){lo(xa[c >> 2][c & 3]), hi(xa[c >> 2][c & 3])} - mr, e1 = (f2){lo(xa[c >> 2][(c & 3) + 1]), hi(xa[c >> 2][(c & 3) + 1])} - mr;
      const unsigned p0 = pk2(e0[0], e0[1]), p1 = pk2(e1[0], e1[1]);
      const f2 n0 = (f2){lo(p0), hi(p0)} * rs, n1 = (f2){lo(p1), hi(p1)} * rs;
      unsigned c0 = rw[c >> 2][c & 3], c1 = rw[c >> 2][(c & 3) + 1];
      d0 = dot2(pk2(n0[0], n0[1]), c0, d0); d1 = dot2(pk2(n1[0], n1[1]), c1, d1);
    }
    const float d = half_sum(d0 + d1);
    const unsigned rb = pk2(d, d) & 0xFFFFu;
    const unsigned mono = (rb & 0x8000u) ? (~rb & 0xFFFFu) : (rb | 0x8000u);
    SBAR();
    GETTAG(tg); tag = tg;
    kg = (u2){(mono << 16) | (unsigned)row, tag};
    sg4 = (u4){__float_as_uint(mr), tag, __float_as_uint(rs), tag};
  }
  MKWS(tag);
  SBAR(); BST(2); SBAR();
  if ((l & 31) == 0) { ST64(kg, WS_RG + srow * 8); ST128(sg4, WS_ST + srow * 16); }
  unsigned aad[2], bad[2];
#pragma unroll
  for (int s = 0; s < 2; ++s) aad[s] = ldsb + (wr * 64 + r15) * 128 + (((4 * s + g4) ^ ((r15 >> 1) & 7)) * 16);
#pragma unroll
  for (int nh = 0; nh < 2; ++nh) bad[nh] = ldsb + 16384 + (8 * g4 + q) * 128 + (((wc * 4 + 2 * (nh ^ wr) + (p >> 1)) ^ (((q >> 1) << 2) | ((g4 & 1) << 1))) * 16) + (p & 1) * 8;
  f4 acc[4][6];
#pragma unroll
  for (int m = 0; m < 4; ++m)
#pragma unroll
    for (int n = 0; n < 6; ++n) { acc[m][n] = (f4)(0.f); asm volatile("" : "+v"(acc[m][n])); }
#define MF(m, n, Fq) do { if (n < 2) acc[m][n] = MFMA16(CAT(Fq.b[2 * n], Fq.b[2 * n + 1]), Fq.a[m], acc[m][n]); \
     else acc[m][n] = MFMA16(Fq.a[m], CAT(Fq.b[2 * n], Fq.b[2 * n + 1]), acc[m][n]); SBAR(); } while (0)
#define RB(G, n, vb0, vb1, SS, hf) RDTR(G.b[2 * (n) + (hf)], (((n) & 1) ? vb1 : vb0), ((n) >> 1) * 8192 + (SS) * 4096 + (hf) * 512)
#define RA(G, m, va) RD128(G.a[m], va, (m) * 2048)
#define SUB(Fq, G, va, vb0, vb1, SS, PRE) do { \
    WAITL(13); SBAR(); \
    MF(0, 0, Fq); PRE; RA(G, 0, va); SBAR(); \
    WAITL(11); SBAR(); \
    MF(1, 0, Fq); RB(G, 0, vb0, vb1, SS, 0); SBAR(); MF(2, 0, Fq); RB(G, 0, vb0, vb1, SS, 1); SBAR(); MF(3, 0, Fq); RA(G, 1, va); SBAR(); \
    WAITL(12); SBAR(); \
    MF(0, 1, Fq); RA(G, 2, va); SBAR(); MF(1, 1, Fq); RA(G, 3, va); SBAR(); MF(2, 1, Fq); RB(G, 1, vb0, vb1, SS, 0); SBAR(); MF(3, 1, Fq); RB(G, 1, vb0, vb1, SS, 1); SBAR(); \
    WAITL(8); SBAR(); \
    MF(0, 2, Fq); RB(G, 2, vb0, vb1, SS, 0); SBAR(); MF(1, 2, Fq); RB(G, 2, vb0, vb1, SS, 1); SBAR(); MF(2, 2, Fq); RB(G, 3, vb0, vb1, SS, 0); SBAR(); MF(3, 2, Fq); RB(G, 3, vb0, vb1, SS, 1); SBAR(); \
    MF(0, 3, Fq); RB(G, 4, vb0, vb1, SS, 0); SBAR(); MF(1, 3, Fq); RB(G, 4, vb0, vb1, SS, 1); SBAR(); MF(2, 3, Fq); RB(G, 5, vb0, vb1, SS, 0); SBAR(); MF(3, 3, Fq); RB(G, 5, vb0, vb1, SS, 1); SBAR(); \
    MF(0, 4, Fq); MF(1, 4, Fq); MF(2, 4, Fq); MF(3, 4, Fq); MF(0, 5, Fq); MF(1, 5, Fq); MF(2, 5, Fq); MF(3, 5, Fq); } while (0)
#define KSTEP(MODE) do { \
    if (kt == sx) { const unsigned xo = xad + buf * STG; \
      _Pragma("unroll") for (int t = 0; t < 8; ++t) { const unsigned a_ = xo ^ (t * 16); \
        asm volatile("ds_read_b64 %0, %1" : "+v"(xv[t]) : "v"(a_) : "memory"); } \
      WAITL(0); SBAR(); } \
    const int nbuf = (buf + 1) & 3; \
    const unsigned bo = buf * STG, nbo = nbuf * STG; \
    { unsigned va = aad[1] + bo, vb0 = bad[0] + bo, vb1 = bad[1] + bo; SUB(f0, f1, va, vb0, vb1, 1, ); } \
    if (MODE < 2) { \
      unsigned va = aad[0] + nbo, vb0 = bad[0] + nbo, vb1 = bad[1] + nbo; \
      SUB(f1, f0, va, vb0, vb1, 0, if (MODE == 0) { WAITL(0); BAR(); } else asm volatile("ds_write_b32 %0, %1" ::"v"(myFL), "v"(1u) : "memory"); SBAR()); \
    } else { asm volatile("ds_write_b32 %1, %2\n ds_read_b32 %0, %3\n s_waitcnt lgkmcnt(2)" : "=&v"(fl0) : "v"(myFL), "v"(2u), "v"(adFL) : "memory"); SBAR(); \
      MF(0, 2, f1); MF(1, 2, f1); MF(2, 2, f1); MF(3, 2, f1); MF(0, 3, f1); MF(1, 3, f1); MF(2, 3, f1); MF(3, 3, f1); \
      MF(0, 4, f1); MF(1, 4, f1); MF(2, 4, f1); MF(3, 4, f1); MF(0, 5, f1); MF(1, 5, f1); MF(2, 5, f1); MF(3, 5, f1); \
      MF(0, 0, f1); MF(1, 0, f1); MF(2, 0, f1); MF(3, 0, f1); MF(0, 1, f1); MF(1, 1, f1); MF(2, 1, f1); MF(3, 1, f1); } \
    buf = nbuf; } while (0)
#define SUBK(Fq, G, va, vb0, vb1, SS, W0, X, PRE, POST) do { \
    WAITL(W0); SBAR(); \
    MF(0, 2, Fq); PRE; RA(G, 0, va); SBAR(); \
    WAITL(7 + (X)); SBAR(); \
    MF(1, 2, Fq); RB(G, 2, vb0, vb1, SS, 0); SBAR(); MF(2, 2, Fq); RB(G, 2, vb0, vb1, SS, 1); SBAR(); MF(3, 2, Fq); RA(G, 1, va); SBAR(); \
    WAITL(8 + (X)); SBAR(); \
    MF(0, 3, Fq); RA(G, 2, va); SBAR(); MF(1, 3, Fq); RA(G, 3, va); SBAR(); MF(2, 3, Fq); RB(G, 3, vb0, vb1, SS, 0); SBAR(); MF(3, 3, Fq); RB(G, 3, vb0, vb1, SS, 1); SBAR(); \
    WAITL(10 + (X)); SBAR(); \
    MF(0, 4, Fq); RB(G, 4, vb0, vb1, SS, 0); SBAR(); MF(1, 4, Fq); RB(G, 4, vb0, vb1, SS, 1); SBAR(); MF(2, 4, Fq); RB(G, 5, vb0, vb1, SS, 0); SBAR(); MF(3, 4, Fq); RB(G, 5, vb0, vb1, SS, 1); SBAR(); \
    WAITL(12); SBAR(); \
    MF(0, 5, Fq); POST; MF(1, 5, Fq); MF(2, 5, Fq); MF(3, 5, Fq); } while (0)
#define RQ(G, va, vb0, vb1, SS) do { RA(G, 0, va); RB(G, 0, vb0, vb1, SS, 0); RB(G, 0, vb0, vb1, SS, 1); RA(G, 1, va); RA(G, 2, va); RA(G, 3, va); \
    RB(G, 1, vb0, vb1, SS, 0); RB(G, 1, vb0, vb1, SS, 1); SBAR(); } while (0)
#define MQ(Fq) do { MF(0, 0, Fq); MF(1, 0, Fq); MF(2, 0, Fq); MF(3, 0, Fq); MF(0, 1, Fq); MF(1, 1, Fq); MF(2, 1, Fq); MF(3, 1, Fq); } while (0)
  u2 xv[8];
#pragma unroll
  for (int t = 0; t < 8; ++t) asm volatile("" : "=v"(xv[t]));
  const int sx = (nb - rot) & 15;
  const unsigned xad = ldsb + (w * 32 + (l & 31)) * 128 + ((r15 >> 1) & 7) * 16 + (l >> 5) * 8;
  STMP(1);
  WAITL(0);
  BAR();
  STMP(2);
  WAITL(0);
  FQ f0, f1;
  { unsigned va = aad[0], vb0 = bad[0], vb1 = bad[1];
    RA(f0, 0, va); RB(f0, 0, vb0, vb1, 0, 0); RB(f0, 0, vb0, vb1, 0, 1); RA(f0, 1, va); RA(f0, 2, va); RA(f0, 3, va);
    RB(f0, 1, vb0, vb1, 0, 0); RB(f0, 1, vb0, vb1, 0, 1); RB(f0, 2, vb0, vb1, 0, 0); RB(f0, 2, vb0, vb1, 0, 1); RB(f0, 3, vb0, vb1, 0, 0); RB(f0, 3, vb0, vb1, 0, 1);
    RB(f0, 4, vb0, vb1, 0, 0); RB(f0, 4, vb0, vb1, 0, 1); RB(f0, 5, vb0, vb1, 0, 0); RB(f0, 5, vb0, vb1, 0, 1); SBAR(); }
  unsigned sad = ldsb + L_M + wr * 256 + g4 * 16, cad = ldsb + L_CS + wc * 384 + r15 * 4 + wr * 64;
  const int hd = hp * 2 + wc;
  unsigned kvo = WS_KVP + (hd * 16 + mb) * 2048 + wr * 1024 + l * 16, zo = r15 == 0 ? WS_ZP + (hd * 16 + mb) * 128 + g4 * 32 + wr * 16 : WS_SZ;
  unsigned xw = ldsb + L_KVX + w * 3072 + l * 16, xr = ldsb + L_KVX + (w ^ 2) * 3072 + l * 16;
  asm volatile("s_waitcnt lgkmcnt(0)" : "+v"(sad), "+v"(cad), "+v"(kvo), "+v"(zo), "+v"(xw), "+v"(xr) : : "memory"); SBAR();
  int buf = 0; unsigned fl0;
#pragma unroll 1
  for (int kt = 0; kt < 14; ++kt) KSTEP(0);
  STMP(14); SBAR();
  f4 sm[4], sr[4], sl[4]; f2 ck, cv;
  unsigned c2 = 2u, c3 = 3u, vfl = myFL, b30, b31;
  u4 ones = (u4)(0x3F803F80u);
#define SC0() asm volatile("ds_read_b32 %0, %2 offset:128\n ds_read_b32 %1, %2 offset:256" : "=&v"(ck[0]), "=&v"(cv[0]) : "v"(cad) : "memory")
#define SC1() asm volatile("ds_read_b32 %0, %2 offset:128\n ds_read_b32 %1, %2 offset:256" : "=&v"(ck[1]), "=&v"(cv[1]) : "v"(cad ^ 64) : "memory")
  const unsigned b20 = bad[0] + 2 * STG, b21 = bad[1] + 2 * STG, a21 = aad[1] + 2 * STG;
  unsigned a20, a30, a31, pfl2, lm, lc, ld, pw0, pw1, pws = ldsb + L_PQ + wr * 8200 + wc * 4096; int sxl = sx;
#define SM(m) do { RD128(sm[m], sad, (m) * 64); RD128(sl[m], sad, L_RL - L_M + (m) * 64); RD128(sr[m], sad, L_RS - L_M + (m) * 64); } while (0)
  {
    { const unsigned va = a21; SUBK(f0, f1, va, b20, b21, 1, 6, 0, asm volatile("" ::"v"(f0.b[0]), "v"(f0.b[1]), "v"(f0.b[2]), "v"(f0.b[3])), ); }
    { const unsigned va = a30 = aad[0] + 3 * STG; b30 = bad[0] + 3 * STG; b31 = bad[1] + 3 * STG;
      unsigned ft, late = 0;
      SUBK(f1, f0, va, b30, b31, 0, 9, LF, asm volatile("" : "+v"(c2), "+v"(c3), "+v"(vfl)); if (LF) asm volatile("ds_read_b32 %0, %1" : "=v"(ft) : "v"(adFL) : "memory"),
           if (LF) { asm volatile("" : "+v"(ft)); late = (unsigned)__ballot(ft < 1u) & 0x30u; asm volatile("" : "+s"(late)); SBAR(); });
      if (__builtin_expect(late != 0u, 0)) {
        RETRY2();
        lwait(adFL, 0x30u, 1);
#define RAP(G, m, va) asm volatile("ds_read_b128 %0, %1 offset:%2" : "+v"(G.a[m]) : "v"(va), "i"((m) * 2048) : "memory")
#define RBP(G, n, hf) asm volatile("ds_read_b64_tr_b16 %0, %1 offset:%2" : "+v"(G.b[2 * (n) + (hf)]) : "v"(((n) & 1) ? b31 : b30), "i"(((n) >> 1) * 8192 + (hf) * 512) : "memory")
        RAP(f0, 0, va); RBP(f0, 2, 0); RBP(f0, 2, 1); RAP(f0, 1, va); RAP(f0, 2, va); RAP(f0, 3, va);
        RBP(f0, 3, 0); RBP(f0, 3, 1); RBP(f0, 4, 0); RBP(f0, 4, 1); RBP(f0, 5, 0); RBP(f0, 5, 1);
      }
      SBAR();
    }
    STMP(7); SBAR();
    { const unsigned va = a31 = aad[1] + 3 * STG; SUBK(f0, f1, va, b30, b31, 1, 9, 0, asm volatile("" : "+v"(ones)), a20 = aad[0] + 2 * STG; pfl2 = vfl ^ 8; asm volatile("" : "+v"(a20), "+v"(pfl2), "+s"(pws), "+s"(sxl)); SBAR()); }
    asm volatile("ds_write_b32 %1, %2\n ds_read_b32 %0, %3\n s_waitcnt lgkmcnt(8)" : "=&v"(fl0) : "v"(vfl), "v"(c2), "v"(adFL) : "memory"); SBAR();
    MF(0, 2, f1); SC0(); SBAR(); MF(1, 2, f1); SC1(); SBAR(); MF(2, 2, f1); MF(3, 2, f1); WAITL(10); SBAR();
    MF(0, 3, f1); SM(0); SBAR(); MF(1, 3, f1); MF(2, 3, f1); SM(1); SBAR(); MF(3, 3, f1);
    WAITL(12); SBAR();
    MF(0, 4, f1); SM(2); SBAR(); MF(1, 4, f1); MF(2, 4, f1); SM(3); SBAR(); MF(3, 4, f1); MF(0, 5, f1); MF(1, 5, f1); MF(2, 5, f1); MF(3, 5, f1);
  }
  asm volatile("s_waitcnt lgkmcnt(0)" : "+v"(fl0) : : "memory");
  if (__builtin_expect(((unsigned)__ballot(fl0 < 1u) & 0xC0u) != 0u, 0)) {
    lwait(adFL, 0xC0u, 1);
    SC0(); SC1(); SM(0); SM(1); SM(2); SM(3); WAITL(0);
  }
  asm volatile("" : "+v"(ck), "+v"(cv), "+v"(sm[0]), "+v"(sm[1]), "+v"(sm[2]), "+v"(sm[3]), "+v"(sr[0]), "+v"(sr[1]), "+v"(sr[2]), "+v"(sr[3]));
  asm volatile("" : "+v"(sl[0]), "+v"(sl[1]), "+v"(sl[2]), "+v"(sl[3]));
  STMP(3);
  __builtin_amdgcn_s_setprio(2);
  SBAR(); AL8(); SBAR();
  {
    f4 kv[4], zt[2];
    u4 kf[2][2], vf[2][2];
#define MKV(t, i) do { if ((i) == 2 || (i) == 5) zt[(i) == 2] = MFMA16(S8(kf[t][(i) == 2]), S8(ones), (t) ? zt[(i) == 2] : (f4)(0.f)); \
      else kv[((i) + 2) % 5] = MFMA16(S8(kf[t][(((i) + 2) % 5) & 1]), S8(vf[t][(((i) + 2) % 5) >> 1]), (t) ? kv[((i) + 2) % 5] : (f4)(0.f)); SBAR(); } while (0)
#pragma unroll
    for (int m = 0; m < 4; ++m) {
      const int t = m >> 1, mm = m & 1;
      f4 ea[2], pp[2];
#pragma unroll
      for (int a = 0; a < 2; ++a) {
        const f4 uv = (acc[m][4 + a] + sm[m] * cv[a]) * sr[m];
        vf[t][a][2 * mm] = pk2(uv[0], uv[1]); vf[t][a][2 * mm + 1] = pk2(uv[2], uv[3]);
        const f4 uk = acc[m][2 + a] + sm[m] * ck[a];
        ea[a] = uk * sl[m]; pp[a] = uk * sr[m] + 1.f;
      }
      SBAR();
#pragma unroll
      for (int e = 0; e < 8; ++e) { if (t && (e == 0 || e == 3 || e == 6)) MKV(0, 3 * mm + e / 3); ea[e >> 2][e & 3] = __builtin_amdgcn_exp2f(ea[e >> 2][e & 3]); SBAR(); }
      AL8(); SBAR();
#pragma unroll
      for (int e = 0; e < 8; ++e) pp[e >> 2][e & 3] = __builtin_amdgcn_fmed3f(ea[e >> 2][e & 3], pp[e >> 2][e & 3], 1.f);
      SBAR();
#pragma unroll
      for (int a = 0; a < 2; ++a) { pp[a] += 1e-6f; kf[t][a][2 * mm] = pk2(pp[a][0], pp[a][1]); kf[t][a][2 * mm + 1] = pk2(pp[a][2], pp[a][3]); }
      SBAR();
    }
    MKV(1, 0); MKV(1, 1); MKV(1, 2); MKV(1, 3);
    asm volatile("ds_write_b128 %0, %1" ::"v"(xw), "v"(kv[2]) : "memory"); SBAR();
    MKV(1, 4);
    asm volatile("ds_write_b128 %0, %1 offset:1024" ::"v"(xw), "v"(kv[3]) : "memory"); SBAR();
    MKV(1, 5);
    asm volatile("ds_write_b128 %0, %1 offset:2048\n ds_write_b32 %2, %3" ::"v"(xw), "v"(zt[1]), "v"(vfl), "v"(c3) : "memory");
    f4 r0, r1, rz; unsigned v;
    asm volatile("ds_read_b32 %0, %4\n ds_read_b128 %1, %5 offset:1024\n ds_read_b128 %2, %5\n ds_read_b128 %3, %5 offset:2048"
                 : "=&v"(v), "=&v"(r0), "=&v"(r1), "=&v"(rz) : "v"(pfl2), "v"(xr) : "memory");
    RQ(f0, a20, b20, b21, 0);
    lm = ldsb + L_M + wr * 256 + r15 * 4; lc = ldsb + L_CS + wc * 384 + wr * 64 + g4 * 16; pw0 = pws + l * 16; pw1 = pw0 ^ 8; ld = lc ^ 64;
    asm volatile("" : "+v"(lm), "+v"(lc), "+v"(pw0), "+v"(pw1), "+v"(ld)); SBAR();
    unsigned nx;
    asm volatile("s_mov_b32 %4, 0x200000\n s_branch .Lxw\n .p2align 3\n s_nop 0\n.Lxl:\n ds_read_b32 %0, %5\n ds_read_b128 %1, %6 offset:1024\n ds_read_b128 %2, %6\n ds_read_b128 %3, %6 offset:2048\n"
                 "s_sub_u32 %4, %4, 1\n s_waitcnt lgkmcnt(0)\n s_cbranch_scc1 .Lxd\n.Lxw:\n s_waitcnt lgkmcnt(8)\n v_cmp_gt_u32 vcc, 3, %0\n s_cbranch_vccnz .Lxl\n.Lxd:"
                 : "+v"(v), "+v"(r0), "+v"(r1), "+v"(rz), "=&s"(nx) : "v"(pfl2), "v"(xr) : "memory", "scc", "vcc");
    const f4 s0 = kv[0] + r0, s1 = kv[1] + r1, sz = zt[0] + rz;
    if (ZF) { ST128(__builtin_bit_cast(u4, sz), zo); SBAR(); }
    u4 o; o[0] = pk2(s0[0], s0[1]); o[1] = pk2(s0[2], s0[3]); o[2] = pk2(s1[0], s1[1]); o[3] = pk2(s1[2], s1[3]);
    ST128(o, kvo);
    if (!ZF) ST128(__builtin_bit_cast(u4, sz), zo);
  }
  STMP(4);
  {
    SBAR();
#define STQ(Fq, G, va, vb0, vb1, SS, POST) do { WAITL(2); SBAR(); \
      MF(0, 0, Fq); RA(G, 0, va); RB(G, 0, vb0, vb1, SS, 0); SBAR(); MF(1, 0, Fq); RB(G, 0, vb0, vb1, SS, 1); RA(G, 1, va); SBAR(); \
      MF(2, 0, Fq); RA(G, 2, va); RA(G, 3, va); SBAR(); MF(3, 0, Fq); RB(G, 1, vb0, vb1, SS, 0); RB(G, 1, vb0, vb1, SS, 1); WAITL(8); SBAR(); \
      MF(0, 1, Fq); POST; SBAR(); MF(1, 1, Fq); MF(2, 1, Fq); MF(3, 1, Fq); } while (0)
    unsigned c4 = 4u;
    STQ(f0, f1, a21, b20, b21, 1, );
    STQ(f1, f0, a30, b30, b31, 0, );
    unsigned lr;
    STQ(f0, f1, a31, b30, b31, 1, lr = lm + (L_RL - L_M); asm volatile("" : "+v"(lr)));
    f4 c0, c1; f2 mv[2], rv[2], rl[2];
#define RD2(d, a, o) asm volatile("ds_read2_b32 %0, %1 offset0:%2 offset1:%3" : "=v"(d) : "v"(a), "i"(o), "i"((o) + 16) : "memory")
    WAITL(2); SBAR();
    MF(0, 0, f1); RD128(c0, lc, 0); RD128(c1, ld, 0); SBAR(); MF(1, 0, f1); RD2(mv[0], lm, 0); RD2(rl[0], lr, 0); SBAR();
    MF(2, 0, f1); RD2(rv[0], lm, 128); RD2(mv[1], lm, 32); SBAR(); MF(3, 0, f1); RD2(rl[1], lr, 32); RD2(rv[1], lm, 160); WAITL(8); SBAR();
    MF(0, 1, f1); asm volatile("" : "+v"(c4)); SBAR(); MF(1, 1, f1); MF(2, 1, f1); MF(3, 1, f1);
    asm volatile("s_waitcnt lgkmcnt(0)" : "+v"(c0), "+v"(c1), "+v"(mv[0]), "+v"(mv[1]), "+v"(rv[0]), "+v"(rv[1]), "+v"(rl[0]), "+v"(rl[1]) : : "memory");
    if (__builtin_expect(__builtin_amdgcn_readfirstlane(sxl) >= 14, 0)) {
      const unsigned xo = xad + (sxl - 12) * STG;
#pragma unroll
      for (int t = 0; t < 8; ++t) { const unsigned a_ = xo ^ (t * 16); asm volatile("ds_read_b64 %0, %1" : "+v"(xv[t]) : "v"(a_) : "memory"); }
    }
    SBAR(); AL8(); SBAR();
    u2 ph[2][2];
#pragma unroll
    for (int m = 0; m < 4; ++m) {
      f4 ea[2], pp[2];
#pragma unroll
      for (int a = 0; a < 2; ++a) {
        const f4 u = acc[m][a] + (a ? c1 : c0) * mv[m >> 1][m & 1];
        ea[a] = u * rl[m >> 1][m & 1]; pp[a] = u * rv[m >> 1][m & 1] + 1.f;
      }
      SBAR();
#pragma unroll
      for (int e = 0; e < 8; ++e) ea[e >> 2][e & 3] = __builtin_amdgcn_exp2f(ea[e >> 2][e & 3]);
      asm volatile(".p2align 3" ::"v"(ea[0]), "v"(ea[1][0]), "v"(ea[1][1]), "v"(ea[1][2]), "v"(pp[0]), "v"(pp[1])); SBAR();
#pragma unroll
      for (int e = 0; e < 8; ++e) pp[e >> 2][e & 3] = __builtin_amdgcn_fmed3f(ea[e >> 2][e & 3], pp[e >> 2][e & 3], 1.f);
      asm volatile("" ::"v"(pp[0]), "v"(pp[1])); SBAR();
      pp[0] += 1e-6f; pp[1] += 1e-6f;

#pragma unroll
      for (int a = 0; a < 2; ++a) ph[a][m & 1] = (u2){pk2(pp[a][0], pp[a][1]), pk2(pp[a][2], pp[a][3])};
      SBAR();
      if (m & 1) asm volatile("ds_write2st64_b64 %0, %2, %3 offset0:%6 offset1:%7\n ds_write2st64_b64 %1, %4, %5 offset0:%6 offset1:%7"
                              ::"v"(pw0), "v"(pw1), "v"(ph[0][0]), "v"(ph[0][1]), "v"(ph[1][0]), "v"(ph[1][1]), "i"(2 * m - 2), "i"(2 * m) : "memory");
      SBAR();
    }
    asm volatile("ds_write_b32 %8, %9\n s_waitcnt lgkmcnt(0)" : "+v"(xv[0]), "+v"(xv[1]), "+v"(xv[2]), "+v"(xv[3]), "+v"(xv[4]), "+v"(xv[5]), "+v"(xv[6]), "+v"(xv[7]) : "v"(vfl), "v"(c4) : "memory");
  }
  __builtin_amdgcn_s_setprio(0);
  STMP(5);
  {
  int l;
  asm volatile("v_mbcnt_lo_u32_b32 %0, -1, 0\n v_mbcnt_hi_u32_b32 %0, -1, %0" : "=v"(l));
  __builtin_assume(l >= 0 && l < 64);
  const int r15 = l & 15, g4 = l >> 4, q = (l & 15) >> 2, p = l & 3;
  const int trow = mb * 128 + w * 32 + (l & 31);
  u2 oh;
  const unsigned long long ptab = mktab();
  const unsigned flo = PAIRAT(r15) * 128;
  const char* fb = uptr(wsx + WS_OH + mb * 2048);
  unsigned vfw[4];
#pragma unroll
  for (int i = 0; i < 4; ++i) { vfw[i] = VFW(i); asm volatile("" : "+v"(vfw[i])); }
  auto wo_part = [&](int s0, int s1) {
#pragma unroll
    for (int i = s0 / 2; i < s1 / 2; i += 2) {
      const int pp = wr ? wtat(i + 1) : wtat(i);
      const char* wp = (const char*)wo + (nb * 128 - 3072) + (2 * PAIRAT(pp) + wc) * 65536; const unsigned wm = ldsb + (2 * pp + wc) * 4096;
      GL4(vfw, wp, wm, L_WO);
    }
  };
  if (WF) {
  lwait<LW3>(adFL, 0x3Fu, (l & 4) ? 3u : 4u);
  asm volatile("global_load_dwordx2 %0, %1, %2 sc1" : "=&v"(oh) : "v"(flo), "s"(fb) : "memory");
  wo_part(WE, WB);
  SBAR();
  }
  int sel = -1;
  {
    const u4 s0 = *(const LAS u4*)(lds + L_SEL), s1 = *(const LAS u4*)(lds + L_SEL + 16);
#pragma unroll
    for (int i = 0; i < 8; ++i) { const int v = (int)(i < 4 ? s0[i & 3] : s1[i & 3]); sel = v == trow ? i : sel; }
  }
  const bool anysel = __ballot(sel >= 0) != 0ull;
  f16v oc[2][2];
  {
    const float m = *(const LAS float*)(lds + L_M + (w * 32 + (l & 31)) * 4), rs = *(const LAS float*)(lds + L_RS + (w * 32 + (l & 31)) * 4);
#pragma unroll
    for (int t = 0; t < 8; ++t) {
      const f4 xf = (f4){lo(xv[t][0]), hi(xv[t][0]), lo(xv[t][1]), hi(xv[t][1])};
      const f4 y = (xf - m) * rs + xf;
#pragma unroll
      for (int i = 0; i < 4; ++i) oc[0][t >> 2][(t & 3) * 4 + i] = y[i];
    }
    asm volatile("" : "+v"(oc[0][0]), "+v"(oc[0][1]));
  }
  unsigned wa[2];
#pragma unroll
  for (int t = 0; t < 2; ++t) wa[t] = ldsb + L_WO + wc * 4096 + (4 * (g4 >> 1) + q) * 128 + (((4 * t + 2 * (g4 & 1) + (p >> 1)) ^ ((q >> 1) << 2)) * 16) + (p & 1) * 8;
  u4 a[RD][4];
#pragma unroll
  for (int k = 0; k < RD; ++k) asm volatile("" : "=v"(a[k][0]), "=v"(a[k][1]), "=v"(a[k][2]), "=v"(a[k][3]));
  const char* ob = uptr(wsx + WS_OF + mb * 262144 + wc * 8192 + wr * 4096);
  const char* ob2 = ob + (8192 - wc * 16384);
  const unsigned ov0 = l * 16 + wc * 2048, ov1 = ov0 ^ 2048;
  unsigned xw = ldsb + L_WO + (4 * wr + wc) * 4096 + l * 16, xr = xw ^ 4096, mf1 = myFL, pf1 = adFL, c7 = 7u, ta[8];
  unsigned ya = ldsb + L_KVX + max(sel, 0) * 256 + (l >> 5) * 16, as = anysel, thr = (l & 7) == (w ^ 1) ? 7u : (l & 7) >= 6 && anysel ? 3u : 0u;
  asm volatile("" : "+v"(ya), "+v"(thr)); as = __builtin_amdgcn_readfirstlane(as); asm volatile("" : "+s"(as));
  unsigned tr = ldsb + L_PQ + w * 4096 + (l >> 3) * 128 + ((l ^ (l >> 3)) & 7) * 16, oo = (mb * 128 + w * 32 + (l >> 3)) * 2048 + nb * 128 + (l & 7) * 16;
#pragma unroll
  for (int t = 0; t < 8; ++t) { ta[t] = ldsb + L_PQ + w * 4096 + (l & 31) * 128 + ((t ^ (l & 7)) * 16) + (l >> 5) * 8; asm volatile("" : "+v"(ta[t])); }
  asm volatile("" : "+v"(xw), "+v"(xr), "+v"(mf1), "+v"(pf1), "+v"(c7), "+v"(tr), "+v"(oo));
  STMP(6);
  if (!WF) {
  lwait<2>(adFL, 0x3Fu, (l & 4) ? 3u : 4u);
  asm volatile("global_load_dwordx2 %0, %1, %2 sc1" : "=&v"(oh) : "v"(flo), "s"(fb) : "memory");
  wo_part(WE, WB);
  }
  WAITV(WB - WE);
  asm volatile("" : "+v"(oh));
  unsigned seen = (unsigned)__ballot(oh[0] == tag), go = 0;
  auto need = [&](unsigned mask) {
    if (__builtin_expect((go & mask) != mask, 0)) {
      RETRY2();
      _Pragma("unroll 1") for (unsigned it = 0; it < SPIN; ++it) {
        if ((seen & mask) != mask) {
          asm volatile("global_load_dwordx2 %0, %1, %2 sc1\n s_waitcnt vmcnt(0)" : "+v"(oh) : "v"(flo), "s"(fb) : "memory");
          seen = (unsigned)__ballot(oh[0] == tag);
        }
        go = seen & (unsigned)__ballot(oh[1] - NOW() - 1u >= 1000u);
        if ((go & mask) == mask) break;
        SLEEP(1);
      }
    }
  };
#define OX 0
#define OSC "sc1"
#define OVA(pos, i) (((i) & 1) && (pos) < 14 ? ov1 : ov0)
#define OBA(pos, i) ((((i) & 1) && (pos) >= 14 ? ob2 : ob) + PAIRAT(pos) * 16384)
#define OL1(pos, i) asm volatile("global_load_dwordx4 %0, %1, %2 offset:%3 sc1" : "+v"(a[(pos) % RD][i]) : "v"(OVA(pos, i)), "s"(OBA(pos, i)), "i"(((i) >> 1) * 1024) : "memory")
#define OLR(pos) do { _Pragma("unroll") for (int i_ = 0; i_ < 4; ++i_) asm volatile("global_load_dwordx4 %0, %1, %2 sc1" : "+v"(a[(pos) % RD][i_]) : "v"(OVA(pos, i_) + (i_ >> 1) * 1024), "s"(OBA(pos, i_)) : "memory"); } while (0)
#define OLD(pos) do { OL1(pos, 0); OL1(pos, 1); OL1(pos, 2); OL1(pos, 3); } while (0)
  auto omax = [&](u4(&v)[4]) {
    asm volatile("" : "+v"(v[0]), "+v"(v[1]), "+v"(v[2]), "+v"(v[3]));
    return max(umax3(v[0][0], v[1][0], v[2][0]), v[3][0]);
  };
#define OFIX(pos, m) do { \
    if (__builtin_expect(__ballot((m) == POIS) != 0ull, 0)) { RETRY(); _Pragma("unroll 1") for (unsigned it = 0; it < SPIN; ++it) { SLEEP(4); OLR(pos); WAITV(0); if (__ballot(omax(a[(pos) % RD]) == POIS) == 0ull) break; } } } while (0)
#define OS1(i)
#define OS2(i)
  u2 oh2 = oh;
  s4 b[8];
#define ORD(pos, f) do { if ((f) < 2 && (pos) == 8) { wa[(f) & 1] += 65536; asm volatile("" : "+v"(wa[(f) & 1])); } \
    RDTR(b[2 * (f)], wa[(f) & 1], ((pos) & 7) * 8192 + ((f) >> 1) * 1024); RDTR(b[2 * (f) + 1], wa[(f) & 1], ((pos) & 7) * 8192 + ((f) >> 1) * 1024 + 2048); } while (0)
  const unsigned lo0 = ldsb + L_PQ + w * 4096 + ov0, lo1 = lo0 ^ 2048;
#pragma unroll
  for (int i = 1; i < RD; ++i) {
    if (i == E0 + 1) {
      lwait<LW4>(adFL, 0x30u, 4);
      if (F5) asm volatile("s_waitcnt vmcnt(0)\n ds_write_b32 %0, %1" ::"v"(myFL), "v"(5u) : "memory");
    }
    if (i == E0 + 2) {
      RD128(a[0][0], lo0, 0); RD128(a[0][1], lo1, 0); RD128(a[0][2], lo0, 1024); RD128(a[0][3], lo1, 1024);
      ORD(0, 0); ORD(0, 1); ORD(0, 2); ORD(0, 3);
    }
    if (i == WP) wo_part(WB, 32);
    if (i == RD - 1) break;
    if (i == 1) {
      __builtin_amdgcn_s_setprio(2);
      if (E0 && ES) SLEEP(ES);
    }
    if (i == 1 || i == E0 + 1) go = seen & (unsigned)__ballot(oh[1] - NOW() - 1u >= 1000u);
    need(1u << i);
    if (i == 1) {
      STMP(15);
      oh2 = oh;
      if (__builtin_expect(seen != ~0u, 0)) asm volatile("global_load_dwordx2 %0, %1, %2 sc1" : "+v"(oh2) : "v"(flo), "s"(fb) : "memory");
    }
    OS1(i); OLD(i);
  }
#define WXT(pos) ((pos) < WP ? 32 - WB : 0)
  STMP(10);
#define OMF(pos, f, r) do { if ((f) < 2 && (pos) == 0 && (r) == 1) asm volatile("v_mfma_f32_32x32x16_bf16 %0, %1, %2, 0" : "=&v"(oc[r][(f) & 1]) \
      : "v"(CAT(b[2 * (f)], b[2 * (f) + 1])), "v"(a[(pos) % RD][((f) & 2) + (r)])); \
    else asm volatile("v_mfma_f32_32x32x16_bf16 %0, %1, %2, %0" : "+v"(oc[r][(f) & 1]) \
      : "v"(CAT(b[2 * (f)], b[2 * (f) + 1])), "v"(a[(pos) % RD][((f) & 2) + (r)])); \
    SBAR(); if ((r) == 1) ORD((pos) + 1, f); SBAR(); } while (0)
#define ONX(pos) ((pos) + RD - 1 < 16)
#define ONL(pos, i) do { if (ONX(pos)) OL1(((pos) + RD - 1) & 15, i); SBAR(); } while (0)
#define OWL(pos, f) do { WAITL(6); SBAR(); } while (0)
#define OHEAD(pos) do { unsigned m_ = 0; \
    if ((pos) == wtmin(WE / 2, WB / 2) - 1) { asm volatile("" : "+v"(fl3)); if (__builtin_expect(((unsigned)__ballot(fl3 < 5u) >> (w ^ 2)) & 1u, 0)) lwait(adFL, 1u << (w ^ 2), 5); SBAR(); } \
    if ((pos) == wtmin(WB / 2, 16) - 1) { asm volatile("" : "+v"(fl3)); if (__builtin_expect(((unsigned)__ballot(fl3 < 6u) >> (w ^ 2)) & 1u, 0)) lwait(adFL, 1u << (w ^ 2), 6); SBAR(); } \
    OWL(pos, 0); OMF(pos, 0, 0); if (ONX(pos)) { need(1u << (((pos) + RD - 1) & 15)); OS1(((pos) + RD - 1) & 15); } SBAR(); \
    OMF(pos, 0, 1); ONL(pos, 0); \
    OWL(pos, 1); OMF(pos, 1, 0); ONL(pos, 1); \
    OMF(pos, 1, 1); ONL(pos, 2); \
    OWL(pos, 2); OMF(pos, 2, 0); ONL(pos, 3); \
    if (((pos) == 1 && !F5) || (pos) == 4) { asm volatile("ds_write_b32 %0, %1" ::"v"(myFL), "v"((pos) > 1 ? 6u : 5u) : "memory"); SBAR(); } \
    if ((pos) == wtmin(WE / 2, WB / 2) - 3 || (pos) == wtmin(WB / 2, 16) - 3) { asm volatile("ds_read_b32 %0, %1" : "=v"(fl3) : "v"(adFL) : "memory"); SBAR(); } \
    OMF(pos, 2, 1); \
    OWL(pos, 3); OMF(pos, 3, 0); \
    if ((pos) < 15) { WAITV(4 * ((pos) + RD - 1 < 16 ? RD - 2 : 14 - (pos)) + WXT((pos) + 1)); m_ = omax(a[((pos) + 1) % RD]); asm volatile("" : "+v"(m_)); SBAR(); } \
    OMF(pos, 3, 1); \
    if ((pos) < 15) { OFIX(((pos) + 1) & 15, m_); OS2(((pos) + 1) & 15); } SBAR(); } while (0)
  static_assert(F5 && wtmin(WE / 2, WB / 2) >= 4 && wtmin(WB / 2, 16) >= wtmin(WE / 2, WB / 2) + 2 && wtmin(WB / 2, 16) >= 8 && wtmin(WB / 2, 16) <= 13 && WB < 32 && RD >= 4 && wtmax(WE / 2, 16) < 14, "");
  unsigned fl3;
  OHEAD(0);
  {
    asm volatile("" : "+v"(oh2));
    if (oh2[0] == tag) oh = oh2;
    seen = (unsigned)__ballot(oh[0] == tag);
    go = seen & (unsigned)__ballot(oh[1] - NOW() - max(r15 - RD, 0) * ADV - 1u >= 1000u);
    SBAR(); }
  OHEAD(1); STMP(11); OHEAD(2); OHEAD(3); OHEAD(4); OHEAD(5); OHEAD(6); OHEAD(7); STMP(12);
  OHEAD(8); OHEAD(9); STMP(13); OHEAD(10); OHEAD(11); OHEAD(12);
  {
#define XW(t) asm volatile("ds_write_b128 %0, %1 offset:%2" ::"v"(xw), "v"(__builtin_shufflevector(oc[1][(t) >> 2], oc[1][(t) >> 2], ((t) & 3) * 4, ((t) & 3) * 4 + 1, ((t) & 3) * 4 + 2, ((t) & 3) * 4 + 3)), \
      "i"(((t) >> 2) * 8192 + ((t) & 3) * 1024) : "memory")
#define OMX(f, r) do { asm volatile("v_mfma_f32_32x32x16_bf16 %0, %1, %2, %0" : "+v"(oc[r][(f) & 1]) : "v"(CAT(b[2 * (f)], b[2 * (f) + 1])), "v"(a[13 % RD][((f) & 2) + (r)])); SBAR(); } while (0)
#define OMU(pos, f, r) do { asm volatile("v_mfma_f32_32x32x16_bf16 %0, %1, %2, %0" : "+v"(oc[0][(f) & 1]) \
      : "v"(CAT(bu[(pos) & 1][r][2 * (f)], bu[(pos) & 1][r][2 * (f) + 1])), "v"(a[(pos) % RD][((f) & 2) + (r)])); SBAR(); } while (0)
#define ORU(pos, f, r) do { RDTR(bu[(pos) & 1][r][2 * (f)], (r) ? wb[(f) & 1] : wa[(f) & 1], ((pos) & 7) * 8192 + ((f) >> 1) * 1024); \
      RDTR(bu[(pos) & 1][r][2 * (f) + 1], (r) ? wb[(f) & 1] : wa[(f) & 1], ((pos) & 7) * 8192 + ((f) >> 1) * 1024 + 2048); SBAR(); } while (0)
#define OH13(f, f2) do { WAITL(6 + 2 * (f)); SBAR(); OMX(f, 1); ORU(14, f2, 0); ORU(14, f2, 1); } while (0)
    s4 bu[2][2][8]; unsigned m_;
    unsigned wb[2] = {wa[0] ^ 4096, wa[1] ^ 4096};
    asm volatile("" : "+v"(wb[0]), "+v"(wb[1])); SBAR();
    OH13(0, 0); OH13(1, 2); OH13(2, 1); OH13(3, 3);
    OMX(0, 0);
    OMX(1, 0); XW(0); XW(1); XW(2); XW(3); SBAR();
    OMX(2, 0); XW(4); XW(5); XW(6); XW(7);
    asm volatile("ds_write_b32 %0, %1" ::"v"(mf1), "v"(c7) : "memory"); SBAR();
    WAITV(4); m_ = omax(a[14 % RD]); asm volatile("" : "+v"(m_)); SBAR();
    OMX(3, 0);
    OFIX(14, m_); SBAR();
    WAITL(13); SBAR();
    OMU(14, 0, 0); ORU(15, 0, 0); OMU(14, 0, 1); ORU(15, 0, 1); OMU(14, 2, 0); ORU(15, 2, 0);
    WAITL(14); SBAR();
    OMU(14, 2, 1); ORU(15, 2, 1);
    OMU(14, 1, 0); ORU(15, 1, 0); ORU(15, 1, 1); OMU(14, 1, 1); ORU(15, 3, 0); ORU(15, 3, 1); OMU(14, 3, 0);
    WAITL(8); SBAR();
    f4 xi[8], y[8]; unsigned v, nx;
#define XRD(L) "ds_read_b32 %0, %18\n ds_read_b128 %5, %19 offset:8192\n ds_read_b128 %6, %19 offset:9216\n ds_read_b128 %7, %19 offset:10240\n ds_read_b128 %8, %19 offset:11264\n" \
      "ds_read_b128 %1, %19\n ds_read_b128 %2, %19 offset:1024\n ds_read_b128 %3, %19 offset:2048\n ds_read_b128 %4, %19 offset:3072\n s_cmp_eq_u32 %21, 0\n s_cbranch_scc1 " L "\n" \
      "ds_read_b128 %13, %20 offset:128\n ds_read_b128 %14, %20 offset:160\n ds_read_b128 %15, %20 offset:192\n ds_read_b128 %16, %20 offset:224\n" \
      "ds_read_b128 %9, %20\n ds_read_b128 %10, %20 offset:32\n ds_read_b128 %11, %20 offset:64\n ds_read_b128 %12, %20 offset:96\n" L ":\n"
#define XOPS(c) c(v), c(xi[0]), c(xi[1]), c(xi[2]), c(xi[3]), c(xi[4]), c(xi[5]), c(xi[6]), c(xi[7]), c(y[0]), c(y[1]), c(y[2]), c(y[3]), c(y[4]), c(y[5]), c(y[6]), c(y[7])
    asm volatile(XRD(".Ly0") : XOPS("=&v"), "=&s"(nx) : "v"(pf1), "v"(xr), "v"(ya), "s"(as) : "memory", "scc");
    SBAR();
    WAITV(0); m_ = omax(a[15 % RD]); asm volatile("" : "+v"(m_)); SBAR();
    OMU(14, 3, 1);
    OFIX(15, m_); SBAR();
    STMP(8);
    OMU(15, 0, 0); OMU(15, 0, 1);
    asm volatile("s_waitcnt lgkmcnt(0)\n v_cmp_gt_u32 vcc, %22, %0\n s_cbranch_vccz .Lyd\n s_mov_b32 %17, 0x200000\n.Lyl:\n" XRD(".Ly1")
                 "s_sub_u32 %17, %17, 1\n s_waitcnt lgkmcnt(0)\n s_cbranch_scc1 .Lyd\n v_cmp_gt_u32 vcc, %22, %0\n s_cbranch_vccnz .Lyl\n.Lyd:"
                 : XOPS("+v"), "=&s"(nx) : "v"(pf1), "v"(xr), "v"(ya), "s"(as), "v"(thr) : "memory", "scc", "vcc");
    SBAR();
#define XA(t) do { _Pragma("unroll") for (int i = 0; i < 4; ++i) oc[0][(t) >> 2][((t) & 3) * 4 + i] += xi[t][i]; } while (0)
#define TW_(t) do { const u2 d_ = (u2){pk2(oc[0][(t) >> 2][((t) & 3) * 4], oc[0][(t) >> 2][((t) & 3) * 4 + 1]), pk2(oc[0][(t) >> 2][((t) & 3) * 4 + 2], oc[0][(t) >> 2][((t) & 3) * 4 + 3])}; \
      asm volatile("ds_write_b64 %0, %1" ::"v"(ta[t]), "v"(d_) : "memory"); SBAR(); } while (0)
    OMU(15, 2, 0);
    if (sel >= 0) { xi[4] += y[4]; xi[5] += y[5]; xi[6] += y[6]; xi[7] += y[7]; }
    asm volatile("" : "+v"(xi[4]), "+v"(xi[5]), "+v"(xi[6]), "+v"(xi[7]), "+v"(oc[0][1])); SBAR();
    OMU(15, 2, 1);
    XA(4); XA(5); XA(6); XA(7); asm volatile("s_nop 0" : "+v"(oc[0][1])); SBAR();
    OMU(15, 1, 0);
    if (sel >= 0) { xi[0] += y[0]; xi[1] += y[1]; xi[2] += y[2]; xi[3] += y[3]; }
    asm volatile("" : "+v"(xi[0]), "+v"(xi[1]), "+v"(xi[2]), "+v"(xi[3])); SBAR();
    OMU(15, 1, 1);
    asm volatile("" : "+v"(oc[0][0])); XA(0); TW_(0);
    OMU(15, 3, 0); XA(1); TW_(1);
    OMU(15, 3, 1); XA(2); TW_(2); XA(3); TW_(3);
    asm volatile("s_nop 1" : "+v"(oc[0][1])); SBAR();
    TW_(4); TW_(5); TW_(6); TW_(7);
    u4 ov[4];
    RD128(ov[0], tr, 0); RD128(ov[1], tr, 1024); RD128(ov[2], tr, 2048); RD128(ov[3], tr, 3072);
    const __amdgpu_buffer_rsrc_t outr = __builtin_amdgcn_make_buffer_rsrc(out, 0, 2048 * D * 2, 0x00020000);
#pragma unroll
    for (int i = 0; i < 4; ++i) {
      asm volatile("s_waitcnt lgkmcnt(%1)" : "+v"(ov[i]) : "i"(3 - i) : "memory");
      __builtin_amdgcn_raw_buffer_store_b128(ov[i], outr, oo, i * 16384, 16);
    }
  }
  if (g == 0 && w == 0 && l == 0) __builtin_amdgcn_raw_buffer_store_b32(tag, wse, WS_E, 0, 16);
  STMP(9);
  STFLUSH();
  asm volatile(".Lend_c:");
  }
  return;
  }
  {
#define LT2(i) BST(i)
    unsigned vb[4], vfw[4];
    STMP(6); SBAR();
#pragma unroll
    for (int i = 0; i < 4; ++i) vb[i] = (8 * i + (l >> 3)) * 6144 + (((l & 7) ^ ((((l >> 4) & 1) << 2) | ((i & 1) << 1))) * 16) + (3 - i) * 1024;
    const unsigned lb0 = lw * 196608, lb1 = 196608 - lw * 194560, mbb = ldsb + 16384 + lw * 12288;
    const char* Ab = kx + ao;
    const char* Bb = kq + (hp * 128 + lw * 2048 - 3072);
    auto issue = [&](int s, int u0, int u1) {
      const int kt = (s + rot) & 15;
      const char* sa = Ab + kt * 128; const char* sb = Bb + kt * (64 * 6144);
      const unsigned so = (s & 3) * STG, ma = mba + so, mq = mbb + so;
#pragma unroll
      for (int u = u0; u < u1; ++u) { if (u < 2) GL4(va, sa + u * 65536, ma, u * 4096); else GL4(vb, sb + (u == 2 ? lb0 : u == 3 ? lb1 : lb0 + 2048), mq, (u - 2) * 4096); }
    };
    int tk = 0;
#define LST(i) BST(i)
#define LBAR() do { LST(tk + 1); BAR(); LST(tk + 21); ++tk; } while (0)
    issue(0, 2, 3); issue(0, 1, 2); issue(0, 3, 5); SBAR(); STMP(8); SBAR(); issue(1, 0, 5); issue(2, 0, U0);
    LT2(60);
    SBAR();
    GETTAG(tag);
    MKWS(tag);
    const unsigned long long ptab = mktab();
#pragma unroll
    for (int i = 0; i < 4; ++i) vfw[i] = VFW(i);
    const int hd = hp * 2 + lw;
    u4 sel;
#pragma unroll
    for (int e = 0; e < 2; ++e) {
      unsigned v = (r15 >> 2) == g4 ? (((r15 & 3) == 2 * e ? 0x3F80u : 0u) | ((r15 & 3) == 2 * e + 1 ? 0x3F800000u : 0u)) : 0u;
      sel[e] = v; sel[2 + e] = v;
    }
    asm volatile("" : "+v"(sel));
    const char* kvb = uptr(wsx + WS_KVP + 4096 + (size_t)hd * 32768);
    const char* zb = uptr(wsx + WS_ZP + (size_t)hd * 2048);
    unsigned zoff = q * 128 + g4 * 32 + p * 8, oho = l == 0 ? WS_OH + (mb * 16 + hp) * 128 : WS_SZ, fla = ldsb + L_FL, v0 = 0u, v1 = 1u, afl = adFL, mfl = myFL;
    unsigned hko = l == 0 ? WS_HK + hp * 512 + (mb >> 2) * 128 + (mb & 3) * 4 : WS_SZ, pzo = WS_OF + (mb * 32 + hp * 2 + (PE >= 2 ? lw : 0)) * 8192 + l * 16;
    u4 pz = (u4)(POIS);
    asm volatile("" : "+v"(zoff), "+v"(oho), "+v"(fla), "+s"(kvb), "+s"(zb), "+v"(v0), "+v"(v1), "+v"(afl), "+v"(mfl), "+v"(hko), "+v"(pzo), "+v"(pz));
    unsigned lv16 = l * 16, pa = ldsb + L_PQ + lw * 4096 + l * 16, thr = (l & 4) ? 3u : 4u;
    unsigned ofo = WS_OF + (mb * 32 + hd) * 8192 + g4 * 512 + r15 * 16, lwa = ldsb + L_PQ + lw * 4096 + g4 * 512 + r15 * 16;
    asm volatile("" : "+v"(vfw[0]), "+v"(vfw[1]), "+v"(vfw[2]), "+v"(vfw[3]), "+v"(lv16), "+v"(pa), "+v"(thr), "+v"(ofo), "+v"(lwa));
    PFB(sp ? 64 - SPS : 64 - SPW, sp ? 2 * SPS : 2 * SPW);
    SBAR();
    WAITV(20 + 4 * U0); LT2(0); BAR(); LT2(20);
    issue(2, U0, 5);
    unsigned ck0 = (unsigned)__builtin_amdgcn_s_memtime(), rt0 = NOW();
    asm volatile("" : "+s"(ck0), "+s"(rt0));
#define LPF() spf(pfb)
    int s0 = 3;
    if (LPL) __builtin_amdgcn_s_setprio(LPL);
    if (U3 < 5) { issue(3, 0, U3); LST(tk + 40); WAITV(20 + 4 * U3); LBAR(); issue(3, U3, 5); s0 = 4; }
#pragma unroll 1
    for (; s0 < (sp ? (LF ? SL5 + 2 : 16) : 6 + ph); ++s0) { issue(s0, 0, 5); LST(tk + 40); if (sp && s0 < 14) LPF(); WAITV(40); LBAR(); }
    if (!sp) {
#pragma unroll 1
    for (; s0 < 16; s0 += 5) {
      LPF(); WAITV(20); LBAR();
      LPF(); WAITV(0); LBAR();
      issue(s0, 0, 5); issue(s0 + 1, 0, U1); LST(tk + 40); WAITV(4 * U1); LBAR();
      issue(s0 + 1, U1, 5); issue(s0 + 2, 0, 5);
      if (LF && s0 > 10) { s0 += 3; break; }
      if (s0 + 3 >= 16) break;
      issue(s0 + 3, 0, U2); LST(tk + 40); WAITV(20 + 4 * U2); LBAR();
      issue(s0 + 3, U2, 5);
      if (s0 + 4 >= 16) break;
      issue(s0 + 4, 0, 5); LST(tk + 40); WAITV(40); LBAR();
    }
    }
    if (LF) {
#pragma unroll 1
      for (; s0 < 16; ++s0) { LST(tk + 40); WAITV(20); LBAR(); issue(s0, 0, 5); }
    }
    if (LPL) __builtin_amdgcn_s_setprio(0);
    int wq = 0;
    auto wo_upto = [&](int lim) {
      _Pragma("unroll 1") for (; wq < lim; ++wq) {
        const int sl = 2 * ((int)(WT >> (4 * (wq >> 1))) & 15) + (wq & 1);
        const char* wp = (const char*)wo + (nb * 128 - 3072) + HEADAT(sl) * 65536; const unsigned wm = ldsb + sl * 4096; GL4(vfw, wp, wm, L_WO); }
    };
    if (!ZH) asm volatile("ds_write_b32 %0, %1" ::"v"(afl), "v"(v0) : "memory");
    if (PE == 1 && lw) {
#pragma unroll
      for (int i = 0; i < 16; ++i) __builtin_amdgcn_raw_buffer_store_b128(pz, wsr, pzo + (i & 3) * 1024, (i >> 2) * 4096, 16);
    }
    auto po8 = [&]() {
#pragma unroll
      for (int i = 0; i < 8; ++i) __builtin_amdgcn_raw_buffer_store_b128(pz, wsr, pzo + (i & 3) * 1024, (i >> 2) * 4096, 16);
    };
    const bool pl = PE == 3 && !sp && ph == 2;
    if (PE >= 2 && !pl) po8();
    SBAR();
    unsigned kd = sp ? KS : KD, od = OD;
    if (CF && lw) {
      const unsigned dc = (unsigned)__builtin_amdgcn_s_memtime() - ck0, dr = NOW() - rt0;
      const float sc = fminf((float)dr * (float)CF * __builtin_amdgcn_rcpf((float)dc), 2.f);
      kd = KL + (unsigned)((float)(kd - KL) * sc); od = OL + (unsigned)((float)(OD - OL) * sc);
      asm volatile("" : "+v"(kd), "+v"(od));
    }
    SBAR();
    if (PE == 1 && lw) WAITV(16); else if (PE >= 2 && !pl) WAITV(8); else WAITV(0);
    WAITL(0); LBAR();
    SBAR();
    u4 dg;
    unsigned now;
    if (lw) {
      const unsigned tb = NOW();
      __builtin_amdgcn_raw_buffer_store_b32(tb + kd, wsr, hko, 0, 16);
#pragma unroll
      for (int i = 0; i < (PE ? 0 : 16); ++i) __builtin_amdgcn_raw_buffer_store_b128(pz, wsr, pzo + (i & 3) * 1024, (i >> 2) * 4096, 16);
      if (pl) po8();
      LT2(61);
      const char* hb = uptr(wsx + WS_HK + hp * 512);
      __builtin_amdgcn_s_setprio(LP1);
      lwait<4>(adFL, PE >= 2 ? 0xD0u : 0xC0u, 1u);
      while (NOW() - tb < HT) SLEEP(1);
      STMP(1);
      unsigned t0, rq[4], m0, m1, nt;
#define HG4(d, a, b, c, e) "s_sub_u32 s" #a ", s" #a ", %0\n s_sub_u32 s" #b ", s" #b ", %0\n s_sub_u32 s" #c ", s" #c ", %0\n s_sub_u32 s" #e ", s" #e ", %0\n" \
      "s_max_u32 s" #a ", s" #a ", s" #b "\n s_max_u32 s" #c ", s" #c ", s" #e "\n s_max_u32 %" #d ", s" #a ", s" #c "\n"
      asm volatile("s_mov_b32 %7, 15\n.Lhint:\n s_load_dwordx4 s[72:75], %8, 0 glc\n s_load_dwordx4 s[76:79], %8, 128 glc\n s_load_dwordx4 s[80:83], %8, 256 glc\n"
                   "s_load_dwordx4 s[84:87], %8, 384 glc\n s_memrealtime s[88:89]\n s_waitcnt lgkmcnt(0)\n s_sub_u32 %0, s88, 0x800\n"
                   HG4(1, 72, 73, 74, 75) HG4(2, 76, 77, 78, 79) HG4(3, 80, 81, 82, 83) HG4(4, 84, 85, 86, 87)
                   "s_max_u32 %5, %1, %2\n s_max_u32 %6, %3, %4\n s_max_u32 s72, %5, %6\n s_cmpk_lt_u32 s72, 0xc00\n s_cbranch_scc1 .Lhdone\n"
                   "s_sleep %9\n s_sub_u32 %7, %7, 1\n s_cbranch_scc0 .Lhint\n"
                   "s_mov_b32 %1, 0\n s_mov_b32 %2, 0\n s_mov_b32 %3, 0\n s_mov_b32 %4, 0\n s_mov_b32 %5, 0\n s_mov_b32 %6, 0\n.Lhdone:"
                   : "=&s"(t0), "=&s"(rq[0]), "=&s"(rq[1]), "=&s"(rq[2]), "=&s"(rq[3]), "=&s"(m0), "=&s"(m1), "=&s"(nt) : "s"(hb), "i"(HKS)
                   : "memory", "scc", "s72", "s73", "s74", "s75", "s76", "s77", "s78", "s79", "s80", "s81", "s82", "s83", "s84", "s85", "s86", "s87", "s88", "s89");
      LT2(16);
      const bool up = m0 > m1;
      const unsigned a0 = up ? rq[2] : rq[0], a1 = up ? rq[3] : rq[1], a2 = up ? rq[0] : rq[2], a3 = up ? rq[1] : rq[3], gu = up ? 2u : 0u;
      unsigned e0, e1, go;
      asm("s_cmp_gt_u32 %3, %4\n s_cselect_b32 %0, %6, %5\n s_cselect_b32 %1, %5, %6\n s_addc_u32 %2, %7, 0" : "=&s"(e0), "=&s"(e1), "=&s"(go) : "s"(a2), "s"(a3), "s"(a0), "s"(a1), "s"(gu) : "scc");
      dg = (u4){((t0 + 3u + e0) & ~3u) | go, t0 + e1, t0 + min(a2, a3), t0 + max(m0, m1)};
      asm volatile("ds_write_b128 %0, %1 offset:32\n ds_write_b32 %0, %2 offset:20" ::"v"(fla), "v"(dg), "v"(2u) : "memory");
      LT2(15);
      now = NOW();
      WAITV(0);
      unsigned de = dg[3] - t0;
      if (GI) { de = max(max(de, dg[2] - t0 + GI), max(dg[1] - t0 + 2 * GI, max(dg[0] - t0, tb + (unsigned)__builtin_amdgcn_readfirstlane(kd) + GO - t0) + 3 * GI)); }
      de += t0;
      ST64(((u2){tag, ((int)(de - now) > 0 ? de : now) + od}), oho);
    } else {
      if (pl) po8();
      if (WD0) { const unsigned tb = NOW(); wo_upto(W00); while (NOW() - tb < WD0) SLEEP(2); }
      wo_upto(WA);
      if (PE >= 2) asm volatile("s_waitcnt vmcnt(%2)\n ds_write_b32 %0, %1" ::"v"(mfl), "v"(v1), "i"(4 * WA) : "memory");
      LT2(15);
      if (WA < WE || GW) { lwait<2>(adFL, 0x0Fu, 3); if (WS0) SLEEP(WS0); wo_upto(WE); }
      STMP(1);
      __builtin_amdgcn_s_setprio(LP0);
      u4 v;
      _Pragma("unroll 1") for (unsigned it = 0; it < SPIN; ++it) {
        unsigned f;
        asm volatile("ds_read_b32 %0, %2 offset:20\n ds_read_b128 %1, %2 offset:32\n s_waitcnt lgkmcnt(0)" : "=&v"(f), "=&v"(v) : "v"(fla) : "memory");
        if (__builtin_amdgcn_readfirstlane(f) >= 2u) break;
        SLEEP(2);
      }
#pragma unroll
      for (int e = 0; e < 4; ++e) dg[e] = __builtin_amdgcn_readfirstlane(v[e]);
      now = NOW();
    }
    unsigned gx = (dg[0] & 3u) * 8192u;
    asm volatile("" : "+s"(gx));
    __builtin_amdgcn_s_setprio(3);
    STMP(2);
    unsigned nr;
#define OST_(i)
#define OSTE :
    asm volatile(
      "s_lshr_b32 s72, %[gx], 4\n v_xor_b32 v252, %[gx], %[lv]\n v_xor_b32 v253, s72, %[zo]\n s_mov_b32 s74, %[now]\n s_sub_i32 s72, s74, %[d0]\n s_cmp_gt_i32 s72, -1\n "
      "s_cbranch_scc1 .Lgo0\n .Lgw0:\n s_memrealtime s[74:75]\n s_waitcnt lgkmcnt(0)\n s_sub_i32 s72, s74, %[d0]\n s_cmp_lt_i32 s72, 0\n s_cbranch_scc1 .Lgw0\n .Lgo0:\n "
      "global_load_dwordx2 v[240:241], v253, %[zb] sc1\n global_load_dwordx4 v[80:83], v252, %[kb] offset:-4096 sc1\n global_load_dwordx4 v[84:87], v252, %[kb] offset:-2048 sc1\n "
      "global_load_dwordx4 v[90:93], v252, %[kb] offset:-3072 sc1\n global_load_dwordx4 v[94:97], v252, %[kb] offset:-1024 sc1\n global_load_dwordx4 v[100:103], v252, %[kb] offset:0 sc1\n "
      "global_load_dwordx4 v[104:107], v252, %[kb] offset:2048 sc1\n global_load_dwordx4 v[110:113], v252, %[kb] offset:1024 sc1\n "
      "global_load_dwordx4 v[114:117], v252, %[kb] offset:3072 sc1\n v_xor_b32 v252, 0x2000, v252\n v_xor_b32 v253, 0x200, v253\n s_sub_i32 s72, s74, %[d1]\n s_cmp_gt_i32 s72, -1\n "
      "s_cbranch_scc1 .Lgo1\n .Lgw1:\n s_memrealtime s[74:75]\n s_waitcnt lgkmcnt(0)\n s_sub_i32 s72, s74, %[d1]\n s_cmp_lt_i32 s72, 0\n s_cbranch_scc1 .Lgw1\n .Lgo1:\n "
      "global_load_dwordx2 v[242:243], v253, %[zb] sc1\n global_load_dwordx4 v[120:123], v252, %[kb] offset:-4096 sc1\n global_load_dwordx4 v[124:127], v252, %[kb] offset:-2048 sc1\n "
      "global_load_dwordx4 v[130:133], v252, %[kb] offset:-3072 sc1\n global_load_dwordx4 v[134:137], v252, %[kb] offset:-1024 sc1\n "
      "global_load_dwordx4 v[140:143], v252, %[kb] offset:0 sc1\n global_load_dwordx4 v[144:147], v252, %[kb] offset:2048 sc1\n global_load_dwordx4 v[150:153], v252, %[kb] offset:1024 sc1\n "
      "global_load_dwordx4 v[154:157], v252, %[kb] offset:3072 sc1\n v_xor_b32 v252, 0x6000, v252\n v_xor_b32 v253, 0x600, v253\n s_sub_i32 s72, s74, %[d2]\n s_cmp_gt_i32 s72, -1\n "
      "s_cbranch_scc1 .Lgo2\n .Lgw2:\n s_memrealtime s[74:75]\n s_waitcnt lgkmcnt(0)\n s_sub_i32 s72, s74, %[d2]\n s_cmp_lt_i32 s72, 0\n s_cbranch_scc1 .Lgw2\n .Lgo2:\n "
      "global_load_dwordx2 v[244:245], v253, %[zb] sc1\n global_load_dwordx4 v[160:163], v252, %[kb] offset:-4096 sc1\n global_load_dwordx4 v[164:167], v252, %[kb] offset:-2048 sc1\n "
      "global_load_dwordx4 v[170:173], v252, %[kb] offset:-3072 sc1\n global_load_dwordx4 v[174:177], v252, %[kb] offset:-1024 sc1\n "
      "global_load_dwordx4 v[180:183], v252, %[kb] offset:0 sc1\n global_load_dwordx4 v[184:187], v252, %[kb] offset:2048 sc1\n global_load_dwordx4 v[190:193], v252, %[kb] offset:1024 sc1\n "
      "global_load_dwordx4 v[194:197], v252, %[kb] offset:3072 sc1\n v_xor_b32 v252, 0x2000, v252\n v_xor_b32 v253, 0x200, v253\n v_mov_b32 v248, 3\n s_sub_i32 s72, s74, %[d3]\n "
      "s_cmp_gt_i32 s72, -1\n s_cbranch_scc1 .Lgo3\n .Lgw3:\n s_memrealtime s[74:75]\n s_waitcnt lgkmcnt(0)\n s_sub_i32 s72, s74, %[d3]\n s_cmp_lt_i32 s72, 0\n s_cbranch_scc1 .Lgw3\n "
      ".Lgo3:\n "
      OST_(0)
      "global_load_dwordx2 v[246:247], v253, %[zb] sc1\n global_load_dwordx4 v[200:203], v252, %[kb] offset:-4096 sc1\n global_load_dwordx4 v[204:207], v252, %[kb] offset:-2048 sc1\n "
      "global_load_dwordx4 v[210:213], v252, %[kb] offset:-3072 sc1\n global_load_dwordx4 v[214:217], v252, %[kb] offset:-1024 sc1\n "
      "global_load_dwordx4 v[220:223], v252, %[kb] offset:0 sc1\n global_load_dwordx4 v[224:227], v252, %[kb] offset:2048 sc1\n global_load_dwordx4 v[230:233], v252, %[kb] offset:1024 sc1\n "
      "global_load_dwordx4 v[234:237], v252, %[kb] offset:3072 sc1\n ds_write_b32 %[mfl], v248\n "
      OST_(1)
      "s_mov_b32 %[nr], 0\n s_movk_i32 s73, 0x1000\n s_waitcnt vmcnt(27)\n .Lck0:\n v_max3_u32 v254, v240, v80, v84\n v_max3_u32 v254, v254, v90, v94\n v_max3_u32 v254, v254, v100, v104\n "
      "v_max3_u32 v254, v254, v110, v114\n v_cmp_eq_u32 vcc, -1, v254\n s_cbranch_vccnz .Lrt0\n .Lok0:\n v_mov_b64 v[88:89], v[82:83]\n v_mov_b64 v[82:83], v[84:85]\n "
      "v_mov_b64 v[98:99], v[92:93]\n v_mov_b64 v[92:93], v[94:95]\n v_mov_b64 v[108:109], v[102:103]\n v_mov_b64 v[102:103], v[104:105]\n v_mov_b64 v[118:119], v[112:113]\n "
      "v_mov_b64 v[112:113], v[114:115]\n v_mfma_f32_16x16x32_bf16 v[68:71], %[sel], v[86:89], 0\n v_mfma_f32_16x16x32_bf16 v[64:67], %[sel], v[80:83], 0\n "
      "v_mfma_f32_16x16x32_bf16 v[72:75], %[sel], v[96:99], 0\n v_mfma_f32_16x16x32_bf16 v[76:79], %[sel], v[90:93], 0\n v_mfma_f32_16x16x32_bf16 v[68:71], %[sel], v[106:109], v[68:71]\n "
      "v_mfma_f32_16x16x32_bf16 v[64:67], %[sel], v[100:103], v[64:67]\n v_mfma_f32_16x16x32_bf16 v[72:75], %[sel], v[116:119], v[72:75]\n "
      "v_mfma_f32_16x16x32_bf16 v[76:79], %[sel], v[110:113], v[76:79]\n s_waitcnt vmcnt(18)\n .Lck1:\n v_max3_u32 v254, v242, v120, v124\n v_max3_u32 v254, v254, v130, v134\n "
      "v_max3_u32 v254, v254, v140, v144\n v_max3_u32 v254, v254, v150, v154\n v_cmp_eq_u32 vcc, -1, v254\n s_cbranch_vccnz .Lrt1\n .Lok1:\n v_mov_b64 v[128:129], v[122:123]\n "
      "v_mov_b64 v[122:123], v[124:125]\n v_mov_b64 v[138:139], v[132:133]\n v_mov_b64 v[132:133], v[134:135]\n v_mov_b64 v[148:149], v[142:143]\n v_mov_b64 v[142:143], v[144:145]\n "
      "v_mov_b64 v[158:159], v[152:153]\n v_mov_b64 v[152:153], v[154:155]\n v_mfma_f32_16x16x32_bf16 v[84:87], %[sel], v[126:129], 0\n "
      "v_mfma_f32_16x16x32_bf16 v[80:83], %[sel], v[120:123], 0\n v_mfma_f32_16x16x32_bf16 v[88:91], %[sel], v[136:139], 0\n v_mfma_f32_16x16x32_bf16 v[92:95], %[sel], v[130:133], 0\n "
      "v_mfma_f32_16x16x32_bf16 v[84:87], %[sel], v[146:149], v[84:87]\n v_mfma_f32_16x16x32_bf16 v[80:83], %[sel], v[140:143], v[80:83]\n "
      "v_mfma_f32_16x16x32_bf16 v[88:91], %[sel], v[156:159], v[88:91]\n v_mfma_f32_16x16x32_bf16 v[92:95], %[sel], v[150:153], v[92:95]\n s_waitcnt vmcnt(9)\n .Lck2:\n "
      "v_max3_u32 v254, v244, v160, v164\n v_max3_u32 v254, v254, v170, v174\n v_max3_u32 v254, v254, v180, v184\n v_max3_u32 v254, v254, v190, v194\n v_cmp_eq_u32 vcc, -1, v254\n "
      "s_cbranch_vccnz .Lrt2\n .Lok2:\n v_mov_b64 v[168:169], v[162:163]\n v_mov_b64 v[162:163], v[164:165]\n v_mov_b64 v[178:179], v[172:173]\n v_mov_b64 v[172:173], v[174:175]\n "
      "v_mov_b64 v[188:189], v[182:183]\n v_mov_b64 v[182:183], v[184:185]\n v_mov_b64 v[198:199], v[192:193]\n v_mov_b64 v[192:193], v[194:195]\n "
      "v_pk_add_f32 v[64:65], v[64:65], v[80:81]\n v_pk_add_f32 v[66:67], v[66:67], v[82:83]\n v_pk_add_f32 v[68:69], v[68:69], v[84:85]\n v_pk_add_f32 v[70:71], v[70:71], v[86:87]\n "
      "v_pk_add_f32 v[72:73], v[72:73], v[88:89]\n v_pk_add_f32 v[74:75], v[74:75], v[90:91]\n v_pk_add_f32 v[76:77], v[76:77], v[92:93]\n v_pk_add_f32 v[78:79], v[78:79], v[94:95]\n "
      "v_pk_add_f32 v[250:251], v[240:241], v[242:243]\n v_mfma_f32_16x16x32_bf16 v[100:103], %[sel], v[166:169], 0\n v_mfma_f32_16x16x32_bf16 v[96:99], %[sel], v[160:163], 0\n "
      "v_mfma_f32_16x16x32_bf16 v[104:107], %[sel], v[176:179], 0\n v_mfma_f32_16x16x32_bf16 v[108:111], %[sel], v[170:173], 0\n "
      "v_mfma_f32_16x16x32_bf16 v[100:103], %[sel], v[186:189], v[100:103]\n v_mfma_f32_16x16x32_bf16 v[96:99], %[sel], v[180:183], v[96:99]\n "
      "v_mfma_f32_16x16x32_bf16 v[104:107], %[sel], v[196:199], v[104:107]\n v_mfma_f32_16x16x32_bf16 v[108:111], %[sel], v[190:193], v[108:111]\n v_mov_b32 v156, 0x358637bd\n "
      "v_mov_b32 v157, 0x358637bd\n v_mov_b32 v158, 0x358637bd\n v_mov_b32 v159, 0x358637bd\n s_mov_b32 s72, 0x200000\n .Lpq:\n ds_read_b32 v248, %[afl]\n s_waitcnt lgkmcnt(0)\n "
      "v_cmp_lt_u32 vcc, v248, %[thr]\n s_and_b32 s76, vcc_lo, %[om]\n s_cbranch_scc0 .Lpd\n s_sub_u32 s72, s72, 1\n s_cbranch_scc0 .Lpq\n .Lpd:\n ds_read_b128 v[120:123], %[pa] offset:0\n "
      "ds_read_b128 v[124:127], %[pa] offset:1024\n ds_read_b128 v[128:131], %[pa] offset:2048\n ds_read_b128 v[132:135], %[pa] offset:3072\n ds_read_b128 v[136:139], %[pa] offset:8192\n "
      "ds_read_b128 v[140:143], %[pa] offset:9216\n ds_read_b128 v[144:147], %[pa] offset:10240\n ds_read_b128 v[148:151], %[pa] offset:11264\n .p2align 3\n .Lg3:\n s_waitcnt vmcnt(8)\n "
      "v_add_f32 v248, v244, v246\n v_add_f32 v249, v245, v247\n v_add_f32 v248, v250, v248\n v_add_f32 v249, v251, v249\n s_nop 0\n "
      "v_add_f32_dpp v248, v248, v248 row_ror:8 row_mask:0xf bank_mask:0xf\n v_add_f32_dpp v249, v249, v249 row_ror:8 row_mask:0xf bank_mask:0xf\n s_nop 0\n "
      "v_add_f32_dpp v248, v248, v248 row_ror:4 row_mask:0xf bank_mask:0xf\n v_add_f32_dpp v249, v249, v249 row_ror:4 row_mask:0xf bank_mask:0xf\n v_cvt_pk_bf16_f32 v248, v248, v249\n "
      "s_nop 1\n v_mov_b32_dpp v112, v248 quad_perm:[0,0,0,0] row_mask:0xf bank_mask:0xf\n v_mov_b32_dpp v113, v248 quad_perm:[1,1,1,1] row_mask:0xf bank_mask:0xf\n "
      "v_mov_b32_dpp v114, v248 quad_perm:[2,2,2,2] row_mask:0xf bank_mask:0xf\n v_mov_b32_dpp v115, v248 quad_perm:[3,3,3,3] row_mask:0xf bank_mask:0xf\n s_waitcnt vmcnt(7) lgkmcnt(0)\n "
      "v_mov_b64 v[208:209], v[202:203]\n v_mfma_f32_16x16x32_bf16 v[176:179], v[112:115], v[120:123], v[156:159]\n v_mfma_f32_16x16x32_bf16 v[180:183], v[112:115], v[124:127], v[156:159]\n "
      "v_mfma_f32_16x16x32_bf16 v[184:187], v[112:115], v[128:131], v[156:159]\n v_rcp_f32_e64 v192, v176\n v_mfma_f32_16x16x32_bf16 v[188:191], v[112:115], v[132:135], v[156:159]\n "
      "v_rcp_f32 v193, v180\n s_waitcnt vmcnt(6)\n v_mfma_f32_16x16x32_bf16 v[164:167], %[sel], v[206:209], 0\n v_mov_b64_e64 v[202:203], v[204:205]\n v_max3_u32 v254, v246, v200, v204\n "
      "v_mfma_f32_16x16x32_bf16 v[176:179], v[112:115], v[136:139], v[156:159]\n v_rcp_f32_e64 v194, v184\n v_mfma_f32_16x16x32_bf16 v[160:163], %[sel], v[200:203], 0\n s_waitcnt vmcnt(5)\n "
      "v_mov_b64 v[218:219], v[212:213]\n v_mfma_f32_16x16x32_bf16 v[180:183], v[112:115], v[140:143], v[156:159]\n v_rcp_f32 v195, v188\n s_waitcnt vmcnt(4)\n "
      "v_mfma_f32_16x16x32_bf16 v[168:171], %[sel], v[216:219], 0\n v_mov_b64_e64 v[212:213], v[214:215]\n v_max3_u32 v254, v254, v210, v214\n "
      "v_mfma_f32_16x16x32_bf16 v[184:187], v[112:115], v[144:147], v[156:159]\n v_rcp_f32_e64 v196, v176\n v_mfma_f32_16x16x32_bf16 v[172:175], %[sel], v[210:213], 0\n s_waitcnt vmcnt(3)\n "
      "v_mov_b64 v[228:229], v[222:223]\n v_mfma_f32_16x16x32_bf16 v[188:191], v[112:115], v[148:151], v[156:159]\n v_rcp_f32 v197, v180\n s_waitcnt vmcnt(2)\n "
      "v_mfma_f32_16x16x32_bf16 v[164:167], %[sel], v[226:229], v[164:167]\n v_mov_b64_e64 v[222:223], v[224:225]\n v_max3_u32 v254, v254, v220, v224\n v_rcp_f32_e64 v198, v184\n "
      "v_mfma_f32_16x16x32_bf16 v[160:163], %[sel], v[220:223], v[160:163]\n v_rcp_f32_e64 v199, v188\n s_waitcnt vmcnt(1)\n v_mov_b64 v[238:239], v[232:233]\n "
      "v_pk_add_f32 v[164:165], v[100:101], v[164:165]\n v_pk_add_f32 v[166:167], v[102:103], v[166:167]\n v_pk_add_f32 v[164:165], v[68:69], v[164:165]\n "
      "v_pk_add_f32 v[166:167], v[70:71], v[166:167]\n v_pk_add_f32 v[160:161], v[96:97], v[160:161]\n v_pk_add_f32 v[162:163], v[98:99], v[162:163]\n "
      "v_pk_add_f32 v[160:161], v[64:65], v[160:161]\n v_pk_add_f32 v[162:163], v[66:67], v[162:163]\n s_waitcnt vmcnt(0)\n "
      OST_(2)
      "v_mov_b64 v[232:233], v[234:235]\n v_mfma_f32_16x16x32_bf16 v[168:171], %[sel], v[236:239], v[168:171]\n v_max3_u32 v254, v254, v230, v234\n v_cvt_pk_bf16_f32 v118, v164, v165\n "
      "v_mfma_f32_16x16x32_bf16 v[172:175], %[sel], v[230:233], v[172:175]\n v_cvt_pk_bf16_f32 v119, v166, v167\n v_cvt_pk_bf16_f32 v116, v160, v161\n v_cvt_pk_bf16_f32 v117, v162, v163\n "
      "v_cmp_eq_u32 vcc, -1, v254\n s_cbranch_vccnz .Lrt3\n .Lok3:\n "
      OST_(3)
      "v_mfma_f32_16x16x32_bf16 v[200:203], v[116:119], v[120:123], 0\n v_add_f32 v168, v104, v168\n v_add_f32 v169, v105, v169\n "
      "v_mfma_f32_16x16x32_bf16 v[204:207], v[116:119], v[124:127], 0\n v_add_f32 v170, v106, v170\n v_add_f32 v171, v107, v171\n "
      "v_mfma_f32_16x16x32_bf16 v[208:211], v[116:119], v[128:131], 0\n v_add_f32 v168, v72, v168\n v_add_f32 v169, v73, v169\n "
      "v_mfma_f32_16x16x32_bf16 v[212:215], v[116:119], v[132:135], 0\n v_add_f32 v170, v74, v170\n v_add_f32 v171, v75, v171\n "
      "v_mfma_f32_16x16x32_bf16 v[216:219], v[116:119], v[136:139], 0\n v_add_f32 v172, v108, v172\n v_add_f32 v173, v109, v173\n v_cvt_pk_bf16_f32 v152, v168, v169\n "
      "v_mfma_f32_16x16x32_bf16 v[220:223], v[116:119], v[140:143], 0\n v_add_f32 v174, v110, v174\n v_add_f32 v175, v111, v175\n v_cvt_pk_bf16_f32 v153, v170, v171\n "
      "v_mfma_f32_16x16x32_bf16 v[224:227], v[116:119], v[144:147], 0\n v_add_f32 v172, v76, v172\n v_add_f32 v173, v77, v173\n v_add_f32 v174, v78, v174\n v_add_f32 v175, v79, v175\n "
      "v_cvt_pk_bf16_f32 v154, v172, v173\n v_cvt_pk_bf16_f32 v155, v174, v175\n v_mfma_f32_16x16x32_bf16 v[228:231], v[116:119], v[148:151], 0\n v_mul_f32 v200, v200, v192\n "
      "v_mul_f32 v201, v201, v192\n v_mfma_f32_16x16x32_bf16 v[64:67], v[152:155], v[120:123], 0\n v_mul_f32 v202, v202, v192\n v_mul_f32 v203, v203, v192\n "
      "v_mfma_f32_16x16x32_bf16 v[68:71], v[152:155], v[124:127], 0\n v_cvt_pk_bf16_f32 v96, v200, v201\n v_cvt_pk_bf16_f32 v97, v202, v203\n "
      "v_mfma_f32_16x16x32_bf16 v[72:75], v[152:155], v[128:131], 0\n v_mul_f32 v204, v204, v193\n v_mul_f32 v205, v205, v193\n "
      "v_mfma_f32_16x16x32_bf16 v[76:79], v[152:155], v[132:135], 0\n v_mul_f32 v206, v206, v193\n v_mul_f32 v207, v207, v193\n "
      "v_mfma_f32_16x16x32_bf16 v[80:83], v[152:155], v[136:139], 0\n v_cvt_pk_bf16_f32 v100, v204, v205\n v_cvt_pk_bf16_f32 v101, v206, v207\n "
      "v_mfma_f32_16x16x32_bf16 v[84:87], v[152:155], v[140:143], 0\n v_mul_f32 v208, v208, v194\n v_mul_f32 v209, v209, v194\n "
      "v_mfma_f32_16x16x32_bf16 v[88:91], v[152:155], v[144:147], 0\n v_mul_f32 v210, v210, v194\n v_mul_f32 v211, v211, v194\n "
      "v_mfma_f32_16x16x32_bf16 v[92:95], v[152:155], v[148:151], 0\n v_cvt_pk_bf16_f32 v104, v208, v209\n v_cvt_pk_bf16_f32 v105, v210, v211\n "
      OST_(4)
      "v_mul_f32 v64, v64, v192\n v_mul_f32 v65, v65, v192\n v_mul_f32 v66, v66, v192\n v_mul_f32 v67, v67, v192\n v_cvt_pk_bf16_f32 v98, v64, v65\n v_cvt_pk_bf16_f32 v99, v66, v67\n "
      "buffer_store_dwordx4 v[96:99], %[ofo], %[ws], 0 offen offset:0 sc1\n v_pk_mul_f32 v[68:69], v[68:69], v[192:193] op_sel:[0,1]\n "
      "v_pk_mul_f32 v[70:71], v[70:71], v[192:193] op_sel:[0,1]\n v_cvt_pk_bf16_f32 v102, v68, v69\n v_cvt_pk_bf16_f32 v103, v70, v71\n "
      "buffer_store_dwordx4 v[100:103], %[ofo], %[ws], 0 offen offset:256 sc1\n v_pk_mul_f32 v[72:73], v[72:73], v[194:195] op_sel_hi:[1,0]\n "
      "v_pk_mul_f32 v[74:75], v[74:75], v[194:195] op_sel_hi:[1,0]\n v_cvt_pk_bf16_f32 v106, v72, v73\n v_cvt_pk_bf16_f32 v107, v74, v75\n "
      "buffer_store_dwordx4 v[104:107], %[ofo], %[ws], 0 offen offset:2048 sc1\n v_pk_mul_f32 v[76:77], v[76:77], v[194:195] op_sel:[0,1]\n "
      "v_pk_mul_f32 v[78:79], v[78:79], v[194:195] op_sel:[0,1]\n v_pk_mul_f32 v[212:213], v[212:213], v[194:195] op_sel:[0,1]\n "
      "v_pk_mul_f32 v[214:215], v[214:215], v[194:195] op_sel:[0,1]\n v_cvt_pk_bf16_f32 v108, v212, v213\n v_cvt_pk_bf16_f32 v109, v214, v215\n v_cvt_pk_bf16_f32 v110, v76, v77\n "
      "v_cvt_pk_bf16_f32 v111, v78, v79\n buffer_store_dwordx4 v[108:111], %[ofo], %[ws], 0 offen offset:2304 sc1\n v_pk_mul_f32 v[80:81], v[80:81], v[196:197] op_sel_hi:[1,0]\n "
      "v_pk_mul_f32 v[82:83], v[82:83], v[196:197] op_sel_hi:[1,0]\n v_pk_mul_f32 v[216:217], v[216:217], v[196:197] op_sel_hi:[1,0]\n "
      "v_pk_mul_f32 v[218:219], v[218:219], v[196:197] op_sel_hi:[1,0]\n v_cvt_pk_bf16_f32 v176, v216, v217\n v_cvt_pk_bf16_f32 v177, v218, v219\n v_cvt_pk_bf16_f32 v178, v80, v81\n "
      "v_cvt_pk_bf16_f32 v179, v82, v83\n buffer_store_dwordx4 v[176:179], %[ofo], %[ws], s73 offen offset:0 sc1\n v_pk_mul_f32 v[84:85], v[84:85], v[196:197] op_sel:[0,1]\n "
      "v_pk_mul_f32 v[86:87], v[86:87], v[196:197] op_sel:[0,1]\n v_pk_mul_f32 v[220:221], v[220:221], v[196:197] op_sel:[0,1]\n "
      "v_pk_mul_f32 v[222:223], v[222:223], v[196:197] op_sel:[0,1]\n v_cvt_pk_bf16_f32 v180, v220, v221\n v_cvt_pk_bf16_f32 v181, v222, v223\n v_cvt_pk_bf16_f32 v182, v84, v85\n "
      "v_cvt_pk_bf16_f32 v183, v86, v87\n buffer_store_dwordx4 v[180:183], %[ofo], %[ws], s73 offen offset:256 sc1\n v_pk_mul_f32 v[88:89], v[88:89], v[198:199] op_sel_hi:[1,0]\n "
      "v_pk_mul_f32 v[90:91], v[90:91], v[198:199] op_sel_hi:[1,0]\n v_pk_mul_f32 v[224:225], v[224:225], v[198:199] op_sel_hi:[1,0]\n "
      "v_pk_mul_f32 v[226:227], v[226:227], v[198:199] op_sel_hi:[1,0]\n v_cvt_pk_bf16_f32 v184, v224, v225\n v_cvt_pk_bf16_f32 v185, v226, v227\n v_cvt_pk_bf16_f32 v186, v88, v89\n "
      "v_cvt_pk_bf16_f32 v187, v90, v91\n buffer_store_dwordx4 v[184:187], %[ofo], %[ws], s73 offen offset:2048 sc1\n v_pk_mul_f32 v[92:93], v[92:93], v[198:199] op_sel:[0,1]\n "
      "v_pk_mul_f32 v[94:95], v[94:95], v[198:199] op_sel:[0,1]\n v_pk_mul_f32 v[228:229], v[228:229], v[198:199] op_sel:[0,1]\n "
      "v_pk_mul_f32 v[230:231], v[230:231], v[198:199] op_sel:[0,1]\n v_cvt_pk_bf16_f32 v188, v228, v229\n v_cvt_pk_bf16_f32 v189, v230, v231\n v_cvt_pk_bf16_f32 v190, v92, v93\n "
      "v_cvt_pk_bf16_f32 v191, v94, v95\n buffer_store_dwordx4 v[188:191], %[ofo], %[ws], s73 offen offset:2304 sc1\n "
      OST_(5)
      "v_mov_b32 v248, 4\n ds_write_b128 %[lwa], v[96:99] offset:0\n ds_write_b128 %[lwa], v[100:103] offset:256\n ds_write_b128 %[lwa], v[104:107] offset:2048\n "
      "ds_write_b128 %[lwa], v[108:111] offset:2304\n ds_write_b128 %[lwa], v[176:179] offset:8192\n ds_write_b128 %[lwa], v[180:183] offset:8448\n "
      "ds_write_b128 %[lwa], v[184:187] offset:10240\n ds_write_b128 %[lwa], v[188:191] offset:10496\n ds_write_b32 %[mfl], v248\n s_branch .Lgd\n .Lrt0:\n s_add_u32 %[nr], %[nr], 1\n "
      "s_bitcmp1_b32 %[nr], 21\n s_cbranch_scc1 .Lok0\n s_sleep 2\n v_xor_b32 v248, 0x6000, v252\n v_xor_b32 v249, 0x600, v253\n global_load_dwordx2 v[240:241], v249, %[zb] sc1\n "
      "global_load_dwordx4 v[80:83], v248, %[kb] offset:-4096 sc1\n global_load_dwordx4 v[84:87], v248, %[kb] offset:-2048 sc1\n global_load_dwordx4 v[90:93], v248, %[kb] offset:-3072 sc1\n "
      "global_load_dwordx4 v[94:97], v248, %[kb] offset:-1024 sc1\n global_load_dwordx4 v[100:103], v248, %[kb] offset:0 sc1\n global_load_dwordx4 v[104:107], v248, %[kb] offset:2048 sc1\n "
      "global_load_dwordx4 v[110:113], v248, %[kb] offset:1024 sc1\n global_load_dwordx4 v[114:117], v248, %[kb] offset:3072 sc1\n s_waitcnt vmcnt(0)\n s_branch .Lck0\n .Lrt1:\n "
      "s_add_u32 %[nr], %[nr], 1\n s_bitcmp1_b32 %[nr], 21\n s_cbranch_scc1 .Lok1\n s_sleep 2\n v_xor_b32 v248, 0x4000, v252\n v_xor_b32 v249, 0x400, v253\n "
      "global_load_dwordx2 v[242:243], v249, %[zb] sc1\n global_load_dwordx4 v[120:123], v248, %[kb] offset:-4096 sc1\n global_load_dwordx4 v[124:127], v248, %[kb] offset:-2048 sc1\n "
      "global_load_dwordx4 v[130:133], v248, %[kb] offset:-3072 sc1\n global_load_dwordx4 v[134:137], v248, %[kb] offset:-1024 sc1\n "
      "global_load_dwordx4 v[140:143], v248, %[kb] offset:0 sc1\n global_load_dwordx4 v[144:147], v248, %[kb] offset:2048 sc1\n global_load_dwordx4 v[150:153], v248, %[kb] offset:1024 sc1\n "
      "global_load_dwordx4 v[154:157], v248, %[kb] offset:3072 sc1\n s_waitcnt vmcnt(0)\n s_branch .Lck1\n .Lrt2:\n s_add_u32 %[nr], %[nr], 1\n s_bitcmp1_b32 %[nr], 21\n "
      "s_cbranch_scc1 .Lok2\n s_sleep 2\n v_xor_b32 v248, 0x2000, v252\n v_xor_b32 v249, 0x200, v253\n global_load_dwordx2 v[244:245], v249, %[zb] sc1\n "
      "global_load_dwordx4 v[160:163], v248, %[kb] offset:-4096 sc1\n global_load_dwordx4 v[164:167], v248, %[kb] offset:-2048 sc1\n "
      "global_load_dwordx4 v[170:173], v248, %[kb] offset:-3072 sc1\n global_load_dwordx4 v[174:177], v248, %[kb] offset:-1024 sc1\n "
      "global_load_dwordx4 v[180:183], v248, %[kb] offset:0 sc1\n global_load_dwordx4 v[184:187], v248, %[kb] offset:2048 sc1\n global_load_dwordx4 v[190:193], v248, %[kb] offset:1024 sc1\n "
      "global_load_dwordx4 v[194:197], v248, %[kb] offset:3072 sc1\n s_waitcnt vmcnt(0)\n s_branch .Lck2\n .Lrt3:\n s_add_u32 %[nr], %[nr], 1\n s_bitcmp1_b32 %[nr], 21\n "
      "s_cbranch_scc1 .Lok3\n s_sleep 2\n global_load_dwordx2 v[246:247], v253, %[zb] sc1\n global_load_dwordx4 v[200:203], v252, %[kb] offset:-4096 sc1\n "
      "global_load_dwordx4 v[204:207], v252, %[kb] offset:-2048 sc1\n global_load_dwordx4 v[210:213], v252, %[kb] offset:-3072 sc1\n "
      "global_load_dwordx4 v[214:217], v252, %[kb] offset:-1024 sc1\n global_load_dwordx4 v[220:223], v252, %[kb] offset:0 sc1\n "
      "global_load_dwordx4 v[224:227], v252, %[kb] offset:2048 sc1\n global_load_dwordx4 v[230:233], v252, %[kb] offset:1024 sc1\n "
      "global_load_dwordx4 v[234:237], v252, %[kb] offset:3072 sc1\n s_branch .Lg3\n .Lgd:\n "
      OSTE
      [nr] "=&s"(nr)
      : [gx] "s"(gx), [now] "s"(now), [d0] "s"(dg[0]), [d1] "s"(dg[1]), [d2] "s"(dg[2]), [d3] "s"(dg[3]), [kb] "s"(kvb), [zb] "s"(zb), [ws] "s"(wsr), [om] "s"(lw ? 0x0Au : 0x25u),
        [lv] "v"(lv16), [zo] "v"(zoff), [mfl] "v"(mfl), [afl] "v"(afl), [thr] "v"(thr), [pa] "v"(pa), [ofo] "v"(ofo), [lwa] "v"(lwa), [sel] "v"(sel)
      : "memory", "scc", "vcc", "s72", "s73", "s74", "s75", "s76",
        "v64", "v65", "v66", "v67", "v68", "v69", "v70", "v71", "v72", "v73", "v74", "v75", "v76", "v77", "v78", "v79", "v80", "v81", "v82", "v83", "v84", "v85", "v86", "v87", "v88", "v89",
        "v90", "v91", "v92", "v93", "v94", "v95", "v96", "v97", "v98", "v99", "v100", "v101", "v102", "v103", "v104", "v105", "v106", "v107", "v108", "v109", "v110", "v111", "v112", "v113",
        "v114", "v115", "v116", "v117", "v118", "v119", "v120", "v121", "v122", "v123", "v124", "v125", "v126", "v127", "v128", "v129", "v130", "v131", "v132", "v133", "v134", "v135",
        "v136", "v137", "v138", "v139", "v140", "v141", "v142", "v143", "v144", "v145", "v146", "v147", "v148", "v149", "v150", "v151", "v152", "v153", "v154", "v155", "v156", "v157",
        "v158", "v159", "v160", "v161", "v162", "v163", "v164", "v165", "v166", "v167", "v168", "v169", "v170", "v171", "v172", "v173", "v174", "v175", "v176", "v177", "v178", "v179",
        "v180", "v181", "v182", "v183", "v184", "v185", "v186", "v187", "v188", "v189", "v190", "v191", "v192", "v193", "v194", "v195", "v196", "v197", "v198", "v199", "v200", "v201",
        "v202", "v203", "v204", "v205", "v206", "v207", "v208", "v209", "v210", "v211", "v212", "v213", "v214", "v215", "v216", "v217", "v218", "v219", "v220", "v221", "v222", "v223",
        "v224", "v225", "v226", "v227", "v228", "v229", "v230", "v231", "v232", "v233", "v234", "v235", "v236", "v237", "v238", "v239", "v240", "v241", "v242", "v243", "v244", "v245",
        "v246", "v247", "v248", "v249", "v250", "v251", "v252", "v253", "v254");
    STMP(4);
    STFLUSH();
    asm volatile(".Lend_l:");
  }

}

extern "C" void kernel_launch(void* const* d_in, const int* in_sizes, int n_in, void* d_out, int out_size,
                              void* d_ws, size_t ws_size, hipStream_t stream) {
  (void)hipFuncSetAttribute((const void*)fused, hipFuncAttributeMaxDynamicSharedMemorySize, LDS_SZ);
  fused<<<256, 512, LDS_SZ, stream>>>((const u16*)d_in[0], (const u16*)d_in[1], (const u16*)d_in[4], (char*)d_ws, (const u16*)d_in[2],
                                      (const u16*)d_in[5], (const u16*)d_in[6], (const u16*)d_in[7], (u16*)d_out);
}
```

```cpp
#include <hip/hip_runtime.h>
#include <stdint.h>

typedef unsigned short u16;
typedef short s4 __attribute__((ext_vector_type(4)));
typedef short s8 __attribute__((ext_vector_type(8)));
typedef float f4 __attribute__((ext_vector_type(4)));
typedef float f16v __attribute__((ext_vector_type(16)));
typedef float f2 __attribute__((ext_vector_type(2)));
typedef unsigned u4 __attribute__((ext_vector_type(4)));
typedef unsigned u2 __attribute__((ext_vector_type(2)));
typedef unsigned u8v __attribute__((ext_vector_type(8)));
typedef __bf16 bf2 __attribute__((ext_vector_type(2)));
#define LAS __attribute__((address_space(3)))
#define DEV __device__ __forceinline__

constexpr int D = 1024, F = 3584;
#define NOMOE 0
#define KD 224
#define KS 222
#define OD 184
#define OHP 1
#define FS 128
#define GSL 8
#define YSL 16
#define TP1 0
#define TP2 3
#define TLBW 0
#define FULL 1
#define HPRIO 1
#define OWN 1
#define NOPOIS 0
#define CAP 7
#define RD 4
#define DYN 1
#define JX 0
#define NOSKIP 0
#define GRP 8
#define ADV 15
#define HB 1
#define RT 1
#define WD 0
#define WU1 12
#define WU2 22
#define PL 0
#define PO 0
#define WG 0
#define DS 0
#define D1 16
#define D2 2
#define HKS 2
#define LW1 1
#define HW1 2
#define HS0 0
#define GPV 2
#define XO 1
#define TP0 3
#define G1 0x31
#define G1S 4
#define G2S 8
#define TD 1
#define YD 170
#define WU0 4
#define WE 16
#define HT 115
#define CF 19
#define KL 145
#define OL 115
#define CL 0
#define WS0 0
#define OM 0
#define HG 128
#define U0 0
#define U3 5
#define TL 0
#define MH 0
#define HWT 3
#define YG 0
#define GG 1
#define PV 1
#define U1 4
#define U2 2
#define QD 1
#define LP0 3
#define LP1 3
#define WA 8
#define PW 3
#define WB 28
#define PF 1
#define PFS 6
#define PA 0
#define PAS 0
#define LPL 0
#define WP 1
#define E0 0
#define ES 0
#define IL 1
#define LW4 1
#define WF 1
#define LW3 2
#define ROT 0
#define LF 0
#define SL5 1
#define GE 0
#define ZH 1
#define TW 0
#define TWS 0
#define TCH 8
#define SPW 50
#define SP2 0
#define SP0 1
#define SPS 35
#define TH 0
#define TE 1
#define TN 4
#define TS 1
#define SE 1
#define TLP 4
#define SSL 64
#define T2 0
#define GI 0
#define GO 0
#define ZF 0
#define GW 1
#define F5 1
#define WT 0xDCB87654FEA93210ull
#define PE 3
#define GX 0
#define GS 0
#define C2 1
#define CB 4
#define DE 0
#define HS1 0
#define WD0 0
#define W00 0
#define KX 50

constexpr unsigned WS_E = 0;
constexpr unsigned WS_CT = 524288;
constexpr unsigned WS_CAND = 12288;
constexpr unsigned WS_RG = 49152;
constexpr unsigned WS_ST = 65536;
constexpr unsigned WS_HK = 589824;
constexpr unsigned WS_ZP = 131072;
constexpr unsigned WS_CS = 196608;
constexpr unsigned WS_OH = 458752;
constexpr unsigned WS_KVP = 1u << 20;
constexpr unsigned WS_GP = 2u << 20;
constexpr unsigned WS_YP = 3u << 20;
constexpr unsigned WS_OF = 5u << 20;
constexpr unsigned WS_SZ = 9u << 20;
#define NR 4
#define RGO(tag) ((64u << 20) + ((tag) & (NR - 1u)) * (10u << 20))
#define MKWS(tag) char* const wsx = ws + RGO(tag); const __amdgpu_buffer_rsrc_t wsr = __builtin_amdgcn_make_buffer_rsrc(wsx, 0, WS_SZ, 0x00020000)

constexpr int STG = 40960;
constexpr int L_KVX = 0;
constexpr int L_PQ = 12288;
constexpr int L_M = 28672, L_RS = L_M + 512, L_CS = L_RS + 512, L_SEL = L_CS + 768, L_FL = L_SEL + 64;
constexpr int L_HX = 30720, L_RL = 31744, L_WO = 32768;
constexpr int LDS_SZ = 163840;
constexpr unsigned POIS = 0xFFFFFFFFu;

DEV unsigned pk2(float a, float b) { bf2 v; v[0] = (__bf16)a; v[1] = (__bf16)b; return __builtin_bit_cast(unsigned, v); }
DEV float lo(unsigned u) { return __uint_as_float(u << 16); }
DEV float hi(unsigned u) { return __uint_as_float(u & 0xFFFF0000u); }
DEV float rbf(float a) { return lo(pk2(a, a)); }
DEV float dot2(unsigned a, unsigned b, float c) { return __builtin_amdgcn_fdot2_f32_bf16(__builtin_bit_cast(bf2, a), __builtin_bit_cast(bf2, b), c, false); }
#define RD128(dst, addr, OFF) asm volatile("ds_read_b128 %0, %1 offset:%2" : "=v"(dst) : "v"(addr), "i"(OFF) : "memory")
#define RDTR(dst, addr, OFF) asm volatile("ds_read_b64_tr_b16 %0, %1 offset:%2" : "=v"(dst) : "v"(addr), "i"(OFF) : "memory")
#define AL8() asm volatile(".p2align 3")
#define SBAR() __builtin_amdgcn_sched_barrier(0)
#define CAT(x, y) __builtin_shufflevector(x, y, 0, 1, 2, 3, 4, 5, 6, 7)
#define WAITV(n) asm volatile("s_waitcnt vmcnt(%0)" ::"i"(n) : "memory")
#define WAITL(n) asm volatile("s_waitcnt lgkmcnt(%0)" ::"i"(n) : "memory")
#define MEMB() asm volatile("" ::: "memory")
#define SLEEP(n) asm volatile("s_sleep %0" ::"i"(n) : "memory")
#define BAR() asm volatile("s_barrier" ::: "memory")
#define GL0(vo, sb, m0v) asm volatile("s_mov_b32 m0, %2\n s_nop 0\n global_load_lds_dwordx4 %0, %1" ::"v"(vo), "s"(sb), "s"(m0v) : "memory")
#define GLI(vo, sb, OFF) asm volatile("global_load_lds_dwordx4 %0, %1 offset:%2" ::"v"(vo), "s"(sb), "i"(OFF) : "memory")
#define GL4(v, sb, mm, MO) do { asm volatile("s_add_i32 m0, %2, %3\n s_nop 0\n global_load_lds_dwordx4 %0, %1" ::"v"((v)[0]), "s"(sb), "s"(mm), "i"(MO) : "memory", "scc"); \
      GLI((v)[1], sb, 1024); GLI((v)[2], sb, 2048); GLI((v)[3], sb, 3072); } while (0)
#define VFW(i) ((8 * (i) + (l >> 3)) * 2048 + (((l & 7) ^ (((l >> 4) & 1) << 2)) * 16) + (3 - (i)) * 1024)
#define MFMA16(a, b, c) __builtin_amdgcn_mfma_f32_16x16x32_bf16(a, b, c, 0, 0, 0)
#define S8(x) __builtin_bit_cast(s8, x)
#define LD128(off) __builtin_amdgcn_raw_buffer_load_b128(wsr, (off), 0, 16)
#define LD64(off) __builtin_amdgcn_raw_buffer_load_b64(wsr, (off), 0, 16)
#define ST128(v, off) __builtin_amdgcn_raw_buffer_store_b128((v), wsr, (off), 0, 16)
#define ST64(v, off) __builtin_amdgcn_raw_buffer_store_b64((v), wsr, (off), 0, 16)
#define OFA(i) (WS_OF + ((mb * 32 + hd) * 8 + (i)) * 1024 + l * 16)
#define POISON(i) ST128(pz, (i) < 2 ? WS_KVP + (hd * 16 + mb) * 2048 + (i) * 1024 + l * 16 : (i) == 2 ? WS_ZP + (hd * 16 + mb) * 128 + (l & 7) * 16 : OFA((i) - 3))
#define NOW() ((unsigned)__builtin_amdgcn_s_memrealtime())
constexpr unsigned SPIN = 1u << 21;
DEV const char* uptr(const char* p) {
  unsigned long long v = (unsigned long long)p;
  unsigned lo_ = __builtin_amdgcn_readfirstlane((unsigned)v), hi_ = __builtin_amdgcn_readfirstlane((unsigned)(v >> 32));
  return (const char*)(((unsigned long long)hi_ << 32) | lo_);
}
DEV const char* padd(const char* p, unsigned o) {
  unsigned lo_, hi_;
  asm("s_add_u32 %0, %2, %4\n s_addc_u32 %1, %3, 0" : "=&s"(lo_), "=s"(hi_) : "s"((unsigned)(unsigned long long)p), "s"((unsigned)((unsigned long long)p >> 32)), "s"(o) : "scc");
  return (const char*)(((unsigned long long)hi_ << 32) | lo_);
}
DEV void lsig(unsigned addr, unsigned val) { asm volatile("s_waitcnt lgkmcnt(0)\n ds_write_b32 %0, %1" ::"v"(addr), "v"(val) : "memory"); }
template <int SL = 0> DEV void lwait(unsigned addr, unsigned mask, unsigned target) {
  _Pragma("unroll 1") for (unsigned it = 0; it < SPIN; ++it) {
    unsigned v;
    asm volatile("ds_read_b32 %0, %1\n s_waitcnt lgkmcnt(0)" : "=v"(v) : "v"(addr) : "memory");
    if (((unsigned)__ballot(v < target) & mask) == 0u) break;
    if (SL) SLEEP(SL);
  }
}
DEV unsigned nopois(unsigned a) { return min(a, POIS - 1u); }
DEV u4 tagf(f4 v, unsigned tag) {
  u4 r;
#pragma unroll
  for (int e = 0; e < 4; ++e) r[e] = __builtin_amdgcn_perm(__float_as_uint(v[e]), tag, 0x07060500u + e);
  return r;
}
DEV void untag(u4& v, unsigned tag, unsigned& bad) {
#pragma unroll
  for (int e = 0; e < 4; ++e) v[e] ^= (tag >> (8 * e)) & 255u;
  bad |= v[0] | v[1]; bad |= v[2] | v[3];
}
DEV unsigned umax3(unsigned a, unsigned b, unsigned c) { return max(max(a, b), c); }
template <int C> DEV float dppf(float v) { return __int_as_float(__builtin_amdgcn_update_dpp(0, __float_as_int(v), C, 0xf, 0xf, false)); }
DEV unsigned wave_max(unsigned v) {
  v = max(v, (unsigned)__builtin_amdgcn_update_dpp(0, (int)v, 0x111, 0xf, 0xf, false));
  v = max(v, (unsigned)__builtin_amdgcn_update_dpp(0, (int)v, 0x112, 0xf, 0xf, false));
  v = max(v, (unsigned)__builtin_amdgcn_update_dpp(0, (int)v, 0x114, 0xf, 0xf, false));
  v = max(v, (unsigned)__builtin_amdgcn_update_dpp(0, (int)v, 0x118, 0xf, 0xf, false));
  v = max(v, (unsigned)__builtin_amdgcn_update_dpp(0, (int)v, 0x142, 0xa, 0xf, false));
  v = max(v, (unsigned)__builtin_amdgcn_update_dpp(0, (int)v, 0x143, 0xc, 0xf, false));
  return (unsigned)__builtin_amdgcn_readlane((int)v, 63);
}
DEV unsigned phi2(f2 u, f2 rs, f2 rl) {
  f2 a = u * rl, p = u * rs + 1.f, r;
  r[0] = __builtin_amdgcn_fmed3f(__builtin_amdgcn_exp2f(a[0]), p[0], 1.f);
  r[1] = __builtin_amdgcn_fmed3f(__builtin_amdgcn_exp2f(a[1]), p[1], 1.f);
  r += 1e-6f;
  return pk2(r[0], r[1]);
}

#define STDECL
#undef HB
#define HB 0
#define BSTV cw
#define STMP(i)
#define BST(i)
#define STFLUSH()
#define RETRY()
#define RETRY2()

DEV float half_sum(float v) {
  asm volatile("s_nop 1\n v_add_f32_dpp %0, %0, %0 row_ror:8 row_mask:0xf bank_mask:0xf\n s_nop 1\n v_add_f32_dpp %0, %0, %0 row_ror:4 row_mask:0xf bank_mask:0xf\n"
               "s_nop 1\n v_add_f32_dpp %0, %0, %0 row_ror:2 row_mask:0xf bank_mask:0xf\n s_nop 1\n v_add_f32_dpp %0, %0, %0 row_ror:1 row_mask:0xf bank_mask:0xf\n s_nop 1" : "+v"(v));
  auto r = __builtin_amdgcn_permlane16_swap(__float_as_uint(v), __float_as_uint(v), false, false);
  return __uint_as_float(r[0]) + __uint_as_float(r[1]);
}

constexpr unsigned long long ownat(unsigned long long a, unsigned long long b, unsigned long long c, unsigned long long d) {
  unsigned long long r = 0;
  for (int h = 0; h < 16; ++h) {
    const unsigned long long t = (h & 2) ? ((h & 1) ? d : c) : ((h & 1) ? b : a);
    for (int i = 0; i < 16; ++i) if ((int)((t >> (4 * i)) & 15) == h) r |= (unsigned long long)i << (4 * h);
  }
  return r;
}
constexpr int wtat(int i) { return (int)(WT >> (4 * i)) & 15; }
constexpr int wtmin(int a, int b) { int m = 99; for (int i = a; i < b; ++i) m = wtat(i) < m ? wtat(i) : m; return m; }
constexpr int wtmax(int a, int b) { int m = 0; for (int i = a; i < b; ++i) m = wtat(i) > m ? wtat(i) : m; return m; }
struct FQ { s8 a[4]; s4 b[12]; };
template <bool B> struct BC { static constexpr bool v = B; };
template <int I> struct IC { static constexpr int v = I; };

__global__ __launch_bounds__(512) void fused(const u16* __restrict__ x, const u16* __restrict__ wqkv, const u16* __restrict__ wrt,
                                             char* __restrict__ ws_, const u16* __restrict__ wo_, const u16* __restrict__ wg,
                                             const u16* __restrict__ wu, const u16* __restrict__ wd, u16* __restrict__ out) {
  extern __shared__ __attribute__((aligned(16))) char lds[];
  const char *kx, *kq, *kr, *wo; char* ws;
  asm volatile("s_load_dwordx2 %0, %5, 0\n s_load_dwordx2 %1, %5, 8\n s_load_dwordx2 %2, %5, 16\n s_load_dwordx2 %3, %5, 24\n s_load_dwordx2 %4, %5, 32"
               : "=&s"(kx), "=&s"(kq), "=&s"(kr), "=&s"(ws), "=&s"(wo) : "s"(__builtin_amdgcn_kernarg_segment_ptr()));
  SBAR();
  const int w = __builtin_amdgcn_readfirstlane(threadIdx.x >> 6), l = threadIdx.x & 63;
  STDECL;
  STMP(0);
  const int g = blockIdx.x, xcd = g & 7, j = g >> 3, ii = j >> 2;
  const int mb = (xcd & 1) * 8 + ii, hp = (xcd >> 1) * 4 + (j & 3);
  unsigned cw, cwo = (l * 32 + j) * 64; unsigned long long cwb;
  asm volatile("s_getpc_b64 %2\n.Lcode_here:\n v_min_u32 %1, max(.Lend_c - .Lcode_here, .Lend_l - .Lcode_here, .Lend_h - .Lcode_here, .Lend_s - .Lcode_here) - 4, %1\n"
               "s_cmp_lg_u32 %3, 7\n s_cbranch_scc1 .Lskip_w\n global_load_dword %0, %1, %2\n.Lskip_w:" : "=&v"(cw), "+v"(cwo), "=&s"(cwb) : "s"(w) : "memory", "scc");
  SBAR();
  const int lw = w & 1, rot = ROT == 0 ? 2 * xcd + 4 * (ii & 3) : ROT == 1 ? 2 * xcd + 8 * (ii & 1) : ROT == 2 ? 2 * xcd : ROT == 3 ? 0 : ROT == 4 ? 4 * (xcd & 1) + 8 * (ii & 1) : ROT == 7 ? 2 * ii + (xcd & 1) + 2 * (xcd & 6) : ROT == 8 ? 2 * ii + 2 * (xcd & 6) : ROT == 9 ? 2 * ii + (xcd & 1) + 2 * (xcd & 6) + 8 * (j & 1) : ROT == 10 ? 2 * ii + (xcd & 1) : 2 * (xcd & 1) + 4 * (ii & 3);
  const unsigned ldsb = (unsigned)(size_t)(LAS char*)lds;
  unsigned va[4];
#pragma unroll
  for (int i = 0; i < 4; ++i) va[i] = (8 * i + (l >> 3)) * 2048 + (((l & 7) ^ ((i & 1) * 4 + (l >> 4))) * 16) + (3 - i) * 1024;
  const int ao = (mb * 128 + lw * 64) * 2048 - 3072;
  long long ao0 = ao + (rot & 15) * 128;
  unsigned mba = ldsb + lw * 8192;
  const int srow = mb * 128 + hp * 8 + 2 * w + (l >> 5);
  unsigned xo = srow * 2048 + (l & 31) * 16, ro = (l & 31) * 16;
  asm volatile("" : "+v"(va[0]), "+v"(va[1]), "+v"(va[2]), "+v"(va[3]), "+v"(xo), "+v"(ro));
  asm volatile("s_waitcnt lgkmcnt(0)" : "+s"(kx), "+s"(kq), "+s"(kr), "+s"(ws), "+s"(wo), "+s"(ao0), "+s"(mba));
  SBAR();
  asm volatile("s_sub_u32 m0, %6, 4\n s_cmp_gt_u32 m0, 1\n s_cbranch_scc1 .Lskip_l\n s_mov_b32 m0, %5\n s_nop 0\n global_load_lds_dwordx4 %0, %4\n global_load_lds_dwordx4 %1, %4 offset:1024\n"
               "global_load_lds_dwordx4 %2, %4 offset:2048\n global_load_lds_dwordx4 %3, %4 offset:3072\n.Lskip_l:"
               :: "v"(va[0]), "v"(va[1]), "v"(va[2]), "v"(va[3]), "s"(kx + ao0), "s"(mba), "s"(w) : "memory", "scc");
  u4 xa[4], rw[4]; unsigned ep;
  asm volatile("s_cmp_gt_u32 %14, 3\n s_cbranch_scc1 .Lskip_x\n"
               "global_load_dwordx4 %0, %9, %11\n global_load_dwordx4 %1, %9, %11 offset:512\n global_load_dwordx4 %2, %9, %11 offset:1024\n global_load_dwordx4 %3, %9, %11 offset:1536\n"
               "global_load_dwordx4 %4, %10, %12\n global_load_dwordx4 %5, %10, %12 offset:512\n global_load_dwordx4 %6, %10, %12 offset:1024\n global_load_dwordx4 %7, %10, %12 offset:1536\n"
               ".Lskip_x:\n s_load_dword %8, %13, 0"
               : "=&v"(xa[0]), "=&v"(xa[1]), "=&v"(xa[2]), "=&v"(xa[3]), "=&v"(rw[0]), "=&v"(rw[1]), "=&v"(rw[2]), "=&v"(rw[3]), "=&s"(ep)
               : "v"(xo), "v"(ro), "s"(kx), "s"(kr), "s"(ws), "s"(w) : "memory", "scc");
#define GETTAG(t) unsigned t; asm volatile("s_waitcnt lgkmcnt(0)\n s_add_u32 %0, %1, 1" : "=s"(t) : "s"(ep) : "memory", "scc")
  SBAR();
#define PTAB 0xA98FEDC1045B7623ull
#define PHT 0x6624924999924924ull
#define CX 4
  const bool sp = (hp & 7) == 3;
  const int nb = hp;
#define PT0 PTAB
#define PT1 PTAB
#define PT2 PTAB
#define PT3 PTAB
  auto mktab = [&]() {
    const unsigned long long t = (hp & 2) ? ((hp & 1) ? PT3 : PT2) : ((hp & 1) ? PT1 : PT0), m = ~0ull << (4 * ((int)(ownat(PT0, PT1, PT2, PT3) >> (4 * hp)) & 15));
    unsigned long long r = (t & (m << 4)) | ((t & ~m) << 4) | (unsigned)hp;
    asm volatile("" : "+s"(r));
    return r;
  };
#define PAIRAT(p) ((int)(ptab >> (4 * (p))) & 15)
#define HEADAT(s) (2 * PAIRAT((s) >> 1) + ((s) & 1))
  const int ph = (int)(PHT >> (2 * j)) & 3;
#define HQT 0xDC1A09806572043Bull
#define CXA 1
#define CHUNK(b) ((b) < 14 ? 4 * (b) + 3 : ((b) - 14) + ((((b) - 14) * 43) >> 7))
  const unsigned adFL = ldsb + L_FL + 4 * (l & 7), myFL = ldsb + L_FL + 4 * w;
  const __amdgpu_buffer_rsrc_t wse = __builtin_amdgcn_make_buffer_rsrc(ws, 0, 64, 0x00020000);
  const int r15 = l & 15, g4 = l >> 4, q = (l & 15) >> 2, p = l & 3;
  unsigned pfo = 0, pfd = 0, pfl;
#define PFL(k) "s_load_dwordx2 s[100:101], %3, %1 offset:" #k " * 2048 + 60\n"
  auto spf = [&](const char* pb) {
    unsigned t;
    if (TCH == 3) asm volatile("s_getreg_b32 %0, hwreg(HW_REG_IB_STS, 8, 4)\n s_or_b32 %0, %0, %2\n s_cbranch_scc1 1f\n" PFL(0) PFL(1) PFL(2) PFL(3) PFL(4) PFL(5) PFL(6)
                 "s_cmp_eq_u32 %1, %4\n s_cselect_b32 %2, 1, 0\n s_add_u32 %1, %1, 7 * 2048\n s_min_u32 %1, %1, %4\n1:"
                 : "=&s"(t), "+s"(pfo), "+s"(pfd) : "s"(pb), "s"(pfl) : "scc", "s100", "s101");
  };
#define PFB(n, s) const char* pfb = wo + hp * 128 + (ii * 128 + (s) + (w & 1) * (n)) * 2048; pfl = ((n) - 7) * 2048; asm volatile("" : "+s"(pfb), "+s"(pfl))

  if (w != 4 && w != 5) {
  if (w >= 6) {
    const int mw = w - 6;
    unsigned pf = 0;
    if (PF && !(xcd & 2) && !sp && (mw == 0 || PF == 3 || (PF == 2 && (ii >> 2)))) {
      const unsigned po = (((rot + 2 - 2 * mw + (ii >> 2)) & 15) * 64 + l) * 6144 + (j & 3) * 2048 + (hp | 3) * 128;
      if (PFS) SLEEP(PFS);
      asm volatile("global_load_dword %0, %1, %2" : "+v"(pf) : "v"(po), "s"(kq) : "memory");
    }
    if (PA && mw == 1) {
      const unsigned po = (mb * 128 + (j & 3) * 32 + (l & 31)) * 2048 + (l >> 5) * 1024 + 384;
      if (PAS) SLEEP(PAS);
      asm volatile("global_load_dword %0, %1, %2" : "+v"(pf) : "v"(po), "s"(kx) : "memory");
    }
    GETTAG(tag);
    const int bq = ((int)(HQT >> (4 * hp)) & 15) * 4 + (ii & 3), ksp = (xcd & 1) * 2 + (ii >> 2);
    const int chunk = CHUNK(bq), bg = (CXA || bq < 14) ? bq ^ (w & 1) : bq, chg = CHUNK(bg);
    auto helper = [&](auto SP) {
    constexpr bool spare = decltype(SP)::v, moe = !spare && !NOMOE;
    int nbar = 0;
#define TICK() do { if (HB) { BST(nbar); asm volatile("" : "+v"(BSTV)); } SBAR(); BAR(); SBAR(); ++nbar; } while (0)
#define ZFL() do { if (ZH) asm volatile("ds_write_b32 %0, %1\n s_waitcnt lgkmcnt(0)" ::"v"(adFL), "v"(0u) : "memory"); } while (0)
#define TICKP() do { TICK(); spf(pfb); SBAR(); } while (0)
#define TICKQ() do { TICK(); spf(pfb); if (SP2) { SLEEP(SP2); spf(pfb); } SBAR(); } while (0)
#define DTICK(lim) do { if (nbar < (lim)) TICK(); else SLEEP(2); } while (0)
    const int hd = hp * 2 + mw;
    const u4 pz = (u4)(POIS);
    MKWS(tag);
    const int np = l & 7, kset = (l >> 3) & 1;
    u4 gw[4][8], dw[2][8];
    const __amdgpu_buffer_rsrc_t wgr = __builtin_amdgcn_make_buffer_rsrc((void*)(mw ? wu : wg), 0, D * F * 2, 0x00020000);
    const __amdgpu_buffer_rsrc_t wdr = __builtin_amdgcn_make_buffer_rsrc((void*)wd, 0, D * F * 2, 0x00020000);
    const __amdgpu_buffer_rsrc_t wor = __builtin_amdgcn_make_buffer_rsrc((void*)wo, 0, D * D * 2, 0x00020000);
    const unsigned gvo = ((256 * ksp + 32 * kset + 8 * g4) * F + 64 * chg + 8 * np) * 2;
    const unsigned dvo = ((64 * chunk + 8 * g4) * D + 256 * ksp + 128 * mw + 8 * r15) * 2;
    auto burst = [&](int k, int i0 = 0, int i1 = 16) {
      if (moe) {
#pragma unroll
        for (int i = i0; i < i1; ++i) {
          if (k < 2) gw[(2 * k + (i >> 3)) & 3][i & 7] = __builtin_amdgcn_raw_buffer_load_b128(wgr, gvo, (128 * k + 64 * (i >> 3) + (i & 7)) * F * 2, 2);
          else dw[i >> 3][i & 7] = __builtin_amdgcn_raw_buffer_load_b128(wdr, dvo, (32 * (i >> 3) + (i & 7)) * D * 2, 2);
        }
      }
    };
    auto transp = [&](u4(&v)[8]) {
      if (moe) {
        u4 f[8];
#pragma unroll
        for (int c = 0; c < 8; ++c)
#pragma unroll
          for (int e = 0; e < 4; ++e) {
            const unsigned ev = v[2 * e][c >> 1], od = v[2 * e + 1][c >> 1];
            f[c][e] = (c & 1) ? __builtin_amdgcn_perm(od, ev, 0x07060302u) : __builtin_amdgcn_perm(od, ev, 0x05040100u);
          }
#pragma unroll
        for (int c = 0; c < 8; ++c) { v[c] = f[c]; asm volatile("" : "+v"(v[c])); }
      }
    };
    auto pois = [&]() {
      ST128(pz, WS_KVP + (hd * 16 + mb) * 2048 + l * 16); ST128(pz, WS_KVP + (hd * 16 + mb) * 2048 + 1024 + l * 16); ST128(pz, WS_ZP + (hd * 16 + mb) * 128 + (l & 7) * 16);
    };
    unsigned wt0 = 0, wt1 = 0, wt2 = 0, wt3 = 0;
    auto touch = [&](int part = 3) {
      const unsigned to = (j * 32 + mw * 16 + (l >> 3)) * 2048 + (xcd >> 1) * 512 + (l & 7) * 64;
      if (TH) wt0 = __builtin_amdgcn_raw_buffer_load_b32(wor, to, (xcd & 1) * 16384, 0);
      else {
        if (part & 1) wt0 = __builtin_amdgcn_raw_buffer_load_b32(wor, to, 0, 0);
        if (part & 2) wt1 = __builtin_amdgcn_raw_buffer_load_b32(wor, to, 16384, 0);
      }
      if (TLBW) {
        const __amdgpu_buffer_rsrc_t outr = __builtin_amdgcn_make_buffer_rsrc(out, 0, 2048 * D * 2, 0x00020000);
        wt2 = __builtin_amdgcn_raw_buffer_load_b32(outr, (mb * 128 + mw * 64 + (l & 3) * 16) * 2048 + nb * 128, 0, 0);
        wt3 = __builtin_amdgcn_raw_buffer_load_b32(wsr, (l & 1) ? WS_GP + g * 4096 + mw * 2048 : WS_YP + (g >> 2) * 32768 + (l & 2) * 8192, 0, 0);
      }
    };
    unsigned teo;
    {
      const int pr = l < 8 ? (hp & 12) + (l & 3) : (int)(PTAB >> (4 * ((l >> 3) - 1))) & 15;
      teo = l < 8 * TN + 8 && mw == 0 ? (pr * 64 + j * 2 + ((l >> 2) & 1)) * 2048 + (xcd >> 1) * 512 + (l & 3) * 128 : D * D * 2;
      if (T2) {
        const int pr2 = l < 8 ? (hp & 12) + ((l >> 1) & 3) : pr;
        teo = l < 8 * TN + 8 ? (pr2 * 64 + j * 2 + mw) * 2048 + (xcd >> 1) * 512 + (l & 7) * 64 : D * D * 2;
      }
      asm volatile("" : "+v"(teo));
    }
    auto tearly = [&]() { wt0 = __builtin_amdgcn_raw_buffer_load_b32(wor, teo, 0, 0); };
    unsigned tlo;
    {
      const int ps = TN + (l >> 3) * 2 + mw, pr = (int)(PTAB >> (4 * (ps & 15))) & 15;
      tlo = ps < 16 ? (pr * 64 + j * 2 + ((l >> 2) & 1)) * 2048 + (xcd >> 1) * 512 + (l & 3) * 128 : D * D * 2;
      asm volatile("" : "+v"(tlo));
    }
    auto tlate = [&]() { wt1 = __builtin_amdgcn_raw_buffer_load_b32(wor, tlo, 0, 0); };
    s4 sb[24];
    auto colrd = [&](int h) {
      const unsigned bo = ldsb + (1 + h) * STG + 16384 + (8 * g4 + q) * 128 + (p & 1) * 8;
      const int sw = ((q >> 1) << 2) | ((g4 & 1) << 1);
      const unsigned b0 = bo + (((mw * 4 + (p >> 1)) ^ sw) * 16), b1 = bo + (((mw * 4 + 2 + (p >> 1)) ^ sw) * 16);
#pragma unroll
      for (int n = 0; n < 12; ++n) { RDTR(sb[2 * n], (n & 1) ? b1 : b0, ((n % 6) >> 1) * 8192 + (n / 6) * 4096); RDTR(sb[2 * n + 1], (n & 1) ? b1 : b0, ((n % 6) >> 1) * 8192 + (n / 6) * 4096 + 512); }
      WAITL(0);
      SBAR();
    };
    auto colsum = [&]() {
      const u4 ones = (u4)(0x3F803F80u);
      f4 c[6];
#pragma unroll
      for (int n = 0; n < 12; ++n) c[n % 6] = MFMA16(S8(ones), CAT(sb[2 * n], sb[2 * n + 1]), n >= 6 ? c[n % 6] : (f4)(0.f));
      const u4 csg = (u4){__float_as_uint(g4 ? c[3][0] : c[0][0]), __float_as_uint(g4 ? c[4][0] : c[1][0]), __float_as_uint(g4 ? c[5][0] : c[2][0]), tag};
      WAITV(0);
      asm volatile("" : "+v"(cw) : "v"(pf));
      ST128(csg, l < 32 ? WS_CS + (hd * 16 + mb) * 512 + l * 16 : WS_SZ);
    };
    u4 stv, cv[8], cd; float cs[3];
    auto gload = [&]() {
      stv = LD128(WS_ST + (mb * 128 + mw * 64 + l) * 16);
      if (C2 && !spare) cv[0] = LD128(WS_CT + hd * 512 + (l & 31) * 16);
      else {
#pragma unroll
        for (int i = 0; i < 8; ++i) cv[i] = LD128(WS_CS + hd * 8192 + i * 1024 + l * 16);
      }
    };
    u4 ra, rb;
    auto rload = [&]() { const unsigned ro = WS_CS + hd * 8192 + (l >> 3) * 512 + (mb & 3) * 128 + (l & 7) * 16; ra = LD128(ro); rb = LD128(ro + 4096); };
    auto rfin = [&]() {
      if (__builtin_expect(__ballot(ra[3] != tag || rb[3] != tag) != 0ull, 0)) {
        RETRY2();
        _Pragma("unroll 1") for (unsigned it = 0; it < SPIN; ++it) { SLEEP(4); rload(); if (__ballot(ra[3] != tag || rb[3] != tag) == 0ull) break; }
      }
#pragma unroll
      for (int e = 0; e < 3; ++e) {
        const float a = __uint_as_float(ra[e]) + __uint_as_float(rb[e]);
        auto r = __builtin_amdgcn_permlane32_swap(__float_as_uint(a), __float_as_uint(a), false, false);
        const float b = __uint_as_float(r[0]) + __uint_as_float(r[1]);
        auto r2 = __builtin_amdgcn_permlane16_swap(__float_as_uint(b), __float_as_uint(b), false, false);
        const float c = __uint_as_float(r2[0]) + __uint_as_float(r2[1]);
        ra[e] = __float_as_uint(c + dppf<0x128>(c));
      }
      ST128(ra, l < 8 ? WS_CT + hd * 512 + (mb & 3) * 128 + l * 16 : WS_SZ);
    };
    auto gbad = [&]() {
      unsigned bad = (stv[1] ^ tag) | (stv[3] ^ tag);
#pragma unroll
      for (int i = 0; i < (C2 && !spare ? 1 : 8); ++i) bad |= cv[i][3] ^ tag;
      return __ballot(bad != 0u) != 0ull;
    };
    int idx;
    auto gfin = [&]() {
      if (gbad()) { RETRY(); _Pragma("unroll 1") for (unsigned it = 0; it < SPIN; ++it) { SLEEP(2); gload(); if (!gbad()) break; } }
#pragma unroll
      for (int e = 0; e < 3; ++e) {
        float a = __uint_as_float(cv[0][e]);
        if (C2 && !spare) { cs[e] = a; continue; }
#pragma unroll
        for (int i = 1; i < 8; ++i) a += __uint_as_float(cv[i][e]);
        auto r = __builtin_amdgcn_permlane32_swap(__float_as_uint(a), __float_as_uint(a), false, false);
        cs[e] = __uint_as_float(r[0]) + __uint_as_float(r[1]);
      }
      if ((TCH == 2 && !spare) || TCH == 8) asm volatile("" : "+v"(cs[0]), "+v"(cs[1]), "+v"(cs[2]));
      else asm volatile("" : "+v"(cs[0]), "+v"(cs[1]), "+v"(cs[2]) : "v"(wt0), "v"(wt1), "v"(wt2), "v"(wt3));
    };
    auto cload = [&]() { cd = LD128(WS_CAND + (((hp >> 2) == 2) * 16 + mb) * 128 + (l & 7) * 16); };
    auto cfin = [&]() {
      if (__builtin_expect(__ballot(cd[1] != tag || cd[3] != tag) != 0ull, 0)) {
        RETRY();
        _Pragma("unroll 1") for (unsigned it = 0; it < SPIN; ++it) { SLEEP(4); cload(); if (__ballot(cd[1] != tag || cd[3] != tag) == 0ull) break; }
      }
      cd[0] = max(cd[0], cd[2]);
      idx = (int)(cd[0] & 2047u);
    };
    auto lpub = [&](bool sig = true) {
      *(LAS float*)(lds + L_M + (mw * 64 + l) * 4) = __uint_as_float(stv[0]);
      *(LAS float*)(lds + L_RS + (mw * 64 + l) * 4) = __uint_as_float(stv[2]);
      *(LAS float*)(lds + L_RL + (mw * 64 + l) * 4) = __uint_as_float(stv[2]) * 1.44269504f;
      if (l < 32) {
#pragma unroll
        for (int e = 0; e < 3; ++e) *(LAS float*)(lds + L_CS + mw * 384 + ((3 * g4 + e) * 16 + r15) * 4) = -cs[e];
      }
      if (mw == 0 && l < 8) *(LAS int*)(lds + L_SEL + l * 4) = idx;
      if (sig) lsig(myFL, 1u);
    };
    u4 xs[4]; float xrs;
    asm volatile("" ::"s"(wg), "s"(wu), "s"(wd), "v"(cw));
    unsigned tw[8] = {0, 0, 0, 0, 0, 0, 0, 0};
    if (TW && moe) {
      if (TWS) SLEEP(TWS);
      const char* gb = (const char*)(mw ? wu : wg) + (256 * ksp * F + 64 * chg) * 2;
      if (TW & 1) asm volatile("s_load_dword %0, %5, 0\n s_load_dword %1, %5, 458752\n s_load_dword %2, %6, 0\n s_load_dword %3, %6, 458752\n s_load_dword %4, %6, 910336"
                               : "+s"(tw[0]), "+s"(tw[1]), "+s"(tw[2]), "+s"(tw[3]), "+s"(tw[4]) : "s"(gb), "s"(gb + 917504) : "memory");
      if (TW & 2) asm volatile("s_load_dword %0, %2, 0\n s_load_dword %1, %3, 0" : "+s"(tw[5]), "+s"(tw[6])
                               : "s"((const char*)wd + ((64 * chunk + 63 * mw) * D + 256 * ksp) * 2), "s"(wo + (j * 32 + mw * 16) * 2048 + (xcd >> 1) * 512) : "memory");
      if (TW & 4) asm volatile("s_load_dword %0, %1, 0" : "+s"(tw[7]) : "s"((const char*)out + (mb * 128 + mw * 127) * 2048 + nb * 128) : "memory");
    }
    PFB(spare ? SPS : SPW, 0);
    if (SP0) { WAITL(0); spf(pfb); }
    if (HPRIO) __builtin_amdgcn_s_setprio(HPRIO);
    TICK();
    if (spare) {
      TICK();
      pois();
      if (ii >> 2) TICK();
      colrd(ii >> 2);
      spf(pfb);
      if (!(ii >> 2)) TICK();
      u4 kk[8];
      auto kload = [&]() {
#pragma unroll
        for (int i = 0; i < 8; ++i) kk[i] = LD128(WS_RG + mw * 8192 + i * 1024 + l * 16);
      };
      kload();
      TICKP();
      _Pragma("unroll 1") for (unsigned it = 0; it < SPIN; ++it) {
        unsigned bad = 0;
#pragma unroll
        for (int i = 0; i < 8; ++i) { bad |= (kk[i][1] ^ tag) | (kk[i][3] ^ tag); asm volatile("" : "+v"(bad)); }
        if (__ballot(bad != 0u) == 0ull) break;
        RETRY();
        kload();
        DTICK(7);
      }
      __builtin_amdgcn_s_setprio(3);
      unsigned m1 = max(kk[0][0], kk[0][2]), m2 = min(kk[0][0], kk[0][2]), m3 = 0;
#pragma unroll
      for (int i = 2; i < 16; ++i) {
        const unsigned a = kk[i >> 1][(i & 1) * 2];
        asm("v_med3_u32 %0, %1, %2, %0" : "+v"(m3) : "v"(m2), "v"(a));
        asm("v_med3_u32 %0, %1, %2, %0" : "+v"(m2) : "v"(m1), "v"(a));
        m1 = max(m1, a);
      }
      unsigned mine8 = 0;
#pragma unroll
      for (int it = 0; it < 8; ++it) {
        const unsigned km = wave_max(m1);
        mine8 = l == it ? km : mine8;
        const bool win = m1 == km;
        if (it < 7) { m1 = win ? m2 : m1; m2 = win ? m3 : m2; m3 = win ? 0u : m3; }
        if (it == 3 && nbar < 7) TICK();
      }
      if (__builtin_expect(__ballot(m1 == 0u) != 0ull, 0)) {
        unsigned lim = 0;
#pragma unroll 1
        for (int it = 0; it < 8; ++it) {
          unsigned m = 0;
#pragma unroll
          for (int i = 0; i < 8; ++i) m = umax3(m, kk[i][0] - lim, kk[i][2] - lim);
          lim += wave_max(m);
          mine8 = l == it ? lim : mine8;
        }
      }
      ST64(((u2){mine8, tag}), l < 8 ? WS_CAND + ((hp >> 3) * 16 + mb) * 128 + (mw ? 7 - l : l) * 16 + mw * 8 : WS_SZ);
      __builtin_amdgcn_s_setprio(HPRIO);
      STMP(2);
      TICKP();
      colsum();
      DTICK(9);
      if (TCH == 8) { tearly(); if (!TS) tlate(); } else if (TCH && TCH != 3) touch();
      spf(pfb);
      while (nbar < 10) TICKQ();
      gload(); cload();
      if (RT && mw) asm volatile("global_load_dword %0, %1, %2 sc1" : "+v"(cw) : "v"(cwo), "s"(cwb) : "memory");
      TICKQ(); TICKP();
      WAITV(0);
      asm volatile("" : "+v"(cw));
      gfin(); cfin();
      STMP(3);
      TICK(); TICK();
      if (TCH == 8 && TS) tlate();
      if (SE) { lpub(false); asm volatile("ds_write_b32 %0, %1\n s_waitcnt lgkmcnt(0)" ::"v"(adFL), "v"((l & 6) == 6 ? 1u : 0u) : "memory"); } else ZFL();
      TICK();
      if (!SE) lpub();
      if (SSL) SLEEP(SSL);
      if (TCH == 8) asm volatile("" ::"v"(wt0), "v"(wt1));
    } else {
      if (PV == 0) pois();
      TICK();
      if (PV == 1) pois();
      if (ii >> 2) TICK();
      colrd(ii >> 2);
      if (TW) asm volatile("" ::"s"(tw[0]), "s"(tw[1]), "s"(tw[2]), "s"(tw[3]), "s"(tw[4]), "s"(tw[5]), "s"(tw[6]), "s"(tw[7]));
      spf(pfb);
      TICK();
      if (CL && !(ii >> 2)) TICK();
      colsum();
      spf(pfb);
      if (!CL && !(ii >> 2)) TICK();
      u4 sv; f4 acc[8];
      auto xload = [&]() {
        const u16* xp = x + (size_t)idx * D + 256 * ksp + 32 * kset + 8 * g4;
        sv = LD128(WS_ST + idx * 16);
#pragma unroll
        for (int it = 0; it < 4; ++it) xs[it] = *(const u4*)(xp + 64 * it);
        SBAR();
      };
      auto xnorm = [&]() {
        if (__builtin_expect(__ballot(sv[1] != tag || sv[3] != tag) != 0ull, 0)) {
          _Pragma("unroll 1") for (unsigned it = 0; it < SPIN; ++it) { SLEEP(1); sv = LD128(WS_ST + idx * 16); if (__ballot(sv[1] != tag || sv[3] != tag) == 0ull) break; }
        }
        const float nm = -__uint_as_float(sv[0]);
        xrs = __uint_as_float(sv[2]);
#pragma unroll
        for (int it = 0; it < 4; ++it) {
          float t0, t1, t2, t3; unsigned x0 = xs[it][0], x1 = xs[it][1], x2 = xs[it][2], x3 = xs[it][3];
          asm volatile("v_dot2_f32_bf16 %4, %0, %8, %10\n v_dot2_f32_bf16 %0, %0, %9, %10\n v_dot2_f32_bf16 %5, %1, %8, %10\n v_dot2_f32_bf16 %1, %1, %9, %10\n"
                       "v_dot2_f32_bf16 %6, %2, %8, %10\n v_dot2_f32_bf16 %2, %2, %9, %10\n v_dot2_f32_bf16 %7, %3, %8, %10\n v_dot2_f32_bf16 %3, %3, %9, %10\n"
                       "v_cvt_pk_bf16_f32 %0, %4, %0\n v_cvt_pk_bf16_f32 %1, %5, %1\n v_cvt_pk_bf16_f32 %2, %6, %2\n s_nop 0\n v_cvt_pk_bf16_f32 %3, %7, %3"
                       : "+v"(x0), "+v"(x1), "+v"(x2), "+v"(x3), "=&v"(t0), "=&v"(t1), "=&v"(t2), "=&v"(t3) : "s"(0x00003F80u), "s"(0x3F800000u), "v"(nm));
          xs[it] = (u4){x0, x1, x2, x3};
        }
        asm volatile("s_nop 1" : "+v"(xs[0]), "+v"(xs[1]), "+v"(xs[2]), "+v"(xs[3]), "+v"(xrs));
        SBAR();
      };
      auto gmul = [&](int i0, int i1) {
#pragma unroll
        for (int it = i0; it < i1; ++it)
#pragma unroll
          for (int c = 0; c < 8; ++c) acc[c] = MFMA16(S8(gw[it][c]), S8(xs[it]), it ? acc[c] : (f4)(0.f));
#pragma unroll
        for (int c = 0; c < 8; ++c) asm volatile("" : "+v"(acc[c]));
        SBAR();
      };
      auto gout = [&]() {
        unsigned go = r15 < 8 ? WS_GP + ((chg * 4 + ksp) * 2 + mw) * 2048 + (g4 >> 1) * 1024 + (g4 & 1) * 512 + r15 * 16 : WS_SZ;
        asm volatile("" : "+v"(go));
#pragma unroll
        for (int i = 0; i < 4; ++i) {
          unsigned a[4], b[4];
#pragma unroll
          for (int c = 0; c < 4; ++c) {
            auto r = __builtin_amdgcn_permlane32_swap(__float_as_uint(acc[c][i]), __float_as_uint(acc[c + 4][i]), false, false);
            a[c] = r[0]; b[c] = r[1];
          }
          unsigned s0, s1, s2, s3;
#define AR(d, x, y) "v_add_f32_dpp %" #d ", %" #x ", %" #y " row_ror:8 row_mask:0xf bank_mask:0xf\n"
          asm volatile("s_nop 1\n" AR(0, 8, 4) AR(1, 9, 5) AR(2, 10, 6) AR(3, 11, 7) : "=&v"(s0), "=&v"(s1), "=&v"(s2), "=&v"(s3)
                       : "v"(a[0]), "v"(a[1]), "v"(a[2]), "v"(a[3]), "v"(b[0]), "v"(b[1]), "v"(b[2]), "v"(b[3]));
          ST128(tagf(__builtin_bit_cast(f4, ((u4){s0, s1, s2, s3})), tag), go + i * 128);
        }
        SBAR();
      };
      for (int i = 0; i < ph; ++i) { spf(pfb); TICK(); }
      burst(0); if (TCH == 1) touch(); if (TCH == 7) touch(1); if (TCH == 8 && TE == 0) tearly();
      spf(pfb);
      TICKQ();
      TICKQ();
      if (TL) { transp(gw[0]); transp(gw[1]); }
      TICKP();
      if (!TL) transp(gw[0]);
      if (C2 && (mb >> 2) == (CB == 4 ? 2 * mw : CB)) { SBAR(); rload(); }
      if (SP2) spf(pfb);
      TICKP();
      if (!TL) transp(gw[1]);
      if (C2 && (mb >> 2) == (CB == 4 ? 2 * mw : CB)) { SBAR(); rfin(); }
      if (SP2) spf(pfb);
      TICK();
      if (ph) cload();
      if (GE == 1) gload();
      if (RT && mw) asm volatile("global_load_dword %0, %1, %2 sc1" : "+v"(cw) : "v"(cwo), "s"(cwb) : "memory");
      SBAR();
      burst(1); if (TCH == 4) touch(); if (TCH == 8 && TE == 1) tearly();
      spf(pfb);
      TICKQ();
      if (TCH == 5) touch();
      if (TCH == 7) touch(2);
      if (TCH == 8 && TE == 2) tearly();
      if (GE && !ph) cload();
      if (GE == 2) gload();
      TICK();
      if (ph < 2) spf(pfb);
      if (GE == 1) gfin();
      TICK();
      transp(gw[2]);
      WAITV(0);
      asm volatile("" : "+v"(cw));
      if (GE == 2) gfin();
      if (ph < 2) TICK();
      transp(gw[3]);
      if (GE) {
        if (ph < 1) TICK();
        cfin();
        if (GX) xload();
        if (GS) { lpub(false); asm volatile("ds_write_b32 %0, %1\n s_waitcnt lgkmcnt(0)" ::"v"(adFL), "v"((l & 6) == 6 ? 1u : 0u) : "memory"); } else ZFL();
        TICK();
        if (!GX) xload();
        if (TCH == 8) { tlate(); SBAR(); }
      } else {
        if (ph < 1) { SBAR(); cload(); TICK(); }
        SBAR();
        if (XO == 0) { cfin(); xload(); gload(); } else { gload(); SBAR(); cfin(); xload(); }
        if (TCH == 2) { touch(); SBAR(); }
        if (TCH == 8 && (TLP == 0 || (TLP == 3 && ph == 0))) { tlate(); SBAR(); }
        ZFL();
        TICK();
        if (TCH == 8 && (TLP == 1 || (TLP == 3 && ph != 0))) { tlate(); SBAR(); }
        if (DE) { burst(2, 0, DE); SBAR(); }
        gfin();
      }
      if (!(GE && GS)) lpub();
      STMP(12);
      SBAR();
      __builtin_amdgcn_s_setprio(0);
      if (TCH == 8 && TLP == 4) { tlate(); SBAR(); }
      if (HS1) SLEEP(HS1);
      if (TCH == 8 && TLP == 5) { tlate(); SBAR(); }
      burst(2, DE, D1);
      if (TCH == 8 && TLP == 2) tlate();
      STMP(3);
      SBAR();
      if (HS0) SLEEP(HS0);
      if (G1 & 1) { lwait<HW1>(adFL, 0x0Fu, G1 >> 4); if (G1S) SLEEP(G1S); __builtin_amdgcn_s_setprio(TP0); }
      xnorm();
      if (G1 & 2) { lwait<2>(adFL, 0x0Fu, G1 >> 4); if (G1S) SLEEP(G1S); __builtin_amdgcn_s_setprio(TP0); }
      STMP(5);
      gmul(0, 4);
      gout();
      if (TCH == 2 || TCH == 8) asm volatile("" ::"v"(wt0), "v"(wt1));
      STMP(6);
      SBAR();
    }
    __builtin_amdgcn_s_setprio(TP1);
    STMP(4);
    if (moe) {
      if (D2 == 0) { burst(2, D1, 16); SBAR(); }
      if (D1 >= 8 || D2 == 0) transp(dw[0]); if (D1 == 16 || D2 == 0) transp(dw[1]);
      SBAR();
      lwait<2>(adFL, 0x30u, 3);
      if (G2S) SLEEP(G2S);
      STMP(7);
      if (D2 == 1) { burst(2, D1, 16); SBAR(); }
      f4 acc[8];
      u4 ha[2];
      {
        u4 pg[4][2];
        auto pload = [&]() {
#pragma unroll
          for (int ks = 0; ks < 4; ++ks)
#pragma unroll
            for (int t = 0; t < 2; ++t) pg[ks][t] = LD128(WS_GP + ((chunk * 4 + ks) * 4 + 2 * t + mw) * 1024 + (4 * kset + g4) * 128 + np * 16);
        };
        auto pbad = [&]() {
          unsigned bad = 0;
#pragma unroll
          for (int i = 0; i < 8; ++i) untag(pg[i >> 1][i & 1], tag, bad);
          return __ballot((bad & 255u) != 0u) != 0ull;
        };
        pload();
        SBAR();
        if (D2 == 2) { burst(2, D1, 16); SBAR(); }
        __builtin_amdgcn_s_setprio(TP2);
        if (D1 < 8 && D2 == 1) transp(dw[0]);
        if (D1 < 16 && D2 == 1) transp(dw[1]);
        SBAR();
        if (__builtin_expect(pbad(), 0)) { _Pragma("unroll 1") for (unsigned it = 0; it < SPIN; ++it) { RETRY2(); SLEEP(4); pload(); if (!pbad()) break; } }
        STMP(10);
        f4 gq[2];
#pragma unroll
        for (int t = 0; t < 2; ++t)
          gq[t] = ((__builtin_bit_cast(f4, pg[0][t]) + __builtin_bit_cast(f4, pg[1][t])) + (__builtin_bit_cast(f4, pg[2][t]) + __builtin_bit_cast(f4, pg[3][t]))) * xrs;
        float hv[4];
#pragma unroll
        for (int e = 0; e < 4; ++e) {
          const float gg = rbf(gq[0][e]), uu = rbf(gq[1][e]);
          hv[e] = rbf(gg / (1.f + __expf(-gg))) * uu;
        }
        *(LAS u2*)(lds + L_HX + l * 16 + mw * 8) = (u2){pk2(hv[0], hv[1]), pk2(hv[2], hv[3])};
        lsig(myFL, 2u);
        if (D1 < 16 && D2 == 2) { SBAR(); if (D1 < 8) transp(dw[0]); transp(dw[1]); SBAR(); }
        lwait(adFL, 0xC0u, 2);
        ha[0] = *(const LAS u4*)(lds + L_HX + l * 16); ha[1] = *(const LAS u4*)(lds + L_HX + (l ^ 8) * 16);
      }
#pragma unroll
      for (int it = 0; it < 2; ++it)
#pragma unroll
        for (int c = 0; c < 8; ++c) acc[c] = MFMA16(S8(ha[it]), S8(dw[it][c]), it ? acc[c] : (f4)(0.f));
      if (g4 < 2) {
#pragma unroll
        for (int i = 0; i < 4; ++i) {
          const unsigned yo = WS_YP + ((chunk * 8 + 4 * g4 + i) * D + 256 * ksp + 128 * mw + 4 * r15) * 4;
          ST128(tagf((f4){acc[0][i], acc[1][i], acc[2][i], acc[3][i]}, tag), yo);
          ST128(tagf((f4){acc[4][i], acc[5][i], acc[6][i], acc[7][i]}, tag), yo + 256);
        }
      }
      STMP(11);
    }
    float sg;
    {
      const unsigned mono = cd[0] >> 16, rb = (mono & 0x8000u) ? (mono & 0x7FFFu) : (~mono & 0xFFFFu);
      sg = rbf(1.f / (1.f + __expf(-lo(rb))));
    }
    unsigned mine = (unsigned)__ballot(!NOMOE && l < 8 && (idx >> 7) == mb && (l & 1) == mw);
    if (mine) {
      if (spare) lwait<4>(adFL, 0x30u, 3);
      {
        const u2 f = LD64(WS_OH + (mb * 16 + r15) * 128);
        const unsigned t0 = NOW();
        unsigned rel = f[1] + YD - t0;
        rel = wave_max(f[0] == tag && rel < 600u ? rel : 0u);
        _Pragma("unroll 1") for (unsigned it = 0; it < SPIN; ++it) { if (NOW() - t0 >= rel) break; SLEEP(2); }
      }
      STMP(8);
      lwait<1>(adFL, 0x0Fu, 4);
      __builtin_amdgcn_s_setprio(TP2);
      while (mine) {
        const int t = __builtin_ctz(mine); mine &= mine - 1;
        u4 yv[14];
        _Pragma("unroll 1") for (unsigned it = 0; it < SPIN; ++it) {
          unsigned bad = 0;
#pragma unroll
          for (int i = 0; i < 14; ++i) yv[i] = LD128(WS_YP + (((i * 4 + g4) * 8 + t) * D + (nb >> 1) * 128 + (r15 >> 3) * 64 + (nb & 1) * 32 + (r15 & 7) * 4) * 4);
          SBAR();
#pragma unroll
          for (int i = 0; i < 14; ++i) untag(yv[i], tag, bad);
          if (__ballot((bad & 255u) != 0u) == 0ull) break;
          RETRY();
          SLEEP(YSL);
        }
        f4 y = __builtin_bit_cast(f4, yv[0]);
#pragma unroll
        for (int i = 1; i < 14; ++i) y += __builtin_bit_cast(f4, yv[i]);
        const float gt = __int_as_float(__builtin_amdgcn_readlane(__float_as_int(sg), t));
#pragma unroll
        for (int e = 0; e < 4; ++e) {
          auto r = __builtin_amdgcn_permlane32_swap(__float_as_uint(y[e]), __float_as_uint(y[e]), false, false);
          const float a = __uint_as_float(r[0]) + __uint_as_float(r[1]);
          auto r2 = __builtin_amdgcn_permlane16_swap(__float_as_uint(a), __float_as_uint(a), false, false);
          y[e] = rbf(rbf(__uint_as_float(r2[0]) + __uint_as_float(r2[1])) * gt);
        }
        if (l < 16) *(LAS f4*)(lds + L_KVX + t * 256 + (l & 7) * 32 + (l >> 3) * 16) = y;
      }
    }
    lsig(myFL, 3u);
    STMP(9);
    STFLUSH();
    if (spare) asm volatile(".Lend_s:"); else asm volatile(".Lend_h:");
    };
    if (sp) helper(BC<true>()); else helper(BC<false>());
    return;
  }

  const int wr = w >> 1, wc = w & 1;
  u2 kg; u4 sg4; unsigned tag;
  {
    const int row = srow;
    SBAR(); BST(0); SBAR();
    asm volatile("s_waitcnt vmcnt(4)" : "+v"(xa[0]), "+v"(xa[1]), "+v"(xa[2]), "+v"(xa[3]));
    SBAR(); BST(1); SBAR();
    float s0 = 0, s1 = 0;
#pragma unroll
    for (int c = 0; c < 4; ++c)
#pragma unroll
      for (int e = 0; e < 4; e += 2) { unsigned x0 = xa[c][e], x1 = xa[c][e + 1]; s0 = dot2(x0, 0x3F803F80u, s0); s1 = dot2(x1, 0x3F803F80u, s1); }
    const float m = half_sum(s0 + s1) * (1.f / D);
    f2 v[16], q2 = (f2)(0.f), q3 = (f2)(0.f);
#pragma unroll
    for (int c = 0; c < 16; ++c) v[c] = (f2){lo(xa[c >> 2][c & 3]), hi(xa[c >> 2][c & 3])} - m;
#pragma unroll
    for (int c = 0; c < 16; c += 2) { q2 += v[c] * v[c]; q3 += v[c + 1] * v[c + 1]; }
    q2 += q3;
    const float mr = rbf(m);
    const float rs = 1.f / rbf(sqrtf(rbf(rbf(half_sum(q2[0] + q2[1]) * (1.f / D)) + 1e-5f)));
    float d0 = 0, d1 = 0;
    asm volatile("s_waitcnt vmcnt(0)" : "+v"(rw[0]), "+v"(rw[1]), "+v"(rw[2]), "+v"(rw[3]), "+v"(d0) : "v"(rs));
#pragma unroll
    for (int c = 0; c < 16; c += 2) {
      const f2 e0 = (f2){lo(xa[c >> 2][c & 3]), hi(xa[c >> 2][c & 3])} - mr, e1 = (f2){lo(xa[c >> 2][(c & 3) + 1]), hi(xa[c >> 2][(c & 3) + 1])} - mr;
      const unsigned p0 = pk2(e0[0], e0[1]), p1 = pk2(e1[0], e1[1]);
      const f2 n0 = (f2){lo(p0), hi(p0)} * rs, n1 = (f2){lo(p1), hi(p1)} * rs;
      unsigned c0 = rw[c >> 2][c & 3], c1 = rw[c >> 2][(c & 3) + 1];
      d0 = dot2(pk2(n0[0], n0[1]), c0, d0); d1 = dot2(pk2(n1[0], n1[1]), c1, d1);
    }
    const float d = half_sum(d0 + d1);
    const unsigned rb = pk2(d, d) & 0xFFFFu;
    const unsigned mono = (rb & 0x8000u) ? (~rb & 0xFFFFu) : (rb | 0x8000u);
    SBAR();
    GETTAG(tg); tag = tg;
    kg = (u2){(mono << 16) | (unsigned)row, tag};
    sg4 = (u4){__float_as_uint(mr), tag, __float_as_uint(rs), tag};
  }
  MKWS(tag);
  SBAR(); BST(2); SBAR();
  if ((l & 31) == 0) { ST64(kg, WS_RG + srow * 8); ST128(sg4, WS_ST + srow * 16); }
  unsigned aad[2], bad[2];
#pragma unroll
  for (int s = 0; s < 2; ++s) aad[s] = ldsb + (wr * 64 + r15) * 128 + (((4 * s + g4) ^ ((r15 >> 1) & 7)) * 16);
#pragma unroll
  for (int nh = 0; nh < 2; ++nh) bad[nh] = ldsb + 16384 + (8 * g4 + q) * 128 + (((wc * 4 + 2 * (nh ^ wr) + (p >> 1)) ^ (((q >> 1) << 2) | ((g4 & 1) << 1))) * 16) + (p & 1) * 8;
  f4 acc[4][6];
#pragma unroll
  for (int m = 0; m < 4; ++m)
#pragma unroll
    for (int n = 0; n < 6; ++n) { acc[m][n] = (f4)(0.f); asm volatile("" : "+v"(acc[m][n])); }
#define MF(m, n, Fq) do { if (n < 2) acc[m][n] = MFMA16(CAT(Fq.b[2 * n], Fq.b[2 * n + 1]), Fq.a[m], acc[m][n]); \
     else acc[m][n] = MFMA16(Fq.a[m], CAT(Fq.b[2 * n], Fq.b[2 * n + 1]), acc[m][n]); SBAR(); } while (0)
#define RB(G, n, vb0, vb1, SS, hf) RDTR(G.b[2 * (n) + (hf)], (((n) & 1) ? vb1 : vb0), ((n) >> 1) * 8192 + (SS) * 4096 + (hf) * 512)
#define RA(G, m, va) RD128(G.a[m], va, (m) * 2048)
#define SUB(Fq, G, va, vb0, vb1, SS, PRE) do { \
    WAITL(13); SBAR(); \
    MF(0, 0, Fq); PRE; RA(G, 0, va); SBAR(); \
    WAITL(11); SBAR(); \
    MF(1, 0, Fq); RB(G, 0, vb0, vb1, SS, 0); SBAR(); MF(2, 0, Fq); RB(G, 0, vb0, vb1, SS, 1); SBAR(); MF(3, 0, Fq); RA(G, 1, va); SBAR(); \
    WAITL(12); SBAR(); \
    MF(0, 1, Fq); RA(G, 2, va); SBAR(); MF(1, 1, Fq); RA(G, 3, va); SBAR(); MF(2, 1, Fq); RB(G, 1, vb0, vb1, SS, 0); SBAR(); MF(3, 1, Fq); RB(G, 1, vb0, vb1, SS, 1); SBAR(); \
    WAITL(8); SBAR(); \
    MF(0, 2, Fq); RB(G, 2, vb0, vb1, SS, 0); SBAR(); MF(1, 2, Fq); RB(G, 2, vb0, vb1, SS, 1); SBAR(); MF(2, 2, Fq); RB(G, 3, vb0, vb1, SS, 0); SBAR(); MF(3, 2, Fq); RB(G, 3, vb0, vb1, SS, 1); SBAR(); \
    MF(0, 3, Fq); RB(G, 4, vb0, vb1, SS, 0); SBAR(); MF(1, 3, Fq); RB(G, 4, vb0, vb1, SS, 1); SBAR(); MF(2, 3, Fq); RB(G, 5, vb0, vb1, SS, 0); SBAR(); MF(3, 3, Fq); RB(G, 5, vb0, vb1, SS, 1); SBAR(); \
    MF(0, 4, Fq); MF(1, 4, Fq); MF(2, 4, Fq); MF(3, 4, Fq); MF(0, 5, Fq); MF(1, 5, Fq); MF(2, 5, Fq); MF(3, 5, Fq); } while (0)
#define KSTEP(MODE) do { \
    if (kt == sx) { const unsigned xo = xad + buf * STG; \
      _Pragma("unroll") for (int t = 0; t < 8; ++t) { const unsigned a_ = xo ^ (t * 16); \
        asm volatile("ds_read_b64 %0, %1" : "+v"(xv[t]) : "v"(a_) : "memory"); } \
      WAITL(0); SBAR(); } \
    const int nbuf = (buf + 1) & 3; \
    const unsigned bo = buf * STG, nbo = nbuf * STG; \
    { unsigned va = aad[1] + bo, vb0 = bad[0] + bo, vb1 = bad[1] + bo; SUB(f0, f1, va, vb0, vb1, 1, ); } \
    if (MODE < 2) { \
      unsigned va = aad[0] + nbo, vb0 = bad[0] + nbo, vb1 = bad[1] + nbo; \
      SUB(f1, f0, va, vb0, vb1, 0, if (MODE == 0) { WAITL(0); BAR(); } else asm volatile("ds_write_b32 %0, %1" ::"v"(myFL), "v"(1u) : "memory"); SBAR()); \
    } else { asm volatile("ds_write_b32 %1, %2\n ds_read_b32 %0, %3\n s_waitcnt lgkmcnt(2)" : "=&v"(fl0) : "v"(myFL), "v"(2u), "v"(adFL) : "memory"); SBAR(); \
      MF(0, 2, f1); MF(1, 2, f1); MF(2, 2, f1); MF(3, 2, f1); MF(0, 3, f1); MF(1, 3, f1); MF(2, 3, f1); MF(3, 3, f1); \
      MF(0, 4, f1); MF(1, 4, f1); MF(2, 4, f1); MF(3, 4, f1); MF(0, 5, f1); MF(1, 5, f1); MF(2, 5, f1); MF(3, 5, f1); \
      MF(0, 0, f1); MF(1, 0, f1); MF(2, 0, f1); MF(3, 0, f1); MF(0, 1, f1); MF(1, 1, f1); MF(2, 1, f1); MF(3, 1, f1); } \
    buf = nbuf; } while (0)
#define SUBK(Fq, G, va, vb0, vb1, SS, W0, X, PRE, POST) do { \
    WAITL(W0); SBAR(); \
    MF(0, 2, Fq); PRE; RA(G, 0, va); SBAR(); \
    WAITL(7 + (X)); SBAR(); \
    MF(1, 2, Fq); RB(G, 2, vb0, vb1, SS, 0); SBAR(); MF(2, 2, Fq); RB(G, 2, vb0, vb1, SS, 1); SBAR(); MF(3, 2, Fq); RA(G, 1, va); SBAR(); \
    WAITL(8 + (X)); SBAR(); \
    MF(0, 3, Fq); RA(G, 2, va); SBAR(); MF(1, 3, Fq); RA(G, 3, va); SBAR(); MF(2, 3, Fq); RB(G, 3, vb0, vb1, SS, 0); SBAR(); MF(3, 3, Fq); RB(G, 3, vb0, vb1, SS, 1); SBAR(); \
    WAITL(10 + (X)); SBAR(); \
    MF(0, 4, Fq); RB(G, 4, vb0, vb1, SS, 0); SBAR(); MF(1, 4, Fq); RB(G, 4, vb0, vb1, SS, 1); SBAR(); MF(2, 4, Fq); RB(G, 5, vb0, vb1, SS, 0); SBAR(); MF(3, 4, Fq); RB(G, 5, vb0, vb1, SS, 1); SBAR(); \
    WAITL(12); SBAR(); \
    MF(0, 5, Fq); POST; MF(1, 5, Fq); MF(2, 5, Fq); MF(3, 5, Fq); } while (0)
#define RQ(G, va, vb0, vb1, SS) do { RA(G, 0, va); RB(G, 0, vb0, vb1, SS, 0); RB(G, 0, vb0, vb1, SS, 1); RA(G, 1, va); RA(G, 2, va); RA(G, 3, va); \
    RB(G, 1, vb0, vb1, SS, 0); RB(G, 1, vb0, vb1, SS, 1); SBAR(); } while (0)
#define MQ(Fq) do { MF(0, 0, Fq); MF(1, 0, Fq); MF(2, 0, Fq); MF(3, 0, Fq); MF(0, 1, Fq); MF(1, 1, Fq); MF(2, 1, Fq); MF(3, 1, Fq); } while (0)
  u2 xv[8];
#pragma unroll
  for (int t = 0; t < 8; ++t) asm volatile("" : "=v"(xv[t]));
  const int sx = (nb - rot) & 15;
  const unsigned xad = ldsb + (w * 32 + (l & 31)) * 128 + ((r15 >> 1) & 7) * 16 + (l >> 5) * 8;
  STMP(1);
  WAITL(0);
  BAR();
  STMP(2);
  WAITL(0);
  FQ f0, f1;
  { unsigned va = aad[0], vb0 = bad[0], vb1 = bad[1];
    RA(f0, 0, va); RB(f0, 0, vb0, vb1, 0, 0); RB(f0, 0, vb0, vb1, 0, 1); RA(f0, 1, va); RA(f0, 2, va); RA(f0, 3, va);
    RB(f0, 1, vb0, vb1, 0, 0); RB(f0, 1, vb0, vb1, 0, 1); RB(f0, 2, vb0, vb1, 0, 0); RB(f0, 2, vb0, vb1, 0, 1); RB(f0, 3, vb0, vb1, 0, 0); RB(f0, 3, vb0, vb1, 0, 1);
    RB(f0, 4, vb0, vb1, 0, 0); RB(f0, 4, vb0, vb1, 0, 1); RB(f0, 5, vb0, vb1, 0, 0); RB(f0, 5, vb0, vb1, 0, 1); SBAR(); }
  unsigned sad = ldsb + L_M + wr * 256 + g4 * 16, cad = ldsb + L_CS + wc * 384 + r15 * 4 + wr * 64;
  const int hd = hp * 2 + wc;
  unsigned kvo = WS_KVP + (hd * 16 + mb) * 2048 + wr * 1024 + l * 16, zo = r15 == 0 ? WS_ZP + (hd * 16 + mb) * 128 + g4 * 32 + wr * 16 : WS_SZ;
  unsigned xw = ldsb + L_KVX + w * 3072 + l * 16, xr = ldsb + L_KVX + (w ^ 2) * 3072 + l * 16;
  asm volatile("s_waitcnt lgkmcnt(0)" : "+v"(sad), "+v"(cad), "+v"(kvo), "+v"(zo), "+v"(xw), "+v"(xr) : : "memory"); SBAR();
  int buf = 0; unsigned fl0;
#pragma unroll 1
  for (int kt = 0; kt < 14; ++kt) KSTEP(0);
  STMP(14); SBAR();
  f4 sm[4], sr[4], sl[4]; f2 ck, cv;
  unsigned c2 = 2u, c3 = 3u, vfl = myFL, b30, b31;
  u4 ones = (u4)(0x3F803F80u);
#define SC0() asm volatile("ds_read_b32 %0, %2 offset:128\n ds_read_b32 %1, %2 offset:256" : "=&v"(ck[0]), "=&v"(cv[0]) : "v"(cad) : "memory")
#define SC1() asm volatile("ds_read_b32 %0, %2 offset:128\n ds_read_b32 %1, %2 offset:256" : "=&v"(ck[1]), "=&v"(cv[1]) : "v"(cad ^ 64) : "memory")
  const unsigned b20 = bad[0] + 2 * STG, b21 = bad[1] + 2 * STG, a21 = aad[1] + 2 * STG;
  unsigned a20, a30, a31, pfl2, lm, lc, ld, pw0, pw1, pws = ldsb + L_PQ + wr * 8200 + wc * 4096; int sxl = sx;
#define SM(m) do { RD128(sm[m], sad, (m) * 64); RD128(sl[m], sad, L_RL - L_M + (m) * 64); RD128(sr[m], sad, L_RS - L_M + (m) * 64); } while (0)
  {
    { const unsigned va = a21; SUBK(f0, f1, va, b20, b21, 1, 6, 0, asm volatile("" ::"v"(f0.b[0]), "v"(f0.b[1]), "v"(f0.b[2]), "v"(f0.b[3])), ); }
    { const unsigned va = a30 = aad[0] + 3 * STG; b30 = bad[0] + 3 * STG; b31 = bad[1] + 3 * STG;
      unsigned ft, late = 0;
      SUBK(f1, f0, va, b30, b31, 0, 9, LF, asm volatile("" : "+v"(c2), "+v"(c3), "+v"(vfl)); if (LF) asm volatile("ds_read_b32 %0, %1" : "=v"(ft) : "v"(adFL) : "memory"),
           if (LF) { asm volatile("" : "+v"(ft)); late = (unsigned)__ballot(ft < 1u) & 0x30u; asm volatile("" : "+s"(late)); SBAR(); });
      if (__builtin_expect(late != 0u, 0)) {
        RETRY2();
        lwait(adFL, 0x30u, 1);
#define RAP(G, m, va) asm volatile("ds_read_b128 %0, %1 offset:%2" : "+v"(G.a[m]) : "v"(va), "i"((m) * 2048) : "memory")
#define RBP(G, n, hf) asm volatile("ds_read_b64_tr_b16 %0, %1 offset:%2" : "+v"(G.b[2 * (n) + (hf)]) : "v"(((n) & 1) ? b31 : b30), "i"(((n) >> 1) * 8192 + (hf) * 512) : "memory")
        RAP(f0, 0, va); RBP(f0, 2, 0); RBP(f0, 2, 1); RAP(f0, 1, va); RAP(f0, 2, va); RAP(f0, 3, va);
        RBP(f0, 3, 0); RBP(f0, 3, 1); RBP(f0, 4, 0); RBP(f0, 4, 1); RBP(f0, 5, 0); RBP(f0, 5, 1);
      }
      SBAR();
    }
    STMP(7); SBAR();
    { const unsigned va = a31 = aad[1] + 3 * STG; SUBK(f0, f1, va, b30, b31, 1, 9, 0, asm volatile("" : "+v"(ones)), a20 = aad[0] + 2 * STG; pfl2 = vfl ^ 8; asm volatile("" : "+v"(a20), "+v"(pfl2), "+s"(pws), "+s"(sxl)); SBAR()); }
    asm volatile("ds_write_b32 %1, %2\n ds_read_b32 %0, %3\n s_waitcnt lgkmcnt(8)" : "=&v"(fl0) : "v"(vfl), "v"(c2), "v"(adFL) : "memory"); SBAR();
    MF(0, 2, f1); SC0(); SBAR(); MF(1, 2, f1); SC1(); SBAR(); MF(2, 2, f1); MF(3, 2, f1); WAITL(10); SBAR();
    MF(0, 3, f1); SM(0); SBAR(); MF(1, 3, f1); MF(2, 3, f1); SM(1); SBAR(); MF(3, 3, f1);
    WAITL(12); SBAR();
    MF(0, 4, f1); SM(2); SBAR(); MF(1, 4, f1); MF(2, 4, f1); SM(3); SBAR(); MF(3, 4, f1); MF(0, 5, f1); MF(1, 5, f1); MF(2, 5, f1); MF(3, 5, f1);
  }
  asm volatile("s_waitcnt lgkmcnt(0)" : "+v"(fl0) : : "memory");
  if (__builtin_expect(((unsigned)__ballot(fl0 < 1u) & 0xC0u) != 0u, 0)) {
    lwait(adFL, 0xC0u, 1);
    SC0(); SC1(); SM(0); SM(1); SM(2); SM(3); WAITL(0);
  }
  asm volatile("" : "+v"(ck), "+v"(cv), "+v"(sm[0]), "+v"(sm[1]), "+v"(sm[2]), "+v"(sm[3]), "+v"(sr[0]), "+v"(sr[1]), "+v"(sr[2]), "+v"(sr[3]));
  asm volatile("" : "+v"(sl[0]), "+v"(sl[1]), "+v"(sl[2]), "+v"(sl[3]));
  STMP(3);
  __builtin_amdgcn_s_setprio(2);
  SBAR(); AL8(); SBAR();
  {
    f4 kv[4], zt[2];
    u4 kf[2][2], vf[2][2];
#define MKV(t, i) do { if ((i) == 2 || (i) == 5) zt[(i) == 2] = MFMA16(S8(kf[t][(i) == 2]), S8(ones), (t) ? zt[(i) == 2] : (f4)(0.f)); \
      else kv[((i) + 2) % 5] = MFMA16(S8(kf[t][(((i) + 2) % 5) & 1]), S8(vf[t][(((i) + 2) % 5) >> 1]), (t) ? kv[((i) + 2) % 5] : (f4)(0.f)); SBAR(); } while (0)
#pragma unroll
    for (int m = 0; m < 4; ++m) {
      const int t = m >> 1, mm = m & 1;
      f4 ea[2], pp[2];
#pragma unroll
      for (int a = 0; a < 2; ++a) {
        const f4 uv = (acc[m][4 + a] + sm[m] * cv[a]) * sr[m];
        vf[t][a][2 * mm] = pk2(uv[0], uv[1]); vf[t][a][2 * mm + 1] = pk2(uv[2], uv[3]);
        const f4 uk = acc[m][2 + a] + sm[m] * ck[a];
        ea[a] = uk * sl[m]; pp[a] = uk * sr[m] + 1.f;
      }
      SBAR();
#pragma unroll
      for (int e = 0; e < 8; ++e) { if (t && (e == 0 || e == 3 || e == 6)) MKV(0, 3 * mm + e / 3); ea[e >> 2][e & 3] = __builtin_amdgcn_exp2f(ea[e >> 2][e & 3]); SBAR(); }
      AL8(); SBAR();
#pragma unroll
      for (int e = 0; e < 8; ++e) pp[e >> 2][e & 3] = __builtin_amdgcn_fmed3f(ea[e >> 2][e & 3], pp[e >> 2][e & 3], 1.f);
      SBAR();
#pragma unroll
      for (int a = 0; a < 2; ++a) { pp[a] += 1e-6f; kf[t][a][2 * mm] = pk2(pp[a][0], pp[a][1]); kf[t][a][2 * mm + 1] = pk2(pp[a][2], pp[a][3]); }
      SBAR();
    }
    MKV(1, 0); MKV(1, 1); MKV(1, 2); MKV(1, 3);
    asm volatile("ds_write_b128 %0, %1" ::"v"(xw), "v"(kv[2]) : "memory"); SBAR();
    MKV(1, 4);
    asm volatile("ds_write_b128 %0, %1 offset:1024" ::"v"(xw), "v"(kv[3]) : "memory"); SBAR();
    MKV(1, 5);
    asm volatile("ds_write_b128 %0, %1 offset:2048\n ds_write_b32 %2, %3" ::"v"(xw), "v"(zt[1]), "v"(vfl), "v"(c3) : "memory");
    f4 r0, r1, rz; unsigned v;
    asm volatile("ds_read_b32 %0, %4\n ds_read_b128 %1, %5 offset:1024\n ds_read_b128 %2, %5\n ds_read_b128 %3, %5 offset:2048"
                 : "=&v"(v), "=&v"(r0), "=&v"(r1), "=&v"(rz) : "v"(pfl2), "v"(xr) : "memory");
    RQ(f0, a20, b20, b21, 0);
    lm = ldsb + L_M + wr * 256 + r15 * 4; lc = ldsb + L_CS + wc * 384 + wr * 64 + g4 * 16; pw0 = pws + l * 16; pw1 = pw0 ^ 8; ld = lc ^ 64;
    asm volatile("" : "+v"(lm), "+v"(lc), "+v"(pw0), "+v"(pw1), "+v"(ld)); SBAR();
    unsigned nx;
    asm volatile("s_mov_b32 %4, 0x200000\n s_branch .Lxw\n .p2align 3\n s_nop 0\n.Lxl:\n ds_read_b32 %0, %5\n ds_read_b128 %1, %6 offset:1024\n ds_read_b128 %2, %6\n ds_read_b128 %3, %6 offset:2048\n"
                 "s_sub_u32 %4, %4, 1\n s_waitcnt lgkmcnt(0)\n s_cbranch_scc1 .Lxd\n.Lxw:\n s_waitcnt lgkmcnt(8)\n v_cmp_gt_u32 vcc, 3, %0\n s_cbranch_vccnz .Lxl\n.Lxd:"
                 : "+v"(v), "+v"(r0), "+v"(r1), "+v"(rz), "=&s"(nx) : "v"(pfl2), "v"(xr) : "memory", "scc", "vcc");
    const f4 s0 = kv[0] + r0, s1 = kv[1] + r1, sz = zt[0] + rz;
    if (ZF) { ST128(__builtin_bit_cast(u4, sz), zo); SBAR(); }
    u4 o; o[0] = pk2(s0[0], s0[1]); o[1] = pk2(s0[2], s0[3]); o[2] = pk2(s1[0], s1[1]); o[3] = pk2(s1[2], s1[3]);
    ST128(o, kvo);
    if (!ZF) ST128(__builtin_bit_cast(u4, sz), zo);
  }
  STMP(4);
  {
    SBAR();
#define STQ(Fq, G, va, vb0, vb1, SS, POST) do { WAITL(2); SBAR(); \
      MF(0, 0, Fq); RA(G, 0, va); RB(G, 0, vb0, vb1, SS, 0); SBAR(); MF(1, 0, Fq); RB(G, 0, vb0, vb1, SS, 1); RA(G, 1, va); SBAR(); \
      MF(2, 0, Fq); RA(G, 2, va); RA(G, 3, va); SBAR(); MF(3, 0, Fq); RB(G, 1, vb0, vb1, SS, 0); RB(G, 1, vb0, vb1, SS, 1); WAITL(8); SBAR(); \
      MF(0, 1, Fq); POST; SBAR(); MF(1, 1, Fq); MF(2, 1, Fq); MF(3, 1, Fq); } while (0)
    unsigned c4 = 4u;
    STQ(f0, f1, a21, b20, b21, 1, );
    STQ(f1, f0, a30, b30, b31, 0, );
    unsigned lr;
    STQ(f0, f1, a31, b30, b31, 1, lr = lm + (L_RL - L_M); asm volatile("" : "+v"(lr)));
    f4 c0, c1; f2 mv[2], rv[2], rl[2];
#define RD2(d, a, o) asm volatile("ds_read2_b32 %0, %1 offset0:%2 offset1:%3" : "=v"(d) : "v"(a), "i"(o), "i"((o) + 16) : "memory")
    WAITL(2); SBAR();
    MF(0, 0, f1); RD128(c0, lc, 0); RD128(c1, ld, 0); SBAR(); MF(1, 0, f1); RD2(mv[0], lm, 0); RD2(rl[0], lr, 0); SBAR();
    MF(2, 0, f1); RD2(rv[0], lm, 128); RD2(mv[1], lm, 32); SBAR(); MF(3, 0, f1); RD2(rl[1], lr, 32); RD2(rv[1], lm, 160); WAITL(8); SBAR();
    MF(0, 1, f1); asm volatile("" : "+v"(c4)); SBAR(); MF(1, 1, f1); MF(2, 1, f1); MF(3, 1, f1);
    asm volatile("s_waitcnt lgkmcnt(0)" : "+v"(c0), "+v"(c1), "+v"(mv[0]), "+v"(mv[1]), "+v"(rv[0]), "+v"(rv[1]), "+v"(rl[0]), "+v"(rl[1]) : : "memory");
    if (__builtin_expect(__builtin_amdgcn_readfirstlane(sxl) >= 14, 0)) {
      const unsigned xo = xad + (sxl - 12) * STG;
#pragma unroll
      for (int t = 0; t < 8; ++t) { const unsigned a_ = xo ^ (t * 16); asm volatile("ds_read_b64 %0, %1" : "+v"(xv[t]) : "v"(a_) : "memory"); }
    }
    SBAR(); AL8(); SBAR();
    u2 ph[2][2];
#pragma unroll
    for (int m = 0; m < 4; ++m) {
      f4 ea[2], pp[2];
#pragma unroll
      for (int a = 0; a < 2; ++a) {
        const f4 u = acc[m][a] + (a ? c1 : c0) * mv[m >> 1][m & 1];
        ea[a] = u * rl[m >> 1][m & 1]; pp[a] = u * rv[m >> 1][m & 1] + 1.f;
      }
      SBAR();
#pragma unroll
      for (int e = 0; e < 8; ++e) ea[e >> 2][e & 3] = __builtin_amdgcn_exp2f(ea[e >> 2][e & 3]);
      asm volatile(".p2align 3" ::"v"(ea[0]), "v"(ea[1][0]), "v"(ea[1][1]), "v"(ea[1][2]), "v"(pp[0]), "v"(pp[1])); SBAR();
#pragma unroll
      for (int e = 0; e < 8; ++e) pp[e >> 2][e & 3] = __builtin_amdgcn_fmed3f(ea[e >> 2][e & 3], pp[e >> 2][e & 3], 1.f);
      asm volatile("" ::"v"(pp[0]), "v"(pp[1])); SBAR();
      pp[0] += 1e-6f; pp[1] += 1e-6f;

#pragma unroll
      for (int a = 0; a < 2; ++a) ph[a][m & 1] = (u2){pk2(pp[a][0], pp[a][1]), pk2(pp[a][2], pp[a][3])};
      SBAR();
      if (m & 1) asm volatile("ds_write2st64_b64 %0, %2, %3 offset0:%6 offset1:%7\n ds_write2st64_b64 %1, %4, %5 offset0:%6 offset1:%7"
                              ::"v"(pw0), "v"(pw1), "v"(ph[0][0]), "v"(ph[0][1]), "v"(ph[1][0]), "v"(ph[1][1]), "i"(2 * m - 2), "i"(2 * m) : "memory");
      SBAR();
    }
    asm volatile("ds_write_b32 %8, %9\n s_waitcnt lgkmcnt(0)" : "+v"(xv[0]), "+v"(xv[1]), "+v"(xv[2]), "+v"(xv[3]), "+v"(xv[4]), "+v"(xv[5]), "+v"(xv[6]), "+v"(xv[7]) : "v"(vfl), "v"(c4) : "memory");
  }
  __builtin_amdgcn_s_setprio(0);
  STMP(5);
  {
  int l;
  asm volatile("v_mbcnt_lo_u32_b32 %0, -1, 0\n v_mbcnt_hi_u32_b32 %0, -1, %0" : "=v"(l));
  __builtin_assume(l >= 0 && l < 64);
  const int r15 = l & 15, g4 = l >> 4, q = (l & 15) >> 2, p = l & 3;
  const int trow = mb * 128 + w * 32 + (l & 31);
  u2 oh;
  const unsigned long long ptab = mktab();
  const unsigned flo = PAIRAT(r15) * 128;
  const char* fb = uptr(wsx + WS_OH + mb * 2048);
  unsigned vfw[4];
#pragma unroll
  for (int i = 0; i < 4; ++i) { vfw[i] = VFW(i); asm volatile("" : "+v"(vfw[i])); }
  auto wo_part = [&](int s0, int s1) {
#pragma unroll
    for (int i = s0 / 2; i < s1 / 2; i += 2) {
      const int pp = wr ? wtat(i + 1) : wtat(i);
      const char* wp = (const char*)wo + (nb * 128 - 3072) + (2 * PAIRAT(pp) + wc) * 65536; const unsigned wm = ldsb + (2 * pp + wc) * 4096;
      GL4(vfw, wp, wm, L_WO);
    }
  };
  if (WF) {
  lwait<LW3>(adFL, 0x3Fu, (l & 4) ? 3u : 4u);
  asm volatile("global_load_dwordx2 %0, %1, %2 sc1" : "=&v"(oh) : "v"(flo), "s"(fb) : "memory");
  wo_part(WE, WB);
  SBAR();
  }
  int sel = -1;
  {
    const u4 s0 = *(const LAS u4*)(lds + L_SEL), s1 = *(const LAS u4*)(lds + L_SEL + 16);
#pragma unroll
    for (int i = 0; i < 8; ++i) { const int v = (int)(i < 4 ? s0[i & 3] : s1[i & 3]); sel = v == trow ? i : sel; }
  }
  const bool anysel = __ballot(sel >= 0) != 0ull;
  f16v oc[2][2];
  {
    const float m = *(const LAS float*)(lds + L_M + (w * 32 + (l & 31)) * 4), rs = *(const LAS float*)(lds + L_RS + (w * 32 + (l & 31)) * 4);
#pragma unroll
    for (int t = 0; t < 8; ++t) {
      const f4 xf = (f4){lo(xv[t][0]), hi(xv[t][0]), lo(xv[t][1]), hi(xv[t][1])};
      const f4 y = (xf - m) * rs + xf;
#pragma unroll
      for (int i = 0; i < 4; ++i) oc[0][t >> 2][(t & 3) * 4 + i] = y[i];
    }
    asm volatile("" : "+v"(oc[0][0]), "+v"(oc[0][1]));
  }
  unsigned wa[2];
#pragma unroll
  for (int t = 0; t < 2; ++t) wa[t] = ldsb + L_WO + wc * 4096 + (4 * (g4 >> 1) + q) * 128 + (((4 * t + 2 * (g4 & 1) + (p >> 1)) ^ ((q >> 1) << 2)) * 16) + (p & 1) * 8;
  u4 a[RD][4];
#pragma unroll
  for (int k = 0; k < RD; ++k) asm volatile("" : "=v"(a[k][0]), "=v"(a[k][1]), "=v"(a[k][2]), "=v"(a[k][3]));
  const char* ob = uptr(wsx + WS_OF + mb * 262144 + wc * 8192 + wr * 4096);
  const char* ob2 = ob + (8192 - wc * 16384);
  const unsigned ov0 = l * 16 + wc * 2048, ov1 = ov0 ^ 2048;
  unsigned xw = ldsb + L_WO + (4 * wr + wc) * 4096 + l * 16, xr = xw ^ 4096, mf1 = myFL, pf1 = adFL, c7 = 7u, ta[8];
  unsigned ya = ldsb + L_KVX + max(sel, 0) * 256 + (l >> 5) * 16, as = anysel, thr = (l & 7) == (w ^ 1) ? 7u : (l & 7) >= 6 && anysel ? 3u : 0u;
  unsigned th2 = sel >= 0 ? ~0u : thr;
  asm volatile("" : "+v"(ya), "+v"(thr), "+v"(th2)); as = __builtin_amdgcn_readfirstlane(as); asm volatile("" : "+s"(as));
  unsigned tr = ldsb + L_PQ + w * 4096 + (l >> 3) * 128 + ((l ^ (l >> 3)) & 7) * 16, oo = (mb * 128 + w * 32 + (l >> 3)) * 2048 + nb * 128 + (l & 7) * 16;
#pragma unroll
  for (int t = 0; t < 8; ++t) { ta[t] = ldsb + L_PQ + w * 4096 + (l & 31) * 128 + ((t ^ (l & 7)) * 16) + (l >> 5) * 8; asm volatile("" : "+v"(ta[t])); }
  asm volatile("" : "+v"(xw), "+v"(xr), "+v"(mf1), "+v"(pf1), "+v"(c7), "+v"(tr), "+v"(oo));
  STMP(6);
  if (!WF) {
  lwait<2>(adFL, 0x3Fu, (l & 4) ? 3u : 4u);
  asm volatile("global_load_dwordx2 %0, %1, %2 sc1" : "=&v"(oh) : "v"(flo), "s"(fb) : "memory");
  wo_part(WE, WB);
  }
  WAITV(WB - WE);
  asm volatile("" : "+v"(oh));
  unsigned seen = (unsigned)__ballot(oh[0] == tag), go = 0;
  auto need = [&](unsigned mask) {
    if (__builtin_expect((go & mask) != mask, 0)) {
      RETRY2();
      _Pragma("unroll 1") for (unsigned it = 0; it < SPIN; ++it) {
        if ((seen & mask) != mask) {
          asm volatile("global_load_dwordx2 %0, %1, %2 sc1\n s_waitcnt vmcnt(0)" : "+v"(oh) : "v"(flo), "s"(fb) : "memory");
          seen = (unsigned)__ballot(oh[0] == tag);
        }
        go = seen & (unsigned)__ballot(oh[1] - NOW() - 1u >= 1000u);
        if ((go & mask) == mask) break;
        SLEEP(1);
      }
    }
  };
#define OX 0
#define OSC "sc1"
#define OVA(pos, i) (((i) & 1) && (pos) < 14 ? ov1 : ov0)
#define OBA(pos, i) ((((i) & 1) && (pos) >= 14 ? ob2 : ob) + PAIRAT(pos) * 16384)
#define OL1(pos, i) asm volatile("global_load_dwordx4 %0, %1, %2 offset:%3 sc1" : "+v"(a[(pos) % RD][i]) : "v"(OVA(pos, i)), "s"(OBA(pos, i)), "i"(((i) >> 1) * 1024) : "memory")
#define OLR(pos) do { _Pragma("unroll") for (int i_ = 0; i_ < 4; ++i_) asm volatile("global_load_dwordx4 %0, %1, %2 sc1" : "+v"(a[(pos) % RD][i_]) : "v"(OVA(pos, i_) + (i_ >> 1) * 1024), "s"(OBA(pos, i_)) : "memory"); } while (0)
#define OLD(pos) do { OL1(pos, 0); OL1(pos, 1); OL1(pos, 2); OL1(pos, 3); } while (0)
  auto omax = [&](u4(&v)[4]) {
    asm volatile("" : "+v"(v[0]), "+v"(v[1]), "+v"(v[2]), "+v"(v[3]));
    return max(umax3(v[0][0], v[1][0], v[2][0]), v[3][0]);
  };
#define OFIX(pos, m) do { \
    if (__builtin_expect(__ballot((m) == POIS) != 0ull, 0)) { RETRY(); _Pragma("unroll 1") for (unsigned it = 0; it < SPIN; ++it) { SLEEP(4); OLR(pos); WAITV(0); if (__ballot(omax(a[(pos) % RD]) == POIS) == 0ull) break; } } } while (0)
#define OS1(i)
#define OS2(i)
  u2 oh2 = oh;
  s4 b[8];
#define ORD(pos, f) do { if ((f) < 2 && (pos) == 8) { wa[(f) & 1] += 65536; asm volatile("" : "+v"(wa[(f) & 1])); } \
    RDTR(b[2 * (f)], wa[(f) & 1], ((pos) & 7) * 8192 + ((f) >> 1) * 1024); RDTR(b[2 * (f) + 1], wa[(f) & 1], ((pos) & 7) * 8192 + ((f) >> 1) * 1024 + 2048); } while (0)
  const unsigned lo0 = ldsb + L_PQ + w * 4096 + ov0, lo1 = lo0 ^ 2048;
#pragma unroll
  for (int i = 1; i < RD; ++i) {
    if (i == E0 + 1) {
      lwait<LW4>(adFL, 0x30u, 4);
      if (F5) asm volatile("s_waitcnt vmcnt(0)\n ds_write_b32 %0, %1" ::"v"(myFL), "v"(5u) : "memory");
    }
    if (i == E0 + 2) {
      RD128(a[0][0], lo0, 0); RD128(a[0][1], lo1, 0); RD128(a[0][2], lo0, 1024); RD128(a[0][3], lo1, 1024);
      ORD(0, 0); ORD(0, 1); ORD(0, 2); ORD(0, 3);
    }
    if (i == WP) wo_part(WB, 32);
    if (i == RD - 1) break;
    if (i == 1) {
      __builtin_amdgcn_s_setprio(2);
      if (E0 && ES) SLEEP(ES);
    }
    if (i == 1 || i == E0 + 1) go = seen & (unsigned)__ballot(oh[1] - NOW() - 1u >= 1000u);
    need(1u << i);
    if (i == 1) {
      STMP(15);
      oh2 = oh;
      if (__builtin_expect(seen != ~0u, 0)) asm volatile("global_load_dwordx2 %0, %1, %2 sc1" : "+v"(oh2) : "v"(flo), "s"(fb) : "memory");
    }
    OS1(i); OLD(i);
  }
#define WXT(pos) ((pos) < WP ? 32 - WB : 0)
  STMP(10);
#define OMF(pos, f, r) do { if ((f) < 2 && (pos) == 0 && (r) == 1) asm volatile("v_mfma_f32_32x32x16_bf16 %0, %1, %2, 0" : "=&v"(oc[r][(f) & 1]) \
      : "v"(CAT(b[2 * (f)], b[2 * (f) + 1])), "v"(a[(pos) % RD][((f) & 2) + (r)])); \
    else asm volatile("v_mfma_f32_32x32x16_bf16 %0, %1, %2, %0" : "+v"(oc[r][(f) & 1]) \
      : "v"(CAT(b[2 * (f)], b[2 * (f) + 1])), "v"(a[(pos) % RD][((f) & 2) + (r)])); \
    SBAR(); if ((r) == 1) ORD((pos) + 1, f); SBAR(); } while (0)
#define ONX(pos) ((pos) + RD - 1 < 16)
#define ONL(pos, i) do { if (ONX(pos)) OL1(((pos) + RD - 1) & 15, i); SBAR(); } while (0)
#define OWL(pos, f) do { WAITL(6); SBAR(); } while (0)
#define OHEAD(pos) do { unsigned m_ = 0; \
    if ((pos) == wtmin(WE / 2, WB / 2) - 1) { asm volatile("" : "+v"(fl3)); if (__builtin_expect(((unsigned)__ballot(fl3 < 5u) >> (w ^ 2)) & 1u, 0)) lwait(adFL, 1u << (w ^ 2), 5); SBAR(); } \
    if ((pos) == wtmin(WB / 2, 16) - 1) { asm volatile("" : "+v"(fl3)); if (__builtin_expect(((unsigned)__ballot(fl3 < 6u) >> (w ^ 2)) & 1u, 0)) lwait(adFL, 1u << (w ^ 2), 6); SBAR(); } \
    OWL(pos, 0); OMF(pos, 0, 0); if (ONX(pos)) { need(1u << (((pos) + RD - 1) & 15)); OS1(((pos) + RD - 1) & 15); } SBAR(); \
    OMF(pos, 0, 1); ONL(pos, 0); \
    OWL(pos, 1); OMF(pos, 1, 0); ONL(pos, 1); \
    OMF(pos, 1, 1); ONL(pos, 2); \
    OWL(pos, 2); OMF(pos, 2, 0); ONL(pos, 3); \
    if (((pos) == 1 && !F5) || (pos) == 4) { asm volatile("ds_write_b32 %0, %1" ::"v"(myFL), "v"((pos) > 1 ? 6u : 5u) : "memory"); SBAR(); } \
    if ((pos) == wtmin(WE / 2, WB / 2) - 3 || (pos) == wtmin(WB / 2, 16) - 3) { asm volatile("ds_read_b32 %0, %1" : "=v"(fl3) : "v"(adFL) : "memory"); SBAR(); } \
    OMF(pos, 2, 1); \
    OWL(pos, 3); OMF(pos, 3, 0); \
    if ((pos) < 15) { WAITV(4 * ((pos) + RD - 1 < 16 ? RD - 2 : 14 - (pos)) + WXT((pos) + 1)); m_ = omax(a[((pos) + 1) % RD]); asm volatile("" : "+v"(m_)); SBAR(); } \
    OMF(pos, 3, 1); \
    if ((pos) < 15) { OFIX(((pos) + 1) & 15, m_); OS2(((pos) + 1) & 15); } SBAR(); } while (0)
  static_assert(F5 && wtmin(WE / 2, WB / 2) >= 4 && wtmin(WB / 2, 16) >= wtmin(WE / 2, WB / 2) + 2 && wtmin(WB / 2, 16) >= 8 && wtmin(WB / 2, 16) <= 13 && WB < 32 && RD >= 4 && wtmax(WE / 2, 16) < 14, "");
  unsigned fl3;
  OHEAD(0);
  {
    asm volatile("" : "+v"(oh2));
    if (oh2[0] == tag) oh = oh2;
    seen = (unsigned)__ballot(oh[0] == tag);
    go = seen & (unsigned)__ballot(oh[1] - NOW() - max(r15 - RD, 0) * ADV - 1u >= 1000u);
    SBAR(); }
  OHEAD(1); STMP(11); OHEAD(2); OHEAD(3); OHEAD(4); OHEAD(5); OHEAD(6); OHEAD(7); STMP(12);
  OHEAD(8); OHEAD(9); STMP(13); OHEAD(10); OHEAD(11); OHEAD(12);
  {
#define XW(t) asm volatile("ds_write_b128 %0, %1 offset:%2" ::"v"(xw), "v"(__builtin_shufflevector(oc[1][(t) >> 2], oc[1][(t) >> 2], ((t) & 3) * 4, ((t) & 3) * 4 + 1, ((t) & 3) * 4 + 2, ((t) & 3) * 4 + 3)), \
      "i"(((t) >> 2) * 8192 + ((t) & 3) * 1024) : "memory")
#define OMX(f, r) do { asm volatile("v_mfma_f32_32x32x16_bf16 %0, %1, %2, %0" : "+v"(oc[r][(f) & 1]) : "v"(CAT(b[2 * (f)], b[2 * (f) + 1])), "v"(a[13 % RD][((f) & 2) + (r)])); SBAR(); } while (0)
#define OMU(pos, f, r) do { asm volatile("v_mfma_f32_32x32x16_bf16 %0, %1, %2, %0" : "+v"(oc[0][(f) & 1]) \
      : "v"(CAT(bu[(pos) & 1][r][2 * (f)], bu[(pos) & 1][r][2 * (f) + 1])), "v"(a[(pos) % RD][((f) & 2) + (r)])); SBAR(); } while (0)
#define ORU(pos, f, r) do { RDTR(bu[(pos) & 1][r][2 * (f)], (r) ? wb[(f) & 1] : wa[(f) & 1], ((pos) & 7) * 8192 + ((f) >> 1) * 1024); \
      RDTR(bu[(pos) & 1][r][2 * (f) + 1], (r) ? wb[(f) & 1] : wa[(f) & 1], ((pos) & 7) * 8192 + ((f) >> 1) * 1024 + 2048); SBAR(); } while (0)
#define OH13(f, f2) do { WAITL(6 + 2 * (f)); SBAR(); OMX(f, 1); ORU(14, f2, 0); ORU(14, f2, 1); } while (0)
    s4 bu[2][2][8]; unsigned m_;
    unsigned wb[2] = {wa[0] ^ 4096, wa[1] ^ 4096};
    asm volatile("" : "+v"(wb[0]), "+v"(wb[1])); SBAR();
    OH13(0, 0); OH13(1, 2); OH13(2, 1); OH13(3, 3);
    OMX(0, 0); asm volatile("s_nop 0" : "+v"(oc[1][0])); XW(0); XW(1); SBAR();
    OMX(1, 0); XW(2); XW(3); SBAR();
    OMX(2, 0); XW(4); XW(5); SBAR();
    WAITV(4); m_ = omax(a[14 % RD]); asm volatile("" : "+v"(m_)); SBAR();
    OMX(3, 0); XW(6); XW(7);
    asm volatile("ds_write_b32 %0, %1" ::"v"(mf1), "v"(c7) : "memory"); SBAR();
    OFIX(14, m_); SBAR();
    WAITL(9); SBAR();
#define ORH(k) RDTR(bu[1][((k) >> 1) & 1][(((k) >> 2) & 1) * 4 + ((k) >> 3) * 2 + ((k) & 1)], ((k) >> 1) & 1 ? wb[(k) >> 3] : wa[(k) >> 3], 7 * 8192 + (((k) >> 2) & 1) * 1024 + ((k) & 1) * 2048)
    OMU(14, 0, 0); ORH(0); ORH(1); ORH(2); SBAR(); OMU(14, 0, 1); ORH(3); ORH(4); ORH(5); SBAR(); OMU(14, 2, 0); ORH(6); ORH(7); ORH(8); SBAR();
    OMU(14, 2, 1); ORH(9); ORH(10); ORH(11); SBAR(); OMU(14, 1, 0); ORH(12); ORH(13); ORH(14); SBAR(); OMU(14, 1, 1); ORH(15); SBAR();
    WAITL(8); SBAR();
    f4 xi[8], y[8]; unsigned v, nx;
#define XRD(L) "ds_read_b32 %0, %18\n ds_read_b128 %5, %19 offset:8192\n ds_read_b128 %6, %19 offset:9216\n ds_read_b128 %7, %19 offset:10240\n ds_read_b128 %8, %19 offset:11264\n" \
      "ds_read_b128 %1, %19\n ds_read_b128 %2, %19 offset:1024\n ds_read_b128 %3, %19 offset:2048\n ds_read_b128 %4, %19 offset:3072\n s_cmp_eq_u32 %21, 0\n s_cbranch_scc1 " L "\n" \
      "ds_read_b128 %13, %20 offset:128\n ds_read_b128 %14, %20 offset:160\n ds_read_b128 %15, %20 offset:192\n ds_read_b128 %16, %20 offset:224\n" \
      "ds_read_b128 %9, %20\n ds_read_b128 %10, %20 offset:32\n ds_read_b128 %11, %20 offset:64\n ds_read_b128 %12, %20 offset:96\n" L ":\n"
#define XOPS(c) c(v), c(xi[0]), c(xi[1]), c(xi[2]), c(xi[3]), c(xi[4]), c(xi[5]), c(xi[6]), c(xi[7]), c(y[0]), c(y[1]), c(y[2]), c(y[3]), c(y[4]), c(y[5]), c(y[6]), c(y[7])
    asm volatile("ds_read_b32 %0, %1" : "=v"(v) : "v"(pf1) : "memory"); RD128(xi[4], xr, 8192); SBAR();
    OMU(14, 3, 0); RD128(xi[5], xr, 9216); RD128(xi[6], xr, 10240); RD128(xi[7], xr, 11264); SBAR();
    WAITV(0); m_ = omax(a[15 % RD]); asm volatile("" : "+v"(m_)); SBAR();
    OMU(14, 3, 1); RD128(xi[0], xr, 0); RD128(xi[1], xr, 1024); RD128(xi[2], xr, 2048); RD128(xi[3], xr, 3072);
    asm volatile("s_cmp_eq_u32 %9, 0\n s_cbranch_scc1 .Ly0\n ds_read_b128 %0, %8\n ds_read_b128 %1, %8 offset:32\n ds_read_b128 %2, %8 offset:64\n ds_read_b128 %3, %8 offset:96\n"
                 "ds_read_b128 %4, %8 offset:128\n ds_read_b128 %5, %8 offset:160\n ds_read_b128 %6, %8 offset:192\n ds_read_b128 %7, %8 offset:224\n.Ly0:"
                 : "=&v"(y[0]), "=&v"(y[1]), "=&v"(y[2]), "=&v"(y[3]), "=&v"(y[4]), "=&v"(y[5]), "=&v"(y[6]), "=&v"(y[7]) : "v"(ya), "s"(as) : "memory", "scc");
    SBAR();
    OFIX(15, m_); SBAR();
    STMP(8);
    OMU(15, 0, 0); OMU(15, 0, 1);
    asm volatile("s_waitcnt lgkmcnt(0)" : XOPS("+v")::"memory");
    if (__builtin_expect(__ballot(v < th2) != 0ull, 1)) {
      asm volatile("v_cmp_gt_u32 vcc, %22, %0\n s_cbranch_vccz .Lyd\n s_mov_b32 %17, 0x200000\n.Lyl:\n" XRD(".Ly1")
                   "s_sub_u32 %17, %17, 1\n s_waitcnt lgkmcnt(0)\n s_cbranch_scc1 .Lyd\n v_cmp_gt_u32 vcc, %22, %0\n s_cbranch_vccnz .Lyl\n.Lyd:"
                   : XOPS("+v"), "=&s"(nx) : "v"(pf1), "v"(xr), "v"(ya), "s"(as), "v"(thr) : "memory", "scc", "vcc");
      if (sel >= 0) { _Pragma("unroll") for (int t = 0; t < 8; ++t) xi[t] += y[t]; }
    }
#define XB(t) do { _Pragma("unroll") for (int i = 0; i < 4; ++i) oc[0][(t) >> 2][((t) & 3) * 4 + i] += xi[t][i]; } while (0)
#define XA(t) do { _Pragma("unroll") for (int i = 0; i < 4; ++i) { float r_; asm("v_add_f32 %0, %1, %2" : "=v"(r_) : "v"(oc[0][(t) >> 2][((t) & 3) * 4 + i]), "v"(xi[t][i])); oc[0][(t) >> 2][((t) & 3) * 4 + i] = r_; } } while (0)
#define TW_(t) do { const u2 d_ = (u2){pk2(oc[0][(t) >> 2][((t) & 3) * 4], oc[0][(t) >> 2][((t) & 3) * 4 + 1]), pk2(oc[0][(t) >> 2][((t) & 3) * 4 + 2], oc[0][(t) >> 2][((t) & 3) * 4 + 3])}; \
      asm volatile("ds_write_b64 %0, %1" ::"v"(ta[t]), "v"(d_) : "memory"); SBAR(); } while (0)
    asm volatile("" : "+v"(xi[0]), "+v"(xi[1]), "+v"(xi[2]), "+v"(xi[3]), "+v"(xi[4]), "+v"(xi[5]), "+v"(xi[6]), "+v"(xi[7]), "+v"(oc[0][1])); SBAR();
    OMU(15, 2, 0);
    XA(4); XA(5); SBAR();
    OMU(15, 2, 1);
    XA(6); XA(7); asm volatile("" : "+v"(oc[0][1])); SBAR();
    OMU(15, 1, 0); asm volatile("s_nop 0" : "+v"(oc[0][0])); XB(0); TW_(0);
    OMU(15, 1, 1); XB(1); TW_(1);
    OMU(15, 3, 0); XB(2); TW_(2);
    OMU(15, 3, 1); XB(3); TW_(3);
    asm volatile("s_nop 4" : "+v"(oc[0][1])); SBAR();
    TW_(4); TW_(5); TW_(6); TW_(7);
    u4 ov[4];
    RD128(ov[0], tr, 0); RD128(ov[1], tr, 1024); RD128(ov[2], tr, 2048); RD128(ov[3], tr, 3072);
    const __amdgpu_buffer_rsrc_t outr = __builtin_amdgcn_make_buffer_rsrc(out, 0, 2048 * D * 2, 0x00020000);
#pragma unroll
    for (int i = 0; i < 4; ++i) {
      asm volatile("s_waitcnt lgkmcnt(%1)" : "+v"(ov[i]) : "i"(3 - i) : "memory");
      __builtin_amdgcn_raw_buffer_store_b128(ov[i], outr, oo, i * 16384, 16);
    }
  }
  if (g == 0 && w == 0 && l == 0) __builtin_amdgcn_raw_buffer_store_b32(tag, wse, WS_E, 0, 16);
  STMP(9);
  STFLUSH();
  asm volatile(".Lend_c:");
  }
  return;
  }
  {
#define LT2(i) BST(i)
    unsigned vb[4], vfw[4];
    STMP(6); SBAR();
#pragma unroll
    for (int i = 0; i < 4; ++i) vb[i] = (8 * i + (l >> 3)) * 6144 + (((l & 7) ^ ((((l >> 4) & 1) << 2) | ((i & 1) << 1))) * 16) + (3 - i) * 1024;
    const unsigned lb0 = lw * 196608, lb1 = 196608 - lw * 194560, mbb = ldsb + 16384 + lw * 12288;
    const char* Ab = kx + ao;
    const char* Bb = kq + (hp * 128 + lw * 2048 - 3072);
    auto issue = [&](int s, int u0, int u1) {
      const int kt = (s + rot) & 15;
      const char* sa = Ab + kt * 128; const char* sb = Bb + kt * (64 * 6144);
      const unsigned so = (s & 3) * STG, ma = mba + so, mq = mbb + so;
#pragma unroll
      for (int u = u0; u < u1; ++u) { if (u < 2) GL4(va, sa + u * 65536, ma, u * 4096); else GL4(vb, sb + (u == 2 ? lb0 : u == 3 ? lb1 : lb0 + 2048), mq, (u - 2) * 4096); }
    };
    int tk = 0;
#define LST(i) BST(i)
#define LBAR() do { LST(tk + 1); BAR(); LST(tk + 21); ++tk; } while (0)
    issue(0, 2, 3); issue(0, 1, 2); issue(0, 3, 5); SBAR(); STMP(8); SBAR(); issue(1, 0, 5); issue(2, 0, U0);
    LT2(60);
    SBAR();
    GETTAG(tag);
    MKWS(tag);
    const unsigned long long ptab = mktab();
#pragma unroll
    for (int i = 0; i < 4; ++i) vfw[i] = VFW(i);
    const int hd = hp * 2 + lw;
    u4 sel;
#pragma unroll
    for (int e = 0; e < 2; ++e) {
      unsigned v = (r15 >> 2) == g4 ? (((r15 & 3) == 2 * e ? 0x3F80u : 0u) | ((r15 & 3) == 2 * e + 1 ? 0x3F800000u : 0u)) : 0u;
      sel[e] = v; sel[2 + e] = v;
    }
    asm volatile("" : "+v"(sel));
    const char* kvb = uptr(wsx + WS_KVP + 4096 + (size_t)hd * 32768);
    const char* zb = uptr(wsx + WS_ZP + (size_t)hd * 2048);
    unsigned zoff = q * 128 + g4 * 32 + p * 8, oho = l == 0 ? WS_OH + (mb * 16 + hp) * 128 : WS_SZ, fla = ldsb + L_FL, v0 = 0u, v1 = 1u, afl = adFL, mfl = myFL;
    unsigned hko = l == 0 ? WS_HK + hp * 512 + (mb >> 2) * 128 + (mb & 3) * 4 : WS_SZ, pzo = WS_OF + (mb * 32 + hp * 2 + (PE >= 2 ? lw : 0)) * 8192 + l * 16;
    u4 pz = (u4)(POIS);
    asm volatile("" : "+v"(zoff), "+v"(oho), "+v"(fla), "+s"(kvb), "+s"(zb), "+v"(v0), "+v"(v1), "+v"(afl), "+v"(mfl), "+v"(hko), "+v"(pzo), "+v"(pz));
    unsigned lv16 = l * 16, pa = ldsb + L_PQ + lw * 4096 + l * 16, thr = (l & 4) ? 3u : 4u;
    unsigned ofo = WS_OF + (mb * 32 + hd) * 8192 + g4 * 512 + r15 * 16, lwa = ldsb + L_PQ + lw * 4096 + g4 * 512 + r15 * 16;
    asm volatile("" : "+v"(vfw[0]), "+v"(vfw[1]), "+v"(vfw[2]), "+v"(vfw[3]), "+v"(lv16), "+v"(pa), "+v"(thr), "+v"(ofo), "+v"(lwa));
    PFB(sp ? 64 - SPS : 64 - SPW, sp ? 2 * SPS : 2 * SPW);
    SBAR();
    WAITV(20 + 4 * U0); LT2(0); BAR(); LT2(20);
    issue(2, U0, 5);
    unsigned ck0 = (unsigned)__builtin_amdgcn_s_memtime(), rt0 = NOW();
    asm volatile("" : "+s"(ck0), "+s"(rt0));
#define LPF() spf(pfb)
    int s0 = 3;
    if (LPL) __builtin_amdgcn_s_setprio(LPL);
    if (U3 < 5) { issue(3, 0, U3); LST(tk + 40); WAITV(20 + 4 * U3); LBAR(); issue(3, U3, 5); s0 = 4; }
#pragma unroll 1
    for (; s0 < (sp ? (LF ? SL5 + 2 : 16) : 6 + ph); ++s0) { issue(s0, 0, 5); LST(tk + 40); if (sp && s0 < 14) LPF(); WAITV(40); LBAR(); }
    if (!sp) {
#pragma unroll 1
    for (; s0 < 16; s0 += 5) {
      LPF(); WAITV(20); LBAR();
      LPF(); WAITV(0); LBAR();
      issue(s0, 0, 5); issue(s0 + 1, 0, U1); LST(tk + 40); WAITV(4 * U1); LBAR();
      issue(s0 + 1, U1, 5); issue(s0 + 2, 0, 5);
      if (LF && s0 > 10) { s0 += 3; break; }
      if (s0 + 3 >= 16) break;
      issue(s0 + 3, 0, U2); LST(tk + 40); WAITV(20 + 4 * U2); LBAR();
      issue(s0 + 3, U2, 5);
      if (s0 + 4 >= 16) break;
      issue(s0 + 4, 0, 5); LST(tk + 40); WAITV(40); LBAR();
    }
    }
    if (LF) {
#pragma unroll 1
      for (; s0 < 16; ++s0) { LST(tk + 40); WAITV(20); LBAR(); issue(s0, 0, 5); }
    }
    if (LPL) __builtin_amdgcn_s_setprio(0);
    int wq = 0;
    auto wo_upto = [&](int lim) {
      _Pragma("unroll 1") for (; wq < lim; ++wq) {
        const int sl = 2 * ((int)(WT >> (4 * (wq >> 1))) & 15) + (wq & 1);
        const char* wp = (const char*)wo + (nb * 128 - 3072) + HEADAT(sl) * 65536; const unsigned wm = ldsb + sl * 4096; GL4(vfw, wp, wm, L_WO); }
    };
    if (!ZH) asm volatile("ds_write_b32 %0, %1" ::"v"(afl), "v"(v0) : "memory");
    if (PE == 1 && lw) {
#pragma unroll
      for (int i = 0; i < 16; ++i) __builtin_amdgcn_raw_buffer_store_b128(pz, wsr, pzo + (i & 3) * 1024, (i >> 2) * 4096, 16);
    }
    auto po8 = [&]() {
#pragma unroll
      for (int i = 0; i < 8; ++i) __builtin_amdgcn_raw_buffer_store_b128(pz, wsr, pzo + (i & 3) * 1024, (i >> 2) * 4096, 16);
    };
    const bool pl = PE == 3 && !sp && ph == 2;
    if (PE >= 2 && !pl) po8();
    SBAR();
    unsigned kd = sp ? KS : KD, od = OD;
    if (CF && lw) {
      const unsigned dc = (unsigned)__builtin_amdgcn_s_memtime() - ck0, dr = NOW() - rt0;
      const float sc = fminf((float)dr * (float)CF * __builtin_amdgcn_rcpf((float)dc), 2.f);
      kd = KL + (unsigned)((float)(kd - KL) * sc); od = OL + (unsigned)((float)(OD - OL) * sc);
      asm volatile("" : "+v"(kd), "+v"(od));
    }
    SBAR();
    if (PE == 1 && lw) WAITV(16); else if (PE >= 2 && !pl) WAITV(8); else WAITV(0);
    WAITL(0); LBAR();
    SBAR();
    u4 dg;
    unsigned now;
    if (lw) {
      const unsigned tb = NOW();
      __builtin_amdgcn_raw_buffer_store_b32(tb + kd, wsr, hko, 0, 16);
#pragma unroll
      for (int i = 0; i < (PE ? 0 : 16); ++i) __builtin_amdgcn_raw_buffer_store_b128(pz, wsr, pzo + (i & 3) * 1024, (i >> 2) * 4096, 16);
      if (pl) po8();
      LT2(61);
      const char* hb = uptr(wsx + WS_HK + hp * 512);
      __builtin_amdgcn_s_setprio(LP1);
      lwait<4>(adFL, PE >= 2 ? 0xD0u : 0xC0u, 1u);
      while (NOW() - tb < HT) SLEEP(1);
      STMP(1);
      unsigned t0, rq[4], m0, m1, nt;
#define HG4(d, a, b, c, e) "s_sub_u32 s" #a ", s" #a ", %0\n s_sub_u32 s" #b ", s" #b ", %0\n s_sub_u32 s" #c ", s" #c ", %0\n s_sub_u32 s" #e ", s" #e ", %0\n" \
      "s_max_u32 s" #a ", s" #a ", s" #b "\n s_max_u32 s" #c ", s" #c ", s" #e "\n s_max_u32 %" #d ", s" #a ", s" #c "\n"
      asm volatile("s_mov_b32 %7, 15\n.Lhint:\n s_load_dwordx4 s[72:75], %8, 0 glc\n s_load_dwordx4 s[76:79], %8, 128 glc\n s_load_dwordx4 s[80:83], %8, 256 glc\n"
                   "s_load_dwordx4 s[84:87], %8, 384 glc\n s_memrealtime s[88:89]\n s_waitcnt lgkmcnt(0)\n s_sub_u32 %0, s88, 0x800\n"
                   HG4(1, 72, 73, 74, 75) HG4(2, 76, 77, 78, 79) HG4(3, 80, 81, 82, 83) HG4(4, 84, 85, 86, 87)
                   "s_max_u32 %5, %1, %2\n s_max_u32 %6, %3, %4\n s_max_u32 s72, %5, %6\n s_cmpk_lt_u32 s72, 0xc00\n s_cbranch_scc1 .Lhdone\n"
                   "s_sleep %9\n s_sub_u32 %7, %7, 1\n s_cbranch_scc0 .Lhint\n"
                   "s_mov_b32 %1, 0\n s_mov_b32 %2, 0\n s_mov_b32 %3, 0\n s_mov_b32 %4, 0\n s_mov_b32 %5, 0\n s_mov_b32 %6, 0\n.Lhdone:"
                   : "=&s"(t0), "=&s"(rq[0]), "=&s"(rq[1]), "=&s"(rq[2]), "=&s"(rq[3]), "=&s"(m0), "=&s"(m1), "=&s"(nt) : "s"(hb), "i"(HKS)
                   : "memory", "scc", "s72", "s73", "s74", "s75", "s76", "s77", "s78", "s79", "s80", "s81", "s82", "s83", "s84", "s85", "s86", "s87", "s88", "s89");
      LT2(16);
      const bool up = m0 > m1;
      const unsigned a0 = up ? rq[2] : rq[0], a1 = up ? rq[3] : rq[1], a2 = up ? rq[0] : rq[2], a3 = up ? rq[1] : rq[3], gu = up ? 2u : 0u;
      unsigned e0, e1, go;
      asm("s_cmp_gt_u32 %3, %4\n s_cselect_b32 %0, %6, %5\n s_cselect_b32 %1, %5, %6\n s_addc_u32 %2, %7, 0" : "=&s"(e0), "=&s"(e1), "=&s"(go) : "s"(a2), "s"(a3), "s"(a0), "s"(a1), "s"(gu) : "scc");
      dg = (u4){((t0 + 3u + e0) & ~3u) | go, t0 + e1, t0 + min(a2, a3), t0 + max(m0, m1)};
      asm volatile("ds_write_b128 %0, %1 offset:32\n ds_write_b32 %0, %2 offset:20" ::"v"(fla), "v"(dg), "v"(2u) : "memory");
      LT2(15);
      now = NOW();
      WAITV(0);
      unsigned de = dg[3] - t0;
      if (GI) { de = max(max(de, dg[2] - t0 + GI), max(dg[1] - t0 + 2 * GI, max(dg[0] - t0, tb + (unsigned)__builtin_amdgcn_readfirstlane(kd) + GO - t0) + 3 * GI)); }
      de += t0;
      ST64(((u2){tag, ((int)(de - now) > 0 ? de : now) + od}), oho);
    } else {
      if (pl) po8();
      if (WD0) { const unsigned tb = NOW(); wo_upto(W00); while (NOW() - tb < WD0) SLEEP(2); }
      wo_upto(WA);
      if (PE >= 2) asm volatile("s_waitcnt vmcnt(%2)\n ds_write_b32 %0, %1" ::"v"(mfl), "v"(v1), "i"(4 * WA) : "memory");
      LT2(15);
      if (WA < WE || GW) { lwait<2>(adFL, 0x0Fu, 3); if (WS0) SLEEP(WS0); wo_upto(WE); }
      STMP(1);
      __builtin_amdgcn_s_setprio(LP0);
      u4 v;
      _Pragma("unroll 1") for (unsigned it = 0; it < SPIN; ++it) {
        unsigned f;
        asm volatile("ds_read_b32 %0, %2 offset:20\n ds_read_b128 %1, %2 offset:32\n s_waitcnt lgkmcnt(0)" : "=&v"(f), "=&v"(v) : "v"(fla) : "memory");
        if (__builtin_amdgcn_readfirstlane(f) >= 2u) break;
        SLEEP(2);
      }
#pragma unroll
      for (int e = 0; e < 4; ++e) dg[e] = __builtin_amdgcn_readfirstlane(v[e]);
      now = NOW();
    }
    unsigned gx = (dg[0] & 3u) * 8192u;
    asm volatile("" : "+s"(gx));
    __builtin_amdgcn_s_setprio(3);
    STMP(2);
    unsigned nr;
#define OST_(i)
#define OSTE :
    asm volatile(
      "s_lshr_b32 s72, %[gx], 4\n v_xor_b32 v252, %[gx], %[lv]\n v_xor_b32 v253, s72, %[zo]\n s_mov_b32 s74, %[now]\n s_sub_i32 s72, s74, %[d0]\n s_cmp_gt_i32 s72, -1\n "
      "s_cbranch_scc1 .Lgo0\n .Lgw0:\n s_memrealtime s[74:75]\n s_waitcnt lgkmcnt(0)\n s_sub_i32 s72, s74, %[d0]\n s_cmp_lt_i32 s72, 0\n s_cbranch_scc1 .Lgw0\n .Lgo0:\n "
      "global_load_dwordx2 v[240:241], v253, %[zb] sc1\n global_load_dwordx4 v[80:83], v252, %[kb] offset:-4096 sc1\n global_load_dwordx4 v[84:87], v252, %[kb] offset:-2048 sc1\n "
      "global_load_dwordx4 v[90:93], v252, %[kb] offset:-3072 sc1\n global_load_dwordx4 v[94:97], v252, %[kb] offset:-1024 sc1\n global_load_dwordx4 v[100:103], v252, %[kb] offset:0 sc1\n "
      "global_load_dwordx4 v[104:107], v252, %[kb] offset:2048 sc1\n global_load_dwordx4 v[110:113], v252, %[kb] offset:1024 sc1\n "
      "global_load_dwordx4 v[114:117], v252, %[kb] offset:3072 sc1\n v_xor_b32 v252, 0x2000, v252\n v_xor_b32 v253, 0x200, v253\n s_sub_i32 s72, s74, %[d1]\n s_cmp_gt_i32 s72, -1\n "
      "s_cbranch_scc1 .Lgo1\n .Lgw1:\n s_memrealtime s[74:75]\n s_waitcnt lgkmcnt(0)\n s_sub_i32 s72, s74, %[d1]\n s_cmp_lt_i32 s72, 0\n s_cbranch_scc1 .Lgw1\n .Lgo1:\n "
      "global_load_dwordx2 v[242:243], v253, %[zb] sc1\n global_load_dwordx4 v[120:123], v252, %[kb] offset:-4096 sc1\n global_load_dwordx4 v[124:127], v252, %[kb] offset:-2048 sc1\n "
      "global_load_dwordx4 v[130:133], v252, %[kb] offset:-3072 sc1\n global_load_dwordx4 v[134:137], v252, %[kb] offset:-1024 sc1\n "
      "global_load_dwordx4 v[140:143], v252, %[kb] offset:0 sc1\n global_load_dwordx4 v[144:147], v252, %[kb] offset:2048 sc1\n global_load_dwordx4 v[150:153], v252, %[kb] offset:1024 sc1\n "
      "global_load_dwordx4 v[154:157], v252, %[kb] offset:3072 sc1\n v_xor_b32 v252, 0x6000, v252\n v_xor_b32 v253, 0x600, v253\n s_sub_i32 s72, s74, %[d2]\n s_cmp_gt_i32 s72, -1\n "
      "s_cbranch_scc1 .Lgo2\n .Lgw2:\n s_memrealtime s[74:75]\n s_waitcnt lgkmcnt(0)\n s_sub_i32 s72, s74, %[d2]\n s_cmp_lt_i32 s72, 0\n s_cbranch_scc1 .Lgw2\n .Lgo2:\n "
      "global_load_dwordx2 v[244:245], v253, %[zb] sc1\n global_load_dwordx4 v[160:163], v252, %[kb] offset:-4096 sc1\n global_load_dwordx4 v[164:167], v252, %[kb] offset:-2048 sc1\n "
      "global_load_dwordx4 v[170:173], v252, %[kb] offset:-3072 sc1\n global_load_dwordx4 v[174:177], v252, %[kb] offset:-1024 sc1\n "
      "global_load_dwordx4 v[180:183], v252, %[kb] offset:0 sc1\n global_load_dwordx4 v[184:187], v252, %[kb] offset:2048 sc1\n global_load_dwordx4 v[190:193], v252, %[kb] offset:1024 sc1\n "
      "global_load_dwordx4 v[194:197], v252, %[kb] offset:3072 sc1\n v_xor_b32 v252, 0x2000, v252\n v_xor_b32 v253, 0x200, v253\n v_mov_b32 v248, 3\n s_sub_i32 s72, s74, %[d3]\n "
      "s_cmp_gt_i32 s72, -1\n s_cbranch_scc1 .Lgo3\n .Lgw3:\n s_memrealtime s[74:75]\n s_waitcnt lgkmcnt(0)\n s_sub_i32 s72, s74, %[d3]\n s_cmp_lt_i32 s72, 0\n s_cbranch_scc1 .Lgw3\n "
      ".Lgo3:\n "
      OST_(0)
      "global_load_dwordx2 v[246:247], v253, %[zb] sc1\n global_load_dwordx4 v[200:203], v252, %[kb] offset:-4096 sc1\n global_load_dwordx4 v[204:207], v252, %[kb] offset:-2048 sc1\n "
      "global_load_dwordx4 v[210:213], v252, %[kb] offset:-3072 sc1\n global_load_dwordx4 v[214:217], v252, %[kb] offset:-1024 sc1\n "
      "global_load_dwordx4 v[220:223], v252, %[kb] offset:0 sc1\n global_load_dwordx4 v[224:227], v252, %[kb] offset:2048 sc1\n global_load_dwordx4 v[230:233], v252, %[kb] offset:1024 sc1\n "
      "global_load_dwordx4 v[234:237], v252, %[kb] offset:3072 sc1\n ds_write_b32 %[mfl], v248\n "
      OST_(1)
      "s_mov_b32 %[nr], 0\n s_movk_i32 s73, 0x1000\n s_waitcnt vmcnt(27)\n .Lck0:\n v_max3_u32 v254, v240, v80, v84\n v_max3_u32 v254, v254, v90, v94\n v_max3_u32 v254, v254, v100, v104\n "
      "v_max3_u32 v254, v254, v110, v114\n v_cmp_eq_u32 vcc, -1, v254\n s_cbranch_vccnz .Lrt0\n .Lok0:\n v_mov_b64 v[88:89], v[82:83]\n v_mov_b64 v[82:83], v[84:85]\n "
      "v_mov_b64 v[98:99], v[92:93]\n v_mov_b64 v[92:93], v[94:95]\n v_mov_b64 v[108:109], v[102:103]\n v_mov_b64 v[102:103], v[104:105]\n v_mov_b64 v[118:119], v[112:113]\n "
      "v_mov_b64 v[112:113], v[114:115]\n v_mfma_f32_16x16x32_bf16 v[68:71], %[sel], v[86:89], 0\n v_mfma_f32_16x16x32_bf16 v[64:67], %[sel], v[80:83], 0\n "
      "v_mfma_f32_16x16x32_bf16 v[72:75], %[sel], v[96:99], 0\n v_mfma_f32_16x16x32_bf16 v[76:79], %[sel], v[90:93], 0\n v_mfma_f32_16x16x32_bf16 v[68:71], %[sel], v[106:109], v[68:71]\n "
      "v_mfma_f32_16x16x32_bf16 v[64:67], %[sel], v[100:103], v[64:67]\n v_mfma_f32_16x16x32_bf16 v[72:75], %[sel], v[116:119], v[72:75]\n "
      "v_mfma_f32_16x16x32_bf16 v[76:79], %[sel], v[110:113], v[76:79]\n s_waitcnt vmcnt(18)\n .Lck1:\n v_max3_u32 v254, v242, v120, v124\n v_max3_u32 v254, v254, v130, v134\n "
      "v_max3_u32 v254, v254, v140, v144\n v_max3_u32 v254, v254, v150, v154\n v_cmp_eq_u32 vcc, -1, v254\n s_cbranch_vccnz .Lrt1\n .Lok1:\n v_mov_b64 v[128:129], v[122:123]\n "
      "v_mov_b64 v[122:123], v[124:125]\n v_mov_b64 v[138:139], v[132:133]\n v_mov_b64 v[132:133], v[134:135]\n v_mov_b64 v[148:149], v[142:143]\n v_mov_b64 v[142:143], v[144:145]\n "
      "v_mov_b64 v[158:159], v[152:153]\n v_mov_b64 v[152:153], v[154:155]\n v_mfma_f32_16x16x32_bf16 v[84:87], %[sel], v[126:129], 0\n "
      "v_mfma_f32_16x16x32_bf16 v[80:83], %[sel], v[120:123], 0\n v_mfma_f32_16x16x32_bf16 v[88:91], %[sel], v[136:139], 0\n v_mfma_f32_16x16x32_bf16 v[92:95], %[sel], v[130:133], 0\n "
      "v_mfma_f32_16x16x32_bf16 v[84:87], %[sel], v[146:149], v[84:87]\n v_mfma_f32_16x16x32_bf16 v[80:83], %[sel], v[140:143], v[80:83]\n "
      "v_mfma_f32_16x16x32_bf16 v[88:91], %[sel], v[156:159], v[88:91]\n v_mfma_f32_16x16x32_bf16 v[92:95], %[sel], v[150:153], v[92:95]\n s_waitcnt vmcnt(9)\n .Lck2:\n "
      "v_max3_u32 v254, v244, v160, v164\n v_max3_u32 v254, v254, v170, v174\n v_max3_u32 v254, v254, v180, v184\n v_max3_u32 v254, v254, v190, v194\n v_cmp_eq_u32 vcc, -1, v254\n "
      "s_cbranch_vccnz .Lrt2\n .Lok2:\n v_mov_b64 v[168:169], v[162:163]\n v_mov_b64 v[162:163], v[164:165]\n v_mov_b64 v[178:179], v[172:173]\n v_mov_b64 v[172:173], v[174:175]\n "
      "v_mov_b64 v[188:189], v[182:183]\n v_mov_b64 v[182:183], v[184:185]\n v_mov_b64 v[198:199], v[192:193]\n v_mov_b64 v[192:193], v[194:195]\n "
      "v_pk_add_f32 v[64:65], v[64:65], v[80:81]\n v_pk_add_f32 v[66:67], v[66:67], v[82:83]\n v_pk_add_f32 v[68:69], v[68:69], v[84:85]\n v_pk_add_f32 v[70:71], v[70:71], v[86:87]\n "
      "v_pk_add_f32 v[72:73], v[72:73], v[88:89]\n v_pk_add_f32 v[74:75], v[74:75], v[90:91]\n v_pk_add_f32 v[76:77], v[76:77], v[92:93]\n v_pk_add_f32 v[78:79], v[78:79], v[94:95]\n "
      "v_pk_add_f32 v[250:251], v[240:241], v[242:243]\n v_mfma_f32_16x16x32_bf16 v[100:103], %[sel], v[166:169], 0\n v_mfma_f32_16x16x32_bf16 v[96:99], %[sel], v[160:163], 0\n "
      "v_mfma_f32_16x16x32_bf16 v[104:107], %[sel], v[176:179], 0\n v_mfma_f32_16x16x32_bf16 v[108:111], %[sel], v[170:173], 0\n "
      "v_mfma_f32_16x16x32_bf16 v[100:103], %[sel], v[186:189], v[100:103]\n v_mfma_f32_16x16x32_bf16 v[96:99], %[sel], v[180:183], v[96:99]\n "
      "v_mfma_f32_16x16x32_bf16 v[104:107], %[sel], v[196:199], v[104:107]\n v_mfma_f32_16x16x32_bf16 v[108:111], %[sel], v[190:193], v[108:111]\n v_mov_b32 v156, 0x358637bd\n "
      "v_mov_b32 v157, 0x358637bd\n v_mov_b32 v158, 0x358637bd\n v_mov_b32 v159, 0x358637bd\n s_mov_b32 s72, 0x200000\n .Lpq:\n ds_read_b32 v248, %[afl]\n s_waitcnt lgkmcnt(0)\n "
      "v_cmp_lt_u32 vcc, v248, %[thr]\n s_and_b32 s76, vcc_lo, %[om]\n s_cbranch_scc0 .Lpd\n s_sub_u32 s72, s72, 1\n s_cbranch_scc0 .Lpq\n .Lpd:\n ds_read_b128 v[120:123], %[pa] offset:0\n "
      "ds_read_b128 v[124:127], %[pa] offset:1024\n ds_read_b128 v[128:131], %[pa] offset:2048\n ds_read_b128 v[132:135], %[pa] offset:3072\n ds_read_b128 v[136:139], %[pa] offset:8192\n "
      "ds_read_b128 v[140:143], %[pa] offset:9216\n ds_read_b128 v[144:147], %[pa] offset:10240\n ds_read_b128 v[148:151], %[pa] offset:11264\n .p2align 3\n .Lg3:\n s_waitcnt vmcnt(8)\n "
      "v_add_f32 v248, v244, v246\n v_add_f32 v249, v245, v247\n v_add_f32 v248, v250, v248\n v_add_f32 v249, v251, v249\n s_nop 0\n "
      "v_add_f32_dpp v248, v248, v248 row_ror:8 row_mask:0xf bank_mask:0xf\n v_add_f32_dpp v249, v249, v249 row_ror:8 row_mask:0xf bank_mask:0xf\n s_nop 0\n "
      "v_add_f32_dpp v248, v248, v248 row_ror:4 row_mask:0xf bank_mask:0xf\n v_add_f32_dpp v249, v249, v249 row_ror:4 row_mask:0xf bank_mask:0xf\n v_cvt_pk_bf16_f32 v248, v248, v249\n "
      "s_nop 1\n v_mov_b32_dpp v112, v248 quad_perm:[0,0,0,0] row_mask:0xf bank_mask:0xf\n v_mov_b32_dpp v113, v248 quad_perm:[1,1,1,1] row_mask:0xf bank_mask:0xf\n "
      "v_mov_b32_dpp v114, v248 quad_perm:[2,2,2,2] row_mask:0xf bank_mask:0xf\n v_mov_b32_dpp v115, v248 quad_perm:[3,3,3,3] row_mask:0xf bank_mask:0xf\n s_waitcnt vmcnt(7) lgkmcnt(0)\n "
      "v_mov_b64 v[208:209], v[202:203]\n v_mfma_f32_16x16x32_bf16 v[176:179], v[112:115], v[120:123], v[156:159]\n v_mfma_f32_16x16x32_bf16 v[180:183], v[112:115], v[124:127], v[156:159]\n "
      "v_mfma_f32_16x16x32_bf16 v[184:187], v[112:115], v[128:131], v[156:159]\n v_rcp_f32_e64 v192, v176\n v_mfma_f32_16x16x32_bf16 v[188:191], v[112:115], v[132:135], v[156:159]\n "
      "v_rcp_f32 v193, v180\n s_waitcnt vmcnt(6)\n v_mfma_f32_16x16x32_bf16 v[164:167], %[sel], v[206:209], 0\n v_mov_b64_e64 v[202:203], v[204:205]\n v_max3_u32 v254, v246, v200, v204\n "
      "v_mfma_f32_16x16x32_bf16 v[176:179], v[112:115], v[136:139], v[156:159]\n v_rcp_f32_e64 v194, v184\n v_mfma_f32_16x16x32_bf16 v[160:163], %[sel], v[200:203], 0\n s_waitcnt vmcnt(5)\n "
      "v_mov_b64 v[218:219], v[212:213]\n v_mfma_f32_16x16x32_bf16 v[180:183], v[112:115], v[140:143], v[156:159]\n v_rcp_f32 v195, v188\n s_waitcnt vmcnt(4)\n "
      "v_mfma_f32_16x16x32_bf16 v[168:171], %[sel], v[216:219], 0\n v_mov_b64_e64 v[212:213], v[214:215]\n v_max3_u32 v254, v254, v210, v214\n "
      "v_mfma_f32_16x16x32_bf16 v[184:187], v[112:115], v[144:147], v[156:159]\n v_rcp_f32_e64 v196, v176\n v_mfma_f32_16x16x32_bf16 v[172:175], %[sel], v[210:213], 0\n s_waitcnt vmcnt(3)\n "
      "v_mov_b64 v[228:229], v[222:223]\n v_mfma_f32_16x16x32_bf16 v[188:191], v[112:115], v[148:151], v[156:159]\n v_rcp_f32 v197, v180\n s_waitcnt vmcnt(2)\n "
      "v_mfma_f32_16x16x32_bf16 v[164:167], %[sel], v[226:229], v[164:167]\n v_mov_b64_e64 v[222:223], v[224:225]\n v_max3_u32 v254, v254, v220, v224\n v_rcp_f32_e64 v198, v184\n "
      "v_mfma_f32_16x16x32_bf16 v[160:163], %[sel], v[220:223], v[160:163]\n v_rcp_f32_e64 v199, v188\n s_waitcnt vmcnt(1)\n v_mov_b64 v[238:239], v[232:233]\n "
      "v_pk_add_f32 v[164:165], v[100:101], v[164:165]\n v_pk_add_f32 v[166:167], v[102:103], v[166:167]\n v_pk_add_f32 v[164:165], v[68:69], v[164:165]\n "
      "v_pk_add_f32 v[166:167], v[70:71], v[166:167]\n v_pk_add_f32 v[160:161], v[96:97], v[160:161]\n v_pk_add_f32 v[162:163], v[98:99], v[162:163]\n "
      "v_pk_add_f32 v[160:161], v[64:65], v[160:161]\n v_pk_add_f32 v[162:163], v[66:67], v[162:163]\n s_waitcnt vmcnt(0)\n "
      OST_(2)
      "v_mov_b64 v[232:233], v[234:235]\n v_mfma_f32_16x16x32_bf16 v[168:171], %[sel], v[236:239], v[168:171]\n v_max3_u32 v254, v254, v230, v234\n v_cvt_pk_bf16_f32 v118, v164, v165\n "
      "v_mfma_f32_16x16x32_bf16 v[172:175], %[sel], v[230:233], v[172:175]\n v_cvt_pk_bf16_f32 v119, v166, v167\n v_cvt_pk_bf16_f32 v116, v160, v161\n v_cvt_pk_bf16_f32 v117, v162, v163\n "
      "v_cmp_eq_u32 vcc, -1, v254\n s_cbranch_vccnz .Lrt3\n .Lok3:\n "
      OST_(3)
      "v_mfma_f32_16x16x32_bf16 v[200:203], v[116:119], v[120:123], 0\n v_add_f32 v168, v104, v168\n v_add_f32 v169, v105, v169\n "
      "v_mfma_f32_16x16x32_bf16 v[204:207], v[116:119], v[124:127], 0\n v_add_f32 v170, v106, v170\n v_add_f32 v171, v107, v171\n "
      "v_mfma_f32_16x16x32_bf16 v[208:211], v[116:119], v[128:131], 0\n v_add_f32 v168, v72, v168\n v_add_f32 v169, v73, v169\n "
      "v_mfma_f32_16x16x32_bf16 v[212:215], v[116:119], v[132:135], 0\n v_add_f32 v170, v74, v170\n v_add_f32 v171, v75, v171\n "
      "v_mfma_f32_16x16x32_bf16 v[216:219], v[116:119], v[136:139], 0\n v_add_f32 v172, v108, v172\n v_add_f32 v173, v109, v173\n v_cvt_pk_bf16_f32 v152, v168, v169\n "
      "v_mfma_f32_16x16x32_bf16 v[220:223], v[116:119], v[140:143], 0\n v_add_f32 v174, v110, v174\n v_add_f32 v175, v111, v175\n v_cvt_pk_bf16_f32 v153, v170, v171\n "
      "v_mfma_f32_16x16x32_bf16 v[224:227], v[116:119], v[144:147], 0\n v_add_f32 v172, v76, v172\n v_add_f32 v173, v77, v173\n v_add_f32 v174, v78, v174\n v_add_f32 v175, v79, v175\n "
      "v_cvt_pk_bf16_f32 v154, v172, v173\n v_cvt_pk_bf16_f32 v155, v174, v175\n v_mfma_f32_16x16x32_bf16 v[228:231], v[116:119], v[148:151], 0\n v_mul_f32 v200, v200, v192\n "
      "v_mul_f32 v201, v201, v192\n v_mfma_f32_16x16x32_bf16 v[64:67], v[152:155], v[120:123], 0\n v_mul_f32 v202, v202, v192\n v_mul_f32 v203, v203, v192\n "
      "v_mfma_f32_16x16x32_bf16 v[68:71], v[152:155], v[124:127], 0\n v_cvt_pk_bf16_f32 v96, v200, v201\n v_cvt_pk_bf16_f32 v97, v202, v203\n "
      "v_mfma_f32_16x16x32_bf16 v[72:75], v[152:155], v[128:131], 0\n v_mul_f32 v204, v204, v193\n v_mul_f32 v205, v205, v193\n "
      "v_mfma_f32_16x16x32_bf16 v[76:79], v[152:155], v[132:135], 0\n v_mul_f32 v206, v206, v193\n v_mul_f32 v207, v207, v193\n "
      "v_mfma_f32_16x16x32_bf16 v[80:83], v[152:155], v[136:139], 0\n v_cvt_pk_bf16_f32 v100, v204, v205\n v_cvt_pk_bf16_f32 v101, v206, v207\n "
      "v_mfma_f32_16x16x32_bf16 v[84:87], v[152:155], v[140:143], 0\n v_mul_f32 v208, v208, v194\n v_mul_f32 v209, v209, v194\n "
      "v_mfma_f32_16x16x32_bf16 v[88:91], v[152:155], v[144:147], 0\n v_mul_f32 v210, v210, v194\n v_mul_f32 v211, v211, v194\n "
      "v_mfma_f32_16x16x32_bf16 v[92:95], v[152:155], v[148:151], 0\n v_cvt_pk_bf16_f32 v104, v208, v209\n v_cvt_pk_bf16_f32 v105, v210, v211\n "
      OST_(4)
      "v_mul_f32 v64, v64, v192\n v_mul_f32 v65, v65, v192\n v_mul_f32 v66, v66, v192\n v_mul_f32 v67, v67, v192\n v_cvt_pk_bf16_f32 v98, v64, v65\n v_cvt_pk_bf16_f32 v99, v66, v67\n "
      "buffer_store_dwordx4 v[96:99], %[ofo], %[ws], 0 offen offset:0 sc1\n v_pk_mul_f32 v[68:69], v[68:69], v[192:193] op_sel:[0,1]\n "
      "v_pk_mul_f32 v[70:71], v[70:71], v[192:193] op_sel:[0,1]\n v_cvt_pk_bf16_f32 v102, v68, v69\n v_cvt_pk_bf16_f32 v103, v70, v71\n "
      "buffer_store_dwordx4 v[100:103], %[ofo], %[ws], 0 offen offset:256 sc1\n v_pk_mul_f32 v[72:73], v[72:73], v[194:195] op_sel_hi:[1,0]\n "
      "v_pk_mul_f32 v[74:75], v[74:75], v[194:195] op_sel_hi:[1,0]\n v_cvt_pk_bf16_f32 v106, v72, v73\n v_cvt_pk_bf16_f32 v107, v74, v75\n "
      "buffer_store_dwordx4 v[104:107], %[ofo], %[ws], 0 offen offset:2048 sc1\n v_pk_mul_f32 v[76:77], v[76:77], v[194:195] op_sel:[0,1]\n "
      "v_pk_mul_f32 v[78:79], v[78:79], v[194:195] op_sel:[0,1]\n v_pk_mul_f32 v[212:213], v[212:213], v[194:195] op_sel:[0,1]\n "
      "v_pk_mul_f32 v[214:215], v[214:215], v[194:195] op_sel:[0,1]\n v_cvt_pk_bf16_f32 v108, v212, v213\n v_cvt_pk_bf16_f32 v109, v214, v215\n v_cvt_pk_bf16_f32 v110, v76, v77\n "
      "v_cvt_pk_bf16_f32 v111, v78, v79\n buffer_store_dwordx4 v[108:111], %[ofo], %[ws], 0 offen offset:2304 sc1\n v_pk_mul_f32 v[80:81], v[80:81], v[196:197] op_sel_hi:[1,0]\n "
      "v_pk_mul_f32 v[82:83], v[82:83], v[196:197] op_sel_hi:[1,0]\n v_pk_mul_f32 v[216:217], v[216:217], v[196:197] op_sel_hi:[1,0]\n "
      "v_pk_mul_f32 v[218:219], v[218:219], v[196:197] op_sel_hi:[1,0]\n v_cvt_pk_bf16_f32 v176, v216, v217\n v_cvt_pk_bf16_f32 v177, v218, v219\n v_cvt_pk_bf16_f32 v178, v80, v81\n "
      "v_cvt_pk_bf16_f32 v179, v82, v83\n buffer_store_dwordx4 v[176:179], %[ofo], %[ws], s73 offen offset:0 sc1\n v_pk_mul_f32 v[84:85], v[84:85], v[196:197] op_sel:[0,1]\n "
      "v_pk_mul_f32 v[86:87], v[86:87], v[196:197] op_sel:[0,1]\n v_pk_mul_f32 v[220:221], v[220:221], v[196:197] op_sel:[0,1]\n "
      "v_pk_mul_f32 v[222:223], v[222:223], v[196:197] op_sel:[0,1]\n v_cvt_pk_bf16_f32 v180, v220, v221\n v_cvt_pk_bf16_f32 v181, v222, v223\n v_cvt_pk_bf16_f32 v182, v84, v85\n "
      "v_cvt_pk_bf16_f32 v183, v86, v87\n buffer_store_dwordx4 v[180:183], %[ofo], %[ws], s73 offen offset:256 sc1\n v_pk_mul_f32 v[88:89], v[88:89], v[198:199] op_sel_hi:[1,0]\n "
      "v_pk_mul_f32 v[90:91], v[90:91], v[198:199] op_sel_hi:[1,0]\n v_pk_mul_f32 v[224:225], v[224:225], v[198:199] op_sel_hi:[1,0]\n "
      "v_pk_mul_f32 v[226:227], v[226:227], v[198:199] op_sel_hi:[1,0]\n v_cvt_pk_bf16_f32 v184, v224, v225\n v_cvt_pk_bf16_f32 v185, v226, v227\n v_cvt_pk_bf16_f32 v186, v88, v89\n "
      "v_cvt_pk_bf16_f32 v187, v90, v91\n buffer_store_dwordx4 v[184:187], %[ofo], %[ws], s73 offen offset:2048 sc1\n v_pk_mul_f32 v[92:93], v[92:93], v[198:199] op_sel:[0,1]\n "
      "v_pk_mul_f32 v[94:95], v[94:95], v[198:199] op_sel:[0,1]\n v_pk_mul_f32 v[228:229], v[228:229], v[198:199] op_sel:[0,1]\n "
      "v_pk_mul_f32 v[230:231], v[230:231], v[198:199] op_sel:[0,1]\n v_cvt_pk_bf16_f32 v188, v228, v229\n v_cvt_pk_bf16_f32 v189, v230, v231\n v_cvt_pk_bf16_f32 v190, v92, v93\n "
      "v_cvt_pk_bf16_f32 v191, v94, v95\n buffer_store_dwordx4 v[188:191], %[ofo], %[ws], s73 offen offset:2304 sc1\n "
      OST_(5)
      "v_mov_b32 v248, 4\n ds_write_b128 %[lwa], v[96:99] offset:0\n ds_write_b128 %[lwa], v[100:103] offset:256\n ds_write_b128 %[lwa], v[104:107] offset:2048\n "
      "ds_write_b128 %[lwa], v[108:111] offset:2304\n ds_write_b128 %[lwa], v[176:179] offset:8192\n ds_write_b128 %[lwa], v[180:183] offset:8448\n "
      "ds_write_b128 %[lwa], v[184:187] offset:10240\n ds_write_b128 %[lwa], v[188:191] offset:10496\n ds_write_b32 %[mfl], v248\n s_branch .Lgd\n .Lrt0:\n s_add_u32 %[nr], %[nr], 1\n "
      "s_bitcmp1_b32 %[nr], 21\n s_cbranch_scc1 .Lok0\n s_sleep 2\n v_xor_b32 v248, 0x6000, v252\n v_xor_b32 v249, 0x600, v253\n global_load_dwordx2 v[240:241], v249, %[zb] sc1\n "
      "global_load_dwordx4 v[80:83], v248, %[kb] offset:-4096 sc1\n global_load_dwordx4 v[84:87], v248, %[kb] offset:-2048 sc1\n global_load_dwordx4 v[90:93], v248, %[kb] offset:-3072 sc1\n "
      "global_load_dwordx4 v[94:97], v248, %[kb] offset:-1024 sc1\n global_load_dwordx4 v[100:103], v248, %[kb] offset:0 sc1\n global_load_dwordx4 v[104:107], v248, %[kb] offset:2048 sc1\n "
      "global_load_dwordx4 v[110:113], v248, %[kb] offset:1024 sc1\n global_load_dwordx4 v[114:117], v248, %[kb] offset:3072 sc1\n s_waitcnt vmcnt(0)\n s_branch .Lck0\n .Lrt1:\n "
      "s_add_u32 %[nr], %[nr], 1\n s_bitcmp1_b32 %[nr], 21\n s_cbranch_scc1 .Lok1\n s_sleep 2\n v_xor_b32 v248, 0x4000, v252\n v_xor_b32 v249, 0x400, v253\n "
      "global_load_dwordx2 v[242:243], v249, %[zb] sc1\n global_load_dwordx4 v[120:123], v248, %[kb] offset:-4096 sc1\n global_load_dwordx4 v[124:127], v248, %[kb] offset:-2048 sc1\n "
      "global_load_dwordx4 v[130:133], v248, %[kb] offset:-3072 sc1\n global_load_dwordx4 v[134:137], v248, %[kb] offset:-1024 sc1\n "
      "global_load_dwordx4 v[140:143], v248, %[kb] offset:0 sc1\n global_load_dwordx4 v[144:147], v248, %[kb] offset:2048 sc1\n global_load_dwordx4 v[150:153], v248, %[kb] offset:1024 sc1\n "
      "global_load_dwordx4 v[154:157], v248, %[kb] offset:3072 sc1\n s_waitcnt vmcnt(0)\n s_branch .Lck1\n .Lrt2:\n s_add_u32 %[nr], %[nr], 1\n s_bitcmp1_b32 %[nr], 21\n "
      "s_cbranch_scc1 .Lok2\n s_sleep 2\n v_xor_b32 v248, 0x2000, v252\n v_xor_b32 v249, 0x200, v253\n global_load_dwordx2 v[244:245], v249, %[zb] sc1\n "
      "global_load_dwordx4 v[160:163], v248, %[kb] offset:-4096 sc1\n global_load_dwordx4 v[164:167], v248, %[kb] offset:-2048 sc1\n "
      "global_load_dwordx4 v[170:173], v248, %[kb] offset:-3072 sc1\n global_load_dwordx4 v[174:177], v248, %[kb] offset:-1024 sc1\n "
      "global_load_dwordx4 v[180:183], v248, %[kb] offset:0 sc1\n global_load_dwordx4 v[184:187], v248, %[kb] offset:2048 sc1\n global_load_dwordx4 v[190:193], v248, %[kb] offset:1024 sc1\n "
      "global_load_dwordx4 v[194:197], v248, %[kb] offset:3072 sc1\n s_waitcnt vmcnt(0)\n s_branch .Lck2\n .Lrt3:\n s_add_u32 %[nr], %[nr], 1\n s_bitcmp1_b32 %[nr], 21\n "
      "s_cbranch_scc1 .Lok3\n s_sleep 2\n global_load_dwordx2 v[246:247], v253, %[zb] sc1\n global_load_dwordx4 v[200:203], v252, %[kb] offset:-4096 sc1\n "
      "global_load_dwordx4 v[204:207], v252, %[kb] offset:-2048 sc1\n global_load_dwordx4 v[210:213], v252, %[kb] offset:-3072 sc1\n "
      "global_load_dwordx4 v[214:217], v252, %[kb] offset:-1024 sc1\n global_load_dwordx4 v[220:223], v252, %[kb] offset:0 sc1\n "
      "global_load_dwordx4 v[224:227], v252, %[kb] offset:2048 sc1\n global_load_dwordx4 v[230:233], v252, %[kb] offset:1024 sc1\n "
      "global_load_dwordx4 v[234:237], v252, %[kb] offset:3072 sc1\n s_branch .Lg3\n .Lgd:\n "
      OSTE
      [nr] "=&s"(nr)
      : [gx] "s"(gx), [now] "s"(now), [d0] "s"(dg[0]), [d1] "s"(dg[1]), [d2] "s"(dg[2]), [d3] "s"(dg[3]), [kb] "s"(kvb), [zb] "s"(zb), [ws] "s"(wsr), [om] "s"(lw ? 0x0Au : 0x25u),
        [lv] "v"(lv16), [zo] "v"(zoff), [mfl] "v"(mfl), [afl] "v"(afl), [thr] "v"(thr), [pa] "v"(pa), [ofo] "v"(ofo), [lwa] "v"(lwa), [sel] "v"(sel)
      : "memory", "scc", "vcc", "s72", "s73", "s74", "s75", "s76",
        "v64", "v65", "v66", "v67", "v68", "v69", "v70", "v71", "v72", "v73", "v74", "v75", "v76", "v77", "v78", "v79", "v80", "v81", "v82", "v83", "v84", "v85", "v86", "v87", "v88", "v89",
        "v90", "v91", "v92", "v93", "v94", "v95", "v96", "v97", "v98", "v99", "v100", "v101", "v102", "v103", "v104", "v105", "v106", "v107", "v108", "v109", "v110", "v111", "v112", "v113",
        "v114", "v115", "v116", "v117", "v118", "v119", "v120", "v121", "v122", "v123", "v124", "v125", "v126", "v127", "v128", "v129", "v130", "v131", "v132", "v133", "v134", "v135",
        "v136", "v137", "v138", "v139", "v140", "v141", "v142", "v143", "v144", "v145", "v146", "v147", "v148", "v149", "v150", "v151", "v152", "v153", "v154", "v155", "v156", "v157",
        "v158", "v159", "v160", "v161", "v162", "v163", "v164", "v165", "v166", "v167", "v168", "v169", "v170", "v171", "v172", "v173", "v174", "v175", "v176", "v177", "v178", "v179",
        "v180", "v181", "v182", "v183", "v184", "v185", "v186", "v187", "v188", "v189", "v190", "v191", "v192", "v193", "v194", "v195", "v196", "v197", "v198", "v199", "v200", "v201",
        "v202", "v203", "v204", "v205", "v206", "v207", "v208", "v209", "v210", "v211", "v212", "v213", "v214", "v215", "v216", "v217", "v218", "v219", "v220", "v221", "v222", "v223",
        "v224", "v225", "v226", "v227", "v228", "v229", "v230", "v231", "v232", "v233", "v234", "v235", "v236", "v237", "v238", "v239", "v240", "v241", "v242", "v243", "v244", "v245",
        "v246", "v247", "v248", "v249", "v250", "v251", "v252", "v253", "v254");
    STMP(4);
    STFLUSH();
    asm volatile(".Lend_l:");
  }

}

extern "C" void kernel_launch(void* const* d_in, const int* in_sizes, int n_in, void* d_out, int out_size,
                              void* d_ws, size_t ws_size, hipStream_t stream) {
  (void)hipFuncSetAttribute((const void*)fused, hipFuncAttributeMaxDynamicSharedMemorySize, LDS_SZ);
  fused<<<256, 512, LDS_SZ, stream>>>((const u16*)d_in[0], (const u16*)d_in[1], (const u16*)d_in[4], (char*)d_ws, (const u16*)d_in[2],
                                      (const u16*)d_in[5], (const u16*)d_in[6], (const u16*)d_in[7], (u16*)d_out);
}
```
